```python
import torch

def _softmax(x, axis=-1):
    return torch.softmax(x, dim=axis)

def _gelu(x):
    return torch.nn.functional.gelu(x, approximate='tanh')

def _silu(x):
    return torch.nn.functional.silu(x)

def _sigmoid(x):
    return torch.sigmoid(x)

def _layernorm(x, axis=-1, eps=1e-05):
    m = x.mean(dim=axis, keepdim=True)
    v = x.var(dim=axis, keepdim=True, unbiased=False)
    return (x - m) / torch.sqrt(v + eps)

def _scatter_add(tgt, idx, src, axis=0):
    return tgt.clone().index_add_(axis, idx, src)

def _segment_sum(src, seg, n):
    out = torch.zeros((n,) + src.shape[1:], dtype=src.dtype)
    return out.index_add_(0, seg, src)

def _pad(x, pw):
    flat = tuple((v for pair in reversed(pw) for v in pair))
    return torch.nn.functional.pad(x, flat)
kernel_name = 'adaLN_cond__memory_aug_recurrent_mem_tr__glu_gated_swiglu__prompt'

def setup_inputs():

    _w = lambda a: (lambda t: t.to(torch.bfloat16) if t.is_floating_point() else t)(torch.as_tensor(a))

    def _A_setup():
        B, S, Dm = (1, 2048, 1024)
        H, Dh = (32, 128)
        M, seg = (8, 32)
        seg = min(seg, S)
        S = S // seg * seg
        return {'x': _w((lambda *s: torch.randn(*s, dtype=torch.bfloat16) * 0.05)(B, S, Dm)), 'mem0': _w((lambda *s: torch.randn(*s, dtype=torch.bfloat16) * 0.8)(B, M, Dm)), 'Wq': _w((lambda *s: torch.randn(*s, dtype=torch.bfloat16) * 0.8)(Dm, H, Dh) * Dm ** (-0.5)), 'Wk': _w((lambda *s: torch.randn(*s, dtype=torch.bfloat16) * 0.8)(Dm, H, Dh) * Dm ** (-0.5)), 'Wv': _w((lambda *s: torch.randn(*s, dtype=torch.bfloat16) * 0.8)(Dm, H, Dh) * Dm ** (-0.5)), 'Wo': _w((lambda *s: torch.randn(*s, dtype=torch.bfloat16) * 0.8)(H, Dh, Dm) * (H * Dh) ** (-0.5)), 'seg_len': seg}
    _A_in = _A_setup()

    def _B_setup():
        B, S, D, F = (1, 2048, 1024, 3584)
        return {'x': _w((lambda *s: torch.randn(*s, dtype=torch.bfloat16) * 0.05)(B, S, D)), 'w1': _w((lambda *s: torch.randn(*s, dtype=torch.bfloat16) * 0.4)(D, F) * (2.0 / D) ** 0.5), 'w3': _w((lambda *s: torch.randn(*s, dtype=torch.bfloat16) * 0.4)(D, F) * (2.0 / D) ** 0.5), 'w2': _w((lambda *s: torch.randn(*s, dtype=torch.bfloat16) * 0.4)(F, D) * (2.0 / F) ** 0.5)}
    _B_in = _B_setup()
    return {'A_x': _A_in['x'], 'A_mem0': _A_in['mem0'], 'A_Wq': _A_in['Wq'], 'A_Wk': _A_in['Wk'], 'A_Wv': _A_in['Wv'], 'A_Wo': _A_in['Wo'], 'A_seg_len': _A_in['seg_len'], 'B_x': _B_in['x'], 'B_w1': _B_in['w1'], 'B_w3': _B_in['w3'], 'B_w2': _B_in['w2'], 'c': _w((lambda *s: torch.randn(*s, dtype=torch.bfloat16) * 0.1)(1, 1024) * 8.0), 'w_mod': _w((lambda *s: torch.randn(*s, dtype=torch.bfloat16) * 0.1)(1024, 6 * 1024) * 1024 ** (-0.5) * 8.0)}

def reference(A_x, A_mem0, A_Wq, A_Wk, A_Wv, A_Wo, A_seg_len, B_x, B_w1, B_w3, B_w2, c, w_mod):

    def _A_ref(x, mem0, Wq, Wk, Wv, Wo, seg_len):
        B, S, Dm = x.shape
        H, Dh = (Wq.shape[1], Wq.shape[2])
        M = mem0.shape[1]
        scale = Dh ** (-0.5)
        n_seg = S // seg_len
        L = M + seg_len
        idx = torch.arange(L)
        mask = (idx[:, None] < M) | (idx[:, None] >= idx[None, :])
        mem = mem0
        outs = []
        for s in range(n_seg):
            xs = x[:, s * seg_len:(s + 1) * seg_len, :].clone()
            h = torch.concatenate([mem, xs], axis=1)
            q = torch.einsum('btd,dhe->bhte', h, Wq)
            k = torch.einsum('btd,dhe->bhte', h, Wk)
            v = torch.einsum('btd,dhe->bhte', h, Wv)
            scores = torch.einsum('bhqe,bhke->bhqk', q, k) * scale
            scores = torch.where(mask[None, None], scores, -1000000000.0)
            p = _softmax(scores, axis=-1)
            o = torch.einsum('bhqk,bhke->bhqe', p, v)
            o = torch.einsum('bhte,hed->btd', o, Wo)
            mem = o[:, :M, :].clone()
            outs.append(o[:, M:, :])
        return torch.concatenate(outs, axis=1)

    def _B_ref(x, w1, w3, w2):
        gate = torch.einsum('bsd,df->bsf', x, w1)
        up = torch.einsum('bsd,df->bsf', x, w3)
        h = _silu(gate) * up
        out = torch.einsum('bsf,fd->bsd', h, w2)
        return out
    x = A_x
    mod = (_silu(c) @ w_mod).reshape(c.shape[0], 6, 1, -1)
    sa, ca, ga, sb, cb, gb = (mod[:, 0].clone(), mod[:, 1].clone(), mod[:, 2].clone(), mod[:, 3].clone(), mod[:, 4].clone(), mod[:, 5].clone())
    h = _layernorm(x, axis=-1) * (1 + ca) + sa
    x = x + ga * _A_ref(h, A_mem0, A_Wq, A_Wk, A_Wv, A_Wo, A_seg_len)
    h = _layernorm(x, axis=-1) * (1 + cb) + sb
    x = x + gb * _B_ref(h, B_w1, B_w3, B_w2)
    return x
```

```cpp
#include <hip/hip_runtime.h>
#include <hip/hip_bf16.h>
#include <stdint.h>
#include <type_traits>
#pragma clang diagnostic ignored "-Winline-asm"
#pragma clang diagnostic ignored "-Wunused-value"

typedef unsigned short u16;
typedef short s16x4 __attribute__((ext_vector_type(4)));
typedef __bf16 bf16x8 __attribute__((ext_vector_type(8)));
typedef float f32x4 __attribute__((ext_vector_type(4)));
typedef float f32x2 __attribute__((ext_vector_type(2)));
typedef unsigned u32x4 __attribute__((ext_vector_type(4)));
typedef unsigned u32x2 __attribute__((ext_vector_type(2)));
#define LAS __attribute__((address_space(3)))

constexpr int SEQ = 2048, DM = 1024, NH = 32, DH = 128, HD = 4096, NMEM = 8, SEG = 32, NSEG = 64, FF = 3584;
constexpr unsigned SENT = 0xFFFFFFFFu;

__device__ __forceinline__ int lane_id() { int l; asm volatile("v_mbcnt_lo_u32_b32 %0, -1, 0\n\tv_mbcnt_hi_u32_b32 %0, -1, %0" : "=v"(l)); return l; }
#define WAVE_ID wave_id_
#define opaque_tid() (WAVE_ID * 64 + lane_id())
__device__ __forceinline__ float bf2f(u16 v) { return __builtin_bit_cast(float, (unsigned)v << 16); }
__device__ __forceinline__ float bflo(unsigned v) { return __builtin_bit_cast(float, v << 16); }
__device__ __forceinline__ float bfhi(unsigned v) { return __builtin_bit_cast(float, v & 0xffff0000u); }
__device__ __forceinline__ float bfr(float x) { return (float)(__bf16)x; }
__device__ __forceinline__ unsigned pack2(float a, float b) {
  typedef __bf16 bf16x2 __attribute__((ext_vector_type(2)));
  bf16x2 v; v[0] = (__bf16)a; v[1] = (__bf16)b; return __builtin_bit_cast(unsigned, v);
}
__device__ __forceinline__ unsigned nosent(unsigned v) { return v == SENT ? 0x7FC07FC0u : v; }
__device__ __forceinline__ bf16x8 asbf8(u32x4 v) { return __builtin_bit_cast(bf16x8, v); }
__device__ __forceinline__ s16x4 ass4(u32x2 v) { return __builtin_bit_cast(s16x4, v); }
__device__ __forceinline__ u32x2 trread(const char* lds_base, unsigned off) {
  s16x4 v = __builtin_amdgcn_ds_read_tr16_b64_v4i16((LAS s16x4*)(lds_base + off));
  return __builtin_bit_cast(u32x2, v);
}
__device__ __forceinline__ void glds16(const void* gsrc, unsigned lds_dst) {
  unsigned keep;
  asm volatile("s_mov_b32 %0, m0\n\ts_mov_b32 m0, %2\n\ts_nop 0\n\tglobal_load_lds_dwordx4 %1, off\n\ts_mov_b32 m0, %0"
               : "=&s"(keep) : "v"(gsrc), "s"(lds_dst) : "memory");
}
#define CNT_ " nt"
#define WNT_ " nt"
__device__ __forceinline__ void glds16_w(const void* gsrc, unsigned lds_dst) {
  unsigned keep;
  asm volatile("s_mov_b32 %0, m0\n\ts_mov_b32 m0, %2\n\ts_nop 0\n\tglobal_load_lds_dwordx4 %1, off" CNT_ "\n\ts_mov_b32 m0, %0"
               : "=&s"(keep) : "v"(gsrc), "s"(lds_dst) : "memory");
}
__device__ __forceinline__ void glds16_sc1(const void* gsrc, unsigned lds_dst) {
  unsigned keep;
  asm volatile("s_mov_b32 %0, m0\n\ts_mov_b32 m0, %2\n\ts_nop 0\n\tglobal_load_lds_dwordx4 %1, off sc1\n\ts_mov_b32 m0, %0"
               : "=&s"(keep) : "v"(gsrc), "s"(lds_dst) : "memory");
}
#define NTM 1
template <int B> __device__ __forceinline__ u32x4 ldnt(const void* p) { if (NTM & B) return __builtin_nontemporal_load((const u32x4*)p); else return *(const u32x4*)p; }
#define MFMA32(a, b, c) __builtin_amdgcn_mfma_f32_16x16x32_bf16((a), (b), (c), 0, 0, 0)

__device__ __forceinline__ void st16_sc1(void* p, u32x4 v) { asm volatile("global_store_dwordx4 %0, %1, off sc1\n\ts_nop 1" :: "v"(p), "v"(v) : "memory"); }
__device__ __forceinline__ void st8_sc1(void* p, u32x2 v) { asm volatile("global_store_dwordx2 %0, %1, off sc1\n\ts_nop 1" :: "v"(p), "v"(v) : "memory"); }
template <bool WT> __device__ __forceinline__ void st8(void* p, u32x2 v) { if (WT) st8_sc1(p, v); else *(u32x2*)p = v; }
template <bool WT> __device__ __forceinline__ void st16(void* p, u32x4 v) { if (WT) st16_sc1(p, v); else *(u32x4*)p = v; }
__device__ __forceinline__ u32x4 ld16_sc1(const void* p) {
  u32x4 r; asm volatile("global_load_dwordx4 %0, %1, off sc1\n\ts_waitcnt vmcnt(0)" : "=&v"(r) : "v"(p) : "memory"); return r;
}
__device__ __forceinline__ void ld16x4_sc1(const void* p, u32x4& a, u32x4& b, u32x4& c, u32x4& d) {
  asm volatile("global_load_dwordx4 %0, %4, off sc1\n\tglobal_load_dwordx4 %1, %4, off offset:1024 sc1\n\t"
               "global_load_dwordx4 %2, %4, off offset:2048 sc1\n\tglobal_load_dwordx4 %3, %4, off offset:3072 sc1\n\ts_waitcnt vmcnt(0)"
               : "=&v"(a), "=&v"(b), "=&v"(c), "=&v"(d) : "v"(p) : "memory");
}
typedef __amdgpu_buffer_rsrc_t rsrc_t;
__device__ __forceinline__ rsrc_t mkrs(const void* p) { return __builtin_amdgcn_make_buffer_rsrc((void*)p, 0, 0x7FFFFFFF, 0x00020000); }
__device__ __forceinline__ u32x4 bld16(rsrc_t r, unsigned off) { return __builtin_amdgcn_raw_buffer_load_b128(r, off, 0, 16); }
__device__ __forceinline__ u32x2 bld8(rsrc_t r, unsigned off) { return __builtin_amdgcn_raw_buffer_load_b64(r, off, 0, 16); }
__device__ __forceinline__ bool ok4(u32x4 v) { return v.x != SENT && v.y != SENT && v.z != SENT && v.w != SENT; }
constexpr int SPIN_CAP = 2000000;
__device__ __forceinline__ bool give_up(int spins, unsigned* tmo, unsigned code) {
  if ((spins & 4095) != 4095) return false;
  if (spins > SPIN_CAP) { __hip_atomic_store(tmo, code, __ATOMIC_RELAXED, __HIP_MEMORY_SCOPE_AGENT); return true; }
  return __hip_atomic_load(tmo, __ATOMIC_RELAXED, __HIP_MEMORY_SCOPE_AGENT) != 0u;
}
template <bool ACQ = true>
__device__ __forceinline__ void wait_cnt(unsigned* c, unsigned n, unsigned* tmo) {
  for (int spins = 0;; ++spins) {
    const unsigned v = __hip_atomic_load(c, __ATOMIC_RELAXED, __HIP_MEMORY_SCOPE_AGENT);
    if (v >= n) break;
    __builtin_amdgcn_s_sleep(8);
    if (give_up(spins, tmo, 5)) break;
  }
  if (ACQ) {
    __builtin_amdgcn_fence(__ATOMIC_ACQUIRE, "agent");
    asm volatile("s_waitcnt vmcnt(0)" ::: "memory");
  }
}

__device__ __forceinline__ const char* uni(const char* p) {
  const unsigned long long v = (unsigned long long)p;
  const unsigned lo = __builtin_amdgcn_readfirstlane((unsigned)v), hi = __builtin_amdgcn_readfirstlane((unsigned)(v >> 32));
  return (const char*)(((unsigned long long)hi << 32) | lo);
}
#define TSCLK "s_memrealtime"
#define TC(k) do { } while (0)
#define TS(k) do { } while (0)
#define TSDECL do { } while (0)
#define TSFLUSH(mask) do { } while (0)

constexpr size_t OFF_MOD  = 0;
constexpr size_t OFF_MOD2 = 65536;
constexpr size_t OFF_TMO  = 98304;
constexpr size_t OFF_CAL  = 98304 + 256;
constexpr int    NCALW    = 16;
constexpr size_t OFF_CNT  = 131072;
constexpr int    NCNT     = 128;
constexpr size_t OFF_H1   = 262144;
constexpr size_t OFF_Q    = OFF_H1 + (size_t)SEQ * DM * 2;
constexpr size_t OFF_K    = OFF_Q + (size_t)SEQ * HD * 2;
constexpr size_t OFF_V    = OFF_K + (size_t)SEQ * HD * 2;
constexpr size_t OFF_O    = OFF_V + (size_t)SEQ * HD * 2;
constexpr size_t OFF_X1   = OFF_O + (size_t)SEQ * HD * 2;
constexpr size_t OFF_H2   = OFF_X1 + (size_t)SEQ * DM * 2;
constexpr size_t OFF_G    = OFF_H2 + (size_t)SEQ * DM * 2;
constexpr size_t OFF_KVM  = (size_t)92 << 20;
constexpr size_t OFF_XCH  = (size_t)100 << 20;
constexpr size_t OFF_P    = (size_t)112 << 20;
constexpr size_t OFF_P2   = (size_t)128 << 20;
constexpr size_t OFF_DUMP = (size_t)144 << 20;
constexpr int PROW = 4 * DM * 2;
#define TLQ 1
#define TLP 1
static_assert(OFF_G + (size_t)SEQ * FF * 2 <= OFF_KVM, "layout");
__device__ __forceinline__ char* xch_base(char* ws) { return (char*)(((size_t)(ws + OFF_XCH) + ((2u << 20) - 1)) & ~(size_t)((2u << 20) - 1)); }
#define XPAR 0
#define XPARF(sum) ((size_t)(((sum) > 0 ? 0 : 1) ^ XPAR))
constexpr int TICK = 0, ARMED = 8;
constexpr int PROG = 16;
constexpr int PQKV = 64;
constexpr int PL1 = 96;
constexpr int PSA = 128;
constexpr int PWO = 192;
constexpr int PLN = 224;
constexpr int PF2 = 320;
constexpr int PF1 = 448;
#define SDIR 56
#define SAW 0
constexpr int L_RED = 131072;
constexpr int L_SLOT = L_RED + 16384;
constexpr int L_GB = L_SLOT + 1024;
constexpr int MEGA_LDS = 163840;
static_assert(L_GB + 12288 + 2048 <= MEGA_LDS, "lds");
constexpr int NMODB = 256;
constexpr int NCALB = 3 * NCALW;
#define XPARC 0

__device__ __forceinline__ int bswz(int row) { return (((row >> 1) & 1) | (((row >> 3) & 1) << 1)) << 1; }
template <int OFF, bool SC1 = false>
__device__ __forceinline__ void glds16s(const char* sbase, unsigned voff, unsigned lds_base) {
  unsigned keep;
  if (SC1)
    asm volatile("s_mov_b32 %0, m0\n\ts_add_u32 m0, %3, %4\n\ts_nop 0\n\tglobal_load_lds_dwordx4 %1, %2 sc1\n\ts_mov_b32 m0, %0"
                 : "=&s"(keep) : "v"(voff), "s"(sbase), "s"(lds_base), "i"(OFF) : "memory", "scc");
  else
    asm volatile("s_mov_b32 %0, m0\n\ts_add_u32 m0, %3, %4\n\ts_nop 0\n\tglobal_load_lds_dwordx4 %1, %2\n\ts_mov_b32 m0, %0"
                 : "=&s"(keep) : "v"(voff), "s"(sbase), "s"(lds_base), "i"(OFF) : "memory", "scc");
}
template <int OFF>
__device__ __forceinline__ void glds16x2s(const char* sbase, unsigned voff, unsigned lds_base) {
  unsigned keep;
  asm volatile("s_mov_b32 %0, m0\n\ts_add_u32 m0, %3, %4\n\ts_nop 0\n\tglobal_load_lds_dwordx4 %1, %2 sc1\n\tglobal_load_lds_dwordx4 %1, %2 offset:1024 sc1\n\ts_mov_b32 m0, %0"
               : "=&s"(keep) : "v"(voff), "s"(sbase), "s"(lds_base), "i"(OFF) : "memory", "scc");
}
__device__ __forceinline__ void st16s(const char* sb, unsigned vo, u32x4 v) { asm volatile("global_store_dwordx4 %0, %1, %2 sc1\n\ts_nop 1" :: "v"(vo), "v"(v), "s"(sb) : "memory"); }
__device__ __forceinline__ void head_to_lds(const char* src, int lane, unsigned lds) {
  if (TLQ) {
    const int c = (lane & 15) ^ (lane >> 4);
    const unsigned v0 = (unsigned)((c >> 2) * 2048 + (lane >> 4) * 64 + (c & 3) * 16);
#pragma unroll
    for (int i = 0; i < 8; ++i) glds16s<0, true>(src + i * 256, v0 ^ ((i & 3) * 2048), lds + i * 1024);
  } else {
#pragma unroll
    for (int i = 0; i < 4; ++i) glds16x2s<0>(src + i * 2048, (unsigned)lane * 16, lds + i * 2048);
  }
}
#define WAIT_V(n) asm volatile("s_waitcnt vmcnt(" #n ")" ::: "memory")
#define BAR() do { asm volatile("s_waitcnt lgkmcnt(0)" ::: "memory"); __builtin_amdgcn_s_barrier(); asm volatile("" ::: "memory"); } while (0)
__device__ __forceinline__ void st4_sc1(void* p, unsigned v) { asm volatile("global_store_dword %0, %1, off sc1" :: "v"(p), "v"(v) : "memory"); }
__device__ __forceinline__ unsigned ld4_sc1(const void* p) {
  unsigned r; asm volatile("global_load_dword %0, %1, off sc1\n\ts_waitcnt vmcnt(0)" : "=&v"(r) : "v"(p) : "memory"); return r;
}
template <int CTRL> __device__ __forceinline__ unsigned dppu(unsigned x) { return (unsigned)__builtin_amdgcn_update_dpp(0, (int)x, CTRL, 0xf, 0xf, false); }
__device__ __forceinline__ unsigned rowmin16(unsigned v) {
  v = min(v, dppu<0x128>(v)); v = min(v, dppu<0x124>(v)); v = min(v, dppu<0x122>(v)); v = min(v, dppu<0x121>(v));
  return v;
}
__device__ __forceinline__ unsigned rdl(unsigned v, int l) { return (unsigned)__builtin_amdgcn_readlane((int)v, l); }
__device__ __forceinline__ unsigned qkv_avail(unsigned v, int m) {
  unsigned a = rdl(v, m);
#pragma unroll
  for (int j = 1; j < 4; ++j) { const unsigned p = rdl(v, 4 * j + m); if ((p >> 8) <= a) a = max(a, p & 255u); }
  return a;
}
constexpr float SCALE = 0.08838834764831845f;
constexpr float LOG2E = 1.4426950408889634f;
template <int N> __device__ __forceinline__ void delay64() {
  if (N >= 16) { __builtin_amdgcn_s_sleep(15); delay64<(N >= 16 ? N - 15 : 0)>(); } else if (N > 0) __builtin_amdgcn_s_sleep(N);
}
__device__ __forceinline__ void sw16(float x, float& a, float& b) {
  a = x; b = x; asm volatile("s_nop 1\n\tv_permlane16_swap_b32 %0, %1" : "+v"(a), "+v"(b));
}
__device__ __forceinline__ void sw32(float x, float& a, float& b) {
  a = x; b = x; asm volatile("s_nop 1\n\tv_permlane32_swap_b32 %0, %1" : "+v"(a), "+v"(b));
}
__device__ __forceinline__ float red16_32_max(float x) {
  float a, b; sw16(x, a, b); x = fmaxf(a, b); sw32(x, a, b); return fmaxf(a, b);
}
__device__ __forceinline__ float red16_32_add(float x) {
  float a, b; sw16(x, a, b); x = a + b; sw32(x, a, b); return a + b;
}
__device__ __forceinline__ unsigned cvtpk(float a, float b) { unsigned r; asm("v_cvt_pk_bf16_f32 %0, %1, %2" : "=v"(r) : "v"(a), "v"(b)); return r; }
__device__ __forceinline__ float max3(float a, float b, float c) { float r; asm("v_max3_f32 %0, %1, %2, %3" : "=v"(r) : "v"(a), "v"(b), "v"(c)); return r; }
__device__ __forceinline__ float max2(float a, float b) { float r; asm("v_max_f32 %0, %1, %2" : "=v"(r) : "v"(a), "v"(b)); return r; }
__device__ __forceinline__ unsigned ror8u(unsigned x) { return (unsigned)__builtin_amdgcn_update_dpp(0, (int)x, 0x128, 0xf, 0xf, false); }
__device__ __forceinline__ float ror8(float x) { return __builtin_bit_cast(float, ror8u(__builtin_bit_cast(unsigned, x))); }
__device__ __forceinline__ float add_ror8(float x) {
  return x + __builtin_bit_cast(float, __builtin_amdgcn_update_dpp(0, __builtin_bit_cast(int, x), 0x128, 0xf, 0xf, false));
}

__device__ __forceinline__ void wait_l1(const unsigned* cnt, unsigned n, unsigned* tmo) {
  const int lane = lane_id();
  for (int spins = 0;; ++spins) {
    const unsigned v = ld4_sc1(cnt + (PL1 + (lane & 31)) * 32);
    if (__all(v >= n)) break;
    __builtin_amdgcn_s_sleep(8);
    if (give_up(spins, tmo, 12)) break;
  }
}
#define GKV 1
__device__ __forceinline__ void wait_k0(const unsigned* cnt, int h, unsigned* tmo) {
  for (int spins = 0;; ++spins) {
    const int l = lane_id();
    const unsigned v = ld4_sc1(cnt + (PQKV + h) * 32 + (l == 0 ? 16 : 1 + (l & 1)));
    if (__all(v >= (l == 0 ? 1u : (unsigned)GKV))) break;
    __builtin_amdgcn_s_sleep(32);
    if (give_up(spins, tmo, 13)) break;
  }
}
#define WCLK(slot, b) do { } while (0)
#define WTM(slot, b, k) do { } while (0)
#define WTS(slot, b, k) do { } while (0)
#define WFB() do { } while (0)
#define STS(ws_, slot_, k_) do { } while (0)
typedef __bf16 bf16x2 __attribute__((ext_vector_type(2)));
__device__ __forceinline__ f32x2 pkfma(f32x2 a, f32x2 b, f32x2 c) { return __builtin_elementwise_fma(a, b, c); }
__device__ __forceinline__ float wavesum(float v) {
  v += __builtin_bit_cast(float, dppu<0x128>(__builtin_bit_cast(unsigned, v))); v += __builtin_bit_cast(float, dppu<0x124>(__builtin_bit_cast(unsigned, v)));
  v += __builtin_bit_cast(float, dppu<0x122>(__builtin_bit_cast(unsigned, v))); v += __builtin_bit_cast(float, dppu<0x121>(__builtin_bit_cast(unsigned, v)));
  return red16_32_add(v);
}
__device__ __forceinline__ void bst16(rsrc_t r, unsigned off, unsigned soff, u32x4 v) { __builtin_amdgcn_raw_buffer_store_b128(v, r, off + soff, 0, 16); }
__device__ __forceinline__ float adds(unsigned v, unsigned sel, float c) { return __builtin_amdgcn_fdot2_f32_bf16(__builtin_bit_cast(bf16x2, v), __builtin_bit_cast(bf16x2, sel), c, false); }
__device__ __forceinline__ void sum4(const u32x4 (&p)[4], f32x2 (&a)[4]) {
  unsigned slo = 0x00003F80u, shi = 0x3F800000u;
  asm("" : "+s"(slo), "+s"(shi));
#pragma unroll
  for (int e = 0; e < 4; ++e) {
    float lo = bflo(p[0][e]), hi = bfhi(p[0][e]);
#pragma unroll
    for (int q = 1; q < 4; ++q) { lo = adds(p[q][e], slo, lo); hi = adds(p[q][e], shi, hi); }
    a[e] = f32x2{lo, hi};
  }
}
struct RowSet { u32x4 p[2][4]; u32x4 rv[2]; };
#define XAUX 16
__device__ __forceinline__ void row_issue2(RowSet& S, rsrc_t rp, rsrc_t rr, int row, unsigned l16) {
#pragma unroll
  for (int i = 0; i < 2; ++i) {
#pragma unroll
    for (int q = 0; q < 4; ++q) S.p[i][q] = TLP ? __builtin_amdgcn_raw_buffer_load_b128(rp, (l16 >> 6) * 2048 + (l16 & 48) + i * 32768, (unsigned)((row >> 5) * 32 * PROW + q * 65536 + (row & 31) * 64), 16)
                                                 : __builtin_amdgcn_raw_buffer_load_b128(rp, l16 + i * 1024, (unsigned)(row * PROW + q * 2048), 16);
    S.rv[i] = __builtin_amdgcn_raw_buffer_load_b128(rr, l16 + i * 1024, (unsigned)(row * DM) * 2, XAUX);
  }
}
__device__ __forceinline__ void row_a(const RowSet& S, f32x2 (&a)[2][4], u32x4 (&xv)[2]) {
#pragma unroll
  for (int i = 0; i < 2; ++i) { sum4(S.p[i], a[i]); xv[i] = S.rv[i]; }
#pragma unroll
  for (int i = 0; i < 2; ++i) asm volatile("" : "+v"(a[i][0]), "+v"(a[i][1]), "+v"(a[i][2]), "+v"(a[i][3]), "+v"(xv[i]));
}
__device__ __forceinline__ u32x4 resid8(const f32x2 (&a)[4], u32x4 xv, const char* mt, f32x2 (&v)[4]) {
  const f32x4 g0 = *(const f32x4*)mt, g1 = *(const f32x4*)(mt + 1024);
  u32x4 o;
  o[0] = [&] { const f32x2 s = pkfma(f32x2{g0[0], g0[1]}, a[0], f32x2{bflo(xv[0]), bfhi(xv[0])}); return pack2(s[0], s[1]); }();
  o[1] = [&] { const f32x2 s = pkfma(f32x2{g0[2], g0[3]}, a[1], f32x2{bflo(xv[1]), bfhi(xv[1])}); return pack2(s[0], s[1]); }();
  o[2] = [&] { const f32x2 s = pkfma(f32x2{g1[0], g1[1]}, a[2], f32x2{bflo(xv[2]), bfhi(xv[2])}); return pack2(s[0], s[1]); }();
  o[3] = [&] { const f32x2 s = pkfma(f32x2{g1[2], g1[3]}, a[3], f32x2{bflo(xv[3]), bfhi(xv[3])}); return pack2(s[0], s[1]); }();
#pragma unroll
  for (int e = 0; e < 4; ++e) v[e] = f32x2{bflo(o[e]), bfhi(o[e])};
  return o;
}
__device__ __forceinline__ u32x4 norm8(const f32x2 (&v)[4], const char* mt, float mb, float rden) {
  const f32x4 h0 = *(const f32x4*)(mt + 4096), h1 = *(const f32x4*)(mt + 4096 + 1024), c0 = *(const f32x4*)(mt + 8192), c1 = *(const f32x4*)(mt + 8192 + 1024);
  const f32x2 m2 = f32x2{mb, mb}, d2 = f32x2{rden, rden};
  const f32x2 r0 = pkfma(v[0] - m2, f32x2{c0[0], c0[1]} * d2, f32x2{h0[0], h0[1]}), r1 = pkfma(v[1] - m2, f32x2{c0[2], c0[3]} * d2, f32x2{h0[2], h0[3]});
  const f32x2 r2 = pkfma(v[2] - m2, f32x2{c1[0], c1[1]} * d2, f32x2{h1[0], h1[1]}), r3 = pkfma(v[3] - m2, f32x2{c1[2], c1[3]} * d2, f32x2{h1[2], h1[3]});
  return u32x4{pack2(r0[0], r0[1]), pack2(r1[0], r1[1]), pack2(r2[0], r2[1]), pack2(r3[0], r3[1])};
}
__device__ __forceinline__ void row_b(const f32x2 (&a)[2][4], const u32x4 (&xv)[2], const char* ml, rsrc_t rx1, rsrc_t rh2, int row, int lane) {
  f32x2 v[2][4];
  unsigned mo = lane * 16; asm volatile("" : "+v"(mo));
  ml += mo;
#pragma unroll
  for (int i = 0; i < 2; ++i) bst16(rx1, mo + i * 1024, (unsigned)(row * DM) * 2, resid8(a[i], xv[i], ml + i * 2048, v[i]));
  const f32x2 s2 = ((v[0][0] + v[0][1]) + (v[0][2] + v[0][3])) + ((v[1][0] + v[1][1]) + (v[1][2] + v[1][3]));
  const float mean = wavesum(s2[0] + s2[1]) * (1.0f / DM);
  f32x2 q2 = f32x2{0, 0};
#pragma unroll
  for (int i = 0; i < 8; ++i) { const f32x2 d = v[i >> 2][i & 3] - f32x2{mean, mean}; q2 = pkfma(d, d, q2); }
  const float q = wavesum(q2[0] + q2[1]);
  const float mb = bfr(mean), vb = bfr(q * (1.0f / DM));
  const float rden = 1.0f / bfr(sqrtf(bfr(vb + 1e-5f)));
  const int rb = row & 31;
  const unsigned hd = (unsigned)((((lane & 7) ^ (rb & 7)) << 4) + (lane >> 3) * 4096);
#pragma unroll
  for (int i = 0; i < 2; ++i) bst16(rh2, hd, (unsigned)((row >> 5) * 65536 + rb * 128 + i * 32768), norm8(v[i], ml + i * 2048, mb, rden));
}
__device__ __forceinline__ void half_load(const char* P, const u16* res, int row, int hf, int lane, f32x2 (&a)[4], u32x4& rv) {
  const rsrc_t rp = mkrs(P), rr = mkrs(res);
  const unsigned po = TLP ? (unsigned)((row >> 5) * 32 * PROW + (hf * 16 + (lane >> 2)) * 2048 + (row & 31) * 64 + (lane & 3) * 16) : (unsigned)(row * PROW + hf * 1024 + lane * 16), ro = (unsigned)(row * DM * 2 + hf * 1024 + lane * 16);
  u32x4 p[4];
#pragma unroll
  for (int q = 0; q < 4; ++q) p[q] = bld16(rp, po + q * (TLP ? 65536 : 2048));
  if (res) rv = bld16(rr, ro);
  sum4(p, a);
}
__device__ __forceinline__ void fin_half(const char* P, const u16* res, const char* gbt, u16* __restrict__ o1, int row, int hf, int lane) {
  f32x2 a[4]; u32x4 rv;
  half_load(P, res, row, hf, lane, a, rv);
  const u32x4 gv = *(const u32x4*)(gbt + hf * 1024 + lane * 16);
  u32x4 o;
#pragma unroll
  for (int e = 0; e < 4; ++e) { const f32x2 s = pkfma(f32x2{bflo(gv[e]), bfhi(gv[e])}, a[e], f32x2{bflo(rv[e]), bfhi(rv[e])}); o[e] = pack2(s[0], s[1]); }
  st16_sc1(o1 + (size_t)row * DM + hf * 512 + lane * 8, o);
}
template <int CTRL> __device__ __forceinline__ float dppf(float x) { return __builtin_bit_cast(float, dppu<CTRL>(__builtin_bit_cast(unsigned, x))); }
__device__ __forceinline__ void ln_row(const f32x2 (&a)[2][4], const f32x2 (&xf)[8], const f32x2 (&tg)[8], const f32x2 (&th)[8], const f32x2 (&tc)[8],
                                       const char* x1, const char* h2, unsigned xo, unsigned ho) {
  f32x2 v[8]; u32x4 o[2];
#pragma unroll
  for (int e = 0; e < 8; ++e) { const f32x2 t = pkfma(tg[e], a[e >> 2][e & 3], xf[e]); const unsigned u = pack2(t[0], t[1]); o[e >> 2][e & 3] = u; v[e] = f32x2{bflo(u), bfhi(u)}; }
  const float pv = __builtin_bit_cast(float, __builtin_amdgcn_readfirstlane(__builtin_bit_cast(int, v[0][0])));
  f32x2 d[8];
#pragma unroll
  for (int e = 0; e < 8; ++e) d[e] = v[e] - f32x2{pv, pv};
  const f32x2 s2 = ((d[0] + d[1]) + (d[2] + d[3])) + ((d[4] + d[5]) + (d[6] + d[7]));
  const f32x2 q2 = pkfma(d[3], d[3], pkfma(d[2], d[2], pkfma(d[1], d[1], d[0] * d[0]))) + pkfma(d[7], d[7], pkfma(d[6], d[6], pkfma(d[5], d[5], d[4] * d[4])));
  float s = s2[0] + s2[1], q = q2[0] + q2[1];
  s += dppf<0x128>(s); q += dppf<0x128>(q); s += dppf<0x124>(s); q += dppf<0x124>(q);
  s += dppf<0x122>(s); q += dppf<0x122>(q); s += dppf<0x121>(s); q += dppf<0x121>(q);
  asm volatile("s_nop 1\n\tv_permlane16_swap_b32 %0, %1" : "+v"(s), "+v"(q));
  s += q;
  { float x, y; sw32(s, x, y); s = x + y; }
  const float sh = __builtin_bit_cast(float, rdl(__builtin_bit_cast(unsigned, s), 0)) * (1.0f / DM), sq = __builtin_bit_cast(float, rdl(__builtin_bit_cast(unsigned, s), 16));
  const float mb = bfr(pv + sh), vb = bfr(fmaxf(sq * (1.0f / DM) - sh * sh, 0.0f));
  const float rden = __builtin_amdgcn_rcpf(bfr(__builtin_amdgcn_sqrtf(bfr(vb + 1e-5f))));
#pragma unroll
  for (int i = 0; i < 2; ++i) {
    u32x4 r;
#pragma unroll
    for (int e = 0; e < 4; ++e) { const f32x2 t = pkfma(v[4 * i + e] - f32x2{mb, mb}, tc[4 * i + e] * f32x2{rden, rden}, th[4 * i + e]); r[e] = pack2(t[0], t[1]); }
    st16s(h2, ho + i * 32768, r);
  }
  st16s(x1, xo, o[0]); st16s(x1, xo + 1024, o[1]);
}
__device__ __forceinline__ void row_table(char* tab, const float* mod, const unsigned* cnt, unsigned* tmo, int tid) {
  wait_l1(cnt, 9u, tmo);
  const rsrc_t rm = mkrs((const char*)mod + OFF_MOD2);
#pragma unroll
  for (int i = 0; i < 2; ++i) {
    const int u = tid + 512 * i, a = u >> 8, c = u & 255;
    f32x4 v = __builtin_bit_cast(f32x4, bld16(rm, (unsigned)u * 16));
    v = f32x4{bfr(v[0]), bfr(v[1]), bfr(v[2]), bfr(v[3])};
    if (a == 2) v = f32x4{bfr(1.0f + v[0]), bfr(1.0f + v[1]), bfr(1.0f + v[2]), bfr(1.0f + v[3])};
    if (a < 3) *(f32x4*)(tab + a * 4096 + ((c >> 7) * 2 + (c & 1)) * 1024 + ((c >> 1) & 63) * 16) = v;
    else *(u32x2*)(tab + 12288 + c * 8) = u32x2{pack2(v[0], v[1]), pack2(v[2], v[3])};
  }
  __syncthreads();
}
#define LNB 48
__device__ __forceinline__ void row_role(char* smem, int wave_id_, int rbase, int par, const float* mod, const char* P, const u16* x, u16* x1, char* h2c,
                                         unsigned* cnt, unsigned* tmo, int slot, unsigned long long* mts) {
  const int tid = opaque_tid(), lane = tid & 63, wave = WAVE_ID;
  if (tid < 4) *(volatile LAS unsigned*)(smem + 16384 + tid * 4) = 0u;
  row_table(smem, mod, cnt, tmo, tid);
  const int rb = rbase + wave + 32 * par;
  const rsrc_t rx1 = mkrs(x1), rh2 = mkrs(h2c), rc = mkrs(cnt), rp = mkrs(P), rr = mkrs(x);
  const unsigned l16 = lane * 16;
  const unsigned pwo = (unsigned)(PWO + (lane & 31)) * 128;
  unsigned* myp = cnt + (PLN + (rb >> 3)) * 32 + wave;
  auto eval = [&](unsigned v) { v = rowmin16(v); return (int)((min(min(rdl(v, 0), rdl(v, 16)), (unsigned)LNB) + 1u - par) >> 1); };
  auto wmk = [&](int n) { return (unsigned)min(2 * n + par, LNB); };
  if (par && lane == 0) st4_sc1(myp, 1u);
  int b = 0, wa = 0;
  f32x2 a[2][4]; u32x4 xv[2];
  while (b < LNB / 2) {
    if (b >= wa) {
      for (int spins = 0;; ++spins) {
        wa = eval(ld4_sc1((const char*)cnt + pwo));
        if (b < wa) break;
        __builtin_amdgcn_s_sleep(2);
        if (give_up(spins, tmo, 11)) { wa = LNB / 2; break; }
      }
    }
    if ((b & 3) == 0 || b >= 28) { WTM(slot, 2 * b + par, 0); }
    if (wa >= b + 8) {
      RowSet S[4];
      const int b0 = b;
      asm volatile("; deep" ::: "memory");
#pragma unroll
      for (int i = 0; i < 4; ++i) row_issue2(S[i], rp, rr, (b + i) * 64 + rb, l16);
      while (b + 7 < wa) {
        const unsigned smp = __builtin_amdgcn_raw_buffer_load_b32(rc, pwo, 0, 16);
#pragma unroll
        for (int i = 0; i < 4; ++i) {
          row_a(S[i], a, xv);
          WAIT_V(42);
          if (lane == 0 && b > b0) st4_sc1(myp, wmk((b + i - 3)));
          __builtin_amdgcn_sched_barrier(0);
          row_issue2(S[i], rp, rr, (b + 4 + i) * 64 + rb, l16);
          __builtin_amdgcn_sched_barrier(0);
          row_b(a, xv, smem, rx1, rh2, (b + i) * 64 + rb, lane);
          __builtin_amdgcn_sched_barrier(0);
        }
        const int w = eval(smp);
        wa = w > wa ? w : wa;
        b += 4;
      }
#pragma unroll
      for (int i = 0; i < 4; ++i) {
        row_a(S[i], a, xv);
        __builtin_amdgcn_sched_barrier(0);
        row_b(a, xv, smem, rx1, rh2, (b + i) * 64 + rb, lane);
        WAIT_V(4);
        if (lane == 0) st4_sc1(myp, wmk((b + i)));
        __builtin_amdgcn_sched_barrier(0);
      }
      WAIT_V(0);
      b += 4;
      if (lane == 0) st4_sc1(myp, wmk(b));
      continue;
    }
    if (wa < b + 2) {
      RowSet T;
      asm volatile("; single" ::: "memory");
      row_issue2(T, rp, rr, b * 64 + rb, l16);
      row_a(T, a, xv);
      row_b(a, xv, smem, rx1, rh2, b * 64 + rb, lane);
      WAIT_V(0);
      ++b;
      if (lane == 0) st4_sc1(myp, wmk(b));
      WTM(slot, 2 * (b - 1) + par, 2);
      continue;
    }
    RowSet S0, S1;
    const int b0 = b;
    asm volatile("; multi" ::: "memory");
    row_issue2(S0, rp, rr, b * 64 + rb, l16);
    row_issue2(S1, rp, rr, (b + 1) * 64 + rb, l16);
    while (b + 3 < wa) {
      const unsigned smp = __builtin_amdgcn_raw_buffer_load_b32(rc, pwo, 0, 16);
      row_a(S0, a, xv);
      WAIT_V(15);
      if (lane == 0 && b > b0) st4_sc1(myp, wmk((b - 1)));
      __builtin_amdgcn_sched_barrier(0);
      row_issue2(S0, rp, rr, (b + 2) * 64 + rb, l16);
      __builtin_amdgcn_sched_barrier(0);
      row_b(a, xv, smem, rx1, rh2, b * 64 + rb, lane);
      __builtin_amdgcn_sched_barrier(0);
      row_a(S1, a, xv);
      WAIT_V(15);
      if (lane == 0 && b > b0) st4_sc1(myp, wmk(b));
      __builtin_amdgcn_sched_barrier(0);
      row_issue2(S1, rp, rr, (b + 3) * 64 + rb, l16);
      __builtin_amdgcn_sched_barrier(0);
      row_b(a, xv, smem, rx1, rh2, (b + 1) * 64 + rb, lane);
      __builtin_amdgcn_sched_barrier(0);
      const int w = eval(smp);
      wa = w > wa ? w : wa;
      b += 2;
    }
    row_a(S0, a, xv);
    row_b(a, xv, smem, rx1, rh2, b * 64 + rb, lane);
    row_a(S1, a, xv);
    WAIT_V(4 - 4);
    if (lane == 0) st4_sc1(myp, wmk((b + 1)));
    WTM(slot, 2 * b + par, 2);
    row_b(a, xv, smem, rx1, rh2, (b + 1) * 64 + rb, lane);
    WAIT_V(0);
    b += 2;
    if (lane == 0) st4_sc1(myp, wmk(b));
    WTM(slot, 2 * (b - 1) + par, 2);
  }
  static_assert(TLP && LNB % 2 == 0, "");
  const int gp = wave >> 2, r4 = rbase + 4 * par + (wave & 3);
  volatile LAS unsigned* lwb = (volatile LAS unsigned*)(smem + 16384 + gp * 8);
  if (gp && lane == 0) st4_sc1(myp, (unsigned)(LNB + 1));
  f32x2 tg[8], th[8], tc[8];
#pragma unroll
  for (int i = 0; i < 4; ++i) {
    const char* mt = smem + lane * 16 + i * 1024;
    const f32x4 g_ = *(const f32x4*)mt, h_ = *(const f32x4*)(mt + 4096), c_ = *(const f32x4*)(mt + 8192);
    tg[2 * i] = f32x2{g_[0], g_[1]}; tg[2 * i + 1] = f32x2{g_[2], g_[3]}; th[2 * i] = f32x2{h_[0], h_[1]}; th[2 * i + 1] = f32x2{h_[2], h_[3]};
    tc[2 * i] = f32x2{c_[0], c_[1]}; tc[2 * i + 1] = f32x2{c_[2], c_[3]};
  }
  const unsigned xo = (unsigned)(r4 * DM * 2 + lane * 16), ho = (unsigned)(r4 * 128 + (((lane & 7) ^ (r4 & 7)) << 4) + (lane >> 3) * 4096);
  const unsigned po = (l16 >> 6) * 2048 + (l16 & 48);
  for (int bb = LNB + gp; bb < NSEG; bb += 2) {
    f32x2 xf[8];
#pragma unroll
    for (int i = 0; i < 2; ++i) {
      const u32x4 rv = __builtin_amdgcn_raw_buffer_load_b128(rr, xo + i * 1024, (unsigned)bb * 65536u, 0);
#pragma unroll
      for (int e = 0; e < 4; ++e) xf[4 * i + e] = f32x2{bflo(rv[e]), bfhi(rv[e])};
    }
    asm volatile("" : "+v"(xf[0]), "+v"(xf[1]), "+v"(xf[2]), "+v"(xf[3]), "+v"(xf[4]), "+v"(xf[5]), "+v"(xf[6]), "+v"(xf[7]));
    {
      if ((wave & 3) == 0) {
        for (int spins = 0;; ++spins) {
          const unsigned v = ld4_sc1((const char*)cnt + pwo);
          if (__all(v > (unsigned)bb)) { lwb[0] = (unsigned)(bb + 1); break; }
          __builtin_amdgcn_s_sleep(2);
          if (give_up(spins, tmo, 11)) { lwb[0] = (unsigned)(bb + 1); break; }
        }
      } else {
        while (__builtin_amdgcn_readfirstlane(lwb[0]) <= (unsigned)bb) __builtin_amdgcn_s_sleep(1);
      }
    }
    WTM(slot, bb, 0);
    u32x4 pp[2][4];
#pragma unroll
    for (int i = 0; i < 2; ++i)
#pragma unroll
      for (int q = 0; q < 4; ++q) pp[i][q] = __builtin_amdgcn_raw_buffer_load_b128(rp, po + i * 32768, (unsigned)(bb * 32 * PROW + q * 65536 + r4 * 64), 16);
    sum4(pp[0], a[0]); sum4(pp[1], a[1]);
    ln_row(a, xf, tg, th, tc, uni((const char*)x1 + (size_t)bb * 65536), uni(h2c + (size_t)bb * 65536), xo, ho);
    WTM(slot, bb, 3);
    WAIT_V(0);
    unsigned o = 0;
    if (lane == 0) o = __hip_atomic_fetch_add((LAS unsigned*)(lwb + 1), 1u, __ATOMIC_RELAXED, __HIP_MEMORY_SCOPE_WORKGROUP);
    if ((__builtin_amdgcn_readfirstlane(o) & 3u) == 3u) {
      if (lane < 4) st4_sc1(myp - (wave & 3) + lane, (unsigned)(bb + 2));
      WTM(slot, bb, 2);
    }
  }
}
__device__ __forceinline__ void l1_role(char* smem, int wave_id_, int c, const u16* __restrict__ x, const float* __restrict__ mod, const u16* __restrict__ cvec,
                                        const u16* __restrict__ wmod, char* __restrict__ h1c, unsigned* cnt, unsigned* tmo, unsigned long long* mts) {
  const int tid = opaque_tid(), lane = tid & 63, wave = WAVE_ID;
  u32x4 xr[8][2];
  const u16* src = x + (size_t)(8 * c + wave) * DM + lane * 8;
  xr[0][0] = ldnt<2>(src); xr[0][1] = ldnt<2>(src + 512);
  {
    f32x4 m[8];
#pragma unroll
    for (int k = 0; k < 8; ++k) m[k] = *(const f32x4*)(mod + k * (2 * DM) + tid * 4);
    const float sva = bf2f(cvec[tid]), svb = bf2f(cvec[tid + 512]);
    f32x4 v = ((m[0] + m[1]) + (m[2] + m[3])) + ((m[4] + m[5]) + (m[6] + m[7]));
    v = f32x4{bfr(v[0]), bfr(v[1]), bfr(v[2]), bfr(v[3])};
    const int a = tid >> 8, cc = tid & 255;
    if (a) v = f32x4{bfr(1.0f + v[0]), bfr(1.0f + v[1]), bfr(1.0f + v[2]), bfr(1.0f + v[3])};
    *(f32x4*)(smem + 4096 + a * 4096 + ((cc >> 7) * 2 + (cc & 1)) * 1024 + ((cc >> 1) & 63) * 16) = v;
    float* sv = (float*)(smem + 12288);
    sv[tid] = bfr(sva / (1.0f + __expf(-sva))); sv[tid + 512] = bfr(svb / (1.0f + __expf(-svb)));
  }
  __syncthreads();
#pragma unroll
  for (int p = 0; p < 8; ++p) {
    f32x2 v[2][4];
#pragma unroll
    for (int i = 0; i < 2; ++i)
#pragma unroll
      for (int e = 0; e < 4; ++e) v[i][e] = f32x2{bflo(xr[p][i][e]), bfhi(xr[p][i][e])};
    const f32x2 s2 = ((v[0][0] + v[0][1]) + (v[0][2] + v[0][3])) + ((v[1][0] + v[1][1]) + (v[1][2] + v[1][3]));
    const float mean = wavesum(s2[0] + s2[1]) * (1.0f / DM);
    f32x2 q2 = f32x2{0, 0};
#pragma unroll
    for (int i = 0; i < 8; ++i) { const f32x2 d = v[i >> 2][i & 3] - f32x2{mean, mean}; q2 = pkfma(d, d, q2); }
    const float q = wavesum(q2[0] + q2[1]);
    if (p == 0 && wave == 0) STS((char*)cnt - OFF_CNT, 128 + (c >> 3) * 32 + 24 + (c & 7), 0);
    const float mb = bfr(mean), vb = bfr(q * (1.0f / DM));
    const float rden = 1.0f / bfr(sqrtf(bfr(vb + 1e-5f)));
    const int row = 256 * p + 8 * c + wave, rb = row & 31;
    char* dst = h1c + (size_t)(row >> 5) * 65536 + (lane >> 3) * 4096 + rb * 128 + (((lane & 7) ^ (rb & 7)) << 4);
    const char* ml = smem + lane * 16;
#pragma unroll
    for (int i = 0; i < 2; ++i) st16_sc1(dst + i * 32768, norm8(v[i], ml + i * 2048, mb, rden));
    WAIT_V(0);
    __builtin_amdgcn_s_barrier();
    if (tid == 0) st4_sc1(cnt + (PL1 + c) * 32, (unsigned)(p + 1));
    if (p == 0) {
#pragma unroll
      for (int pp = 1; pp < 8; ++pp) { xr[pp][0] = ldnt<2>(src + (size_t)pp * 256 * DM); xr[pp][1] = ldnt<2>(src + (size_t)pp * 256 * DM + 512); }
    }
    if (wave == 0) WTS(c, p, 0);
    if (p == 0 && wave == 0) STS((char*)cnt - OFF_CNT, 128 + (c >> 3) * 32 + 24 + (c & 7), 1);
  }
  wait_k0(cnt, c, tmo);
  {
    const int cg = tid & 15, r0 = tid >> 4;
    const u16* wp = wmod + (size_t)r0 * (6 * DM) + 2 * DM + 128 * c + cg * 8;
    const float* sv = (const float*)(smem + 12288) + r0;
    f32x2 acc[4] = {f32x2{0, 0}, f32x2{0, 0}, f32x2{0, 0}, f32x2{0, 0}};
#pragma unroll 1
    for (int o = 0; o < 4; ++o) {
      u32x4 w[8];
#pragma unroll
      for (int i = 0; i < 8; ++i) w[i] = ldnt<1>(wp + (size_t)(o * 8 + i) * 32 * (6 * DM));
#pragma unroll
      for (int i = 0; i < 8; ++i) {
        const float s = sv[(o * 8 + i) * 32];
#pragma unroll
        for (int e = 0; e < 4; ++e) acc[e] = pkfma(f32x2{s, s}, f32x2{bflo(w[i][e]), bfhi(w[i][e])}, acc[e]);
      }
    }
    float* red = (float*)(smem + 16384);
    *(f32x4*)(red + r0 * 128 + cg * 8) = f32x4{acc[0][0], acc[0][1], acc[1][0], acc[1][1]};
    *(f32x4*)(red + r0 * 128 + cg * 8 + 4) = f32x4{acc[2][0], acc[2][1], acc[3][0], acc[3][1]};
    __syncthreads();
    if (tid < 128) {
      float s = 0;
#pragma unroll
      for (int r = 0; r < 32; ++r) s += red[r * 128 + tid];
      st4_sc1((char*)mod + OFF_MOD2 + (128 * c + tid) * 4, __builtin_bit_cast(unsigned, s));
    }
    WAIT_V(0);
    __builtin_amdgcn_s_barrier();
    if (tid == 0) st4_sc1(cnt + (PL1 + c) * 32, 9u);
  }
}
enum { R_QKV = 0, R_PART = 1, R_F1 = 2, R_PART2 = 3 };
struct Dep {
  const char* base; unsigned voff; unsigned mul, add;
};
__device__ __forceinline__ unsigned dep_eval(unsigned v, const Dep& d) {
  v = rowmin16(v * d.mul + (d.add + 64u));
  return min(min(rdl(v, 0), rdl(v, 16)), min(rdl(v, 32), rdl(v, 48))) - 64u;
}
typedef unsigned u32x32 __attribute__((ext_vector_type(32)));
typedef unsigned u32x16 __attribute__((ext_vector_type(16)));
__device__ __forceinline__ void fast_ld0_2(const u32x32 (&W)[4], u32x16& pd, unsigned ea, unsigned da, int& b, int& lf, int cp, unsigned sa, unsigned mu, unsigned ad,
    unsigned rw, unsigned ab, const char* p1, const char* p0, unsigned l1, unsigned l0, const unsigned (&av)[8]) {
  asm volatile("s_setprio 0\n\t"
    "s_waitcnt vmcnt(4)\n\t"
    "s_barrier\n\t"
    "ds_read_b128 v[96:99], %[ea] offset:0\n\t"
    "ds_read_b128 v[100:103], %[ea] offset:2048\n\t"
    "ds_read_b128 v[104:107], %[da] offset:0\n\t"
    "ds_read_b128 v[108:111], %[da] offset:2048\n\t"
    "ds_read_b128 v[112:115], %[ea] offset:4096\n\t"
    "ds_read_b128 v[116:119], %[ea] offset:6144\n\t"
    "s_branch 2f\n\t"
    "1:\n\t"
    "s_waitcnt vmcnt(4)\n\t"
    "s_nop 1\n\t"
    "s_barrier\n\t"
    "2:\n\t"
    "s_waitcnt lgkmcnt(4)\n\t"
    "v_mfma_f32_16x16x32_bf16 v[64:67], v[128:131], v[96:99], 0\n\t"
    "ds_read_b128 v[120:123], %[da] offset:4096\n\t"
    "s_add_u32 m0, %[l1], 0\n\t"
    "ds_write_b128 %[rw], v[80:83] offset:0\n\t"
    "v_mfma_f32_16x16x32_bf16 v[68:71], v[132:135], v[96:99], 0\n\t"
    "ds_read_b128 v[124:127], %[da] offset:6144\n\t"
    "global_load_lds_dwordx4 %[av0], s[36:37] sc1\n\t"
    "ds_write_b128 %[rw], v[84:87] offset:1024\n\t"
    "v_mfma_f32_16x16x32_bf16 v[72:75], v[128:131], v[100:103], 0\n\t"
    "s_add_u32 m0, %[l1], 32768\n\t"
    "ds_write_b128 %[rw], v[88:91] offset:2048\n\t"
    "v_mfma_f32_16x16x32_bf16 v[76:79], v[132:135], v[100:103], 0\n\t"
    "global_load_lds_dwordx4 %[av1], s[36:37] sc1\n\t"
    "ds_write_b128 %[rw], v[92:95] offset:3072\n\t"
    "s_waitcnt lgkmcnt(8)\n\t"
    "v_mfma_f32_16x16x32_bf16 v[64:67], v[136:139], v[104:107], v[64:67]\n\t"
    "ds_read_b128 v[96:99], %[ea] offset:8192\n\t"
    "s_add_u32 m0, %[l1], 4096\n\t"
    "v_mfma_f32_16x16x32_bf16 v[68:71], v[140:143], v[104:107], v[68:71]\n\t"
    "ds_read_b128 v[100:103], %[ea] offset:10240\n\t"
    "global_load_lds_dwordx4 %[av2], s[36:37] sc1\n\t"
    "v_mfma_f32_16x16x32_bf16 v[72:75], v[136:139], v[108:111], v[72:75]\n\t"
    "s_add_u32 m0, %[l1], 36864\n\t"
    "v_mfma_f32_16x16x32_bf16 v[76:79], v[140:143], v[108:111], v[76:79]\n\t"
    "global_load_lds_dwordx4 %[av3], s[36:37] sc1\n\t"
    "s_waitcnt lgkmcnt(8)\n\t"
    "v_mfma_f32_16x16x32_bf16 v[64:67], v[144:147], v[112:115], v[64:67]\n\t"
    "ds_read_b128 v[104:107], %[da] offset:8192\n\t"
    "s_add_u32 m0, %[l1], 8192\n\t"
    "v_mfma_f32_16x16x32_bf16 v[68:71], v[148:151], v[112:115], v[68:71]\n\t"
    "ds_read_b128 v[108:111], %[da] offset:10240\n\t"
    "global_load_lds_dwordx4 %[av4], s[36:37] sc1\n\t"
    "v_mfma_f32_16x16x32_bf16 v[72:75], v[144:147], v[116:119], v[72:75]\n\t"
    "s_add_u32 m0, %[l1], 40960\n\t"
    "v_mfma_f32_16x16x32_bf16 v[76:79], v[148:151], v[116:119], v[76:79]\n\t"
    "global_load_lds_dwordx4 %[av5], s[36:37] sc1\n\t"
    "s_waitcnt lgkmcnt(7)\n\t"
    "v_mfma_f32_16x16x32_bf16 v[64:67], v[152:155], v[120:123], v[64:67]\n\t"
    "ds_read_b128 v[112:115], %[ea] offset:12288\n\t"
    "s_add_u32 m0, %[l1], 12288\n\t"
    "v_mfma_f32_16x16x32_bf16 v[68:71], v[156:159], v[120:123], v[68:71]\n\t"
    "ds_read_b128 v[116:119], %[ea] offset:14336\n\t"
    "global_load_lds_dwordx4 %[av6], s[36:37] sc1\n\t"
    "v_mfma_f32_16x16x32_bf16 v[72:75], v[152:155], v[124:127], v[72:75]\n\t"
    "s_add_u32 m0, %[l1], 45056\n\t"
    "v_mfma_f32_16x16x32_bf16 v[76:79], v[156:159], v[124:127], v[76:79]\n\t"
    "global_load_lds_dwordx4 %[av7], s[36:37] sc1\n\t"
    "s_waitcnt lgkmcnt(4)\n\t"
    "v_mfma_f32_16x16x32_bf16 v[64:67], v[160:163], v[96:99], v[64:67]\n\t"
    "ds_read_b128 v[120:123], %[da] offset:12288\n\t"
    "v_mfma_f32_16x16x32_bf16 v[68:71], v[164:167], v[96:99], v[68:71]\n\t"
    "ds_read_b128 v[124:127], %[da] offset:14336\n\t"
    "v_mfma_f32_16x16x32_bf16 v[72:75], v[160:163], v[100:103], v[72:75]\n\t"
    "v_mfma_f32_16x16x32_bf16 v[76:79], v[164:167], v[100:103], v[76:79]\n\t"
    "s_waitcnt lgkmcnt(4)\n\t"
    "v_mfma_f32_16x16x32_bf16 v[64:67], v[168:171], v[104:107], v[64:67]\n\t"
    "ds_read_b128 v[96:99], %[ea] offset:16384\n\t"
    "v_mfma_f32_16x16x32_bf16 v[68:71], v[172:175], v[104:107], v[68:71]\n\t"
    "ds_read_b128 v[100:103], %[ea] offset:18432\n\t"
    "v_mfma_f32_16x16x32_bf16 v[72:75], v[168:171], v[108:111], v[72:75]\n\t"
    "v_mfma_f32_16x16x32_bf16 v[76:79], v[172:175], v[108:111], v[76:79]\n\t"
    "s_waitcnt lgkmcnt(4)\n\t"
    "v_mfma_f32_16x16x32_bf16 v[64:67], v[176:179], v[112:115], v[64:67]\n\t"
    "ds_read_b128 v[104:107], %[da] offset:16384\n\t"
    "v_mfma_f32_16x16x32_bf16 v[68:71], v[180:183], v[112:115], v[68:71]\n\t"
    "ds_read_b128 v[108:111], %[da] offset:18432\n\t"
    "v_mfma_f32_16x16x32_bf16 v[72:75], v[176:179], v[116:119], v[72:75]\n\t"
    "v_mfma_f32_16x16x32_bf16 v[76:79], v[180:183], v[116:119], v[76:79]\n\t"
    "s_waitcnt lgkmcnt(4)\n\t"
    "v_mfma_f32_16x16x32_bf16 v[64:67], v[184:187], v[120:123], v[64:67]\n\t"
    "ds_read_b128 v[112:115], %[ea] offset:20480\n\t"
    "v_mfma_f32_16x16x32_bf16 v[68:71], v[188:191], v[120:123], v[68:71]\n\t"
    "ds_read_b128 v[116:119], %[ea] offset:22528\n\t"
    "v_mfma_f32_16x16x32_bf16 v[72:75], v[184:187], v[124:127], v[72:75]\n\t"
    "v_mfma_f32_16x16x32_bf16 v[76:79], v[188:191], v[124:127], v[76:79]\n\t"
    "s_waitcnt vmcnt(4)\n\t"
    "s_barrier\n\t"
    "s_waitcnt lgkmcnt(4)\n\t"
    "v_mfma_f32_16x16x32_bf16 v[64:67], v[192:195], v[96:99], v[64:67]\n\t"
    "ds_read_b128 v[120:123], %[da] offset:20480\n\t"
    "s_add_u32 m0, %[l0], 0\n\t"
    "v_mfma_f32_16x16x32_bf16 v[68:71], v[196:199], v[96:99], v[68:71]\n\t"
    "ds_read_b128 v[124:127], %[da] offset:22528\n\t"
    "global_load_lds_dwordx4 %[av0], s[38:39] sc1\n\t"
    "v_mfma_f32_16x16x32_bf16 v[72:75], v[192:195], v[100:103], v[72:75]\n\t"
    "s_add_u32 m0, %[l0], 32768\n\t"
    "v_mfma_f32_16x16x32_bf16 v[76:79], v[196:199], v[100:103], v[76:79]\n\t"
    "global_load_lds_dwordx4 %[av1], s[38:39] sc1\n\t"
    "s_waitcnt lgkmcnt(4)\n\t"
    "v_mfma_f32_16x16x32_bf16 v[64:67], v[200:203], v[104:107], v[64:67]\n\t"
    "ds_read_b128 v[96:99], %[ea] offset:24576\n\t"
    "s_add_u32 m0, %[l0], 4096\n\t"
    "v_mfma_f32_16x16x32_bf16 v[68:71], v[204:207], v[104:107], v[68:71]\n\t"
    "ds_read_b128 v[100:103], %[ea] offset:26624\n\t"
    "global_load_lds_dwordx4 %[av2], s[38:39] sc1\n\t"
    "v_mfma_f32_16x16x32_bf16 v[72:75], v[200:203], v[108:111], v[72:75]\n\t"
    "s_add_u32 m0, %[l0], 36864\n\t"
    "v_mfma_f32_16x16x32_bf16 v[76:79], v[204:207], v[108:111], v[76:79]\n\t"
    "global_load_lds_dwordx4 %[av3], s[38:39] sc1\n\t"
    "s_add_u32 s36, s36, %[ab]\n\t"
    "s_addc_u32 s37, s37, 0\n\t"
    "s_waitcnt lgkmcnt(4)\n\t"
    "v_mfma_f32_16x16x32_bf16 v[64:67], v[208:211], v[112:115], v[64:67]\n\t"
    "ds_read_b128 v[104:107], %[da] offset:24576\n\t"
    "s_add_u32 m0, %[l0], 8192\n\t"
    "v_mfma_f32_16x16x32_bf16 v[68:71], v[212:215], v[112:115], v[68:71]\n\t"
    "ds_read_b128 v[108:111], %[da] offset:26624\n\t"
    "global_load_lds_dwordx4 %[av4], s[38:39] sc1\n\t"
    "v_mfma_f32_16x16x32_bf16 v[72:75], v[208:211], v[116:119], v[72:75]\n\t"
    "ds_read_b32 v39, %[sa]\n\t"
    "s_add_u32 m0, %[l0], 40960\n\t"
    "v_mfma_f32_16x16x32_bf16 v[76:79], v[212:215], v[116:119], v[76:79]\n\t"
    "global_load_lds_dwordx4 %[av5], s[38:39] sc1\n\t"
    "s_xor_b32 %[l1], %[l1], 0x10000\n\t"
    "s_waitcnt lgkmcnt(5)\n\t"
    "v_mfma_f32_16x16x32_bf16 v[64:67], v[216:219], v[120:123], v[64:67]\n\t"
    "ds_read_b128 v[112:115], %[ea] offset:28672\n\t"
    "s_add_u32 m0, %[l0], 12288\n\t"
    "v_mfma_f32_16x16x32_bf16 v[68:71], v[220:223], v[120:123], v[68:71]\n\t"
    "ds_read_b128 v[116:119], %[ea] offset:30720\n\t"
    "global_load_lds_dwordx4 %[av6], s[38:39] sc1\n\t"
    "v_mfma_f32_16x16x32_bf16 v[72:75], v[216:219], v[124:127], v[72:75]\n\t"
    "v_xor_b32 %[ea], 0x10000, %[ea]\n\t"
    "s_add_u32 m0, %[l0], 45056\n\t"
    "v_mfma_f32_16x16x32_bf16 v[76:79], v[220:223], v[124:127], v[76:79]\n\t"
    "global_load_lds_dwordx4 %[av7], s[38:39] sc1\n\t"
    "s_waitcnt lgkmcnt(5)\n\t"
    "v_mfma_f32_16x16x32_bf16 v[64:67], v[224:227], v[96:99], v[64:67]\n\t"
    "ds_read_b128 v[120:123], %[da] offset:28672\n\t"
    "v_mfma_f32_16x16x32_bf16 v[68:71], v[228:231], v[96:99], v[68:71]\n\t"
    "ds_read_b128 v[124:127], %[da] offset:30720\n\t"
    "v_mfma_f32_16x16x32_bf16 v[72:75], v[224:227], v[100:103], v[72:75]\n\t"
    "v_xor_b32 %[da], 0x10000, %[da]\n\t"
    "v_mfma_f32_16x16x32_bf16 v[76:79], v[228:231], v[100:103], v[76:79]\n\t"
    "s_waitcnt lgkmcnt(5)\n\t"
    "v_mfma_f32_16x16x32_bf16 v[64:67], v[232:235], v[104:107], v[64:67]\n\t"
    "ds_read_b128 v[96:99], %[ea] offset:0\n\t"
    "v_mfma_f32_16x16x32_bf16 v[68:71], v[236:239], v[104:107], v[68:71]\n\t"
    "ds_read_b128 v[100:103], %[ea] offset:2048\n\t"
    "v_mfma_f32_16x16x32_bf16 v[72:75], v[232:235], v[108:111], v[72:75]\n\t"
    "v_mfma_f32_16x16x32_bf16 v[76:79], v[236:239], v[108:111], v[76:79]\n\t"
    "s_waitcnt lgkmcnt(4)\n\t"
    "v_mfma_f32_16x16x32_bf16 v[64:67], v[240:243], v[112:115], v[64:67]\n\t"
    "ds_read_b128 v[104:107], %[da] offset:0\n\t"
    "v_mad_u32_u24 v39, v39, %[mu], %[ad]\n\t"
    "v_mfma_f32_16x16x32_bf16 v[68:71], v[244:247], v[112:115], v[68:71]\n\t"
    "ds_read_b128 v[108:111], %[da] offset:2048\n\t"
    "v_cmp_ge_i32 vcc, %[lf], v39\n\t"
    "s_cmp_eq_u64 vcc, 0\n\t"
    "s_addc_u32 %[lf], %[lf], 0\n\t"
    "v_mfma_f32_16x16x32_bf16 v[72:75], v[240:243], v[116:119], v[72:75]\n\t"
    "v_mfma_f32_16x16x32_bf16 v[76:79], v[244:247], v[116:119], v[76:79]\n\t"
    "v_cmp_ge_i32 vcc, %[lf], v39\n\t"
    "s_cmp_eq_u64 vcc, 0\n\t"
    "s_addc_u32 %[lf], %[lf], 0\n\t"
    "s_waitcnt lgkmcnt(4)\n\t"
    "v_mfma_f32_16x16x32_bf16 v[80:83], v[248:251], v[120:123], v[64:67]\n\t"
    "ds_read_b128 v[112:115], %[ea] offset:4096\n\t"
    "s_min_i32 %[lf], %[lf], %[cp]\n\t"
    "s_add_u32 %[b], %[b], 1\n\t"
    "v_mfma_f32_16x16x32_bf16 v[84:87], v[252:255], v[120:123], v[68:71]\n\t"
    "ds_read_b128 v[116:119], %[ea] offset:6144\n\t"
    "v_mfma_f32_16x16x32_bf16 v[88:91], v[248:251], v[124:127], v[72:75]\n\t"
    "v_mfma_f32_16x16x32_bf16 v[92:95], v[252:255], v[124:127], v[76:79]\n\t"
    "s_add_u32 s38, s38, %[ab]\n\t"
    "s_addc_u32 s39, s39, 0\n\t"
    "s_xor_b32 %[l0], %[l0], 0x10000\n\t"
    "s_cmp_lt_i32 %[b], %[lf]\n\t"
    "s_cbranch_scc1 1b\n\t"
    "s_waitcnt lgkmcnt(0)\n\t"
    "s_nop 4"
    : "=&{v[80:95]}"(pd), [ea] "+&v"(ea), [da] "+&v"(da), [b] "+&s"(b), [lf] "+&s"(lf), "+{s[36:37]}"(p1), "+{s[38:39]}"(p0), [sa] "+&v"(sa), [l1] "+&s"(l1), [l0] "+&s"(l0)
    : [rw] "v"(rw), [ab] "s"(ab), [cp] "s"(cp), [mu] "s"(mu), [ad] "v"(ad), "{v[128:159]}"(W[0]), "{v[160:191]}"(W[1]), "{v[192:223]}"(W[2]), "{v[224:255]}"(W[3]), [av0] "v"(av[0]), [av1] "v"(av[1]), [av2] "v"(av[2]), [av3] "v"(av[3]), [av4] "v"(av[4]), [av5] "v"(av[5]), [av6] "v"(av[6]), [av7] "v"(av[7])
    : "v39", "v64", "v65", "v66", "v67", "v68", "v69", "v70", "v71", "v72", "v73", "v74", "v75", "v76", "v77", "v78", "v79", "v96", "v97", "v98", "v99", "v100", "v101", "v102", "v103", "v104", "v105", "v106", "v107", "v108", "v109", "v110", "v111", "v112", "v113", "v114", "v115", "v116", "v117", "v118", "v119", "v120", "v121", "v122", "v123", "v124", "v125", "v126", "v127", "vcc", "scc", "m0", "memory");
}
__device__ __forceinline__ void fast_ld2_2(const u32x32 (&W)[4], u32x16& pd, unsigned ea, unsigned da, int& b, int& lf, int cp, unsigned sa, unsigned mu, unsigned ad,
    unsigned rw, unsigned ab, const char* p1, const char* p0, unsigned l1, unsigned l0, const unsigned (&av)[8]) {
  asm volatile("s_setprio 0\n\t"
    "s_waitcnt vmcnt(2)\n\t"
    "s_barrier\n\t"
    "ds_read_b128 v[96:99], %[ea] offset:0\n\t"
    "ds_read_b128 v[100:103], %[ea] offset:2048\n\t"
    "ds_read_b128 v[104:107], %[da] offset:0\n\t"
    "ds_read_b128 v[108:111], %[da] offset:2048\n\t"
    "ds_read_b128 v[112:115], %[ea] offset:4096\n\t"
    "ds_read_b128 v[116:119], %[ea] offset:6144\n\t"
    "s_branch 2f\n\t"
    "1:\n\t"
    "s_waitcnt vmcnt(2)\n\t"
    "s_nop 1\n\t"
    "s_barrier\n\t"
    "2:\n\t"
    "s_waitcnt lgkmcnt(4)\n\t"
    "v_mfma_f32_16x16x32_bf16 v[64:67], v[128:131], v[96:99], 0\n\t"
    "ds_read_b128 v[120:123], %[da] offset:4096\n\t"
    "s_add_u32 m0, %[l1], 0\n\t"
    "ds_write_b128 %[rw], v[80:83] offset:0\n\t"
    "v_mfma_f32_16x16x32_bf16 v[68:71], v[132:135], v[96:99], 0\n\t"
    "ds_read_b128 v[124:127], %[da] offset:6144\n\t"
    "global_load_lds_dwordx4 %[av0], s[36:37] sc1\n\t"
    "ds_write_b128 %[rw], v[84:87] offset:1024\n\t"
    "v_mfma_f32_16x16x32_bf16 v[72:75], v[128:131], v[100:103], 0\n\t"
    "ds_write_b128 %[rw], v[88:91] offset:2048\n\t"
    "v_mfma_f32_16x16x32_bf16 v[76:79], v[132:135], v[100:103], 0\n\t"
    "ds_write_b128 %[rw], v[92:95] offset:3072\n\t"
    "s_waitcnt lgkmcnt(8)\n\t"
    "v_mfma_f32_16x16x32_bf16 v[64:67], v[136:139], v[104:107], v[64:67]\n\t"
    "ds_read_b128 v[96:99], %[ea] offset:8192\n\t"
    "s_add_u32 m0, %[l1], 4096\n\t"
    "v_mfma_f32_16x16x32_bf16 v[68:71], v[140:143], v[104:107], v[68:71]\n\t"
    "ds_read_b128 v[100:103], %[ea] offset:10240\n\t"
    "global_load_lds_dwordx4 %[av2], s[36:37] sc1\n\t"
    "v_mfma_f32_16x16x32_bf16 v[72:75], v[136:139], v[108:111], v[72:75]\n\t"
    "v_mfma_f32_16x16x32_bf16 v[76:79], v[140:143], v[108:111], v[76:79]\n\t"
    "s_waitcnt lgkmcnt(8)\n\t"
    "v_mfma_f32_16x16x32_bf16 v[64:67], v[144:147], v[112:115], v[64:67]\n\t"
    "ds_read_b128 v[104:107], %[da] offset:8192\n\t"
    "s_add_u32 m0, %[l1], 8192\n\t"
    "v_mfma_f32_16x16x32_bf16 v[68:71], v[148:151], v[112:115], v[68:71]\n\t"
    "ds_read_b128 v[108:111], %[da] offset:10240\n\t"
    "global_load_lds_dwordx4 %[av4], s[36:37] sc1\n\t"
    "v_mfma_f32_16x16x32_bf16 v[72:75], v[144:147], v[116:119], v[72:75]\n\t"
    "v_mfma_f32_16x16x32_bf16 v[76:79], v[148:151], v[116:119], v[76:79]\n\t"
    "s_waitcnt lgkmcnt(7)\n\t"
    "v_mfma_f32_16x16x32_bf16 v[64:67], v[152:155], v[120:123], v[64:67]\n\t"
    "ds_read_b128 v[112:115], %[ea] offset:12288\n\t"
    "s_add_u32 m0, %[l1], 12288\n\t"
    "v_mfma_f32_16x16x32_bf16 v[68:71], v[156:159], v[120:123], v[68:71]\n\t"
    "ds_read_b128 v[116:119], %[ea] offset:14336\n\t"
    "global_load_lds_dwordx4 %[av6], s[36:37] sc1\n\t"
    "v_mfma_f32_16x16x32_bf16 v[72:75], v[152:155], v[124:127], v[72:75]\n\t"
    "v_mfma_f32_16x16x32_bf16 v[76:79], v[156:159], v[124:127], v[76:79]\n\t"
    "s_waitcnt lgkmcnt(4)\n\t"
    "v_mfma_f32_16x16x32_bf16 v[64:67], v[160:163], v[96:99], v[64:67]\n\t"
    "ds_read_b128 v[120:123], %[da] offset:12288\n\t"
    "v_mfma_f32_16x16x32_bf16 v[68:71], v[164:167], v[96:99], v[68:71]\n\t"
    "ds_read_b128 v[124:127], %[da] offset:14336\n\t"
    "v_mfma_f32_16x16x32_bf16 v[72:75], v[160:163], v[100:103], v[72:75]\n\t"
    "v_mfma_f32_16x16x32_bf16 v[76:79], v[164:167], v[100:103], v[76:79]\n\t"
    "s_waitcnt lgkmcnt(4)\n\t"
    "v_mfma_f32_16x16x32_bf16 v[64:67], v[168:171], v[104:107], v[64:67]\n\t"
    "ds_read_b128 v[96:99], %[ea] offset:16384\n\t"
    "v_mfma_f32_16x16x32_bf16 v[68:71], v[172:175], v[104:107], v[68:71]\n\t"
    "ds_read_b128 v[100:103], %[ea] offset:18432\n\t"
    "v_mfma_f32_16x16x32_bf16 v[72:75], v[168:171], v[108:111], v[72:75]\n\t"
    "v_mfma_f32_16x16x32_bf16 v[76:79], v[172:175], v[108:111], v[76:79]\n\t"
    "s_waitcnt lgkmcnt(4)\n\t"
    "v_mfma_f32_16x16x32_bf16 v[64:67], v[176:179], v[112:115], v[64:67]\n\t"
    "ds_read_b128 v[104:107], %[da] offset:16384\n\t"
    "v_mfma_f32_16x16x32_bf16 v[68:71], v[180:183], v[112:115], v[68:71]\n\t"
    "ds_read_b128 v[108:111], %[da] offset:18432\n\t"
    "v_mfma_f32_16x16x32_bf16 v[72:75], v[176:179], v[116:119], v[72:75]\n\t"
    "v_mfma_f32_16x16x32_bf16 v[76:79], v[180:183], v[116:119], v[76:79]\n\t"
    "s_waitcnt lgkmcnt(4)\n\t"
    "v_mfma_f32_16x16x32_bf16 v[64:67], v[184:187], v[120:123], v[64:67]\n\t"
    "ds_read_b128 v[112:115], %[ea] offset:20480\n\t"
    "v_mfma_f32_16x16x32_bf16 v[68:71], v[188:191], v[120:123], v[68:71]\n\t"
    "ds_read_b128 v[116:119], %[ea] offset:22528\n\t"
    "v_mfma_f32_16x16x32_bf16 v[72:75], v[184:187], v[124:127], v[72:75]\n\t"
    "v_mfma_f32_16x16x32_bf16 v[76:79], v[188:191], v[124:127], v[76:79]\n\t"
    "s_waitcnt vmcnt(2)\n\t"
    "s_barrier\n\t"
    "s_waitcnt lgkmcnt(4)\n\t"
    "v_mfma_f32_16x16x32_bf16 v[64:67], v[192:195], v[96:99], v[64:67]\n\t"
    "ds_read_b128 v[120:123], %[da] offset:20480\n\t"
    "s_add_u32 m0, %[l0], 0\n\t"
    "v_mfma_f32_16x16x32_bf16 v[68:71], v[196:199], v[96:99], v[68:71]\n\t"
    "ds_read_b128 v[124:127], %[da] offset:22528\n\t"
    "global_load_lds_dwordx4 %[av0], s[38:39] sc1\n\t"
    "v_mfma_f32_16x16x32_bf16 v[72:75], v[192:195], v[100:103], v[72:75]\n\t"
    "v_mfma_f32_16x16x32_bf16 v[76:79], v[196:199], v[100:103], v[76:79]\n\t"
    "s_waitcnt lgkmcnt(4)\n\t"
    "v_mfma_f32_16x16x32_bf16 v[64:67], v[200:203], v[104:107], v[64:67]\n\t"
    "ds_read_b128 v[96:99], %[ea] offset:24576\n\t"
    "s_add_u32 m0, %[l0], 4096\n\t"
    "v_mfma_f32_16x16x32_bf16 v[68:71], v[204:207], v[104:107], v[68:71]\n\t"
    "ds_read_b128 v[100:103], %[ea] offset:26624\n\t"
    "global_load_lds_dwordx4 %[av2], s[38:39] sc1\n\t"
    "v_mfma_f32_16x16x32_bf16 v[72:75], v[200:203], v[108:111], v[72:75]\n\t"
    "v_mfma_f32_16x16x32_bf16 v[76:79], v[204:207], v[108:111], v[76:79]\n\t"
    "s_add_u32 s36, s36, %[ab]\n\t"
    "s_addc_u32 s37, s37, 0\n\t"
    "s_waitcnt lgkmcnt(4)\n\t"
    "v_mfma_f32_16x16x32_bf16 v[64:67], v[208:211], v[112:115], v[64:67]\n\t"
    "ds_read_b128 v[104:107], %[da] offset:24576\n\t"
    "s_add_u32 m0, %[l0], 8192\n\t"
    "v_mfma_f32_16x16x32_bf16 v[68:71], v[212:215], v[112:115], v[68:71]\n\t"
    "ds_read_b128 v[108:111], %[da] offset:26624\n\t"
    "global_load_lds_dwordx4 %[av4], s[38:39] sc1\n\t"
    "v_mfma_f32_16x16x32_bf16 v[72:75], v[208:211], v[116:119], v[72:75]\n\t"
    "ds_read_b32 v39, %[sa]\n\t"
    "v_mfma_f32_16x16x32_bf16 v[76:79], v[212:215], v[116:119], v[76:79]\n\t"
    "s_xor_b32 %[l1], %[l1], 0x10000\n\t"
    "s_waitcnt lgkmcnt(5)\n\t"
    "v_mfma_f32_16x16x32_bf16 v[64:67], v[216:219], v[120:123], v[64:67]\n\t"
    "ds_read_b128 v[112:115], %[ea] offset:28672\n\t"
    "s_add_u32 m0, %[l0], 12288\n\t"
    "v_mfma_f32_16x16x32_bf16 v[68:71], v[220:223], v[120:123], v[68:71]\n\t"
    "ds_read_b128 v[116:119], %[ea] offset:30720\n\t"
    "global_load_lds_dwordx4 %[av6], s[38:39] sc1\n\t"
    "v_mfma_f32_16x16x32_bf16 v[72:75], v[216:219], v[124:127], v[72:75]\n\t"
    "v_xor_b32 %[ea], 0x10000, %[ea]\n\t"
    "v_mfma_f32_16x16x32_bf16 v[76:79], v[220:223], v[124:127], v[76:79]\n\t"
    "s_waitcnt lgkmcnt(5)\n\t"
    "v_mfma_f32_16x16x32_bf16 v[64:67], v[224:227], v[96:99], v[64:67]\n\t"
    "ds_read_b128 v[120:123], %[da] offset:28672\n\t"
    "v_mfma_f32_16x16x32_bf16 v[68:71], v[228:231], v[96:99], v[68:71]\n\t"
    "ds_read_b128 v[124:127], %[da] offset:30720\n\t"
    "v_mfma_f32_16x16x32_bf16 v[72:75], v[224:227], v[100:103], v[72:75]\n\t"
    "v_xor_b32 %[da], 0x10000, %[da]\n\t"
    "v_mfma_f32_16x16x32_bf16 v[76:79], v[228:231], v[100:103], v[76:79]\n\t"
    "s_waitcnt lgkmcnt(5)\n\t"
    "v_mfma_f32_16x16x32_bf16 v[64:67], v[232:235], v[104:107], v[64:67]\n\t"
    "ds_read_b128 v[96:99], %[ea] offset:0\n\t"
    "v_mfma_f32_16x16x32_bf16 v[68:71], v[236:239], v[104:107], v[68:71]\n\t"
    "ds_read_b128 v[100:103], %[ea] offset:2048\n\t"
    "v_mfma_f32_16x16x32_bf16 v[72:75], v[232:235], v[108:111], v[72:75]\n\t"
    "v_mfma_f32_16x16x32_bf16 v[76:79], v[236:239], v[108:111], v[76:79]\n\t"
    "s_waitcnt lgkmcnt(4)\n\t"
    "v_mfma_f32_16x16x32_bf16 v[64:67], v[240:243], v[112:115], v[64:67]\n\t"
    "ds_read_b128 v[104:107], %[da] offset:0\n\t"
    "v_mad_u32_u24 v39, v39, %[mu], %[ad]\n\t"
    "v_mfma_f32_16x16x32_bf16 v[68:71], v[244:247], v[112:115], v[68:71]\n\t"
    "ds_read_b128 v[108:111], %[da] offset:2048\n\t"
    "v_cmp_ge_i32 vcc, %[lf], v39\n\t"
    "s_cmp_eq_u64 vcc, 0\n\t"
    "s_addc_u32 %[lf], %[lf], 0\n\t"
    "v_mfma_f32_16x16x32_bf16 v[72:75], v[240:243], v[116:119], v[72:75]\n\t"
    "v_mfma_f32_16x16x32_bf16 v[76:79], v[244:247], v[116:119], v[76:79]\n\t"
    "v_cmp_ge_i32 vcc, %[lf], v39\n\t"
    "s_cmp_eq_u64 vcc, 0\n\t"
    "s_addc_u32 %[lf], %[lf], 0\n\t"
    "s_waitcnt lgkmcnt(4)\n\t"
    "v_mfma_f32_16x16x32_bf16 v[80:83], v[248:251], v[120:123], v[64:67]\n\t"
    "ds_read_b128 v[112:115], %[ea] offset:4096\n\t"
    "s_min_i32 %[lf], %[lf], %[cp]\n\t"
    "s_add_u32 %[b], %[b], 1\n\t"
    "v_mfma_f32_16x16x32_bf16 v[84:87], v[252:255], v[120:123], v[68:71]\n\t"
    "ds_read_b128 v[116:119], %[ea] offset:6144\n\t"
    "v_mfma_f32_16x16x32_bf16 v[88:91], v[248:251], v[124:127], v[72:75]\n\t"
    "v_mfma_f32_16x16x32_bf16 v[92:95], v[252:255], v[124:127], v[76:79]\n\t"
    "s_add_u32 s38, s38, %[ab]\n\t"
    "s_addc_u32 s39, s39, 0\n\t"
    "s_xor_b32 %[l0], %[l0], 0x10000\n\t"
    "s_cmp_lt_i32 %[b], %[lf]\n\t"
    "s_cbranch_scc1 1b\n\t"
    "s_waitcnt lgkmcnt(0)\n\t"
    "s_nop 4"
    : "=&{v[80:95]}"(pd), [ea] "+&v"(ea), [da] "+&v"(da), [b] "+&s"(b), [lf] "+&s"(lf), "+{s[36:37]}"(p1), "+{s[38:39]}"(p0), [sa] "+&v"(sa), [l1] "+&s"(l1), [l0] "+&s"(l0)
    : [rw] "v"(rw), [ab] "s"(ab), [cp] "s"(cp), [mu] "s"(mu), [ad] "v"(ad), "{v[128:159]}"(W[0]), "{v[160:191]}"(W[1]), "{v[192:223]}"(W[2]), "{v[224:255]}"(W[3]), [av0] "v"(av[0]), [av1] "v"(av[1]), [av2] "v"(av[2]), [av3] "v"(av[3]), [av4] "v"(av[4]), [av5] "v"(av[5]), [av6] "v"(av[6]), [av7] "v"(av[7])
    : "v39", "v64", "v65", "v66", "v67", "v68", "v69", "v70", "v71", "v72", "v73", "v74", "v75", "v76", "v77", "v78", "v79", "v96", "v97", "v98", "v99", "v100", "v101", "v102", "v103", "v104", "v105", "v106", "v107", "v108", "v109", "v110", "v111", "v112", "v113", "v114", "v115", "v116", "v117", "v118", "v119", "v120", "v121", "v122", "v123", "v124", "v125", "v126", "v127", "vcc", "scc", "m0", "memory");
}
__device__ __forceinline__ void fast_ld0_3(const u32x32 (&W)[4], u32x16& pd, unsigned ea, unsigned da, int& b, int& lf, int cp, unsigned sa, unsigned mu, unsigned ad,
    unsigned rw, unsigned ab, const char* p1, const char* p0, unsigned l1, unsigned l0, const unsigned (&av)[8]) {
  asm volatile("s_setprio 0\n\t"
    "s_waitcnt vmcnt(12)\n\t"
    "s_barrier\n\t"
    "ds_read_b128 v[96:99], %[ea] offset:0\n\t"
    "ds_read_b128 v[100:103], %[ea] offset:2048\n\t"
    "ds_read_b128 v[104:107], %[da] offset:0\n\t"
    "ds_read_b128 v[108:111], %[da] offset:2048\n\t"
    "ds_read_b128 v[112:115], %[ea] offset:4096\n\t"
    "ds_read_b128 v[116:119], %[ea] offset:6144\n\t"
    "s_branch 2f\n\t"
    "1:\n\t"
    "s_waitcnt vmcnt(12)\n\t"
    "s_nop 1\n\t"
    "s_barrier\n\t"
    "2:\n\t"
    "s_waitcnt lgkmcnt(4)\n\t"
    "v_mfma_f32_16x16x32_bf16 v[64:67], v[128:131], v[96:99], 0\n\t"
    "ds_read_b128 v[120:123], %[da] offset:4096\n\t"
    "s_add_u32 m0, %[l1], 0\n\t"
    "ds_write_b128 %[rw], v[80:83] offset:0\n\t"
    "v_mfma_f32_16x16x32_bf16 v[68:71], v[132:135], v[96:99], 0\n\t"
    "ds_read_b128 v[124:127], %[da] offset:6144\n\t"
    "global_load_lds_dwordx4 %[av0], s[36:37] sc1\n\t"
    "ds_write_b128 %[rw], v[84:87] offset:1024\n\t"
    "v_mfma_f32_16x16x32_bf16 v[72:75], v[128:131], v[100:103], 0\n\t"
    "s_add_u32 m0, %[l1], 32768\n\t"
    "ds_write_b128 %[rw], v[88:91] offset:2048\n\t"
    "v_mfma_f32_16x16x32_bf16 v[76:79], v[132:135], v[100:103], 0\n\t"
    "global_load_lds_dwordx4 %[av1], s[36:37] sc1\n\t"
    "ds_write_b128 %[rw], v[92:95] offset:3072\n\t"
    "s_waitcnt lgkmcnt(8)\n\t"
    "v_mfma_f32_16x16x32_bf16 v[64:67], v[136:139], v[104:107], v[64:67]\n\t"
    "ds_read_b128 v[96:99], %[ea] offset:8192\n\t"
    "s_add_u32 m0, %[l1], 4096\n\t"
    "v_mfma_f32_16x16x32_bf16 v[68:71], v[140:143], v[104:107], v[68:71]\n\t"
    "ds_read_b128 v[100:103], %[ea] offset:10240\n\t"
    "global_load_lds_dwordx4 %[av2], s[36:37] sc1\n\t"
    "v_mfma_f32_16x16x32_bf16 v[72:75], v[136:139], v[108:111], v[72:75]\n\t"
    "s_add_u32 m0, %[l1], 36864\n\t"
    "v_mfma_f32_16x16x32_bf16 v[76:79], v[140:143], v[108:111], v[76:79]\n\t"
    "global_load_lds_dwordx4 %[av3], s[36:37] sc1\n\t"
    "s_waitcnt lgkmcnt(8)\n\t"
    "v_mfma_f32_16x16x32_bf16 v[64:67], v[144:147], v[112:115], v[64:67]\n\t"
    "ds_read_b128 v[104:107], %[da] offset:8192\n\t"
    "s_add_u32 m0, %[l1], 8192\n\t"
    "v_mfma_f32_16x16x32_bf16 v[68:71], v[148:151], v[112:115], v[68:71]\n\t"
    "ds_read_b128 v[108:111], %[da] offset:10240\n\t"
    "global_load_lds_dwordx4 %[av4], s[36:37] sc1\n\t"
    "v_mfma_f32_16x16x32_bf16 v[72:75], v[144:147], v[116:119], v[72:75]\n\t"
    "s_add_u32 m0, %[l1], 40960\n\t"
    "v_mfma_f32_16x16x32_bf16 v[76:79], v[148:151], v[116:119], v[76:79]\n\t"
    "global_load_lds_dwordx4 %[av5], s[36:37] sc1\n\t"
    "s_waitcnt lgkmcnt(7)\n\t"
    "v_mfma_f32_16x16x32_bf16 v[64:67], v[152:155], v[120:123], v[64:67]\n\t"
    "ds_read_b128 v[112:115], %[ea] offset:12288\n\t"
    "s_add_u32 m0, %[l1], 12288\n\t"
    "v_mfma_f32_16x16x32_bf16 v[68:71], v[156:159], v[120:123], v[68:71]\n\t"
    "ds_read_b128 v[116:119], %[ea] offset:14336\n\t"
    "global_load_lds_dwordx4 %[av6], s[36:37] sc1\n\t"
    "v_mfma_f32_16x16x32_bf16 v[72:75], v[152:155], v[124:127], v[72:75]\n\t"
    "s_add_u32 m0, %[l1], 45056\n\t"
    "v_mfma_f32_16x16x32_bf16 v[76:79], v[156:159], v[124:127], v[76:79]\n\t"
    "global_load_lds_dwordx4 %[av7], s[36:37] sc1\n\t"
    "s_waitcnt lgkmcnt(4)\n\t"
    "v_mfma_f32_16x16x32_bf16 v[64:67], v[160:163], v[96:99], v[64:67]\n\t"
    "ds_read_b128 v[120:123], %[da] offset:12288\n\t"
    "v_mfma_f32_16x16x32_bf16 v[68:71], v[164:167], v[96:99], v[68:71]\n\t"
    "ds_read_b128 v[124:127], %[da] offset:14336\n\t"
    "v_mfma_f32_16x16x32_bf16 v[72:75], v[160:163], v[100:103], v[72:75]\n\t"
    "v_mfma_f32_16x16x32_bf16 v[76:79], v[164:167], v[100:103], v[76:79]\n\t"
    "s_waitcnt lgkmcnt(4)\n\t"
    "v_mfma_f32_16x16x32_bf16 v[64:67], v[168:171], v[104:107], v[64:67]\n\t"
    "ds_read_b128 v[96:99], %[ea] offset:16384\n\t"
    "v_mfma_f32_16x16x32_bf16 v[68:71], v[172:175], v[104:107], v[68:71]\n\t"
    "ds_read_b128 v[100:103], %[ea] offset:18432\n\t"
    "v_mfma_f32_16x16x32_bf16 v[72:75], v[168:171], v[108:111], v[72:75]\n\t"
    "v_mfma_f32_16x16x32_bf16 v[76:79], v[172:175], v[108:111], v[76:79]\n\t"
    "s_waitcnt lgkmcnt(4)\n\t"
    "v_mfma_f32_16x16x32_bf16 v[64:67], v[176:179], v[112:115], v[64:67]\n\t"
    "ds_read_b128 v[104:107], %[da] offset:16384\n\t"
    "v_mfma_f32_16x16x32_bf16 v[68:71], v[180:183], v[112:115], v[68:71]\n\t"
    "ds_read_b128 v[108:111], %[da] offset:18432\n\t"
    "v_mfma_f32_16x16x32_bf16 v[72:75], v[176:179], v[116:119], v[72:75]\n\t"
    "v_mfma_f32_16x16x32_bf16 v[76:79], v[180:183], v[116:119], v[76:79]\n\t"
    "s_waitcnt lgkmcnt(4)\n\t"
    "v_mfma_f32_16x16x32_bf16 v[64:67], v[184:187], v[120:123], v[64:67]\n\t"
    "ds_read_b128 v[112:115], %[ea] offset:20480\n\t"
    "v_mfma_f32_16x16x32_bf16 v[68:71], v[188:191], v[120:123], v[68:71]\n\t"
    "ds_read_b128 v[116:119], %[ea] offset:22528\n\t"
    "v_mfma_f32_16x16x32_bf16 v[72:75], v[184:187], v[124:127], v[72:75]\n\t"
    "v_mfma_f32_16x16x32_bf16 v[76:79], v[188:191], v[124:127], v[76:79]\n\t"
    "s_waitcnt vmcnt(12)\n\t"
    "s_barrier\n\t"
    "s_waitcnt lgkmcnt(4)\n\t"
    "v_mfma_f32_16x16x32_bf16 v[64:67], v[192:195], v[96:99], v[64:67]\n\t"
    "ds_read_b128 v[120:123], %[da] offset:20480\n\t"
    "s_add_u32 m0, %[l0], 0\n\t"
    "v_mfma_f32_16x16x32_bf16 v[68:71], v[196:199], v[96:99], v[68:71]\n\t"
    "ds_read_b128 v[124:127], %[da] offset:22528\n\t"
    "global_load_lds_dwordx4 %[av0], s[38:39] sc1\n\t"
    "v_mfma_f32_16x16x32_bf16 v[72:75], v[192:195], v[100:103], v[72:75]\n\t"
    "s_add_u32 m0, %[l0], 32768\n\t"
    "v_mfma_f32_16x16x32_bf16 v[76:79], v[196:199], v[100:103], v[76:79]\n\t"
    "global_load_lds_dwordx4 %[av1], s[38:39] sc1\n\t"
    "s_waitcnt lgkmcnt(4)\n\t"
    "v_mfma_f32_16x16x32_bf16 v[64:67], v[200:203], v[104:107], v[64:67]\n\t"
    "ds_read_b128 v[96:99], %[ea] offset:24576\n\t"
    "s_add_u32 m0, %[l0], 4096\n\t"
    "v_mfma_f32_16x16x32_bf16 v[68:71], v[204:207], v[104:107], v[68:71]\n\t"
    "ds_read_b128 v[100:103], %[ea] offset:26624\n\t"
    "global_load_lds_dwordx4 %[av2], s[38:39] sc1\n\t"
    "v_mfma_f32_16x16x32_bf16 v[72:75], v[200:203], v[108:111], v[72:75]\n\t"
    "s_add_u32 m0, %[l0], 36864\n\t"
    "v_mfma_f32_16x16x32_bf16 v[76:79], v[204:207], v[108:111], v[76:79]\n\t"
    "global_load_lds_dwordx4 %[av3], s[38:39] sc1\n\t"
    "s_add_u32 s36, s36, %[ab]\n\t"
    "s_addc_u32 s37, s37, 0\n\t"
    "s_waitcnt lgkmcnt(4)\n\t"
    "v_mfma_f32_16x16x32_bf16 v[64:67], v[208:211], v[112:115], v[64:67]\n\t"
    "ds_read_b128 v[104:107], %[da] offset:24576\n\t"
    "s_add_u32 m0, %[l0], 8192\n\t"
    "v_mfma_f32_16x16x32_bf16 v[68:71], v[212:215], v[112:115], v[68:71]\n\t"
    "ds_read_b128 v[108:111], %[da] offset:26624\n\t"
    "global_load_lds_dwordx4 %[av4], s[38:39] sc1\n\t"
    "v_mfma_f32_16x16x32_bf16 v[72:75], v[208:211], v[116:119], v[72:75]\n\t"
    "ds_read_b32 v39, %[sa]\n\t"
    "s_add_u32 m0, %[l0], 40960\n\t"
    "v_mfma_f32_16x16x32_bf16 v[76:79], v[212:215], v[116:119], v[76:79]\n\t"
    "global_load_lds_dwordx4 %[av5], s[38:39] sc1\n\t"
    "s_xor_b32 %[l1], %[l1], 0x10000\n\t"
    "s_waitcnt lgkmcnt(5)\n\t"
    "v_mfma_f32_16x16x32_bf16 v[64:67], v[216:219], v[120:123], v[64:67]\n\t"
    "ds_read_b128 v[112:115], %[ea] offset:28672\n\t"
    "s_add_u32 m0, %[l0], 12288\n\t"
    "v_mfma_f32_16x16x32_bf16 v[68:71], v[220:223], v[120:123], v[68:71]\n\t"
    "ds_read_b128 v[116:119], %[ea] offset:30720\n\t"
    "global_load_lds_dwordx4 %[av6], s[38:39] sc1\n\t"
    "v_mfma_f32_16x16x32_bf16 v[72:75], v[216:219], v[124:127], v[72:75]\n\t"
    "v_xor_b32 %[ea], 0x10000, %[ea]\n\t"
    "s_add_u32 m0, %[l0], 45056\n\t"
    "v_mfma_f32_16x16x32_bf16 v[76:79], v[220:223], v[124:127], v[76:79]\n\t"
    "global_load_lds_dwordx4 %[av7], s[38:39] sc1\n\t"
    "s_waitcnt lgkmcnt(5)\n\t"
    "v_mfma_f32_16x16x32_bf16 v[64:67], v[224:227], v[96:99], v[64:67]\n\t"
    "ds_read_b128 v[120:123], %[da] offset:28672\n\t"
    "v_mfma_f32_16x16x32_bf16 v[68:71], v[228:231], v[96:99], v[68:71]\n\t"
    "ds_read_b128 v[124:127], %[da] offset:30720\n\t"
    "v_mfma_f32_16x16x32_bf16 v[72:75], v[224:227], v[100:103], v[72:75]\n\t"
    "v_xor_b32 %[da], 0x10000, %[da]\n\t"
    "v_mfma_f32_16x16x32_bf16 v[76:79], v[228:231], v[100:103], v[76:79]\n\t"
    "s_waitcnt lgkmcnt(5)\n\t"
    "v_mfma_f32_16x16x32_bf16 v[64:67], v[232:235], v[104:107], v[64:67]\n\t"
    "ds_read_b128 v[96:99], %[ea] offset:0\n\t"
    "v_mfma_f32_16x16x32_bf16 v[68:71], v[236:239], v[104:107], v[68:71]\n\t"
    "ds_read_b128 v[100:103], %[ea] offset:2048\n\t"
    "v_mfma_f32_16x16x32_bf16 v[72:75], v[232:235], v[108:111], v[72:75]\n\t"
    "v_mfma_f32_16x16x32_bf16 v[76:79], v[236:239], v[108:111], v[76:79]\n\t"
    "s_waitcnt lgkmcnt(4)\n\t"
    "v_mfma_f32_16x16x32_bf16 v[64:67], v[240:243], v[112:115], v[64:67]\n\t"
    "ds_read_b128 v[104:107], %[da] offset:0\n\t"
    "v_mad_u32_u24 v39, v39, %[mu], %[ad]\n\t"
    "v_mfma_f32_16x16x32_bf16 v[68:71], v[244:247], v[112:115], v[68:71]\n\t"
    "ds_read_b128 v[108:111], %[da] offset:2048\n\t"
    "v_cmp_ge_i32 vcc, %[lf], v39\n\t"
    "s_cmp_eq_u64 vcc, 0\n\t"
    "s_addc_u32 %[lf], %[lf], 0\n\t"
    "v_mfma_f32_16x16x32_bf16 v[72:75], v[240:243], v[116:119], v[72:75]\n\t"
    "v_mfma_f32_16x16x32_bf16 v[76:79], v[244:247], v[116:119], v[76:79]\n\t"
    "v_cmp_ge_i32 vcc, %[lf], v39\n\t"
    "s_cmp_eq_u64 vcc, 0\n\t"
    "s_addc_u32 %[lf], %[lf], 0\n\t"
    "s_waitcnt lgkmcnt(4)\n\t"
    "v_mfma_f32_16x16x32_bf16 v[80:83], v[248:251], v[120:123], v[64:67]\n\t"
    "ds_read_b128 v[112:115], %[ea] offset:4096\n\t"
    "s_min_i32 %[lf], %[lf], %[cp]\n\t"
    "s_add_u32 %[b], %[b], 1\n\t"
    "v_mfma_f32_16x16x32_bf16 v[84:87], v[252:255], v[120:123], v[68:71]\n\t"
    "ds_read_b128 v[116:119], %[ea] offset:6144\n\t"
    "v_mfma_f32_16x16x32_bf16 v[88:91], v[248:251], v[124:127], v[72:75]\n\t"
    "v_mfma_f32_16x16x32_bf16 v[92:95], v[252:255], v[124:127], v[76:79]\n\t"
    "s_add_u32 s38, s38, %[ab]\n\t"
    "s_addc_u32 s39, s39, 0\n\t"
    "s_xor_b32 %[l0], %[l0], 0x10000\n\t"
    "s_cmp_lt_i32 %[b], %[lf]\n\t"
    "s_cbranch_scc1 1b\n\t"
    "s_waitcnt lgkmcnt(0)\n\t"
    "s_nop 4"
    : "=&{v[80:95]}"(pd), [ea] "+&v"(ea), [da] "+&v"(da), [b] "+&s"(b), [lf] "+&s"(lf), "+{s[36:37]}"(p1), "+{s[38:39]}"(p0), [sa] "+&v"(sa), [l1] "+&s"(l1), [l0] "+&s"(l0)
    : [rw] "v"(rw), [ab] "s"(ab), [cp] "s"(cp), [mu] "s"(mu), [ad] "v"(ad), "{v[128:159]}"(W[0]), "{v[160:191]}"(W[1]), "{v[192:223]}"(W[2]), "{v[224:255]}"(W[3]), [av0] "v"(av[0]), [av1] "v"(av[1]), [av2] "v"(av[2]), [av3] "v"(av[3]), [av4] "v"(av[4]), [av5] "v"(av[5]), [av6] "v"(av[6]), [av7] "v"(av[7])
    : "v39", "v64", "v65", "v66", "v67", "v68", "v69", "v70", "v71", "v72", "v73", "v74", "v75", "v76", "v77", "v78", "v79", "v96", "v97", "v98", "v99", "v100", "v101", "v102", "v103", "v104", "v105", "v106", "v107", "v108", "v109", "v110", "v111", "v112", "v113", "v114", "v115", "v116", "v117", "v118", "v119", "v120", "v121", "v122", "v123", "v124", "v125", "v126", "v127", "vcc", "scc", "m0", "memory");
}
__device__ __forceinline__ void fast_ld2_3(const u32x32 (&W)[4], u32x16& pd, unsigned ea, unsigned da, int& b, int& lf, int cp, unsigned sa, unsigned mu, unsigned ad,
    unsigned rw, unsigned ab, const char* p1, const char* p0, unsigned l1, unsigned l0, const unsigned (&av)[8]) {
  asm volatile("s_setprio 0\n\t"
    "s_waitcnt vmcnt(6)\n\t"
    "s_barrier\n\t"
    "ds_read_b128 v[96:99], %[ea] offset:0\n\t"
    "ds_read_b128 v[100:103], %[ea] offset:2048\n\t"
    "ds_read_b128 v[104:107], %[da] offset:0\n\t"
    "ds_read_b128 v[108:111], %[da] offset:2048\n\t"
    "ds_read_b128 v[112:115], %[ea] offset:4096\n\t"
    "ds_read_b128 v[116:119], %[ea] offset:6144\n\t"
    "s_branch 2f\n\t"
    "1:\n\t"
    "s_waitcnt vmcnt(6)\n\t"
    "s_nop 1\n\t"
    "s_barrier\n\t"
    "2:\n\t"
    "s_waitcnt lgkmcnt(4)\n\t"
    "v_mfma_f32_16x16x32_bf16 v[64:67], v[128:131], v[96:99], 0\n\t"
    "ds_read_b128 v[120:123], %[da] offset:4096\n\t"
    "s_add_u32 m0, %[l1], 0\n\t"
    "ds_write_b128 %[rw], v[80:83] offset:0\n\t"
    "v_mfma_f32_16x16x32_bf16 v[68:71], v[132:135], v[96:99], 0\n\t"
    "ds_read_b128 v[124:127], %[da] offset:6144\n\t"
    "global_load_lds_dwordx4 %[av0], s[36:37] sc1\n\t"
    "ds_write_b128 %[rw], v[84:87] offset:1024\n\t"
    "v_mfma_f32_16x16x32_bf16 v[72:75], v[128:131], v[100:103], 0\n\t"
    "ds_write_b128 %[rw], v[88:91] offset:2048\n\t"
    "v_mfma_f32_16x16x32_bf16 v[76:79], v[132:135], v[100:103], 0\n\t"
    "ds_write_b128 %[rw], v[92:95] offset:3072\n\t"
    "s_waitcnt lgkmcnt(8)\n\t"
    "v_mfma_f32_16x16x32_bf16 v[64:67], v[136:139], v[104:107], v[64:67]\n\t"
    "ds_read_b128 v[96:99], %[ea] offset:8192\n\t"
    "s_add_u32 m0, %[l1], 4096\n\t"
    "v_mfma_f32_16x16x32_bf16 v[68:71], v[140:143], v[104:107], v[68:71]\n\t"
    "ds_read_b128 v[100:103], %[ea] offset:10240\n\t"
    "global_load_lds_dwordx4 %[av2], s[36:37] sc1\n\t"
    "v_mfma_f32_16x16x32_bf16 v[72:75], v[136:139], v[108:111], v[72:75]\n\t"
    "v_mfma_f32_16x16x32_bf16 v[76:79], v[140:143], v[108:111], v[76:79]\n\t"
    "s_waitcnt lgkmcnt(8)\n\t"
    "v_mfma_f32_16x16x32_bf16 v[64:67], v[144:147], v[112:115], v[64:67]\n\t"
    "ds_read_b128 v[104:107], %[da] offset:8192\n\t"
    "s_add_u32 m0, %[l1], 8192\n\t"
    "v_mfma_f32_16x16x32_bf16 v[68:71], v[148:151], v[112:115], v[68:71]\n\t"
    "ds_read_b128 v[108:111], %[da] offset:10240\n\t"
    "global_load_lds_dwordx4 %[av4], s[36:37] sc1\n\t"
    "v_mfma_f32_16x16x32_bf16 v[72:75], v[144:147], v[116:119], v[72:75]\n\t"
    "v_mfma_f32_16x16x32_bf16 v[76:79], v[148:151], v[116:119], v[76:79]\n\t"
    "s_waitcnt lgkmcnt(7)\n\t"
    "v_mfma_f32_16x16x32_bf16 v[64:67], v[152:155], v[120:123], v[64:67]\n\t"
    "ds_read_b128 v[112:115], %[ea] offset:12288\n\t"
    "s_add_u32 m0, %[l1], 12288\n\t"
    "v_mfma_f32_16x16x32_bf16 v[68:71], v[156:159], v[120:123], v[68:71]\n\t"
    "ds_read_b128 v[116:119], %[ea] offset:14336\n\t"
    "global_load_lds_dwordx4 %[av6], s[36:37] sc1\n\t"
    "v_mfma_f32_16x16x32_bf16 v[72:75], v[152:155], v[124:127], v[72:75]\n\t"
    "v_mfma_f32_16x16x32_bf16 v[76:79], v[156:159], v[124:127], v[76:79]\n\t"
    "s_waitcnt lgkmcnt(4)\n\t"
    "v_mfma_f32_16x16x32_bf16 v[64:67], v[160:163], v[96:99], v[64:67]\n\t"
    "ds_read_b128 v[120:123], %[da] offset:12288\n\t"
    "v_mfma_f32_16x16x32_bf16 v[68:71], v[164:167], v[96:99], v[68:71]\n\t"
    "ds_read_b128 v[124:127], %[da] offset:14336\n\t"
    "v_mfma_f32_16x16x32_bf16 v[72:75], v[160:163], v[100:103], v[72:75]\n\t"
    "v_mfma_f32_16x16x32_bf16 v[76:79], v[164:167], v[100:103], v[76:79]\n\t"
    "s_waitcnt lgkmcnt(4)\n\t"
    "v_mfma_f32_16x16x32_bf16 v[64:67], v[168:171], v[104:107], v[64:67]\n\t"
    "ds_read_b128 v[96:99], %[ea] offset:16384\n\t"
    "v_mfma_f32_16x16x32_bf16 v[68:71], v[172:175], v[104:107], v[68:71]\n\t"
    "ds_read_b128 v[100:103], %[ea] offset:18432\n\t"
    "v_mfma_f32_16x16x32_bf16 v[72:75], v[168:171], v[108:111], v[72:75]\n\t"
    "v_mfma_f32_16x16x32_bf16 v[76:79], v[172:175], v[108:111], v[76:79]\n\t"
    "s_waitcnt lgkmcnt(4)\n\t"
    "v_mfma_f32_16x16x32_bf16 v[64:67], v[176:179], v[112:115], v[64:67]\n\t"
    "ds_read_b128 v[104:107], %[da] offset:16384\n\t"
    "v_mfma_f32_16x16x32_bf16 v[68:71], v[180:183], v[112:115], v[68:71]\n\t"
    "ds_read_b128 v[108:111], %[da] offset:18432\n\t"
    "v_mfma_f32_16x16x32_bf16 v[72:75], v[176:179], v[116:119], v[72:75]\n\t"
    "v_mfma_f32_16x16x32_bf16 v[76:79], v[180:183], v[116:119], v[76:79]\n\t"
    "s_waitcnt lgkmcnt(4)\n\t"
    "v_mfma_f32_16x16x32_bf16 v[64:67], v[184:187], v[120:123], v[64:67]\n\t"
    "ds_read_b128 v[112:115], %[ea] offset:20480\n\t"
    "v_mfma_f32_16x16x32_bf16 v[68:71], v[188:191], v[120:123], v[68:71]\n\t"
    "ds_read_b128 v[116:119], %[ea] offset:22528\n\t"
    "v_mfma_f32_16x16x32_bf16 v[72:75], v[184:187], v[124:127], v[72:75]\n\t"
    "v_mfma_f32_16x16x32_bf16 v[76:79], v[188:191], v[124:127], v[76:79]\n\t"
    "s_waitcnt vmcnt(6)\n\t"
    "s_barrier\n\t"
    "s_waitcnt lgkmcnt(4)\n\t"
    "v_mfma_f32_16x16x32_bf16 v[64:67], v[192:195], v[96:99], v[64:67]\n\t"
    "ds_read_b128 v[120:123], %[da] offset:20480\n\t"
    "s_add_u32 m0, %[l0], 0\n\t"
    "v_mfma_f32_16x16x32_bf16 v[68:71], v[196:199], v[96:99], v[68:71]\n\t"
    "ds_read_b128 v[124:127], %[da] offset:22528\n\t"
    "global_load_lds_dwordx4 %[av0], s[38:39] sc1\n\t"
    "v_mfma_f32_16x16x32_bf16 v[72:75], v[192:195], v[100:103], v[72:75]\n\t"
    "v_mfma_f32_16x16x32_bf16 v[76:79], v[196:199], v[100:103], v[76:79]\n\t"
    "s_waitcnt lgkmcnt(4)\n\t"
    "v_mfma_f32_16x16x32_bf16 v[64:67], v[200:203], v[104:107], v[64:67]\n\t"
    "ds_read_b128 v[96:99], %[ea] offset:24576\n\t"
    "s_add_u32 m0, %[l0], 4096\n\t"
    "v_mfma_f32_16x16x32_bf16 v[68:71], v[204:207], v[104:107], v[68:71]\n\t"
    "ds_read_b128 v[100:103], %[ea] offset:26624\n\t"
    "global_load_lds_dwordx4 %[av2], s[38:39] sc1\n\t"
    "v_mfma_f32_16x16x32_bf16 v[72:75], v[200:203], v[108:111], v[72:75]\n\t"
    "v_mfma_f32_16x16x32_bf16 v[76:79], v[204:207], v[108:111], v[76:79]\n\t"
    "s_add_u32 s36, s36, %[ab]\n\t"
    "s_addc_u32 s37, s37, 0\n\t"
    "s_waitcnt lgkmcnt(4)\n\t"
    "v_mfma_f32_16x16x32_bf16 v[64:67], v[208:211], v[112:115], v[64:67]\n\t"
    "ds_read_b128 v[104:107], %[da] offset:24576\n\t"
    "s_add_u32 m0, %[l0], 8192\n\t"
    "v_mfma_f32_16x16x32_bf16 v[68:71], v[212:215], v[112:115], v[68:71]\n\t"
    "ds_read_b128 v[108:111], %[da] offset:26624\n\t"
    "global_load_lds_dwordx4 %[av4], s[38:39] sc1\n\t"
    "v_mfma_f32_16x16x32_bf16 v[72:75], v[208:211], v[116:119], v[72:75]\n\t"
    "ds_read_b32 v39, %[sa]\n\t"
    "v_mfma_f32_16x16x32_bf16 v[76:79], v[212:215], v[116:119], v[76:79]\n\t"
    "s_xor_b32 %[l1], %[l1], 0x10000\n\t"
    "s_waitcnt lgkmcnt(5)\n\t"
    "v_mfma_f32_16x16x32_bf16 v[64:67], v[216:219], v[120:123], v[64:67]\n\t"
    "ds_read_b128 v[112:115], %[ea] offset:28672\n\t"
    "s_add_u32 m0, %[l0], 12288\n\t"
    "v_mfma_f32_16x16x32_bf16 v[68:71], v[220:223], v[120:123], v[68:71]\n\t"
    "ds_read_b128 v[116:119], %[ea] offset:30720\n\t"
    "global_load_lds_dwordx4 %[av6], s[38:39] sc1\n\t"
    "v_mfma_f32_16x16x32_bf16 v[72:75], v[216:219], v[124:127], v[72:75]\n\t"
    "v_xor_b32 %[ea], 0x10000, %[ea]\n\t"
    "v_mfma_f32_16x16x32_bf16 v[76:79], v[220:223], v[124:127], v[76:79]\n\t"
    "s_waitcnt lgkmcnt(5)\n\t"
    "v_mfma_f32_16x16x32_bf16 v[64:67], v[224:227], v[96:99], v[64:67]\n\t"
    "ds_read_b128 v[120:123], %[da] offset:28672\n\t"
    "v_mfma_f32_16x16x32_bf16 v[68:71], v[228:231], v[96:99], v[68:71]\n\t"
    "ds_read_b128 v[124:127], %[da] offset:30720\n\t"
    "v_mfma_f32_16x16x32_bf16 v[72:75], v[224:227], v[100:103], v[72:75]\n\t"
    "v_xor_b32 %[da], 0x10000, %[da]\n\t"
    "v_mfma_f32_16x16x32_bf16 v[76:79], v[228:231], v[100:103], v[76:79]\n\t"
    "s_waitcnt lgkmcnt(5)\n\t"
    "v_mfma_f32_16x16x32_bf16 v[64:67], v[232:235], v[104:107], v[64:67]\n\t"
    "ds_read_b128 v[96:99], %[ea] offset:0\n\t"
    "v_mfma_f32_16x16x32_bf16 v[68:71], v[236:239], v[104:107], v[68:71]\n\t"
    "ds_read_b128 v[100:103], %[ea] offset:2048\n\t"
    "v_mfma_f32_16x16x32_bf16 v[72:75], v[232:235], v[108:111], v[72:75]\n\t"
    "v_mfma_f32_16x16x32_bf16 v[76:79], v[236:239], v[108:111], v[76:79]\n\t"
    "s_waitcnt lgkmcnt(4)\n\t"
    "v_mfma_f32_16x16x32_bf16 v[64:67], v[240:243], v[112:115], v[64:67]\n\t"
    "ds_read_b128 v[104:107], %[da] offset:0\n\t"
    "v_mad_u32_u24 v39, v39, %[mu], %[ad]\n\t"
    "v_mfma_f32_16x16x32_bf16 v[68:71], v[244:247], v[112:115], v[68:71]\n\t"
    "ds_read_b128 v[108:111], %[da] offset:2048\n\t"
    "v_cmp_ge_i32 vcc, %[lf], v39\n\t"
    "s_cmp_eq_u64 vcc, 0\n\t"
    "s_addc_u32 %[lf], %[lf], 0\n\t"
    "v_mfma_f32_16x16x32_bf16 v[72:75], v[240:243], v[116:119], v[72:75]\n\t"
    "v_mfma_f32_16x16x32_bf16 v[76:79], v[244:247], v[116:119], v[76:79]\n\t"
    "v_cmp_ge_i32 vcc, %[lf], v39\n\t"
    "s_cmp_eq_u64 vcc, 0\n\t"
    "s_addc_u32 %[lf], %[lf], 0\n\t"
    "s_waitcnt lgkmcnt(4)\n\t"
    "v_mfma_f32_16x16x32_bf16 v[80:83], v[248:251], v[120:123], v[64:67]\n\t"
    "ds_read_b128 v[112:115], %[ea] offset:4096\n\t"
    "s_min_i32 %[lf], %[lf], %[cp]\n\t"
    "s_add_u32 %[b], %[b], 1\n\t"
    "v_mfma_f32_16x16x32_bf16 v[84:87], v[252:255], v[120:123], v[68:71]\n\t"
    "ds_read_b128 v[116:119], %[ea] offset:6144\n\t"
    "v_mfma_f32_16x16x32_bf16 v[88:91], v[248:251], v[124:127], v[72:75]\n\t"
    "v_mfma_f32_16x16x32_bf16 v[92:95], v[252:255], v[124:127], v[76:79]\n\t"
    "s_add_u32 s38, s38, %[ab]\n\t"
    "s_addc_u32 s39, s39, 0\n\t"
    "s_xor_b32 %[l0], %[l0], 0x10000\n\t"
    "s_cmp_lt_i32 %[b], %[lf]\n\t"
    "s_cbranch_scc1 1b\n\t"
    "s_waitcnt lgkmcnt(0)\n\t"
    "s_nop 4"
    : "=&{v[80:95]}"(pd), [ea] "+&v"(ea), [da] "+&v"(da), [b] "+&s"(b), [lf] "+&s"(lf), "+{s[36:37]}"(p1), "+{s[38:39]}"(p0), [sa] "+&v"(sa), [l1] "+&s"(l1), [l0] "+&s"(l0)
    : [rw] "v"(rw), [ab] "s"(ab), [cp] "s"(cp), [mu] "s"(mu), [ad] "v"(ad), "{v[128:159]}"(W[0]), "{v[160:191]}"(W[1]), "{v[192:223]}"(W[2]), "{v[224:255]}"(W[3]), [av0] "v"(av[0]), [av1] "v"(av[1]), [av2] "v"(av[2]), [av3] "v"(av[3]), [av4] "v"(av[4]), [av5] "v"(av[5]), [av6] "v"(av[6]), [av7] "v"(av[7])
    : "v39", "v64", "v65", "v66", "v67", "v68", "v69", "v70", "v71", "v72", "v73", "v74", "v75", "v76", "v77", "v78", "v79", "v96", "v97", "v98", "v99", "v100", "v101", "v102", "v103", "v104", "v105", "v106", "v107", "v108", "v109", "v110", "v111", "v112", "v113", "v114", "v115", "v116", "v117", "v118", "v119", "v120", "v121", "v122", "v123", "v124", "v125", "v126", "v127", "vcc", "scc", "m0", "memory");
}
__device__ __forceinline__ void fast_st0(const u32x32 (&W)[4], u32x16& pd, unsigned ea, unsigned da, int& b, int& lf, int cp, unsigned sa, unsigned mu, unsigned ad,
    unsigned rw, unsigned ab, const char* p1, const char* p0, unsigned co0, unsigned co1, int po, int pf, unsigned w0, unsigned sl, unsigned dv, const char* db, const char* pg) {
  unsigned st;
  asm volatile("s_barrier\n\t"
    "ds_read_b128 v[96:99], %[ea] offset:0\n\t"
    "ds_read_b128 v[100:103], %[ea] offset:2048\n\t"
    "ds_read_b128 v[104:107], %[da] offset:0\n\t"
    "ds_read_b128 v[108:111], %[da] offset:2048\n\t"
    "ds_read_b128 v[112:115], %[ea] offset:4096\n\t"
    "ds_read_b128 v[116:119], %[ea] offset:6144\n\t"
    "s_branch 2f\n\t"
    "1:\n\t"
    "s_barrier\n\t"
    "2:\n\t"
    "s_waitcnt lgkmcnt(4)\n\t"
    "v_mfma_f32_16x16x32_bf16 v[64:67], v[128:131], v[96:99], 0\n\t"
    "ds_read_b128 v[120:123], %[da] offset:4096\n\t"
    "v_mfma_f32_16x16x32_bf16 v[68:71], v[132:135], v[96:99], 0\n\t"
    "ds_read_b128 v[124:127], %[da] offset:6144\n\t"
    "v_mfma_f32_16x16x32_bf16 v[72:75], v[128:131], v[100:103], 0\n\t"
    "v_mfma_f32_16x16x32_bf16 v[76:79], v[132:135], v[100:103], 0\n\t"
    "s_waitcnt lgkmcnt(4)\n\t"
    "v_mfma_f32_16x16x32_bf16 v[64:67], v[136:139], v[104:107], v[64:67]\n\t"
    "ds_read_b128 v[96:99], %[ea] offset:8192\n\t"
    "v_mfma_f32_16x16x32_bf16 v[68:71], v[140:143], v[104:107], v[68:71]\n\t"
    "ds_read_b128 v[100:103], %[ea] offset:10240\n\t"
    "v_mfma_f32_16x16x32_bf16 v[72:75], v[136:139], v[108:111], v[72:75]\n\t"
    "v_mfma_f32_16x16x32_bf16 v[76:79], v[140:143], v[108:111], v[76:79]\n\t"
    "s_cmp_eq_u32 %[w0], 1\n\t"
    "s_cbranch_scc1 7f\n\t"
    "8:\n\t"
    "s_waitcnt lgkmcnt(4)\n\t"
    "v_mfma_f32_16x16x32_bf16 v[64:67], v[144:147], v[112:115], v[64:67]\n\t"
    "ds_read_b128 v[104:107], %[da] offset:8192\n\t"
    "v_mfma_f32_16x16x32_bf16 v[68:71], v[148:151], v[112:115], v[68:71]\n\t"
    "ds_read_b128 v[108:111], %[da] offset:10240\n\t"
    "v_mfma_f32_16x16x32_bf16 v[72:75], v[144:147], v[116:119], v[72:75]\n\t"
    "v_mfma_f32_16x16x32_bf16 v[76:79], v[148:151], v[116:119], v[76:79]\n\t"
    "s_waitcnt lgkmcnt(4)\n\t"
    "v_mfma_f32_16x16x32_bf16 v[64:67], v[152:155], v[120:123], v[64:67]\n\t"
    "ds_read_b128 v[112:115], %[ea] offset:12288\n\t"
    "v_mfma_f32_16x16x32_bf16 v[68:71], v[156:159], v[120:123], v[68:71]\n\t"
    "ds_read_b128 v[116:119], %[ea] offset:14336\n\t"
    "v_mfma_f32_16x16x32_bf16 v[72:75], v[152:155], v[124:127], v[72:75]\n\t"
    "v_mfma_f32_16x16x32_bf16 v[76:79], v[156:159], v[124:127], v[76:79]\n\t"
    "s_waitcnt lgkmcnt(4)\n\t"
    "v_mfma_f32_16x16x32_bf16 v[64:67], v[160:163], v[96:99], v[64:67]\n\t"
    "ds_read_b128 v[120:123], %[da] offset:12288\n\t"
    "v_mfma_f32_16x16x32_bf16 v[68:71], v[164:167], v[96:99], v[68:71]\n\t"
    "ds_read_b128 v[124:127], %[da] offset:14336\n\t"
    "v_mfma_f32_16x16x32_bf16 v[72:75], v[160:163], v[100:103], v[72:75]\n\t"
    "v_mfma_f32_16x16x32_bf16 v[76:79], v[164:167], v[100:103], v[76:79]\n\t"
    "s_waitcnt lgkmcnt(4)\n\t"
    "v_mfma_f32_16x16x32_bf16 v[64:67], v[168:171], v[104:107], v[64:67]\n\t"
    "ds_read_b128 v[96:99], %[ea] offset:16384\n\t"
    "v_mfma_f32_16x16x32_bf16 v[68:71], v[172:175], v[104:107], v[68:71]\n\t"
    "ds_read_b128 v[100:103], %[ea] offset:18432\n\t"
    "v_mfma_f32_16x16x32_bf16 v[72:75], v[168:171], v[108:111], v[72:75]\n\t"
    "v_mfma_f32_16x16x32_bf16 v[76:79], v[172:175], v[108:111], v[76:79]\n\t"
    "s_waitcnt lgkmcnt(4)\n\t"
    "v_mfma_f32_16x16x32_bf16 v[64:67], v[176:179], v[112:115], v[64:67]\n\t"
    "ds_read_b128 v[104:107], %[da] offset:16384\n\t"
    "v_mfma_f32_16x16x32_bf16 v[68:71], v[180:183], v[112:115], v[68:71]\n\t"
    "ds_read_b128 v[108:111], %[da] offset:18432\n\t"
    "v_mfma_f32_16x16x32_bf16 v[72:75], v[176:179], v[116:119], v[72:75]\n\t"
    "v_mfma_f32_16x16x32_bf16 v[76:79], v[180:183], v[116:119], v[76:79]\n\t"
    "s_waitcnt lgkmcnt(4)\n\t"
    "v_mfma_f32_16x16x32_bf16 v[64:67], v[184:187], v[120:123], v[64:67]\n\t"
    "ds_read_b128 v[112:115], %[ea] offset:20480\n\t"
    "v_mfma_f32_16x16x32_bf16 v[68:71], v[188:191], v[120:123], v[68:71]\n\t"
    "ds_read_b128 v[116:119], %[ea] offset:22528\n\t"
    "v_mfma_f32_16x16x32_bf16 v[72:75], v[184:187], v[124:127], v[72:75]\n\t"
    "v_mfma_f32_16x16x32_bf16 v[76:79], v[188:191], v[124:127], v[76:79]\n\t"
    "s_waitcnt vmcnt(0)\n\t"
    "s_barrier\n\t"
    "s_waitcnt lgkmcnt(4)\n\t"
    "v_mfma_f32_16x16x32_bf16 v[64:67], v[192:195], v[96:99], v[64:67]\n\t"
    "ds_read_b128 v[120:123], %[da] offset:20480\n\t"
    "v_mfma_f32_16x16x32_bf16 v[68:71], v[196:199], v[96:99], v[68:71]\n\t"
    "ds_read_b128 v[124:127], %[da] offset:22528\n\t"
    "v_mfma_f32_16x16x32_bf16 v[72:75], v[192:195], v[100:103], v[72:75]\n\t"
    "ds_read_b128 v[48:51], %[rw] offset:0\n\t"
    "v_mfma_f32_16x16x32_bf16 v[76:79], v[196:199], v[100:103], v[76:79]\n\t"
    "ds_read_b128 v[52:55], %[rw] offset:1024\n\t"
    "s_cmp_eq_u32 %[w0], 0\n\t"
    "s_cbranch_scc1 5f\n\t"
    "6:\n\t"
    "s_waitcnt lgkmcnt(6)\n\t"
    "v_mfma_f32_16x16x32_bf16 v[64:67], v[200:203], v[104:107], v[64:67]\n\t"
    "ds_read_b128 v[96:99], %[ea] offset:24576\n\t"
    "v_mfma_f32_16x16x32_bf16 v[68:71], v[204:207], v[104:107], v[68:71]\n\t"
    "ds_read_b128 v[100:103], %[ea] offset:26624\n\t"
    "v_mfma_f32_16x16x32_bf16 v[72:75], v[200:203], v[108:111], v[72:75]\n\t"
    "ds_read_b128 v[56:59], %[rw] offset:2048\n\t"
    "v_mfma_f32_16x16x32_bf16 v[76:79], v[204:207], v[108:111], v[76:79]\n\t"
    "ds_read_b128 v[60:63], %[rw] offset:3072\n\t"
    "s_waitcnt lgkmcnt(0)\n\t"
    "v_mfma_f32_16x16x32_bf16 v[64:67], v[208:211], v[112:115], v[64:67]\n\t"
    "ds_read_b128 v[104:107], %[da] offset:24576\n\t"
    "v_add_f32 v48, v48, v80\n\t"
    "v_add_f32 v49, v49, v81\n\t"
    "v_mfma_f32_16x16x32_bf16 v[68:71], v[212:215], v[112:115], v[68:71]\n\t"
    "ds_read_b128 v[108:111], %[da] offset:26624\n\t"
    "v_add_f32 v50, v50, v82\n\t"
    "v_mfma_f32_16x16x32_bf16 v[72:75], v[208:211], v[116:119], v[72:75]\n\t"
    "ds_read_b32 v39, %[sa]\n\t"
    "v_add_f32 v51, v51, v83\n\t"
    "v_mfma_f32_16x16x32_bf16 v[76:79], v[212:215], v[116:119], v[76:79]\n\t"
    "v_add_f32 v52, v52, v84\n\t"
    "v_add_f32 v53, v53, v85\n\t"
    "v_mfma_f32_16x16x32_bf16 v[64:67], v[216:219], v[120:123], v[64:67]\n\t"
    "ds_read_b128 v[112:115], %[ea] offset:28672\n\t"
    "v_add_f32 v54, v54, v86\n\t"
    "v_mfma_f32_16x16x32_bf16 v[68:71], v[220:223], v[120:123], v[68:71]\n\t"
    "ds_read_b128 v[116:119], %[ea] offset:30720\n\t"
    "v_add_f32 v55, v55, v87\n\t"
    "v_mfma_f32_16x16x32_bf16 v[72:75], v[216:219], v[124:127], v[72:75]\n\t"
    "v_xor_b32 %[ea], 0x10000, %[ea]\n\t"
    "v_add_f32 v56, v56, v88\n\t"
    "v_add_f32 v57, v57, v89\n\t"
    "v_mfma_f32_16x16x32_bf16 v[76:79], v[220:223], v[124:127], v[76:79]\n\t"
    "v_add_f32 v58, v58, v90\n\t"
    "v_mfma_f32_16x16x32_bf16 v[64:67], v[224:227], v[96:99], v[64:67]\n\t"
    "ds_read_b128 v[120:123], %[da] offset:28672\n\t"
    "v_add_f32 v59, v59, v91\n\t"
    "v_mfma_f32_16x16x32_bf16 v[68:71], v[228:231], v[96:99], v[68:71]\n\t"
    "ds_read_b128 v[124:127], %[da] offset:30720\n\t"
    "v_add_f32 v60, v60, v92\n\t"
    "v_mfma_f32_16x16x32_bf16 v[72:75], v[224:227], v[100:103], v[72:75]\n\t"
    "v_xor_b32 %[da], 0x10000, %[da]\n\t"
    "v_add_f32 v61, v61, v93\n\t"
    "v_add_f32 v62, v62, v94\n\t"
    "v_mfma_f32_16x16x32_bf16 v[76:79], v[228:231], v[100:103], v[76:79]\n\t"
    "v_add_f32 v63, v63, v95\n\t"
    "s_waitcnt lgkmcnt(5)\n\t"
    "v_mfma_f32_16x16x32_bf16 v[64:67], v[232:235], v[104:107], v[64:67]\n\t"
    "ds_read_b128 v[96:99], %[ea] offset:0\n\t"
    "v_cvt_pk_bf16_f32 v40, v48, v49\n\t"
    "v_mfma_f32_16x16x32_bf16 v[68:71], v[236:239], v[104:107], v[68:71]\n\t"
    "ds_read_b128 v[100:103], %[ea] offset:2048\n\t"
    "v_cvt_pk_bf16_f32 v41, v50, v51\n\t"
    "v_cvt_pk_bf16_f32 v42, v52, v53\n\t"
    "v_mfma_f32_16x16x32_bf16 v[72:75], v[232:235], v[108:111], v[72:75]\n\t"
    "v_cvt_pk_bf16_f32 v43, v54, v55\n\t"
    "v_mfma_f32_16x16x32_bf16 v[76:79], v[236:239], v[108:111], v[76:79]\n\t"
    "global_store_dwordx4 %[co0], v[40:43], s[36:37] sc1\n\t"
    "s_waitcnt lgkmcnt(4)\n\t"
    "v_mfma_f32_16x16x32_bf16 v[64:67], v[240:243], v[112:115], v[64:67]\n\t"
    "ds_read_b128 v[104:107], %[da] offset:0\n\t"
    "v_mad_u32_u24 v39, v39, %[mu], %[ad]\n\t"
    "v_cvt_pk_bf16_f32 v44, v56, v57\n\t"
    "v_cvt_pk_bf16_f32 v45, v58, v59\n\t"
    "v_mfma_f32_16x16x32_bf16 v[68:71], v[244:247], v[112:115], v[68:71]\n\t"
    "ds_read_b128 v[108:111], %[da] offset:2048\n\t"
    "v_cmp_ge_i32 vcc, %[lf], v39\n\t"
    "s_cmp_eq_u64 vcc, 0\n\t"
    "s_addc_u32 %[lf], %[lf], 0\n\t"
    "v_cvt_pk_bf16_f32 v46, v60, v61\n\t"
    "v_mfma_f32_16x16x32_bf16 v[72:75], v[240:243], v[116:119], v[72:75]\n\t"
    "v_cvt_pk_bf16_f32 v47, v62, v63\n\t"
    "v_mfma_f32_16x16x32_bf16 v[76:79], v[244:247], v[116:119], v[76:79]\n\t"
    "v_cmp_ge_i32 vcc, %[lf], v39\n\t"
    "s_cmp_eq_u64 vcc, 0\n\t"
    "s_addc_u32 %[lf], %[lf], 0\n\t"
    "global_store_dwordx4 %[co1], v[44:47], s[36:37] sc1\n\t"
    "s_waitcnt lgkmcnt(4)\n\t"
    "v_mfma_f32_16x16x32_bf16 v[80:83], v[248:251], v[120:123], v[64:67]\n\t"
    "ds_read_b128 v[112:115], %[ea] offset:4096\n\t"
    "s_min_i32 %[lf], %[lf], %[cp]\n\t"
    "s_add_u32 %[b], %[b], 1\n\t"
    "v_mfma_f32_16x16x32_bf16 v[84:87], v[252:255], v[120:123], v[68:71]\n\t"
    "ds_read_b128 v[116:119], %[ea] offset:6144\n\t"
    "v_mfma_f32_16x16x32_bf16 v[88:91], v[248:251], v[124:127], v[72:75]\n\t"
    "v_mfma_f32_16x16x32_bf16 v[92:95], v[252:255], v[124:127], v[76:79]\n\t"
    "s_mov_b64 s[36:37], s[38:39]\n\t"
    "s_add_u32 s38, s38, %[ab]\n\t"
    "s_addc_u32 s39, s39, 0\n\t"
    "s_cmp_lt_i32 %[b], %[lf]\n\t"
    "s_cbranch_scc1 1b\n\t"
    "s_waitcnt lgkmcnt(0)\n\t"
    "s_nop 4\n\t"
    "s_branch 9f\n\t"
    "5:\n\t"
    "s_add_u32 %[st], %[b], %[po]\n\t"
    "s_max_i32 %[st], %[st], %[p0]\n\t"
    "v_mov_b32 v39, %[st]\n\t"
    "v_mov_b32 v38, 0\n\t"
    "s_mov_b64 exec, 1\n\t"
    "global_store_dword v38, v39, %[pg] sc1\n\t"
    "s_mov_b64 exec, -1\n\t"
    "s_mov_b32 %[p0], %[p1]\n\t"
    "s_branch 6b\n\t"
    "7:\n\t"
    "s_cmp_ge_i32 %[lf], %[cp]\n\t"
    "s_cbranch_scc1 8b\n\t"
    "s_mov_b32 m0, %[sl]\n\t"
    "s_nop 0\n\t"
    "global_load_lds_dword %[dv], %[db] sc1\n\t"
    "s_branch 8b\n\t"
    "9:"
    : "=&{v[80:95]}"(pd), [ea] "+&v"(ea), [da] "+&v"(da), [b] "+&s"(b), [lf] "+&s"(lf), "+{s[36:37]}"(p1), "+{s[38:39]}"(p0), [sa] "+&v"(sa), [st] "=&s"(st), [sl] "+&s"(sl), [p0] "+&s"(pf)
    : [rw] "v"(rw), [ab] "s"(ab), [cp] "s"(cp), [mu] "s"(mu), [ad] "v"(ad), "{v[128:159]}"(W[0]), "{v[160:191]}"(W[1]), "{v[192:223]}"(W[2]), "{v[224:255]}"(W[3]), [co0] "v"(co0), [w0] "s"(w0), [dv] "v"(dv), [db] "s"(db), [pg] "s"(pg), [po] "s"(po - 1), [p1] "s"(b + po), [co1] "v"(co1)
    : "v38", "v39", "v40", "v41", "v42", "v43", "v44", "v45", "v46", "v47", "v48", "v49", "v50", "v51", "v52", "v53", "v54", "v55", "v56", "v57", "v58", "v59", "v60", "v61", "v62", "v63", "v64", "v65", "v66", "v67", "v68", "v69", "v70", "v71", "v72", "v73", "v74", "v75", "v76", "v77", "v78", "v79", "v96", "v97", "v98", "v99", "v100", "v101", "v102", "v103", "v104", "v105", "v106", "v107", "v108", "v109", "v110", "v111", "v112", "v113", "v114", "v115", "v116", "v117", "v118", "v119", "v120", "v121", "v122", "v123", "v124", "v125", "v126", "v127", "vcc", "scc", "m0", "memory");
}
__device__ __forceinline__ void fast_st1(const u32x32 (&W)[4], u32x16& pd, unsigned ea, unsigned da, int& b, int& lf, int cp, unsigned sa, unsigned mu, unsigned ad,
    unsigned rw, unsigned ab, const char* p1, const char* p0, unsigned co0, unsigned co1, int po, int pf, unsigned w0, unsigned sl, unsigned dv, const char* db, const char* pg) {
  unsigned st;
  asm volatile("s_barrier\n\t"
    "ds_read_b128 v[96:99], %[ea] offset:0\n\t"
    "ds_read_b128 v[100:103], %[ea] offset:2048\n\t"
    "ds_read_b128 v[104:107], %[da] offset:0\n\t"
    "ds_read_b128 v[108:111], %[da] offset:2048\n\t"
    "ds_read_b128 v[112:115], %[ea] offset:4096\n\t"
    "ds_read_b128 v[116:119], %[ea] offset:6144\n\t"
    "s_branch 2f\n\t"
    "1:\n\t"
    "s_barrier\n\t"
    "2:\n\t"
    "s_waitcnt lgkmcnt(4)\n\t"
    "v_mfma_f32_16x16x32_bf16 v[64:67], v[128:131], v[96:99], 0\n\t"
    "ds_read_b128 v[120:123], %[da] offset:4096\n\t"
    "v_mfma_f32_16x16x32_bf16 v[68:71], v[132:135], v[96:99], 0\n\t"
    "ds_read_b128 v[124:127], %[da] offset:6144\n\t"
    "v_mfma_f32_16x16x32_bf16 v[72:75], v[128:131], v[100:103], 0\n\t"
    "v_mfma_f32_16x16x32_bf16 v[76:79], v[132:135], v[100:103], 0\n\t"
    "s_waitcnt lgkmcnt(4)\n\t"
    "v_mfma_f32_16x16x32_bf16 v[64:67], v[136:139], v[104:107], v[64:67]\n\t"
    "ds_read_b128 v[96:99], %[ea] offset:8192\n\t"
    "v_mfma_f32_16x16x32_bf16 v[68:71], v[140:143], v[104:107], v[68:71]\n\t"
    "ds_read_b128 v[100:103], %[ea] offset:10240\n\t"
    "v_mfma_f32_16x16x32_bf16 v[72:75], v[136:139], v[108:111], v[72:75]\n\t"
    "v_mfma_f32_16x16x32_bf16 v[76:79], v[140:143], v[108:111], v[76:79]\n\t"
    "s_cmp_eq_u32 %[w0], 1\n\t"
    "s_cbranch_scc1 7f\n\t"
    "8:\n\t"
    "s_waitcnt lgkmcnt(4)\n\t"
    "v_mfma_f32_16x16x32_bf16 v[64:67], v[144:147], v[112:115], v[64:67]\n\t"
    "ds_read_b128 v[104:107], %[da] offset:8192\n\t"
    "v_mfma_f32_16x16x32_bf16 v[68:71], v[148:151], v[112:115], v[68:71]\n\t"
    "ds_read_b128 v[108:111], %[da] offset:10240\n\t"
    "v_mfma_f32_16x16x32_bf16 v[72:75], v[144:147], v[116:119], v[72:75]\n\t"
    "v_mfma_f32_16x16x32_bf16 v[76:79], v[148:151], v[116:119], v[76:79]\n\t"
    "s_waitcnt lgkmcnt(4)\n\t"
    "v_mfma_f32_16x16x32_bf16 v[64:67], v[152:155], v[120:123], v[64:67]\n\t"
    "ds_read_b128 v[112:115], %[ea] offset:12288\n\t"
    "v_mfma_f32_16x16x32_bf16 v[68:71], v[156:159], v[120:123], v[68:71]\n\t"
    "ds_read_b128 v[116:119], %[ea] offset:14336\n\t"
    "v_mfma_f32_16x16x32_bf16 v[72:75], v[152:155], v[124:127], v[72:75]\n\t"
    "v_mfma_f32_16x16x32_bf16 v[76:79], v[156:159], v[124:127], v[76:79]\n\t"
    "s_waitcnt lgkmcnt(4)\n\t"
    "v_mfma_f32_16x16x32_bf16 v[64:67], v[160:163], v[96:99], v[64:67]\n\t"
    "ds_read_b128 v[120:123], %[da] offset:12288\n\t"
    "v_mfma_f32_16x16x32_bf16 v[68:71], v[164:167], v[96:99], v[68:71]\n\t"
    "ds_read_b128 v[124:127], %[da] offset:14336\n\t"
    "v_mfma_f32_16x16x32_bf16 v[72:75], v[160:163], v[100:103], v[72:75]\n\t"
    "v_mfma_f32_16x16x32_bf16 v[76:79], v[164:167], v[100:103], v[76:79]\n\t"
    "s_waitcnt lgkmcnt(4)\n\t"
    "v_mfma_f32_16x16x32_bf16 v[64:67], v[168:171], v[104:107], v[64:67]\n\t"
    "ds_read_b128 v[96:99], %[ea] offset:16384\n\t"
    "v_mfma_f32_16x16x32_bf16 v[68:71], v[172:175], v[104:107], v[68:71]\n\t"
    "ds_read_b128 v[100:103], %[ea] offset:18432\n\t"
    "v_mfma_f32_16x16x32_bf16 v[72:75], v[168:171], v[108:111], v[72:75]\n\t"
    "v_mfma_f32_16x16x32_bf16 v[76:79], v[172:175], v[108:111], v[76:79]\n\t"
    "s_waitcnt lgkmcnt(4)\n\t"
    "v_mfma_f32_16x16x32_bf16 v[64:67], v[176:179], v[112:115], v[64:67]\n\t"
    "ds_read_b128 v[104:107], %[da] offset:16384\n\t"
    "v_mfma_f32_16x16x32_bf16 v[68:71], v[180:183], v[112:115], v[68:71]\n\t"
    "ds_read_b128 v[108:111], %[da] offset:18432\n\t"
    "v_mfma_f32_16x16x32_bf16 v[72:75], v[176:179], v[116:119], v[72:75]\n\t"
    "v_mfma_f32_16x16x32_bf16 v[76:79], v[180:183], v[116:119], v[76:79]\n\t"
    "s_waitcnt lgkmcnt(4)\n\t"
    "v_mfma_f32_16x16x32_bf16 v[64:67], v[184:187], v[120:123], v[64:67]\n\t"
    "ds_read_b128 v[112:115], %[ea] offset:20480\n\t"
    "v_mfma_f32_16x16x32_bf16 v[68:71], v[188:191], v[120:123], v[68:71]\n\t"
    "ds_read_b128 v[116:119], %[ea] offset:22528\n\t"
    "v_mfma_f32_16x16x32_bf16 v[72:75], v[184:187], v[124:127], v[72:75]\n\t"
    "v_mfma_f32_16x16x32_bf16 v[76:79], v[188:191], v[124:127], v[76:79]\n\t"
    "s_waitcnt vmcnt(0)\n\t"
    "s_barrier\n\t"
    "s_waitcnt lgkmcnt(4)\n\t"
    "v_mfma_f32_16x16x32_bf16 v[64:67], v[192:195], v[96:99], v[64:67]\n\t"
    "ds_read_b128 v[120:123], %[da] offset:20480\n\t"
    "v_mfma_f32_16x16x32_bf16 v[68:71], v[196:199], v[96:99], v[68:71]\n\t"
    "ds_read_b128 v[124:127], %[da] offset:22528\n\t"
    "v_mfma_f32_16x16x32_bf16 v[72:75], v[192:195], v[100:103], v[72:75]\n\t"
    "ds_read_b128 v[48:51], %[rw] offset:0\n\t"
    "v_mfma_f32_16x16x32_bf16 v[76:79], v[196:199], v[100:103], v[76:79]\n\t"
    "ds_read_b128 v[52:55], %[rw] offset:1024\n\t"
    "s_cmp_eq_u32 %[w0], 0\n\t"
    "s_cbranch_scc1 5f\n\t"
    "6:\n\t"
    "s_waitcnt lgkmcnt(6)\n\t"
    "v_mfma_f32_16x16x32_bf16 v[64:67], v[200:203], v[104:107], v[64:67]\n\t"
    "ds_read_b128 v[96:99], %[ea] offset:24576\n\t"
    "v_mfma_f32_16x16x32_bf16 v[68:71], v[204:207], v[104:107], v[68:71]\n\t"
    "ds_read_b128 v[100:103], %[ea] offset:26624\n\t"
    "v_mfma_f32_16x16x32_bf16 v[72:75], v[200:203], v[108:111], v[72:75]\n\t"
    "ds_read_b128 v[56:59], %[rw] offset:2048\n\t"
    "v_mfma_f32_16x16x32_bf16 v[76:79], v[204:207], v[108:111], v[76:79]\n\t"
    "ds_read_b128 v[60:63], %[rw] offset:3072\n\t"
    "s_waitcnt lgkmcnt(0)\n\t"
    "v_mfma_f32_16x16x32_bf16 v[64:67], v[208:211], v[112:115], v[64:67]\n\t"
    "ds_read_b128 v[104:107], %[da] offset:24576\n\t"
    "v_add_f32 v48, v48, v80\n\t"
    "v_add_f32 v49, v49, v81\n\t"
    "v_add_f32 v50, v50, v82\n\t"
    "v_mfma_f32_16x16x32_bf16 v[68:71], v[212:215], v[112:115], v[68:71]\n\t"
    "ds_read_b128 v[108:111], %[da] offset:26624\n\t"
    "v_add_f32 v51, v51, v83\n\t"
    "v_add_f32 v52, v52, v84\n\t"
    "v_add_f32 v53, v53, v85\n\t"
    "v_mfma_f32_16x16x32_bf16 v[72:75], v[208:211], v[116:119], v[72:75]\n\t"
    "ds_read_b32 v39, %[sa]\n\t"
    "v_add_f32 v54, v54, v86\n\t"
    "v_add_f32 v55, v55, v87\n\t"
    "v_add_f32 v56, v56, v88\n\t"
    "v_mfma_f32_16x16x32_bf16 v[76:79], v[212:215], v[116:119], v[76:79]\n\t"
    "v_add_f32 v57, v57, v89\n\t"
    "v_add_f32 v58, v58, v90\n\t"
    "v_add_f32 v59, v59, v91\n\t"
    "v_mfma_f32_16x16x32_bf16 v[64:67], v[216:219], v[120:123], v[64:67]\n\t"
    "ds_read_b128 v[112:115], %[ea] offset:28672\n\t"
    "v_add_f32 v60, v60, v92\n\t"
    "v_add_f32 v61, v61, v93\n\t"
    "v_add_f32 v62, v62, v94\n\t"
    "v_mfma_f32_16x16x32_bf16 v[68:71], v[220:223], v[120:123], v[68:71]\n\t"
    "ds_read_b128 v[116:119], %[ea] offset:30720\n\t"
    "v_add_f32 v63, v63, v95\n\t"
    "v_mul_f32 v40, %[nl], v48\n\t"
    "v_mul_f32 v41, %[nl], v49\n\t"
    "v_mfma_f32_16x16x32_bf16 v[72:75], v[216:219], v[124:127], v[72:75]\n\t"
    "v_xor_b32 %[ea], 0x10000, %[ea]\n\t"
    "v_mul_f32 v42, %[nl], v50\n\t"
    "v_mul_f32 v43, %[nl], v51\n\t"
    "v_mul_f32 v44, %[nl], v56\n\t"
    "v_mfma_f32_16x16x32_bf16 v[76:79], v[220:223], v[124:127], v[76:79]\n\t"
    "v_mul_f32 v45, %[nl], v57\n\t"
    "v_mul_f32 v46, %[nl], v58\n\t"
    "v_mul_f32 v47, %[nl], v59\n\t"
    "v_mfma_f32_16x16x32_bf16 v[64:67], v[224:227], v[96:99], v[64:67]\n\t"
    "ds_read_b128 v[120:123], %[da] offset:28672\n\t"
    "v_exp_f32 v40, v40\n\t"
    "v_exp_f32 v41, v41\n\t"
    "v_exp_f32 v42, v42\n\t"
    "v_mfma_f32_16x16x32_bf16 v[68:71], v[228:231], v[96:99], v[68:71]\n\t"
    "ds_read_b128 v[124:127], %[da] offset:30720\n\t"
    "v_exp_f32 v43, v43\n\t"
    "v_exp_f32 v44, v44\n\t"
    "v_exp_f32 v45, v45\n\t"
    "v_mfma_f32_16x16x32_bf16 v[72:75], v[224:227], v[100:103], v[72:75]\n\t"
    "v_xor_b32 %[da], 0x10000, %[da]\n\t"
    "v_exp_f32 v46, v46\n\t"
    "v_exp_f32 v47, v47\n\t"
    "v_add_f32 v40, 1.0, v40\n\t"
    "v_mfma_f32_16x16x32_bf16 v[76:79], v[228:231], v[100:103], v[76:79]\n\t"
    "v_add_f32 v41, 1.0, v41\n\t"
    "v_add_f32 v42, 1.0, v42\n\t"
    "v_add_f32 v43, 1.0, v43\n\t"
    "s_waitcnt lgkmcnt(5)\n\t"
    "v_mfma_f32_16x16x32_bf16 v[64:67], v[232:235], v[104:107], v[64:67]\n\t"
    "ds_read_b128 v[96:99], %[ea] offset:0\n\t"
    "v_add_f32 v44, 1.0, v44\n\t"
    "v_add_f32 v45, 1.0, v45\n\t"
    "v_add_f32 v46, 1.0, v46\n\t"
    "v_mfma_f32_16x16x32_bf16 v[68:71], v[236:239], v[104:107], v[68:71]\n\t"
    "ds_read_b128 v[100:103], %[ea] offset:2048\n\t"
    "v_add_f32 v47, 1.0, v47\n\t"
    "v_rcp_f32 v40, v40\n\t"
    "v_rcp_f32 v41, v41\n\t"
    "v_mfma_f32_16x16x32_bf16 v[72:75], v[232:235], v[108:111], v[72:75]\n\t"
    "v_rcp_f32 v42, v42\n\t"
    "v_rcp_f32 v43, v43\n\t"
    "v_rcp_f32 v44, v44\n\t"
    "v_mfma_f32_16x16x32_bf16 v[76:79], v[236:239], v[108:111], v[76:79]\n\t"
    "v_rcp_f32 v45, v45\n\t"
    "v_rcp_f32 v46, v46\n\t"
    "v_rcp_f32 v47, v47\n\t"
    "s_waitcnt lgkmcnt(4)\n\t"
    "v_mfma_f32_16x16x32_bf16 v[64:67], v[240:243], v[112:115], v[64:67]\n\t"
    "ds_read_b128 v[104:107], %[da] offset:0\n\t"
    "v_mad_u32_u24 v39, v39, %[mu], %[ad]\n\t"
    "v_mul_f32 v40, v48, v40\n\t"
    "v_mul_f32 v41, v49, v41\n\t"
    "v_mul_f32 v42, v50, v42\n\t"
    "v_mfma_f32_16x16x32_bf16 v[68:71], v[244:247], v[112:115], v[68:71]\n\t"
    "ds_read_b128 v[108:111], %[da] offset:2048\n\t"
    "v_cmp_ge_i32 vcc, %[lf], v39\n\t"
    "s_cmp_eq_u64 vcc, 0\n\t"
    "s_addc_u32 %[lf], %[lf], 0\n\t"
    "v_mul_f32 v43, v51, v43\n\t"
    "v_mul_f32 v44, v56, v44\n\t"
    "v_mul_f32 v45, v57, v45\n\t"
    "v_mfma_f32_16x16x32_bf16 v[72:75], v[240:243], v[116:119], v[72:75]\n\t"
    "v_mul_f32 v46, v58, v46\n\t"
    "v_mul_f32 v47, v59, v47\n\t"
    "v_mul_f32 v40, v40, v52\n\t"
    "v_mfma_f32_16x16x32_bf16 v[76:79], v[244:247], v[116:119], v[76:79]\n\t"
    "v_cmp_ge_i32 vcc, %[lf], v39\n\t"
    "s_cmp_eq_u64 vcc, 0\n\t"
    "s_addc_u32 %[lf], %[lf], 0\n\t"
    "v_mul_f32 v41, v41, v53\n\t"
    "v_mul_f32 v42, v42, v54\n\t"
    "v_mul_f32 v43, v43, v55\n\t"
    "s_waitcnt lgkmcnt(4)\n\t"
    "v_mfma_f32_16x16x32_bf16 v[80:83], v[248:251], v[120:123], v[64:67]\n\t"
    "ds_read_b128 v[112:115], %[ea] offset:4096\n\t"
    "s_min_i32 %[lf], %[lf], %[cp]\n\t"
    "s_add_u32 %[b], %[b], 1\n\t"
    "v_mul_f32 v44, v44, v60\n\t"
    "v_mul_f32 v45, v45, v61\n\t"
    "v_mul_f32 v46, v46, v62\n\t"
    "v_mfma_f32_16x16x32_bf16 v[84:87], v[252:255], v[120:123], v[68:71]\n\t"
    "ds_read_b128 v[116:119], %[ea] offset:6144\n\t"
    "v_mul_f32 v47, v47, v63\n\t"
    "v_cvt_pk_bf16_f32 v48, v40, v41\n\t"
    "v_cvt_pk_bf16_f32 v49, v42, v43\n\t"
    "v_mfma_f32_16x16x32_bf16 v[88:91], v[248:251], v[124:127], v[72:75]\n\t"
    "v_cvt_pk_bf16_f32 v50, v44, v45\n\t"
    "v_cvt_pk_bf16_f32 v51, v46, v47\n\t"
    "s_nop 1\n\t"
    "v_mfma_f32_16x16x32_bf16 v[92:95], v[252:255], v[124:127], v[76:79]\n\t"
    "v_permlane16_swap_b32 v48, v50\n\t"
    "v_permlane16_swap_b32 v49, v51\n\t"
    "global_store_dwordx4 %[co0], v[48:51], s[36:37] sc1\n\t"
    "s_mov_b64 s[36:37], s[38:39]\n\t"
    "s_add_u32 s38, s38, %[ab]\n\t"
    "s_addc_u32 s39, s39, 0\n\t"
    "s_cmp_lt_i32 %[b], %[lf]\n\t"
    "s_cbranch_scc1 1b\n\t"
    "s_waitcnt lgkmcnt(0)\n\t"
    "s_nop 4\n\t"
    "s_branch 9f\n\t"
    "5:\n\t"
    "s_add_u32 %[st], %[b], %[po]\n\t"
    "s_max_i32 %[st], %[st], %[p0]\n\t"
    "v_mov_b32 v39, %[st]\n\t"
    "v_mov_b32 v38, 0\n\t"
    "s_mov_b64 exec, 1\n\t"
    "global_store_dword v38, v39, %[pg] sc1\n\t"
    "s_mov_b64 exec, -1\n\t"
    "s_mov_b32 %[p0], %[p1]\n\t"
    "s_branch 6b\n\t"
    "7:\n\t"
    "s_cmp_ge_i32 %[lf], %[cp]\n\t"
    "s_cbranch_scc1 8b\n\t"
    "s_mov_b32 m0, %[sl]\n\t"
    "s_nop 0\n\t"
    "global_load_lds_dword %[dv], %[db] sc1\n\t"
    "s_branch 8b\n\t"
    "9:"
    : "=&{v[80:95]}"(pd), [ea] "+&v"(ea), [da] "+&v"(da), [b] "+&s"(b), [lf] "+&s"(lf), "+{s[36:37]}"(p1), "+{s[38:39]}"(p0), [sa] "+&v"(sa), [st] "=&s"(st), [sl] "+&s"(sl), [p0] "+&s"(pf)
    : [rw] "v"(rw), [ab] "s"(ab), [cp] "s"(cp), [mu] "s"(mu), [ad] "v"(ad), "{v[128:159]}"(W[0]), "{v[160:191]}"(W[1]), "{v[192:223]}"(W[2]), "{v[224:255]}"(W[3]), [co0] "v"(co0), [w0] "s"(w0), [dv] "v"(dv), [db] "s"(db), [pg] "s"(pg), [po] "s"(po - 1), [p1] "s"(b + po), [nl] "s"(-LOG2E)
    : "v38", "v39", "v40", "v41", "v42", "v43", "v44", "v45", "v46", "v47", "v48", "v49", "v50", "v51", "v52", "v53", "v54", "v55", "v56", "v57", "v58", "v59", "v60", "v61", "v62", "v63", "v64", "v65", "v66", "v67", "v68", "v69", "v70", "v71", "v72", "v73", "v74", "v75", "v76", "v77", "v78", "v79", "v96", "v97", "v98", "v99", "v100", "v101", "v102", "v103", "v104", "v105", "v106", "v107", "v108", "v109", "v110", "v111", "v112", "v113", "v114", "v115", "v116", "v117", "v118", "v119", "v120", "v121", "v122", "v123", "v124", "v125", "v126", "v127", "vcc", "scc", "m0", "memory");
}
__device__ __forceinline__ void fast_st2(const u32x32 (&W)[4], u32x16& pd, unsigned ea, unsigned da, int& b, int& lf, int cp, unsigned sa, unsigned mu, unsigned ad,
    unsigned rw, unsigned ab, const char* p1, const char* p0, unsigned co0, unsigned co1, int po, int pf, unsigned w0, unsigned sl, unsigned dv, const char* db, const char* pg) {
  unsigned st;
  asm volatile("s_barrier\n\t"
    "ds_read_b128 v[96:99], %[ea] offset:0\n\t"
    "ds_read_b128 v[100:103], %[ea] offset:2048\n\t"
    "ds_read_b128 v[104:107], %[da] offset:0\n\t"
    "ds_read_b128 v[108:111], %[da] offset:2048\n\t"
    "ds_read_b128 v[112:115], %[ea] offset:4096\n\t"
    "ds_read_b128 v[116:119], %[ea] offset:6144\n\t"
    "s_branch 2f\n\t"
    "1:\n\t"
    "s_barrier\n\t"
    "2:\n\t"
    "s_waitcnt lgkmcnt(4)\n\t"
    "v_mfma_f32_16x16x32_bf16 v[64:67], v[128:131], v[96:99], 0\n\t"
    "ds_read_b128 v[120:123], %[da] offset:4096\n\t"
    "v_mfma_f32_16x16x32_bf16 v[68:71], v[132:135], v[96:99], 0\n\t"
    "ds_read_b128 v[124:127], %[da] offset:6144\n\t"
    "v_mfma_f32_16x16x32_bf16 v[72:75], v[128:131], v[100:103], 0\n\t"
    "v_mfma_f32_16x16x32_bf16 v[76:79], v[132:135], v[100:103], 0\n\t"
    "s_waitcnt lgkmcnt(4)\n\t"
    "v_mfma_f32_16x16x32_bf16 v[64:67], v[136:139], v[104:107], v[64:67]\n\t"
    "ds_read_b128 v[96:99], %[ea] offset:8192\n\t"
    "v_mfma_f32_16x16x32_bf16 v[68:71], v[140:143], v[104:107], v[68:71]\n\t"
    "ds_read_b128 v[100:103], %[ea] offset:10240\n\t"
    "v_mfma_f32_16x16x32_bf16 v[72:75], v[136:139], v[108:111], v[72:75]\n\t"
    "v_mfma_f32_16x16x32_bf16 v[76:79], v[140:143], v[108:111], v[76:79]\n\t"
    "s_cmp_eq_u32 %[w0], 1\n\t"
    "s_cbranch_scc1 7f\n\t"
    "8:\n\t"
    "s_waitcnt lgkmcnt(4)\n\t"
    "v_mfma_f32_16x16x32_bf16 v[64:67], v[144:147], v[112:115], v[64:67]\n\t"
    "ds_read_b128 v[104:107], %[da] offset:8192\n\t"
    "v_mfma_f32_16x16x32_bf16 v[68:71], v[148:151], v[112:115], v[68:71]\n\t"
    "ds_read_b128 v[108:111], %[da] offset:10240\n\t"
    "v_mfma_f32_16x16x32_bf16 v[72:75], v[144:147], v[116:119], v[72:75]\n\t"
    "v_mfma_f32_16x16x32_bf16 v[76:79], v[148:151], v[116:119], v[76:79]\n\t"
    "s_waitcnt lgkmcnt(4)\n\t"
    "v_mfma_f32_16x16x32_bf16 v[64:67], v[152:155], v[120:123], v[64:67]\n\t"
    "ds_read_b128 v[112:115], %[ea] offset:12288\n\t"
    "v_mfma_f32_16x16x32_bf16 v[68:71], v[156:159], v[120:123], v[68:71]\n\t"
    "ds_read_b128 v[116:119], %[ea] offset:14336\n\t"
    "v_mfma_f32_16x16x32_bf16 v[72:75], v[152:155], v[124:127], v[72:75]\n\t"
    "v_mfma_f32_16x16x32_bf16 v[76:79], v[156:159], v[124:127], v[76:79]\n\t"
    "s_waitcnt lgkmcnt(4)\n\t"
    "v_mfma_f32_16x16x32_bf16 v[64:67], v[160:163], v[96:99], v[64:67]\n\t"
    "ds_read_b128 v[120:123], %[da] offset:12288\n\t"
    "v_mfma_f32_16x16x32_bf16 v[68:71], v[164:167], v[96:99], v[68:71]\n\t"
    "ds_read_b128 v[124:127], %[da] offset:14336\n\t"
    "v_mfma_f32_16x16x32_bf16 v[72:75], v[160:163], v[100:103], v[72:75]\n\t"
    "v_mfma_f32_16x16x32_bf16 v[76:79], v[164:167], v[100:103], v[76:79]\n\t"
    "s_waitcnt lgkmcnt(4)\n\t"
    "v_mfma_f32_16x16x32_bf16 v[64:67], v[168:171], v[104:107], v[64:67]\n\t"
    "ds_read_b128 v[96:99], %[ea] offset:16384\n\t"
    "v_mfma_f32_16x16x32_bf16 v[68:71], v[172:175], v[104:107], v[68:71]\n\t"
    "ds_read_b128 v[100:103], %[ea] offset:18432\n\t"
    "v_mfma_f32_16x16x32_bf16 v[72:75], v[168:171], v[108:111], v[72:75]\n\t"
    "v_mfma_f32_16x16x32_bf16 v[76:79], v[172:175], v[108:111], v[76:79]\n\t"
    "s_waitcnt lgkmcnt(4)\n\t"
    "v_mfma_f32_16x16x32_bf16 v[64:67], v[176:179], v[112:115], v[64:67]\n\t"
    "ds_read_b128 v[104:107], %[da] offset:16384\n\t"
    "v_mfma_f32_16x16x32_bf16 v[68:71], v[180:183], v[112:115], v[68:71]\n\t"
    "ds_read_b128 v[108:111], %[da] offset:18432\n\t"
    "v_mfma_f32_16x16x32_bf16 v[72:75], v[176:179], v[116:119], v[72:75]\n\t"
    "v_mfma_f32_16x16x32_bf16 v[76:79], v[180:183], v[116:119], v[76:79]\n\t"
    "s_waitcnt lgkmcnt(4)\n\t"
    "v_mfma_f32_16x16x32_bf16 v[64:67], v[184:187], v[120:123], v[64:67]\n\t"
    "ds_read_b128 v[112:115], %[ea] offset:20480\n\t"
    "v_mfma_f32_16x16x32_bf16 v[68:71], v[188:191], v[120:123], v[68:71]\n\t"
    "ds_read_b128 v[116:119], %[ea] offset:22528\n\t"
    "v_mfma_f32_16x16x32_bf16 v[72:75], v[184:187], v[124:127], v[72:75]\n\t"
    "v_mfma_f32_16x16x32_bf16 v[76:79], v[188:191], v[124:127], v[76:79]\n\t"
    "s_waitcnt vmcnt(0)\n\t"
    "s_barrier\n\t"
    "s_waitcnt lgkmcnt(4)\n\t"
    "v_mfma_f32_16x16x32_bf16 v[64:67], v[192:195], v[96:99], v[64:67]\n\t"
    "ds_read_b128 v[120:123], %[da] offset:20480\n\t"
    "v_mfma_f32_16x16x32_bf16 v[68:71], v[196:199], v[96:99], v[68:71]\n\t"
    "ds_read_b128 v[124:127], %[da] offset:22528\n\t"
    "v_mfma_f32_16x16x32_bf16 v[72:75], v[192:195], v[100:103], v[72:75]\n\t"
    "ds_read_b128 v[48:51], %[rw] offset:0\n\t"
    "v_mfma_f32_16x16x32_bf16 v[76:79], v[196:199], v[100:103], v[76:79]\n\t"
    "ds_read_b128 v[52:55], %[rw] offset:1024\n\t"
    "s_cmp_eq_u32 %[w0], 0\n\t"
    "s_cbranch_scc1 5f\n\t"
    "6:\n\t"
    "s_waitcnt lgkmcnt(6)\n\t"
    "v_mfma_f32_16x16x32_bf16 v[64:67], v[200:203], v[104:107], v[64:67]\n\t"
    "ds_read_b128 v[96:99], %[ea] offset:24576\n\t"
    "v_mfma_f32_16x16x32_bf16 v[68:71], v[204:207], v[104:107], v[68:71]\n\t"
    "ds_read_b128 v[100:103], %[ea] offset:26624\n\t"
    "v_mfma_f32_16x16x32_bf16 v[72:75], v[200:203], v[108:111], v[72:75]\n\t"
    "ds_read_b128 v[56:59], %[rw] offset:2048\n\t"
    "v_mfma_f32_16x16x32_bf16 v[76:79], v[204:207], v[108:111], v[76:79]\n\t"
    "ds_read_b128 v[60:63], %[rw] offset:3072\n\t"
    "s_waitcnt lgkmcnt(0)\n\t"
    "v_mfma_f32_16x16x32_bf16 v[64:67], v[208:211], v[112:115], v[64:67]\n\t"
    "ds_read_b128 v[104:107], %[da] offset:24576\n\t"
    "v_cvt_pk_bf16_f32 v40, v80, v81\n\t"
    "v_mfma_f32_16x16x32_bf16 v[68:71], v[212:215], v[112:115], v[68:71]\n\t"
    "ds_read_b128 v[108:111], %[da] offset:26624\n\t"
    "v_cvt_pk_bf16_f32 v41, v82, v83\n\t"
    "v_mfma_f32_16x16x32_bf16 v[72:75], v[208:211], v[116:119], v[72:75]\n\t"
    "ds_read_b32 v39, %[sa]\n\t"
    "v_cvt_pk_bf16_f32 v42, v84, v85\n\t"
    "v_mfma_f32_16x16x32_bf16 v[76:79], v[212:215], v[116:119], v[76:79]\n\t"
    "v_cvt_pk_bf16_f32 v43, v86, v87\n\t"
    "v_mfma_f32_16x16x32_bf16 v[64:67], v[216:219], v[120:123], v[64:67]\n\t"
    "ds_read_b128 v[112:115], %[ea] offset:28672\n\t"
    "global_store_dwordx4 %[co0], v[40:43], s[36:37] sc1\n\t"
    "v_mfma_f32_16x16x32_bf16 v[68:71], v[220:223], v[120:123], v[68:71]\n\t"
    "ds_read_b128 v[116:119], %[ea] offset:30720\n\t"
    "v_cvt_pk_bf16_f32 v44, v88, v89\n\t"
    "v_mfma_f32_16x16x32_bf16 v[72:75], v[216:219], v[124:127], v[72:75]\n\t"
    "v_xor_b32 %[ea], 0x10000, %[ea]\n\t"
    "v_cvt_pk_bf16_f32 v45, v90, v91\n\t"
    "v_mfma_f32_16x16x32_bf16 v[76:79], v[220:223], v[124:127], v[76:79]\n\t"
    "v_cvt_pk_bf16_f32 v46, v92, v93\n\t"
    "v_mfma_f32_16x16x32_bf16 v[64:67], v[224:227], v[96:99], v[64:67]\n\t"
    "ds_read_b128 v[120:123], %[da] offset:28672\n\t"
    "v_cvt_pk_bf16_f32 v47, v94, v95\n\t"
    "v_mfma_f32_16x16x32_bf16 v[68:71], v[228:231], v[96:99], v[68:71]\n\t"
    "ds_read_b128 v[124:127], %[da] offset:30720\n\t"
    "global_store_dwordx4 %[co1], v[44:47], s[36:37] sc1\n\t"
    "v_mfma_f32_16x16x32_bf16 v[72:75], v[224:227], v[100:103], v[72:75]\n\t"
    "v_xor_b32 %[da], 0x10000, %[da]\n\t"
    "v_cvt_pk_bf16_f32 v48, v48, v49\n\t"
    "v_mfma_f32_16x16x32_bf16 v[76:79], v[228:231], v[100:103], v[76:79]\n\t"
    "v_cvt_pk_bf16_f32 v49, v50, v51\n\t"
    "s_waitcnt lgkmcnt(5)\n\t"
    "v_mfma_f32_16x16x32_bf16 v[64:67], v[232:235], v[104:107], v[64:67]\n\t"
    "ds_read_b128 v[96:99], %[ea] offset:0\n\t"
    "v_cvt_pk_bf16_f32 v50, v52, v53\n\t"
    "v_mfma_f32_16x16x32_bf16 v[68:71], v[236:239], v[104:107], v[68:71]\n\t"
    "ds_read_b128 v[100:103], %[ea] offset:2048\n\t"
    "v_cvt_pk_bf16_f32 v51, v54, v55\n\t"
    "v_mfma_f32_16x16x32_bf16 v[72:75], v[232:235], v[108:111], v[72:75]\n\t"
    "global_store_dwordx4 %[co0], v[48:51], s[36:37] offset:2048 sc1\n\t"
    "v_mfma_f32_16x16x32_bf16 v[76:79], v[236:239], v[108:111], v[76:79]\n\t"
    "v_cvt_pk_bf16_f32 v56, v56, v57\n\t"
    "s_waitcnt lgkmcnt(4)\n\t"
    "v_mfma_f32_16x16x32_bf16 v[64:67], v[240:243], v[112:115], v[64:67]\n\t"
    "ds_read_b128 v[104:107], %[da] offset:0\n\t"
    "v_mad_u32_u24 v39, v39, %[mu], %[ad]\n\t"
    "v_cvt_pk_bf16_f32 v57, v58, v59\n\t"
    "v_mfma_f32_16x16x32_bf16 v[68:71], v[244:247], v[112:115], v[68:71]\n\t"
    "ds_read_b128 v[108:111], %[da] offset:2048\n\t"
    "v_cmp_ge_i32 vcc, %[lf], v39\n\t"
    "s_cmp_eq_u64 vcc, 0\n\t"
    "s_addc_u32 %[lf], %[lf], 0\n\t"
    "v_cvt_pk_bf16_f32 v58, v60, v61\n\t"
    "v_mfma_f32_16x16x32_bf16 v[72:75], v[240:243], v[116:119], v[72:75]\n\t"
    "v_cvt_pk_bf16_f32 v59, v62, v63\n\t"
    "v_mfma_f32_16x16x32_bf16 v[76:79], v[244:247], v[116:119], v[76:79]\n\t"
    "v_cmp_ge_i32 vcc, %[lf], v39\n\t"
    "s_cmp_eq_u64 vcc, 0\n\t"
    "s_addc_u32 %[lf], %[lf], 0\n\t"
    "global_store_dwordx4 %[co1], v[56:59], s[36:37] offset:2048 sc1\n\t"
    "s_waitcnt lgkmcnt(4)\n\t"
    "v_mfma_f32_16x16x32_bf16 v[80:83], v[248:251], v[120:123], v[64:67]\n\t"
    "ds_read_b128 v[112:115], %[ea] offset:4096\n\t"
    "s_min_i32 %[lf], %[lf], %[cp]\n\t"
    "s_add_u32 %[b], %[b], 1\n\t"
    "v_mfma_f32_16x16x32_bf16 v[84:87], v[252:255], v[120:123], v[68:71]\n\t"
    "ds_read_b128 v[116:119], %[ea] offset:6144\n\t"
    "v_mfma_f32_16x16x32_bf16 v[88:91], v[248:251], v[124:127], v[72:75]\n\t"
    "v_mfma_f32_16x16x32_bf16 v[92:95], v[252:255], v[124:127], v[76:79]\n\t"
    "s_mov_b64 s[36:37], s[38:39]\n\t"
    "s_add_u32 s38, s38, %[ab]\n\t"
    "s_addc_u32 s39, s39, 0\n\t"
    "s_cmp_lt_i32 %[b], %[lf]\n\t"
    "s_cbranch_scc1 1b\n\t"
    "s_waitcnt lgkmcnt(0)\n\t"
    "s_nop 4\n\t"
    "s_branch 9f\n\t"
    "5:\n\t"
    "s_add_u32 %[st], %[b], %[po]\n\t"
    "s_max_i32 %[st], %[st], %[p0]\n\t"
    "v_mov_b32 v39, %[st]\n\t"
    "v_mov_b32 v38, 0\n\t"
    "s_mov_b64 exec, 1\n\t"
    "global_store_dword v38, v39, %[pg] sc1\n\t"
    "s_mov_b64 exec, -1\n\t"
    "s_mov_b32 %[p0], %[p1]\n\t"
    "s_branch 6b\n\t"
    "7:\n\t"
    "s_cmp_ge_i32 %[lf], %[cp]\n\t"
    "s_cbranch_scc1 8b\n\t"
    "s_mov_b32 m0, %[sl]\n\t"
    "s_nop 0\n\t"
    "global_load_lds_dword %[dv], %[db] sc1\n\t"
    "s_branch 8b\n\t"
    "9:"
    : "=&{v[80:95]}"(pd), [ea] "+&v"(ea), [da] "+&v"(da), [b] "+&s"(b), [lf] "+&s"(lf), "+{s[36:37]}"(p1), "+{s[38:39]}"(p0), [sa] "+&v"(sa), [st] "=&s"(st), [sl] "+&s"(sl), [p0] "+&s"(pf)
    : [rw] "v"(rw), [ab] "s"(ab), [cp] "s"(cp), [mu] "s"(mu), [ad] "v"(ad), "{v[128:159]}"(W[0]), "{v[160:191]}"(W[1]), "{v[192:223]}"(W[2]), "{v[224:255]}"(W[3]), [co0] "v"(co0), [w0] "s"(w0), [dv] "v"(dv), [db] "s"(db), [pg] "s"(pg), [po] "s"(po - 1), [p1] "s"(b + po), [co1] "v"(co1)
    : "v38", "v39", "v40", "v41", "v42", "v43", "v44", "v45", "v46", "v47", "v48", "v49", "v50", "v51", "v52", "v53", "v54", "v55", "v56", "v57", "v58", "v59", "v60", "v61", "v62", "v63", "v64", "v65", "v66", "v67", "v68", "v69", "v70", "v71", "v72", "v73", "v74", "v75", "v76", "v77", "v78", "v79", "v96", "v97", "v98", "v99", "v100", "v101", "v102", "v103", "v104", "v105", "v106", "v107", "v108", "v109", "v110", "v111", "v112", "v113", "v114", "v115", "v116", "v117", "v118", "v119", "v120", "v121", "v122", "v123", "v124", "v125", "v126", "v127", "vcc", "scc", "m0", "memory");
}
__device__ __forceinline__ void lean_ld0(const u32x32 (&W)[4], unsigned ea, unsigned da, unsigned x, unsigned lb, const char* s0, const char* s1, const unsigned (&av)[8], unsigned ps, unsigned dv, const char* db, unsigned rw, unsigned fa, unsigned fv,
    unsigned ga, unsigned mu, unsigned ad, int th, unsigned& sv, unsigned& got) {
  asm volatile("s_setprio 2\n\t"
    "ds_read_b128 v[96:99], %[ea] offset:0\n\t"
    "ds_read_b128 v[100:103], %[ea] offset:2048\n\t"
    "ds_read_b128 v[104:107], %[da] offset:0\n\t"
    "ds_read_b128 v[108:111], %[da] offset:2048\n\t"
    "ds_read_b128 v[112:115], %[ea] offset:4096\n\t"
    "ds_read_b128 v[116:119], %[ea] offset:6144\n\t"
    "s_waitcnt lgkmcnt(4)\n\t"
    "v_mfma_f32_16x16x32_bf16 v[80:83], v[128:131], v[96:99], 0\n\t"
    "ds_read_b128 v[120:123], %[da] offset:4096\n\t"
    "v_mfma_f32_16x16x32_bf16 v[84:87], v[132:135], v[96:99], 0\n\t"
    "ds_read_b128 v[124:127], %[da] offset:6144\n\t"
    "v_mfma_f32_16x16x32_bf16 v[88:91], v[128:131], v[100:103], 0\n\t"
    "v_mfma_f32_16x16x32_bf16 v[92:95], v[132:135], v[100:103], 0\n\t"
    "s_waitcnt vmcnt(24)\n\t"
    "s_cmp_lt_u32 %[x], 2\n\t"
    "s_cbranch_scc0 41f\n\t"
    "s_waitcnt vmcnt(8)\n\t"
    "41:\n\t"
    "s_barrier\n\t"
    "s_waitcnt lgkmcnt(4)\n\t"
    "v_mfma_f32_16x16x32_bf16 v[80:83], v[136:139], v[104:107], v[80:83]\n\t"
    "ds_read_b128 v[96:99], %[ea] offset:8192\n\t"
    "v_mfma_f32_16x16x32_bf16 v[84:87], v[140:143], v[104:107], v[84:87]\n\t"
    "ds_read_b128 v[100:103], %[ea] offset:10240\n\t"
    "v_mfma_f32_16x16x32_bf16 v[88:91], v[136:139], v[108:111], v[88:91]\n\t"
    "v_mfma_f32_16x16x32_bf16 v[92:95], v[140:143], v[108:111], v[92:95]\n\t"
    "s_waitcnt lgkmcnt(4)\n\t"
    "v_mfma_f32_16x16x32_bf16 v[80:83], v[144:147], v[112:115], v[80:83]\n\t"
    "ds_read_b128 v[104:107], %[da] offset:8192\n\t"
    "v_mfma_f32_16x16x32_bf16 v[84:87], v[148:151], v[112:115], v[84:87]\n\t"
    "ds_read_b128 v[108:111], %[da] offset:10240\n\t"
    "v_mfma_f32_16x16x32_bf16 v[88:91], v[144:147], v[116:119], v[88:91]\n\t"
    "v_mfma_f32_16x16x32_bf16 v[92:95], v[148:151], v[116:119], v[92:95]\n\t"
    "s_waitcnt lgkmcnt(4)\n\t"
    "v_mfma_f32_16x16x32_bf16 v[80:83], v[152:155], v[120:123], v[80:83]\n\t"
    "ds_read_b128 v[112:115], %[ea] offset:12288\n\t"
    "v_mfma_f32_16x16x32_bf16 v[84:87], v[156:159], v[120:123], v[84:87]\n\t"
    "ds_read_b128 v[116:119], %[ea] offset:14336\n\t"
    "v_mfma_f32_16x16x32_bf16 v[88:91], v[152:155], v[124:127], v[88:91]\n\t"
    "v_mfma_f32_16x16x32_bf16 v[92:95], v[156:159], v[124:127], v[92:95]\n\t"
    "s_waitcnt lgkmcnt(4)\n\t"
    "v_mfma_f32_16x16x32_bf16 v[80:83], v[160:163], v[96:99], v[80:83]\n\t"
    "ds_read_b128 v[120:123], %[da] offset:12288\n\t"
    "v_mfma_f32_16x16x32_bf16 v[84:87], v[164:167], v[96:99], v[84:87]\n\t"
    "ds_read_b128 v[124:127], %[da] offset:14336\n\t"
    "v_mfma_f32_16x16x32_bf16 v[88:91], v[160:163], v[100:103], v[88:91]\n\t"
    "v_mfma_f32_16x16x32_bf16 v[92:95], v[164:167], v[100:103], v[92:95]\n\t"
    "s_waitcnt vmcnt(20)\n\t"
    "s_cmp_lt_u32 %[x], 2\n\t"
    "s_cbranch_scc0 45f\n\t"
    "s_waitcnt vmcnt(4)\n\t"
    "45:\n\t"
    "s_barrier\n\t"
    "s_waitcnt lgkmcnt(4)\n\t"
    "v_mfma_f32_16x16x32_bf16 v[80:83], v[168:171], v[104:107], v[80:83]\n\t"
    "ds_read_b128 v[96:99], %[ea] offset:16384\n\t"
    "v_mfma_f32_16x16x32_bf16 v[84:87], v[172:175], v[104:107], v[84:87]\n\t"
    "ds_read_b128 v[100:103], %[ea] offset:18432\n\t"
    "v_mfma_f32_16x16x32_bf16 v[88:91], v[168:171], v[108:111], v[88:91]\n\t"
    "v_mfma_f32_16x16x32_bf16 v[92:95], v[172:175], v[108:111], v[92:95]\n\t"
    "s_waitcnt lgkmcnt(4)\n\t"
    "v_mfma_f32_16x16x32_bf16 v[80:83], v[176:179], v[112:115], v[80:83]\n\t"
    "ds_read_b128 v[104:107], %[da] offset:16384\n\t"
    "v_mfma_f32_16x16x32_bf16 v[84:87], v[180:183], v[112:115], v[84:87]\n\t"
    "ds_read_b128 v[108:111], %[da] offset:18432\n\t"
    "v_mfma_f32_16x16x32_bf16 v[88:91], v[176:179], v[116:119], v[88:91]\n\t"
    "v_mfma_f32_16x16x32_bf16 v[92:95], v[180:183], v[116:119], v[92:95]\n\t"
    "s_waitcnt lgkmcnt(4)\n\t"
    "v_mfma_f32_16x16x32_bf16 v[80:83], v[184:187], v[120:123], v[80:83]\n\t"
    "ds_read_b128 v[112:115], %[ea] offset:20480\n\t"
    "v_mfma_f32_16x16x32_bf16 v[84:87], v[188:191], v[120:123], v[84:87]\n\t"
    "ds_read_b128 v[116:119], %[ea] offset:22528\n\t"
    "v_mfma_f32_16x16x32_bf16 v[88:91], v[184:187], v[124:127], v[88:91]\n\t"
    "v_mfma_f32_16x16x32_bf16 v[92:95], v[188:191], v[124:127], v[92:95]\n\t"
    "s_waitcnt lgkmcnt(4)\n\t"
    "v_mfma_f32_16x16x32_bf16 v[80:83], v[192:195], v[96:99], v[80:83]\n\t"
    "ds_read_b128 v[120:123], %[da] offset:20480\n\t"
    "v_mfma_f32_16x16x32_bf16 v[84:87], v[196:199], v[96:99], v[84:87]\n\t"
    "ds_read_b128 v[124:127], %[da] offset:22528\n\t"
    "v_mfma_f32_16x16x32_bf16 v[88:91], v[192:195], v[100:103], v[88:91]\n\t"
    "v_mfma_f32_16x16x32_bf16 v[92:95], v[196:199], v[100:103], v[92:95]\n\t"
    "s_waitcnt vmcnt(16)\n\t"
    "s_cmp_lt_u32 %[x], 2\n\t"
    "s_cbranch_scc0 49f\n\t"
    "s_waitcnt vmcnt(0)\n\t"
    "49:\n\t"
    "s_barrier\n\t"
    "s_waitcnt lgkmcnt(4)\n\t"
    "v_mfma_f32_16x16x32_bf16 v[80:83], v[200:203], v[104:107], v[80:83]\n\t"
    "ds_read_b128 v[96:99], %[ea] offset:24576\n\t"
    "v_mfma_f32_16x16x32_bf16 v[84:87], v[204:207], v[104:107], v[84:87]\n\t"
    "ds_read_b128 v[100:103], %[ea] offset:26624\n\t"
    "v_mfma_f32_16x16x32_bf16 v[88:91], v[200:203], v[108:111], v[88:91]\n\t"
    "v_mfma_f32_16x16x32_bf16 v[92:95], v[204:207], v[108:111], v[92:95]\n\t"
    "s_waitcnt lgkmcnt(4)\n\t"
    "v_mfma_f32_16x16x32_bf16 v[80:83], v[208:211], v[112:115], v[80:83]\n\t"
    "ds_read_b128 v[104:107], %[da] offset:24576\n\t"
    "v_mfma_f32_16x16x32_bf16 v[84:87], v[212:215], v[112:115], v[84:87]\n\t"
    "ds_read_b128 v[108:111], %[da] offset:26624\n\t"
    "v_mfma_f32_16x16x32_bf16 v[88:91], v[208:211], v[116:119], v[88:91]\n\t"
    "v_mfma_f32_16x16x32_bf16 v[92:95], v[212:215], v[116:119], v[92:95]\n\t"
    "s_cmp_eq_u32 %[ps], 0\n\t"
    "s_cbranch_scc1 10f\n\t"
    "global_load_dword %[sv], %[dv], %[db] sc1\n\t"
    "10:\n\t"
    "s_waitcnt lgkmcnt(4)\n\t"
    "v_mfma_f32_16x16x32_bf16 v[80:83], v[216:219], v[120:123], v[80:83]\n\t"
    "ds_read_b128 v[112:115], %[ea] offset:28672\n\t"
    "v_mfma_f32_16x16x32_bf16 v[84:87], v[220:223], v[120:123], v[84:87]\n\t"
    "ds_read_b128 v[116:119], %[ea] offset:30720\n\t"
    "v_mfma_f32_16x16x32_bf16 v[88:91], v[216:219], v[124:127], v[88:91]\n\t"
    "v_mfma_f32_16x16x32_bf16 v[92:95], v[220:223], v[124:127], v[92:95]\n\t"
    "s_waitcnt lgkmcnt(4)\n\t"
    "v_mfma_f32_16x16x32_bf16 v[80:83], v[224:227], v[96:99], v[80:83]\n\t"
    "ds_read_b128 v[120:123], %[da] offset:28672\n\t"
    "v_mfma_f32_16x16x32_bf16 v[84:87], v[228:231], v[96:99], v[84:87]\n\t"
    "ds_read_b128 v[124:127], %[da] offset:30720\n\t"
    "v_mfma_f32_16x16x32_bf16 v[88:91], v[224:227], v[100:103], v[88:91]\n\t"
    "v_mfma_f32_16x16x32_bf16 v[92:95], v[228:231], v[100:103], v[92:95]\n\t"
    "s_waitcnt lgkmcnt(4)\n\t"
    "v_mfma_f32_16x16x32_bf16 v[80:83], v[232:235], v[104:107], v[80:83]\n\t"
    "v_mfma_f32_16x16x32_bf16 v[84:87], v[236:239], v[104:107], v[84:87]\n\t"
    "v_mfma_f32_16x16x32_bf16 v[88:91], v[232:235], v[108:111], v[88:91]\n\t"
    "v_mfma_f32_16x16x32_bf16 v[92:95], v[236:239], v[108:111], v[92:95]\n\t"
    "s_waitcnt lgkmcnt(2)\n\t"
    "v_mfma_f32_16x16x32_bf16 v[80:83], v[240:243], v[112:115], v[80:83]\n\t"
    "v_mfma_f32_16x16x32_bf16 v[84:87], v[244:247], v[112:115], v[84:87]\n\t"
    "v_mfma_f32_16x16x32_bf16 v[88:91], v[240:243], v[116:119], v[88:91]\n\t"
    "v_mfma_f32_16x16x32_bf16 v[92:95], v[244:247], v[116:119], v[92:95]\n\t"
    "s_waitcnt lgkmcnt(0)\n\t"
    "v_mfma_f32_16x16x32_bf16 v[80:83], v[248:251], v[120:123], v[80:83]\n\t"
    "v_mfma_f32_16x16x32_bf16 v[84:87], v[252:255], v[120:123], v[84:87]\n\t"
    "v_mfma_f32_16x16x32_bf16 v[88:91], v[248:251], v[124:127], v[88:91]\n\t"
    "v_mfma_f32_16x16x32_bf16 v[92:95], v[252:255], v[124:127], v[92:95]\n\t"
    "s_mov_b32 %[got], 0\n\t"
    "v_mov_b32 v76, %[fa]\n\t"
    "v_mov_b32 v77, %[fv]\n\t"
    "v_add_u32 v78, 1, v77\n\t"
    "v_mov_b32 v79, %[ga]\n\t"
    "ds_write_b128 %[rw], v[80:83] offset:0\n\t"
    "ds_write_b128 %[rw], v[84:87] offset:1024\n\t"
    "ds_write_b128 %[rw], v[88:91] offset:2048\n\t"
    "ds_write_b128 %[rw], v[92:95] offset:3072\n\t"
    "ds_write_b32 v76, v77\n\t"
    "s_cmp_eq_u32 %[ps], 0\n\t"
    "s_cbranch_scc1 9f\n\t"
    "s_waitcnt vmcnt(0)\n\t"
    "v_mad_u32_u24 v76, %[sv], %[mu], %[ad]\n\t"
    "v_cmp_ge_i32 vcc, %[th], v76\n\t"
    "s_cbranch_vccnz 9f\n\t"
    "ds_write2_b32 v79, v78, v78 offset1:1\n\t"
    "s_add_u32 m0, %[lb], 0\n\t"
    "s_nop 0\n\t"
    "global_load_lds_dwordx4 %[av0], %[s0] sc1\n\t"
    "s_add_u32 m0, %[lb], 32768\n\t"
    "s_nop 0\n\t"
    "global_load_lds_dwordx4 %[av1], %[s0] sc1\n\t"
    "s_add_u32 m0, %[lb], 4096\n\t"
    "s_nop 0\n\t"
    "global_load_lds_dwordx4 %[av2], %[s0] sc1\n\t"
    "s_add_u32 m0, %[lb], 36864\n\t"
    "s_nop 0\n\t"
    "global_load_lds_dwordx4 %[av3], %[s0] sc1\n\t"
    "s_add_u32 m0, %[lb], 8192\n\t"
    "s_nop 0\n\t"
    "global_load_lds_dwordx4 %[av4], %[s0] sc1\n\t"
    "s_add_u32 m0, %[lb], 40960\n\t"
    "s_nop 0\n\t"
    "global_load_lds_dwordx4 %[av5], %[s0] sc1\n\t"
    "s_add_u32 m0, %[lb], 12288\n\t"
    "s_nop 0\n\t"
    "global_load_lds_dwordx4 %[av6], %[s0] sc1\n\t"
    "s_add_u32 m0, %[lb], 45056\n\t"
    "s_nop 0\n\t"
    "global_load_lds_dwordx4 %[av7], %[s0] sc1\n\t"
    "s_add_u32 m0, %[lb], 16384\n\t"
    "s_nop 0\n\t"
    "global_load_lds_dwordx4 %[av0], %[s1] sc1\n\t"
    "s_add_u32 m0, %[lb], 49152\n\t"
    "s_nop 0\n\t"
    "global_load_lds_dwordx4 %[av1], %[s1] sc1\n\t"
    "s_add_u32 m0, %[lb], 20480\n\t"
    "s_nop 0\n\t"
    "global_load_lds_dwordx4 %[av2], %[s1] sc1\n\t"
    "s_add_u32 m0, %[lb], 53248\n\t"
    "s_nop 0\n\t"
    "global_load_lds_dwordx4 %[av3], %[s1] sc1\n\t"
    "s_add_u32 m0, %[lb], 24576\n\t"
    "s_nop 0\n\t"
    "global_load_lds_dwordx4 %[av4], %[s1] sc1\n\t"
    "s_add_u32 m0, %[lb], 57344\n\t"
    "s_nop 0\n\t"
    "global_load_lds_dwordx4 %[av5], %[s1] sc1\n\t"
    "s_add_u32 m0, %[lb], 28672\n\t"
    "s_nop 0\n\t"
    "global_load_lds_dwordx4 %[av6], %[s1] sc1\n\t"
    "s_add_u32 m0, %[lb], 61440\n\t"
    "s_nop 0\n\t"
    "global_load_lds_dwordx4 %[av7], %[s1] sc1\n\t"
    "s_mov_b32 %[got], 1\n\t"
    "9:"
    : [sv] "+&v"(sv), [got] "=&s"(got)
    : [ea] "v"(ea), [da] "v"(da), [x] "s"(x), "{v[128:159]}"(W[0]), "{v[160:191]}"(W[1]), "{v[192:223]}"(W[2]), "{v[224:255]}"(W[3]), [lb] "s"(lb), [s0] "s"(s0), [s1] "s"(s1), [av0] "v"(av[0]), [av1] "v"(av[1]), [av2] "v"(av[2]), [av3] "v"(av[3]), [av4] "v"(av[4]), [av5] "v"(av[5]), [av6] "v"(av[6]), [av7] "v"(av[7]), [ps] "s"(ps), [dv] "v"(dv), [db] "s"(db), [rw] "v"(rw), [fa] "s"(fa), [fv] "s"(fv), [ga] "s"(ga), [mu] "s"(mu), [ad] "v"(ad), [th] "s"(th)
    : "v96", "v97", "v98", "v99", "v100", "v101", "v102", "v103", "v104", "v105", "v106", "v107", "v108", "v109", "v110", "v111", "v112", "v113", "v114", "v115", "v116", "v117", "v118", "v119", "v120", "v121", "v122", "v123", "v124", "v125", "v126", "v127", "v76", "v77", "v78", "v79", "v80", "v81", "v82", "v83", "v84", "v85", "v86", "v87", "v88", "v89", "v90", "v91", "v92", "v93", "v94", "v95", "vcc", "scc", "m0", "memory");
}
__device__ __forceinline__ void lean_dm0(const u32x32 (&W)[4], unsigned ea, unsigned da, unsigned x, unsigned lb, const char* s0, const char* s1, const unsigned (&av)[8], unsigned ps, unsigned dv, const char* db, unsigned rw, unsigned fa, unsigned fv,
    unsigned ga, unsigned mu, unsigned ad, int th, unsigned& sv, unsigned& got) {
  asm volatile("s_setprio 2\n\t"
    "ds_read_b128 v[96:99], %[ea] offset:0\n\t"
    "ds_read_b128 v[100:103], %[ea] offset:2048\n\t"
    "ds_read_b128 v[104:107], %[da] offset:0\n\t"
    "ds_read_b128 v[108:111], %[da] offset:2048\n\t"
    "ds_read_b128 v[112:115], %[ea] offset:4096\n\t"
    "ds_read_b128 v[116:119], %[ea] offset:6144\n\t"
    "s_waitcnt lgkmcnt(4)\n\t"
    "v_mfma_f32_16x16x32_bf16 v[80:83], v[128:131], v[96:99], 0\n\t"
    "ds_read_b128 v[120:123], %[da] offset:4096\n\t"
    "s_add_u32 m0, %[lb], 0\n\t"
    "v_mfma_f32_16x16x32_bf16 v[84:87], v[132:135], v[96:99], 0\n\t"
    "ds_read_b128 v[124:127], %[da] offset:6144\n\t"
    "global_load_lds_dwordx4 %[av0], %[s0] sc1\n\t"
    "v_mfma_f32_16x16x32_bf16 v[88:91], v[128:131], v[100:103], 0\n\t"
    "s_add_u32 m0, %[lb], 32768\n\t"
    "v_mfma_f32_16x16x32_bf16 v[92:95], v[132:135], v[100:103], 0\n\t"
    "global_load_lds_dwordx4 %[av1], %[s0] sc1\n\t"
    "s_waitcnt vmcnt(10)\n\t"
    "s_barrier\n\t"
    "s_waitcnt lgkmcnt(4)\n\t"
    "v_mfma_f32_16x16x32_bf16 v[80:83], v[136:139], v[104:107], v[80:83]\n\t"
    "ds_read_b128 v[96:99], %[ea] offset:8192\n\t"
    "s_add_u32 m0, %[lb], 4096\n\t"
    "v_mfma_f32_16x16x32_bf16 v[84:87], v[140:143], v[104:107], v[84:87]\n\t"
    "ds_read_b128 v[100:103], %[ea] offset:10240\n\t"
    "global_load_lds_dwordx4 %[av2], %[s0] sc1\n\t"
    "v_mfma_f32_16x16x32_bf16 v[88:91], v[136:139], v[108:111], v[88:91]\n\t"
    "s_add_u32 m0, %[lb], 36864\n\t"
    "v_mfma_f32_16x16x32_bf16 v[92:95], v[140:143], v[108:111], v[92:95]\n\t"
    "global_load_lds_dwordx4 %[av3], %[s0] sc1\n\t"
    "s_waitcnt lgkmcnt(4)\n\t"
    "v_mfma_f32_16x16x32_bf16 v[80:83], v[144:147], v[112:115], v[80:83]\n\t"
    "ds_read_b128 v[104:107], %[da] offset:8192\n\t"
    "s_add_u32 m0, %[lb], 8192\n\t"
    "v_mfma_f32_16x16x32_bf16 v[84:87], v[148:151], v[112:115], v[84:87]\n\t"
    "ds_read_b128 v[108:111], %[da] offset:10240\n\t"
    "global_load_lds_dwordx4 %[av4], %[s0] sc1\n\t"
    "v_mfma_f32_16x16x32_bf16 v[88:91], v[144:147], v[116:119], v[88:91]\n\t"
    "s_add_u32 m0, %[lb], 40960\n\t"
    "v_mfma_f32_16x16x32_bf16 v[92:95], v[148:151], v[116:119], v[92:95]\n\t"
    "global_load_lds_dwordx4 %[av5], %[s0] sc1\n\t"
    "s_waitcnt lgkmcnt(4)\n\t"
    "v_mfma_f32_16x16x32_bf16 v[80:83], v[152:155], v[120:123], v[80:83]\n\t"
    "ds_read_b128 v[112:115], %[ea] offset:12288\n\t"
    "s_add_u32 m0, %[lb], 12288\n\t"
    "v_mfma_f32_16x16x32_bf16 v[84:87], v[156:159], v[120:123], v[84:87]\n\t"
    "ds_read_b128 v[116:119], %[ea] offset:14336\n\t"
    "global_load_lds_dwordx4 %[av6], %[s0] sc1\n\t"
    "v_mfma_f32_16x16x32_bf16 v[88:91], v[152:155], v[124:127], v[88:91]\n\t"
    "s_add_u32 m0, %[lb], 45056\n\t"
    "v_mfma_f32_16x16x32_bf16 v[92:95], v[156:159], v[124:127], v[92:95]\n\t"
    "global_load_lds_dwordx4 %[av7], %[s0] sc1\n\t"
    "s_waitcnt lgkmcnt(4)\n\t"
    "v_mfma_f32_16x16x32_bf16 v[80:83], v[160:163], v[96:99], v[80:83]\n\t"
    "ds_read_b128 v[120:123], %[da] offset:12288\n\t"
    "s_add_u32 m0, %[lb], 16384\n\t"
    "v_mfma_f32_16x16x32_bf16 v[84:87], v[164:167], v[96:99], v[84:87]\n\t"
    "ds_read_b128 v[124:127], %[da] offset:14336\n\t"
    "global_load_lds_dwordx4 %[av0], %[s1] sc1\n\t"
    "v_mfma_f32_16x16x32_bf16 v[88:91], v[160:163], v[100:103], v[88:91]\n\t"
    "s_add_u32 m0, %[lb], 49152\n\t"
    "v_mfma_f32_16x16x32_bf16 v[92:95], v[164:167], v[100:103], v[92:95]\n\t"
    "global_load_lds_dwordx4 %[av1], %[s1] sc1\n\t"
    "s_waitcnt vmcnt(14)\n\t"
    "s_barrier\n\t"
    "s_waitcnt lgkmcnt(4)\n\t"
    "v_mfma_f32_16x16x32_bf16 v[80:83], v[168:171], v[104:107], v[80:83]\n\t"
    "ds_read_b128 v[96:99], %[ea] offset:16384\n\t"
    "s_add_u32 m0, %[lb], 20480\n\t"
    "v_mfma_f32_16x16x32_bf16 v[84:87], v[172:175], v[104:107], v[84:87]\n\t"
    "ds_read_b128 v[100:103], %[ea] offset:18432\n\t"
    "global_load_lds_dwordx4 %[av2], %[s1] sc1\n\t"
    "v_mfma_f32_16x16x32_bf16 v[88:91], v[168:171], v[108:111], v[88:91]\n\t"
    "s_add_u32 m0, %[lb], 53248\n\t"
    "v_mfma_f32_16x16x32_bf16 v[92:95], v[172:175], v[108:111], v[92:95]\n\t"
    "global_load_lds_dwordx4 %[av3], %[s1] sc1\n\t"
    "s_waitcnt lgkmcnt(4)\n\t"
    "v_mfma_f32_16x16x32_bf16 v[80:83], v[176:179], v[112:115], v[80:83]\n\t"
    "ds_read_b128 v[104:107], %[da] offset:16384\n\t"
    "s_add_u32 m0, %[lb], 24576\n\t"
    "v_mfma_f32_16x16x32_bf16 v[84:87], v[180:183], v[112:115], v[84:87]\n\t"
    "ds_read_b128 v[108:111], %[da] offset:18432\n\t"
    "global_load_lds_dwordx4 %[av4], %[s1] sc1\n\t"
    "v_mfma_f32_16x16x32_bf16 v[88:91], v[176:179], v[116:119], v[88:91]\n\t"
    "s_add_u32 m0, %[lb], 57344\n\t"
    "v_mfma_f32_16x16x32_bf16 v[92:95], v[180:183], v[116:119], v[92:95]\n\t"
    "global_load_lds_dwordx4 %[av5], %[s1] sc1\n\t"
    "s_waitcnt lgkmcnt(4)\n\t"
    "v_mfma_f32_16x16x32_bf16 v[80:83], v[184:187], v[120:123], v[80:83]\n\t"
    "ds_read_b128 v[112:115], %[ea] offset:20480\n\t"
    "s_add_u32 m0, %[lb], 28672\n\t"
    "v_mfma_f32_16x16x32_bf16 v[84:87], v[188:191], v[120:123], v[84:87]\n\t"
    "ds_read_b128 v[116:119], %[ea] offset:22528\n\t"
    "global_load_lds_dwordx4 %[av6], %[s1] sc1\n\t"
    "v_mfma_f32_16x16x32_bf16 v[88:91], v[184:187], v[124:127], v[88:91]\n\t"
    "s_add_u32 m0, %[lb], 61440\n\t"
    "v_mfma_f32_16x16x32_bf16 v[92:95], v[188:191], v[124:127], v[92:95]\n\t"
    "global_load_lds_dwordx4 %[av7], %[s1] sc1\n\t"
    "s_waitcnt lgkmcnt(4)\n\t"
    "v_mfma_f32_16x16x32_bf16 v[80:83], v[192:195], v[96:99], v[80:83]\n\t"
    "ds_read_b128 v[120:123], %[da] offset:20480\n\t"
    "v_mfma_f32_16x16x32_bf16 v[84:87], v[196:199], v[96:99], v[84:87]\n\t"
    "ds_read_b128 v[124:127], %[da] offset:22528\n\t"
    "v_mfma_f32_16x16x32_bf16 v[88:91], v[192:195], v[100:103], v[88:91]\n\t"
    "v_mfma_f32_16x16x32_bf16 v[92:95], v[196:199], v[100:103], v[92:95]\n\t"
    "s_waitcnt vmcnt(16)\n\t"
    "s_barrier\n\t"
    "s_waitcnt lgkmcnt(4)\n\t"
    "v_mfma_f32_16x16x32_bf16 v[80:83], v[200:203], v[104:107], v[80:83]\n\t"
    "ds_read_b128 v[96:99], %[ea] offset:24576\n\t"
    "v_mfma_f32_16x16x32_bf16 v[84:87], v[204:207], v[104:107], v[84:87]\n\t"
    "ds_read_b128 v[100:103], %[ea] offset:26624\n\t"
    "v_mfma_f32_16x16x32_bf16 v[88:91], v[200:203], v[108:111], v[88:91]\n\t"
    "v_mfma_f32_16x16x32_bf16 v[92:95], v[204:207], v[108:111], v[92:95]\n\t"
    "s_waitcnt lgkmcnt(4)\n\t"
    "v_mfma_f32_16x16x32_bf16 v[80:83], v[208:211], v[112:115], v[80:83]\n\t"
    "ds_read_b128 v[104:107], %[da] offset:24576\n\t"
    "v_mfma_f32_16x16x32_bf16 v[84:87], v[212:215], v[112:115], v[84:87]\n\t"
    "ds_read_b128 v[108:111], %[da] offset:26624\n\t"
    "v_mfma_f32_16x16x32_bf16 v[88:91], v[208:211], v[116:119], v[88:91]\n\t"
    "v_mfma_f32_16x16x32_bf16 v[92:95], v[212:215], v[116:119], v[92:95]\n\t"
    "s_cmp_eq_u32 %[ps], 0\n\t"
    "s_cbranch_scc1 10f\n\t"
    "global_load_dword %[sv], %[dv], %[db] sc1\n\t"
    "10:\n\t"
    "s_waitcnt lgkmcnt(4)\n\t"
    "v_mfma_f32_16x16x32_bf16 v[80:83], v[216:219], v[120:123], v[80:83]\n\t"
    "ds_read_b128 v[112:115], %[ea] offset:28672\n\t"
    "v_mfma_f32_16x16x32_bf16 v[84:87], v[220:223], v[120:123], v[84:87]\n\t"
    "ds_read_b128 v[116:119], %[ea] offset:30720\n\t"
    "v_mfma_f32_16x16x32_bf16 v[88:91], v[216:219], v[124:127], v[88:91]\n\t"
    "v_mfma_f32_16x16x32_bf16 v[92:95], v[220:223], v[124:127], v[92:95]\n\t"
    "s_waitcnt lgkmcnt(4)\n\t"
    "v_mfma_f32_16x16x32_bf16 v[80:83], v[224:227], v[96:99], v[80:83]\n\t"
    "ds_read_b128 v[120:123], %[da] offset:28672\n\t"
    "v_mfma_f32_16x16x32_bf16 v[84:87], v[228:231], v[96:99], v[84:87]\n\t"
    "ds_read_b128 v[124:127], %[da] offset:30720\n\t"
    "v_mfma_f32_16x16x32_bf16 v[88:91], v[224:227], v[100:103], v[88:91]\n\t"
    "v_mfma_f32_16x16x32_bf16 v[92:95], v[228:231], v[100:103], v[92:95]\n\t"
    "s_waitcnt lgkmcnt(4)\n\t"
    "v_mfma_f32_16x16x32_bf16 v[80:83], v[232:235], v[104:107], v[80:83]\n\t"
    "v_mfma_f32_16x16x32_bf16 v[84:87], v[236:239], v[104:107], v[84:87]\n\t"
    "v_mfma_f32_16x16x32_bf16 v[88:91], v[232:235], v[108:111], v[88:91]\n\t"
    "v_mfma_f32_16x16x32_bf16 v[92:95], v[236:239], v[108:111], v[92:95]\n\t"
    "s_waitcnt lgkmcnt(2)\n\t"
    "v_mfma_f32_16x16x32_bf16 v[80:83], v[240:243], v[112:115], v[80:83]\n\t"
    "v_mfma_f32_16x16x32_bf16 v[84:87], v[244:247], v[112:115], v[84:87]\n\t"
    "v_mfma_f32_16x16x32_bf16 v[88:91], v[240:243], v[116:119], v[88:91]\n\t"
    "v_mfma_f32_16x16x32_bf16 v[92:95], v[244:247], v[116:119], v[92:95]\n\t"
    "s_waitcnt lgkmcnt(0)\n\t"
    "v_mfma_f32_16x16x32_bf16 v[80:83], v[248:251], v[120:123], v[80:83]\n\t"
    "v_mfma_f32_16x16x32_bf16 v[84:87], v[252:255], v[120:123], v[84:87]\n\t"
    "v_mfma_f32_16x16x32_bf16 v[88:91], v[248:251], v[124:127], v[88:91]\n\t"
    "v_mfma_f32_16x16x32_bf16 v[92:95], v[252:255], v[124:127], v[92:95]\n\t"
    "s_mov_b32 %[got], 0\n\t"
    "v_mov_b32 v76, %[fa]\n\t"
    "v_mov_b32 v77, %[fv]\n\t"
    "v_add_u32 v78, 1, v77\n\t"
    "v_mov_b32 v79, %[ga]\n\t"
    "ds_write_b128 %[rw], v[80:83] offset:0\n\t"
    "ds_write_b128 %[rw], v[84:87] offset:1024\n\t"
    "ds_write_b128 %[rw], v[88:91] offset:2048\n\t"
    "ds_write_b128 %[rw], v[92:95] offset:3072\n\t"
    "ds_write_b32 v76, v77\n\t"
    "s_cmp_eq_u32 %[ps], 0\n\t"
    "s_cbranch_scc1 9f\n\t"
    "s_waitcnt vmcnt(0)\n\t"
    "9:"
    : [sv] "+&v"(sv), [got] "=&s"(got)
    : [ea] "v"(ea), [da] "v"(da), [x] "s"(x), "{v[128:159]}"(W[0]), "{v[160:191]}"(W[1]), "{v[192:223]}"(W[2]), "{v[224:255]}"(W[3]), [lb] "s"(lb), [s0] "s"(s0), [s1] "s"(s1), [av0] "v"(av[0]), [av1] "v"(av[1]), [av2] "v"(av[2]), [av3] "v"(av[3]), [av4] "v"(av[4]), [av5] "v"(av[5]), [av6] "v"(av[6]), [av7] "v"(av[7]), [ps] "s"(ps), [dv] "v"(dv), [db] "s"(db), [rw] "v"(rw), [fa] "s"(fa), [fv] "s"(fv), [ga] "s"(ga), [mu] "s"(mu), [ad] "v"(ad), [th] "s"(th)
    : "v96", "v97", "v98", "v99", "v100", "v101", "v102", "v103", "v104", "v105", "v106", "v107", "v108", "v109", "v110", "v111", "v112", "v113", "v114", "v115", "v116", "v117", "v118", "v119", "v120", "v121", "v122", "v123", "v124", "v125", "v126", "v127", "v76", "v77", "v78", "v79", "v80", "v81", "v82", "v83", "v84", "v85", "v86", "v87", "v88", "v89", "v90", "v91", "v92", "v93", "v94", "v95", "vcc", "scc", "m0", "memory");
}
__device__ __forceinline__ void lean_ld1(const u32x32 (&W)[4], unsigned ea, unsigned da, unsigned x, unsigned lb, const char* s0, const char* s1, const unsigned (&av)[8], unsigned ps, unsigned dv, const char* db, unsigned rw, unsigned fa, unsigned fv,
    unsigned ga, unsigned mu, unsigned ad, int th, unsigned& sv, unsigned& got) {
  asm volatile("s_setprio 2\n\t"
    "ds_read_b128 v[96:99], %[ea] offset:0\n\t"
    "ds_read_b128 v[100:103], %[ea] offset:2048\n\t"
    "ds_read_b128 v[104:107], %[da] offset:0\n\t"
    "ds_read_b128 v[108:111], %[da] offset:2048\n\t"
    "ds_read_b128 v[112:115], %[ea] offset:4096\n\t"
    "ds_read_b128 v[116:119], %[ea] offset:6144\n\t"
    "s_waitcnt lgkmcnt(4)\n\t"
    "v_mfma_f32_16x16x32_bf16 v[80:83], v[128:131], v[96:99], 0\n\t"
    "ds_read_b128 v[120:123], %[da] offset:4096\n\t"
    "v_mfma_f32_16x16x32_bf16 v[84:87], v[132:135], v[96:99], 0\n\t"
    "ds_read_b128 v[124:127], %[da] offset:6144\n\t"
    "v_mfma_f32_16x16x32_bf16 v[88:91], v[128:131], v[100:103], 0\n\t"
    "v_mfma_f32_16x16x32_bf16 v[92:95], v[132:135], v[100:103], 0\n\t"
    "s_waitcnt vmcnt(12)\n\t"
    "s_cmp_lt_u32 %[x], 2\n\t"
    "s_cbranch_scc0 41f\n\t"
    "s_waitcnt vmcnt(4)\n\t"
    "41:\n\t"
    "s_barrier\n\t"
    "s_waitcnt lgkmcnt(4)\n\t"
    "v_mfma_f32_16x16x32_bf16 v[80:83], v[136:139], v[104:107], v[80:83]\n\t"
    "ds_read_b128 v[96:99], %[ea] offset:8192\n\t"
    "v_mfma_f32_16x16x32_bf16 v[84:87], v[140:143], v[104:107], v[84:87]\n\t"
    "ds_read_b128 v[100:103], %[ea] offset:10240\n\t"
    "v_mfma_f32_16x16x32_bf16 v[88:91], v[136:139], v[108:111], v[88:91]\n\t"
    "v_mfma_f32_16x16x32_bf16 v[92:95], v[140:143], v[108:111], v[92:95]\n\t"
    "s_waitcnt lgkmcnt(4)\n\t"
    "v_mfma_f32_16x16x32_bf16 v[80:83], v[144:147], v[112:115], v[80:83]\n\t"
    "ds_read_b128 v[104:107], %[da] offset:8192\n\t"
    "v_mfma_f32_16x16x32_bf16 v[84:87], v[148:151], v[112:115], v[84:87]\n\t"
    "ds_read_b128 v[108:111], %[da] offset:10240\n\t"
    "v_mfma_f32_16x16x32_bf16 v[88:91], v[144:147], v[116:119], v[88:91]\n\t"
    "v_mfma_f32_16x16x32_bf16 v[92:95], v[148:151], v[116:119], v[92:95]\n\t"
    "s_waitcnt lgkmcnt(4)\n\t"
    "v_mfma_f32_16x16x32_bf16 v[80:83], v[152:155], v[120:123], v[80:83]\n\t"
    "ds_read_b128 v[112:115], %[ea] offset:12288\n\t"
    "v_mfma_f32_16x16x32_bf16 v[84:87], v[156:159], v[120:123], v[84:87]\n\t"
    "ds_read_b128 v[116:119], %[ea] offset:14336\n\t"
    "v_mfma_f32_16x16x32_bf16 v[88:91], v[152:155], v[124:127], v[88:91]\n\t"
    "v_mfma_f32_16x16x32_bf16 v[92:95], v[156:159], v[124:127], v[92:95]\n\t"
    "s_waitcnt lgkmcnt(4)\n\t"
    "v_mfma_f32_16x16x32_bf16 v[80:83], v[160:163], v[96:99], v[80:83]\n\t"
    "ds_read_b128 v[120:123], %[da] offset:12288\n\t"
    "v_mfma_f32_16x16x32_bf16 v[84:87], v[164:167], v[96:99], v[84:87]\n\t"
    "ds_read_b128 v[124:127], %[da] offset:14336\n\t"
    "v_mfma_f32_16x16x32_bf16 v[88:91], v[160:163], v[100:103], v[88:91]\n\t"
    "v_mfma_f32_16x16x32_bf16 v[92:95], v[164:167], v[100:103], v[92:95]\n\t"
    "s_waitcnt vmcnt(10)\n\t"
    "s_cmp_lt_u32 %[x], 2\n\t"
    "s_cbranch_scc0 45f\n\t"
    "s_waitcnt vmcnt(2)\n\t"
    "45:\n\t"
    "s_barrier\n\t"
    "s_waitcnt lgkmcnt(4)\n\t"
    "v_mfma_f32_16x16x32_bf16 v[80:83], v[168:171], v[104:107], v[80:83]\n\t"
    "ds_read_b128 v[96:99], %[ea] offset:16384\n\t"
    "v_mfma_f32_16x16x32_bf16 v[84:87], v[172:175], v[104:107], v[84:87]\n\t"
    "ds_read_b128 v[100:103], %[ea] offset:18432\n\t"
    "v_mfma_f32_16x16x32_bf16 v[88:91], v[168:171], v[108:111], v[88:91]\n\t"
    "v_mfma_f32_16x16x32_bf16 v[92:95], v[172:175], v[108:111], v[92:95]\n\t"
    "s_waitcnt lgkmcnt(4)\n\t"
    "v_mfma_f32_16x16x32_bf16 v[80:83], v[176:179], v[112:115], v[80:83]\n\t"
    "ds_read_b128 v[104:107], %[da] offset:16384\n\t"
    "v_mfma_f32_16x16x32_bf16 v[84:87], v[180:183], v[112:115], v[84:87]\n\t"
    "ds_read_b128 v[108:111], %[da] offset:18432\n\t"
    "v_mfma_f32_16x16x32_bf16 v[88:91], v[176:179], v[116:119], v[88:91]\n\t"
    "v_mfma_f32_16x16x32_bf16 v[92:95], v[180:183], v[116:119], v[92:95]\n\t"
    "s_waitcnt lgkmcnt(4)\n\t"
    "v_mfma_f32_16x16x32_bf16 v[80:83], v[184:187], v[120:123], v[80:83]\n\t"
    "ds_read_b128 v[112:115], %[ea] offset:20480\n\t"
    "v_mfma_f32_16x16x32_bf16 v[84:87], v[188:191], v[120:123], v[84:87]\n\t"
    "ds_read_b128 v[116:119], %[ea] offset:22528\n\t"
    "v_mfma_f32_16x16x32_bf16 v[88:91], v[184:187], v[124:127], v[88:91]\n\t"
    "v_mfma_f32_16x16x32_bf16 v[92:95], v[188:191], v[124:127], v[92:95]\n\t"
    "s_waitcnt lgkmcnt(4)\n\t"
    "v_mfma_f32_16x16x32_bf16 v[80:83], v[192:195], v[96:99], v[80:83]\n\t"
    "ds_read_b128 v[120:123], %[da] offset:20480\n\t"
    "v_mfma_f32_16x16x32_bf16 v[84:87], v[196:199], v[96:99], v[84:87]\n\t"
    "ds_read_b128 v[124:127], %[da] offset:22528\n\t"
    "v_mfma_f32_16x16x32_bf16 v[88:91], v[192:195], v[100:103], v[88:91]\n\t"
    "v_mfma_f32_16x16x32_bf16 v[92:95], v[196:199], v[100:103], v[92:95]\n\t"
    "s_waitcnt vmcnt(8)\n\t"
    "s_cmp_lt_u32 %[x], 2\n\t"
    "s_cbranch_scc0 49f\n\t"
    "s_waitcnt vmcnt(0)\n\t"
    "49:\n\t"
    "s_barrier\n\t"
    "s_waitcnt lgkmcnt(4)\n\t"
    "v_mfma_f32_16x16x32_bf16 v[80:83], v[200:203], v[104:107], v[80:83]\n\t"
    "ds_read_b128 v[96:99], %[ea] offset:24576\n\t"
    "v_mfma_f32_16x16x32_bf16 v[84:87], v[204:207], v[104:107], v[84:87]\n\t"
    "ds_read_b128 v[100:103], %[ea] offset:26624\n\t"
    "v_mfma_f32_16x16x32_bf16 v[88:91], v[200:203], v[108:111], v[88:91]\n\t"
    "v_mfma_f32_16x16x32_bf16 v[92:95], v[204:207], v[108:111], v[92:95]\n\t"
    "s_waitcnt lgkmcnt(4)\n\t"
    "v_mfma_f32_16x16x32_bf16 v[80:83], v[208:211], v[112:115], v[80:83]\n\t"
    "ds_read_b128 v[104:107], %[da] offset:24576\n\t"
    "v_mfma_f32_16x16x32_bf16 v[84:87], v[212:215], v[112:115], v[84:87]\n\t"
    "ds_read_b128 v[108:111], %[da] offset:26624\n\t"
    "v_mfma_f32_16x16x32_bf16 v[88:91], v[208:211], v[116:119], v[88:91]\n\t"
    "v_mfma_f32_16x16x32_bf16 v[92:95], v[212:215], v[116:119], v[92:95]\n\t"
    "s_cmp_eq_u32 %[ps], 0\n\t"
    "s_cbranch_scc1 10f\n\t"
    "global_load_dword %[sv], %[dv], %[db] sc1\n\t"
    "10:\n\t"
    "s_waitcnt lgkmcnt(4)\n\t"
    "v_mfma_f32_16x16x32_bf16 v[80:83], v[216:219], v[120:123], v[80:83]\n\t"
    "ds_read_b128 v[112:115], %[ea] offset:28672\n\t"
    "v_mfma_f32_16x16x32_bf16 v[84:87], v[220:223], v[120:123], v[84:87]\n\t"
    "ds_read_b128 v[116:119], %[ea] offset:30720\n\t"
    "v_mfma_f32_16x16x32_bf16 v[88:91], v[216:219], v[124:127], v[88:91]\n\t"
    "v_mfma_f32_16x16x32_bf16 v[92:95], v[220:223], v[124:127], v[92:95]\n\t"
    "s_waitcnt lgkmcnt(4)\n\t"
    "v_mfma_f32_16x16x32_bf16 v[80:83], v[224:227], v[96:99], v[80:83]\n\t"
    "ds_read_b128 v[120:123], %[da] offset:28672\n\t"
    "v_mfma_f32_16x16x32_bf16 v[84:87], v[228:231], v[96:99], v[84:87]\n\t"
    "ds_read_b128 v[124:127], %[da] offset:30720\n\t"
    "v_mfma_f32_16x16x32_bf16 v[88:91], v[224:227], v[100:103], v[88:91]\n\t"
    "v_mfma_f32_16x16x32_bf16 v[92:95], v[228:231], v[100:103], v[92:95]\n\t"
    "s_waitcnt lgkmcnt(4)\n\t"
    "v_mfma_f32_16x16x32_bf16 v[80:83], v[232:235], v[104:107], v[80:83]\n\t"
    "v_mfma_f32_16x16x32_bf16 v[84:87], v[236:239], v[104:107], v[84:87]\n\t"
    "v_mfma_f32_16x16x32_bf16 v[88:91], v[232:235], v[108:111], v[88:91]\n\t"
    "v_mfma_f32_16x16x32_bf16 v[92:95], v[236:239], v[108:111], v[92:95]\n\t"
    "s_waitcnt lgkmcnt(2)\n\t"
    "v_mfma_f32_16x16x32_bf16 v[80:83], v[240:243], v[112:115], v[80:83]\n\t"
    "v_mfma_f32_16x16x32_bf16 v[84:87], v[244:247], v[112:115], v[84:87]\n\t"
    "v_mfma_f32_16x16x32_bf16 v[88:91], v[240:243], v[116:119], v[88:91]\n\t"
    "v_mfma_f32_16x16x32_bf16 v[92:95], v[244:247], v[116:119], v[92:95]\n\t"
    "s_waitcnt lgkmcnt(0)\n\t"
    "v_mfma_f32_16x16x32_bf16 v[80:83], v[248:251], v[120:123], v[80:83]\n\t"
    "v_mfma_f32_16x16x32_bf16 v[84:87], v[252:255], v[120:123], v[84:87]\n\t"
    "v_mfma_f32_16x16x32_bf16 v[88:91], v[248:251], v[124:127], v[88:91]\n\t"
    "v_mfma_f32_16x16x32_bf16 v[92:95], v[252:255], v[124:127], v[92:95]\n\t"
    "s_mov_b32 %[got], 0\n\t"
    "v_mov_b32 v76, %[fa]\n\t"
    "v_mov_b32 v77, %[fv]\n\t"
    "v_add_u32 v78, 1, v77\n\t"
    "v_mov_b32 v79, %[ga]\n\t"
    "ds_write_b128 %[rw], v[80:83] offset:0\n\t"
    "ds_write_b128 %[rw], v[84:87] offset:1024\n\t"
    "ds_write_b128 %[rw], v[88:91] offset:2048\n\t"
    "ds_write_b128 %[rw], v[92:95] offset:3072\n\t"
    "ds_write_b32 v76, v77\n\t"
    "s_cmp_eq_u32 %[ps], 0\n\t"
    "s_cbranch_scc1 9f\n\t"
    "s_waitcnt vmcnt(0)\n\t"
    "v_mad_u32_u24 v76, %[sv], %[mu], %[ad]\n\t"
    "v_cmp_ge_i32 vcc, %[th], v76\n\t"
    "s_cbranch_vccnz 9f\n\t"
    "ds_write2_b32 v79, v78, v78 offset1:1\n\t"
    "s_add_u32 m0, %[lb], 0\n\t"
    "s_nop 0\n\t"
    "global_load_lds_dwordx4 %[av0], %[s0] sc1\n\t"
    "s_add_u32 m0, %[lb], 4096\n\t"
    "s_nop 0\n\t"
    "global_load_lds_dwordx4 %[av2], %[s0] sc1\n\t"
    "s_add_u32 m0, %[lb], 8192\n\t"
    "s_nop 0\n\t"
    "global_load_lds_dwordx4 %[av4], %[s0] sc1\n\t"
    "s_add_u32 m0, %[lb], 12288\n\t"
    "s_nop 0\n\t"
    "global_load_lds_dwordx4 %[av6], %[s0] sc1\n\t"
    "s_add_u32 m0, %[lb], 16384\n\t"
    "s_nop 0\n\t"
    "global_load_lds_dwordx4 %[av0], %[s1] sc1\n\t"
    "s_add_u32 m0, %[lb], 20480\n\t"
    "s_nop 0\n\t"
    "global_load_lds_dwordx4 %[av2], %[s1] sc1\n\t"
    "s_add_u32 m0, %[lb], 24576\n\t"
    "s_nop 0\n\t"
    "global_load_lds_dwordx4 %[av4], %[s1] sc1\n\t"
    "s_add_u32 m0, %[lb], 28672\n\t"
    "s_nop 0\n\t"
    "global_load_lds_dwordx4 %[av6], %[s1] sc1\n\t"
    "s_mov_b32 %[got], 1\n\t"
    "9:"
    : [sv] "+&v"(sv), [got] "=&s"(got)
    : [ea] "v"(ea), [da] "v"(da), [x] "s"(x), "{v[128:159]}"(W[0]), "{v[160:191]}"(W[1]), "{v[192:223]}"(W[2]), "{v[224:255]}"(W[3]), [lb] "s"(lb), [s0] "s"(s0), [s1] "s"(s1), [av0] "v"(av[0]), [av1] "v"(av[1]), [av2] "v"(av[2]), [av3] "v"(av[3]), [av4] "v"(av[4]), [av5] "v"(av[5]), [av6] "v"(av[6]), [av7] "v"(av[7]), [ps] "s"(ps), [dv] "v"(dv), [db] "s"(db), [rw] "v"(rw), [fa] "s"(fa), [fv] "s"(fv), [ga] "s"(ga), [mu] "s"(mu), [ad] "v"(ad), [th] "s"(th)
    : "v96", "v97", "v98", "v99", "v100", "v101", "v102", "v103", "v104", "v105", "v106", "v107", "v108", "v109", "v110", "v111", "v112", "v113", "v114", "v115", "v116", "v117", "v118", "v119", "v120", "v121", "v122", "v123", "v124", "v125", "v126", "v127", "v76", "v77", "v78", "v79", "v80", "v81", "v82", "v83", "v84", "v85", "v86", "v87", "v88", "v89", "v90", "v91", "v92", "v93", "v94", "v95", "vcc", "scc", "m0", "memory");
}
__device__ __forceinline__ void lean_dm1(const u32x32 (&W)[4], unsigned ea, unsigned da, unsigned x, unsigned lb, const char* s0, const char* s1, const unsigned (&av)[8], unsigned ps, unsigned dv, const char* db, unsigned rw, unsigned fa, unsigned fv,
    unsigned ga, unsigned mu, unsigned ad, int th, unsigned& sv, unsigned& got) {
  asm volatile("s_setprio 2\n\t"
    "ds_read_b128 v[96:99], %[ea] offset:0\n\t"
    "ds_read_b128 v[100:103], %[ea] offset:2048\n\t"
    "ds_read_b128 v[104:107], %[da] offset:0\n\t"
    "ds_read_b128 v[108:111], %[da] offset:2048\n\t"
    "ds_read_b128 v[112:115], %[ea] offset:4096\n\t"
    "ds_read_b128 v[116:119], %[ea] offset:6144\n\t"
    "s_waitcnt lgkmcnt(4)\n\t"
    "v_mfma_f32_16x16x32_bf16 v[80:83], v[128:131], v[96:99], 0\n\t"
    "ds_read_b128 v[120:123], %[da] offset:4096\n\t"
    "s_add_u32 m0, %[lb], 0\n\t"
    "v_mfma_f32_16x16x32_bf16 v[84:87], v[132:135], v[96:99], 0\n\t"
    "ds_read_b128 v[124:127], %[da] offset:6144\n\t"
    "global_load_lds_dwordx4 %[av0], %[s0] sc1\n\t"
    "v_mfma_f32_16x16x32_bf16 v[88:91], v[128:131], v[100:103], 0\n\t"
    "v_mfma_f32_16x16x32_bf16 v[92:95], v[132:135], v[100:103], 0\n\t"
    "s_waitcnt vmcnt(5)\n\t"
    "s_barrier\n\t"
    "s_waitcnt lgkmcnt(4)\n\t"
    "v_mfma_f32_16x16x32_bf16 v[80:83], v[136:139], v[104:107], v[80:83]\n\t"
    "ds_read_b128 v[96:99], %[ea] offset:8192\n\t"
    "s_add_u32 m0, %[lb], 4096\n\t"
    "v_mfma_f32_16x16x32_bf16 v[84:87], v[140:143], v[104:107], v[84:87]\n\t"
    "ds_read_b128 v[100:103], %[ea] offset:10240\n\t"
    "global_load_lds_dwordx4 %[av2], %[s0] sc1\n\t"
    "v_mfma_f32_16x16x32_bf16 v[88:91], v[136:139], v[108:111], v[88:91]\n\t"
    "v_mfma_f32_16x16x32_bf16 v[92:95], v[140:143], v[108:111], v[92:95]\n\t"
    "s_waitcnt lgkmcnt(4)\n\t"
    "v_mfma_f32_16x16x32_bf16 v[80:83], v[144:147], v[112:115], v[80:83]\n\t"
    "ds_read_b128 v[104:107], %[da] offset:8192\n\t"
    "s_add_u32 m0, %[lb], 8192\n\t"
    "v_mfma_f32_16x16x32_bf16 v[84:87], v[148:151], v[112:115], v[84:87]\n\t"
    "ds_read_b128 v[108:111], %[da] offset:10240\n\t"
    "global_load_lds_dwordx4 %[av4], %[s0] sc1\n\t"
    "v_mfma_f32_16x16x32_bf16 v[88:91], v[144:147], v[116:119], v[88:91]\n\t"
    "v_mfma_f32_16x16x32_bf16 v[92:95], v[148:151], v[116:119], v[92:95]\n\t"
    "s_waitcnt lgkmcnt(4)\n\t"
    "v_mfma_f32_16x16x32_bf16 v[80:83], v[152:155], v[120:123], v[80:83]\n\t"
    "ds_read_b128 v[112:115], %[ea] offset:12288\n\t"
    "s_add_u32 m0, %[lb], 12288\n\t"
    "v_mfma_f32_16x16x32_bf16 v[84:87], v[156:159], v[120:123], v[84:87]\n\t"
    "ds_read_b128 v[116:119], %[ea] offset:14336\n\t"
    "global_load_lds_dwordx4 %[av6], %[s0] sc1\n\t"
    "v_mfma_f32_16x16x32_bf16 v[88:91], v[152:155], v[124:127], v[88:91]\n\t"
    "v_mfma_f32_16x16x32_bf16 v[92:95], v[156:159], v[124:127], v[92:95]\n\t"
    "s_waitcnt lgkmcnt(4)\n\t"
    "v_mfma_f32_16x16x32_bf16 v[80:83], v[160:163], v[96:99], v[80:83]\n\t"
    "ds_read_b128 v[120:123], %[da] offset:12288\n\t"
    "s_add_u32 m0, %[lb], 16384\n\t"
    "v_mfma_f32_16x16x32_bf16 v[84:87], v[164:167], v[96:99], v[84:87]\n\t"
    "ds_read_b128 v[124:127], %[da] offset:14336\n\t"
    "global_load_lds_dwordx4 %[av0], %[s1] sc1\n\t"
    "v_mfma_f32_16x16x32_bf16 v[88:91], v[160:163], v[100:103], v[88:91]\n\t"
    "v_mfma_f32_16x16x32_bf16 v[92:95], v[164:167], v[100:103], v[92:95]\n\t"
    "s_waitcnt vmcnt(7)\n\t"
    "s_barrier\n\t"
    "s_waitcnt lgkmcnt(4)\n\t"
    "v_mfma_f32_16x16x32_bf16 v[80:83], v[168:171], v[104:107], v[80:83]\n\t"
    "ds_read_b128 v[96:99], %[ea] offset:16384\n\t"
    "s_add_u32 m0, %[lb], 20480\n\t"
    "v_mfma_f32_16x16x32_bf16 v[84:87], v[172:175], v[104:107], v[84:87]\n\t"
    "ds_read_b128 v[100:103], %[ea] offset:18432\n\t"
    "global_load_lds_dwordx4 %[av2], %[s1] sc1\n\t"
    "v_mfma_f32_16x16x32_bf16 v[88:91], v[168:171], v[108:111], v[88:91]\n\t"
    "v_mfma_f32_16x16x32_bf16 v[92:95], v[172:175], v[108:111], v[92:95]\n\t"
    "s_waitcnt lgkmcnt(4)\n\t"
    "v_mfma_f32_16x16x32_bf16 v[80:83], v[176:179], v[112:115], v[80:83]\n\t"
    "ds_read_b128 v[104:107], %[da] offset:16384\n\t"
    "s_add_u32 m0, %[lb], 24576\n\t"
    "v_mfma_f32_16x16x32_bf16 v[84:87], v[180:183], v[112:115], v[84:87]\n\t"
    "ds_read_b128 v[108:111], %[da] offset:18432\n\t"
    "global_load_lds_dwordx4 %[av4], %[s1] sc1\n\t"
    "v_mfma_f32_16x16x32_bf16 v[88:91], v[176:179], v[116:119], v[88:91]\n\t"
    "v_mfma_f32_16x16x32_bf16 v[92:95], v[180:183], v[116:119], v[92:95]\n\t"
    "s_waitcnt lgkmcnt(4)\n\t"
    "v_mfma_f32_16x16x32_bf16 v[80:83], v[184:187], v[120:123], v[80:83]\n\t"
    "ds_read_b128 v[112:115], %[ea] offset:20480\n\t"
    "s_add_u32 m0, %[lb], 28672\n\t"
    "v_mfma_f32_16x16x32_bf16 v[84:87], v[188:191], v[120:123], v[84:87]\n\t"
    "ds_read_b128 v[116:119], %[ea] offset:22528\n\t"
    "global_load_lds_dwordx4 %[av6], %[s1] sc1\n\t"
    "v_mfma_f32_16x16x32_bf16 v[88:91], v[184:187], v[124:127], v[88:91]\n\t"
    "v_mfma_f32_16x16x32_bf16 v[92:95], v[188:191], v[124:127], v[92:95]\n\t"
    "s_waitcnt lgkmcnt(4)\n\t"
    "v_mfma_f32_16x16x32_bf16 v[80:83], v[192:195], v[96:99], v[80:83]\n\t"
    "ds_read_b128 v[120:123], %[da] offset:20480\n\t"
    "v_mfma_f32_16x16x32_bf16 v[84:87], v[196:199], v[96:99], v[84:87]\n\t"
    "ds_read_b128 v[124:127], %[da] offset:22528\n\t"
    "v_mfma_f32_16x16x32_bf16 v[88:91], v[192:195], v[100:103], v[88:91]\n\t"
    "v_mfma_f32_16x16x32_bf16 v[92:95], v[196:199], v[100:103], v[92:95]\n\t"
    "s_waitcnt vmcnt(8)\n\t"
    "s_barrier\n\t"
    "s_waitcnt lgkmcnt(4)\n\t"
    "v_mfma_f32_16x16x32_bf16 v[80:83], v[200:203], v[104:107], v[80:83]\n\t"
    "ds_read_b128 v[96:99], %[ea] offset:24576\n\t"
    "v_mfma_f32_16x16x32_bf16 v[84:87], v[204:207], v[104:107], v[84:87]\n\t"
    "ds_read_b128 v[100:103], %[ea] offset:26624\n\t"
    "v_mfma_f32_16x16x32_bf16 v[88:91], v[200:203], v[108:111], v[88:91]\n\t"
    "v_mfma_f32_16x16x32_bf16 v[92:95], v[204:207], v[108:111], v[92:95]\n\t"
    "s_waitcnt lgkmcnt(4)\n\t"
    "v_mfma_f32_16x16x32_bf16 v[80:83], v[208:211], v[112:115], v[80:83]\n\t"
    "ds_read_b128 v[104:107], %[da] offset:24576\n\t"
    "v_mfma_f32_16x16x32_bf16 v[84:87], v[212:215], v[112:115], v[84:87]\n\t"
    "ds_read_b128 v[108:111], %[da] offset:26624\n\t"
    "v_mfma_f32_16x16x32_bf16 v[88:91], v[208:211], v[116:119], v[88:91]\n\t"
    "v_mfma_f32_16x16x32_bf16 v[92:95], v[212:215], v[116:119], v[92:95]\n\t"
    "s_cmp_eq_u32 %[ps], 0\n\t"
    "s_cbranch_scc1 10f\n\t"
    "global_load_dword %[sv], %[dv], %[db] sc1\n\t"
    "10:\n\t"
    "s_waitcnt lgkmcnt(4)\n\t"
    "v_mfma_f32_16x16x32_bf16 v[80:83], v[216:219], v[120:123], v[80:83]\n\t"
    "ds_read_b128 v[112:115], %[ea] offset:28672\n\t"
    "v_mfma_f32_16x16x32_bf16 v[84:87], v[220:223], v[120:123], v[84:87]\n\t"
    "ds_read_b128 v[116:119], %[ea] offset:30720\n\t"
    "v_mfma_f32_16x16x32_bf16 v[88:91], v[216:219], v[124:127], v[88:91]\n\t"
    "v_mfma_f32_16x16x32_bf16 v[92:95], v[220:223], v[124:127], v[92:95]\n\t"
    "s_waitcnt lgkmcnt(4)\n\t"
    "v_mfma_f32_16x16x32_bf16 v[80:83], v[224:227], v[96:99], v[80:83]\n\t"
    "ds_read_b128 v[120:123], %[da] offset:28672\n\t"
    "v_mfma_f32_16x16x32_bf16 v[84:87], v[228:231], v[96:99], v[84:87]\n\t"
    "ds_read_b128 v[124:127], %[da] offset:30720\n\t"
    "v_mfma_f32_16x16x32_bf16 v[88:91], v[224:227], v[100:103], v[88:91]\n\t"
    "v_mfma_f32_16x16x32_bf16 v[92:95], v[228:231], v[100:103], v[92:95]\n\t"
    "s_waitcnt lgkmcnt(4)\n\t"
    "v_mfma_f32_16x16x32_bf16 v[80:83], v[232:235], v[104:107], v[80:83]\n\t"
    "v_mfma_f32_16x16x32_bf16 v[84:87], v[236:239], v[104:107], v[84:87]\n\t"
    "v_mfma_f32_16x16x32_bf16 v[88:91], v[232:235], v[108:111], v[88:91]\n\t"
    "v_mfma_f32_16x16x32_bf16 v[92:95], v[236:239], v[108:111], v[92:95]\n\t"
    "s_waitcnt lgkmcnt(2)\n\t"
    "v_mfma_f32_16x16x32_bf16 v[80:83], v[240:243], v[112:115], v[80:83]\n\t"
    "v_mfma_f32_16x16x32_bf16 v[84:87], v[244:247], v[112:115], v[84:87]\n\t"
    "v_mfma_f32_16x16x32_bf16 v[88:91], v[240:243], v[116:119], v[88:91]\n\t"
    "v_mfma_f32_16x16x32_bf16 v[92:95], v[244:247], v[116:119], v[92:95]\n\t"
    "s_waitcnt lgkmcnt(0)\n\t"
    "v_mfma_f32_16x16x32_bf16 v[80:83], v[248:251], v[120:123], v[80:83]\n\t"
    "v_mfma_f32_16x16x32_bf16 v[84:87], v[252:255], v[120:123], v[84:87]\n\t"
    "v_mfma_f32_16x16x32_bf16 v[88:91], v[248:251], v[124:127], v[88:91]\n\t"
    "v_mfma_f32_16x16x32_bf16 v[92:95], v[252:255], v[124:127], v[92:95]\n\t"
    "s_mov_b32 %[got], 0\n\t"
    "v_mov_b32 v76, %[fa]\n\t"
    "v_mov_b32 v77, %[fv]\n\t"
    "v_add_u32 v78, 1, v77\n\t"
    "v_mov_b32 v79, %[ga]\n\t"
    "ds_write_b128 %[rw], v[80:83] offset:0\n\t"
    "ds_write_b128 %[rw], v[84:87] offset:1024\n\t"
    "ds_write_b128 %[rw], v[88:91] offset:2048\n\t"
    "ds_write_b128 %[rw], v[92:95] offset:3072\n\t"
    "ds_write_b32 v76, v77\n\t"
    "s_cmp_eq_u32 %[ps], 0\n\t"
    "s_cbranch_scc1 9f\n\t"
    "s_waitcnt vmcnt(0)\n\t"
    "9:"
    : [sv] "+&v"(sv), [got] "=&s"(got)
    : [ea] "v"(ea), [da] "v"(da), [x] "s"(x), "{v[128:159]}"(W[0]), "{v[160:191]}"(W[1]), "{v[192:223]}"(W[2]), "{v[224:255]}"(W[3]), [lb] "s"(lb), [s0] "s"(s0), [s1] "s"(s1), [av0] "v"(av[0]), [av1] "v"(av[1]), [av2] "v"(av[2]), [av3] "v"(av[3]), [av4] "v"(av[4]), [av5] "v"(av[5]), [av6] "v"(av[6]), [av7] "v"(av[7]), [ps] "s"(ps), [dv] "v"(dv), [db] "s"(db), [rw] "v"(rw), [fa] "s"(fa), [fv] "s"(fv), [ga] "s"(ga), [mu] "s"(mu), [ad] "v"(ad), [th] "s"(th)
    : "v96", "v97", "v98", "v99", "v100", "v101", "v102", "v103", "v104", "v105", "v106", "v107", "v108", "v109", "v110", "v111", "v112", "v113", "v114", "v115", "v116", "v117", "v118", "v119", "v120", "v121", "v122", "v123", "v124", "v125", "v126", "v127", "v76", "v77", "v78", "v79", "v80", "v81", "v82", "v83", "v84", "v85", "v86", "v87", "v88", "v89", "v90", "v91", "v92", "v93", "v94", "v95", "vcc", "scc", "m0", "memory");
}
__device__ __forceinline__ void lean_st0(const u32x32 (&W)[4], unsigned ea, unsigned da, unsigned x, u32x16& pd, unsigned pv, unsigned cn, const char* pg) {
  unsigned st;
  asm volatile("ds_read_b128 v[96:99], %[ea] offset:0\n\t"
    "ds_read_b128 v[100:103], %[ea] offset:2048\n\t"
    "ds_read_b128 v[104:107], %[da] offset:0\n\t"
    "ds_read_b128 v[108:111], %[da] offset:2048\n\t"
    "ds_read_b128 v[112:115], %[ea] offset:4096\n\t"
    "ds_read_b128 v[116:119], %[ea] offset:6144\n\t"
    "s_waitcnt lgkmcnt(4)\n\t"
    "v_mfma_f32_16x16x32_bf16 v[80:83], v[128:131], v[96:99], 0\n\t"
    "ds_read_b128 v[120:123], %[da] offset:4096\n\t"
    "v_mfma_f32_16x16x32_bf16 v[84:87], v[132:135], v[96:99], 0\n\t"
    "ds_read_b128 v[124:127], %[da] offset:6144\n\t"
    "v_mfma_f32_16x16x32_bf16 v[88:91], v[128:131], v[100:103], 0\n\t"
    "v_mfma_f32_16x16x32_bf16 v[92:95], v[132:135], v[100:103], 0\n\t"
    "s_barrier\n\t"
    "s_bitcmp1_b32 %[x], 2\n\t"
    "s_cbranch_scc1 7f\n\t"
    "8:\n\t"
    "s_waitcnt lgkmcnt(4)\n\t"
    "v_mfma_f32_16x16x32_bf16 v[80:83], v[136:139], v[104:107], v[80:83]\n\t"
    "ds_read_b128 v[96:99], %[ea] offset:8192\n\t"
    "v_mfma_f32_16x16x32_bf16 v[84:87], v[140:143], v[104:107], v[84:87]\n\t"
    "ds_read_b128 v[100:103], %[ea] offset:10240\n\t"
    "v_mfma_f32_16x16x32_bf16 v[88:91], v[136:139], v[108:111], v[88:91]\n\t"
    "v_mfma_f32_16x16x32_bf16 v[92:95], v[140:143], v[108:111], v[92:95]\n\t"
    "s_waitcnt lgkmcnt(4)\n\t"
    "v_mfma_f32_16x16x32_bf16 v[80:83], v[144:147], v[112:115], v[80:83]\n\t"
    "ds_read_b128 v[104:107], %[da] offset:8192\n\t"
    "v_mfma_f32_16x16x32_bf16 v[84:87], v[148:151], v[112:115], v[84:87]\n\t"
    "ds_read_b128 v[108:111], %[da] offset:10240\n\t"
    "v_mfma_f32_16x16x32_bf16 v[88:91], v[144:147], v[116:119], v[88:91]\n\t"
    "v_mfma_f32_16x16x32_bf16 v[92:95], v[148:151], v[116:119], v[92:95]\n\t"
    "s_waitcnt lgkmcnt(4)\n\t"
    "v_mfma_f32_16x16x32_bf16 v[80:83], v[152:155], v[120:123], v[80:83]\n\t"
    "ds_read_b128 v[112:115], %[ea] offset:12288\n\t"
    "v_mfma_f32_16x16x32_bf16 v[84:87], v[156:159], v[120:123], v[84:87]\n\t"
    "ds_read_b128 v[116:119], %[ea] offset:14336\n\t"
    "v_mfma_f32_16x16x32_bf16 v[88:91], v[152:155], v[124:127], v[88:91]\n\t"
    "v_mfma_f32_16x16x32_bf16 v[92:95], v[156:159], v[124:127], v[92:95]\n\t"
    "s_waitcnt lgkmcnt(4)\n\t"
    "v_mfma_f32_16x16x32_bf16 v[80:83], v[160:163], v[96:99], v[80:83]\n\t"
    "ds_read_b128 v[120:123], %[da] offset:12288\n\t"
    "v_mfma_f32_16x16x32_bf16 v[84:87], v[164:167], v[96:99], v[84:87]\n\t"
    "ds_read_b128 v[124:127], %[da] offset:14336\n\t"
    "v_mfma_f32_16x16x32_bf16 v[88:91], v[160:163], v[100:103], v[88:91]\n\t"
    "v_mfma_f32_16x16x32_bf16 v[92:95], v[164:167], v[100:103], v[92:95]\n\t"
    "s_barrier\n\t"
    "s_waitcnt lgkmcnt(4)\n\t"
    "v_mfma_f32_16x16x32_bf16 v[80:83], v[168:171], v[104:107], v[80:83]\n\t"
    "ds_read_b128 v[96:99], %[ea] offset:16384\n\t"
    "v_mfma_f32_16x16x32_bf16 v[84:87], v[172:175], v[104:107], v[84:87]\n\t"
    "ds_read_b128 v[100:103], %[ea] offset:18432\n\t"
    "v_mfma_f32_16x16x32_bf16 v[88:91], v[168:171], v[108:111], v[88:91]\n\t"
    "v_mfma_f32_16x16x32_bf16 v[92:95], v[172:175], v[108:111], v[92:95]\n\t"
    "s_waitcnt lgkmcnt(4)\n\t"
    "v_mfma_f32_16x16x32_bf16 v[80:83], v[176:179], v[112:115], v[80:83]\n\t"
    "ds_read_b128 v[104:107], %[da] offset:16384\n\t"
    "v_mfma_f32_16x16x32_bf16 v[84:87], v[180:183], v[112:115], v[84:87]\n\t"
    "ds_read_b128 v[108:111], %[da] offset:18432\n\t"
    "v_mfma_f32_16x16x32_bf16 v[88:91], v[176:179], v[116:119], v[88:91]\n\t"
    "v_mfma_f32_16x16x32_bf16 v[92:95], v[180:183], v[116:119], v[92:95]\n\t"
    "s_waitcnt lgkmcnt(4)\n\t"
    "v_mfma_f32_16x16x32_bf16 v[80:83], v[184:187], v[120:123], v[80:83]\n\t"
    "ds_read_b128 v[112:115], %[ea] offset:20480\n\t"
    "v_mfma_f32_16x16x32_bf16 v[84:87], v[188:191], v[120:123], v[84:87]\n\t"
    "ds_read_b128 v[116:119], %[ea] offset:22528\n\t"
    "v_mfma_f32_16x16x32_bf16 v[88:91], v[184:187], v[124:127], v[88:91]\n\t"
    "v_mfma_f32_16x16x32_bf16 v[92:95], v[188:191], v[124:127], v[92:95]\n\t"
    "s_waitcnt lgkmcnt(4)\n\t"
    "v_mfma_f32_16x16x32_bf16 v[80:83], v[192:195], v[96:99], v[80:83]\n\t"
    "ds_read_b128 v[120:123], %[da] offset:20480\n\t"
    "v_mfma_f32_16x16x32_bf16 v[84:87], v[196:199], v[96:99], v[84:87]\n\t"
    "ds_read_b128 v[124:127], %[da] offset:22528\n\t"
    "v_mfma_f32_16x16x32_bf16 v[88:91], v[192:195], v[100:103], v[88:91]\n\t"
    "v_mfma_f32_16x16x32_bf16 v[92:95], v[196:199], v[100:103], v[92:95]\n\t"
    "s_barrier\n\t"
    "s_and_b32 %[st], %[x], 3\n\t"
    "s_cbranch_scc1 3f\n\t"
    "4:\n\t"
    "s_waitcnt lgkmcnt(4)\n\t"
    "v_mfma_f32_16x16x32_bf16 v[80:83], v[200:203], v[104:107], v[80:83]\n\t"
    "ds_read_b128 v[96:99], %[ea] offset:24576\n\t"
    "v_mfma_f32_16x16x32_bf16 v[84:87], v[204:207], v[104:107], v[84:87]\n\t"
    "ds_read_b128 v[100:103], %[ea] offset:26624\n\t"
    "v_mfma_f32_16x16x32_bf16 v[88:91], v[200:203], v[108:111], v[88:91]\n\t"
    "v_mfma_f32_16x16x32_bf16 v[92:95], v[204:207], v[108:111], v[92:95]\n\t"
    "s_waitcnt lgkmcnt(4)\n\t"
    "v_mfma_f32_16x16x32_bf16 v[80:83], v[208:211], v[112:115], v[80:83]\n\t"
    "ds_read_b128 v[104:107], %[da] offset:24576\n\t"
    "v_mfma_f32_16x16x32_bf16 v[84:87], v[212:215], v[112:115], v[84:87]\n\t"
    "ds_read_b128 v[108:111], %[da] offset:26624\n\t"
    "v_mfma_f32_16x16x32_bf16 v[88:91], v[208:211], v[116:119], v[88:91]\n\t"
    "v_mfma_f32_16x16x32_bf16 v[92:95], v[212:215], v[116:119], v[92:95]\n\t"
    "s_waitcnt lgkmcnt(4)\n\t"
    "v_mfma_f32_16x16x32_bf16 v[80:83], v[216:219], v[120:123], v[80:83]\n\t"
    "ds_read_b128 v[112:115], %[ea] offset:28672\n\t"
    "v_mfma_f32_16x16x32_bf16 v[84:87], v[220:223], v[120:123], v[84:87]\n\t"
    "ds_read_b128 v[116:119], %[ea] offset:30720\n\t"
    "v_mfma_f32_16x16x32_bf16 v[88:91], v[216:219], v[124:127], v[88:91]\n\t"
    "v_mfma_f32_16x16x32_bf16 v[92:95], v[220:223], v[124:127], v[92:95]\n\t"
    "s_waitcnt lgkmcnt(4)\n\t"
    "v_mfma_f32_16x16x32_bf16 v[80:83], v[224:227], v[96:99], v[80:83]\n\t"
    "ds_read_b128 v[120:123], %[da] offset:28672\n\t"
    "v_mfma_f32_16x16x32_bf16 v[84:87], v[228:231], v[96:99], v[84:87]\n\t"
    "ds_read_b128 v[124:127], %[da] offset:30720\n\t"
    "v_mfma_f32_16x16x32_bf16 v[88:91], v[224:227], v[100:103], v[88:91]\n\t"
    "v_mfma_f32_16x16x32_bf16 v[92:95], v[228:231], v[100:103], v[92:95]\n\t"
    "s_waitcnt lgkmcnt(4)\n\t"
    "v_mfma_f32_16x16x32_bf16 v[80:83], v[232:235], v[104:107], v[80:83]\n\t"
    "v_mfma_f32_16x16x32_bf16 v[84:87], v[236:239], v[104:107], v[84:87]\n\t"
    "v_mfma_f32_16x16x32_bf16 v[88:91], v[232:235], v[108:111], v[88:91]\n\t"
    "v_mfma_f32_16x16x32_bf16 v[92:95], v[236:239], v[108:111], v[92:95]\n\t"
    "s_waitcnt lgkmcnt(2)\n\t"
    "v_mfma_f32_16x16x32_bf16 v[80:83], v[240:243], v[112:115], v[80:83]\n\t"
    "v_mfma_f32_16x16x32_bf16 v[84:87], v[244:247], v[112:115], v[84:87]\n\t"
    "v_mfma_f32_16x16x32_bf16 v[88:91], v[240:243], v[116:119], v[88:91]\n\t"
    "v_mfma_f32_16x16x32_bf16 v[92:95], v[244:247], v[116:119], v[92:95]\n\t"
    "s_waitcnt lgkmcnt(0)\n\t"
    "v_mfma_f32_16x16x32_bf16 v[80:83], v[248:251], v[120:123], v[80:83]\n\t"
    "v_mfma_f32_16x16x32_bf16 v[84:87], v[252:255], v[120:123], v[84:87]\n\t"
    "v_mfma_f32_16x16x32_bf16 v[88:91], v[248:251], v[124:127], v[88:91]\n\t"
    "v_mfma_f32_16x16x32_bf16 v[92:95], v[252:255], v[124:127], v[92:95]\n\t"
    "s_nop 6\n\t"
    "s_branch 9f\n\t"
    "3:\n\t"
    "7:\n\t"
    "v_mov_b32 v76, %[cn]\n\t"
    "v_mov_b32 v77, 1\n\t"
    "v_mov_b32 v78, %[pv]\n\t"
    "v_mov_b32 v79, 0\n\t"
    "s_waitcnt vmcnt(0)\n\t"
    "s_mov_b64 exec, 1\n\t"
    "ds_add_rtn_u32 v77, v76, v77\n\t"
    "s_waitcnt lgkmcnt(0)\n\t"
    "v_readfirstlane_b32 %[st], v77\n\t"
    "s_and_b32 %[st], %[st], 3\n\t"
    "s_cmp_lg_u32 %[st], 3\n\t"
    "s_cbranch_scc1 6f\n\t"
    "global_store_dword v79, v78, %[pg] sc1\n\t"
    "6:\n\t"
    "s_mov_b64 exec, -1\n\t"
    "s_bitcmp1_b32 %[x], 2\n\t"
    "s_cbranch_scc1 8b\n\t"
    "s_branch 4b\n\t"
    "9:"
    : "=&{v[80:95]}"(pd), [st] "=&s"(st)
    : [ea] "v"(ea), [da] "v"(da), [x] "s"(x), "{v[128:159]}"(W[0]), "{v[160:191]}"(W[1]), "{v[192:223]}"(W[2]), "{v[224:255]}"(W[3]), [pv] "s"(pv), [cn] "s"(cn), [pg] "s"(pg)
    : "v96", "v97", "v98", "v99", "v100", "v101", "v102", "v103", "v104", "v105", "v106", "v107", "v108", "v109", "v110", "v111", "v112", "v113", "v114", "v115", "v116", "v117", "v118", "v119", "v120", "v121", "v122", "v123", "v124", "v125", "v126", "v127", "v76", "v77", "v78", "v79", "vcc", "scc", "m0", "memory");
}
#define LEAN_QB 1
#define PREK 10
#define NEARLY 16
__device__ __forceinline__ void fin_ld(const u32x16& pd, unsigned rw, unsigned fa, unsigned fv) {
  asm volatile("ds_write_b128 %[rw], v[80:83] offset:0\n\t"
    "ds_write_b128 %[rw], v[84:87] offset:1024\n\t"
    "ds_write_b128 %[rw], v[88:91] offset:2048\n\t"
    "ds_write_b128 %[rw], v[92:95] offset:3072\n\t"
    "ds_write_b32 %[fa], %[fv]"
    :
    : [rw] "v"(rw), "{v[80:95]}"(pd), [fa] "v"(fa), [fv] "v"(fv)
    : "memory");
}
__device__ __forceinline__ void fin_st0(const u32x16& pd, unsigned rw, unsigned fa, unsigned fv, const char* cb, unsigned co0, unsigned co1) {
  asm volatile("1:\n\t"
    "ds_read_b32 v39, %[fa]\n\t"
    "ds_read_b128 v[48:51], %[rw] offset:0\n\t"
    "ds_read_b128 v[52:55], %[rw] offset:1024\n\t"
    "ds_read_b128 v[56:59], %[rw] offset:2048\n\t"
    "ds_read_b128 v[60:63], %[rw] offset:3072\n\t"
    "s_waitcnt lgkmcnt(2)\n\t"
    "v_cmp_ne_u32 vcc, v39, %[fv]\n\t"
    "s_cbranch_vccz 2f\n\t"
    "s_waitcnt lgkmcnt(0)\n\t"
    "s_branch 1b\n\t"
    "2:\n\t"
    "v_add_f32 v48, v48, v80\n\t"
    "v_add_f32 v49, v49, v81\n\t"
    "v_add_f32 v50, v50, v82\n\t"
    "v_add_f32 v51, v51, v83\n\t"
    "v_add_f32 v52, v52, v84\n\t"
    "v_add_f32 v53, v53, v85\n\t"
    "v_add_f32 v54, v54, v86\n\t"
    "v_add_f32 v55, v55, v87\n\t"
    "v_cvt_pk_bf16_f32 v40, v48, v49\n\t"
    "v_cvt_pk_bf16_f32 v41, v50, v51\n\t"
    "v_cvt_pk_bf16_f32 v42, v52, v53\n\t"
    "v_cvt_pk_bf16_f32 v43, v54, v55\n\t"
    "global_store_dwordx4 %[co0], v[40:43], %[cb] sc1\n\t"
    "s_waitcnt lgkmcnt(0)\n\t"
    "v_add_f32 v56, v56, v88\n\t"
    "v_add_f32 v57, v57, v89\n\t"
    "v_add_f32 v58, v58, v90\n\t"
    "v_add_f32 v59, v59, v91\n\t"
    "v_add_f32 v60, v60, v92\n\t"
    "v_add_f32 v61, v61, v93\n\t"
    "v_add_f32 v62, v62, v94\n\t"
    "v_add_f32 v63, v63, v95\n\t"
    "v_cvt_pk_bf16_f32 v44, v56, v57\n\t"
    "v_cvt_pk_bf16_f32 v45, v58, v59\n\t"
    "v_cvt_pk_bf16_f32 v46, v60, v61\n\t"
    "v_cvt_pk_bf16_f32 v47, v62, v63\n\t"
    "global_store_dwordx4 %[co1], v[44:47], %[cb] sc1\n\t"
    "s_nop 1"
    :
    : [rw] "v"(rw), "{v[80:95]}"(pd), [fa] "v"(fa), [fv] "v"(fv), [cb] "s"(cb), [co0] "v"(co0), [co1] "v"(co1)
    : "v39", "v40", "v41", "v42", "v43", "v44", "v45", "v46", "v47", "v48", "v49", "v50", "v51", "v52", "v53", "v54", "v55", "v56", "v57", "v58", "v59", "v60", "v61", "v62", "v63", "vcc", "memory");
}
__device__ __forceinline__ void fin_st1(const u32x16& pd, unsigned rw, unsigned fa, unsigned fv, const char* cb, unsigned co0, unsigned co1) {
  asm volatile("1:\n\t"
    "ds_read_b32 v39, %[fa]\n\t"
    "ds_read_b128 v[48:51], %[rw] offset:0\n\t"
    "ds_read_b128 v[52:55], %[rw] offset:1024\n\t"
    "ds_read_b128 v[56:59], %[rw] offset:2048\n\t"
    "ds_read_b128 v[60:63], %[rw] offset:3072\n\t"
    "s_waitcnt lgkmcnt(2)\n\t"
    "v_cmp_ne_u32 vcc, v39, %[fv]\n\t"
    "s_cbranch_vccz 2f\n\t"
    "s_waitcnt lgkmcnt(0)\n\t"
    "s_branch 1b\n\t"
    "2:\n\t"
    "v_add_f32 v48, v48, v80\n\t"
    "v_add_f32 v49, v49, v81\n\t"
    "v_add_f32 v50, v50, v82\n\t"
    "v_add_f32 v51, v51, v83\n\t"
    "v_add_f32 v52, v52, v84\n\t"
    "v_add_f32 v53, v53, v85\n\t"
    "v_add_f32 v54, v54, v86\n\t"
    "v_add_f32 v55, v55, v87\n\t"
    "s_waitcnt lgkmcnt(0)\n\t"
    "v_add_f32 v56, v56, v88\n\t"
    "v_add_f32 v57, v57, v89\n\t"
    "v_add_f32 v58, v58, v90\n\t"
    "v_add_f32 v59, v59, v91\n\t"
    "v_add_f32 v60, v60, v92\n\t"
    "v_add_f32 v61, v61, v93\n\t"
    "v_add_f32 v62, v62, v94\n\t"
    "v_add_f32 v63, v63, v95\n\t"
    "v_mul_f32 v40, %[nl], v48\n\t"
    "v_mul_f32 v41, %[nl], v49\n\t"
    "v_mul_f32 v42, %[nl], v50\n\t"
    "v_mul_f32 v43, %[nl], v51\n\t"
    "v_mul_f32 v44, %[nl], v56\n\t"
    "v_mul_f32 v45, %[nl], v57\n\t"
    "v_mul_f32 v46, %[nl], v58\n\t"
    "v_mul_f32 v47, %[nl], v59\n\t"
    "v_exp_f32 v40, v40\n\t"
    "v_exp_f32 v41, v41\n\t"
    "v_exp_f32 v42, v42\n\t"
    "v_exp_f32 v43, v43\n\t"
    "v_exp_f32 v44, v44\n\t"
    "v_exp_f32 v45, v45\n\t"
    "v_exp_f32 v46, v46\n\t"
    "v_exp_f32 v47, v47\n\t"
    "v_add_f32 v40, 1.0, v40\n\t"
    "v_add_f32 v41, 1.0, v41\n\t"
    "v_add_f32 v42, 1.0, v42\n\t"
    "v_add_f32 v43, 1.0, v43\n\t"
    "v_add_f32 v44, 1.0, v44\n\t"
    "v_add_f32 v45, 1.0, v45\n\t"
    "v_add_f32 v46, 1.0, v46\n\t"
    "v_add_f32 v47, 1.0, v47\n\t"
    "v_rcp_f32 v40, v40\n\t"
    "v_rcp_f32 v41, v41\n\t"
    "v_rcp_f32 v42, v42\n\t"
    "v_rcp_f32 v43, v43\n\t"
    "v_rcp_f32 v44, v44\n\t"
    "v_rcp_f32 v45, v45\n\t"
    "v_rcp_f32 v46, v46\n\t"
    "v_rcp_f32 v47, v47\n\t"
    "v_mul_f32 v40, v48, v40\n\t"
    "v_mul_f32 v41, v49, v41\n\t"
    "v_mul_f32 v42, v50, v42\n\t"
    "v_mul_f32 v43, v51, v43\n\t"
    "v_mul_f32 v44, v56, v44\n\t"
    "v_mul_f32 v45, v57, v45\n\t"
    "v_mul_f32 v46, v58, v46\n\t"
    "v_mul_f32 v47, v59, v47\n\t"
    "v_mul_f32 v40, v40, v52\n\t"
    "v_mul_f32 v41, v41, v53\n\t"
    "v_mul_f32 v42, v42, v54\n\t"
    "v_mul_f32 v43, v43, v55\n\t"
    "v_mul_f32 v44, v44, v60\n\t"
    "v_mul_f32 v45, v45, v61\n\t"
    "v_mul_f32 v46, v46, v62\n\t"
    "v_mul_f32 v47, v47, v63\n\t"
    "v_cvt_pk_bf16_f32 v48, v40, v41\n\t"
    "v_cvt_pk_bf16_f32 v49, v42, v43\n\t"
    "v_cvt_pk_bf16_f32 v50, v44, v45\n\t"
    "v_cvt_pk_bf16_f32 v51, v46, v47\n\t"
    "s_nop 1\n\t"
    "v_permlane16_swap_b32 v48, v50\n\t"
    "v_permlane16_swap_b32 v49, v51\n\t"
    "global_store_dwordx4 %[co0], v[48:51], %[cb] sc1\n\t"
    "s_nop 1"
    :
    : [rw] "v"(rw), "{v[80:95]}"(pd), [fa] "v"(fa), [fv] "v"(fv), [cb] "s"(cb), [co0] "v"(co0), [nl] "s"(-LOG2E)
    : "v39", "v40", "v41", "v42", "v43", "v44", "v45", "v46", "v47", "v48", "v49", "v50", "v51", "v52", "v53", "v54", "v55", "v56", "v57", "v58", "v59", "v60", "v61", "v62", "v63", "vcc", "memory");
}
__device__ __forceinline__ void fin_st2(const u32x16& pd, unsigned rw, unsigned fa, unsigned fv, const char* cb, unsigned co0, unsigned co1) {
  asm volatile("s_nop 1\n\t"
    "v_cvt_pk_bf16_f32 v40, v80, v81\n\t"
    "v_cvt_pk_bf16_f32 v41, v82, v83\n\t"
    "v_cvt_pk_bf16_f32 v42, v84, v85\n\t"
    "v_cvt_pk_bf16_f32 v43, v86, v87\n\t"
    "global_store_dwordx4 %[co0], v[40:43], %[cb] sc1\n\t"
    "v_cvt_pk_bf16_f32 v44, v88, v89\n\t"
    "v_cvt_pk_bf16_f32 v45, v90, v91\n\t"
    "v_cvt_pk_bf16_f32 v46, v92, v93\n\t"
    "v_cvt_pk_bf16_f32 v47, v94, v95\n\t"
    "global_store_dwordx4 %[co1], v[44:47], %[cb] sc1\n\t"
    "1:\n\t"
    "ds_read_b32 v39, %[fa]\n\t"
    "ds_read_b128 v[48:51], %[rw] offset:0\n\t"
    "ds_read_b128 v[52:55], %[rw] offset:1024\n\t"
    "ds_read_b128 v[56:59], %[rw] offset:2048\n\t"
    "ds_read_b128 v[60:63], %[rw] offset:3072\n\t"
    "s_waitcnt lgkmcnt(2)\n\t"
    "v_cmp_ne_u32 vcc, v39, %[fv]\n\t"
    "s_cbranch_vccz 2f\n\t"
    "s_waitcnt lgkmcnt(0)\n\t"
    "s_branch 1b\n\t"
    "2:\n\t"
    "v_cvt_pk_bf16_f32 v48, v48, v49\n\t"
    "v_cvt_pk_bf16_f32 v49, v50, v51\n\t"
    "v_cvt_pk_bf16_f32 v50, v52, v53\n\t"
    "v_cvt_pk_bf16_f32 v51, v54, v55\n\t"
    "global_store_dwordx4 %[co0], v[48:51], %[cb] offset:2048 sc1\n\t"
    "s_waitcnt lgkmcnt(0)\n\t"
    "v_cvt_pk_bf16_f32 v56, v56, v57\n\t"
    "v_cvt_pk_bf16_f32 v57, v58, v59\n\t"
    "v_cvt_pk_bf16_f32 v58, v60, v61\n\t"
    "v_cvt_pk_bf16_f32 v59, v62, v63\n\t"
    "global_store_dwordx4 %[co1], v[56:59], %[cb] offset:2048 sc1\n\t"
    "s_nop 1"
    :
    : [rw] "v"(rw), "{v[80:95]}"(pd), [fa] "v"(fa), [fv] "v"(fv), [cb] "s"(cb), [co0] "v"(co0), [co1] "v"(co1)
    : "v39", "v40", "v41", "v42", "v43", "v44", "v45", "v46", "v47", "v48", "v49", "v50", "v51", "v52", "v53", "v54", "v55", "v56", "v57", "v58", "v59", "v60", "v61", "v62", "v63", "vcc", "memory");
}
template <int R> __device__ __forceinline__ void gw_load(u32x32& w, unsigned a0, unsigned a1);
template <> __device__ __forceinline__ void gw_load<0>(u32x32& w, unsigned a0, unsigned a1) {
  asm volatile("ds_read_b64_tr_b16 v[128:129], %1 offset:0\n\t"
    "ds_read_b64_tr_b16 v[130:131], %1 offset:512\n\t"
    "ds_read_b64_tr_b16 v[132:133], %2 offset:0\n\t"
    "ds_read_b64_tr_b16 v[134:135], %2 offset:512\n\t"
    "ds_read_b64_tr_b16 v[136:137], %1 offset:4096\n\t"
    "ds_read_b64_tr_b16 v[138:139], %1 offset:4608\n\t"
    "ds_read_b64_tr_b16 v[140:141], %2 offset:4096\n\t"
    "ds_read_b64_tr_b16 v[142:143], %2 offset:4608\n\t"
    "s_waitcnt lgkmcnt(0)\n\t"
    "ds_read_b64_tr_b16 v[144:145], %1 offset:16384\n\t"
    "ds_read_b64_tr_b16 v[146:147], %1 offset:16896\n\t"
    "ds_read_b64_tr_b16 v[148:149], %2 offset:16384\n\t"
    "ds_read_b64_tr_b16 v[150:151], %2 offset:16896\n\t"
    "ds_read_b64_tr_b16 v[152:153], %1 offset:20480\n\t"
    "ds_read_b64_tr_b16 v[154:155], %1 offset:20992\n\t"
    "ds_read_b64_tr_b16 v[156:157], %2 offset:20480\n\t"
    "ds_read_b64_tr_b16 v[158:159], %2 offset:20992\n\t"
    "s_waitcnt lgkmcnt(0)"
    : "=&{v[128:159]}"(w) : "v"(a0), "v"(a1) : "memory");
}
template <> __device__ __forceinline__ void gw_load<1>(u32x32& w, unsigned a0, unsigned a1) {
  asm volatile("ds_read_b64_tr_b16 v[160:161], %1 offset:0\n\t"
    "ds_read_b64_tr_b16 v[162:163], %1 offset:512\n\t"
    "ds_read_b64_tr_b16 v[164:165], %2 offset:0\n\t"
    "ds_read_b64_tr_b16 v[166:167], %2 offset:512\n\t"
    "ds_read_b64_tr_b16 v[168:169], %1 offset:4096\n\t"
    "ds_read_b64_tr_b16 v[170:171], %1 offset:4608\n\t"
    "ds_read_b64_tr_b16 v[172:173], %2 offset:4096\n\t"
    "ds_read_b64_tr_b16 v[174:175], %2 offset:4608\n\t"
    "s_waitcnt lgkmcnt(0)\n\t"
    "ds_read_b64_tr_b16 v[176:177], %1 offset:16384\n\t"
    "ds_read_b64_tr_b16 v[178:179], %1 offset:16896\n\t"
    "ds_read_b64_tr_b16 v[180:181], %2 offset:16384\n\t"
    "ds_read_b64_tr_b16 v[182:183], %2 offset:16896\n\t"
    "ds_read_b64_tr_b16 v[184:185], %1 offset:20480\n\t"
    "ds_read_b64_tr_b16 v[186:187], %1 offset:20992\n\t"
    "ds_read_b64_tr_b16 v[188:189], %2 offset:20480\n\t"
    "ds_read_b64_tr_b16 v[190:191], %2 offset:20992\n\t"
    "s_waitcnt lgkmcnt(0)"
    : "=&{v[160:191]}"(w) : "v"(a0), "v"(a1) : "memory");
}
template <> __device__ __forceinline__ void gw_load<2>(u32x32& w, unsigned a0, unsigned a1) {
  asm volatile("ds_read_b64_tr_b16 v[192:193], %1 offset:0\n\t"
    "ds_read_b64_tr_b16 v[194:195], %1 offset:512\n\t"
    "ds_read_b64_tr_b16 v[196:197], %2 offset:0\n\t"
    "ds_read_b64_tr_b16 v[198:199], %2 offset:512\n\t"
    "ds_read_b64_tr_b16 v[200:201], %1 offset:4096\n\t"
    "ds_read_b64_tr_b16 v[202:203], %1 offset:4608\n\t"
    "ds_read_b64_tr_b16 v[204:205], %2 offset:4096\n\t"
    "ds_read_b64_tr_b16 v[206:207], %2 offset:4608\n\t"
    "s_waitcnt lgkmcnt(0)\n\t"
    "ds_read_b64_tr_b16 v[208:209], %1 offset:16384\n\t"
    "ds_read_b64_tr_b16 v[210:211], %1 offset:16896\n\t"
    "ds_read_b64_tr_b16 v[212:213], %2 offset:16384\n\t"
    "ds_read_b64_tr_b16 v[214:215], %2 offset:16896\n\t"
    "ds_read_b64_tr_b16 v[216:217], %1 offset:20480\n\t"
    "ds_read_b64_tr_b16 v[218:219], %1 offset:20992\n\t"
    "ds_read_b64_tr_b16 v[220:221], %2 offset:20480\n\t"
    "ds_read_b64_tr_b16 v[222:223], %2 offset:20992\n\t"
    "s_waitcnt lgkmcnt(0)"
    : "=&{v[192:223]}"(w) : "v"(a0), "v"(a1) : "memory");
}
template <> __device__ __forceinline__ void gw_load<3>(u32x32& w, unsigned a0, unsigned a1) {
  asm volatile("ds_read_b64_tr_b16 v[224:225], %1 offset:0\n\t"
    "ds_read_b64_tr_b16 v[226:227], %1 offset:512\n\t"
    "ds_read_b64_tr_b16 v[228:229], %2 offset:0\n\t"
    "ds_read_b64_tr_b16 v[230:231], %2 offset:512\n\t"
    "ds_read_b64_tr_b16 v[232:233], %1 offset:4096\n\t"
    "ds_read_b64_tr_b16 v[234:235], %1 offset:4608\n\t"
    "ds_read_b64_tr_b16 v[236:237], %2 offset:4096\n\t"
    "ds_read_b64_tr_b16 v[238:239], %2 offset:4608\n\t"
    "s_waitcnt lgkmcnt(0)\n\t"
    "ds_read_b64_tr_b16 v[240:241], %1 offset:16384\n\t"
    "ds_read_b64_tr_b16 v[242:243], %1 offset:16896\n\t"
    "ds_read_b64_tr_b16 v[244:245], %2 offset:16384\n\t"
    "ds_read_b64_tr_b16 v[246:247], %2 offset:16896\n\t"
    "ds_read_b64_tr_b16 v[248:249], %1 offset:20480\n\t"
    "ds_read_b64_tr_b16 v[250:251], %1 offset:20992\n\t"
    "ds_read_b64_tr_b16 v[252:253], %2 offset:20480\n\t"
    "ds_read_b64_tr_b16 v[254:255], %2 offset:20992\n\t"
    "s_waitcnt lgkmcnt(0)"
    : "=&{v[224:255]}"(w) : "v"(a0), "v"(a1) : "memory");
}
template <int I, int E, class F> __device__ __forceinline__ void sfor(F&& f) { if constexpr (I < E) { f(std::integral_constant<int, I>{}); sfor<I + 1, E>(f); } }
template <int ROLE, int KQ, class F>
__device__ __forceinline__ void wload(char* smem, int wave_id_, const u16* W0, const u16* W1, int ldb, u32x32 (&bw)[4], int rot, F&& hook) {
  constexpr int ABUF = 65536;
  const int tid = opaque_tid(), lane = tid & 63, wave = WAVE_ID;
  const int kq = KQ == 2 ? wave >> 2 : 0, nq = KQ == 2 ? wave & 3 : wave;
  const int g = lane >> 4, r16 = lane & 15, q4 = r16 >> 2, p4 = lane & 3;
  const unsigned ldsb = __builtin_amdgcn_readfirstlane((unsigned)(size_t)(LAS char*)smem);
  {
    const int srow = 8 * wave + (lane >> 3), ch = (lane & 7) ^ bswz(srow);
    const unsigned voff = (unsigned)(srow * ldb + ch * 8) * 2;
    const char* w0 = uni((const char*)W0); const char* w1 = uni((const char*)(ROLE == R_F1 ? W1 : W0 + 64));
    const size_t kb = (size_t)64 * ldb * 2;
    auto wstage = [&](int r) {
      const unsigned ld = ldsb + (r & 1) * ABUF + wave * 1024;
#pragma unroll
      for (int i = 0; i < 8; ++i) {
        const int kbi = KQ == 2 ? (ROLE == R_PART2 ? 0 : (i >> 2) * 8) + ((2 * r + ((i >> 1) & 1)) ^ rot) : 2 * r + (i >> 2);
        const char* src = KQ == 2 ? ((i & 1) ? w1 : w0) + kbi * kb + (ROLE == R_PART2 ? (i >> 2) * 256 : 0) : w0 + kbi * kb + (i & 3) * 128;
        unsigned keep;
        asm volatile("s_mov_b32 %0, m0\n\ts_mov_b32 m0, %3\n\ts_nop 0\n\tglobal_load_lds_dwordx4 %1, %2" WNT_ "\n\ts_mov_b32 m0, %0"
                     : "=&s"(keep) : "v"(voff), "s"(src), "s"(ld + i * 8192) : "memory");
      }
    };
    const int brow = 8 * g + q4, bs = bswz(brow);
    unsigned la[2];
#pragma unroll
    for (int j = 0; j < 2; ++j) {
      int blk, c16, sub;
      if (ROLE == R_F1) { blk = j; c16 = nq * 2 + (p4 >> 1); sub = (p4 & 1) * 8; }
      else { blk = nq >> 1; c16 = (nq & 1) * 4 + p4; sub = j * 8; }
      int set = kq;
      if (ROLE == R_PART2 && TLP) { set = nq >> 1; blk = nq & 1; c16 = kq * 4 + p4; }
      la[j] = (unsigned)((KQ == 2 ? set * 4 + blk : blk) * 8192 + brow * 128 + ((c16 ^ bs) << 4) + sub);
    }
    wstage(0); wstage(1);
    sfor<0, 4>([&](auto r_c) __attribute__((always_inline)) {
      constexpr int r = decltype(r_c)::value;
      if (r < 3) WAIT_V(8); else WAIT_V(0);
      __builtin_amdgcn_s_barrier(); asm volatile("" ::: "memory");
      gw_load<r>(bw[r], ldsb + (r & 1) * ABUF + la[0], ldsb + (r & 1) * ABUF + la[1]);
      BAR();
      if (r + 2 < 4) wstage(r + 2);
      if (r == 2) hook();
    });
  }
}
#define DSR2(d0, d1, a0_, a1_, off) asm volatile("ds_read_b128 %0, %2 offset:%4\n\tds_read_b128 %1, %3 offset:%4" : "=&v"(d0), "=&v"(d1) : "v"(a0_), "v"(a1_), "i"(off))
template <int N> __device__ __forceinline__ void waitl(u32x4& a, u32x4& b) { asm volatile("s_waitcnt lgkmcnt(%2)" : "+v"(a), "+v"(b) : "i"(N)); }
__device__ __forceinline__ void glds1(const char* sbase, unsigned voff, unsigned lds) {
  unsigned keep;
  asm volatile("s_mov_b32 %0, m0\n\ts_mov_b32 m0, %3\n\ts_nop 0\n\tglobal_load_lds_dwordx4 %1, %2 sc1\n\ts_mov_b32 m0, %0"
               : "=&s"(keep) : "v"(voff), "s"(sbase), "s"(lds) : "memory");
}
#define XS(i) do { } while (0)
#define XFL(b_) do { } while (0)
#define XA_ 0
#define XF_ 0
#define XE_ 0
#define XP_ 0
#define PUBK 9
#define LATE 0
#define NOWP 0
#define PEEK 3
#define LAQ 2
#define LAP 2
#define PDM 2
#define PDEEP 4
template <int ROLE>
__device__ __forceinline__ void gemm7(char* smem, int wave_id_, const u16* W0, const u16* W1, int ldb, const char* A_, size_t a_blk, const Dep& dep,
                                      char* C_, size_t c_blk, unsigned* myprog, unsigned* tmo, int slot, unsigned long long* mts,
                                      bool gl, int nimm = 0, int nseg = NSEG, int wm0 = 0, int pofs = 0) {
  volatile LAS unsigned* wsl = (volatile LAS unsigned*)(smem + L_SLOT + 512);
  if (opaque_tid() < 144) *(volatile LAS unsigned*)(smem + L_SLOT + opaque_tid() * 4) = 0u;
  const int tid = opaque_tid(), lane = tid & 63, wave = WAVE_ID;
  const int kq = wave >> 2, nq = wave & 3;
  const unsigned ldsb = __builtin_amdgcn_readfirstlane((unsigned)(size_t)(LAS char*)smem);
  constexpr int rot = 0;
  const char* A = uni(A_) + nq * 1024;
  constexpr int LA = ROLE == R_QKV ? LAQ : LAP, PD = LA == 3 ? PDEEP : PDM;
  constexpr bool P2C = ROLE == R_PART2;
  int pw = 0;
  int wm = wm0, nh = 0;
  auto lgo = [&](int b, unsigned& v) {
    if (wave == 4) {
      for (int spins = 0; __builtin_amdgcn_readfirstlane(wsl[2]) != (unsigned)(b + 1); ++spins) {
        asm volatile("global_load_dword %0, %1, %2 sc1\n\ts_waitcnt vmcnt(0)" : "=&v"(v) : "v"(dep.voff), "s"(dep.base) : "memory");
        if (__all((int)(v * dep.mul + dep.add) > b)) break;
        __builtin_amdgcn_s_sleep(2);
        if (give_up(spins, tmo, 9)) { v = 255u; break; }
      }
      wsl[2] = (unsigned)(b + 1);
    } else {
      while (__builtin_amdgcn_readfirstlane(wsl[2]) != (unsigned)(b + 1)) __builtin_amdgcn_s_sleep(1);
    }
  };
  auto lwm = [&](int b, unsigned v) {
    unsigned w;
    if (wave == 4) {
      const int w2 = (int)dep_eval(v, dep);
      w = (unsigned)min(max(max(w2, wm), b + 1), 255);
      wsl[0] = ((unsigned)(b + 1) << 8) | w;
    } else {
      for (;;) {
        w = __builtin_amdgcn_readfirstlane(wsl[0]);
        if ((w >> 8) == (unsigned)(b + 1)) break;
        __builtin_amdgcn_s_sleep(1);
      }
      w &= 255u;
    }
    return (int)w;
  };
  auto pubw = [&](int n) {
    WAIT_V(0);
    if (lane == 0) {
      const unsigned o = __hip_atomic_fetch_add((LAS unsigned*)(smem + L_SLOT + 516), 1u, __ATOMIC_RELAXED, __HIP_MEMORY_SCOPE_WORKGROUP);
      if ((o & 3u) == 3u) {
        st4_sc1(myprog, (unsigned)(n + pofs));
      }
    }
    asm volatile("s_waitcnt lgkmcnt(0)" ::: "memory");
  };
  auto hsrc = [&](int hh) { return A + (size_t)(hh >> 1) * a_blk + ((hh ^ (rot >> 2)) & 1) * 16384; };
  auto hlds = [&](int hh) { return ldsb + (unsigned)(((hh >> 1) & 1) * 65536 + (hh & 1) * 16384 + nq * 1024); };
#define POFF(i_) (((i_) & 1) * 32768 + ((i_) >> 1) * 4096)
#define PIECE(s_, l_, i_) asm volatile("s_add_u32 m0, %2, %3\n\ts_nop 0\n\tglobal_load_lds_dwordx4 %0, %1 sc1" :: "v"(av[i_]), "s"(s_), "s"(l_), "i"(POFF(i_)) : "memory", "scc", "m0")
  u32x32 BW[4];
  wload<ROLE, 2>(smem, wave_id_, W0, W1, ldb, BW, rot, [&]() {
    if (ROLE == R_QKV && nimm > 0) {
      if (kq) {
        unsigned v0 = 0;
        lgo(0, v0);
        wm = lwm(0, v0);
        const unsigned l16 = lane_id() * 16;
#pragma unroll
        for (int i = 0; i < 16; ++i) glds1(hsrc(i >> 3) + (i & 1) * 32768 + ((((i & 7) >> 1) ^ rot) & 3) * 4096, l16, hlds(i >> 3) + POFF(i & 7));
      }
      nh = 2;
    }
  });
  if (ROLE == R_QKV && nimm > 0 && !kq) wm = (int)(__builtin_amdgcn_readfirstlane(wsl[0]) & 255u);
  if (ROLE == R_QKV && opaque_tid() == 0) STS((char*)tmo - OFF_TMO, 128 + slot, 0);
  if (wave == 0) WFB();
  auto body = [&](auto ldr_c) __attribute__((always_inline)) {
    constexpr bool LDR = decltype(ldr_c)::value;
    if (XP_ == 0 ? !LDR : (XP_ == 1 ? LDR : false)) __builtin_amdgcn_s_setprio(1);
    unsigned av[8];
#pragma unroll
    for (int i = 0; i < 8; ++i) av[i] = lane * 16 + (P2C ? 0 : (i & 1) * 32768) + (((i >> 1) ^ rot) & 3) * 4096;
    auto issue_all = [&](int hh) {
      if (LDR) {
        const char* s_ = hsrc(hh); const unsigned l_ = hlds(hh);
#pragma unroll
        for (int i = 0; i < 8; ++i) if (!(P2C && (i & 1))) PIECE(s_, l_, i);
      }
    };
    int b = 0;
    bool pre = false;
    unsigned smp = 0, got = 0;
    while (b < nseg) {
      const bool fast = (wm < nseg ? wm : nseg) >= b + PD && b >= nimm && nh <= 2 * b + LA;
      const bool pl = wm <= b || pre, cold = nh < 2 * b + 2, nh3 = nh == 2 * b + 3;
      if (!fast) {
        XS(0);
        if (LDR && wm <= b && !got) lgo(b, smp);
        XS(1);
        if (got) nh += NEARLY / 8;
        { const int upto = nh3 ? 2 * b + 4 : 2 * b + 2; for (; nh < upto; ++nh) issue_all(nh); }
        if (LDR && pl) wm = lwm(b, smp);
        XS(2);
      }
      const int ln = lane_id(), g = ln >> 4, r16 = ln & 15;
      const unsigned e0 = ldsb + (unsigned)((P2C ? 0 : kq * 32768) + (b & 1) * 65536 + (gl ? (g >> 1) * 512 + r16 * 32 + (g & 1) * 16 : r16 * 128 + ((g ^ (r16 & 7)) << 4)));
      const unsigned d0 = e0 ^ (gl ? 1024u : 64u);
      const unsigned rw = ldsb + L_RED + nq * 4096 + ln * 16;
      const unsigned co0 = ROLE == R_F1 ? (unsigned)((g & 1) * 2048 + nq * 512 + r16 * 32 + (g >> 1) * 16)
                         : ((ROLE == R_QKV ? TLQ : TLP) ? (unsigned)(nq * (P2C ? 4096 : 2048) + r16 * 64 + g * 16)
                         : (ROLE == R_QKV ? (unsigned)(r16 * 256 + (((4 * nq + g) ^ r16) << 4)) : (unsigned)(r16 * PROW + (32 * nq + 8 * g) * 2)));
      const unsigned co1 = co0 + ((ROLE == R_QKV ? TLQ : TLP) ? 1024u : (ROLE == R_QKV ? 4096u : 16u * PROW));
      u32x16 pv_;
      bool pr2 = false;
      if (fast) {
        __builtin_amdgcn_s_barrier(); asm volatile("" ::: "memory");
        for (; nh < 2 * b + LA; ++nh) issue_all(nh);
        int lf = (wm < nseg ? wm : nseg) - (LA - 1), cp = nseg - (LA - 1);
        const unsigned sa = ldsb + L_SLOT + ln * 4, ad = dep.add - (unsigned)(LA - 1);
        if constexpr (LDR) (LA == 3 ? (P2C ? fast_ld2_3 : fast_ld0_3) : (P2C ? fast_ld2_2 : fast_ld0_2))(BW, pv_, e0, d0, b, lf, cp, sa, dep.mul, ad, rw, (unsigned)a_blk,
                                                                                                       hsrc(2 * b + LA), hsrc(2 * b + LA + 1), hlds(2 * b + LA), hlds(2 * b + LA + 1), av);
        else {
          const char* c0_ = uni((const char*)tmo - OFF_TMO + OFF_DUMP); const char* c1_ = uni(C_ + (size_t)b * c_blk);
          const int sf = b + pofs;
          const char* pg = uni((const char*)myprog);
          (ROLE == R_F1 ? fast_st1 : (P2C ? fast_st2 : fast_st0))(BW, pv_, e0, d0, b, lf, cp, sa, dep.mul, ad, rw, (unsigned)c_blk, c0_, c1_, co0, co1, pofs, sf, (unsigned)wave,
                                                                   ldsb + L_SLOT, dep.voff, dep.base, pg);
        }
        wm = lf + LA - 1 > wm ? lf + LA - 1 : wm;
        nh = 2 * b + LA;
        --b;
        __builtin_amdgcn_s_barrier(); asm volatile("" ::: "memory");
      } else {
        if (LDR) { if (nh >= 2 * b + 4) { if (P2C) WAIT_V(14); else WAIT_V(28); } else { if (P2C) WAIT_V(6); else WAIT_V(12); } }
        BAR();
        XS(3);
        if (ROLE == R_QKV && b < 2 && opaque_tid() == 0) STS((char*)tmo - OFF_TMO, 128 + slot, 1 + b);
        if constexpr (!LDR) {
          const unsigned wv = wsl[0];
          lean_st0(BW, e0, d0, __builtin_amdgcn_readfirstlane(pw > 0 ? (cold ? 4u : 1u) : 0u), pv_, (unsigned)(pw + pofs), ldsb + L_SLOT + 516, uni((const char*)myprog));
          if (pl) wm = (int)(__builtin_amdgcn_readfirstlane(wv) & 255u);
        }
        const bool dm = nh == 2 * b + 2 && wm > b + 1 && b + 1 < nseg;
        pr2 = b + 1 < nseg && wm < nseg;
        if constexpr (LDR) {
          const bool er = nh == 2 * b + 2 && !dm;
          const unsigned ps = pr2 && (wave == 4 || er) ? (unsigned)(b + 1) : 0u;
#define LARGS BW, e0, d0, (unsigned)(nh - 2 * b - 2), hlds(nh), hsrc(nh), hsrc(nh + 1), av, ps, dep.voff, dep.base, \
              rw, ldsb + L_SLOT + 528 + nq * 4, (unsigned)(b + 1), ldsb + L_SLOT + 520, dep.mul, dep.add, er ? b + 1 : 0x7FFFFFFF, smp, got
          if (dm) (P2C ? lean_dm1 : lean_dm0)(LARGS); else (P2C ? lean_ld1 : lean_ld0)(LARGS);
#undef LARGS
          got = __builtin_amdgcn_readfirstlane(got);
        }
        if (dm) nh += 2;
        XS(5);
      }
      if constexpr (LDR) { if (fast) fin_ld(pv_, rw, ldsb + L_SLOT + 528 + nq * 4, (unsigned)(b + 1)); }
      else (ROLE == R_F1 ? fin_st1 : (P2C ? fin_st2 : fin_st0))(pv_, rw, ldsb + L_SLOT + 528 + nq * 4, (unsigned)(b + 1), uni(C_ + (size_t)b * c_blk), co0, co1);
      XS(6);
      pre = pr2;
      bool now = (nh < 2 * b + 4 && wm < b + 1 + PD) || b + 1 >= nseg || b < nimm;
      if (!LDR && now) {
        if (NOWP == 0 && b >= nimm && __builtin_amdgcn_readfirstlane(wsl[PEEK]) == (unsigned)(b + 2)) now = false;
        else pubw(b + 1);
      }
      XS(7);
      XFL(b);
      pw = now ? 0 : b + 1;
      ++b;
    }
    __builtin_amdgcn_s_setprio(0);
  };
  if (kq) body(std::true_type{}); else body(std::false_type{});
#undef PIECE
#undef POFF
  WAIT_V(0);
  BAR();
}
__device__ __forceinline__ void fin_role(char* smem, int wave_id_, int fidx, const float* mod, const char* P2, const u16* x1, u16* out, unsigned* cnt, unsigned* tmo,
                                         unsigned long long* mts) {
  const int tid = opaque_tid(), lane = tid & 63, wave = WAVE_ID;
  row_table(smem + L_GB, mod, cnt, tmo, tid);
  volatile LAS unsigned* wsl = (volatile LAS unsigned*)(smem + L_SLOT + 512);
  int wf = 0, par = 0;
  for (int nf = fidx >> 3; nf < NSEG; nf += 2) {
    if (wf <= nf) {
      if (wave == 0) {
        unsigned w;
        for (int spins = 0;; ++spins) {
          const unsigned v = rowmin16(ld4_sc1(cnt + (PF2 + ((lane & 31) < 28 ? (lane & 31) : 0)) * 32));
          w = min(rdl(v, 0), rdl(v, 16));
          if ((int)w > nf) break;
          __builtin_amdgcn_s_sleep(2);
          if (give_up(spins, tmo, 9)) { w = NSEG; break; }
        }
        if (lane == 0) wsl[par] = w;
      }
      BAR();
      wf = (int)__builtin_amdgcn_readfirstlane(wsl[par]);
      par ^= 1;
    }
    int ln = lane; asm volatile("" : "+v"(ln));
    if (wave == 0) WTS(256 + 8 + fidx, nf / 2, 0);
    fin_half(P2, x1, smem + L_GB + 12288, out, nf * 32 + 4 * (fidx & 7) + (wave >> 1), wave & 1, ln);
    if (wave == 0) WTS(256 + 8 + fidx, nf / 2, 2);
  }
}
#define LD8(r, vo, sb) asm volatile("global_load_dwordx4 %0, %8, %12 sc1\n\tglobal_load_dwordx4 %1, %9, %12 sc1\n\t" \
    "global_load_dwordx4 %2, %10, %12 sc1\n\tglobal_load_dwordx4 %3, %11, %12 sc1\n\t" \
    "global_load_dwordx4 %4, %8, %13 sc1\n\tglobal_load_dwordx4 %5, %9, %13 sc1\n\t" \
    "global_load_dwordx4 %6, %10, %13 sc1\n\tglobal_load_dwordx4 %7, %11, %13 sc1" \
    : "=&v"(r[0][0]), "=&v"(r[0][1]), "=&v"(r[0][2]), "=&v"(r[0][3]), "=&v"(r[1][0]), "=&v"(r[1][1]), "=&v"(r[1][2]), "=&v"(r[1][3]) \
    : "v"(vo), "v"(vo ^ (TLQ ? 2048u : 64u)), "v"(vo ^ (TLQ ? 4096u : 128u)), "v"(vo ^ (TLQ ? 6144u : 192u)), "s"(sb), "s"((sb) + (TLQ ? 1024 : 4096)) : "memory")
#define TIE8(r) "+v"(r[0][0]), "+v"(r[0][1]), "+v"(r[0][2]), "+v"(r[0][3]), "+v"(r[1][0]), "+v"(r[1][1]), "+v"(r[1][2]), "+v"(r[1][3])
#define SADL 44
#define SASL 8
__device__ __forceinline__ void sa_role(char* smem, int wave_id_, int s0, int hbase, const char* __restrict__ Q, const char* __restrict__ Kx, const char* __restrict__ V,
                                        const char* kvm, char* __restrict__ O, unsigned* cnt, unsigned* tmo, int slot, unsigned long long* mts) {
  constexpr float LS = LOG2E * SCALE;
  const int tid = opaque_tid(), wave = WAVE_ID;
  const int h = hbase + wave;
  char* vl = smem + wave * 17408;
  char* ol = vl + 8192;
  const unsigned ldsw = __builtin_amdgcn_readfirstlane((unsigned)(size_t)(LAS char*)vl);
  unsigned avail = 0;
  for (int s = s0, it = 1; s < NSEG; s += 4, ++it) {
    u32x4 qv[2][4], kv[2][4];
    int lane = tid & 63; asm volatile("" : "+v"(lane));
    const int g = lane >> 4, r16 = lane & 15, q4 = r16 >> 2, p4 = lane & 3;
    auto request = [&](int sr) {
      const size_t o = ((size_t)sr * NH + h) * 8192;
      const char* qs = uni(Q + o); const char* ks = uni(Kx + o); const char* vs = uni(V + o);
      const unsigned fo = TLQ ? (unsigned)(r16 * 64 + g * 16) : (unsigned)(r16 * 256 + ((g ^ r16) << 4));
      LD8(qv, fo, qs); LD8(kv, fo, ks);
      head_to_lds(vs, lane, ldsw);
    };
    {
      for (int spins = 0; avail <= (unsigned)s; ++spins) {
        const unsigned p = ld4_sc1(cnt + (PQKV + h) * 32 + (lane & 15));
        avail = min(min(qkv_avail(p, 0), qkv_avail(p, 1)), qkv_avail(p, 2));
        if (avail > (unsigned)s) break;
        __builtin_amdgcn_s_sleep(32);
        if (give_up(spins, tmo, 10)) break;
      }
      request(s);
      asm volatile("s_waitcnt vmcnt(0)" : TIE8(qv), TIE8(kv) :: "memory");
    }
    if (wave == 0) WTS(slot, s, 0);
    f32x4 s00 = f32x4{0, 0, 0, 0}, s01 = s00, s11 = s00;
#pragma unroll
    for (int ks = 0; ks < 4; ++ks) {
      s00 = MFMA32(asbf8(kv[0][ks]), asbf8(qv[0][ks]), s00);
      s01 = MFMA32(asbf8(kv[0][ks]), asbf8(qv[1][ks]), s01);
      s11 = MFMA32(asbf8(kv[1][ks]), asbf8(qv[1][ks]), s11);
    }
    float mx[2], sum[2];
    u32x4 pseg[2];
    {
#pragma unroll
      for (int r = 0; r < 4; ++r) { const bool ok = 4 * g + r <= r16; s00[r] = ok ? s00[r] : -3.0e38f; s11[r] = ok ? s11[r] : -3.0e38f; }
      mx[0] = red16_32_max(max3(max3(s00[0], s00[1], s00[2]), s00[3], -1.0e38f));
      mx[1] = red16_32_max(max3(max3(fmaxf(fmaxf(s01[0], s01[1]), fmaxf(s01[2], s01[3])), s11[0], -1.0e38f), max3(s11[1], s11[2], s11[3]), -1.0e38f));
      const float n0 = -mx[0] * LS, n1 = -mx[1] * LS;
#pragma unroll
      for (int r = 0; r < 4; ++r) {
        s00[r] = __builtin_amdgcn_exp2f(s00[r] * LS + n0); s01[r] = __builtin_amdgcn_exp2f(s01[r] * LS + n1); s11[r] = __builtin_amdgcn_exp2f(s11[r] * LS + n1);
      }
      sum[0] = (s00[0] + s00[1]) + (s00[2] + s00[3]);
      sum[1] = ((s01[0] + s01[1]) + (s01[2] + s01[3])) + ((s11[0] + s11[1]) + (s11[2] + s11[3]));
      pseg[0] = u32x4{pack2(s00[0], s00[1]), pack2(s00[2], s00[3]), 0u, 0u};
      pseg[1] = u32x4{pack2(s01[0], s01[1]), pack2(s01[2], s01[3]), pack2(s11[0], s11[1]), pack2(s11[2], s11[3])};
    }
    f32x4 o[8][2];
#pragma unroll
    for (int dt = 0; dt < 8; ++dt) {
      const int row0 = 4 * g + q4;
      const unsigned xo = (unsigned)(row0 * 256 + (((4 * (dt >> 1) + p4) ^ row0) << 4) + (dt & 1) * 8);
      const u32x2 lo = trread(vl, xo), hi = trread(vl, xo + 4096);
      o[dt][0] = MFMA32(asbf8(u32x4{lo.x, lo.y, 0u, 0u}), asbf8(pseg[0]), (f32x4{0, 0, 0, 0}));
      o[dt][1] = MFMA32(asbf8(u32x4{lo.x, lo.y, hi.x, hi.y}), asbf8(pseg[1]), (f32x4{0, 0, 0, 0}));
    }
#pragma unroll
    for (int dt = 0; dt < 8; ++dt) asm volatile("" : "+v"(o[dt][0]), "+v"(o[dt][1]));
    asm volatile("s_waitcnt lgkmcnt(0)" ::: "memory");
    for (int spins = 0;; ++spins) {
      if (__all(ld4_sc1(cnt + (PROG + h) * 32 + ((lane & 4) ? 5 : (lane & 3))) + (s >= SDIR ? 1u : 0u) > (unsigned)s)) break;
      __builtin_amdgcn_s_sleep(SASL);
      if (give_up(spins, tmo, 6)) break;
    }
    if (s >= SDIR) delay64<SADL>();
    const char* mk = kvm + ((size_t)s * NH + h) * 4096;
    u32x4 kmv[4]; u32x2 vmv[8];
    {
      u32x4 k0, k1;
      const char* pk = mk + lane * 16;
      const char* pv = mk + 2048 + (8 * q4 + p4) * 16 + (g & 1) * 8;
      for (int spins = 0;; ++spins) {
      asm volatile("global_load_dwordx4 %0, %10, off sc1\n\tglobal_load_dwordx4 %1, %10, off offset:1024 sc1\n\t"
                   "global_load_dwordx2 %2, %11, off sc1\n\tglobal_load_dwordx2 %3, %11, off offset:64 sc1\n\t"
                   "global_load_dwordx2 %4, %11, off offset:512 sc1\n\tglobal_load_dwordx2 %5, %11, off offset:576 sc1\n\t"
                   "global_load_dwordx2 %6, %11, off offset:1024 sc1\n\tglobal_load_dwordx2 %7, %11, off offset:1088 sc1\n\t"
                   "global_load_dwordx2 %8, %11, off offset:1536 sc1\n\tglobal_load_dwordx2 %9, %11, off offset:1600 sc1\n\ts_waitcnt vmcnt(0)"
                   : "=&v"(k0), "=&v"(k1), "=&v"(vmv[0]), "=&v"(vmv[1]), "=&v"(vmv[2]), "=&v"(vmv[3]),
                     "=&v"(vmv[4]), "=&v"(vmv[5]), "=&v"(vmv[6]), "=&v"(vmv[7])
                   : "v"(pk), "v"(pv) : "memory");
      unsigned mx_ = max(max(max(k0.x, k0.y), max(k0.z, k0.w)), max(max(k1.x, k1.y), max(k1.z, k1.w)));
#pragma unroll
      for (int i = 0; i < 8; ++i) mx_ = max(mx_, max(vmv[i].x, vmv[i].y));
      if (__all(mx_ != SENT)) break;
      __builtin_amdgcn_s_sleep(2);
      if (give_up(spins, tmo, 6)) break;
      }
      WTM(slot, s, 1);
      *(u32x4*)(ol + lane * 16) = k0; *(u32x4*)(ol + 1024 + lane * 16) = k1;
      asm volatile("s_waitcnt lgkmcnt(0)" ::: "memory");
      __builtin_amdgcn_wave_barrier();
      const int a = (8 * g + q4) * 16 + (lane & 1) * 8;
#pragma unroll
      for (int ks = 0; ks < 4; ++ks) {
        const u32x2 lo = trread(ol, a + ks * 512), hi = trread(ol, a + ks * 512 + 64);
        kmv[ks] = u32x4{lo.x, lo.y, hi.x, hi.y};
      }
      asm volatile("s_waitcnt lgkmcnt(0)" ::: "memory");
      __builtin_amdgcn_wave_barrier();
    }
    u32x2 pm[2]; float cs[2], inv[2];
#pragma unroll
    for (int b = 0; b < 2; ++b) {
      f32x4 t = f32x4{0, 0, 0, 0};
#pragma unroll
      for (int ks = 0; ks < 4; ++ks) t = MFMA32(asbf8(kmv[ks]), asbf8(qv[b][ks]), t);
      float m = fmaxf(fmaxf(t[0], t[1]), fmaxf(fmaxf(t[2], t[3]), mx[b]));
      { float x, y; sw16(m, x, y); m = max2(x, y); }
      const float n = -m * LS;
      const float f = __builtin_amdgcn_exp2f(mx[b] * LS + n);
#pragma unroll
      for (int r = 0; r < 4; ++r) t[r] = g < 2 ? __builtin_amdgcn_exp2f(t[r] * LS + n) : 0.0f;
      inv[b] = __builtin_amdgcn_rcpf(red16_32_add(sum[b] * f + ((t[0] + t[1]) + (t[2] + t[3]))));
      cs[b] = f * inv[b];
      pm[b] = u32x2{pack2(t[0] * inv[b], t[1] * inv[b]), pack2(t[2] * inv[b], t[3] * inv[b])};
    }
    const char* od = uni(O + ((size_t)s * 64 + 2 * h) * 4096);
#pragma unroll
    for (int j = 0; j < 4; ++j) {
      u32x2 v0 = vmv[2 * j], v1 = vmv[2 * j + 1];
      if (g >= 2) { v0 = u32x2{0, 0}; v1 = v0; }
#pragma unroll
      for (int b = 0; b < 2; ++b) {
        const f32x4 r0 = __builtin_amdgcn_mfma_f32_16x16x16bf16_1k(ass4(v0), ass4(pm[b]), o[2 * j][b] * cs[b], 0, 0, 0);
        const f32x4 r1 = __builtin_amdgcn_mfma_f32_16x16x16bf16_1k(ass4(v1), ass4(pm[b]), o[2 * j + 1][b] * cs[b], 0, 0, 0);
        st16s(od, (unsigned)((j >> 1) * 4096 + (16 * b + r16) * 128 + (((4 * (j & 1) + g) ^ (r16 & 7)) << 4)),
              u32x4{pack2(r0[0], r0[1]), pack2(r0[2], r0[3]), pack2(r1[0], r1[1]), pack2(r1[2], r1[3])});
      }
    }
    WTM(slot, s, 3);
    WAIT_V(0);
    __builtin_amdgcn_s_barrier();
    if (wave == 0 && lane == 0) st4_sc1(cnt + (PSA + (hbase >> 1) + s0) * 32, (unsigned)it);
    WTM(slot, s, 2);
  }
}
#undef LD8
#undef TIE8

constexpr int Q_MEM = 0;
constexpr int Q_K   = 16384;
constexpr int Q_V   = 24576;
constexpr int Q_QK  = 32768;
constexpr int Q_TM  = 36864;
constexpr int Q_CB  = 40960;
constexpr int Q_RING = 32768;
constexpr int V_P1  = 0;
constexpr int V_P2  = 16384;
constexpr int NCHAIN = 96;
#define NQQ 6
#define NQK 4
#define E_AG 9
#define RDYI -2
#define ROTW 2
#define DQU 6
#define DQM 18
#define DQD 1
#define ORSEL (xg >> 1)
#define D_Q 105
#define D_O 4
#define E_B 6
#define E_O 1900
#define KDEL 5
#define VPOS 0

typedef __bf16 bf16x16 __attribute__((ext_vector_type(16)));
typedef unsigned u32x8 __attribute__((ext_vector_type(8)));
__device__ __forceinline__ f32x4 SMF(u32x4 a, u32x8 b, f32x4 c, int idx) {
  return __builtin_amdgcn_smfmac_f32_16x16x64_bf16(__builtin_bit_cast(bf16x8, a), __builtin_bit_cast(bf16x16, b), c, idx, 0, 0);
}
__device__ __forceinline__ float fold32(float a, float b) {
  const auto p = __builtin_amdgcn_permlane32_swap(__float_as_uint(a), __float_as_uint(b), false, false);
  return __uint_as_float(p[0]) + __uint_as_float(p[1]);
}
__device__ __forceinline__ f32x2 rowsum(f32x4 x) { return f32x2{fold32(x[0], x[2]), fold32(x[1], x[3])}; }
__device__ __forceinline__ unsigned nosent1(unsigned v) { return v < 0xFFFFFFFEu ? v : 0xFFFFFFFEu; }
__device__ __forceinline__ void st4s(const char* sb, unsigned vo, unsigned v) { asm volatile("global_store_dword %0, %1, %2 sc1\n\ts_nop 0" :: "v"(vo), "v"(v), "s"(sb) : "memory"); }
__device__ __forceinline__ void st8s(const char* sb, unsigned vo, u32x2 v) { asm volatile("global_store_dwordx2 %0, %1, %2 sc1\n\ts_nop 1" :: "v"(vo), "v"(v), "s"(sb) : "memory"); }
__device__ __forceinline__ void gldsw(const char* sbase, unsigned voff, unsigned lds) {
  unsigned keep;
  asm volatile("s_mov_b32 %0, m0\n\ts_mov_b32 m0, %3\n\ts_nop 0\n\tglobal_load_lds_dwordx4 %1, %2" CNT_ "\n\ts_mov_b32 m0, %0"
               : "=&s"(keep) : "v"(voff), "s"(sbase), "s"(lds) : "memory");
}
__device__ __forceinline__ void wait_clk(unsigned t0, unsigned d) {
  while ((unsigned)__builtin_amdgcn_s_memtime() - t0 < d) __builtin_amdgcn_s_sleep(1);
}
__device__ __forceinline__ unsigned bell_time(const char* a, unsigned vo, unsigned tag, unsigned cap) {
  unsigned v;
  for (unsigned spins = 0;; ++spins) {
    asm volatile("global_load_dword %0, %1, %2 sc1\n\ts_waitcnt vmcnt(0)" : "=&v"(v) : "v"(vo), "s"(a) : "memory");
    if (__all((v >> 24) >= tag) || spins >= cap) break;
  }
  const unsigned now = (unsigned)__builtin_amdgcn_s_memrealtime() + 250u;
  const unsigned age = rowmin16((now - v) & 0xFFFFFFu);
  return now - min(min(rdl(age, 0), rdl(age, 16)), 500u);
}
__device__ __forceinline__ void wait_rt(unsigned t0, int d) {
  while ((int)((unsigned)__builtin_amdgcn_s_memrealtime() - t0) < d) { }
}
#define WBAR() do { __builtin_amdgcn_s_barrier(); asm volatile("" ::: "memory"); } while (0)
#define SB() __builtin_amdgcn_sched_barrier(0)
#define TN(k, v) do { } while (0)
__device__ __forceinline__ void stage_proj(const char* src, unsigned lds, int tid, int wave) {
  const int row = tid >> 4, f = 2 * ((row & 1) | (((row >> 2) & 1) << 1));
#pragma unroll
  for (int j = 0; j < 4; ++j)
    gldsw(src + (size_t)j * 32 * HD * 2, (unsigned)(row * HD * 2 + (((tid & 15) ^ f ^ ((j & 1) << 3)) << 4)), lds + (wave * 64 + 512 * j) * 16);
}
__device__ __forceinline__ unsigned proj_base(int T, int lane) {
  const int g = lane >> 4, q4 = (lane & 15) >> 2, p4 = lane & 3, fs = 2 * ((q4 & 1) | ((g & 1) << 1) | ((q4 >> 1) << 2));
  return (unsigned)((4 * g + (q4 & 1) + 32 * (q4 >> 1)) * 256 + (((2 * T) ^ fs) << 4) + p4 * 8);
}
__device__ __forceinline__ u32x8 proj_frag(const char* smem, unsigned a) {
  const u32x2 t0 = trread(smem, a), t1 = trread(smem, a + 512), t2 = trread(smem, a + 4096), t3 = trread(smem, a + 4608);
  return u32x8{t0.x, t0.y, t1.x, t1.y, t2.x, t2.y, t3.x, t3.y};
}
__device__ __forceinline__ void flag_frags(unsigned fa, unsigned a, unsigned want, bf16x8 (&f)[4]) {
  u32x2 t[8];
  for (;;) {
    u32x4 fl;
    asm volatile("ds_read_b128 %0, %9\n\t"
                 "ds_read_b64_tr_b16 %1, %10\n\tds_read_b64_tr_b16 %2, %10 offset:64\n\t"
                 "ds_read_b64_tr_b16 %3, %10 offset:512\n\tds_read_b64_tr_b16 %4, %10 offset:576\n\t"
                 "ds_read_b64_tr_b16 %5, %10 offset:1024\n\tds_read_b64_tr_b16 %6, %10 offset:1088\n\t"
                 "ds_read_b64_tr_b16 %7, %10 offset:1536\n\tds_read_b64_tr_b16 %8, %10 offset:1600\n\ts_waitcnt lgkmcnt(0)"
                 : "=&v"(fl), "=&v"(t[0]), "=&v"(t[1]), "=&v"(t[2]), "=&v"(t[3]), "=&v"(t[4]), "=&v"(t[5]), "=&v"(t[6]), "=&v"(t[7])
                 : "v"(fa), "v"(a) : "memory");
    if (__builtin_amdgcn_readfirstlane(min(min(fl.x, fl.y), min(fl.z, fl.w))) >= want) break;
  }
#pragma unroll
  for (int ks = 0; ks < 4; ++ks) f[ks] = asbf8(u32x4{t[2 * ks].x, t[2 * ks].y, t[2 * ks + 1].x, t[2 * ks + 1].y});
}
__device__ __forceinline__ void wait_flags(const char* p, unsigned want) {
  for (;;) {
    const u32x4 fl = *(volatile LAS u32x4*)p;
    if (__builtin_amdgcn_readfirstlane(min(min(fl.x, fl.y), min(fl.z, fl.w))) >= want) break;
  }
}
__device__ __forceinline__ f32x4 zero4() { float z = 0.0f; asm volatile("" : "+v"(z)); return f32x4{z, z, z, z}; }
__device__ __forceinline__ unsigned lds_word(const char* p) { return __builtin_amdgcn_readfirstlane(*(volatile LAS unsigned*)p); }

#define MFMA16(a, b, c) __builtin_amdgcn_mfma_f32_16x16x16bf16_1k((a), (b), (c), 0, 0, 0)
#define UF(x) __uint_as_float(x)
__device__ __forceinline__ unsigned umax3(unsigned a, unsigned b, unsigned c) { return max(max(a, b), c); }
__device__ __forceinline__ f32x2 logit2(float a, float b) {
  return f32x2{a, b};
}
constexpr float LSC = LOG2E * SCALE;

struct QKW { u32x32 w0, w1, w2, w3, w4, w5; u32x8 x; };
template <int W_> __device__ __forceinline__ void qk_step(const QKW& W, const char* ag, const char* vs, const char* od, const char* kv, unsigned& cap, unsigned& sp,
                                                          unsigned long long& tc, unsigned long long* ts, unsigned& d0, unsigned& d1);
template <> __device__ __forceinline__ void qk_step<0>(const QKW& W, const char* ag, const char* vs, const char* od, const char* kv, unsigned& cap, unsigned& sp,
                                                        unsigned long long& tc, unsigned long long* ts, unsigned& d0, unsigned& d1) {
  unsigned st, bg;
  asm volatile("v_mbcnt_lo_u32_b32 v60, -1, 0\n\t"
    "v_mbcnt_hi_u32_b32 v60, -1, v60\n\t"
    "v_lshlrev_b32 v6, 4, v60\n\t"
    "s_mov_b32 %[sp], 0\n\t"
    "s_setprio 3\n\t"
    "global_load_dwordx4 v[40:43], v6, %[ag] sc1\n\t"
    "global_load_dwordx4 v[44:47], v6, %[ag] offset:1024 sc1\n\t"
    "s_memtime %[tc]\n\t"
    "v_add_u32 v7, 40960, v6\n\t"
    "v_add_u32 v8, 98304, v6\n\t"
    "ds_read_b128 v[24:27], v7 offset:0\n\t"
    "ds_read_b128 v[28:31], v7 offset:1024\n\t"
    "ds_read_b128 v[32:35], v7 offset:8192\n\t"
    "ds_read_b128 v[36:39], v7 offset:9216\n\t"
    "v_lshrrev_b32 v48, 4, v60\n\t"
    "v_and_b32 v49, 15, v60\n\t"
    "v_and_b32 v61, 4, v60\n\t"
    "v_mul_u32_u24 v61, 0x2aaa, v61\n\t"
    "v_lshrrev_b32 v9, 1, v60\n\t"
    "v_and_b32 v9, 2, v9\n\t"
    "v_or3_b32 v9, v9, v61, %[c44]\n\t"
    "v_and_b32 v61, 1, v48\n\t"
    "v_lshlrev_b32 v61, 3, v61\n\t"
    "v_lshl_or_b32 v14, v49, 4, v61\n\t"
    "v_add_u32 v14, 0, v14\n\t"
    "v_lshlrev_b32 v61, 2, v48\n\t"
    "v_lshl_or_b32 v10, v49, 4, v61\n\t"
    "v_lshrrev_b32 v50, 2, v49\n\t"
    "v_and_b32 v51, 3, v60\n\t"
    "v_and_b32 v61, 1, v60\n\t"
    "v_lshlrev_b32 v61, 3, v61\n\t"
    "v_lshl_or_b32 v11, v50, 4, v61\n\t"
    "v_lshl_or_b32 v11, v48, 7, v11\n\t"
    "v_xor_b32 v12, v48, v49\n\t"
    "v_lshlrev_b32 v12, 4, v12\n\t"
    "v_lshl_or_b32 v12, v49, 8, v12\n\t"
    "v_lshl_or_b32 v50, v48, 2, v50\n\t"
    "v_lshrrev_b32 v51, 1, v51\n\t"
    "v_or_b32 v51, 0, v51\n\t"
    "v_xor_b32 v51, v51, v50\n\t"
    "v_lshl_or_b32 v13, v51, 4, v61\n\t"
    "v_lshl_or_b32 v13, v50, 8, v13\n\t"
    "v_and_b32 v61, 3, v60\n\t"
    "v_lshlrev_b32 v61, 4, v61\n\t"
    "v_lshl_or_b32 v15, v48, 6, v61\n\t"
    "v_and_b32 v61, 8, v60\n\t"
    "v_lshl_or_b32 v15, v61, 14, v15\n\t"
    "v_and_b32 v61, 4, v60\n\t"
    "v_lshl_or_b32 v15, v61, 8, v15\n\t"
    "v_add_u32 v15, 0, v15\n\t"
    "3:\n\t"
    "v_mov_b32 v16, 0\n\t"
    "v_mov_b32 v17, 0\n\t"
    "v_mov_b32 v18, 0\n\t"
    "v_mov_b32 v19, 0\n\t"
    "v_mov_b32 v20, 0\n\t"
    "v_mov_b32 v21, 0\n\t"
    "v_mov_b32 v22, 0\n\t"
    "v_mov_b32 v23, 0\n\t"
    "s_waitcnt vmcnt(0) lgkmcnt(0)\n\t"
    "14:\n\t"
    "ds_write_b128 v6, v[40:43] offset:0\n\t"
    "ds_write_b128 v6, v[44:47] offset:1024\n\t"
    "v_smfmac_f32_16x16x64_bf16 v[16:19], v[40:43], v[64:71], v9\n\t"
    "v_max3_u32 v60, v40, v41, v42\n\t"
    "v_max3_u32 v61, v43, v44, v45\n\t"
    "v_smfmac_f32_16x16x64_bf16 v[20:23], v[40:43], v[72:79], v9\n\t"
    "v_max3_u32 v60, v60, v46, v47\n\t"
    "v_max_u32 v60, v60, v61\n\t"
    "v_cmp_eq_u32 vcc, -1, v60\n\t"
    "s_cbranch_vccnz 1f\n\t"
    "2:\n\t"
    "s_waitcnt lgkmcnt(0)\n\t"
    "s_barrier\n\t"
    "ds_read_b128 v[40:43], v6 offset:12288\n\t"
    "ds_read_b128 v[48:51], v6 offset:2048\n\t"
    "v_smfmac_f32_16x16x64_bf16 v[16:19], v[44:47], v[80:87], v9\n\t"
    "ds_read_b128 v[52:55], v6 offset:3072\n\t"
    "ds_read_b128 v[56:59], v6 offset:4096\n\t"
    "v_smfmac_f32_16x16x64_bf16 v[20:23], v[44:47], v[88:95], v9\n\t"
    "ds_read_b128 v[60:63], v6 offset:13312\n\t"
    "ds_read_b128 v[44:47], v6 offset:5120\n\t"
    "s_waitcnt lgkmcnt(5)\n\t"
    "v_smfmac_f32_16x16x64_bf16 v[16:19], v[40:43], v[24:31], v9\n\t"
    "ds_read_b128 v[24:27], v7 offset:16384\n\t"
    "ds_read_b128 v[28:31], v7 offset:17408\n\t"
    "v_smfmac_f32_16x16x64_bf16 v[20:23], v[40:43], v[32:39], v9\n\t"
    "ds_read_b128 v[32:35], v7 offset:24576\n\t"
    "ds_read_b128 v[36:39], v7 offset:25600\n\t"
    "ds_read_b128 v[40:43], v6 offset:6144\n\t"
    "s_waitcnt lgkmcnt(9)\n\t"
    "v_smfmac_f32_16x16x64_bf16 v[16:19], v[48:51], v[96:103], v9\n\t"
    "v_smfmac_f32_16x16x64_bf16 v[20:23], v[48:51], v[104:111], v9\n\t"
    "ds_read_b128 v[48:51], v6 offset:7168\n\t"
    "s_waitcnt lgkmcnt(9)\n\t"
    "v_smfmac_f32_16x16x64_bf16 v[16:19], v[52:55], v[112:119], v9\n\t"
    "v_smfmac_f32_16x16x64_bf16 v[20:23], v[52:55], v[120:127], v9\n\t"
    "ds_read_b128 v[52:55], v6 offset:14336\n\t"
    "s_waitcnt lgkmcnt(9)\n\t"
    "v_smfmac_f32_16x16x64_bf16 v[16:19], v[56:59], v[128:135], v9\n\t"
    "v_smfmac_f32_16x16x64_bf16 v[20:23], v[56:59], v[136:143], v9\n\t"
    "ds_read_b128 v[56:59], v6 offset:8192\n\t"
    "s_waitcnt lgkmcnt(6)\n\t"
    "v_smfmac_f32_16x16x64_bf16 v[16:19], v[60:63], v[24:31], v9\n\t"
    "ds_read_b128 v[24:27], v8 offset:0\n\t"
    "ds_read_b128 v[28:31], v8 offset:1024\n\t"
    "s_waitcnt lgkmcnt(6)\n\t"
    "v_smfmac_f32_16x16x64_bf16 v[20:23], v[60:63], v[32:39], v9\n\t"
    "ds_read_b128 v[32:35], v8 offset:8192\n\t"
    "ds_read_b128 v[36:39], v8 offset:9216\n\t"
    "ds_read_b128 v[60:63], v6 offset:9216\n\t"
    "v_smfmac_f32_16x16x64_bf16 v[16:19], v[44:47], v[144:151], v9\n\t"
    "v_smfmac_f32_16x16x64_bf16 v[20:23], v[44:47], v[152:159], v9\n\t"
    "ds_read_b128 v[44:47], v6 offset:10240\n\t"
    "s_waitcnt lgkmcnt(9)\n\t"
    "v_smfmac_f32_16x16x64_bf16 v[16:19], v[40:43], v[160:167], v9\n\t"
    "v_smfmac_f32_16x16x64_bf16 v[20:23], v[40:43], v[168:175], v9\n\t"
    "ds_read_b128 v[40:43], v6 offset:15360\n\t"
    "s_waitcnt lgkmcnt(9)\n\t"
    "v_smfmac_f32_16x16x64_bf16 v[16:19], v[48:51], v[176:183], v9\n\t"
    "v_smfmac_f32_16x16x64_bf16 v[20:23], v[48:51], v[184:191], v9\n\t"
    "ds_read_b128 v[48:51], v6 offset:11264\n\t"
    "s_waitcnt lgkmcnt(6)\n\t"
    "v_smfmac_f32_16x16x64_bf16 v[16:19], v[52:55], v[24:31], v9\n\t"
    "ds_read_b128 v[24:27], v8 offset:16384\n\t"
    "ds_read_b128 v[28:31], v8 offset:17408\n\t"
    "s_waitcnt lgkmcnt(6)\n\t"
    "v_smfmac_f32_16x16x64_bf16 v[20:23], v[52:55], v[32:39], v9\n\t"
    "ds_read_b128 v[32:35], v8 offset:24576\n\t"
    "ds_read_b128 v[36:39], v8 offset:25600\n\t"
    "v_smfmac_f32_16x16x64_bf16 v[16:19], v[56:59], v[192:199], v9\n\t"
    "v_smfmac_f32_16x16x64_bf16 v[20:23], v[56:59], v[200:207], v9\n\t"
    "s_waitcnt lgkmcnt(7)\n\t"
    "v_smfmac_f32_16x16x64_bf16 v[16:19], v[60:63], v[208:215], v9\n\t"
    "v_smfmac_f32_16x16x64_bf16 v[20:23], v[60:63], v[216:223], v9\n\t"
    "s_waitcnt lgkmcnt(6)\n\t"
    "v_smfmac_f32_16x16x64_bf16 v[16:19], v[44:47], v[224:231], v9\n\t"
    "v_smfmac_f32_16x16x64_bf16 v[20:23], v[44:47], v[232:239], v9\n\t"
    "s_waitcnt lgkmcnt(2)\n\t"
    "v_smfmac_f32_16x16x64_bf16 v[16:19], v[40:43], v[24:31], v9\n\t"
    "s_waitcnt lgkmcnt(0)\n\t"
    "v_smfmac_f32_16x16x64_bf16 v[20:23], v[40:43], v[32:39], v9\n\t"
    "v_smfmac_f32_16x16x64_bf16 v[16:19], v[48:51], v[240:247], v9\n\t"
    "v_smfmac_f32_16x16x64_bf16 v[20:23], v[48:51], v[248:255], v9\n\t"
    "v_xor_b32 v7, 64, v12\n\t"
    "v_xor_b32 v8, 0x80, v12\n\t"
    "v_xor_b32 v6, 0xc0, v12\n\t"
    "ds_read_b128 v[24:27], v12 offset:16384\n\t"
    "ds_read_b128 v[28:31], v12 offset:20480\n\t"
    "ds_read_b128 v[32:35], v7 offset:16384\n\t"
    "ds_read_b128 v[36:39], v7 offset:20480\n\t"
    "v_permlane16_swap_b32 v16, v18\n\t"
    "v_permlane16_swap_b32 v17, v19\n\t"
    "v_permlane16_swap_b32 v20, v22\n\t"
    "v_permlane16_swap_b32 v21, v23\n\t"
    "v_add_f32 v16, v16, v18\n\t"
    "v_add_f32 v17, v17, v19\n\t"
    "v_add_f32 v20, v20, v22\n\t"
    "v_mul_f32 v16, %[lsc], v16\n\t"
    "v_mul_f32 v17, %[lsc], v17\n\t"
    "v_cvt_pk_bf16_f32 v16, v16, v17\n\t"
    "v_add_f32 v21, v21, v23\n\t"
    "ds_write_b32 v10, v16 offset:32768\n\t"
    "v_mul_f32 v20, %[lsc], v20\n\t"
    "v_mul_f32 v21, %[lsc], v21\n\t"
    "v_cvt_pk_bf16_f32 v20, v20, v21\n\t"
    "ds_write_b32 v10, v20 offset:33024\n\t"
    "ds_read_b128 v[40:43], v8 offset:16384\n\t"
    "ds_read_b128 v[44:47], v8 offset:20480\n\t"
    "ds_read_b128 v[48:51], v6 offset:16384\n\t"
    "ds_read_b128 v[52:55], v6 offset:20480\n\t"
    "s_waitcnt lgkmcnt(4)\n\t"
    "s_barrier\n\t"
    "ds_read_b64_tr_b16 v[16:17], v11 offset:32768\n\t"
    "ds_read_b64_tr_b16 v[18:19], v11 offset:32832\n\t"
    "ds_read_b64_tr_b16 v[20:21], v11 offset:33280\n\t"
    "ds_read_b64_tr_b16 v[22:23], v11 offset:33344\n\t"
    "ds_read_b64_tr_b16 v[56:57], v11 offset:33792\n\t"
    "ds_read_b64_tr_b16 v[58:59], v11 offset:33856\n\t"
    "ds_read_b64_tr_b16 v[60:61], v11 offset:34304\n\t"
    "ds_read_b64_tr_b16 v[62:63], v11 offset:34368\n\t"
    "v_mov_b32 v6, 0\n\t"
    "v_mov_b32 v7, 0\n\t"
    "v_mov_b32 v8, 0\n\t"
    "v_mov_b32 v9, 0\n\t"
    "s_mov_b32 exec_hi, 0\n\t"
    "global_load_dwordx2 v[6:7], v14, %[vs] sc1\n\t"
    "global_load_dwordx2 v[8:9], v14, %[vs] offset:256 sc1\n\t"
    "s_mov_b32 exec_hi, -1\n\t"
    "s_waitcnt lgkmcnt(6)\n\t"
    "v_mfma_f32_16x16x32_bf16 v[24:27], v[24:27], v[16:19], 0\n\t"
    "v_mfma_f32_16x16x32_bf16 v[28:31], v[28:31], v[16:19], 0\n\t"
    "s_waitcnt lgkmcnt(4)\n\t"
    "v_mfma_f32_16x16x32_bf16 v[24:27], v[32:35], v[20:23], v[24:27]\n\t"
    "v_mfma_f32_16x16x32_bf16 v[28:31], v[36:39], v[20:23], v[28:31]\n\t"
    "v_xor_b32 v12, 32, v13\n\t"
    "ds_read_b64_tr_b16 v[32:33], v13 offset:24576\n\t"
    "ds_read_b64_tr_b16 v[34:35], v13 offset:28672\n\t"
    "ds_read_b64_tr_b16 v[36:37], v12 offset:24576\n\t"
    "ds_read_b64_tr_b16 v[38:39], v12 offset:28672\n\t"
    "s_waitcnt lgkmcnt(6)\n\t"
    "v_mfma_f32_16x16x32_bf16 v[24:27], v[40:43], v[56:59], v[24:27]\n\t"
    "v_mfma_f32_16x16x32_bf16 v[28:31], v[44:47], v[56:59], v[28:31]\n\t"
    "s_waitcnt lgkmcnt(4)\n\t"
    "v_mfma_f32_16x16x32_bf16 v[24:27], v[48:51], v[60:63], v[24:27]\n\t"
    "v_mfma_f32_16x16x32_bf16 v[28:31], v[52:55], v[60:63], v[28:31]\n\t"
    "s_setprio 1\n\t"
    "v_mov_b32 v52, %[one]\n\t"
    "v_mov_b32 v53, %[one]\n\t"
    "v_mov_b32 v54, %[one]\n\t"
    "v_mov_b32 v55, %[one]\n\t"
    "s_nop 2\n\t"
    "v_max3_f32 v10, |v24|, |v25|, |v26|\n\t"
    "v_max3_f32 v40, |v27|, |v28|, |v29|\n\t"
    "v_max3_f32 v10, v10, |v30|, |v31|\n\t"
    "v_max_f32 v10, v10, v40\n\t"
    "v_cmp_nge_f32 vcc, %[big], v10\n\t"
    "s_mov_b32 %[bg], %[big]\n\t"
    "v_mov_b32 v10, 0\n\t"
    "s_cbranch_vccnz 12f\n\t"
    "13:\n\t"
    "v_exp_f32 v24, v24\n\t"
    "v_exp_f32 v25, v25\n\t"
    "v_exp_f32 v26, v26\n\t"
    "v_exp_f32 v27, v27\n\t"
    "v_exp_f32 v28, v28\n\t"
    "v_exp_f32 v29, v29\n\t"
    "v_exp_f32 v30, v30\n\t"
    "v_exp_f32 v31, v31\n\t"
    "v_cvt_pk_bf16_f32 v40, v24, v25\n\t"
    "v_cvt_pk_bf16_f32 v41, v26, v27\n\t"
    "v_cvt_pk_bf16_f32 v42, v28, v29\n\t"
    "v_cvt_pk_bf16_f32 v43, v30, v31\n\t"
    "s_setprio 3\n\t"
    "s_waitcnt lgkmcnt(0)\n\t"
    "s_nop 0\n\t"
    "v_mfma_f32_16x16x32_bf16 v[44:47], v[32:35], v[40:43], 0\n\t"
    "v_mfma_f32_16x16x32_bf16 v[48:51], v[36:39], v[40:43], 0\n\t"
    "v_mfma_f32_16x16x32_bf16 v[52:55], v[52:55], v[40:43], 0\n\t"
    "v_mov_b32 v12, 0\n\t"
    "v_mov_b32 v13, 0\n\t"
    "s_mov_b32 exec_hi, 0\n\t"
    "v_mov_b32 v12, %[one]\n\t"
    "v_mov_b32 v13, %[one]\n\t"
    "s_mov_b32 exec_hi, -1\n\t"
    "s_barrier\n\t"
    "ds_read_b64_tr_b16 v[24:25], v11 offset:34816\n\t"
    "ds_read_b64_tr_b16 v[26:27], v11 offset:34880\n\t"
    "ds_read_b64_tr_b16 v[28:29], v11 offset:35328\n\t"
    "ds_read_b64_tr_b16 v[30:31], v11 offset:35392\n\t"
    "ds_read_b64_tr_b16 v[32:33], v11 offset:35840\n\t"
    "ds_read_b64_tr_b16 v[34:35], v11 offset:35904\n\t"
    "ds_read_b64_tr_b16 v[36:37], v11 offset:36352\n\t"
    "ds_read_b64_tr_b16 v[38:39], v11 offset:36416\n\t"
    "s_waitcnt lgkmcnt(6)\n\t"
    "v_mfma_f32_16x16x32_bf16 v[40:43], v[24:27], v[16:19], 0\n\t"
    "s_waitcnt lgkmcnt(4)\n\t"
    "v_mfma_f32_16x16x32_bf16 v[40:43], v[28:31], v[20:23], v[40:43]\n\t"
    "s_waitcnt lgkmcnt(2)\n\t"
    "v_mfma_f32_16x16x32_bf16 v[40:43], v[32:35], v[56:59], v[40:43]\n\t"
    "s_waitcnt lgkmcnt(0)\n\t"
    "v_mfma_f32_16x16x32_bf16 v[40:43], v[36:39], v[60:63], v[40:43]\n\t"
    "5:\n\t"
    "s_waitcnt vmcnt(0)\n\t"
    "v_max3_u32 v11, v6, v7, v8\n\t"
    "v_max_u32 v11, v11, v9\n\t"
    "v_cmp_eq_u32 vcc, -1, v11\n\t"
    "s_cbranch_vccnz 4f\n\t"
    "6:\n\t"
    "s_nop 2\n\t"
    "v_exp_f32 v28, v40\n\t"
    "v_max3_f32 v11, v40, v41, v42\n\t"
    "v_exp_f32 v29, v41\n\t"
    "v_max_f32 v11, v11, v43\n\t"
    "v_exp_f32 v30, v42\n\t"
    "v_cmp_nge_f32 vcc, %[bg], v11\n\t"
    "v_exp_f32 v31, v43\n\t"
    "s_cbranch_vccnz 10f\n\t"
    "11:\n\t"
    "v_cvt_pk_bf16_f32 v24, v28, v29\n\t"
    "v_cvt_pk_bf16_f32 v25, v30, v31\n\t"
    "s_nop 1\n\t"
    "v_mfma_f32_16x16x16_bf16 v[52:55], v[12:13], v[24:25], v[52:55]\n\t"
    "v_mfma_f32_16x16x16_bf16 v[44:47], v[6:7], v[24:25], v[44:47]\n\t"
    "v_mfma_f32_16x16x16_bf16 v[48:51], v[8:9], v[24:25], v[48:51]\n\t"
    "s_nop 5\n\t"
    "v_rcp_f32 v26, v52\n\t"
    "s_nop 0\n\t"
    "v_pk_mul_f32 v[44:45], v[44:45], v[26:27] op_sel_hi:[1,0]\n\t"
    "v_pk_mul_f32 v[46:47], v[46:47], v[26:27] op_sel_hi:[1,0]\n\t"
    "v_pk_mul_f32 v[48:49], v[48:49], v[26:27] op_sel_hi:[1,0]\n\t"
    "v_pk_mul_f32 v[50:51], v[50:51], v[26:27] op_sel_hi:[1,0]\n\t"
    "v_cvt_pk_bf16_f32 v36, v44, v45\n\t"
    "v_cvt_pk_bf16_f32 v37, v46, v47\n\t"
    "v_cvt_pk_bf16_f32 v38, v48, v49\n\t"
    "v_cvt_pk_bf16_f32 v39, v50, v51\n\t"
    "v_min_u32 v36, -2, v36\n\t"
    "v_min_u32 v37, -2, v37\n\t"
    "v_min_u32 v38, -2, v38\n\t"
    "v_min_u32 v39, -2, v39\n\t"
    "global_store_dwordx4 v15, v[36:39], %[od] sc1\n\t"
    "s_barrier\n\t"
    "v_permlane16_swap_b32 v6, v8\n\t"
    "v_permlane16_swap_b32 v7, v9\n\t"
    "v_and_b32 v11, 8, v14\n\t"
    "v_mul_u32_u24 v11, 31, v11\n\t"
    "v_add_u32 v14, v14, v11\n\t"
    "s_mov_b32 exec_hi, 0\n\t"
    "global_store_dwordx4 v14, v[6:9], %[kv] sc1\n\t"
    "s_mov_b32 exec_hi, -1\n\t"
    "s_nop 0\n\t"
    "s_waitcnt lgkmcnt(0)\n\t"
    "s_branch 9f\n\t"
    "1:\n\t"
    "s_add_u32 %[sp], %[sp], 1\n\t"
    "s_cmp_ge_u32 %[sp], %[cap]\n\t"
    "s_cbranch_scc1 7f\n\t"
    "global_load_dwordx4 v[40:43], v6, %[ag] sc1\n\t"
    "global_load_dwordx4 v[44:47], v6, %[ag] offset:1024 sc1\n\t"
    "s_waitcnt lgkmcnt(0)\n\t"
    "s_memtime %[tc]\n\t"
    "s_nop 7\n\t"
    "s_branch 3b\n\t"
    "7:\n\t"
    "s_mov_b32 %[cap], 0\n\t"
    "s_branch 2b\n\t"
    "4:\n\t"
    "s_add_u32 %[sp], %[sp], 0x10000\n\t"
    "s_lshr_b32 %[st], %[sp], 16\n\t"
    "s_cmp_ge_u32 %[st], %[cap]\n\t"
    "s_cbranch_scc1 8f\n\t"
    "s_mov_b32 exec_hi, 0\n\t"
    "global_load_dwordx2 v[6:7], v14, %[vs] sc1\n\t"
    "global_load_dwordx2 v[8:9], v14, %[vs] offset:256 sc1\n\t"
    "s_mov_b32 exec_hi, -1\n\t"
    "s_branch 5b\n\t"
    "8:\n\t"
    "s_mov_b32 %[cap], 0\n\t"
    "s_branch 6b\n\t"
    "10:\n\t"
    "v_max_f32 v11, v11, v10\n\t"
    "v_mov_b32 v26, v11\n\t"
    "s_nop 1\n\t"
    "v_permlane16_swap_b32 v11, v26\n\t"
    "v_max_f32 v11, v11, v26\n\t"
    "v_sub_f32 v28, v40, v11\n\t"
    "v_sub_f32 v29, v41, v11\n\t"
    "v_sub_f32 v30, v42, v11\n\t"
    "v_sub_f32 v31, v43, v11\n\t"
    "v_sub_f32 v11, v10, v11\n\t"
    "v_exp_f32 v11, v11\n\t"
    "v_exp_f32 v28, v28\n\t"
    "v_exp_f32 v29, v29\n\t"
    "v_exp_f32 v30, v30\n\t"
    "v_exp_f32 v31, v31\n\t"
    "v_mul_f32 v44, v44, v11\n\t"
    "v_mul_f32 v45, v45, v11\n\t"
    "v_mul_f32 v46, v46, v11\n\t"
    "v_mul_f32 v47, v47, v11\n\t"
    "v_mul_f32 v48, v48, v11\n\t"
    "v_mul_f32 v49, v49, v11\n\t"
    "v_mul_f32 v50, v50, v11\n\t"
    "v_mul_f32 v51, v51, v11\n\t"
    "v_mul_f32 v52, v52, v11\n\t"
    "v_mul_f32 v53, v53, v11\n\t"
    "v_mul_f32 v54, v54, v11\n\t"
    "v_mul_f32 v55, v55, v11\n\t"
    "s_branch 11b\n\t"
    "12:\n\t"
    "v_max3_f32 v10, v24, v25, v26\n\t"
    "v_max3_f32 v40, v27, v28, v29\n\t"
    "v_max3_f32 v10, v10, v30, v31\n\t"
    "v_max3_f32 v10, v10, v40, %[neg]\n\t"
    "v_mov_b32 v40, v10\n\t"
    "s_nop 1\n\t"
    "v_permlane16_swap_b32 v10, v40\n\t"
    "v_max_f32 v10, v10, v40\n\t"
    "v_mov_b32 v40, v10\n\t"
    "s_nop 1\n\t"
    "v_permlane32_swap_b32 v10, v40\n\t"
    "v_max_f32 v10, v10, v40\n\t"
    "v_sub_f32 v24, v24, v10\n\t"
    "v_sub_f32 v25, v25, v10\n\t"
    "v_sub_f32 v26, v26, v10\n\t"
    "v_sub_f32 v27, v27, v10\n\t"
    "v_sub_f32 v28, v28, v10\n\t"
    "v_sub_f32 v29, v29, v10\n\t"
    "v_sub_f32 v30, v30, v10\n\t"
    "v_sub_f32 v31, v31, v10\n\t"
    "s_mov_b32 %[bg], 0x7fc00000\n\t"
    "s_branch 13b\n\t"
    "9:"
    : [tc] "=&s"(tc), [sp] "=&s"(sp), [cap] "+s"(cap), [st] "=&s"(st), [bg] "=&s"(bg)
    : [ag] "s"(ag), [c44] "s"(0x4444u), [kv] "s"(kv), [vs] "s"(vs), [od] "s"(od), [od2] "s"(od + 262144), [neg] "s"(-1.0e38f), [lsc] "s"(LSC), [one] "s"(0x3F803F80u), [big] "s"(24.0f), "{v[64:95]}"(W.w0), "{v[96:127]}"(W.w1), "{v[128:159]}"(W.w2), "{v[160:191]}"(W.w3), "{v[192:223]}"(W.w4), "{v[224:255]}"(W.w5)
    : "v6", "v7", "v8", "v9", "v10", "v11", "v12", "v13", "v14", "v15", "v16", "v17", "v18", "v19", "v20", "v21", "v22", "v23", "v24", "v25", "v26", "v27", "v28", "v29", "v30", "v31", "v32", "v33", "v34", "v35", "v36", "v37", "v38", "v39", "v40", "v41", "v42", "v43", "v44", "v45", "v46", "v47", "v48", "v49", "v50", "v51", "v52", "v53", "v54", "v55", "v56", "v57", "v58", "v59", "v60", "v61", "v62", "v63", "vcc", "scc", "memory");
}
template <> __device__ __forceinline__ void qk_step<1>(const QKW& W, const char* ag, const char* vs, const char* od, const char* kv, unsigned& cap, unsigned& sp,
                                                        unsigned long long& tc, unsigned long long* ts, unsigned& d0, unsigned& d1) {
  unsigned st, bg;
  asm volatile("v_mbcnt_lo_u32_b32 v60, -1, 0\n\t"
    "v_mbcnt_hi_u32_b32 v60, -1, v60\n\t"
    "v_lshlrev_b32 v6, 4, v60\n\t"
    "s_mov_b32 %[sp], 0\n\t"
    "s_setprio 3\n\t"
    "global_load_dwordx4 v[40:43], v6, %[ag] sc1\n\t"
    "global_load_dwordx4 v[44:47], v6, %[ag] offset:1024 sc1\n\t"
    "s_memtime %[tc]\n\t"
    "v_add_u32 v7, 43008, v6\n\t"
    "v_add_u32 v8, 100352, v6\n\t"
    "ds_read_b128 v[24:27], v7 offset:0\n\t"
    "ds_read_b128 v[28:31], v7 offset:1024\n\t"
    "ds_read_b128 v[32:35], v7 offset:8192\n\t"
    "ds_read_b128 v[36:39], v7 offset:9216\n\t"
    "v_lshrrev_b32 v48, 4, v60\n\t"
    "v_and_b32 v49, 15, v60\n\t"
    "v_and_b32 v61, 4, v60\n\t"
    "v_mul_u32_u24 v61, 0x2aaa, v61\n\t"
    "v_lshrrev_b32 v9, 1, v60\n\t"
    "v_and_b32 v9, 2, v9\n\t"
    "v_or3_b32 v9, v9, v61, %[c44]\n\t"
    "v_and_b32 v61, 1, v48\n\t"
    "v_lshlrev_b32 v61, 3, v61\n\t"
    "v_lshl_or_b32 v14, v49, 4, v61\n\t"
    "v_add_u32 v14, 512, v14\n\t"
    "v_lshlrev_b32 v61, 2, v48\n\t"
    "v_lshl_or_b32 v10, v49, 4, v61\n\t"
    "v_lshrrev_b32 v50, 2, v49\n\t"
    "v_and_b32 v51, 3, v60\n\t"
    "v_and_b32 v61, 1, v60\n\t"
    "v_lshlrev_b32 v61, 3, v61\n\t"
    "v_lshl_or_b32 v11, v50, 4, v61\n\t"
    "v_lshl_or_b32 v11, v48, 7, v11\n\t"
    "v_xor_b32 v12, v48, v49\n\t"
    "v_lshlrev_b32 v12, 4, v12\n\t"
    "v_lshl_or_b32 v12, v49, 8, v12\n\t"
    "v_lshl_or_b32 v50, v48, 2, v50\n\t"
    "v_lshrrev_b32 v51, 1, v51\n\t"
    "v_or_b32 v51, 4, v51\n\t"
    "v_xor_b32 v51, v51, v50\n\t"
    "v_lshl_or_b32 v13, v51, 4, v61\n\t"
    "v_lshl_or_b32 v13, v50, 8, v13\n\t"
    "v_and_b32 v61, 3, v60\n\t"
    "v_lshlrev_b32 v61, 4, v61\n\t"
    "v_lshl_or_b32 v15, v48, 6, v61\n\t"
    "v_and_b32 v61, 8, v60\n\t"
    "v_lshl_or_b32 v15, v61, 14, v15\n\t"
    "v_and_b32 v61, 4, v60\n\t"
    "v_lshl_or_b32 v15, v61, 8, v15\n\t"
    "v_add_u32 v15, 256, v15\n\t"
    "3:\n\t"
    "v_mov_b32 v16, 0\n\t"
    "v_mov_b32 v17, 0\n\t"
    "v_mov_b32 v18, 0\n\t"
    "v_mov_b32 v19, 0\n\t"
    "v_mov_b32 v20, 0\n\t"
    "v_mov_b32 v21, 0\n\t"
    "v_mov_b32 v22, 0\n\t"
    "v_mov_b32 v23, 0\n\t"
    "s_waitcnt vmcnt(0) lgkmcnt(0)\n\t"
    "14:\n\t"
    "ds_write_b128 v6, v[40:43] offset:2048\n\t"
    "ds_write_b128 v6, v[44:47] offset:3072\n\t"
    "v_smfmac_f32_16x16x64_bf16 v[16:19], v[40:43], v[96:103], v9\n\t"
    "v_max3_u32 v60, v40, v41, v42\n\t"
    "v_max3_u32 v61, v43, v44, v45\n\t"
    "v_smfmac_f32_16x16x64_bf16 v[20:23], v[40:43], v[104:111], v9\n\t"
    "v_max3_u32 v60, v60, v46, v47\n\t"
    "v_max_u32 v60, v60, v61\n\t"
    "v_cmp_eq_u32 vcc, -1, v60\n\t"
    "s_cbranch_vccnz 1f\n\t"
    "2:\n\t"
    "s_waitcnt lgkmcnt(0)\n\t"
    "s_barrier\n\t"
    "ds_read_b128 v[40:43], v6 offset:12288\n\t"
    "ds_read_b128 v[48:51], v6 offset:0\n\t"
    "v_smfmac_f32_16x16x64_bf16 v[16:19], v[44:47], v[112:119], v9\n\t"
    "ds_read_b128 v[52:55], v6 offset:1024\n\t"
    "ds_read_b128 v[56:59], v6 offset:4096\n\t"
    "v_smfmac_f32_16x16x64_bf16 v[20:23], v[44:47], v[120:127], v9\n\t"
    "ds_read_b128 v[60:63], v6 offset:13312\n\t"
    "ds_read_b128 v[44:47], v6 offset:5120\n\t"
    "s_waitcnt lgkmcnt(5)\n\t"
    "v_smfmac_f32_16x16x64_bf16 v[16:19], v[40:43], v[24:31], v9\n\t"
    "ds_read_b128 v[24:27], v7 offset:16384\n\t"
    "ds_read_b128 v[28:31], v7 offset:17408\n\t"
    "v_smfmac_f32_16x16x64_bf16 v[20:23], v[40:43], v[32:39], v9\n\t"
    "ds_read_b128 v[32:35], v7 offset:24576\n\t"
    "ds_read_b128 v[36:39], v7 offset:25600\n\t"
    "ds_read_b128 v[40:43], v6 offset:6144\n\t"
    "s_waitcnt lgkmcnt(9)\n\t"
    "v_smfmac_f32_16x16x64_bf16 v[16:19], v[48:51], v[64:71], v9\n\t"
    "v_smfmac_f32_16x16x64_bf16 v[20:23], v[48:51], v[72:79], v9\n\t"
    "ds_read_b128 v[48:51], v6 offset:7168\n\t"
    "s_waitcnt lgkmcnt(9)\n\t"
    "v_smfmac_f32_16x16x64_bf16 v[16:19], v[52:55], v[80:87], v9\n\t"
    "v_smfmac_f32_16x16x64_bf16 v[20:23], v[52:55], v[88:95], v9\n\t"
    "ds_read_b128 v[52:55], v6 offset:14336\n\t"
    "s_waitcnt lgkmcnt(9)\n\t"
    "v_smfmac_f32_16x16x64_bf16 v[16:19], v[56:59], v[128:135], v9\n\t"
    "v_smfmac_f32_16x16x64_bf16 v[20:23], v[56:59], v[136:143], v9\n\t"
    "ds_read_b128 v[56:59], v6 offset:8192\n\t"
    "s_waitcnt lgkmcnt(6)\n\t"
    "v_smfmac_f32_16x16x64_bf16 v[16:19], v[60:63], v[24:31], v9\n\t"
    "ds_read_b128 v[24:27], v8 offset:0\n\t"
    "ds_read_b128 v[28:31], v8 offset:1024\n\t"
    "s_waitcnt lgkmcnt(6)\n\t"
    "v_smfmac_f32_16x16x64_bf16 v[20:23], v[60:63], v[32:39], v9\n\t"
    "ds_read_b128 v[32:35], v8 offset:8192\n\t"
    "ds_read_b128 v[36:39], v8 offset:9216\n\t"
    "ds_read_b128 v[60:63], v6 offset:9216\n\t"
    "v_smfmac_f32_16x16x64_bf16 v[16:19], v[44:47], v[144:151], v9\n\t"
    "v_smfmac_f32_16x16x64_bf16 v[20:23], v[44:47], v[152:159], v9\n\t"
    "ds_read_b128 v[44:47], v6 offset:10240\n\t"
    "s_waitcnt lgkmcnt(9)\n\t"
    "v_smfmac_f32_16x16x64_bf16 v[16:19], v[40:43], v[160:167], v9\n\t"
    "v_smfmac_f32_16x16x64_bf16 v[20:23], v[40:43], v[168:175], v9\n\t"
    "ds_read_b128 v[40:43], v6 offset:15360\n\t"
    "s_waitcnt lgkmcnt(9)\n\t"
    "v_smfmac_f32_16x16x64_bf16 v[16:19], v[48:51], v[176:183], v9\n\t"
    "v_smfmac_f32_16x16x64_bf16 v[20:23], v[48:51], v[184:191], v9\n\t"
    "ds_read_b128 v[48:51], v6 offset:11264\n\t"
    "s_waitcnt lgkmcnt(6)\n\t"
    "v_smfmac_f32_16x16x64_bf16 v[16:19], v[52:55], v[24:31], v9\n\t"
    "ds_read_b128 v[24:27], v8 offset:16384\n\t"
    "ds_read_b128 v[28:31], v8 offset:17408\n\t"
    "s_waitcnt lgkmcnt(6)\n\t"
    "v_smfmac_f32_16x16x64_bf16 v[20:23], v[52:55], v[32:39], v9\n\t"
    "ds_read_b128 v[32:35], v8 offset:24576\n\t"
    "ds_read_b128 v[36:39], v8 offset:25600\n\t"
    "v_smfmac_f32_16x16x64_bf16 v[16:19], v[56:59], v[192:199], v9\n\t"
    "v_smfmac_f32_16x16x64_bf16 v[20:23], v[56:59], v[200:207], v9\n\t"
    "s_waitcnt lgkmcnt(7)\n\t"
    "v_smfmac_f32_16x16x64_bf16 v[16:19], v[60:63], v[208:215], v9\n\t"
    "v_smfmac_f32_16x16x64_bf16 v[20:23], v[60:63], v[216:223], v9\n\t"
    "s_waitcnt lgkmcnt(6)\n\t"
    "v_smfmac_f32_16x16x64_bf16 v[16:19], v[44:47], v[224:231], v9\n\t"
    "v_smfmac_f32_16x16x64_bf16 v[20:23], v[44:47], v[232:239], v9\n\t"
    "s_waitcnt lgkmcnt(2)\n\t"
    "v_smfmac_f32_16x16x64_bf16 v[16:19], v[40:43], v[24:31], v9\n\t"
    "s_waitcnt lgkmcnt(0)\n\t"
    "v_smfmac_f32_16x16x64_bf16 v[20:23], v[40:43], v[32:39], v9\n\t"
    "v_smfmac_f32_16x16x64_bf16 v[16:19], v[48:51], v[240:247], v9\n\t"
    "v_smfmac_f32_16x16x64_bf16 v[20:23], v[48:51], v[248:255], v9\n\t"
    "v_xor_b32 v7, 64, v12\n\t"
    "v_xor_b32 v8, 0x80, v12\n\t"
    "v_xor_b32 v6, 0xc0, v12\n\t"
    "ds_read_b128 v[24:27], v12 offset:16384\n\t"
    "ds_read_b128 v[28:31], v12 offset:20480\n\t"
    "ds_read_b128 v[32:35], v7 offset:16384\n\t"
    "ds_read_b128 v[36:39], v7 offset:20480\n\t"
    "v_permlane16_swap_b32 v16, v18\n\t"
    "v_permlane16_swap_b32 v17, v19\n\t"
    "v_permlane16_swap_b32 v20, v22\n\t"
    "v_permlane16_swap_b32 v21, v23\n\t"
    "v_add_f32 v16, v16, v18\n\t"
    "v_add_f32 v17, v17, v19\n\t"
    "v_add_f32 v20, v20, v22\n\t"
    "v_mul_f32 v16, %[lsc], v16\n\t"
    "v_mul_f32 v17, %[lsc], v17\n\t"
    "v_cvt_pk_bf16_f32 v16, v16, v17\n\t"
    "v_add_f32 v21, v21, v23\n\t"
    "ds_write_b32 v10, v16 offset:33280\n\t"
    "v_mul_f32 v20, %[lsc], v20\n\t"
    "v_mul_f32 v21, %[lsc], v21\n\t"
    "v_cvt_pk_bf16_f32 v20, v20, v21\n\t"
    "ds_write_b32 v10, v20 offset:33536\n\t"
    "ds_read_b128 v[40:43], v8 offset:16384\n\t"
    "ds_read_b128 v[44:47], v8 offset:20480\n\t"
    "ds_read_b128 v[48:51], v6 offset:16384\n\t"
    "ds_read_b128 v[52:55], v6 offset:20480\n\t"
    "s_waitcnt lgkmcnt(4)\n\t"
    "s_barrier\n\t"
    "ds_read_b64_tr_b16 v[16:17], v11 offset:32768\n\t"
    "ds_read_b64_tr_b16 v[18:19], v11 offset:32832\n\t"
    "ds_read_b64_tr_b16 v[20:21], v11 offset:33280\n\t"
    "ds_read_b64_tr_b16 v[22:23], v11 offset:33344\n\t"
    "ds_read_b64_tr_b16 v[56:57], v11 offset:33792\n\t"
    "ds_read_b64_tr_b16 v[58:59], v11 offset:33856\n\t"
    "ds_read_b64_tr_b16 v[60:61], v11 offset:34304\n\t"
    "ds_read_b64_tr_b16 v[62:63], v11 offset:34368\n\t"
    "v_mov_b32 v6, 0\n\t"
    "v_mov_b32 v7, 0\n\t"
    "v_mov_b32 v8, 0\n\t"
    "v_mov_b32 v9, 0\n\t"
    "s_mov_b32 exec_hi, 0\n\t"
    "global_load_dwordx2 v[6:7], v14, %[vs] sc1\n\t"
    "global_load_dwordx2 v[8:9], v14, %[vs] offset:256 sc1\n\t"
    "s_mov_b32 exec_hi, -1\n\t"
    "s_waitcnt lgkmcnt(6)\n\t"
    "v_mfma_f32_16x16x32_bf16 v[24:27], v[24:27], v[16:19], 0\n\t"
    "v_mfma_f32_16x16x32_bf16 v[28:31], v[28:31], v[16:19], 0\n\t"
    "s_waitcnt lgkmcnt(4)\n\t"
    "v_mfma_f32_16x16x32_bf16 v[24:27], v[32:35], v[20:23], v[24:27]\n\t"
    "v_mfma_f32_16x16x32_bf16 v[28:31], v[36:39], v[20:23], v[28:31]\n\t"
    "v_xor_b32 v12, 32, v13\n\t"
    "ds_read_b64_tr_b16 v[32:33], v13 offset:24576\n\t"
    "ds_read_b64_tr_b16 v[34:35], v13 offset:28672\n\t"
    "ds_read_b64_tr_b16 v[36:37], v12 offset:24576\n\t"
    "ds_read_b64_tr_b16 v[38:39], v12 offset:28672\n\t"
    "s_waitcnt lgkmcnt(6)\n\t"
    "v_mfma_f32_16x16x32_bf16 v[24:27], v[40:43], v[56:59], v[24:27]\n\t"
    "v_mfma_f32_16x16x32_bf16 v[28:31], v[44:47], v[56:59], v[28:31]\n\t"
    "s_waitcnt lgkmcnt(4)\n\t"
    "v_mfma_f32_16x16x32_bf16 v[24:27], v[48:51], v[60:63], v[24:27]\n\t"
    "v_mfma_f32_16x16x32_bf16 v[28:31], v[52:55], v[60:63], v[28:31]\n\t"
    "s_setprio 1\n\t"
    "v_mov_b32 v52, %[one]\n\t"
    "v_mov_b32 v53, %[one]\n\t"
    "v_mov_b32 v54, %[one]\n\t"
    "v_mov_b32 v55, %[one]\n\t"
    "s_nop 2\n\t"
    "v_max3_f32 v10, |v24|, |v25|, |v26|\n\t"
    "v_max3_f32 v40, |v27|, |v28|, |v29|\n\t"
    "v_max3_f32 v10, v10, |v30|, |v31|\n\t"
    "v_max_f32 v10, v10, v40\n\t"
    "v_cmp_nge_f32 vcc, %[big], v10\n\t"
    "s_mov_b32 %[bg], %[big]\n\t"
    "v_mov_b32 v10, 0\n\t"
    "s_cbranch_vccnz 12f\n\t"
    "13:\n\t"
    "v_exp_f32 v24, v24\n\t"
    "v_exp_f32 v25, v25\n\t"
    "v_exp_f32 v26, v26\n\t"
    "v_exp_f32 v27, v27\n\t"
    "v_exp_f32 v28, v28\n\t"
    "v_exp_f32 v29, v29\n\t"
    "v_exp_f32 v30, v30\n\t"
    "v_exp_f32 v31, v31\n\t"
    "v_cvt_pk_bf16_f32 v40, v24, v25\n\t"
    "v_cvt_pk_bf16_f32 v41, v26, v27\n\t"
    "v_cvt_pk_bf16_f32 v42, v28, v29\n\t"
    "v_cvt_pk_bf16_f32 v43, v30, v31\n\t"
    "s_setprio 3\n\t"
    "s_waitcnt lgkmcnt(0)\n\t"
    "s_nop 0\n\t"
    "v_mfma_f32_16x16x32_bf16 v[44:47], v[32:35], v[40:43], 0\n\t"
    "v_mfma_f32_16x16x32_bf16 v[48:51], v[36:39], v[40:43], 0\n\t"
    "v_mfma_f32_16x16x32_bf16 v[52:55], v[52:55], v[40:43], 0\n\t"
    "v_mov_b32 v12, 0\n\t"
    "v_mov_b32 v13, 0\n\t"
    "s_mov_b32 exec_hi, 0\n\t"
    "v_mov_b32 v12, %[one]\n\t"
    "v_mov_b32 v13, %[one]\n\t"
    "s_mov_b32 exec_hi, -1\n\t"
    "s_barrier\n\t"
    "ds_read_b64_tr_b16 v[24:25], v11 offset:34816\n\t"
    "ds_read_b64_tr_b16 v[26:27], v11 offset:34880\n\t"
    "ds_read_b64_tr_b16 v[28:29], v11 offset:35328\n\t"
    "ds_read_b64_tr_b16 v[30:31], v11 offset:35392\n\t"
    "ds_read_b64_tr_b16 v[32:33], v11 offset:35840\n\t"
    "ds_read_b64_tr_b16 v[34:35], v11 offset:35904\n\t"
    "ds_read_b64_tr_b16 v[36:37], v11 offset:36352\n\t"
    "ds_read_b64_tr_b16 v[38:39], v11 offset:36416\n\t"
    "s_waitcnt lgkmcnt(6)\n\t"
    "v_mfma_f32_16x16x32_bf16 v[40:43], v[24:27], v[16:19], 0\n\t"
    "s_waitcnt lgkmcnt(4)\n\t"
    "v_mfma_f32_16x16x32_bf16 v[40:43], v[28:31], v[20:23], v[40:43]\n\t"
    "s_waitcnt lgkmcnt(2)\n\t"
    "v_mfma_f32_16x16x32_bf16 v[40:43], v[32:35], v[56:59], v[40:43]\n\t"
    "s_waitcnt lgkmcnt(0)\n\t"
    "v_mfma_f32_16x16x32_bf16 v[40:43], v[36:39], v[60:63], v[40:43]\n\t"
    "5:\n\t"
    "s_waitcnt vmcnt(0)\n\t"
    "v_max3_u32 v11, v6, v7, v8\n\t"
    "v_max_u32 v11, v11, v9\n\t"
    "v_cmp_eq_u32 vcc, -1, v11\n\t"
    "s_cbranch_vccnz 4f\n\t"
    "6:\n\t"
    "s_nop 2\n\t"
    "v_exp_f32 v28, v40\n\t"
    "v_max3_f32 v11, v40, v41, v42\n\t"
    "v_exp_f32 v29, v41\n\t"
    "v_max_f32 v11, v11, v43\n\t"
    "v_exp_f32 v30, v42\n\t"
    "v_cmp_nge_f32 vcc, %[bg], v11\n\t"
    "v_exp_f32 v31, v43\n\t"
    "s_cbranch_vccnz 10f\n\t"
    "11:\n\t"
    "v_cvt_pk_bf16_f32 v24, v28, v29\n\t"
    "v_cvt_pk_bf16_f32 v25, v30, v31\n\t"
    "s_nop 1\n\t"
    "v_mfma_f32_16x16x16_bf16 v[52:55], v[12:13], v[24:25], v[52:55]\n\t"
    "v_mfma_f32_16x16x16_bf16 v[44:47], v[6:7], v[24:25], v[44:47]\n\t"
    "v_mfma_f32_16x16x16_bf16 v[48:51], v[8:9], v[24:25], v[48:51]\n\t"
    "s_nop 5\n\t"
    "v_rcp_f32 v26, v52\n\t"
    "s_nop 0\n\t"
    "v_pk_mul_f32 v[44:45], v[44:45], v[26:27] op_sel_hi:[1,0]\n\t"
    "v_pk_mul_f32 v[46:47], v[46:47], v[26:27] op_sel_hi:[1,0]\n\t"
    "v_pk_mul_f32 v[48:49], v[48:49], v[26:27] op_sel_hi:[1,0]\n\t"
    "v_pk_mul_f32 v[50:51], v[50:51], v[26:27] op_sel_hi:[1,0]\n\t"
    "v_cvt_pk_bf16_f32 v36, v44, v45\n\t"
    "v_cvt_pk_bf16_f32 v37, v46, v47\n\t"
    "v_cvt_pk_bf16_f32 v38, v48, v49\n\t"
    "v_cvt_pk_bf16_f32 v39, v50, v51\n\t"
    "v_min_u32 v36, -2, v36\n\t"
    "v_min_u32 v37, -2, v37\n\t"
    "v_min_u32 v38, -2, v38\n\t"
    "v_min_u32 v39, -2, v39\n\t"
    "global_store_dwordx4 v15, v[36:39], %[od] sc1\n\t"
    "s_barrier\n\t"
    "v_permlane16_swap_b32 v6, v8\n\t"
    "v_permlane16_swap_b32 v7, v9\n\t"
    "v_and_b32 v11, 8, v14\n\t"
    "v_mul_u32_u24 v11, 31, v11\n\t"
    "v_add_u32 v14, v14, v11\n\t"
    "s_mov_b32 exec_hi, 0\n\t"
    "global_store_dwordx4 v14, v[6:9], %[kv] sc1\n\t"
    "s_mov_b32 exec_hi, -1\n\t"
    "s_nop 0\n\t"
    "s_waitcnt lgkmcnt(0)\n\t"
    "s_branch 9f\n\t"
    "1:\n\t"
    "s_add_u32 %[sp], %[sp], 1\n\t"
    "s_cmp_ge_u32 %[sp], %[cap]\n\t"
    "s_cbranch_scc1 7f\n\t"
    "global_load_dwordx4 v[40:43], v6, %[ag] sc1\n\t"
    "global_load_dwordx4 v[44:47], v6, %[ag] offset:1024 sc1\n\t"
    "s_waitcnt lgkmcnt(0)\n\t"
    "s_memtime %[tc]\n\t"
    "s_nop 7\n\t"
    "s_branch 3b\n\t"
    "7:\n\t"
    "s_mov_b32 %[cap], 0\n\t"
    "s_branch 2b\n\t"
    "4:\n\t"
    "s_add_u32 %[sp], %[sp], 0x10000\n\t"
    "s_lshr_b32 %[st], %[sp], 16\n\t"
    "s_cmp_ge_u32 %[st], %[cap]\n\t"
    "s_cbranch_scc1 8f\n\t"
    "s_mov_b32 exec_hi, 0\n\t"
    "global_load_dwordx2 v[6:7], v14, %[vs] sc1\n\t"
    "global_load_dwordx2 v[8:9], v14, %[vs] offset:256 sc1\n\t"
    "s_mov_b32 exec_hi, -1\n\t"
    "s_branch 5b\n\t"
    "8:\n\t"
    "s_mov_b32 %[cap], 0\n\t"
    "s_branch 6b\n\t"
    "10:\n\t"
    "v_max_f32 v11, v11, v10\n\t"
    "v_mov_b32 v26, v11\n\t"
    "s_nop 1\n\t"
    "v_permlane16_swap_b32 v11, v26\n\t"
    "v_max_f32 v11, v11, v26\n\t"
    "v_sub_f32 v28, v40, v11\n\t"
    "v_sub_f32 v29, v41, v11\n\t"
    "v_sub_f32 v30, v42, v11\n\t"
    "v_sub_f32 v31, v43, v11\n\t"
    "v_sub_f32 v11, v10, v11\n\t"
    "v_exp_f32 v11, v11\n\t"
    "v_exp_f32 v28, v28\n\t"
    "v_exp_f32 v29, v29\n\t"
    "v_exp_f32 v30, v30\n\t"
    "v_exp_f32 v31, v31\n\t"
    "v_mul_f32 v44, v44, v11\n\t"
    "v_mul_f32 v45, v45, v11\n\t"
    "v_mul_f32 v46, v46, v11\n\t"
    "v_mul_f32 v47, v47, v11\n\t"
    "v_mul_f32 v48, v48, v11\n\t"
    "v_mul_f32 v49, v49, v11\n\t"
    "v_mul_f32 v50, v50, v11\n\t"
    "v_mul_f32 v51, v51, v11\n\t"
    "v_mul_f32 v52, v52, v11\n\t"
    "v_mul_f32 v53, v53, v11\n\t"
    "v_mul_f32 v54, v54, v11\n\t"
    "v_mul_f32 v55, v55, v11\n\t"
    "s_branch 11b\n\t"
    "12:\n\t"
    "v_max3_f32 v10, v24, v25, v26\n\t"
    "v_max3_f32 v40, v27, v28, v29\n\t"
    "v_max3_f32 v10, v10, v30, v31\n\t"
    "v_max3_f32 v10, v10, v40, %[neg]\n\t"
    "v_mov_b32 v40, v10\n\t"
    "s_nop 1\n\t"
    "v_permlane16_swap_b32 v10, v40\n\t"
    "v_max_f32 v10, v10, v40\n\t"
    "v_mov_b32 v40, v10\n\t"
    "s_nop 1\n\t"
    "v_permlane32_swap_b32 v10, v40\n\t"
    "v_max_f32 v10, v10, v40\n\t"
    "v_sub_f32 v24, v24, v10\n\t"
    "v_sub_f32 v25, v25, v10\n\t"
    "v_sub_f32 v26, v26, v10\n\t"
    "v_sub_f32 v27, v27, v10\n\t"
    "v_sub_f32 v28, v28, v10\n\t"
    "v_sub_f32 v29, v29, v10\n\t"
    "v_sub_f32 v30, v30, v10\n\t"
    "v_sub_f32 v31, v31, v10\n\t"
    "s_mov_b32 %[bg], 0x7fc00000\n\t"
    "s_branch 13b\n\t"
    "9:"
    : [tc] "=&s"(tc), [sp] "=&s"(sp), [cap] "+s"(cap), [st] "=&s"(st), [bg] "=&s"(bg)
    : [ag] "s"(ag), [c44] "s"(0x4444u), [kv] "s"(kv), [vs] "s"(vs), [od] "s"(od), [od2] "s"(od + 262144), [neg] "s"(-1.0e38f), [lsc] "s"(LSC), [one] "s"(0x3F803F80u), [big] "s"(24.0f), "{v[64:95]}"(W.w0), "{v[96:127]}"(W.w1), "{v[128:159]}"(W.w2), "{v[160:191]}"(W.w3), "{v[192:223]}"(W.w4), "{v[224:255]}"(W.w5)
    : "v6", "v7", "v8", "v9", "v10", "v11", "v12", "v13", "v14", "v15", "v16", "v17", "v18", "v19", "v20", "v21", "v22", "v23", "v24", "v25", "v26", "v27", "v28", "v29", "v30", "v31", "v32", "v33", "v34", "v35", "v36", "v37", "v38", "v39", "v40", "v41", "v42", "v43", "v44", "v45", "v46", "v47", "v48", "v49", "v50", "v51", "v52", "v53", "v54", "v55", "v56", "v57", "v58", "v59", "v60", "v61", "v62", "v63", "vcc", "scc", "memory");
}
template <> __device__ __forceinline__ void qk_step<2>(const QKW& W, const char* ag, const char* vs, const char* od, const char* kv, unsigned& cap, unsigned& sp,
                                                        unsigned long long& tc, unsigned long long* ts, unsigned& d0, unsigned& d1) {
  unsigned st, bg;
  asm volatile("v_mbcnt_lo_u32_b32 v60, -1, 0\n\t"
    "v_mbcnt_hi_u32_b32 v60, -1, v60\n\t"
    "v_lshlrev_b32 v6, 4, v60\n\t"
    "s_mov_b32 %[sp], 0\n\t"
    "s_setprio 3\n\t"
    "global_load_dwordx4 v[40:43], v6, %[ag] sc1\n\t"
    "global_load_dwordx4 v[44:47], v6, %[ag] offset:1024 sc1\n\t"
    "s_memtime %[tc]\n\t"
    "v_add_u32 v7, 45056, v6\n\t"
    "v_add_u32 v8, 102400, v6\n\t"
    "ds_read_b128 v[24:27], v7 offset:0\n\t"
    "ds_read_b128 v[28:31], v7 offset:1024\n\t"
    "ds_read_b128 v[32:35], v7 offset:8192\n\t"
    "ds_read_b128 v[36:39], v7 offset:9216\n\t"
    "v_lshrrev_b32 v48, 4, v60\n\t"
    "v_and_b32 v49, 15, v60\n\t"
    "v_and_b32 v61, 4, v60\n\t"
    "v_mul_u32_u24 v61, 0x2aaa, v61\n\t"
    "v_lshrrev_b32 v9, 1, v60\n\t"
    "v_and_b32 v9, 2, v9\n\t"
    "v_or3_b32 v9, v9, v61, %[c44]\n\t"
    "v_and_b32 v61, 1, v48\n\t"
    "v_lshlrev_b32 v61, 3, v61\n\t"
    "v_lshl_or_b32 v14, v49, 4, v61\n\t"
    "v_add_u32 v14, 1024, v14\n\t"
    "v_lshlrev_b32 v61, 2, v48\n\t"
    "v_lshl_or_b32 v10, v49, 4, v61\n\t"
    "v_lshrrev_b32 v50, 2, v49\n\t"
    "v_and_b32 v51, 3, v60\n\t"
    "v_and_b32 v61, 1, v60\n\t"
    "v_lshlrev_b32 v61, 3, v61\n\t"
    "v_lshl_or_b32 v11, v50, 4, v61\n\t"
    "v_lshl_or_b32 v11, v48, 7, v11\n\t"
    "v_xor_b32 v12, v48, v49\n\t"
    "v_lshlrev_b32 v12, 4, v12\n\t"
    "v_lshl_or_b32 v12, v49, 8, v12\n\t"
    "v_lshl_or_b32 v50, v48, 2, v50\n\t"
    "v_lshrrev_b32 v51, 1, v51\n\t"
    "v_or_b32 v51, 8, v51\n\t"
    "v_xor_b32 v51, v51, v50\n\t"
    "v_lshl_or_b32 v13, v51, 4, v61\n\t"
    "v_lshl_or_b32 v13, v50, 8, v13\n\t"
    "v_and_b32 v61, 3, v60\n\t"
    "v_lshlrev_b32 v61, 4, v61\n\t"
    "v_lshl_or_b32 v15, v48, 6, v61\n\t"
    "v_and_b32 v61, 8, v60\n\t"
    "v_lshl_or_b32 v15, v61, 14, v15\n\t"
    "v_and_b32 v61, 4, v60\n\t"
    "v_lshl_or_b32 v15, v61, 8, v15\n\t"
    "v_add_u32 v15, 512, v15\n\t"
    "3:\n\t"
    "v_mov_b32 v16, 0\n\t"
    "v_mov_b32 v17, 0\n\t"
    "v_mov_b32 v18, 0\n\t"
    "v_mov_b32 v19, 0\n\t"
    "v_mov_b32 v20, 0\n\t"
    "v_mov_b32 v21, 0\n\t"
    "v_mov_b32 v22, 0\n\t"
    "v_mov_b32 v23, 0\n\t"
    "s_waitcnt vmcnt(0) lgkmcnt(0)\n\t"
    "14:\n\t"
    "ds_write_b128 v6, v[40:43] offset:4096\n\t"
    "ds_write_b128 v6, v[44:47] offset:5120\n\t"
    "v_smfmac_f32_16x16x64_bf16 v[16:19], v[40:43], v[128:135], v9\n\t"
    "v_max3_u32 v60, v40, v41, v42\n\t"
    "v_max3_u32 v61, v43, v44, v45\n\t"
    "v_smfmac_f32_16x16x64_bf16 v[20:23], v[40:43], v[136:143], v9\n\t"
    "v_max3_u32 v60, v60, v46, v47\n\t"
    "v_max_u32 v60, v60, v61\n\t"
    "v_cmp_eq_u32 vcc, -1, v60\n\t"
    "s_cbranch_vccnz 1f\n\t"
    "2:\n\t"
    "s_waitcnt lgkmcnt(0)\n\t"
    "s_barrier\n\t"
    "ds_read_b128 v[40:43], v6 offset:12288\n\t"
    "ds_read_b128 v[48:51], v6 offset:0\n\t"
    "v_smfmac_f32_16x16x64_bf16 v[16:19], v[44:47], v[144:151], v9\n\t"
    "ds_read_b128 v[52:55], v6 offset:1024\n\t"
    "ds_read_b128 v[56:59], v6 offset:2048\n\t"
    "v_smfmac_f32_16x16x64_bf16 v[20:23], v[44:47], v[152:159], v9\n\t"
    "ds_read_b128 v[60:63], v6 offset:13312\n\t"
    "ds_read_b128 v[44:47], v6 offset:3072\n\t"
    "s_waitcnt lgkmcnt(5)\n\t"
    "v_smfmac_f32_16x16x64_bf16 v[16:19], v[40:43], v[24:31], v9\n\t"
    "ds_read_b128 v[24:27], v7 offset:16384\n\t"
    "ds_read_b128 v[28:31], v7 offset:17408\n\t"
    "v_smfmac_f32_16x16x64_bf16 v[20:23], v[40:43], v[32:39], v9\n\t"
    "ds_read_b128 v[32:35], v7 offset:24576\n\t"
    "ds_read_b128 v[36:39], v7 offset:25600\n\t"
    "ds_read_b128 v[40:43], v6 offset:6144\n\t"
    "s_waitcnt lgkmcnt(9)\n\t"
    "v_smfmac_f32_16x16x64_bf16 v[16:19], v[48:51], v[64:71], v9\n\t"
    "v_smfmac_f32_16x16x64_bf16 v[20:23], v[48:51], v[72:79], v9\n\t"
    "ds_read_b128 v[48:51], v6 offset:7168\n\t"
    "s_waitcnt lgkmcnt(9)\n\t"
    "v_smfmac_f32_16x16x64_bf16 v[16:19], v[52:55], v[80:87], v9\n\t"
    "v_smfmac_f32_16x16x64_bf16 v[20:23], v[52:55], v[88:95], v9\n\t"
    "ds_read_b128 v[52:55], v6 offset:14336\n\t"
    "s_waitcnt lgkmcnt(9)\n\t"
    "v_smfmac_f32_16x16x64_bf16 v[16:19], v[56:59], v[96:103], v9\n\t"
    "v_smfmac_f32_16x16x64_bf16 v[20:23], v[56:59], v[104:111], v9\n\t"
    "ds_read_b128 v[56:59], v6 offset:8192\n\t"
    "s_waitcnt lgkmcnt(6)\n\t"
    "v_smfmac_f32_16x16x64_bf16 v[16:19], v[60:63], v[24:31], v9\n\t"
    "ds_read_b128 v[24:27], v8 offset:0\n\t"
    "ds_read_b128 v[28:31], v8 offset:1024\n\t"
    "s_waitcnt lgkmcnt(6)\n\t"
    "v_smfmac_f32_16x16x64_bf16 v[20:23], v[60:63], v[32:39], v9\n\t"
    "ds_read_b128 v[32:35], v8 offset:8192\n\t"
    "ds_read_b128 v[36:39], v8 offset:9216\n\t"
    "ds_read_b128 v[60:63], v6 offset:9216\n\t"
    "v_smfmac_f32_16x16x64_bf16 v[16:19], v[44:47], v[112:119], v9\n\t"
    "v_smfmac_f32_16x16x64_bf16 v[20:23], v[44:47], v[120:127], v9\n\t"
    "ds_read_b128 v[44:47], v6 offset:10240\n\t"
    "s_waitcnt lgkmcnt(9)\n\t"
    "v_smfmac_f32_16x16x64_bf16 v[16:19], v[40:43], v[160:167], v9\n\t"
    "v_smfmac_f32_16x16x64_bf16 v[20:23], v[40:43], v[168:175], v9\n\t"
    "ds_read_b128 v[40:43], v6 offset:15360\n\t"
    "s_waitcnt lgkmcnt(9)\n\t"
    "v_smfmac_f32_16x16x64_bf16 v[16:19], v[48:51], v[176:183], v9\n\t"
    "v_smfmac_f32_16x16x64_bf16 v[20:23], v[48:51], v[184:191], v9\n\t"
    "ds_read_b128 v[48:51], v6 offset:11264\n\t"
    "s_waitcnt lgkmcnt(6)\n\t"
    "v_smfmac_f32_16x16x64_bf16 v[16:19], v[52:55], v[24:31], v9\n\t"
    "ds_read_b128 v[24:27], v8 offset:16384\n\t"
    "ds_read_b128 v[28:31], v8 offset:17408\n\t"
    "s_waitcnt lgkmcnt(6)\n\t"
    "v_smfmac_f32_16x16x64_bf16 v[20:23], v[52:55], v[32:39], v9\n\t"
    "ds_read_b128 v[32:35], v8 offset:24576\n\t"
    "ds_read_b128 v[36:39], v8 offset:25600\n\t"
    "v_smfmac_f32_16x16x64_bf16 v[16:19], v[56:59], v[192:199], v9\n\t"
    "v_smfmac_f32_16x16x64_bf16 v[20:23], v[56:59], v[200:207], v9\n\t"
    "s_waitcnt lgkmcnt(7)\n\t"
    "v_smfmac_f32_16x16x64_bf16 v[16:19], v[60:63], v[208:215], v9\n\t"
    "v_smfmac_f32_16x16x64_bf16 v[20:23], v[60:63], v[216:223], v9\n\t"
    "s_waitcnt lgkmcnt(6)\n\t"
    "v_smfmac_f32_16x16x64_bf16 v[16:19], v[44:47], v[224:231], v9\n\t"
    "v_smfmac_f32_16x16x64_bf16 v[20:23], v[44:47], v[232:239], v9\n\t"
    "s_waitcnt lgkmcnt(2)\n\t"
    "v_smfmac_f32_16x16x64_bf16 v[16:19], v[40:43], v[24:31], v9\n\t"
    "s_waitcnt lgkmcnt(0)\n\t"
    "v_smfmac_f32_16x16x64_bf16 v[20:23], v[40:43], v[32:39], v9\n\t"
    "v_smfmac_f32_16x16x64_bf16 v[16:19], v[48:51], v[240:247], v9\n\t"
    "v_smfmac_f32_16x16x64_bf16 v[20:23], v[48:51], v[248:255], v9\n\t"
    "v_xor_b32 v7, 64, v12\n\t"
    "v_xor_b32 v8, 0x80, v12\n\t"
    "v_xor_b32 v6, 0xc0, v12\n\t"
    "ds_read_b128 v[24:27], v12 offset:16384\n\t"
    "ds_read_b128 v[28:31], v12 offset:20480\n\t"
    "ds_read_b128 v[32:35], v7 offset:16384\n\t"
    "ds_read_b128 v[36:39], v7 offset:20480\n\t"
    "v_permlane16_swap_b32 v16, v18\n\t"
    "v_permlane16_swap_b32 v17, v19\n\t"
    "v_permlane16_swap_b32 v20, v22\n\t"
    "v_permlane16_swap_b32 v21, v23\n\t"
    "v_add_f32 v16, v16, v18\n\t"
    "v_add_f32 v17, v17, v19\n\t"
    "v_add_f32 v20, v20, v22\n\t"
    "v_mul_f32 v16, %[lsc], v16\n\t"
    "v_mul_f32 v17, %[lsc], v17\n\t"
    "v_cvt_pk_bf16_f32 v16, v16, v17\n\t"
    "v_add_f32 v21, v21, v23\n\t"
    "ds_write_b32 v10, v16 offset:33792\n\t"
    "v_mul_f32 v20, %[lsc], v20\n\t"
    "v_mul_f32 v21, %[lsc], v21\n\t"
    "v_cvt_pk_bf16_f32 v20, v20, v21\n\t"
    "ds_write_b32 v10, v20 offset:34048\n\t"
    "ds_read_b128 v[40:43], v8 offset:16384\n\t"
    "ds_read_b128 v[44:47], v8 offset:20480\n\t"
    "ds_read_b128 v[48:51], v6 offset:16384\n\t"
    "ds_read_b128 v[52:55], v6 offset:20480\n\t"
    "s_waitcnt lgkmcnt(4)\n\t"
    "s_barrier\n\t"
    "ds_read_b64_tr_b16 v[16:17], v11 offset:32768\n\t"
    "ds_read_b64_tr_b16 v[18:19], v11 offset:32832\n\t"
    "ds_read_b64_tr_b16 v[20:21], v11 offset:33280\n\t"
    "ds_read_b64_tr_b16 v[22:23], v11 offset:33344\n\t"
    "ds_read_b64_tr_b16 v[56:57], v11 offset:33792\n\t"
    "ds_read_b64_tr_b16 v[58:59], v11 offset:33856\n\t"
    "ds_read_b64_tr_b16 v[60:61], v11 offset:34304\n\t"
    "ds_read_b64_tr_b16 v[62:63], v11 offset:34368\n\t"
    "v_mov_b32 v6, 0\n\t"
    "v_mov_b32 v7, 0\n\t"
    "v_mov_b32 v8, 0\n\t"
    "v_mov_b32 v9, 0\n\t"
    "s_mov_b32 exec_hi, 0\n\t"
    "global_load_dwordx2 v[6:7], v14, %[vs] sc1\n\t"
    "global_load_dwordx2 v[8:9], v14, %[vs] offset:256 sc1\n\t"
    "s_mov_b32 exec_hi, -1\n\t"
    "s_waitcnt lgkmcnt(6)\n\t"
    "v_mfma_f32_16x16x32_bf16 v[24:27], v[24:27], v[16:19], 0\n\t"
    "v_mfma_f32_16x16x32_bf16 v[28:31], v[28:31], v[16:19], 0\n\t"
    "s_waitcnt lgkmcnt(4)\n\t"
    "v_mfma_f32_16x16x32_bf16 v[24:27], v[32:35], v[20:23], v[24:27]\n\t"
    "v_mfma_f32_16x16x32_bf16 v[28:31], v[36:39], v[20:23], v[28:31]\n\t"
    "v_xor_b32 v12, 32, v13\n\t"
    "ds_read_b64_tr_b16 v[32:33], v13 offset:24576\n\t"
    "ds_read_b64_tr_b16 v[34:35], v13 offset:28672\n\t"
    "ds_read_b64_tr_b16 v[36:37], v12 offset:24576\n\t"
    "ds_read_b64_tr_b16 v[38:39], v12 offset:28672\n\t"
    "s_waitcnt lgkmcnt(6)\n\t"
    "v_mfma_f32_16x16x32_bf16 v[24:27], v[40:43], v[56:59], v[24:27]\n\t"
    "v_mfma_f32_16x16x32_bf16 v[28:31], v[44:47], v[56:59], v[28:31]\n\t"
    "s_waitcnt lgkmcnt(4)\n\t"
    "v_mfma_f32_16x16x32_bf16 v[24:27], v[48:51], v[60:63], v[24:27]\n\t"
    "v_mfma_f32_16x16x32_bf16 v[28:31], v[52:55], v[60:63], v[28:31]\n\t"
    "s_setprio 1\n\t"
    "v_mov_b32 v52, %[one]\n\t"
    "v_mov_b32 v53, %[one]\n\t"
    "v_mov_b32 v54, %[one]\n\t"
    "v_mov_b32 v55, %[one]\n\t"
    "s_nop 2\n\t"
    "v_max3_f32 v10, |v24|, |v25|, |v26|\n\t"
    "v_max3_f32 v40, |v27|, |v28|, |v29|\n\t"
    "v_max3_f32 v10, v10, |v30|, |v31|\n\t"
    "v_max_f32 v10, v10, v40\n\t"
    "v_cmp_nge_f32 vcc, %[big], v10\n\t"
    "s_mov_b32 %[bg], %[big]\n\t"
    "v_mov_b32 v10, 0\n\t"
    "s_cbranch_vccnz 12f\n\t"
    "13:\n\t"
    "v_exp_f32 v24, v24\n\t"
    "v_exp_f32 v25, v25\n\t"
    "v_exp_f32 v26, v26\n\t"
    "v_exp_f32 v27, v27\n\t"
    "v_exp_f32 v28, v28\n\t"
    "v_exp_f32 v29, v29\n\t"
    "v_exp_f32 v30, v30\n\t"
    "v_exp_f32 v31, v31\n\t"
    "v_cvt_pk_bf16_f32 v40, v24, v25\n\t"
    "v_cvt_pk_bf16_f32 v41, v26, v27\n\t"
    "v_cvt_pk_bf16_f32 v42, v28, v29\n\t"
    "v_cvt_pk_bf16_f32 v43, v30, v31\n\t"
    "s_setprio 3\n\t"
    "s_waitcnt lgkmcnt(0)\n\t"
    "s_nop 0\n\t"
    "v_mfma_f32_16x16x32_bf16 v[44:47], v[32:35], v[40:43], 0\n\t"
    "v_mfma_f32_16x16x32_bf16 v[48:51], v[36:39], v[40:43], 0\n\t"
    "v_mfma_f32_16x16x32_bf16 v[52:55], v[52:55], v[40:43], 0\n\t"
    "v_mov_b32 v12, 0\n\t"
    "v_mov_b32 v13, 0\n\t"
    "s_mov_b32 exec_hi, 0\n\t"
    "v_mov_b32 v12, %[one]\n\t"
    "v_mov_b32 v13, %[one]\n\t"
    "s_mov_b32 exec_hi, -1\n\t"
    "s_barrier\n\t"
    "ds_read_b64_tr_b16 v[24:25], v11 offset:34816\n\t"
    "ds_read_b64_tr_b16 v[26:27], v11 offset:34880\n\t"
    "ds_read_b64_tr_b16 v[28:29], v11 offset:35328\n\t"
    "ds_read_b64_tr_b16 v[30:31], v11 offset:35392\n\t"
    "ds_read_b64_tr_b16 v[32:33], v11 offset:35840\n\t"
    "ds_read_b64_tr_b16 v[34:35], v11 offset:35904\n\t"
    "ds_read_b64_tr_b16 v[36:37], v11 offset:36352\n\t"
    "ds_read_b64_tr_b16 v[38:39], v11 offset:36416\n\t"
    "s_waitcnt lgkmcnt(6)\n\t"
    "v_mfma_f32_16x16x32_bf16 v[40:43], v[24:27], v[16:19], 0\n\t"
    "s_waitcnt lgkmcnt(4)\n\t"
    "v_mfma_f32_16x16x32_bf16 v[40:43], v[28:31], v[20:23], v[40:43]\n\t"
    "s_waitcnt lgkmcnt(2)\n\t"
    "v_mfma_f32_16x16x32_bf16 v[40:43], v[32:35], v[56:59], v[40:43]\n\t"
    "s_waitcnt lgkmcnt(0)\n\t"
    "v_mfma_f32_16x16x32_bf16 v[40:43], v[36:39], v[60:63], v[40:43]\n\t"
    "5:\n\t"
    "s_waitcnt vmcnt(0)\n\t"
    "v_max3_u32 v11, v6, v7, v8\n\t"
    "v_max_u32 v11, v11, v9\n\t"
    "v_cmp_eq_u32 vcc, -1, v11\n\t"
    "s_cbranch_vccnz 4f\n\t"
    "6:\n\t"
    "s_nop 2\n\t"
    "v_exp_f32 v28, v40\n\t"
    "v_max3_f32 v11, v40, v41, v42\n\t"
    "v_exp_f32 v29, v41\n\t"
    "v_max_f32 v11, v11, v43\n\t"
    "v_exp_f32 v30, v42\n\t"
    "v_cmp_nge_f32 vcc, %[bg], v11\n\t"
    "v_exp_f32 v31, v43\n\t"
    "s_cbranch_vccnz 10f\n\t"
    "11:\n\t"
    "v_cvt_pk_bf16_f32 v24, v28, v29\n\t"
    "v_cvt_pk_bf16_f32 v25, v30, v31\n\t"
    "s_nop 1\n\t"
    "v_mfma_f32_16x16x16_bf16 v[52:55], v[12:13], v[24:25], v[52:55]\n\t"
    "v_mfma_f32_16x16x16_bf16 v[44:47], v[6:7], v[24:25], v[44:47]\n\t"
    "v_mfma_f32_16x16x16_bf16 v[48:51], v[8:9], v[24:25], v[48:51]\n\t"
    "s_nop 5\n\t"
    "v_rcp_f32 v26, v52\n\t"
    "s_nop 0\n\t"
    "v_pk_mul_f32 v[44:45], v[44:45], v[26:27] op_sel_hi:[1,0]\n\t"
    "v_pk_mul_f32 v[46:47], v[46:47], v[26:27] op_sel_hi:[1,0]\n\t"
    "v_pk_mul_f32 v[48:49], v[48:49], v[26:27] op_sel_hi:[1,0]\n\t"
    "v_pk_mul_f32 v[50:51], v[50:51], v[26:27] op_sel_hi:[1,0]\n\t"
    "v_cvt_pk_bf16_f32 v36, v44, v45\n\t"
    "v_cvt_pk_bf16_f32 v37, v46, v47\n\t"
    "v_cvt_pk_bf16_f32 v38, v48, v49\n\t"
    "v_cvt_pk_bf16_f32 v39, v50, v51\n\t"
    "v_min_u32 v36, -2, v36\n\t"
    "v_min_u32 v37, -2, v37\n\t"
    "v_min_u32 v38, -2, v38\n\t"
    "v_min_u32 v39, -2, v39\n\t"
    "global_store_dwordx4 v15, v[36:39], %[od] sc1\n\t"
    "s_barrier\n\t"
    "v_permlane16_swap_b32 v6, v8\n\t"
    "v_permlane16_swap_b32 v7, v9\n\t"
    "v_and_b32 v11, 8, v14\n\t"
    "v_mul_u32_u24 v11, 31, v11\n\t"
    "v_add_u32 v14, v14, v11\n\t"
    "s_mov_b32 exec_hi, 0\n\t"
    "global_store_dwordx4 v14, v[6:9], %[kv] sc1\n\t"
    "s_mov_b32 exec_hi, -1\n\t"
    "s_nop 0\n\t"
    "s_waitcnt lgkmcnt(0)\n\t"
    "s_branch 9f\n\t"
    "1:\n\t"
    "s_add_u32 %[sp], %[sp], 1\n\t"
    "s_cmp_ge_u32 %[sp], %[cap]\n\t"
    "s_cbranch_scc1 7f\n\t"
    "global_load_dwordx4 v[40:43], v6, %[ag] sc1\n\t"
    "global_load_dwordx4 v[44:47], v6, %[ag] offset:1024 sc1\n\t"
    "s_waitcnt lgkmcnt(0)\n\t"
    "s_memtime %[tc]\n\t"
    "s_nop 7\n\t"
    "s_branch 3b\n\t"
    "7:\n\t"
    "s_mov_b32 %[cap], 0\n\t"
    "s_branch 2b\n\t"
    "4:\n\t"
    "s_add_u32 %[sp], %[sp], 0x10000\n\t"
    "s_lshr_b32 %[st], %[sp], 16\n\t"
    "s_cmp_ge_u32 %[st], %[cap]\n\t"
    "s_cbranch_scc1 8f\n\t"
    "s_mov_b32 exec_hi, 0\n\t"
    "global_load_dwordx2 v[6:7], v14, %[vs] sc1\n\t"
    "global_load_dwordx2 v[8:9], v14, %[vs] offset:256 sc1\n\t"
    "s_mov_b32 exec_hi, -1\n\t"
    "s_branch 5b\n\t"
    "8:\n\t"
    "s_mov_b32 %[cap], 0\n\t"
    "s_branch 6b\n\t"
    "10:\n\t"
    "v_max_f32 v11, v11, v10\n\t"
    "v_mov_b32 v26, v11\n\t"
    "s_nop 1\n\t"
    "v_permlane16_swap_b32 v11, v26\n\t"
    "v_max_f32 v11, v11, v26\n\t"
    "v_sub_f32 v28, v40, v11\n\t"
    "v_sub_f32 v29, v41, v11\n\t"
    "v_sub_f32 v30, v42, v11\n\t"
    "v_sub_f32 v31, v43, v11\n\t"
    "v_sub_f32 v11, v10, v11\n\t"
    "v_exp_f32 v11, v11\n\t"
    "v_exp_f32 v28, v28\n\t"
    "v_exp_f32 v29, v29\n\t"
    "v_exp_f32 v30, v30\n\t"
    "v_exp_f32 v31, v31\n\t"
    "v_mul_f32 v44, v44, v11\n\t"
    "v_mul_f32 v45, v45, v11\n\t"
    "v_mul_f32 v46, v46, v11\n\t"
    "v_mul_f32 v47, v47, v11\n\t"
    "v_mul_f32 v48, v48, v11\n\t"
    "v_mul_f32 v49, v49, v11\n\t"
    "v_mul_f32 v50, v50, v11\n\t"
    "v_mul_f32 v51, v51, v11\n\t"
    "v_mul_f32 v52, v52, v11\n\t"
    "v_mul_f32 v53, v53, v11\n\t"
    "v_mul_f32 v54, v54, v11\n\t"
    "v_mul_f32 v55, v55, v11\n\t"
    "s_branch 11b\n\t"
    "12:\n\t"
    "v_max3_f32 v10, v24, v25, v26\n\t"
    "v_max3_f32 v40, v27, v28, v29\n\t"
    "v_max3_f32 v10, v10, v30, v31\n\t"
    "v_max3_f32 v10, v10, v40, %[neg]\n\t"
    "v_mov_b32 v40, v10\n\t"
    "s_nop 1\n\t"
    "v_permlane16_swap_b32 v10, v40\n\t"
    "v_max_f32 v10, v10, v40\n\t"
    "v_mov_b32 v40, v10\n\t"
    "s_nop 1\n\t"
    "v_permlane32_swap_b32 v10, v40\n\t"
    "v_max_f32 v10, v10, v40\n\t"
    "v_sub_f32 v24, v24, v10\n\t"
    "v_sub_f32 v25, v25, v10\n\t"
    "v_sub_f32 v26, v26, v10\n\t"
    "v_sub_f32 v27, v27, v10\n\t"
    "v_sub_f32 v28, v28, v10\n\t"
    "v_sub_f32 v29, v29, v10\n\t"
    "v_sub_f32 v30, v30, v10\n\t"
    "v_sub_f32 v31, v31, v10\n\t"
    "s_mov_b32 %[bg], 0x7fc00000\n\t"
    "s_branch 13b\n\t"
    "9:"
    : [tc] "=&s"(tc), [sp] "=&s"(sp), [cap] "+s"(cap), [st] "=&s"(st), [bg] "=&s"(bg)
    : [ag] "s"(ag), [c44] "s"(0x4444u), [kv] "s"(kv), [vs] "s"(vs), [od] "s"(od), [od2] "s"(od + 262144), [neg] "s"(-1.0e38f), [lsc] "s"(LSC), [one] "s"(0x3F803F80u), [big] "s"(24.0f), "{v[64:95]}"(W.w0), "{v[96:127]}"(W.w1), "{v[128:159]}"(W.w2), "{v[160:191]}"(W.w3), "{v[192:223]}"(W.w4), "{v[224:255]}"(W.w5)
    : "v6", "v7", "v8", "v9", "v10", "v11", "v12", "v13", "v14", "v15", "v16", "v17", "v18", "v19", "v20", "v21", "v22", "v23", "v24", "v25", "v26", "v27", "v28", "v29", "v30", "v31", "v32", "v33", "v34", "v35", "v36", "v37", "v38", "v39", "v40", "v41", "v42", "v43", "v44", "v45", "v46", "v47", "v48", "v49", "v50", "v51", "v52", "v53", "v54", "v55", "v56", "v57", "v58", "v59", "v60", "v61", "v62", "v63", "vcc", "scc", "memory");
}
template <> __device__ __forceinline__ void qk_step<3>(const QKW& W, const char* ag, const char* vs, const char* od, const char* kv, unsigned& cap, unsigned& sp,
                                                        unsigned long long& tc, unsigned long long* ts, unsigned& d0, unsigned& d1) {
  unsigned st, bg;
  asm volatile("v_mbcnt_lo_u32_b32 v60, -1, 0\n\t"
    "v_mbcnt_hi_u32_b32 v60, -1, v60\n\t"
    "v_lshlrev_b32 v6, 4, v60\n\t"
    "s_mov_b32 %[sp], 0\n\t"
    "s_setprio 3\n\t"
    "global_load_dwordx4 v[40:43], v6, %[ag] sc1\n\t"
    "global_load_dwordx4 v[44:47], v6, %[ag] offset:1024 sc1\n\t"
    "s_memtime %[tc]\n\t"
    "v_add_u32 v7, 47104, v6\n\t"
    "v_add_u32 v8, 104448, v6\n\t"
    "ds_read_b128 v[24:27], v7 offset:0\n\t"
    "ds_read_b128 v[28:31], v7 offset:1024\n\t"
    "ds_read_b128 v[32:35], v7 offset:8192\n\t"
    "ds_read_b128 v[36:39], v7 offset:9216\n\t"
    "v_lshrrev_b32 v48, 4, v60\n\t"
    "v_and_b32 v49, 15, v60\n\t"
    "v_and_b32 v61, 4, v60\n\t"
    "v_mul_u32_u24 v61, 0x2aaa, v61\n\t"
    "v_lshrrev_b32 v9, 1, v60\n\t"
    "v_and_b32 v9, 2, v9\n\t"
    "v_or3_b32 v9, v9, v61, %[c44]\n\t"
    "v_and_b32 v61, 1, v48\n\t"
    "v_lshlrev_b32 v61, 3, v61\n\t"
    "v_lshl_or_b32 v14, v49, 4, v61\n\t"
    "v_add_u32 v14, 1536, v14\n\t"
    "v_lshlrev_b32 v61, 2, v48\n\t"
    "v_lshl_or_b32 v10, v49, 4, v61\n\t"
    "v_lshrrev_b32 v50, 2, v49\n\t"
    "v_and_b32 v51, 3, v60\n\t"
    "v_and_b32 v61, 1, v60\n\t"
    "v_lshlrev_b32 v61, 3, v61\n\t"
    "v_lshl_or_b32 v11, v50, 4, v61\n\t"
    "v_lshl_or_b32 v11, v48, 7, v11\n\t"
    "v_xor_b32 v12, v48, v49\n\t"
    "v_lshlrev_b32 v12, 4, v12\n\t"
    "v_lshl_or_b32 v12, v49, 8, v12\n\t"
    "v_lshl_or_b32 v50, v48, 2, v50\n\t"
    "v_lshrrev_b32 v51, 1, v51\n\t"
    "v_or_b32 v51, 12, v51\n\t"
    "v_xor_b32 v51, v51, v50\n\t"
    "v_lshl_or_b32 v13, v51, 4, v61\n\t"
    "v_lshl_or_b32 v13, v50, 8, v13\n\t"
    "v_and_b32 v61, 3, v60\n\t"
    "v_lshlrev_b32 v61, 4, v61\n\t"
    "v_lshl_or_b32 v15, v48, 6, v61\n\t"
    "v_and_b32 v61, 8, v60\n\t"
    "v_lshl_or_b32 v15, v61, 14, v15\n\t"
    "v_and_b32 v61, 4, v60\n\t"
    "v_lshl_or_b32 v15, v61, 8, v15\n\t"
    "v_add_u32 v15, 768, v15\n\t"
    "3:\n\t"
    "v_mov_b32 v16, 0\n\t"
    "v_mov_b32 v17, 0\n\t"
    "v_mov_b32 v18, 0\n\t"
    "v_mov_b32 v19, 0\n\t"
    "v_mov_b32 v20, 0\n\t"
    "v_mov_b32 v21, 0\n\t"
    "v_mov_b32 v22, 0\n\t"
    "v_mov_b32 v23, 0\n\t"
    "s_waitcnt vmcnt(0) lgkmcnt(0)\n\t"
    "14:\n\t"
    "ds_write_b128 v6, v[40:43] offset:6144\n\t"
    "ds_write_b128 v6, v[44:47] offset:7168\n\t"
    "v_smfmac_f32_16x16x64_bf16 v[16:19], v[40:43], v[160:167], v9\n\t"
    "v_max3_u32 v60, v40, v41, v42\n\t"
    "v_max3_u32 v61, v43, v44, v45\n\t"
    "v_smfmac_f32_16x16x64_bf16 v[20:23], v[40:43], v[168:175], v9\n\t"
    "v_max3_u32 v60, v60, v46, v47\n\t"
    "v_max_u32 v60, v60, v61\n\t"
    "v_cmp_eq_u32 vcc, -1, v60\n\t"
    "s_cbranch_vccnz 1f\n\t"
    "2:\n\t"
    "s_waitcnt lgkmcnt(0)\n\t"
    "s_barrier\n\t"
    "ds_read_b128 v[40:43], v6 offset:12288\n\t"
    "ds_read_b128 v[48:51], v6 offset:0\n\t"
    "v_smfmac_f32_16x16x64_bf16 v[16:19], v[44:47], v[176:183], v9\n\t"
    "ds_read_b128 v[52:55], v6 offset:1024\n\t"
    "ds_read_b128 v[56:59], v6 offset:2048\n\t"
    "v_smfmac_f32_16x16x64_bf16 v[20:23], v[44:47], v[184:191], v9\n\t"
    "ds_read_b128 v[60:63], v6 offset:13312\n\t"
    "ds_read_b128 v[44:47], v6 offset:3072\n\t"
    "s_waitcnt lgkmcnt(5)\n\t"
    "v_smfmac_f32_16x16x64_bf16 v[16:19], v[40:43], v[24:31], v9\n\t"
    "ds_read_b128 v[24:27], v7 offset:16384\n\t"
    "ds_read_b128 v[28:31], v7 offset:17408\n\t"
    "v_smfmac_f32_16x16x64_bf16 v[20:23], v[40:43], v[32:39], v9\n\t"
    "ds_read_b128 v[32:35], v7 offset:24576\n\t"
    "ds_read_b128 v[36:39], v7 offset:25600\n\t"
    "ds_read_b128 v[40:43], v6 offset:4096\n\t"
    "s_waitcnt lgkmcnt(9)\n\t"
    "v_smfmac_f32_16x16x64_bf16 v[16:19], v[48:51], v[64:71], v9\n\t"
    "v_smfmac_f32_16x16x64_bf16 v[20:23], v[48:51], v[72:79], v9\n\t"
    "ds_read_b128 v[48:51], v6 offset:5120\n\t"
    "s_waitcnt lgkmcnt(9)\n\t"
    "v_smfmac_f32_16x16x64_bf16 v[16:19], v[52:55], v[80:87], v9\n\t"
    "v_smfmac_f32_16x16x64_bf16 v[20:23], v[52:55], v[88:95], v9\n\t"
    "ds_read_b128 v[52:55], v6 offset:14336\n\t"
    "s_waitcnt lgkmcnt(9)\n\t"
    "v_smfmac_f32_16x16x64_bf16 v[16:19], v[56:59], v[96:103], v9\n\t"
    "v_smfmac_f32_16x16x64_bf16 v[20:23], v[56:59], v[104:111], v9\n\t"
    "ds_read_b128 v[56:59], v6 offset:8192\n\t"
    "s_waitcnt lgkmcnt(6)\n\t"
    "v_smfmac_f32_16x16x64_bf16 v[16:19], v[60:63], v[24:31], v9\n\t"
    "ds_read_b128 v[24:27], v8 offset:0\n\t"
    "ds_read_b128 v[28:31], v8 offset:1024\n\t"
    "s_waitcnt lgkmcnt(6)\n\t"
    "v_smfmac_f32_16x16x64_bf16 v[20:23], v[60:63], v[32:39], v9\n\t"
    "ds_read_b128 v[32:35], v8 offset:8192\n\t"
    "ds_read_b128 v[36:39], v8 offset:9216\n\t"
    "ds_read_b128 v[60:63], v6 offset:9216\n\t"
    "v_smfmac_f32_16x16x64_bf16 v[16:19], v[44:47], v[112:119], v9\n\t"
    "v_smfmac_f32_16x16x64_bf16 v[20:23], v[44:47], v[120:127], v9\n\t"
    "ds_read_b128 v[44:47], v6 offset:10240\n\t"
    "s_waitcnt lgkmcnt(9)\n\t"
    "v_smfmac_f32_16x16x64_bf16 v[16:19], v[40:43], v[128:135], v9\n\t"
    "v_smfmac_f32_16x16x64_bf16 v[20:23], v[40:43], v[136:143], v9\n\t"
    "ds_read_b128 v[40:43], v6 offset:15360\n\t"
    "s_waitcnt lgkmcnt(9)\n\t"
    "v_smfmac_f32_16x16x64_bf16 v[16:19], v[48:51], v[144:151], v9\n\t"
    "v_smfmac_f32_16x16x64_bf16 v[20:23], v[48:51], v[152:159], v9\n\t"
    "ds_read_b128 v[48:51], v6 offset:11264\n\t"
    "s_waitcnt lgkmcnt(6)\n\t"
    "v_smfmac_f32_16x16x64_bf16 v[16:19], v[52:55], v[24:31], v9\n\t"
    "ds_read_b128 v[24:27], v8 offset:16384\n\t"
    "ds_read_b128 v[28:31], v8 offset:17408\n\t"
    "s_waitcnt lgkmcnt(6)\n\t"
    "v_smfmac_f32_16x16x64_bf16 v[20:23], v[52:55], v[32:39], v9\n\t"
    "ds_read_b128 v[32:35], v8 offset:24576\n\t"
    "ds_read_b128 v[36:39], v8 offset:25600\n\t"
    "v_smfmac_f32_16x16x64_bf16 v[16:19], v[56:59], v[192:199], v9\n\t"
    "v_smfmac_f32_16x16x64_bf16 v[20:23], v[56:59], v[200:207], v9\n\t"
    "s_waitcnt lgkmcnt(7)\n\t"
    "v_smfmac_f32_16x16x64_bf16 v[16:19], v[60:63], v[208:215], v9\n\t"
    "v_smfmac_f32_16x16x64_bf16 v[20:23], v[60:63], v[216:223], v9\n\t"
    "s_waitcnt lgkmcnt(6)\n\t"
    "v_smfmac_f32_16x16x64_bf16 v[16:19], v[44:47], v[224:231], v9\n\t"
    "v_smfmac_f32_16x16x64_bf16 v[20:23], v[44:47], v[232:239], v9\n\t"
    "s_waitcnt lgkmcnt(2)\n\t"
    "v_smfmac_f32_16x16x64_bf16 v[16:19], v[40:43], v[24:31], v9\n\t"
    "s_waitcnt lgkmcnt(0)\n\t"
    "v_smfmac_f32_16x16x64_bf16 v[20:23], v[40:43], v[32:39], v9\n\t"
    "v_smfmac_f32_16x16x64_bf16 v[16:19], v[48:51], v[240:247], v9\n\t"
    "v_smfmac_f32_16x16x64_bf16 v[20:23], v[48:51], v[248:255], v9\n\t"
    "v_xor_b32 v7, 64, v12\n\t"
    "v_xor_b32 v8, 0x80, v12\n\t"
    "v_xor_b32 v6, 0xc0, v12\n\t"
    "ds_read_b128 v[24:27], v12 offset:16384\n\t"
    "ds_read_b128 v[28:31], v12 offset:20480\n\t"
    "ds_read_b128 v[32:35], v7 offset:16384\n\t"
    "ds_read_b128 v[36:39], v7 offset:20480\n\t"
    "v_permlane16_swap_b32 v16, v18\n\t"
    "v_permlane16_swap_b32 v17, v19\n\t"
    "v_permlane16_swap_b32 v20, v22\n\t"
    "v_permlane16_swap_b32 v21, v23\n\t"
    "v_add_f32 v16, v16, v18\n\t"
    "v_add_f32 v17, v17, v19\n\t"
    "v_add_f32 v20, v20, v22\n\t"
    "v_mul_f32 v16, %[lsc], v16\n\t"
    "v_mul_f32 v17, %[lsc], v17\n\t"
    "v_cvt_pk_bf16_f32 v16, v16, v17\n\t"
    "v_add_f32 v21, v21, v23\n\t"
    "ds_write_b32 v10, v16 offset:34304\n\t"
    "v_mul_f32 v20, %[lsc], v20\n\t"
    "v_mul_f32 v21, %[lsc], v21\n\t"
    "v_cvt_pk_bf16_f32 v20, v20, v21\n\t"
    "ds_write_b32 v10, v20 offset:34560\n\t"
    "ds_read_b128 v[40:43], v8 offset:16384\n\t"
    "ds_read_b128 v[44:47], v8 offset:20480\n\t"
    "ds_read_b128 v[48:51], v6 offset:16384\n\t"
    "ds_read_b128 v[52:55], v6 offset:20480\n\t"
    "s_waitcnt lgkmcnt(4)\n\t"
    "s_barrier\n\t"
    "ds_read_b64_tr_b16 v[16:17], v11 offset:32768\n\t"
    "ds_read_b64_tr_b16 v[18:19], v11 offset:32832\n\t"
    "ds_read_b64_tr_b16 v[20:21], v11 offset:33280\n\t"
    "ds_read_b64_tr_b16 v[22:23], v11 offset:33344\n\t"
    "ds_read_b64_tr_b16 v[56:57], v11 offset:33792\n\t"
    "ds_read_b64_tr_b16 v[58:59], v11 offset:33856\n\t"
    "ds_read_b64_tr_b16 v[60:61], v11 offset:34304\n\t"
    "ds_read_b64_tr_b16 v[62:63], v11 offset:34368\n\t"
    "v_mov_b32 v6, 0\n\t"
    "v_mov_b32 v7, 0\n\t"
    "v_mov_b32 v8, 0\n\t"
    "v_mov_b32 v9, 0\n\t"
    "s_mov_b32 exec_hi, 0\n\t"
    "global_load_dwordx2 v[6:7], v14, %[vs] sc1\n\t"
    "global_load_dwordx2 v[8:9], v14, %[vs] offset:256 sc1\n\t"
    "s_mov_b32 exec_hi, -1\n\t"
    "s_waitcnt lgkmcnt(6)\n\t"
    "v_mfma_f32_16x16x32_bf16 v[24:27], v[24:27], v[16:19], 0\n\t"
    "v_mfma_f32_16x16x32_bf16 v[28:31], v[28:31], v[16:19], 0\n\t"
    "s_waitcnt lgkmcnt(4)\n\t"
    "v_mfma_f32_16x16x32_bf16 v[24:27], v[32:35], v[20:23], v[24:27]\n\t"
    "v_mfma_f32_16x16x32_bf16 v[28:31], v[36:39], v[20:23], v[28:31]\n\t"
    "v_xor_b32 v12, 32, v13\n\t"
    "ds_read_b64_tr_b16 v[32:33], v13 offset:24576\n\t"
    "ds_read_b64_tr_b16 v[34:35], v13 offset:28672\n\t"
    "ds_read_b64_tr_b16 v[36:37], v12 offset:24576\n\t"
    "ds_read_b64_tr_b16 v[38:39], v12 offset:28672\n\t"
    "s_waitcnt lgkmcnt(6)\n\t"
    "v_mfma_f32_16x16x32_bf16 v[24:27], v[40:43], v[56:59], v[24:27]\n\t"
    "v_mfma_f32_16x16x32_bf16 v[28:31], v[44:47], v[56:59], v[28:31]\n\t"
    "s_waitcnt lgkmcnt(4)\n\t"
    "v_mfma_f32_16x16x32_bf16 v[24:27], v[48:51], v[60:63], v[24:27]\n\t"
    "v_mfma_f32_16x16x32_bf16 v[28:31], v[52:55], v[60:63], v[28:31]\n\t"
    "s_setprio 1\n\t"
    "v_mov_b32 v52, %[one]\n\t"
    "v_mov_b32 v53, %[one]\n\t"
    "v_mov_b32 v54, %[one]\n\t"
    "v_mov_b32 v55, %[one]\n\t"
    "s_nop 2\n\t"
    "v_max3_f32 v10, |v24|, |v25|, |v26|\n\t"
    "v_max3_f32 v40, |v27|, |v28|, |v29|\n\t"
    "v_max3_f32 v10, v10, |v30|, |v31|\n\t"
    "v_max_f32 v10, v10, v40\n\t"
    "v_cmp_nge_f32 vcc, %[big], v10\n\t"
    "s_mov_b32 %[bg], %[big]\n\t"
    "v_mov_b32 v10, 0\n\t"
    "s_cbranch_vccnz 12f\n\t"
    "13:\n\t"
    "v_exp_f32 v24, v24\n\t"
    "v_exp_f32 v25, v25\n\t"
    "v_exp_f32 v26, v26\n\t"
    "v_exp_f32 v27, v27\n\t"
    "v_exp_f32 v28, v28\n\t"
    "v_exp_f32 v29, v29\n\t"
    "v_exp_f32 v30, v30\n\t"
    "v_exp_f32 v31, v31\n\t"
    "v_cvt_pk_bf16_f32 v40, v24, v25\n\t"
    "v_cvt_pk_bf16_f32 v41, v26, v27\n\t"
    "v_cvt_pk_bf16_f32 v42, v28, v29\n\t"
    "v_cvt_pk_bf16_f32 v43, v30, v31\n\t"
    "s_setprio 3\n\t"
    "s_waitcnt lgkmcnt(0)\n\t"
    "s_nop 0\n\t"
    "v_mfma_f32_16x16x32_bf16 v[44:47], v[32:35], v[40:43], 0\n\t"
    "v_mfma_f32_16x16x32_bf16 v[48:51], v[36:39], v[40:43], 0\n\t"
    "v_mfma_f32_16x16x32_bf16 v[52:55], v[52:55], v[40:43], 0\n\t"
    "v_mov_b32 v12, 0\n\t"
    "v_mov_b32 v13, 0\n\t"
    "s_mov_b32 exec_hi, 0\n\t"
    "v_mov_b32 v12, %[one]\n\t"
    "v_mov_b32 v13, %[one]\n\t"
    "s_mov_b32 exec_hi, -1\n\t"
    "s_barrier\n\t"
    "ds_read_b64_tr_b16 v[24:25], v11 offset:34816\n\t"
    "ds_read_b64_tr_b16 v[26:27], v11 offset:34880\n\t"
    "ds_read_b64_tr_b16 v[28:29], v11 offset:35328\n\t"
    "ds_read_b64_tr_b16 v[30:31], v11 offset:35392\n\t"
    "ds_read_b64_tr_b16 v[32:33], v11 offset:35840\n\t"
    "ds_read_b64_tr_b16 v[34:35], v11 offset:35904\n\t"
    "ds_read_b64_tr_b16 v[36:37], v11 offset:36352\n\t"
    "ds_read_b64_tr_b16 v[38:39], v11 offset:36416\n\t"
    "s_waitcnt lgkmcnt(6)\n\t"
    "v_mfma_f32_16x16x32_bf16 v[40:43], v[24:27], v[16:19], 0\n\t"
    "s_waitcnt lgkmcnt(4)\n\t"
    "v_mfma_f32_16x16x32_bf16 v[40:43], v[28:31], v[20:23], v[40:43]\n\t"
    "s_waitcnt lgkmcnt(2)\n\t"
    "v_mfma_f32_16x16x32_bf16 v[40:43], v[32:35], v[56:59], v[40:43]\n\t"
    "s_waitcnt lgkmcnt(0)\n\t"
    "v_mfma_f32_16x16x32_bf16 v[40:43], v[36:39], v[60:63], v[40:43]\n\t"
    "5:\n\t"
    "s_waitcnt vmcnt(0)\n\t"
    "v_max3_u32 v11, v6, v7, v8\n\t"
    "v_max_u32 v11, v11, v9\n\t"
    "v_cmp_eq_u32 vcc, -1, v11\n\t"
    "s_cbranch_vccnz 4f\n\t"
    "6:\n\t"
    "s_nop 2\n\t"
    "v_exp_f32 v28, v40\n\t"
    "v_max3_f32 v11, v40, v41, v42\n\t"
    "v_exp_f32 v29, v41\n\t"
    "v_max_f32 v11, v11, v43\n\t"
    "v_exp_f32 v30, v42\n\t"
    "v_cmp_nge_f32 vcc, %[bg], v11\n\t"
    "v_exp_f32 v31, v43\n\t"
    "s_cbranch_vccnz 10f\n\t"
    "11:\n\t"
    "v_cvt_pk_bf16_f32 v24, v28, v29\n\t"
    "v_cvt_pk_bf16_f32 v25, v30, v31\n\t"
    "s_nop 1\n\t"
    "v_mfma_f32_16x16x16_bf16 v[52:55], v[12:13], v[24:25], v[52:55]\n\t"
    "v_mfma_f32_16x16x16_bf16 v[44:47], v[6:7], v[24:25], v[44:47]\n\t"
    "v_mfma_f32_16x16x16_bf16 v[48:51], v[8:9], v[24:25], v[48:51]\n\t"
    "s_nop 5\n\t"
    "v_rcp_f32 v26, v52\n\t"
    "s_nop 0\n\t"
    "v_pk_mul_f32 v[44:45], v[44:45], v[26:27] op_sel_hi:[1,0]\n\t"
    "v_pk_mul_f32 v[46:47], v[46:47], v[26:27] op_sel_hi:[1,0]\n\t"
    "v_pk_mul_f32 v[48:49], v[48:49], v[26:27] op_sel_hi:[1,0]\n\t"
    "v_pk_mul_f32 v[50:51], v[50:51], v[26:27] op_sel_hi:[1,0]\n\t"
    "v_cvt_pk_bf16_f32 v36, v44, v45\n\t"
    "v_cvt_pk_bf16_f32 v37, v46, v47\n\t"
    "v_cvt_pk_bf16_f32 v38, v48, v49\n\t"
    "v_cvt_pk_bf16_f32 v39, v50, v51\n\t"
    "v_min_u32 v36, -2, v36\n\t"
    "v_min_u32 v37, -2, v37\n\t"
    "v_min_u32 v38, -2, v38\n\t"
    "v_min_u32 v39, -2, v39\n\t"
    "global_store_dwordx4 v15, v[36:39], %[od] sc1\n\t"
    "s_barrier\n\t"
    "v_permlane16_swap_b32 v6, v8\n\t"
    "v_permlane16_swap_b32 v7, v9\n\t"
    "v_and_b32 v11, 8, v14\n\t"
    "v_mul_u32_u24 v11, 31, v11\n\t"
    "v_add_u32 v14, v14, v11\n\t"
    "s_mov_b32 exec_hi, 0\n\t"
    "global_store_dwordx4 v14, v[6:9], %[kv] sc1\n\t"
    "s_mov_b32 exec_hi, -1\n\t"
    "s_nop 0\n\t"
    "s_waitcnt lgkmcnt(0)\n\t"
    "s_branch 9f\n\t"
    "1:\n\t"
    "s_add_u32 %[sp], %[sp], 1\n\t"
    "s_cmp_ge_u32 %[sp], %[cap]\n\t"
    "s_cbranch_scc1 7f\n\t"
    "global_load_dwordx4 v[40:43], v6, %[ag] sc1\n\t"
    "global_load_dwordx4 v[44:47], v6, %[ag] offset:1024 sc1\n\t"
    "s_waitcnt lgkmcnt(0)\n\t"
    "s_memtime %[tc]\n\t"
    "s_nop 7\n\t"
    "s_branch 3b\n\t"
    "7:\n\t"
    "s_mov_b32 %[cap], 0\n\t"
    "s_branch 2b\n\t"
    "4:\n\t"
    "s_add_u32 %[sp], %[sp], 0x10000\n\t"
    "s_lshr_b32 %[st], %[sp], 16\n\t"
    "s_cmp_ge_u32 %[st], %[cap]\n\t"
    "s_cbranch_scc1 8f\n\t"
    "s_mov_b32 exec_hi, 0\n\t"
    "global_load_dwordx2 v[6:7], v14, %[vs] sc1\n\t"
    "global_load_dwordx2 v[8:9], v14, %[vs] offset:256 sc1\n\t"
    "s_mov_b32 exec_hi, -1\n\t"
    "s_branch 5b\n\t"
    "8:\n\t"
    "s_mov_b32 %[cap], 0\n\t"
    "s_branch 6b\n\t"
    "10:\n\t"
    "v_max_f32 v11, v11, v10\n\t"
    "v_mov_b32 v26, v11\n\t"
    "s_nop 1\n\t"
    "v_permlane16_swap_b32 v11, v26\n\t"
    "v_max_f32 v11, v11, v26\n\t"
    "v_sub_f32 v28, v40, v11\n\t"
    "v_sub_f32 v29, v41, v11\n\t"
    "v_sub_f32 v30, v42, v11\n\t"
    "v_sub_f32 v31, v43, v11\n\t"
    "v_sub_f32 v11, v10, v11\n\t"
    "v_exp_f32 v11, v11\n\t"
    "v_exp_f32 v28, v28\n\t"
    "v_exp_f32 v29, v29\n\t"
    "v_exp_f32 v30, v30\n\t"
    "v_exp_f32 v31, v31\n\t"
    "v_mul_f32 v44, v44, v11\n\t"
    "v_mul_f32 v45, v45, v11\n\t"
    "v_mul_f32 v46, v46, v11\n\t"
    "v_mul_f32 v47, v47, v11\n\t"
    "v_mul_f32 v48, v48, v11\n\t"
    "v_mul_f32 v49, v49, v11\n\t"
    "v_mul_f32 v50, v50, v11\n\t"
    "v_mul_f32 v51, v51, v11\n\t"
    "v_mul_f32 v52, v52, v11\n\t"
    "v_mul_f32 v53, v53, v11\n\t"
    "v_mul_f32 v54, v54, v11\n\t"
    "v_mul_f32 v55, v55, v11\n\t"
    "s_branch 11b\n\t"
    "12:\n\t"
    "v_max3_f32 v10, v24, v25, v26\n\t"
    "v_max3_f32 v40, v27, v28, v29\n\t"
    "v_max3_f32 v10, v10, v30, v31\n\t"
    "v_max3_f32 v10, v10, v40, %[neg]\n\t"
    "v_mov_b32 v40, v10\n\t"
    "s_nop 1\n\t"
    "v_permlane16_swap_b32 v10, v40\n\t"
    "v_max_f32 v10, v10, v40\n\t"
    "v_mov_b32 v40, v10\n\t"
    "s_nop 1\n\t"
    "v_permlane32_swap_b32 v10, v40\n\t"
    "v_max_f32 v10, v10, v40\n\t"
    "v_sub_f32 v24, v24, v10\n\t"
    "v_sub_f32 v25, v25, v10\n\t"
    "v_sub_f32 v26, v26, v10\n\t"
    "v_sub_f32 v27, v27, v10\n\t"
    "v_sub_f32 v28, v28, v10\n\t"
    "v_sub_f32 v29, v29, v10\n\t"
    "v_sub_f32 v30, v30, v10\n\t"
    "v_sub_f32 v31, v31, v10\n\t"
    "s_mov_b32 %[bg], 0x7fc00000\n\t"
    "s_branch 13b\n\t"
    "9:"
    : [tc] "=&s"(tc), [sp] "=&s"(sp), [cap] "+s"(cap), [st] "=&s"(st), [bg] "=&s"(bg)
    : [ag] "s"(ag), [c44] "s"(0x4444u), [kv] "s"(kv), [vs] "s"(vs), [od] "s"(od), [od2] "s"(od + 262144), [neg] "s"(-1.0e38f), [lsc] "s"(LSC), [one] "s"(0x3F803F80u), [big] "s"(24.0f), "{v[64:95]}"(W.w0), "{v[96:127]}"(W.w1), "{v[128:159]}"(W.w2), "{v[160:191]}"(W.w3), "{v[192:223]}"(W.w4), "{v[224:255]}"(W.w5)
    : "v6", "v7", "v8", "v9", "v10", "v11", "v12", "v13", "v14", "v15", "v16", "v17", "v18", "v19", "v20", "v21", "v22", "v23", "v24", "v25", "v26", "v27", "v28", "v29", "v30", "v31", "v32", "v33", "v34", "v35", "v36", "v37", "v38", "v39", "v40", "v41", "v42", "v43", "v44", "v45", "v46", "v47", "v48", "v49", "v50", "v51", "v52", "v53", "v54", "v55", "v56", "v57", "v58", "v59", "v60", "v61", "v62", "v63", "vcc", "scc", "memory");
}
template <> __device__ __forceinline__ void qk_step<4>(const QKW& W, const char* ag, const char* vs, const char* od, const char* kv, unsigned& cap, unsigned& sp,
                                                        unsigned long long& tc, unsigned long long* ts, unsigned& d0, unsigned& d1) {
  unsigned st, bg;
  asm volatile("v_mbcnt_lo_u32_b32 v52, -1, 0\n\t"
    "v_mbcnt_hi_u32_b32 v52, -1, v52\n\t"
    "v_lshlrev_b32 v6, 4, v52\n\t"
    "s_mov_b32 %[sp], 0\n\t"
    "global_load_dwordx4 v[36:39], v6, %[ag] sc1\n\t"
    "global_load_dwordx4 v[40:43], v6, %[ag] offset:1024 sc1\n\t"
    "s_memtime %[tc]\n\t"
    "v_add_u32 v7, 73728, v6\n\t"
    "v_add_u32 v8, 131072, v6\n\t"
    "ds_read_b128 v[20:23], v7 offset:8192\n\t"
    "ds_read_b128 v[24:27], v7 offset:9216\n\t"
    "ds_read_b128 v[28:31], v7 offset:0\n\t"
    "ds_read_b128 v[32:35], v7 offset:1024\n\t"
    "v_lshrrev_b32 v44, 4, v52\n\t"
    "v_and_b32 v45, 15, v52\n\t"
    "v_and_b32 v53, 4, v52\n\t"
    "v_mul_u32_u24 v53, 0x2aaa, v53\n\t"
    "v_lshrrev_b32 v9, 1, v52\n\t"
    "v_and_b32 v9, 2, v9\n\t"
    "v_or3_b32 v9, v9, v53, %[c44]\n\t"
    "v_lshlrev_b32 v53, 2, v44\n\t"
    "v_lshl_or_b32 v10, v45, 4, v53\n\t"
    "3:\n\t"
    "v_mov_b32 v12, 0\n\t"
    "v_mov_b32 v13, 0\n\t"
    "v_mov_b32 v14, 0\n\t"
    "v_mov_b32 v15, 0\n\t"
    "v_mov_b32 v16, 0\n\t"
    "v_mov_b32 v17, 0\n\t"
    "v_mov_b32 v18, 0\n\t"
    "v_mov_b32 v19, 0\n\t"
    "s_waitcnt vmcnt(0) lgkmcnt(0)\n\t"
    "14:\n\t"
    "ds_write_b128 v6, v[36:39] offset:8192\n\t"
    "ds_write_b128 v6, v[40:43] offset:9216\n\t"
    "v_smfmac_f32_16x16x64_bf16 v[12:15], v[36:39], v[128:135], v9\n\t"
    "v_max3_u32 v52, v36, v37, v38\n\t"
    "v_max3_u32 v53, v39, v40, v41\n\t"
    "v_smfmac_f32_16x16x64_bf16 v[16:19], v[36:39], v[136:143], v9\n\t"
    "v_max3_u32 v52, v52, v42, v43\n\t"
    "v_max_u32 v52, v52, v53\n\t"
    "v_cmp_eq_u32 vcc, -1, v52\n\t"
    "s_cbranch_vccnz 1f\n\t"
    "2:\n\t"
    "s_waitcnt lgkmcnt(0)\n\t"
    "s_barrier\n\t"
    "s_memrealtime s[36:37]\n\t"
    "ds_read_b128 v[36:39], v6 offset:0\n\t"
    "ds_read_b128 v[44:47], v6 offset:4096\n\t"
    "v_smfmac_f32_16x16x64_bf16 v[12:15], v[40:43], v[144:151], v9\n\t"
    "ds_read_b128 v[48:51], v6 offset:5120\n\t"
    "ds_read_b128 v[52:55], v6 offset:6144\n\t"
    "v_smfmac_f32_16x16x64_bf16 v[16:19], v[40:43], v[152:159], v9\n\t"
    "ds_read_b128 v[40:43], v6 offset:1024\n\t"
    "s_waitcnt lgkmcnt(0)\n\t"
    "s_mov_b32 s38, s36\n\t"
    "s_min_u32 s37, %[d0], %[d1]\n\t"
    "s_add_u32 s36, s36, s37\n\t"
    "s_and_b32 s36, s36, 0xffffff\n\t"
    "s_or_b32 s36, s36, %[sn]\n\t"
    "v_lshlrev_b32 v11, 10, v6\n\t"
    "v_mov_b32 v5, s36\n\t"
    "s_mov_b64 exec, 15\n\t"
    "global_store_dword v11, v5, %[vs] offset:-4096 sc1\n\t"
    "s_mov_b64 exec, -1\n\t"
    "s_sleep 0\n\t"
    "v_smfmac_f32_16x16x64_bf16 v[12:15], v[36:39], v[56:63], v9\n\t"
    "v_smfmac_f32_16x16x64_bf16 v[16:19], v[36:39], v[28:35], v9\n\t"
    "ds_read_b128 v[28:31], v7 offset:16384\n\t"
    "ds_read_b128 v[32:35], v7 offset:17408\n\t"
    "ds_read_b128 v[36:39], v6 offset:7168\n\t"
    "v_smfmac_f32_16x16x64_bf16 v[12:15], v[44:47], v[64:71], v9\n\t"
    "v_smfmac_f32_16x16x64_bf16 v[16:19], v[44:47], v[72:79], v9\n\t"
    "ds_read_b128 v[44:47], v6 offset:10240\n\t"
    "v_smfmac_f32_16x16x64_bf16 v[12:15], v[48:51], v[80:87], v9\n\t"
    "v_smfmac_f32_16x16x64_bf16 v[16:19], v[48:51], v[88:95], v9\n\t"
    "ds_read_b128 v[48:51], v6 offset:11264\n\t"
    "v_smfmac_f32_16x16x64_bf16 v[12:15], v[52:55], v[96:103], v9\n\t"
    "v_smfmac_f32_16x16x64_bf16 v[16:19], v[52:55], v[104:111], v9\n\t"
    "ds_read_b128 v[52:55], v6 offset:2048\n\t"
    "v_smfmac_f32_16x16x64_bf16 v[12:15], v[40:43], v[20:27], v9\n\t"
    "ds_read_b128 v[20:23], v8 offset:0\n\t"
    "ds_read_b128 v[24:27], v8 offset:1024\n\t"
    "s_waitcnt lgkmcnt(6)\n\t"
    "v_smfmac_f32_16x16x64_bf16 v[16:19], v[40:43], v[28:35], v9\n\t"
    "ds_read_b128 v[28:31], v8 offset:8192\n\t"
    "ds_read_b128 v[32:35], v8 offset:9216\n\t"
    "ds_read_b128 v[40:43], v6 offset:12288\n\t"
    "s_waitcnt lgkmcnt(8)\n\t"
    "v_smfmac_f32_16x16x64_bf16 v[12:15], v[36:39], v[112:119], v9\n\t"
    "v_smfmac_f32_16x16x64_bf16 v[16:19], v[36:39], v[120:127], v9\n\t"
    "ds_read_b128 v[36:39], v6 offset:13312\n\t"
    "s_waitcnt lgkmcnt(8)\n\t"
    "v_smfmac_f32_16x16x64_bf16 v[12:15], v[44:47], v[160:167], v9\n\t"
    "v_smfmac_f32_16x16x64_bf16 v[16:19], v[44:47], v[168:175], v9\n\t"
    "ds_read_b128 v[44:47], v6 offset:14336\n\t"
    "s_barrier\n\t"
    "s_setprio 2\n\t"
    "s_waitcnt lgkmcnt(8)\n\t"
    "v_smfmac_f32_16x16x64_bf16 v[12:15], v[48:51], v[176:183], v9\n\t"
    "v_smfmac_f32_16x16x64_bf16 v[16:19], v[48:51], v[184:191], v9\n\t"
    "ds_read_b128 v[48:51], v6 offset:3072\n\t"
    "s_waitcnt lgkmcnt(6)\n\t"
    "v_smfmac_f32_16x16x64_bf16 v[12:15], v[52:55], v[20:27], v9\n\t"
    "ds_read_b128 v[20:23], v8 offset:16384\n\t"
    "ds_read_b128 v[24:27], v8 offset:17408\n\t"
    "s_waitcnt lgkmcnt(6)\n\t"
    "v_smfmac_f32_16x16x64_bf16 v[16:19], v[52:55], v[28:35], v9\n\t"
    "ds_read_b128 v[28:31], v8 offset:24576\n\t"
    "ds_read_b128 v[32:35], v8 offset:25600\n\t"
    "ds_read_b128 v[52:55], v6 offset:15360\n\t"
    "s_waitcnt lgkmcnt(8)\n\t"
    "v_smfmac_f32_16x16x64_bf16 v[12:15], v[40:43], v[192:199], v9\n\t"
    "v_smfmac_f32_16x16x64_bf16 v[16:19], v[40:43], v[200:207], v9\n\t"
    "s_waitcnt lgkmcnt(7)\n\t"
    "v_smfmac_f32_16x16x64_bf16 v[12:15], v[36:39], v[208:215], v9\n\t"
    "v_smfmac_f32_16x16x64_bf16 v[16:19], v[36:39], v[216:223], v9\n\t"
    "s_waitcnt lgkmcnt(6)\n\t"
    "v_smfmac_f32_16x16x64_bf16 v[12:15], v[44:47], v[224:231], v9\n\t"
    "v_smfmac_f32_16x16x64_bf16 v[16:19], v[44:47], v[232:239], v9\n\t"
    "s_waitcnt lgkmcnt(3)\n\t"
    "v_smfmac_f32_16x16x64_bf16 v[12:15], v[48:51], v[20:27], v9\n\t"
    "s_waitcnt lgkmcnt(1)\n\t"
    "v_smfmac_f32_16x16x64_bf16 v[16:19], v[48:51], v[28:35], v9\n\t"
    "s_waitcnt lgkmcnt(0)\n\t"
    "v_smfmac_f32_16x16x64_bf16 v[12:15], v[52:55], v[240:247], v9\n\t"
    "v_smfmac_f32_16x16x64_bf16 v[16:19], v[52:55], v[248:255], v9\n\t"
    "s_nop 6\n\t"
    "v_permlane16_swap_b32 v12, v14\n\t"
    "v_permlane16_swap_b32 v13, v15\n\t"
    "v_permlane16_swap_b32 v16, v18\n\t"
    "v_permlane16_swap_b32 v17, v19\n\t"
    "v_add_f32 v12, v12, v14\n\t"
    "v_add_f32 v13, v13, v15\n\t"
    "v_add_f32 v16, v16, v18\n\t"
    "v_cvt_pk_bf16_f32 v12, v12, v13\n\t"
    "v_add_f32 v17, v17, v19\n\t"
    "ds_write_b32 v10, v12 offset:34816\n\t"
    "v_cvt_pk_bf16_f32 v16, v16, v17\n\t"
    "ds_write_b32 v10, v16 offset:35072\n\t"
    "s_waitcnt lgkmcnt(0)\n\t"
    "s_setprio 0\n\t"
    "s_barrier\n\t"
    "s_memrealtime s[36:37]\n\t"
    "s_waitcnt lgkmcnt(0)\n\t"
    "s_and_b32 s36, s36, 0xffffff\n\t"
    "s_or_b32 s36, s36, %[sn]\n\t"
    "v_lshlrev_b32 v7, 10, v6\n\t"
    "v_mov_b32 v8, s36\n\t"
    "s_mov_b64 exec, 15\n\t"
    "global_store_dword v7, v8, %[vs] offset:0 sc1\n\t"
    "s_mov_b64 exec, -1\n\t"
    "s_barrier\n\t"
    "s_memrealtime s[36:37]\n\t"
    "s_mov_b32 %[d1], %[d0]\n\t"
    "s_waitcnt lgkmcnt(0)\n\t"
    "s_sub_u32 %[d0], s36, s38\n\t"
    "s_min_u32 %[d0], %[d0], 200\n\t"
    "s_waitcnt lgkmcnt(0)\n\t"
    "s_branch 9f\n\t"
    "1:\n\t"
    "s_add_u32 %[sp], %[sp], 1\n\t"
    "s_cmp_ge_u32 %[sp], %[cap]\n\t"
    "s_cbranch_scc1 7f\n\t"
    "global_load_dwordx4 v[36:39], v6, %[ag] sc1\n\t"
    "global_load_dwordx4 v[40:43], v6, %[ag] offset:1024 sc1\n\t"
    "s_waitcnt lgkmcnt(0)\n\t"
    "s_memtime %[tc]\n\t"
    "s_nop 7\n\t"
    "s_branch 3b\n\t"
    "7:\n\t"
    "s_mov_b32 %[cap], 0\n\t"
    "s_branch 2b\n\t"
    "9:"
    : [tc] "=&s"(tc), [sp] "=&s"(sp), [cap] "+s"(cap), [d0] "+s"(d0), [d1] "+s"(d1)
    : [ag] "s"(ag), [c44] "s"(0x4444u), [vs] "s"(vs), [sn] "s"((unsigned)(size_t)od), "{v[64:95]}"(W.w0), "{v[96:127]}"(W.w1), "{v[128:159]}"(W.w2), "{v[160:191]}"(W.w3), "{v[192:223]}"(W.w4), "{v[224:255]}"(W.w5), "{v[56:63]}"(W.x)
    : "v5", "v6", "v7", "v8", "v9", "v10", "v11", "v12", "v13", "v14", "v15", "v16", "v17", "v18", "v19", "v20", "v21", "v22", "v23", "v24", "v25", "v26", "v27", "v28", "v29", "v30", "v31", "v32", "v33", "v34", "v35", "v36", "v37", "v38", "v39", "v40", "v41", "v42", "v43", "v44", "v45", "v46", "v47", "v48", "v49", "v50", "v51", "v52", "v53", "v54", "v55", "vcc", "scc", "memory", "s36", "s37", "s38");
}
template <> __device__ __forceinline__ void qk_step<5>(const QKW& W, const char* ag, const char* vs, const char* od, const char* kv, unsigned& cap, unsigned& sp,
                                                        unsigned long long& tc, unsigned long long* ts, unsigned& d0, unsigned& d1) {
  unsigned st, bg;
  asm volatile("v_mbcnt_lo_u32_b32 v52, -1, 0\n\t"
    "v_mbcnt_hi_u32_b32 v52, -1, v52\n\t"
    "v_lshlrev_b32 v6, 4, v52\n\t"
    "s_mov_b32 %[sp], 0\n\t"
    "global_load_dwordx4 v[36:39], v6, %[ag] sc1\n\t"
    "global_load_dwordx4 v[40:43], v6, %[ag] offset:1024 sc1\n\t"
    "s_memtime %[tc]\n\t"
    "v_add_u32 v7, 75776, v6\n\t"
    "v_add_u32 v8, 133120, v6\n\t"
    "ds_read_b128 v[20:23], v7 offset:8192\n\t"
    "ds_read_b128 v[24:27], v7 offset:9216\n\t"
    "ds_read_b128 v[28:31], v7 offset:0\n\t"
    "ds_read_b128 v[32:35], v7 offset:1024\n\t"
    "v_lshrrev_b32 v44, 4, v52\n\t"
    "v_and_b32 v45, 15, v52\n\t"
    "v_and_b32 v53, 4, v52\n\t"
    "v_mul_u32_u24 v53, 0x2aaa, v53\n\t"
    "v_lshrrev_b32 v9, 1, v52\n\t"
    "v_and_b32 v9, 2, v9\n\t"
    "v_or3_b32 v9, v9, v53, %[c44]\n\t"
    "v_lshlrev_b32 v53, 2, v44\n\t"
    "v_lshl_or_b32 v10, v45, 4, v53\n\t"
    "3:\n\t"
    "v_mov_b32 v12, 0\n\t"
    "v_mov_b32 v13, 0\n\t"
    "v_mov_b32 v14, 0\n\t"
    "v_mov_b32 v15, 0\n\t"
    "v_mov_b32 v16, 0\n\t"
    "v_mov_b32 v17, 0\n\t"
    "v_mov_b32 v18, 0\n\t"
    "v_mov_b32 v19, 0\n\t"
    "s_waitcnt vmcnt(0) lgkmcnt(0)\n\t"
    "14:\n\t"
    "ds_write_b128 v6, v[36:39] offset:10240\n\t"
    "ds_write_b128 v6, v[40:43] offset:11264\n\t"
    "v_smfmac_f32_16x16x64_bf16 v[12:15], v[36:39], v[160:167], v9\n\t"
    "v_max3_u32 v52, v36, v37, v38\n\t"
    "v_max3_u32 v53, v39, v40, v41\n\t"
    "v_smfmac_f32_16x16x64_bf16 v[16:19], v[36:39], v[168:175], v9\n\t"
    "v_max3_u32 v52, v52, v42, v43\n\t"
    "v_max_u32 v52, v52, v53\n\t"
    "v_cmp_eq_u32 vcc, -1, v52\n\t"
    "s_cbranch_vccnz 1f\n\t"
    "2:\n\t"
    "s_waitcnt lgkmcnt(0)\n\t"
    "s_barrier\n\t"
    "ds_read_b128 v[36:39], v6 offset:0\n\t"
    "ds_read_b128 v[44:47], v6 offset:4096\n\t"
    "v_smfmac_f32_16x16x64_bf16 v[12:15], v[40:43], v[176:183], v9\n\t"
    "ds_read_b128 v[48:51], v6 offset:5120\n\t"
    "ds_read_b128 v[52:55], v6 offset:6144\n\t"
    "v_smfmac_f32_16x16x64_bf16 v[16:19], v[40:43], v[184:191], v9\n\t"
    "ds_read_b128 v[40:43], v6 offset:1024\n\t"
    "s_sleep 0\n\t"
    "s_waitcnt lgkmcnt(4)\n\t"
    "v_smfmac_f32_16x16x64_bf16 v[12:15], v[36:39], v[56:63], v9\n\t"
    "v_smfmac_f32_16x16x64_bf16 v[16:19], v[36:39], v[28:35], v9\n\t"
    "ds_read_b128 v[28:31], v7 offset:16384\n\t"
    "ds_read_b128 v[32:35], v7 offset:17408\n\t"
    "ds_read_b128 v[36:39], v6 offset:7168\n\t"
    "s_waitcnt lgkmcnt(6)\n\t"
    "v_smfmac_f32_16x16x64_bf16 v[12:15], v[44:47], v[64:71], v9\n\t"
    "v_smfmac_f32_16x16x64_bf16 v[16:19], v[44:47], v[72:79], v9\n\t"
    "ds_read_b128 v[44:47], v6 offset:8192\n\t"
    "s_waitcnt lgkmcnt(6)\n\t"
    "v_smfmac_f32_16x16x64_bf16 v[12:15], v[48:51], v[80:87], v9\n\t"
    "v_smfmac_f32_16x16x64_bf16 v[16:19], v[48:51], v[88:95], v9\n\t"
    "ds_read_b128 v[48:51], v6 offset:9216\n\t"
    "s_waitcnt lgkmcnt(6)\n\t"
    "v_smfmac_f32_16x16x64_bf16 v[12:15], v[52:55], v[96:103], v9\n\t"
    "v_smfmac_f32_16x16x64_bf16 v[16:19], v[52:55], v[104:111], v9\n\t"
    "ds_read_b128 v[52:55], v6 offset:2048\n\t"
    "s_waitcnt lgkmcnt(6)\n\t"
    "v_smfmac_f32_16x16x64_bf16 v[12:15], v[40:43], v[20:27], v9\n\t"
    "ds_read_b128 v[20:23], v8 offset:0\n\t"
    "ds_read_b128 v[24:27], v8 offset:1024\n\t"
    "s_waitcnt lgkmcnt(6)\n\t"
    "v_smfmac_f32_16x16x64_bf16 v[16:19], v[40:43], v[28:35], v9\n\t"
    "ds_read_b128 v[28:31], v8 offset:8192\n\t"
    "ds_read_b128 v[32:35], v8 offset:9216\n\t"
    "ds_read_b128 v[40:43], v6 offset:12288\n\t"
    "s_waitcnt lgkmcnt(8)\n\t"
    "v_smfmac_f32_16x16x64_bf16 v[12:15], v[36:39], v[112:119], v9\n\t"
    "v_smfmac_f32_16x16x64_bf16 v[16:19], v[36:39], v[120:127], v9\n\t"
    "ds_read_b128 v[36:39], v6 offset:13312\n\t"
    "s_waitcnt lgkmcnt(8)\n\t"
    "v_smfmac_f32_16x16x64_bf16 v[12:15], v[44:47], v[128:135], v9\n\t"
    "v_smfmac_f32_16x16x64_bf16 v[16:19], v[44:47], v[136:143], v9\n\t"
    "ds_read_b128 v[44:47], v6 offset:14336\n\t"
    "s_barrier\n\t"
    "s_setprio 2\n\t"
    "s_waitcnt lgkmcnt(8)\n\t"
    "v_smfmac_f32_16x16x64_bf16 v[12:15], v[48:51], v[144:151], v9\n\t"
    "v_smfmac_f32_16x16x64_bf16 v[16:19], v[48:51], v[152:159], v9\n\t"
    "ds_read_b128 v[48:51], v6 offset:3072\n\t"
    "s_waitcnt lgkmcnt(6)\n\t"
    "v_smfmac_f32_16x16x64_bf16 v[12:15], v[52:55], v[20:27], v9\n\t"
    "ds_read_b128 v[20:23], v8 offset:16384\n\t"
    "ds_read_b128 v[24:27], v8 offset:17408\n\t"
    "s_waitcnt lgkmcnt(6)\n\t"
    "v_smfmac_f32_16x16x64_bf16 v[16:19], v[52:55], v[28:35], v9\n\t"
    "ds_read_b128 v[28:31], v8 offset:24576\n\t"
    "ds_read_b128 v[32:35], v8 offset:25600\n\t"
    "ds_read_b128 v[52:55], v6 offset:15360\n\t"
    "s_waitcnt lgkmcnt(8)\n\t"
    "v_smfmac_f32_16x16x64_bf16 v[12:15], v[40:43], v[192:199], v9\n\t"
    "v_smfmac_f32_16x16x64_bf16 v[16:19], v[40:43], v[200:207], v9\n\t"
    "s_waitcnt lgkmcnt(7)\n\t"
    "v_smfmac_f32_16x16x64_bf16 v[12:15], v[36:39], v[208:215], v9\n\t"
    "v_smfmac_f32_16x16x64_bf16 v[16:19], v[36:39], v[216:223], v9\n\t"
    "s_waitcnt lgkmcnt(6)\n\t"
    "v_smfmac_f32_16x16x64_bf16 v[12:15], v[44:47], v[224:231], v9\n\t"
    "v_smfmac_f32_16x16x64_bf16 v[16:19], v[44:47], v[232:239], v9\n\t"
    "s_waitcnt lgkmcnt(3)\n\t"
    "v_smfmac_f32_16x16x64_bf16 v[12:15], v[48:51], v[20:27], v9\n\t"
    "s_waitcnt lgkmcnt(1)\n\t"
    "v_smfmac_f32_16x16x64_bf16 v[16:19], v[48:51], v[28:35], v9\n\t"
    "s_waitcnt lgkmcnt(0)\n\t"
    "v_smfmac_f32_16x16x64_bf16 v[12:15], v[52:55], v[240:247], v9\n\t"
    "v_smfmac_f32_16x16x64_bf16 v[16:19], v[52:55], v[248:255], v9\n\t"
    "s_nop 6\n\t"
    "v_permlane16_swap_b32 v12, v14\n\t"
    "v_permlane16_swap_b32 v13, v15\n\t"
    "v_permlane16_swap_b32 v16, v18\n\t"
    "v_permlane16_swap_b32 v17, v19\n\t"
    "v_add_f32 v12, v12, v14\n\t"
    "v_add_f32 v13, v13, v15\n\t"
    "v_add_f32 v16, v16, v18\n\t"
    "v_cvt_pk_bf16_f32 v12, v12, v13\n\t"
    "v_add_f32 v17, v17, v19\n\t"
    "ds_write_b32 v10, v12 offset:35328\n\t"
    "v_cvt_pk_bf16_f32 v16, v16, v17\n\t"
    "ds_write_b32 v10, v16 offset:35584\n\t"
    "s_waitcnt lgkmcnt(0)\n\t"
    "s_setprio 0\n\t"
    "s_barrier\n\t"
    "ds_read_b128 v[36:39], v6 offset:34816\n\t"
    "ds_read_b128 v[40:43], v6 offset:35840\n\t"
    "s_waitcnt lgkmcnt(0)\n\t"
    "v_min_u32 v36, -2, v36\n\t"
    "v_min_u32 v37, -2, v37\n\t"
    "v_min_u32 v38, -2, v38\n\t"
    "v_min_u32 v39, -2, v39\n\t"
    "v_min_u32 v40, -2, v40\n\t"
    "v_min_u32 v41, -2, v41\n\t"
    "v_min_u32 v42, -2, v42\n\t"
    "v_min_u32 v43, -2, v43\n\t"
    "global_store_dwordx4 v6, v[36:39], %[vs] sc1\n\t"
    "global_store_dwordx4 v6, v[40:43], %[vs] offset:1024 sc1\n\t"
    "s_nop 1\n\t"
    "s_barrier\n\t"
    "s_waitcnt lgkmcnt(0)\n\t"
    "s_branch 9f\n\t"
    "1:\n\t"
    "s_add_u32 %[sp], %[sp], 1\n\t"
    "s_cmp_ge_u32 %[sp], %[cap]\n\t"
    "s_cbranch_scc1 7f\n\t"
    "global_load_dwordx4 v[36:39], v6, %[ag] sc1\n\t"
    "global_load_dwordx4 v[40:43], v6, %[ag] offset:1024 sc1\n\t"
    "s_waitcnt lgkmcnt(0)\n\t"
    "s_memtime %[tc]\n\t"
    "s_nop 7\n\t"
    "s_branch 3b\n\t"
    "7:\n\t"
    "s_mov_b32 %[cap], 0\n\t"
    "s_branch 2b\n\t"
    "9:"
    : [tc] "=&s"(tc), [sp] "=&s"(sp), [cap] "+s"(cap)
    : [ag] "s"(ag), [c44] "s"(0x4444u), [vs] "s"(vs), "{v[64:95]}"(W.w0), "{v[96:127]}"(W.w1), "{v[128:159]}"(W.w2), "{v[160:191]}"(W.w3), "{v[192:223]}"(W.w4), "{v[224:255]}"(W.w5), "{v[56:63]}"(W.x)
    : "v6", "v7", "v8", "v9", "v10", "v11", "v12", "v13", "v14", "v15", "v16", "v17", "v18", "v19", "v20", "v21", "v22", "v23", "v24", "v25", "v26", "v27", "v28", "v29", "v30", "v31", "v32", "v33", "v34", "v35", "v36", "v37", "v38", "v39", "v40", "v41", "v42", "v43", "v44", "v45", "v46", "v47", "v48", "v49", "v50", "v51", "v52", "v53", "v54", "v55", "vcc", "scc", "memory");
}
template <> __device__ __forceinline__ void qk_step<6>(const QKW& W, const char* ag, const char* vs, const char* od, const char* kv, unsigned& cap, unsigned& sp,
                                                        unsigned long long& tc, unsigned long long* ts, unsigned& d0, unsigned& d1) {
  unsigned st, bg;
  asm volatile("v_mbcnt_lo_u32_b32 v52, -1, 0\n\t"
    "v_mbcnt_hi_u32_b32 v52, -1, v52\n\t"
    "v_lshlrev_b32 v6, 4, v52\n\t"
    "s_mov_b32 %[sp], 0\n\t"
    "global_load_dwordx4 v[36:39], v6, %[ag] sc1\n\t"
    "global_load_dwordx4 v[40:43], v6, %[ag] offset:1024 sc1\n\t"
    "s_memtime %[tc]\n\t"
    "v_add_u32 v7, 77824, v6\n\t"
    "v_add_u32 v8, 135168, v6\n\t"
    "ds_read_b128 v[20:23], v7 offset:8192\n\t"
    "ds_read_b128 v[24:27], v7 offset:9216\n\t"
    "ds_read_b128 v[28:31], v7 offset:0\n\t"
    "ds_read_b128 v[32:35], v7 offset:1024\n\t"
    "v_lshrrev_b32 v44, 4, v52\n\t"
    "v_and_b32 v45, 15, v52\n\t"
    "v_and_b32 v53, 4, v52\n\t"
    "v_mul_u32_u24 v53, 0x2aaa, v53\n\t"
    "v_lshrrev_b32 v9, 1, v52\n\t"
    "v_and_b32 v9, 2, v9\n\t"
    "v_or3_b32 v9, v9, v53, %[c44]\n\t"
    "v_lshlrev_b32 v53, 2, v44\n\t"
    "v_lshl_or_b32 v10, v45, 4, v53\n\t"
    "3:\n\t"
    "v_mov_b32 v12, 0\n\t"
    "v_mov_b32 v13, 0\n\t"
    "v_mov_b32 v14, 0\n\t"
    "v_mov_b32 v15, 0\n\t"
    "v_mov_b32 v16, 0\n\t"
    "v_mov_b32 v17, 0\n\t"
    "v_mov_b32 v18, 0\n\t"
    "v_mov_b32 v19, 0\n\t"
    "s_waitcnt vmcnt(0) lgkmcnt(0)\n\t"
    "14:\n\t"
    "ds_write_b128 v6, v[36:39] offset:12288\n\t"
    "ds_write_b128 v6, v[40:43] offset:13312\n\t"
    "v_smfmac_f32_16x16x64_bf16 v[12:15], v[36:39], v[192:199], v9\n\t"
    "v_max3_u32 v52, v36, v37, v38\n\t"
    "v_max3_u32 v53, v39, v40, v41\n\t"
    "v_smfmac_f32_16x16x64_bf16 v[16:19], v[36:39], v[200:207], v9\n\t"
    "v_max3_u32 v52, v52, v42, v43\n\t"
    "v_max_u32 v52, v52, v53\n\t"
    "v_cmp_eq_u32 vcc, -1, v52\n\t"
    "s_cbranch_vccnz 1f\n\t"
    "2:\n\t"
    "s_waitcnt lgkmcnt(0)\n\t"
    "s_barrier\n\t"
    "ds_read_b128 v[36:39], v6 offset:0\n\t"
    "ds_read_b128 v[44:47], v6 offset:4096\n\t"
    "v_smfmac_f32_16x16x64_bf16 v[12:15], v[40:43], v[208:215], v9\n\t"
    "ds_read_b128 v[48:51], v6 offset:5120\n\t"
    "ds_read_b128 v[52:55], v6 offset:6144\n\t"
    "v_smfmac_f32_16x16x64_bf16 v[16:19], v[40:43], v[216:223], v9\n\t"
    "ds_read_b128 v[40:43], v6 offset:1024\n\t"
    "s_sleep 0\n\t"
    "s_waitcnt lgkmcnt(4)\n\t"
    "v_smfmac_f32_16x16x64_bf16 v[12:15], v[36:39], v[56:63], v9\n\t"
    "v_smfmac_f32_16x16x64_bf16 v[16:19], v[36:39], v[28:35], v9\n\t"
    "ds_read_b128 v[28:31], v7 offset:16384\n\t"
    "ds_read_b128 v[32:35], v7 offset:17408\n\t"
    "ds_read_b128 v[36:39], v6 offset:7168\n\t"
    "s_waitcnt lgkmcnt(6)\n\t"
    "v_smfmac_f32_16x16x64_bf16 v[12:15], v[44:47], v[64:71], v9\n\t"
    "v_smfmac_f32_16x16x64_bf16 v[16:19], v[44:47], v[72:79], v9\n\t"
    "ds_read_b128 v[44:47], v6 offset:8192\n\t"
    "s_waitcnt lgkmcnt(6)\n\t"
    "v_smfmac_f32_16x16x64_bf16 v[12:15], v[48:51], v[80:87], v9\n\t"
    "v_smfmac_f32_16x16x64_bf16 v[16:19], v[48:51], v[88:95], v9\n\t"
    "ds_read_b128 v[48:51], v6 offset:9216\n\t"
    "s_waitcnt lgkmcnt(6)\n\t"
    "v_smfmac_f32_16x16x64_bf16 v[12:15], v[52:55], v[96:103], v9\n\t"
    "v_smfmac_f32_16x16x64_bf16 v[16:19], v[52:55], v[104:111], v9\n\t"
    "ds_read_b128 v[52:55], v6 offset:2048\n\t"
    "s_waitcnt lgkmcnt(6)\n\t"
    "v_smfmac_f32_16x16x64_bf16 v[12:15], v[40:43], v[20:27], v9\n\t"
    "ds_read_b128 v[20:23], v8 offset:0\n\t"
    "ds_read_b128 v[24:27], v8 offset:1024\n\t"
    "s_waitcnt lgkmcnt(6)\n\t"
    "v_smfmac_f32_16x16x64_bf16 v[16:19], v[40:43], v[28:35], v9\n\t"
    "ds_read_b128 v[28:31], v8 offset:8192\n\t"
    "ds_read_b128 v[32:35], v8 offset:9216\n\t"
    "ds_read_b128 v[40:43], v6 offset:10240\n\t"
    "s_waitcnt lgkmcnt(8)\n\t"
    "v_smfmac_f32_16x16x64_bf16 v[12:15], v[36:39], v[112:119], v9\n\t"
    "v_smfmac_f32_16x16x64_bf16 v[16:19], v[36:39], v[120:127], v9\n\t"
    "ds_read_b128 v[36:39], v6 offset:11264\n\t"
    "s_waitcnt lgkmcnt(8)\n\t"
    "v_smfmac_f32_16x16x64_bf16 v[12:15], v[44:47], v[128:135], v9\n\t"
    "v_smfmac_f32_16x16x64_bf16 v[16:19], v[44:47], v[136:143], v9\n\t"
    "ds_read_b128 v[44:47], v6 offset:14336\n\t"
    "s_barrier\n\t"
    "s_setprio 2\n\t"
    "s_waitcnt lgkmcnt(8)\n\t"
    "v_smfmac_f32_16x16x64_bf16 v[12:15], v[48:51], v[144:151], v9\n\t"
    "v_smfmac_f32_16x16x64_bf16 v[16:19], v[48:51], v[152:159], v9\n\t"
    "ds_read_b128 v[48:51], v6 offset:3072\n\t"
    "s_waitcnt lgkmcnt(6)\n\t"
    "v_smfmac_f32_16x16x64_bf16 v[12:15], v[52:55], v[20:27], v9\n\t"
    "ds_read_b128 v[20:23], v8 offset:16384\n\t"
    "ds_read_b128 v[24:27], v8 offset:17408\n\t"
    "s_waitcnt lgkmcnt(6)\n\t"
    "v_smfmac_f32_16x16x64_bf16 v[16:19], v[52:55], v[28:35], v9\n\t"
    "ds_read_b128 v[28:31], v8 offset:24576\n\t"
    "ds_read_b128 v[32:35], v8 offset:25600\n\t"
    "ds_read_b128 v[52:55], v6 offset:15360\n\t"
    "s_waitcnt lgkmcnt(8)\n\t"
    "v_smfmac_f32_16x16x64_bf16 v[12:15], v[40:43], v[160:167], v9\n\t"
    "v_smfmac_f32_16x16x64_bf16 v[16:19], v[40:43], v[168:175], v9\n\t"
    "s_waitcnt lgkmcnt(7)\n\t"
    "v_smfmac_f32_16x16x64_bf16 v[12:15], v[36:39], v[176:183], v9\n\t"
    "v_smfmac_f32_16x16x64_bf16 v[16:19], v[36:39], v[184:191], v9\n\t"
    "s_waitcnt lgkmcnt(6)\n\t"
    "v_smfmac_f32_16x16x64_bf16 v[12:15], v[44:47], v[224:231], v9\n\t"
    "v_smfmac_f32_16x16x64_bf16 v[16:19], v[44:47], v[232:239], v9\n\t"
    "s_waitcnt lgkmcnt(3)\n\t"
    "v_smfmac_f32_16x16x64_bf16 v[12:15], v[48:51], v[20:27], v9\n\t"
    "s_waitcnt lgkmcnt(1)\n\t"
    "v_smfmac_f32_16x16x64_bf16 v[16:19], v[48:51], v[28:35], v9\n\t"
    "s_waitcnt lgkmcnt(0)\n\t"
    "v_smfmac_f32_16x16x64_bf16 v[12:15], v[52:55], v[240:247], v9\n\t"
    "v_smfmac_f32_16x16x64_bf16 v[16:19], v[52:55], v[248:255], v9\n\t"
    "s_nop 6\n\t"
    "v_permlane16_swap_b32 v12, v14\n\t"
    "v_permlane16_swap_b32 v13, v15\n\t"
    "v_permlane16_swap_b32 v16, v18\n\t"
    "v_permlane16_swap_b32 v17, v19\n\t"
    "v_add_f32 v12, v12, v14\n\t"
    "v_add_f32 v13, v13, v15\n\t"
    "v_add_f32 v16, v16, v18\n\t"
    "v_cvt_pk_bf16_f32 v12, v12, v13\n\t"
    "v_add_f32 v17, v17, v19\n\t"
    "ds_write_b32 v10, v12 offset:35840\n\t"
    "v_cvt_pk_bf16_f32 v16, v16, v17\n\t"
    "ds_write_b32 v10, v16 offset:36096\n\t"
    "s_waitcnt lgkmcnt(0)\n\t"
    "s_setprio 0\n\t"
    "s_barrier\n\t"
    "s_barrier\n\t"
    "s_waitcnt lgkmcnt(0)\n\t"
    "s_branch 9f\n\t"
    "1:\n\t"
    "s_add_u32 %[sp], %[sp], 1\n\t"
    "s_cmp_ge_u32 %[sp], %[cap]\n\t"
    "s_cbranch_scc1 7f\n\t"
    "global_load_dwordx4 v[36:39], v6, %[ag] sc1\n\t"
    "global_load_dwordx4 v[40:43], v6, %[ag] offset:1024 sc1\n\t"
    "s_waitcnt lgkmcnt(0)\n\t"
    "s_memtime %[tc]\n\t"
    "s_nop 7\n\t"
    "s_branch 3b\n\t"
    "7:\n\t"
    "s_mov_b32 %[cap], 0\n\t"
    "s_branch 2b\n\t"
    "9:"
    : [tc] "=&s"(tc), [sp] "=&s"(sp), [cap] "+s"(cap)
    : [ag] "s"(ag), [c44] "s"(0x4444u), "{v[64:95]}"(W.w0), "{v[96:127]}"(W.w1), "{v[128:159]}"(W.w2), "{v[160:191]}"(W.w3), "{v[192:223]}"(W.w4), "{v[224:255]}"(W.w5), "{v[56:63]}"(W.x)
    : "v6", "v7", "v8", "v9", "v10", "v11", "v12", "v13", "v14", "v15", "v16", "v17", "v18", "v19", "v20", "v21", "v22", "v23", "v24", "v25", "v26", "v27", "v28", "v29", "v30", "v31", "v32", "v33", "v34", "v35", "v36", "v37", "v38", "v39", "v40", "v41", "v42", "v43", "v44", "v45", "v46", "v47", "v48", "v49", "v50", "v51", "v52", "v53", "v54", "v55", "vcc", "scc", "memory");
}
template <> __device__ __forceinline__ void qk_step<7>(const QKW& W, const char* ag, const char* vs, const char* od, const char* kv, unsigned& cap, unsigned& sp,
                                                        unsigned long long& tc, unsigned long long* ts, unsigned& d0, unsigned& d1) {
  unsigned st, bg;
  asm volatile("v_mbcnt_lo_u32_b32 v52, -1, 0\n\t"
    "v_mbcnt_hi_u32_b32 v52, -1, v52\n\t"
    "v_lshlrev_b32 v6, 4, v52\n\t"
    "s_mov_b32 %[sp], 0\n\t"
    "global_load_dwordx4 v[36:39], v6, %[ag] sc1\n\t"
    "global_load_dwordx4 v[40:43], v6, %[ag] offset:1024 sc1\n\t"
    "s_memtime %[tc]\n\t"
    "v_add_u32 v7, 79872, v6\n\t"
    "v_add_u32 v8, 137216, v6\n\t"
    "ds_read_b128 v[20:23], v7 offset:8192\n\t"
    "ds_read_b128 v[24:27], v7 offset:9216\n\t"
    "ds_read_b128 v[28:31], v7 offset:0\n\t"
    "ds_read_b128 v[32:35], v7 offset:1024\n\t"
    "v_lshrrev_b32 v44, 4, v52\n\t"
    "v_and_b32 v45, 15, v52\n\t"
    "v_and_b32 v53, 4, v52\n\t"
    "v_mul_u32_u24 v53, 0x2aaa, v53\n\t"
    "v_lshrrev_b32 v9, 1, v52\n\t"
    "v_and_b32 v9, 2, v9\n\t"
    "v_or3_b32 v9, v9, v53, %[c44]\n\t"
    "v_lshlrev_b32 v53, 2, v44\n\t"
    "v_lshl_or_b32 v10, v45, 4, v53\n\t"
    "3:\n\t"
    "v_mov_b32 v12, 0\n\t"
    "v_mov_b32 v13, 0\n\t"
    "v_mov_b32 v14, 0\n\t"
    "v_mov_b32 v15, 0\n\t"
    "v_mov_b32 v16, 0\n\t"
    "v_mov_b32 v17, 0\n\t"
    "v_mov_b32 v18, 0\n\t"
    "v_mov_b32 v19, 0\n\t"
    "s_waitcnt vmcnt(0) lgkmcnt(0)\n\t"
    "14:\n\t"
    "ds_write_b128 v6, v[36:39] offset:14336\n\t"
    "ds_write_b128 v6, v[40:43] offset:15360\n\t"
    "v_smfmac_f32_16x16x64_bf16 v[12:15], v[36:39], v[224:231], v9\n\t"
    "v_max3_u32 v52, v36, v37, v38\n\t"
    "v_max3_u32 v53, v39, v40, v41\n\t"
    "v_smfmac_f32_16x16x64_bf16 v[16:19], v[36:39], v[232:239], v9\n\t"
    "v_max3_u32 v52, v52, v42, v43\n\t"
    "v_max_u32 v52, v52, v53\n\t"
    "v_cmp_eq_u32 vcc, -1, v52\n\t"
    "s_cbranch_vccnz 1f\n\t"
    "2:\n\t"
    "s_waitcnt lgkmcnt(0)\n\t"
    "s_barrier\n\t"
    "ds_read_b128 v[36:39], v6 offset:0\n\t"
    "ds_read_b128 v[44:47], v6 offset:4096\n\t"
    "v_smfmac_f32_16x16x64_bf16 v[12:15], v[40:43], v[240:247], v9\n\t"
    "ds_read_b128 v[48:51], v6 offset:5120\n\t"
    "ds_read_b128 v[52:55], v6 offset:6144\n\t"
    "v_smfmac_f32_16x16x64_bf16 v[16:19], v[40:43], v[248:255], v9\n\t"
    "ds_read_b128 v[40:43], v6 offset:1024\n\t"
    "s_sleep 0\n\t"
    "s_waitcnt lgkmcnt(4)\n\t"
    "v_smfmac_f32_16x16x64_bf16 v[12:15], v[36:39], v[56:63], v9\n\t"
    "v_smfmac_f32_16x16x64_bf16 v[16:19], v[36:39], v[28:35], v9\n\t"
    "ds_read_b128 v[28:31], v7 offset:16384\n\t"
    "ds_read_b128 v[32:35], v7 offset:17408\n\t"
    "ds_read_b128 v[36:39], v6 offset:7168\n\t"
    "s_waitcnt lgkmcnt(6)\n\t"
    "v_smfmac_f32_16x16x64_bf16 v[12:15], v[44:47], v[64:71], v9\n\t"
    "v_smfmac_f32_16x16x64_bf16 v[16:19], v[44:47], v[72:79], v9\n\t"
    "ds_read_b128 v[44:47], v6 offset:8192\n\t"
    "s_waitcnt lgkmcnt(6)\n\t"
    "v_smfmac_f32_16x16x64_bf16 v[12:15], v[48:51], v[80:87], v9\n\t"
    "v_smfmac_f32_16x16x64_bf16 v[16:19], v[48:51], v[88:95], v9\n\t"
    "ds_read_b128 v[48:51], v6 offset:9216\n\t"
    "s_waitcnt lgkmcnt(6)\n\t"
    "v_smfmac_f32_16x16x64_bf16 v[12:15], v[52:55], v[96:103], v9\n\t"
    "v_smfmac_f32_16x16x64_bf16 v[16:19], v[52:55], v[104:111], v9\n\t"
    "ds_read_b128 v[52:55], v6 offset:2048\n\t"
    "s_waitcnt lgkmcnt(6)\n\t"
    "v_smfmac_f32_16x16x64_bf16 v[12:15], v[40:43], v[20:27], v9\n\t"
    "ds_read_b128 v[20:23], v8 offset:0\n\t"
    "ds_read_b128 v[24:27], v8 offset:1024\n\t"
    "s_waitcnt lgkmcnt(6)\n\t"
    "v_smfmac_f32_16x16x64_bf16 v[16:19], v[40:43], v[28:35], v9\n\t"
    "ds_read_b128 v[28:31], v8 offset:8192\n\t"
    "ds_read_b128 v[32:35], v8 offset:9216\n\t"
    "ds_read_b128 v[40:43], v6 offset:10240\n\t"
    "s_waitcnt lgkmcnt(8)\n\t"
    "v_smfmac_f32_16x16x64_bf16 v[12:15], v[36:39], v[112:119], v9\n\t"
    "v_smfmac_f32_16x16x64_bf16 v[16:19], v[36:39], v[120:127], v9\n\t"
    "ds_read_b128 v[36:39], v6 offset:11264\n\t"
    "s_waitcnt lgkmcnt(8)\n\t"
    "v_smfmac_f32_16x16x64_bf16 v[12:15], v[44:47], v[128:135], v9\n\t"
    "v_smfmac_f32_16x16x64_bf16 v[16:19], v[44:47], v[136:143], v9\n\t"
    "ds_read_b128 v[44:47], v6 offset:12288\n\t"
    "s_barrier\n\t"
    "s_setprio 2\n\t"
    "s_waitcnt lgkmcnt(8)\n\t"
    "v_smfmac_f32_16x16x64_bf16 v[12:15], v[48:51], v[144:151], v9\n\t"
    "v_smfmac_f32_16x16x64_bf16 v[16:19], v[48:51], v[152:159], v9\n\t"
    "ds_read_b128 v[48:51], v6 offset:3072\n\t"
    "s_waitcnt lgkmcnt(6)\n\t"
    "v_smfmac_f32_16x16x64_bf16 v[12:15], v[52:55], v[20:27], v9\n\t"
    "ds_read_b128 v[20:23], v8 offset:16384\n\t"
    "ds_read_b128 v[24:27], v8 offset:17408\n\t"
    "s_waitcnt lgkmcnt(6)\n\t"
    "v_smfmac_f32_16x16x64_bf16 v[16:19], v[52:55], v[28:35], v9\n\t"
    "ds_read_b128 v[28:31], v8 offset:24576\n\t"
    "ds_read_b128 v[32:35], v8 offset:25600\n\t"
    "ds_read_b128 v[52:55], v6 offset:13312\n\t"
    "s_waitcnt lgkmcnt(8)\n\t"
    "v_smfmac_f32_16x16x64_bf16 v[12:15], v[40:43], v[160:167], v9\n\t"
    "v_smfmac_f32_16x16x64_bf16 v[16:19], v[40:43], v[168:175], v9\n\t"
    "s_waitcnt lgkmcnt(7)\n\t"
    "v_smfmac_f32_16x16x64_bf16 v[12:15], v[36:39], v[176:183], v9\n\t"
    "v_smfmac_f32_16x16x64_bf16 v[16:19], v[36:39], v[184:191], v9\n\t"
    "s_waitcnt lgkmcnt(6)\n\t"
    "v_smfmac_f32_16x16x64_bf16 v[12:15], v[44:47], v[192:199], v9\n\t"
    "v_smfmac_f32_16x16x64_bf16 v[16:19], v[44:47], v[200:207], v9\n\t"
    "s_waitcnt lgkmcnt(3)\n\t"
    "v_smfmac_f32_16x16x64_bf16 v[12:15], v[48:51], v[20:27], v9\n\t"
    "s_waitcnt lgkmcnt(1)\n\t"
    "v_smfmac_f32_16x16x64_bf16 v[16:19], v[48:51], v[28:35], v9\n\t"
    "s_waitcnt lgkmcnt(0)\n\t"
    "v_smfmac_f32_16x16x64_bf16 v[12:15], v[52:55], v[208:215], v9\n\t"
    "v_smfmac_f32_16x16x64_bf16 v[16:19], v[52:55], v[216:223], v9\n\t"
    "s_nop 6\n\t"
    "v_permlane16_swap_b32 v12, v14\n\t"
    "v_permlane16_swap_b32 v13, v15\n\t"
    "v_permlane16_swap_b32 v16, v18\n\t"
    "v_permlane16_swap_b32 v17, v19\n\t"
    "v_add_f32 v12, v12, v14\n\t"
    "v_add_f32 v13, v13, v15\n\t"
    "v_add_f32 v16, v16, v18\n\t"
    "v_cvt_pk_bf16_f32 v12, v12, v13\n\t"
    "v_add_f32 v17, v17, v19\n\t"
    "ds_write_b32 v10, v12 offset:36352\n\t"
    "v_cvt_pk_bf16_f32 v16, v16, v17\n\t"
    "ds_write_b32 v10, v16 offset:36608\n\t"
    "s_waitcnt lgkmcnt(0)\n\t"
    "s_setprio 0\n\t"
    "s_barrier\n\t"
    "s_barrier\n\t"
    "s_waitcnt lgkmcnt(0)\n\t"
    "s_branch 9f\n\t"
    "1:\n\t"
    "s_add_u32 %[sp], %[sp], 1\n\t"
    "s_cmp_ge_u32 %[sp], %[cap]\n\t"
    "s_cbranch_scc1 7f\n\t"
    "global_load_dwordx4 v[36:39], v6, %[ag] sc1\n\t"
    "global_load_dwordx4 v[40:43], v6, %[ag] offset:1024 sc1\n\t"
    "s_waitcnt lgkmcnt(0)\n\t"
    "s_memtime %[tc]\n\t"
    "s_nop 7\n\t"
    "s_branch 3b\n\t"
    "7:\n\t"
    "s_mov_b32 %[cap], 0\n\t"
    "s_branch 2b\n\t"
    "9:"
    : [tc] "=&s"(tc), [sp] "=&s"(sp), [cap] "+s"(cap)
    : [ag] "s"(ag), [c44] "s"(0x4444u), "{v[64:95]}"(W.w0), "{v[96:127]}"(W.w1), "{v[128:159]}"(W.w2), "{v[160:191]}"(W.w3), "{v[192:223]}"(W.w4), "{v[224:255]}"(W.w5), "{v[56:63]}"(W.x)
    : "v6", "v7", "v8", "v9", "v10", "v11", "v12", "v13", "v14", "v15", "v16", "v17", "v18", "v19", "v20", "v21", "v22", "v23", "v24", "v25", "v26", "v27", "v28", "v29", "v30", "v31", "v32", "v33", "v34", "v35", "v36", "v37", "v38", "v39", "v40", "v41", "v42", "v43", "v44", "v45", "v46", "v47", "v48", "v49", "v50", "v51", "v52", "v53", "v54", "v55", "vcc", "scc", "memory");
}
template <int I> __device__ __forceinline__ void qk_wload(u32x32& w, unsigned a0, unsigned a1);
template <> __device__ __forceinline__ void qk_wload<0>(u32x32& w, unsigned a0, unsigned a1) {
  asm volatile("ds_read_b64_tr_b16 v[64:65], %1 offset:0\n\t"
    "ds_read_b64_tr_b16 v[66:67], %1 offset:512\n\t"
    "ds_read_b64_tr_b16 v[68:69], %1 offset:4096\n\t"
    "ds_read_b64_tr_b16 v[70:71], %1 offset:4608\n\t"
    "ds_read_b64_tr_b16 v[72:73], %2 offset:0\n\t"
    "ds_read_b64_tr_b16 v[74:75], %2 offset:512\n\t"
    "ds_read_b64_tr_b16 v[76:77], %2 offset:4096\n\t"
    "ds_read_b64_tr_b16 v[78:79], %2 offset:4608\n\t"
    "s_waitcnt lgkmcnt(0)\n\t"
    "ds_read_b64_tr_b16 v[80:81], %1 offset:16384\n\t"
    "ds_read_b64_tr_b16 v[82:83], %1 offset:16896\n\t"
    "ds_read_b64_tr_b16 v[84:85], %1 offset:20480\n\t"
    "ds_read_b64_tr_b16 v[86:87], %1 offset:20992\n\t"
    "ds_read_b64_tr_b16 v[88:89], %2 offset:16384\n\t"
    "ds_read_b64_tr_b16 v[90:91], %2 offset:16896\n\t"
    "ds_read_b64_tr_b16 v[92:93], %2 offset:20480\n\t"
    "ds_read_b64_tr_b16 v[94:95], %2 offset:20992\n\t"
    "s_waitcnt lgkmcnt(0)"
    : "=&{v[64:95]}"(w) : "v"(a0), "v"(a1) : "memory");
}
template <> __device__ __forceinline__ void qk_wload<1>(u32x32& w, unsigned a0, unsigned a1) {
  asm volatile("ds_read_b64_tr_b16 v[96:97], %1 offset:0\n\t"
    "ds_read_b64_tr_b16 v[98:99], %1 offset:512\n\t"
    "ds_read_b64_tr_b16 v[100:101], %1 offset:4096\n\t"
    "ds_read_b64_tr_b16 v[102:103], %1 offset:4608\n\t"
    "ds_read_b64_tr_b16 v[104:105], %2 offset:0\n\t"
    "ds_read_b64_tr_b16 v[106:107], %2 offset:512\n\t"
    "ds_read_b64_tr_b16 v[108:109], %2 offset:4096\n\t"
    "ds_read_b64_tr_b16 v[110:111], %2 offset:4608\n\t"
    "s_waitcnt lgkmcnt(0)\n\t"
    "ds_read_b64_tr_b16 v[112:113], %1 offset:16384\n\t"
    "ds_read_b64_tr_b16 v[114:115], %1 offset:16896\n\t"
    "ds_read_b64_tr_b16 v[116:117], %1 offset:20480\n\t"
    "ds_read_b64_tr_b16 v[118:119], %1 offset:20992\n\t"
    "ds_read_b64_tr_b16 v[120:121], %2 offset:16384\n\t"
    "ds_read_b64_tr_b16 v[122:123], %2 offset:16896\n\t"
    "ds_read_b64_tr_b16 v[124:125], %2 offset:20480\n\t"
    "ds_read_b64_tr_b16 v[126:127], %2 offset:20992\n\t"
    "s_waitcnt lgkmcnt(0)"
    : "=&{v[96:127]}"(w) : "v"(a0), "v"(a1) : "memory");
}
template <> __device__ __forceinline__ void qk_wload<2>(u32x32& w, unsigned a0, unsigned a1) {
  asm volatile("ds_read_b64_tr_b16 v[128:129], %1 offset:0\n\t"
    "ds_read_b64_tr_b16 v[130:131], %1 offset:512\n\t"
    "ds_read_b64_tr_b16 v[132:133], %1 offset:4096\n\t"
    "ds_read_b64_tr_b16 v[134:135], %1 offset:4608\n\t"
    "ds_read_b64_tr_b16 v[136:137], %2 offset:0\n\t"
    "ds_read_b64_tr_b16 v[138:139], %2 offset:512\n\t"
    "ds_read_b64_tr_b16 v[140:141], %2 offset:4096\n\t"
    "ds_read_b64_tr_b16 v[142:143], %2 offset:4608\n\t"
    "s_waitcnt lgkmcnt(0)\n\t"
    "ds_read_b64_tr_b16 v[144:145], %1 offset:16384\n\t"
    "ds_read_b64_tr_b16 v[146:147], %1 offset:16896\n\t"
    "ds_read_b64_tr_b16 v[148:149], %1 offset:20480\n\t"
    "ds_read_b64_tr_b16 v[150:151], %1 offset:20992\n\t"
    "ds_read_b64_tr_b16 v[152:153], %2 offset:16384\n\t"
    "ds_read_b64_tr_b16 v[154:155], %2 offset:16896\n\t"
    "ds_read_b64_tr_b16 v[156:157], %2 offset:20480\n\t"
    "ds_read_b64_tr_b16 v[158:159], %2 offset:20992\n\t"
    "s_waitcnt lgkmcnt(0)"
    : "=&{v[128:159]}"(w) : "v"(a0), "v"(a1) : "memory");
}
template <> __device__ __forceinline__ void qk_wload<3>(u32x32& w, unsigned a0, unsigned a1) {
  asm volatile("ds_read_b64_tr_b16 v[160:161], %1 offset:0\n\t"
    "ds_read_b64_tr_b16 v[162:163], %1 offset:512\n\t"
    "ds_read_b64_tr_b16 v[164:165], %1 offset:4096\n\t"
    "ds_read_b64_tr_b16 v[166:167], %1 offset:4608\n\t"
    "ds_read_b64_tr_b16 v[168:169], %2 offset:0\n\t"
    "ds_read_b64_tr_b16 v[170:171], %2 offset:512\n\t"
    "ds_read_b64_tr_b16 v[172:173], %2 offset:4096\n\t"
    "ds_read_b64_tr_b16 v[174:175], %2 offset:4608\n\t"
    "s_waitcnt lgkmcnt(0)\n\t"
    "ds_read_b64_tr_b16 v[176:177], %1 offset:16384\n\t"
    "ds_read_b64_tr_b16 v[178:179], %1 offset:16896\n\t"
    "ds_read_b64_tr_b16 v[180:181], %1 offset:20480\n\t"
    "ds_read_b64_tr_b16 v[182:183], %1 offset:20992\n\t"
    "ds_read_b64_tr_b16 v[184:185], %2 offset:16384\n\t"
    "ds_read_b64_tr_b16 v[186:187], %2 offset:16896\n\t"
    "ds_read_b64_tr_b16 v[188:189], %2 offset:20480\n\t"
    "ds_read_b64_tr_b16 v[190:191], %2 offset:20992\n\t"
    "s_waitcnt lgkmcnt(0)"
    : "=&{v[160:191]}"(w) : "v"(a0), "v"(a1) : "memory");
}
template <> __device__ __forceinline__ void qk_wload<4>(u32x32& w, unsigned a0, unsigned a1) {
  asm volatile("ds_read_b64_tr_b16 v[192:193], %1 offset:0\n\t"
    "ds_read_b64_tr_b16 v[194:195], %1 offset:512\n\t"
    "ds_read_b64_tr_b16 v[196:197], %1 offset:4096\n\t"
    "ds_read_b64_tr_b16 v[198:199], %1 offset:4608\n\t"
    "ds_read_b64_tr_b16 v[200:201], %2 offset:0\n\t"
    "ds_read_b64_tr_b16 v[202:203], %2 offset:512\n\t"
    "ds_read_b64_tr_b16 v[204:205], %2 offset:4096\n\t"
    "ds_read_b64_tr_b16 v[206:207], %2 offset:4608\n\t"
    "s_waitcnt lgkmcnt(0)\n\t"
    "ds_read_b64_tr_b16 v[208:209], %1 offset:16384\n\t"
    "ds_read_b64_tr_b16 v[210:211], %1 offset:16896\n\t"
    "ds_read_b64_tr_b16 v[212:213], %1 offset:20480\n\t"
    "ds_read_b64_tr_b16 v[214:215], %1 offset:20992\n\t"
    "ds_read_b64_tr_b16 v[216:217], %2 offset:16384\n\t"
    "ds_read_b64_tr_b16 v[218:219], %2 offset:16896\n\t"
    "ds_read_b64_tr_b16 v[220:221], %2 offset:20480\n\t"
    "ds_read_b64_tr_b16 v[222:223], %2 offset:20992\n\t"
    "s_waitcnt lgkmcnt(0)"
    : "=&{v[192:223]}"(w) : "v"(a0), "v"(a1) : "memory");
}
template <> __device__ __forceinline__ void qk_wload<5>(u32x32& w, unsigned a0, unsigned a1) {
  asm volatile("ds_read_b64_tr_b16 v[224:225], %1 offset:0\n\t"
    "ds_read_b64_tr_b16 v[226:227], %1 offset:512\n\t"
    "ds_read_b64_tr_b16 v[228:229], %1 offset:4096\n\t"
    "ds_read_b64_tr_b16 v[230:231], %1 offset:4608\n\t"
    "ds_read_b64_tr_b16 v[232:233], %2 offset:0\n\t"
    "ds_read_b64_tr_b16 v[234:235], %2 offset:512\n\t"
    "ds_read_b64_tr_b16 v[236:237], %2 offset:4096\n\t"
    "ds_read_b64_tr_b16 v[238:239], %2 offset:4608\n\t"
    "s_waitcnt lgkmcnt(0)\n\t"
    "ds_read_b64_tr_b16 v[240:241], %1 offset:16384\n\t"
    "ds_read_b64_tr_b16 v[242:243], %1 offset:16896\n\t"
    "ds_read_b64_tr_b16 v[244:245], %1 offset:20480\n\t"
    "ds_read_b64_tr_b16 v[246:247], %1 offset:20992\n\t"
    "ds_read_b64_tr_b16 v[248:249], %2 offset:16384\n\t"
    "ds_read_b64_tr_b16 v[250:251], %2 offset:16896\n\t"
    "ds_read_b64_tr_b16 v[252:253], %2 offset:20480\n\t"
    "ds_read_b64_tr_b16 v[254:255], %2 offset:20992\n\t"
    "s_waitcnt lgkmcnt(0)"
    : "=&{v[224:255]}"(w) : "v"(a0), "v"(a1) : "memory");
}
template <int I> __device__ __forceinline__ void vr_wload(u32x32& w, unsigned a);
template <> __device__ __forceinline__ void vr_wload<0>(u32x32& w, unsigned a) {
  asm volatile("ds_read_b64_tr_b16 v[64:65], %1 offset:0\n\t"
    "ds_read_b64_tr_b16 v[66:67], %1 offset:256\n\t"
    "ds_read_b64_tr_b16 v[68:69], %1 offset:2048\n\t"
    "ds_read_b64_tr_b16 v[70:71], %1 offset:2304\n\t"
    "ds_read_b64_tr_b16 v[72:73], %1 offset:32\n\t"
    "ds_read_b64_tr_b16 v[74:75], %1 offset:288\n\t"
    "ds_read_b64_tr_b16 v[76:77], %1 offset:2080\n\t"
    "ds_read_b64_tr_b16 v[78:79], %1 offset:2336\n\t"
    "s_waitcnt lgkmcnt(0)\n\t"
    "ds_read_b64_tr_b16 v[80:81], %1 offset:64\n\t"
    "ds_read_b64_tr_b16 v[82:83], %1 offset:320\n\t"
    "ds_read_b64_tr_b16 v[84:85], %1 offset:2112\n\t"
    "ds_read_b64_tr_b16 v[86:87], %1 offset:2368\n\t"
    "ds_read_b64_tr_b16 v[88:89], %1 offset:96\n\t"
    "ds_read_b64_tr_b16 v[90:91], %1 offset:352\n\t"
    "ds_read_b64_tr_b16 v[92:93], %1 offset:2144\n\t"
    "ds_read_b64_tr_b16 v[94:95], %1 offset:2400\n\t"
    "s_waitcnt lgkmcnt(0)"
    : "=&{v[64:95]}"(w) : "v"(a) : "memory");
}
template <> __device__ __forceinline__ void vr_wload<1>(u32x32& w, unsigned a) {
  asm volatile("ds_read_b64_tr_b16 v[96:97], %1 offset:8192\n\t"
    "ds_read_b64_tr_b16 v[98:99], %1 offset:8448\n\t"
    "ds_read_b64_tr_b16 v[100:101], %1 offset:10240\n\t"
    "ds_read_b64_tr_b16 v[102:103], %1 offset:10496\n\t"
    "ds_read_b64_tr_b16 v[104:105], %1 offset:8224\n\t"
    "ds_read_b64_tr_b16 v[106:107], %1 offset:8480\n\t"
    "ds_read_b64_tr_b16 v[108:109], %1 offset:10272\n\t"
    "ds_read_b64_tr_b16 v[110:111], %1 offset:10528\n\t"
    "s_waitcnt lgkmcnt(0)\n\t"
    "ds_read_b64_tr_b16 v[112:113], %1 offset:8256\n\t"
    "ds_read_b64_tr_b16 v[114:115], %1 offset:8512\n\t"
    "ds_read_b64_tr_b16 v[116:117], %1 offset:10304\n\t"
    "ds_read_b64_tr_b16 v[118:119], %1 offset:10560\n\t"
    "ds_read_b64_tr_b16 v[120:121], %1 offset:8288\n\t"
    "ds_read_b64_tr_b16 v[122:123], %1 offset:8544\n\t"
    "ds_read_b64_tr_b16 v[124:125], %1 offset:10336\n\t"
    "ds_read_b64_tr_b16 v[126:127], %1 offset:10592\n\t"
    "s_waitcnt lgkmcnt(0)"
    : "=&{v[96:127]}"(w) : "v"(a) : "memory");
}
template <> __device__ __forceinline__ void vr_wload<2>(u32x32& w, unsigned a) {
  asm volatile("ds_read_b64_tr_b16 v[128:129], %1 offset:0\n\t"
    "ds_read_b64_tr_b16 v[130:131], %1 offset:128\n\t"
    "ds_read_b64_tr_b16 v[132:133], %1 offset:1024\n\t"
    "ds_read_b64_tr_b16 v[134:135], %1 offset:1152\n\t"
    "ds_read_b64_tr_b16 v[136:137], %1 offset:32\n\t"
    "ds_read_b64_tr_b16 v[138:139], %1 offset:160\n\t"
    "ds_read_b64_tr_b16 v[140:141], %1 offset:1056\n\t"
    "ds_read_b64_tr_b16 v[142:143], %1 offset:1184\n\t"
    "s_waitcnt lgkmcnt(0)\n\t"
    "ds_read_b64_tr_b16 v[144:145], %1 offset:4096\n\t"
    "ds_read_b64_tr_b16 v[146:147], %1 offset:4224\n\t"
    "ds_read_b64_tr_b16 v[148:149], %1 offset:5120\n\t"
    "ds_read_b64_tr_b16 v[150:151], %1 offset:5248\n\t"
    "ds_read_b64_tr_b16 v[152:153], %1 offset:4128\n\t"
    "ds_read_b64_tr_b16 v[154:155], %1 offset:4256\n\t"
    "ds_read_b64_tr_b16 v[156:157], %1 offset:5152\n\t"
    "ds_read_b64_tr_b16 v[158:159], %1 offset:5280\n\t"
    "s_waitcnt lgkmcnt(0)"
    : "=&{v[128:159]}"(w) : "v"(a) : "memory");
}
template <> __device__ __forceinline__ void vr_wload<3>(u32x32& w, unsigned a) {
  asm volatile("ds_read_b64_tr_b16 v[160:161], %1 offset:8192\n\t"
    "ds_read_b64_tr_b16 v[162:163], %1 offset:8320\n\t"
    "ds_read_b64_tr_b16 v[164:165], %1 offset:9216\n\t"
    "ds_read_b64_tr_b16 v[166:167], %1 offset:9344\n\t"
    "ds_read_b64_tr_b16 v[168:169], %1 offset:8224\n\t"
    "ds_read_b64_tr_b16 v[170:171], %1 offset:8352\n\t"
    "ds_read_b64_tr_b16 v[172:173], %1 offset:9248\n\t"
    "ds_read_b64_tr_b16 v[174:175], %1 offset:9376\n\t"
    "s_waitcnt lgkmcnt(0)\n\t"
    "ds_read_b64_tr_b16 v[176:177], %1 offset:12288\n\t"
    "ds_read_b64_tr_b16 v[178:179], %1 offset:12416\n\t"
    "ds_read_b64_tr_b16 v[180:181], %1 offset:13312\n\t"
    "ds_read_b64_tr_b16 v[182:183], %1 offset:13440\n\t"
    "ds_read_b64_tr_b16 v[184:185], %1 offset:12320\n\t"
    "ds_read_b64_tr_b16 v[186:187], %1 offset:12448\n\t"
    "ds_read_b64_tr_b16 v[188:189], %1 offset:13344\n\t"
    "ds_read_b64_tr_b16 v[190:191], %1 offset:13472\n\t"
    "s_waitcnt lgkmcnt(0)"
    : "=&{v[160:191]}"(w) : "v"(a) : "memory");
}
template <> __device__ __forceinline__ void vr_wload<4>(u32x32& w, unsigned a) {
  asm volatile("ds_read_b64_tr_b16 v[192:193], %1 offset:0\n\t"
    "ds_read_b64_tr_b16 v[194:195], %1 offset:128\n\t"
    "ds_read_b64_tr_b16 v[196:197], %1 offset:1024\n\t"
    "ds_read_b64_tr_b16 v[198:199], %1 offset:1152\n\t"
    "ds_read_b64_tr_b16 v[200:201], %1 offset:32\n\t"
    "ds_read_b64_tr_b16 v[202:203], %1 offset:160\n\t"
    "ds_read_b64_tr_b16 v[204:205], %1 offset:1056\n\t"
    "ds_read_b64_tr_b16 v[206:207], %1 offset:1184\n\t"
    "s_waitcnt lgkmcnt(0)\n\t"
    "ds_read_b64_tr_b16 v[208:209], %1 offset:4096\n\t"
    "ds_read_b64_tr_b16 v[210:211], %1 offset:4224\n\t"
    "ds_read_b64_tr_b16 v[212:213], %1 offset:5120\n\t"
    "ds_read_b64_tr_b16 v[214:215], %1 offset:5248\n\t"
    "ds_read_b64_tr_b16 v[216:217], %1 offset:4128\n\t"
    "ds_read_b64_tr_b16 v[218:219], %1 offset:4256\n\t"
    "ds_read_b64_tr_b16 v[220:221], %1 offset:5152\n\t"
    "ds_read_b64_tr_b16 v[222:223], %1 offset:5280\n\t"
    "s_waitcnt lgkmcnt(0)"
    : "=&{v[192:223]}"(w) : "v"(a) : "memory");
}
template <> __device__ __forceinline__ void vr_wload<5>(u32x32& w, unsigned a) {
  asm volatile("ds_read_b64_tr_b16 v[224:225], %1 offset:8192\n\t"
    "ds_read_b64_tr_b16 v[226:227], %1 offset:8320\n\t"
    "ds_read_b64_tr_b16 v[228:229], %1 offset:9216\n\t"
    "ds_read_b64_tr_b16 v[230:231], %1 offset:9344\n\t"
    "ds_read_b64_tr_b16 v[232:233], %1 offset:8224\n\t"
    "ds_read_b64_tr_b16 v[234:235], %1 offset:8352\n\t"
    "ds_read_b64_tr_b16 v[236:237], %1 offset:9248\n\t"
    "ds_read_b64_tr_b16 v[238:239], %1 offset:9376\n\t"
    "s_waitcnt lgkmcnt(0)\n\t"
    "ds_read_b64_tr_b16 v[240:241], %1 offset:12288\n\t"
    "ds_read_b64_tr_b16 v[242:243], %1 offset:12416\n\t"
    "ds_read_b64_tr_b16 v[244:245], %1 offset:13312\n\t"
    "ds_read_b64_tr_b16 v[246:247], %1 offset:13440\n\t"
    "ds_read_b64_tr_b16 v[248:249], %1 offset:12320\n\t"
    "ds_read_b64_tr_b16 v[250:251], %1 offset:12448\n\t"
    "ds_read_b64_tr_b16 v[252:253], %1 offset:13344\n\t"
    "ds_read_b64_tr_b16 v[254:255], %1 offset:13472\n\t"
    "s_waitcnt lgkmcnt(0)"
    : "=&{v[224:255]}"(w) : "v"(a) : "memory");
}
#define OREP 2
#define AREP 2
struct VRW { u32x32 v0, v1, o0, o1, o2, o3; };
__device__ __forceinline__ void vr_v(const VRW& W, const char* ag, const char* vd, unsigned wa, unsigned ra, unsigned& cap, unsigned& sp, unsigned long long& tc, unsigned long long* ts) {
  asm volatile("v_mbcnt_lo_u32_b32 v16, -1, 0\n\t"
    "v_mbcnt_hi_u32_b32 v16, -1, v16\n\t"
    "v_lshlrev_b32 v17, 4, v16\n\t"
    "s_mov_b32 %[sp], 0\n\t"
    "global_load_dwordx4 v[24:27], v17, %[ag] sc1\n\t"
    "global_load_dwordx4 v[28:31], v17, %[ag] offset:1024 sc1\n\t"
    "s_memtime %[tc]\n\t"
    "v_and_b32 v22, 4, v16\n\t"
    "v_mul_u32_u24 v22, 0x2aaa, v22\n\t"
    "v_lshrrev_b32 v18, 1, v16\n\t"
    "v_and_b32 v18, 2, v18\n\t"
    "v_or3_b32 v18, v18, v22, %[c44]\n\t"
    "v_lshl_add_u32 v19, v16, 3, %[wa]\n\t"
    "v_lshl_add_u32 v20, v16, 3, %[ra]\n\t"
    "v_and_b32 v22, 15, v16\n\t"
    "v_lshlrev_b32 v21, 4, v22\n\t"
    "v_lshrrev_b32 v22, 2, v16\n\t"
    "v_and_or_b32 v21, v22, 12, v21\n\t"
    "3:\n\t"
    "v_mov_b32 v32, 0\n\t"
    "v_mov_b32 v33, 0\n\t"
    "v_mov_b32 v34, 0\n\t"
    "v_mov_b32 v35, 0\n\t"
    "v_mov_b32 v36, 0\n\t"
    "v_mov_b32 v37, 0\n\t"
    "v_mov_b32 v38, 0\n\t"
    "v_mov_b32 v39, 0\n\t"
    "v_mov_b32 v40, 0\n\t"
    "v_mov_b32 v41, 0\n\t"
    "v_mov_b32 v42, 0\n\t"
    "v_mov_b32 v43, 0\n\t"
    "v_mov_b32 v44, 0\n\t"
    "v_mov_b32 v45, 0\n\t"
    "v_mov_b32 v46, 0\n\t"
    "v_mov_b32 v47, 0\n\t"
    "s_waitcnt vmcnt(0) lgkmcnt(0)\n\t"
    "v_smfmac_f32_16x16x64_bf16 v[32:35], v[24:27], v[64:71], v18\n\t"
    "v_smfmac_f32_16x16x64_bf16 v[32:35], v[28:31], v[96:103], v18\n\t"
    "v_max3_u32 v22, v24, v25, v26\n\t"
    "v_max3_u32 v23, v27, v28, v29\n\t"
    "v_smfmac_f32_16x16x64_bf16 v[36:39], v[24:27], v[72:79], v18\n\t"
    "v_max3_u32 v22, v22, v30, v31\n\t"
    "v_max_u32 v22, v22, v23\n\t"
    "v_smfmac_f32_16x16x64_bf16 v[36:39], v[28:31], v[104:111], v18\n\t"
    "v_cmp_eq_u32 vcc, -1, v22\n\t"
    "s_cbranch_vccnz 1f\n\t"
    "2:\n\t"
    "v_smfmac_f32_16x16x64_bf16 v[40:43], v[24:27], v[80:87], v18\n\t"
    "v_permlane16_swap_b32 v32, v34\n\t"
    "v_permlane16_swap_b32 v33, v35\n\t"
    "v_smfmac_f32_16x16x64_bf16 v[40:43], v[28:31], v[112:119], v18\n\t"
    "v_pk_add_f32 v[32:33], v[32:33], v[34:35]\n\t"
    "ds_write_b64 v19, v[32:33] offset:0\n\t"
    "v_smfmac_f32_16x16x64_bf16 v[44:47], v[24:27], v[88:95], v18\n\t"
    "s_nop 0\n\t"
    "v_permlane16_swap_b32 v36, v38\n\t"
    "v_permlane16_swap_b32 v37, v39\n\t"
    "v_smfmac_f32_16x16x64_bf16 v[44:47], v[28:31], v[120:127], v18\n\t"
    "v_pk_add_f32 v[36:37], v[36:37], v[38:39]\n\t"
    "ds_write_b64 v19, v[36:37] offset:512\n\t"
    "v_permlane16_swap_b32 v40, v42\n\t"
    "v_permlane16_swap_b32 v41, v43\n\t"
    "v_pk_add_f32 v[40:41], v[40:41], v[42:43]\n\t"
    "ds_write_b64 v19, v[40:41] offset:1024\n\t"
    "s_nop 1\n\t"
    "v_permlane16_swap_b32 v44, v46\n\t"
    "v_permlane16_swap_b32 v45, v47\n\t"
    "v_pk_add_f32 v[44:45], v[44:45], v[46:47]\n\t"
    "ds_write_b64 v19, v[44:45] offset:1536\n\t"
    "s_cmp_ge_u32 %[wa], 8192\n\t"
    "s_waitcnt lgkmcnt(0)\n\t"
    "s_barrier\n\t"
    "s_cbranch_scc1 9f\n\t"
    "ds_read_b64 v[48:49], v20 offset:0\n\t"
    "ds_read_b64 v[50:51], v20 offset:2048\n\t"
    "ds_read_b64 v[52:53], v20 offset:4096\n\t"
    "ds_read_b64 v[54:55], v20 offset:6144\n\t"
    "ds_read_b64 v[56:57], v20 offset:8192\n\t"
    "ds_read_b64 v[58:59], v20 offset:10240\n\t"
    "ds_read_b64 v[60:61], v20 offset:12288\n\t"
    "ds_read_b64 v[62:63], v20 offset:14336\n\t"
    "s_waitcnt lgkmcnt(6)\n\t"
    "v_pk_add_f32 v[48:49], v[48:49], v[50:51]\n\t"
    "s_waitcnt lgkmcnt(4)\n\t"
    "v_pk_add_f32 v[52:53], v[52:53], v[54:55]\n\t"
    "v_pk_add_f32 v[48:49], v[48:49], v[52:53]\n\t"
    "s_waitcnt lgkmcnt(2)\n\t"
    "v_pk_add_f32 v[56:57], v[56:57], v[58:59]\n\t"
    "s_waitcnt lgkmcnt(0)\n\t"
    "v_pk_add_f32 v[60:61], v[60:61], v[62:63]\n\t"
    "v_pk_add_f32 v[56:57], v[56:57], v[60:61]\n\t"
    "v_pk_add_f32 v[48:49], v[48:49], v[56:57]\n\t"
    "v_cvt_pk_bf16_f32 v22, v48, v49\n\t"
    "v_min_u32 v22, -2, v22\n\t"
    "global_store_dword v21, v22, %[vd] sc1\n\t"
    "s_waitcnt lgkmcnt(0)\n\t"
    "s_branch 9f\n\t"
    "1:\n\t"
    "s_add_u32 %[sp], %[sp], 1\n\t"
    "s_cmp_ge_u32 %[sp], %[cap]\n\t"
    "s_cbranch_scc1 7f\n\t"
    "global_load_dwordx4 v[24:27], v17, %[ag] sc1\n\t"
    "global_load_dwordx4 v[28:31], v17, %[ag] offset:1024 sc1\n\t"
    "s_waitcnt lgkmcnt(0)\n\t"
    "s_memtime %[tc]\n\t"
    "s_nop 7\n\t"
    "s_branch 3b\n\t"
    "7:\n\t"
    "s_mov_b32 %[cap], 0\n\t"
    "s_branch 2b\n\t"
    "9:"
    : [tc] "=&s"(tc), [sp] "=&s"(sp), [cap] "+s"(cap)
    : [ag] "s"(ag), [vd] "s"(vd), [wa] "s"(wa), [ra] "s"(ra), [c44] "s"(0x4444u), "{v[64:95]}"(W.v0), "{v[96:127]}"(W.v1), "{v[128:159]}"(W.o0), "{v[160:191]}"(W.o1), "{v[192:223]}"(W.o2), "{v[224:255]}"(W.o3)
    : "v16", "v17", "v18", "v19", "v20", "v21", "v22", "v23", "v24", "v25", "v26", "v27", "v28", "v29", "v30", "v31", "v32", "v33", "v34", "v35", "v36", "v37", "v38", "v39", "v40", "v41", "v42", "v43", "v44", "v45", "v46", "v47", "v48", "v49", "v50", "v51", "v52", "v53", "v54", "v55", "v56", "v57", "v58", "v59", "v60", "v61", "v62", "v63", "vcc", "scc", "memory");
}
__device__ __forceinline__ void vr_r(const VRW& W, const char* os, const char* ad, unsigned wa, unsigned& cap, unsigned& sp, unsigned long long* ts) {
  asm volatile("v_mbcnt_lo_u32_b32 v16, -1, 0\n\t"
    "v_mbcnt_hi_u32_b32 v16, -1, v16\n\t"
    "v_lshlrev_b32 v17, 4, v16\n\t"
    "v_add_u32 v18, 0x1000, v17\n\t"
    "global_load_dwordx4 v[24:27], v17, %[os] offset:0 sc1\n\t"
    "global_load_dwordx4 v[28:31], v17, %[os] offset:2048 sc1\n\t"
    "global_load_dwordx4 v[32:35], v18, %[os] offset:0 sc1\n\t"
    "global_load_dwordx4 v[36:39], v18, %[os] offset:2048 sc1\n\t"
    "s_mov_b32 %[sp], 0\n\t"
    "v_and_b32 v23, 4, v16\n\t"
    "v_mul_u32_u24 v23, 0x2aaa, v23\n\t"
    "v_lshrrev_b32 v19, 1, v16\n\t"
    "v_and_b32 v19, 2, v19\n\t"
    "v_or3_b32 v19, v19, v23, %[c44]\n\t"
    "v_and_b32 v23, 15, v16\n\t"
    "v_lshl_add_u32 v20, v23, 2, %[wa]\n\t"
    "v_and_b32 v23, 48, v16\n\t"
    "v_lshl_add_u32 v20, v23, 3, v20\n\t"
    "v_and_b32 v23, 32, v16\n\t"
    "v_and_b32 v21, 28, v16\n\t"
    "v_lshlrev_b32 v21, 4, v21\n\t"
    "v_or_b32 v21, v21, v23\n\t"
    "v_lshlrev_b32 v22, 3, v23\n\t"
    "v_and_b32 v23, 3, v16\n\t"
    "v_lshl_or_b32 v21, v23, 3, v21\n\t"
    "v_and_b32 v23, 31, v16\n\t"
    "v_lshl_or_b32 v22, v23, 2, v22\n\t"
    "3:\n\t"
    "v_mov_b32 v40, 0\n\t"
    "v_mov_b32 v41, 0\n\t"
    "v_mov_b32 v42, 0\n\t"
    "v_mov_b32 v43, 0\n\t"
    "v_mov_b32 v44, 0\n\t"
    "v_mov_b32 v45, 0\n\t"
    "v_mov_b32 v46, 0\n\t"
    "v_mov_b32 v47, 0\n\t"
    "v_mov_b32 v48, 0\n\t"
    "v_mov_b32 v49, 0\n\t"
    "v_mov_b32 v50, 0\n\t"
    "v_mov_b32 v51, 0\n\t"
    "v_mov_b32 v52, 0\n\t"
    "v_mov_b32 v53, 0\n\t"
    "v_mov_b32 v54, 0\n\t"
    "v_mov_b32 v55, 0\n\t"
    "v_mov_b32 v56, 0\n\t"
    "s_waitcnt vmcnt(3)\n\t"
    "v_smfmac_f32_16x16x64_bf16 v[44:47], v[24:27], v[144:151], v19\n\t"
    "v_max3_u32 v56, v56, v24, v25\n\t"
    "v_smfmac_f32_16x16x64_bf16 v[48:51], v[24:27], v[136:143], v19\n\t"
    "v_max3_u32 v56, v56, v26, v27\n\t"
    "v_smfmac_f32_16x16x64_bf16 v[40:43], v[24:27], v[128:135], v19\n\t"
    "v_smfmac_f32_16x16x64_bf16 v[52:55], v[24:27], v[152:159], v19\n\t"
    "s_waitcnt vmcnt(2)\n\t"
    "v_smfmac_f32_16x16x64_bf16 v[44:47], v[28:31], v[176:183], v19\n\t"
    "v_max3_u32 v56, v56, v28, v29\n\t"
    "v_smfmac_f32_16x16x64_bf16 v[48:51], v[28:31], v[168:175], v19\n\t"
    "v_max3_u32 v56, v56, v30, v31\n\t"
    "v_smfmac_f32_16x16x64_bf16 v[40:43], v[28:31], v[160:167], v19\n\t"
    "v_smfmac_f32_16x16x64_bf16 v[52:55], v[28:31], v[184:191], v19\n\t"
    "s_waitcnt vmcnt(1)\n\t"
    "v_smfmac_f32_16x16x64_bf16 v[44:47], v[32:35], v[208:215], v19\n\t"
    "v_max3_u32 v56, v56, v32, v33\n\t"
    "v_smfmac_f32_16x16x64_bf16 v[48:51], v[32:35], v[200:207], v19\n\t"
    "v_max3_u32 v56, v56, v34, v35\n\t"
    "v_smfmac_f32_16x16x64_bf16 v[40:43], v[32:35], v[192:199], v19\n\t"
    "v_smfmac_f32_16x16x64_bf16 v[52:55], v[32:35], v[216:223], v19\n\t"
    "s_waitcnt vmcnt(0)\n\t"
    "v_smfmac_f32_16x16x64_bf16 v[44:47], v[36:39], v[240:247], v19\n\t"
    "v_max3_u32 v56, v56, v36, v37\n\t"
    "v_smfmac_f32_16x16x64_bf16 v[48:51], v[36:39], v[232:239], v19\n\t"
    "v_max3_u32 v56, v56, v38, v39\n\t"
    "v_smfmac_f32_16x16x64_bf16 v[40:43], v[36:39], v[224:231], v19\n\t"
    "v_cmp_eq_u32 vcc, -1, v56\n\t"
    "v_smfmac_f32_16x16x64_bf16 v[52:55], v[36:39], v[248:255], v19\n\t"
    "s_cbranch_vccnz 4f\n\t"
    "5:\n\t"
    "s_nop 2\n\t"
    "v_permlane32_swap_b32 v44, v48\n\t"
    "v_permlane32_swap_b32 v45, v49\n\t"
    "v_permlane32_swap_b32 v46, v50\n\t"
    "v_permlane32_swap_b32 v47, v51\n\t"
    "s_mov_b32 exec_hi, 0\n\t"
    "v_pk_add_f32 v[40:41], v[40:41], v[48:49]\n\t"
    "v_pk_add_f32 v[42:43], v[42:43], v[50:51]\n\t"
    "s_mov_b64 exec, %[hi]\n\t"
    "v_pk_add_f32 v[40:41], v[44:45], v[52:53]\n\t"
    "v_pk_add_f32 v[42:43], v[46:47], v[54:55]\n\t"
    "s_mov_b32 exec_lo, -1\n\t"
    "s_nop 0\n\t"
    "v_permlane16_swap_b32 v40, v42\n\t"
    "v_permlane16_swap_b32 v41, v43\n\t"
    "v_add_f32 v40, v40, v42\n\t"
    "v_add_f32 v41, v41, v43\n\t"
    "ds_write_b32 v20, v40 offset:16384\n\t"
    "ds_write_b32 v20, v41 offset:16448\n\t"
    "s_cmp_ge_u32 %[wa], 1024\n\t"
    "s_waitcnt lgkmcnt(0)\n\t"
    "s_barrier\n\t"
    "s_cbranch_scc1 9f\n\t"
    "ds_read_b64 v[24:25], v21 offset:16384\n\t"
    "ds_read_b64 v[26:27], v21 offset:16896\n\t"
    "ds_read_b64 v[28:29], v21 offset:17408\n\t"
    "ds_read_b64 v[30:31], v21 offset:17920\n\t"
    "ds_read_b64 v[32:33], v21 offset:18432\n\t"
    "ds_read_b64 v[34:35], v21 offset:18944\n\t"
    "ds_read_b64 v[36:37], v21 offset:19456\n\t"
    "ds_read_b64 v[38:39], v21 offset:19968\n\t"
    "s_waitcnt lgkmcnt(6)\n\t"
    "v_pk_add_f32 v[24:25], v[24:25], v[26:27]\n\t"
    "s_waitcnt lgkmcnt(4)\n\t"
    "v_pk_add_f32 v[28:29], v[28:29], v[30:31]\n\t"
    "v_pk_add_f32 v[24:25], v[24:25], v[28:29]\n\t"
    "s_waitcnt lgkmcnt(2)\n\t"
    "v_pk_add_f32 v[32:33], v[32:33], v[34:35]\n\t"
    "s_waitcnt lgkmcnt(0)\n\t"
    "v_pk_add_f32 v[36:37], v[36:37], v[38:39]\n\t"
    "v_pk_add_f32 v[32:33], v[32:33], v[36:37]\n\t"
    "v_pk_add_f32 v[24:25], v[24:25], v[32:33]\n\t"
    "v_cvt_pk_bf16_f32 v23, v24, v25\n\t"
    "v_min_u32 v23, -2, v23\n\t"
    "global_store_dword v22, v23, %[ad] sc1\n\t"
    "s_waitcnt lgkmcnt(0)\n\t"
    "s_branch 9f\n\t"
    "4:\n\t"
    "s_add_u32 %[sp], %[sp], 1\n\t"
    "s_cmp_ge_u32 %[sp], %[cap]\n\t"
    "s_cbranch_scc1 7f\n\t"
    "global_load_dwordx4 v[24:27], v17, %[os] offset:0 sc1\n\t"
    "global_load_dwordx4 v[28:31], v17, %[os] offset:2048 sc1\n\t"
    "global_load_dwordx4 v[32:35], v18, %[os] offset:0 sc1\n\t"
    "global_load_dwordx4 v[36:39], v18, %[os] offset:2048 sc1\n\t"
    "s_nop 7\n\t"
    "s_branch 3b\n\t"
    "7:\n\t"
    "s_mov_b32 %[cap], 0\n\t"
    "s_branch 5b\n\t"
    "9:"
    : [sp] "=&s"(sp), [cap] "+s"(cap)
    : [os] "s"(os), [ad] "s"(ad), [wa] "s"(wa), [c44] "s"(0x4444u), [hi] "s"(0xFFFFFFFF00000000ull), "{v[64:95]}"(W.v0), "{v[96:127]}"(W.v1), "{v[128:159]}"(W.o0), "{v[160:191]}"(W.o1), "{v[192:223]}"(W.o2), "{v[224:255]}"(W.o3)
    : "v16", "v17", "v18", "v19", "v20", "v21", "v22", "v23", "v24", "v25", "v26", "v27", "v28", "v29", "v30", "v31", "v32", "v33", "v34", "v35", "v36", "v37", "v38", "v39", "v40", "v41", "v42", "v43", "v44", "v45", "v46", "v47", "v48", "v49", "v50", "v51", "v52", "v53", "v54", "v55", "v56", "v57", "v58", "v59", "v60", "v61", "v62", "v63", "vcc", "scc", "memory");
}
__device__ __forceinline__ void qk_body(char* smem, int wave_id_, int xg, int h, int wg, const u16* __restrict__ Wq, const u16* __restrict__ Wk,
                                        const char* __restrict__ Kx, const char* __restrict__ V, char* ws, unsigned* tmo, unsigned* cnt,
                                        const char* xb, const char* ob) {
  const int wave = WAVE_ID;
  const int ty = wave >> 2, w4 = wave & 3;
  const char* oh = ob + (size_t)(h >> 2) * 16384 + (h & 3) * 2048;
  const char* wq = uni((const char*)(Wq + h * DH)); const char* wk = uni((const char*)(Wk + h * DH));
  auto stage2 = [&](int i) {
    const int tid = wave * 64 + lane_id();
    stage_proj(wq + (size_t)i * 128 * HD * 2, Q_RING + ((2 * i) & 3) * 32768, tid, wave);
    stage_proj(wk + (size_t)((i + 2) & 7) * 128 * HD * 2, Q_RING + ((2 * i + 1) & 3) * 32768, tid, wave);
  };
  auto rearm = [&](int ps) {
    unsigned z = SENT; asm volatile("" : "+v"(z));
    const unsigned l16 = lane_id() * 16;
#pragma unroll
    for (int r = 0; r < 2 * OREP; ++r) st16s(oh + (size_t)(ps * 4 + (r >> 1)) * 131072, (r & 1) * 1024u + l16, u32x4{z, z, z, z});
  };
  const char* dbw = xb + (size_t)48 * 16384 + 4096 + h * 128;
  QKW W;
  stage2(0); stage2(1);
#define WLD(i, w) do { WAIT_V(8); \
    WBAR(); { const int l_ = lane_id(); const unsigned o_ = Q_RING + ty * 32768 + ((i) & 1) * 65536; qk_wload<i>(w, proj_base(2 * w4, l_) + o_, proj_base(2 * w4 + 1, l_) + o_); } \
    WBAR(); stage2((i) + 2); if (wave == 0) STS(ws, wg, 1 + (i)); if ((i) - 6 == RDYI && opaque_tid() == 64) st4_sc1(cnt + (PQKV + h) * 32 + 16, 1u); } while (0)
  WLD(0, W.w0); WLD(1, W.w1); WLD(2, W.w2); WLD(3, W.w3); WLD(4, W.w4); WLD(5, W.w5);
#undef WLD
#pragma unroll
  for (int i = 0; i < 2; ++i) {
    if (i == 0) WAIT_V(8); else WAIT_V(0);
    WBAR();
    if (i == RDYI && opaque_tid() == 64) st4_sc1(cnt + (PQKV + h) * 32 + 16, 1u);
    const int lane = lane_id();
    const unsigned o = Q_RING + ty * 32768 + i * 65536, f0 = proj_base(2 * w4, lane) + o, f1 = proj_base(2 * w4 + 1, lane) + o;
    u32x8 x[4] = {proj_frag(smem, f0), proj_frag(smem, f1), proj_frag(smem, f0 + 16384), proj_frag(smem, f1 + 16384)};
    asm volatile("" : "+v"(x[0]), "+v"(x[1]), "+v"(x[2]), "+v"(x[3]));
    BAR();
    char* d = smem + Q_CB + i * 57344 + ty * (i ? 32768 : 24576) + w4 * 2048 + lane * 16;
#pragma unroll
    for (int j = 0; j < 4; ++j)
      if (i || j || !ty) { *(u32x4*)(d + j * 8192) = u32x4{x[j][0], x[j][1], x[j][2], x[j][3]}; *(u32x4*)(d + j * 8192 + 1024) = u32x4{x[j][4], x[j][5], x[j][6], x[j][7]}; }
    if (i == 0 && ty) { *(u32x4*)(smem + Q_MEM + wave * 2048 + lane * 16) = u32x4{x[0][0], x[0][1], x[0][2], x[0][3]}; *(u32x4*)(smem + Q_MEM + wave * 2048 + 1024 + lane * 16) = u32x4{x[0][4], x[0][5], x[0][6], x[0][7]}; }
  }
  if (wave == 4) *(volatile LAS u32x2*)(smem + Q_TM) = u32x2{0u, 0u};
  BAR();
  if (wave == 0) STS(ws, wg, 0);
  unsigned avail = 0;
  auto stage_kv = [&](int s) {
    if (avail <= (unsigned)s) {
      for (int spins = 0;; ++spins) {
        const unsigned p = ld4_sc1(cnt + (PQKV + h) * 32 + (lane_id() & 15));
        avail = min(qkv_avail(p, 1), qkv_avail(p, 2));
        if (avail > (unsigned)s) break;
        __builtin_amdgcn_s_sleep(8);
        if (give_up(spins, tmo, 5)) break;
      }
    }
    const char* ks = uni(Kx + ((size_t)s * NH + h) * 8192); const char* vs = uni(V + ((size_t)s * NH + h) * 8192);
    const int lane = lane_id();
    head_to_lds(ks, lane, Q_K); head_to_lds(vs, lane, Q_V);
  };
  auto loop = [&](auto w_c) __attribute__((always_inline)) {
    constexpr int WV = decltype(w_c)::value;
    if (WV >= 4) asm volatile("ds_read_b128 v[56:59], %1\n\tds_read_b128 v[60:63], %1 offset:1024\n\ts_waitcnt lgkmcnt(0)" : "=&{v[56:63]}"(W.x) : "v"(Q_MEM + WV * 2048 + lane_id() * 16) : "memory");
    if (WV == 6) stage_kv(0);
    if (WV < 4) __builtin_amdgcn_s_setprio(2);
    unsigned t0 = 0, cap = 60000, d0 = 105, d1 = 200;
    int dq = D_Q;
    int sl = 0;
    for (int s = 0; s < NSEG; ++s) {
      TSDECL;
      const char* ag = xb + (size_t)((sl * 4 + (AREP == 4 ? xg : xg >> 1)) * 2 + (WV >> 2)) * 16384 + (WV & 3) * 2048;
      const char* vs = WV == 4 ? dbw : WV == 5 ? ws + OFF_KVM + ((size_t)s * NH + h) * 4096 : xb + (size_t)(24 + sl * 8 + (h >> 2)) * 16384 + (h & 3) * 2048;
      const char* od = WV == 4 ? (const char*)(size_t)((unsigned)(s + 1) << 24) : oh + (size_t)sl * 524288;
      if (s > 0) wait_rt(t0, dq);
      TS(0);
      unsigned long long tc; unsigned sp;
      qk_step<WV>(W, ag, vs, od, ws + OFF_KVM + ((size_t)s * NH + h) * 4096 + 2048, cap, sp, tc, nullptr, d0, d1);
      dq = (sp & 0xFFFFu) ? min(dq + DQU, D_Q + DQM) : max(dq - DQD, D_Q);
      if (WV < 4 || WV == 5) { WAIT_V(0); if (lane_id() == 0) st4_sc1(cnt + (PROG + h) * 32 + WV, (unsigned)(s + 1)); }
      if (WV == 6 && s + 1 < NSEG - 1) stage_kv(s + 1);
      if (WV == 7 && s > 0 && s < NSEG - 1) rearm(sl == 0 ? 2 : sl - 1);
      if (s < NSEG - 1) {
        if (WV == 4) {
          delay64<E_B>();
          t0 = bell_time(xb + (size_t)(48 + xg) * 16384 + 4096, (lane_id() & 31) * 128, (unsigned)(s + 1), cap);
          *(volatile LAS u32x2*)(smem + Q_TM) = u32x2{(unsigned)(s + 1), t0};
        } else {
          for (;;) {
            const u32x2 v = *(volatile LAS u32x2*)(smem + Q_TM);
            t0 = __builtin_amdgcn_readfirstlane(v.y);
            if (__builtin_amdgcn_readfirstlane(v.x) == (unsigned)(s + 1)) break;
            __builtin_amdgcn_s_sleep(1);
          }
        }
      }
      sl = sl == 2 ? 0 : sl + 1;
    }
    if (cap == 0 && lane_id() == 0) st4_sc1(tmo, 1u);
  };
  switch (wave) {
    case 0: loop(std::integral_constant<int, 0>{}); break;
    case 1: loop(std::integral_constant<int, 1>{}); break;
    case 2: loop(std::integral_constant<int, 2>{}); break;
    case 3: loop(std::integral_constant<int, 3>{}); break;
    case 4: loop(std::integral_constant<int, 4>{}); break;
    case 5: loop(std::integral_constant<int, 5>{}); break;
    case 6: loop(std::integral_constant<int, 6>{}); break;
    default: loop(std::integral_constant<int, 7>{}); break;
  }
  __builtin_amdgcn_s_setprio(0);
}

__device__ __forceinline__ void vr_body(char* smem, int wave_id_, int xg, int h, int c, int r, int wg, const u16* __restrict__ Wv, const u16* __restrict__ Wo,
                                        char* ws, unsigned* tmo, unsigned* cnt, const char* xb, const char* ob) {
  const int wave = WAVE_ID;
  const unsigned po = (unsigned)(r >> 5) * 16384u + ((r >> 2) & 7) * 1024 + (r & 1) * 512 + ((r >> 1) & 1) * 128;
  const unsigned vo_ = (unsigned)(24 + (h >> 2)) * 16384u + (h & 3) * 2048 + c * 1024;
  auto rearm = [&](int ps, bool ag) {
    const int lane = lane_id();
    unsigned z = SENT; asm volatile("" : "+v"(z));
    const u32x4 sv = u32x4{z, z, z, z};
    if (ag && lane < 16 * AREP) st16s(xb, (unsigned)(ps * 8 + (lane >> 4) * 2) * 16384u + po + ((lane >> 3) & 1) * 256 + (lane & 7) * 16, sv);
    st16s(xb, (unsigned)ps * 131072u + vo_ + lane * 16, sv);
  };
  const int hg = (wave + ROTW * (r & 3)) & 7;
  VRW W;
  {
    const int lane = lane_id();
    const int g = lane >> 4, q4 = (lane & 15) >> 2, p4 = lane & 3, fr = 4 * g + (q4 & 1) + 32 * (q4 >> 1);
    const unsigned lw = wave * 16384;
    const char* sv_ = uni((const char*)(Wv + (size_t)(128 * wave) * HD + h * DH + 64 * c));
    const char* so = uni((const char*)(Wo + (size_t)(512 * hg) * DM + 64 * (r >> 2) + 16 * (r & 1)));
    const unsigned vo = (unsigned)((32 * (lane >> 5) + 16 * ((lane >> 4) & 1) + ((lane >> 2) & 3)) * DM * 2 + ((lane >> 1) & 1) * 64 + (lane & 1) * 16);
    auto issue = [&](int p) {
#pragma unroll
      for (int i = 0; i < 8; ++i) {
        if (p >= 2) glds16s<0>(so + (size_t)(128 * (p - 2) + 64 * (i & 1) + 8 * (i >> 2) + 4 * ((i >> 1) & 1)) * DM * 2, vo, lw + (p & 1) * 8192 + i * 1024);
        else gldsw(sv_ + (size_t)(8 * p + i) * 8 * HD * 2, (unsigned)((lane >> 3) * HD * 2 + (lane & 7) * 16), lw + (p & 1) * 8192 + i * 1024);
      }
    };
#define VTS(p) do { } while (0)
    issue(0); issue(1);
    const unsigned av = lw + (unsigned)(fr * 128 + p4 * 8), ao = lw + (unsigned)(fr * 64 + p4 * 8);
    WAIT_V(8); VTS(0); vr_wload<0>(W.v0, av); issue(2);
    WAIT_V(8); VTS(1); vr_wload<1>(W.v1, av); issue(3);
    WAIT_V(8); VTS(2); vr_wload<2>(W.o0, ao); issue(4);
    WAIT_V(8); vr_wload<3>(W.o1, ao); issue(5);
    WAIT_V(8); vr_wload<4>(W.o2, ao);
    WAIT_V(0); VTS(3); vr_wload<5>(W.o3, ao);
#undef VTS
  }
  if (wave == 0) STS(ws, wg, 5);
  if (wave == 0) STS(ws, wg, 6);
  BAR();
  if (wave == 0) STS(ws, wg, 0);
  __builtin_amdgcn_s_setprio(2);
  unsigned cap = 60000;
  int sl = 0;
  for (int s = 0; s < NSEG; ++s) {
    TSDECL;
    const int sn = sl == 2 ? 0 : sl + 1;
    const char* ag = xb + (size_t)((sl * 4 + (AREP == 4 ? xg : xg >> 1)) * 2 + (wave >> 2)) * 16384 + (wave & 3) * 2048;
    const char* vd = xb + (size_t)sl * 131072 + vo_ + (wave & 3) * 256;
    const char* os = ob + (size_t)((sl * 4 + (OREP == 4 ? xg : ORSEL)) * 8 + hg) * 16384 + ((r >> 1) & 1) * 1024;
    const char* ad = xb + (size_t)((sn * 4 + (wave & 3)) * 2) * 16384 + po;
    if (s > 0) delay64<E_AG>();
    TS(0);
    unsigned long long tc; unsigned sp;
    unsigned long long* tsp = nullptr;
    vr_v(W, ag, vd, wave * 2048, (wave & 3) * 512, cap, sp, tc, tsp);
    TN(12, sp + 1);
    if (s == NSEG - 1) break;
    if (wave == 7 && s > 0) rearm(sl == 0 ? 2 : sl - 1, true);
    wait_clk((unsigned)tc, E_O);
    TS(3);
    const unsigned tb = bell_time(xb + (size_t)(48 + xg) * 16384 + hg * 512, (lane_id() & 3) * 128, (unsigned)(s + 1), cap);
    wait_rt(tb, D_O);
    TS(4);
    vr_r(W, os, ad, wave * 512, cap, sp, tsp);
    TN(14, (sp & 0xFFFF) + 1);
    if (wave == 1) TSFLUSH(0x6078);
    sl = sn;
  }
  __builtin_amdgcn_s_setprio(0);
  if (cap == 0 && lane_id() == 0) st4_sc1(tmo, 2u);
}
#define X3
#define NOPF
#define PF0 4
#define NIMM 3
constexpr unsigned TK(int m, int hl, int part, int bo, int be) { return (unsigned)(m | hl << 2 | part << 5 | bo << 8 | be << 16); }
#define PLAN 1
#define QX 30
#define C1 43
#define C2 46
constexpr unsigned task_tab(int t, int it) {
  return t < 8 ? (it == 0 ? TK(0, t, 1, QX, 64) : 0)
       : t < 13 ? (it == 0 ? TK(1, t - 8, 0, 0, 64) : it == 1 ? TK(2, t - 8, 1, C1, 64) : 0)
       : t < 16 ? (it == 0 ? TK(1, t - 8, 0, 0, C2) : 0)
       : t < 24 ? (it == 0 ? TK(2, t - 16, 0, 0, t < 22 ? C1 : C2) : 0)
       : it > 0 ? 0
       : t < 26 ? TK(2, t - 18, 1, C2, 64)
       : t == 26 ? TK(1, 6, 1, C2, (C2 + 64) / 2) : t == 27 ? TK(1, 6, 2, (C2 + 64) / 2, 64)
       : t == 28 ? TK(2, 5, 1, C1, 64) : t == 29 ? TK(1, 5, 1, C2, 64)
       : t == 30 ? TK(1, 7, 1, C2, (C2 + 64) / 2) : t == 31 ? TK(1, 7, 2, (C2 + 64) / 2, 64)
       : TK(0, t - 32, 0, 0, QX);
}
template <int IT, int I = 0> __device__ __forceinline__ unsigned task_sel(int t) {
  if constexpr (I == 40) return 0u;
  else { unsigned r = task_sel<IT, I + 1>(t); constexpr unsigned c = task_tab(I, IT); if (c) asm("s_cmp_eq_u32 %1, %2\n\ts_cselect_b32 %0, %3, %0" : "+s"(r) : "s"(t), "i"(I), "i"(c) : "scc"); return r; }
}
__device__ __forceinline__ unsigned task_of(int t, int it) { return it == 0 ? task_sel<0>(t) : (it == 1 ? task_sel<1>(t) : task_sel<2>(t)); }
__global__ void __launch_bounds__(512, 2) k_mega(const u16* __restrict__ Wq, const u16* __restrict__ Wk, const u16* __restrict__ Wv,
                                                 const u16* __restrict__ Wo, const u16* __restrict__ w1, const u16* __restrict__ w3,
                                                 const u16* __restrict__ w2, const u16* __restrict__ x, const u16* __restrict__ cvec,
                                                 const u16* __restrict__ wmod, char* ws, u16* out) {
  extern __shared__ __attribute__((aligned(16))) char smem[];
  int wave_id_ = __builtin_amdgcn_readfirstlane((int)threadIdx.x >> 6);
  asm volatile("" : "+s"(wave_id_));
  unsigned* tmo = (unsigned*)(ws + OFF_TMO); unsigned* tick = (unsigned*)(ws + OFF_CNT);
  unsigned long long* mts = nullptr;
  const float* mod = (const float*)(ws + OFF_MOD);
  volatile LAS unsigned* tslot = (volatile LAS unsigned*)(smem + L_SLOT + 768);
  if (opaque_tid() == 0) {
    unsigned xcc; asm volatile("s_getreg_b32 %0, hwreg(HW_REG_XCC_ID)" : "=s"(xcc));
    const int4* cq = (const int4*)(ws + OFF_CAL + 4 * NCALW * 4);
    const int4 ca = cq[0], cb = cq[1], cc = cq[2], cd = cq[3];
    unsigned sl = 0xFFFFu;
    for (int i = 0; i < 8; ++i) {
      const unsigned xg = (xcc + i) & 7;
      const unsigned t = __hip_atomic_fetch_add(tick + (TICK + xg) * 32, 1u, __ATOMIC_RELAXED, __HIP_MEMORY_SCOPE_AGENT);
      if (t < 32) { sl = xg * 32 + t; break; }
    }
    const int sum = ca.x + ca.y + ca.z + ca.w + cb.x + cb.y + cb.z + cb.w + cc.x + cc.y + cc.z + cc.w + cd.x + cd.y + cd.z + cd.w;
    *tslot = sl | ((unsigned)((sum > 0 ? 0 : 1) ^ XPARC) << 16);
  }
  __syncthreads();
  const unsigned tsl = __builtin_amdgcn_readfirstlane(*tslot);
  __syncthreads();
  const int slot = (int)(tsl & 0xFFFFu);
  unsigned* cnt = (unsigned*)(xch_base(ws) + ((size_t)4 << 21) + (size_t)((tsl >> 16) << 13));
  if (slot >= 256) return;
  const int xg = slot >> 5, t = xg == 3 ? 24 + (slot & 7) : slot & 31;
  const char* cb = uni((const char*)cnt);
  if (xg < 3) {
    char* xch = xch_base(ws);
    const int* cal = (const int*)(ws + OFF_CAL);
    auto chunk = [&](int c) { int sum = 0; for (int w = 0; w < NCALW; ++w) sum += cal[c * NCALW + w]; return uni(xch + ((size_t)c << 21) + (XPARF(sum) << 13)); };
    const char* xb = chunk(3);
    const char* ob = chunk(0);
    const int nq = xg == 2 ? 8 : 12;
    if (t < nq) { const int h = xg * 12 + t; qk_body(smem, wave_id_, xg, h, (h >> 3) * 32 + (h & 7), Wq, Wk, ws + OFF_K, ws + OFF_V, ws, tmo, cnt, xb, ob); }
    else { const int r = xg * 20 + t - nq; vr_body(smem, wave_id_, xg, r >> 1, r & 1, r, (r >> 4) * 32 + 8 + (r & 15), Wv, Wo, ws, tmo, cnt, xb, ob); }
    return;
  }
  const bool cx = xg < 4;
  const int xq = cx ? (slot >> 3) & 3 : xg & 3, wsl = xq * 32 + t;
  if (!cx && t >= 24) l1_role(smem, wave_id_, xq * 8 + t - 24, x, mod, cvec, wmod, ws + OFF_H1, cnt, tmo, mts);
#define GATE 0
#define GDEL 600
  if (GDEL && (cx || t < 8)) { const unsigned t0 = (unsigned)__builtin_amdgcn_s_memrealtime(); while ((unsigned)__builtin_amdgcn_s_memrealtime() - t0 < GDEL) __builtin_amdgcn_s_sleep(8); }
  if (cx && (GATE & 1)) { if (wave_id_ == 0) wait_k0(cnt, 8 * xq + t - 24, tmo); __syncthreads(); }
  for (int it = 0; it < 3; ++it) {
    const int ti = cx ? t + 8 : t;
    const unsigned tk = task_of(ti, it);
    if (tk == 0) break;
    const int m = tk & 3, hl = (tk >> 2) & 7, part = (tk >> 5) & 3, bo = (tk >> 8) & 255, ns = (int)(tk >> 16) - bo, h = 8 * xq + hl;
    const bool first = m != 0 && bo == 0, late = it > 0;
    unsigned* pw = cnt + (PQKV + h) * 32;
    const int lane = lane_id();
    const Dep d{cb, (unsigned)(PL1 + (lane & 31)) * 128, 8u, (unsigned)-bo};
    if (wave_id_ == 0) {
      if (late) wait_l1(cnt, 8u, tmo);
      else if (!first) {
        if (!cx && t < 24 && (GATE & 2)) wait_k0(cnt, h, tmo);
      }
    }
    __syncthreads();
    gemm7<R_QKV>(smem, wave_id_, (m == 0 ? Wq : (m == 1 ? Wk : Wv)) + h * DH, nullptr, HD, ws + OFF_H1 + (size_t)bo * 65536, 65536, d,
                    ws + (m == 0 ? OFF_Q : (m == 1 ? OFF_K : OFF_V)) + h * 8192 + (size_t)bo * 32 * 8192, 32 * 8192,
                    pw + 4 * part + m, tmo, wsl, mts, false, first ? NIMM : 0, ns, late ? ns : 0, bo * 257);
  }
  __syncthreads();
  const int u2 = cx ? -1 : (t == 22 ? 0 : (t == 23 ? 6 : (t >= 13 && t < 16 ? t - 11 : (t == 24 ? 5 : (t == 25 ? 1 : -1)))));
  if (!cx && (t == 24 || t == 25)) {
    const int fl = ((3 + 2 * xq) & 7) + (t - 24) * 8, tid = opaque_tid();
    if (fl < 14) {
      unsigned acc = 0;
#pragma unroll
      for (int i = 0; i < 8; ++i) {
        const unsigned* p = (const unsigned*)(((i & 1) ? w3 : w1) + (size_t)((tid >> 1) + 256 * (i >> 1)) * FF + 64 * (14 * xq + fl) + (tid & 1) * 32);
        acc ^= *p;
      }
      asm volatile("" :: "v"(acc));
    }
  }
  if (cx || (u2 >= 0 && (xq < 3 || u2 < 3))) {
    const int ks = cx || xq < 3 ? xq : u2, cn = cx ? (t + 1) & 7 : (xq < 3 ? u2 : 7);
    const int lane = lane_id();
    const Dep d{cb, (unsigned)(cx ? PSA + 4 * xq + (lane & 3) : PF1 + 16 * ks + (lane & 15)) * 128, cx ? 4u : 1u, cx ? (unsigned)(lane & 3) : 0u};
    gemm7<R_PART>(smem, wave_id_, (cx ? Wo : w2) + (size_t)ks * 1024 * DM + cn * 128, nullptr, DM, ws + (cx ? OFF_O : OFF_G) + ks * 16 * 4096, (cx ? 64 : 56) * 4096, d,
                  ws + (cx ? OFF_P : OFF_P2) + (ks * DM + cn * 128) * (TLP ? 64 : 2), (size_t)32 * PROW, cnt + ((cx ? PWO : PF2) + ks * 8 + cn) * 32, tmo,
                  128 + xq * 32 + (cx ? cn : 22 + u2), mts, !cx);
  } else if (u2 >= 0) {
    const int c4 = u2 - 3;
    const int lane = lane_id();
    const Dep d{cb, (unsigned)(PF1 + 48 + (lane & 7)) * 128, 1u, 0u};
    gemm7<R_PART2>(smem, wave_id_, w2 + (size_t)3072 * DM + c4 * 256, nullptr, DM, ws + OFF_G + 48 * 4096, 56 * 4096, d,
                   ws + OFF_P2 + (3 * DM + c4 * 256) * (TLP ? 64 : 2), (size_t)32 * PROW, cnt + (PF2 + 24 + c4) * 32, tmo, 128 + xq * 32 + 22 + u2, mts, true);
  } else if (t < 8 || (t >= 16 && t < 22)) {
    int fl = t < 8 ? 6 + t : t - 16;
    {
      const int sa = (3 + 2 * xq) & 7, sb = sa + 8 < 14 ? sa + 8 : 13;
      if (fl == 12) fl = sa; else if (fl == sa) fl = 12;
      else if (fl == 13) fl = sb; else if (fl == sb) fl = 13;
    }
    const int f = 14 * xq + fl;
    const int lane = lane_id();
    const Dep d{cb, (unsigned)((PLN + (lane >> 3)) * 128 + (lane & 7) * 4), 1u, 0u};
    gemm7<R_F1>(smem, wave_id_, w1 + 64 * f, w3 + 64 * f, FF, ws + OFF_H2, 65536, d, ws + OFF_G + f * 4096, 56 * 4096, cnt + (PF1 + f) * 32, tmo, 128 + xq * 32 + 8 + fl, mts, false);
  } else if (t >= 9 && t < 13) {
    fin_role(smem, wave_id_, xq * 4 + t - 9, mod, ws + OFF_P2, (const u16*)(ws + OFF_X1), out, cnt, tmo, mts);
  } else if (t == 28 || t == 29) {
    row_role(smem, wave_id_, 8 * xq, t - 28, mod, ws + OFF_P, x, (u16*)(ws + OFF_X1), ws + OFF_H2, cnt, tmo, 256 + xq, mts);
  } else if (t >= 26) {
    sa_role(smem, wave_id_, t >= 30 ? t - 30 : t - 24, 8 * xq, ws + OFF_Q, ws + OFF_K, ws + OFF_V, ws + OFF_KVM, ws + OFF_O, cnt, tmo, 128 + xq * 32 + 30 + (t & 1), mts);
  }
}

#define PPF 0
#define PPV 0
constexpr int NARM = 8 + 145 + (NSEG - SDIR) * 8;
#define NCS 6
__global__ void __launch_bounds__(256) k_prep(const u16* __restrict__ c, const u16* __restrict__ wmod, const u16* __restrict__ mem0,
                                              float* __restrict__ mod, char* __restrict__ ws, unsigned* __restrict__ cnt,
                                              const u16* __restrict__ Wq, const u16* __restrict__ Wk, const u16* __restrict__ Wv) {
  const int t = threadIdx.x;
  int b = blockIdx.x;
  char* xch = xch_base(ws);
  if (b < NCALB) {
    if (t >= 64) return;
    const int ci = b % 3, w = b / 3, chunk = ci == 0 ? 0 : ci + 2;
    char* base = xch + ((size_t)chunk << 21) + 127 * 16384 + w * 512;
    st16_sc1(base + (t >> 5) * 8192 + (t & 31) * 16, u32x4{SENT, SENT, SENT, SENT});
    asm volatile("s_waitcnt vmcnt(0)" ::: "memory");
    unsigned best[2] = {1u << 30, 1u << 30};
    const char* sb = uni(base);
#pragma unroll
    for (int r = 1; r < NCS; ++r) {
      unsigned long long t0, t1; unsigned v;
      asm volatile("s_memtime %0\n\ts_waitcnt lgkmcnt(0)\n\ts_load_dword %2, %3, %4 glc\n\ts_waitcnt lgkmcnt(0)\n\ts_memtime %1\n\ts_waitcnt lgkmcnt(0)"
                   : "=&s"(t0), "=&s"(t1), "=&s"(v) : "s"(sb), "i"((r & 1) * 8192 + (r >> 1) * 128) : "memory");
      if (r > 1) best[r & 1] = min(best[r & 1], (unsigned)(t1 - t0));
    }
    unsigned xcc; asm volatile("s_getreg_b32 %0, hwreg(HW_REG_XCC_ID)" : "=s"(xcc));
    const int d = max(-250, min(250, (int)best[1] - (int)best[0]));
    if (t == 0) ((int*)(ws + OFF_CAL))[chunk * NCALW + w] = (xcc & 4) ? -d : d;
    return;
  }
  b -= NCALB;
  if (b < NMODB) {
    __shared__ float red[32][65];
    const int cgp = b & 31, ksl = b >> 5;
    const int cg = t & 7, r0 = t >> 3, n0 = cgp * 64 + cg * 8;
    float acc[8] = {0, 0, 0, 0, 0, 0, 0, 0};
    u32x4 w[4]; u16 cv16[4];
#pragma unroll
    for (int i = 0; i < 4; ++i) {
      const int k = ksl * 128 + r0 + 32 * i;
      w[i] = ldnt<1>(wmod + (size_t)k * (6 * DM) + n0);
      cv16[i] = c[k];
    }
#pragma unroll
    for (int i = 0; i < 4; ++i) {
      const float cv = bf2f(cv16[i]);
      const float sv = bfr(cv / (1.0f + __expf(-cv)));
      acc[0] += sv * bflo(w[i].x); acc[1] += sv * bfhi(w[i].x); acc[2] += sv * bflo(w[i].y); acc[3] += sv * bfhi(w[i].y);
      acc[4] += sv * bflo(w[i].z); acc[5] += sv * bfhi(w[i].z); acc[6] += sv * bflo(w[i].w); acc[7] += sv * bfhi(w[i].w);
    }
#pragma unroll
    for (int e = 0; e < 8; ++e) red[r0][cg * 8 + e] = acc[e];
    __syncthreads();
    if (t < 64) {
      float s = 0;
#pragma unroll
      for (int r = 0; r < 32; ++r) s += red[r][t];
      mod[ksl * (2 * DM) + cgp * 64 + t] = s;
    }
    return;
  }
  b -= NMODB;
  if (b >= 8) {
    b -= 8;
    char* p = b < 96 ? xch + (size_t)b * 16384 : (b < 140 ? xch + ((size_t)3 << 21) + (size_t)(b - 96 + 8) * 16384 : xch + ((size_t)4 << 21) + (size_t)(b - 140) * 16384);
    if (b >= 145) p = ws + OFF_KVM + (size_t)SDIR * NH * 4096 + (size_t)(b - 145) * 16384;
    const unsigned v = b < 96 + 40 || b >= 145 ? SENT : 0u;
#pragma unroll
    for (int i = 0; i < 4; ++i) *(u32x4*)(p + i * 4096 + t * 16) = u32x4{v, v, v, v};
    return;
  }
  {
    const int rep = b & 3, par = b >> 2;
    if (b == 2 && t < 64) *(u32x4*)(cnt + t * 4) = u32x4{0u, 0u, 0u, 0u};
    if (b == 1 && t == 0) *(unsigned*)(ws + OFF_TMO) = 0;
    for (int i = 0; i < 4; ++i) {
      const int u = t + 256 * i, pairidx = u >> 6, lane = u & 63, g = lane >> 4, hf = (lane >> 2) & 1, row = ((lane >> 3) & 1) * 4 + (lane & 3);
      const int k0 = 32 * (2 * pairidx + hf) + 8 * g;
      u32x4 v = *(const u32x4*)(mem0 + row * DM + k0);
      v.x = nosent(v.x); v.y = nosent(v.y); v.z = nosent(v.z); v.w = nosent(v.w);
      *(u32x4*)(xch + ((size_t)3 << 21) + (size_t)(rep * 2 + (u >> 9)) * 16384 + par * 8192 + (u & 511) * 16) = v;
    }
  }
}
extern "C" void kernel_launch(void* const* d_in, const int* in_sizes, int n_in, void* d_out, int out_size, void* d_ws, size_t ws_size,
                              hipStream_t stream) {
  const u16* x    = (const u16*)d_in[0];
  const u16* mem0 = (const u16*)d_in[1];
  const u16* Wq   = (const u16*)d_in[2];
  const u16* Wk   = (const u16*)d_in[3];
  const u16* Wv   = (const u16*)d_in[4];
  const u16* Wo   = (const u16*)d_in[5];
  const u16* w1   = (const u16*)d_in[8];
  const u16* w3   = (const u16*)d_in[9];
  const u16* w2   = (const u16*)d_in[10];
  const u16* c    = (const u16*)d_in[11];
  const u16* wmod = (const u16*)d_in[12];
#define WSHIFT 0
  char* ws = (char*)d_ws + ((size_t)WSHIFT << 20);
  k_prep<<<NCALB + NMODB + NARM + (PPF > 0 ? 256 : 0), 256, 0, stream>>>(c, wmod, mem0, (float*)(ws + OFF_MOD), ws, (unsigned*)(ws + OFF_CNT), Wq, Wk, Wv);
  hipFuncSetAttribute((const void*)k_mega, hipFuncAttributeMaxDynamicSharedMemorySize, MEGA_LDS);
  k_mega<<<256, 512, MEGA_LDS, stream>>>(Wq, Wk, Wv, Wo, w1, w3, w2, x, c, wmod, ws, (u16*)d_out);
}
```

```cpp
#include <hip/hip_runtime.h>
#include <hip/hip_bf16.h>
#include <stdint.h>
#include <type_traits>
#pragma clang diagnostic ignored "-Winline-asm"
#pragma clang diagnostic ignored "-Wunused-value"

typedef unsigned short u16;
typedef short s16x4 __attribute__((ext_vector_type(4)));
typedef __bf16 bf16x8 __attribute__((ext_vector_type(8)));
typedef float f32x4 __attribute__((ext_vector_type(4)));
typedef float f32x2 __attribute__((ext_vector_type(2)));
typedef unsigned u32x4 __attribute__((ext_vector_type(4)));
typedef unsigned u32x2 __attribute__((ext_vector_type(2)));
#define LAS __attribute__((address_space(3)))

constexpr int SEQ = 2048, DM = 1024, NH = 32, DH = 128, HD = 4096, NMEM = 8, SEG = 32, NSEG = 64, FF = 3584;
constexpr unsigned SENT = 0xFFFFFFFFu;

__device__ __forceinline__ int lane_id() { int l; asm volatile("v_mbcnt_lo_u32_b32 %0, -1, 0\n\tv_mbcnt_hi_u32_b32 %0, -1, %0" : "=v"(l)); return l; }
#define WAVE_ID wave_id_
#define opaque_tid() (WAVE_ID * 64 + lane_id())
__device__ __forceinline__ float bf2f(u16 v) { return __builtin_bit_cast(float, (unsigned)v << 16); }
__device__ __forceinline__ float bflo(unsigned v) { return __builtin_bit_cast(float, v << 16); }
__device__ __forceinline__ float bfhi(unsigned v) { return __builtin_bit_cast(float, v & 0xffff0000u); }
__device__ __forceinline__ float bfr(float x) { return (float)(__bf16)x; }
__device__ __forceinline__ unsigned pack2(float a, float b) {
  typedef __bf16 bf16x2 __attribute__((ext_vector_type(2)));
  bf16x2 v; v[0] = (__bf16)a; v[1] = (__bf16)b; return __builtin_bit_cast(unsigned, v);
}
__device__ __forceinline__ unsigned nosent(unsigned v) { return v == SENT ? 0x7FC07FC0u : v; }
__device__ __forceinline__ bf16x8 asbf8(u32x4 v) { return __builtin_bit_cast(bf16x8, v); }
__device__ __forceinline__ s16x4 ass4(u32x2 v) { return __builtin_bit_cast(s16x4, v); }
__device__ __forceinline__ u32x2 trread(const char* lds_base, unsigned off) {
  s16x4 v = __builtin_amdgcn_ds_read_tr16_b64_v4i16((LAS s16x4*)(lds_base + off));
  return __builtin_bit_cast(u32x2, v);
}
__device__ __forceinline__ void glds16(const void* gsrc, unsigned lds_dst) {
  unsigned keep;
  asm volatile("s_mov_b32 %0, m0\n\ts_mov_b32 m0, %2\n\ts_nop 0\n\tglobal_load_lds_dwordx4 %1, off\n\ts_mov_b32 m0, %0"
               : "=&s"(keep) : "v"(gsrc), "s"(lds_dst) : "memory");
}
#define CNT_ " nt"
#define WNT_ " nt"
__device__ __forceinline__ void glds16_w(const void* gsrc, unsigned lds_dst) {
  unsigned keep;
  asm volatile("s_mov_b32 %0, m0\n\ts_mov_b32 m0, %2\n\ts_nop 0\n\tglobal_load_lds_dwordx4 %1, off" CNT_ "\n\ts_mov_b32 m0, %0"
               : "=&s"(keep) : "v"(gsrc), "s"(lds_dst) : "memory");
}
__device__ __forceinline__ void glds16_sc1(const void* gsrc, unsigned lds_dst) {
  unsigned keep;
  asm volatile("s_mov_b32 %0, m0\n\ts_mov_b32 m0, %2\n\ts_nop 0\n\tglobal_load_lds_dwordx4 %1, off sc1\n\ts_mov_b32 m0, %0"
               : "=&s"(keep) : "v"(gsrc), "s"(lds_dst) : "memory");
}
#define NTM 1
template <int B> __device__ __forceinline__ u32x4 ldnt(const void* p) { if (NTM & B) return __builtin_nontemporal_load((const u32x4*)p); else return *(const u32x4*)p; }
#define MFMA32(a, b, c) __builtin_amdgcn_mfma_f32_16x16x32_bf16((a), (b), (c), 0, 0, 0)

__device__ __forceinline__ void st16_sc1(void* p, u32x4 v) { asm volatile("global_store_dwordx4 %0, %1, off sc1\n\ts_nop 1" :: "v"(p), "v"(v) : "memory"); }
__device__ __forceinline__ void st8_sc1(void* p, u32x2 v) { asm volatile("global_store_dwordx2 %0, %1, off sc1\n\ts_nop 1" :: "v"(p), "v"(v) : "memory"); }
template <bool WT> __device__ __forceinline__ void st8(void* p, u32x2 v) { if (WT) st8_sc1(p, v); else *(u32x2*)p = v; }
template <bool WT> __device__ __forceinline__ void st16(void* p, u32x4 v) { if (WT) st16_sc1(p, v); else *(u32x4*)p = v; }
__device__ __forceinline__ u32x4 ld16_sc1(const void* p) {
  u32x4 r; asm volatile("global_load_dwordx4 %0, %1, off sc1\n\ts_waitcnt vmcnt(0)" : "=&v"(r) : "v"(p) : "memory"); return r;
}
__device__ __forceinline__ void ld16x4_sc1(const void* p, u32x4& a, u32x4& b, u32x4& c, u32x4& d) {
  asm volatile("global_load_dwordx4 %0, %4, off sc1\n\tglobal_load_dwordx4 %1, %4, off offset:1024 sc1\n\t"
               "global_load_dwordx4 %2, %4, off offset:2048 sc1\n\tglobal_load_dwordx4 %3, %4, off offset:3072 sc1\n\ts_waitcnt vmcnt(0)"
               : "=&v"(a), "=&v"(b), "=&v"(c), "=&v"(d) : "v"(p) : "memory");
}
typedef __amdgpu_buffer_rsrc_t rsrc_t;
__device__ __forceinline__ rsrc_t mkrs(const void* p) { return __builtin_amdgcn_make_buffer_rsrc((void*)p, 0, 0x7FFFFFFF, 0x00020000); }
__device__ __forceinline__ u32x4 bld16(rsrc_t r, unsigned off) { return __builtin_amdgcn_raw_buffer_load_b128(r, off, 0, 16); }
__device__ __forceinline__ u32x2 bld8(rsrc_t r, unsigned off) { return __builtin_amdgcn_raw_buffer_load_b64(r, off, 0, 16); }
__device__ __forceinline__ bool ok4(u32x4 v) { return v.x != SENT && v.y != SENT && v.z != SENT && v.w != SENT; }
constexpr int SPIN_CAP = 2000000;
__device__ __forceinline__ bool give_up(int spins, unsigned* tmo, unsigned code) {
  if ((spins & 4095) != 4095) return false;
  if (spins > SPIN_CAP) { __hip_atomic_store(tmo, code, __ATOMIC_RELAXED, __HIP_MEMORY_SCOPE_AGENT); return true; }
  return __hip_atomic_load(tmo, __ATOMIC_RELAXED, __HIP_MEMORY_SCOPE_AGENT) != 0u;
}
template <bool ACQ = true>
__device__ __forceinline__ void wait_cnt(unsigned* c, unsigned n, unsigned* tmo) {
  for (int spins = 0;; ++spins) {
    const unsigned v = __hip_atomic_load(c, __ATOMIC_RELAXED, __HIP_MEMORY_SCOPE_AGENT);
    if (v >= n) break;
    __builtin_amdgcn_s_sleep(8);
    if (give_up(spins, tmo, 5)) break;
  }
  if (ACQ) {
    __builtin_amdgcn_fence(__ATOMIC_ACQUIRE, "agent");
    asm volatile("s_waitcnt vmcnt(0)" ::: "memory");
  }
}

__device__ __forceinline__ const char* uni(const char* p) {
  const unsigned long long v = (unsigned long long)p;
  const unsigned lo = __builtin_amdgcn_readfirstlane((unsigned)v), hi = __builtin_amdgcn_readfirstlane((unsigned)(v >> 32));
  return (const char*)(((unsigned long long)hi << 32) | lo);
}
#define TSCLK "s_memrealtime"
#define TC(k) do { } while (0)
#define TS(k) do { } while (0)
#define TSDECL do { } while (0)
#define TSFLUSH(mask) do { } while (0)

constexpr size_t OFF_MOD  = 0;
constexpr size_t OFF_MOD2 = 65536;
constexpr size_t OFF_TMO  = 98304;
constexpr size_t OFF_TAG  = 131072;
constexpr size_t OFF_CLAIM = 135168;
constexpr size_t OFF_INIT = 196608;
constexpr size_t OFF_H1   = 262144;
constexpr size_t OFF_Q    = OFF_H1 + (size_t)SEQ * DM * 2;
constexpr size_t OFF_K    = OFF_Q + (size_t)SEQ * HD * 2;
constexpr size_t OFF_V    = OFF_K + (size_t)SEQ * HD * 2;
constexpr size_t OFF_O    = OFF_V + (size_t)SEQ * HD * 2;
constexpr size_t OFF_X1   = OFF_O + (size_t)SEQ * HD * 2;
constexpr size_t OFF_H2   = OFF_X1 + (size_t)SEQ * DM * 2;
constexpr size_t OFF_G    = OFF_H2 + (size_t)SEQ * DM * 2;
constexpr size_t OFF_KVM  = (size_t)92 << 20;
constexpr size_t OFF_XCH  = (size_t)100 << 20;
constexpr size_t OFF_P    = (size_t)112 << 20;
constexpr size_t OFF_P2   = (size_t)128 << 20;
constexpr size_t OFF_DUMP = (size_t)144 << 20;
constexpr int PROW = 4 * DM * 2;
#define TLQ 1
#define TLP 1
static_assert(OFF_G + (size_t)SEQ * FF * 2 <= OFF_KVM, "layout");
__device__ __forceinline__ char* xch_base(char* ws) { return (char*)(((size_t)(ws + OFF_XCH) + ((2u << 20) - 1)) & ~(size_t)((2u << 20) - 1)); }
#define XPAR 0
constexpr int PROG = 16;
constexpr int PQKV = 64;
constexpr int PL1 = 96;
constexpr int PSA = 128;
constexpr int PWO = 192;
constexpr int PLN = 224;
constexpr int PF2 = 320;
constexpr int PF1 = 448;
#define SDIR 56
#define SAW 0
constexpr int L_RED = 131072;
constexpr int L_SLOT = L_RED + 16384;
constexpr int L_GB = L_SLOT + 1024;
constexpr int MEGA_LDS = 163840;
static_assert(L_GB + 12288 + 2048 <= MEGA_LDS, "lds");
#define XPARC 0

__device__ __forceinline__ int bswz(int row) { return (((row >> 1) & 1) | (((row >> 3) & 1) << 1)) << 1; }
template <int OFF, bool SC1 = false>
__device__ __forceinline__ void glds16s(const char* sbase, unsigned voff, unsigned lds_base) {
  unsigned keep;
  if (SC1)
    asm volatile("s_mov_b32 %0, m0\n\ts_add_u32 m0, %3, %4\n\ts_nop 0\n\tglobal_load_lds_dwordx4 %1, %2 sc1\n\ts_mov_b32 m0, %0"
                 : "=&s"(keep) : "v"(voff), "s"(sbase), "s"(lds_base), "i"(OFF) : "memory", "scc");
  else
    asm volatile("s_mov_b32 %0, m0\n\ts_add_u32 m0, %3, %4\n\ts_nop 0\n\tglobal_load_lds_dwordx4 %1, %2\n\ts_mov_b32 m0, %0"
                 : "=&s"(keep) : "v"(voff), "s"(sbase), "s"(lds_base), "i"(OFF) : "memory", "scc");
}
template <int OFF>
__device__ __forceinline__ void glds16x2s(const char* sbase, unsigned voff, unsigned lds_base) {
  unsigned keep;
  asm volatile("s_mov_b32 %0, m0\n\ts_add_u32 m0, %3, %4\n\ts_nop 0\n\tglobal_load_lds_dwordx4 %1, %2 sc1\n\tglobal_load_lds_dwordx4 %1, %2 offset:1024 sc1\n\ts_mov_b32 m0, %0"
               : "=&s"(keep) : "v"(voff), "s"(sbase), "s"(lds_base), "i"(OFF) : "memory", "scc");
}
__device__ __forceinline__ void st16s(const char* sb, unsigned vo, u32x4 v) { asm volatile("global_store_dwordx4 %0, %1, %2 sc1\n\ts_nop 1" :: "v"(vo), "v"(v), "s"(sb) : "memory"); }
__device__ __forceinline__ void head_to_lds(const char* src, int lane, unsigned lds) {
  if (TLQ) {
    const int c = (lane & 15) ^ (lane >> 4);
    const unsigned v0 = (unsigned)((c >> 2) * 2048 + (lane >> 4) * 64 + (c & 3) * 16);
#pragma unroll
    for (int i = 0; i < 8; ++i) glds16s<0, true>(src + i * 256, v0 ^ ((i & 3) * 2048), lds + i * 1024);
  } else {
#pragma unroll
    for (int i = 0; i < 4; ++i) glds16x2s<0>(src + i * 2048, (unsigned)lane * 16, lds + i * 2048);
  }
}
#define TCHP ""
__device__ __forceinline__ void touch(const void* sb, unsigned vo, unsigned lds) {
  asm volatile("s_mov_b32 m0, %0\n\ts_nop 0\n\tglobal_load_lds_dword %1, %2" TCHP :: "s"(lds), "v"(vo), "s"(sb) : "memory", "m0");
}
#define WAIT_V(n) asm volatile("s_waitcnt vmcnt(" #n ")" ::: "memory")
#define BAR() do { asm volatile("s_waitcnt lgkmcnt(0)" ::: "memory"); __builtin_amdgcn_s_barrier(); asm volatile("" ::: "memory"); } while (0)
__device__ __forceinline__ void st4_sc1(void* p, unsigned v) { asm volatile("global_store_dword %0, %1, off sc1" :: "v"(p), "v"(v) : "memory"); }
__device__ __forceinline__ unsigned ld4_sc1(const void* p) {
  unsigned r; asm volatile("global_load_dword %0, %1, off sc1\n\ts_waitcnt vmcnt(0)" : "=&v"(r) : "v"(p) : "memory"); return r;
}
template <int CTRL> __device__ __forceinline__ unsigned dppu(unsigned x) { return (unsigned)__builtin_amdgcn_update_dpp(0, (int)x, CTRL, 0xf, 0xf, false); }
__device__ __forceinline__ unsigned rowmin16(unsigned v) {
  v = min(v, dppu<0x128>(v)); v = min(v, dppu<0x124>(v)); v = min(v, dppu<0x122>(v)); v = min(v, dppu<0x121>(v));
  return v;
}
__device__ __forceinline__ unsigned rdl(unsigned v, int l) { return (unsigned)__builtin_amdgcn_readlane((int)v, l); }
__device__ __forceinline__ unsigned qkv_avail(unsigned v, int m) {
  unsigned a = rdl(v, m);
#pragma unroll
  for (int j = 1; j < 4; ++j) { const unsigned p = rdl(v, 4 * j + m); if ((p >> 8) <= a) a = max(a, p & 255u); }
  return a;
}
constexpr float SCALE = 0.08838834764831845f;
constexpr float LOG2E = 1.4426950408889634f;
template <int N> __device__ __forceinline__ void delay64() {
  if (N >= 16) { __builtin_amdgcn_s_sleep(15); delay64<(N >= 16 ? N - 15 : 0)>(); } else if (N > 0) __builtin_amdgcn_s_sleep(N);
}
__device__ __forceinline__ void sw16(float x, float& a, float& b) {
  a = x; b = x; asm volatile("s_nop 1\n\tv_permlane16_swap_b32 %0, %1" : "+v"(a), "+v"(b));
}
__device__ __forceinline__ void sw32(float x, float& a, float& b) {
  a = x; b = x; asm volatile("s_nop 1\n\tv_permlane32_swap_b32 %0, %1" : "+v"(a), "+v"(b));
}
__device__ __forceinline__ float red16_32_max(float x) {
  float a, b; sw16(x, a, b); x = fmaxf(a, b); sw32(x, a, b); return fmaxf(a, b);
}
__device__ __forceinline__ float red16_32_add(float x) {
  float a, b; sw16(x, a, b); x = a + b; sw32(x, a, b); return a + b;
}
__device__ __forceinline__ unsigned cvtpk(float a, float b) { unsigned r; asm("v_cvt_pk_bf16_f32 %0, %1, %2" : "=v"(r) : "v"(a), "v"(b)); return r; }
__device__ __forceinline__ float max3(float a, float b, float c) { float r; asm("v_max3_f32 %0, %1, %2, %3" : "=v"(r) : "v"(a), "v"(b), "v"(c)); return r; }
__device__ __forceinline__ float max2(float a, float b) { float r; asm("v_max_f32 %0, %1, %2" : "=v"(r) : "v"(a), "v"(b)); return r; }
__device__ __forceinline__ unsigned ror8u(unsigned x) { return (unsigned)__builtin_amdgcn_update_dpp(0, (int)x, 0x128, 0xf, 0xf, false); }
__device__ __forceinline__ float ror8(float x) { return __builtin_bit_cast(float, ror8u(__builtin_bit_cast(unsigned, x))); }
__device__ __forceinline__ float add_ror8(float x) {
  return x + __builtin_bit_cast(float, __builtin_amdgcn_update_dpp(0, __builtin_bit_cast(int, x), 0x128, 0xf, 0xf, false));
}

typedef unsigned long long u64;
__device__ __forceinline__ unsigned tag2(u64 tag) { return (unsigned)(tag >> 32) ^ ((unsigned)tag * 0x9E3779B1u); }
__device__ __forceinline__ unsigned init_eval(u32x4 v, u64 tag) {
  if (!__all((v.x & ~7u) == (unsigned)tag << 3 && v.y == (unsigned)(tag >> 29))) return 0u;
  return (8u | (__popcll(__ballot(v.x & 1u)) > 32 ? 1u : 0u) | (__popcll(__ballot(v.x & 2u)) > 32 ? 2u : 0u) | (__popcll(__ballot(v.x & 4u)) > 32 ? 4u : 0u)) ^ (XPAR * 3u + XPARC * 4u);
}
__device__ __forceinline__ unsigned init_wait(const char* ws, u64 tag) {
  for (int spins = 0; spins < SPIN_CAP; ++spins) {
    const unsigned r = init_eval(ld16_sc1(ws + OFF_INIT + (lane_id() & 31) * 128), tag);
    if (r) return r;
    __builtin_amdgcn_s_sleep(4);
  }
  return 8u;
}
__device__ __forceinline__ void init_issue(const char* ws, unsigned lds) { glds16s<0, true>(uni(ws + OFF_INIT), (unsigned)(lane_id() & 31) * 128u, lds); }
__device__ __forceinline__ unsigned init_get(const char* ws, u64 tag, const char* copy) {
  const unsigned r = init_eval(*(volatile LAS u32x4*)(copy + lane_id() * 16), tag);
  return r ? r : init_wait(ws, tag);
}
__device__ __forceinline__ unsigned* cnt_of(char* ws, unsigned in) { return (unsigned*)uni(xch_base(ws) + ((size_t)4 << 21) + (size_t)((in & 4u) << 11)); }
__device__ __forceinline__ void wait_l1(const unsigned* cnt, unsigned n, unsigned* tmo) {
  const int lane = lane_id();
  for (int spins = 0;; ++spins) {
    const unsigned v = ld4_sc1(cnt + (PL1 + (lane & 31)) * 32);
    if (__all(v >= n)) break;
    __builtin_amdgcn_s_sleep(8);
    if (give_up(spins, tmo, 12)) break;
  }
}
#define GKV 1
__device__ __forceinline__ void wait_k0(const unsigned* cnt, int h, unsigned* tmo) {
  for (int spins = 0;; ++spins) {
    const int l = lane_id();
    const unsigned v = ld4_sc1(cnt + (PQKV + h) * 32 + 1 + (l & 1));
    if (__all(v >= (unsigned)GKV)) break;
    __builtin_amdgcn_s_sleep(32);
    if (give_up(spins, tmo, 13)) break;
  }
}
#define WCLK(slot, b) do { } while (0)
#define WTM(slot, b, k) do { } while (0)
#define WTS(slot, b, k) do { } while (0)
#define WFB() do { } while (0)
#define STS(ws_, slot_, k_) do { } while (0)
typedef __bf16 bf16x2 __attribute__((ext_vector_type(2)));
__device__ __forceinline__ f32x2 pkfma(f32x2 a, f32x2 b, f32x2 c) { return __builtin_elementwise_fma(a, b, c); }
__device__ __forceinline__ float wavesum(float v) {
  v += __builtin_bit_cast(float, dppu<0x128>(__builtin_bit_cast(unsigned, v))); v += __builtin_bit_cast(float, dppu<0x124>(__builtin_bit_cast(unsigned, v)));
  v += __builtin_bit_cast(float, dppu<0x122>(__builtin_bit_cast(unsigned, v))); v += __builtin_bit_cast(float, dppu<0x121>(__builtin_bit_cast(unsigned, v)));
  return red16_32_add(v);
}
__device__ __forceinline__ void bst16(rsrc_t r, unsigned off, unsigned soff, u32x4 v) { __builtin_amdgcn_raw_buffer_store_b128(v, r, off + soff, 0, 16); }
__device__ __forceinline__ float adds(unsigned v, unsigned sel, float c) { return __builtin_amdgcn_fdot2_f32_bf16(__builtin_bit_cast(bf16x2, v), __builtin_bit_cast(bf16x2, sel), c, false); }
__device__ __forceinline__ void sum4(const u32x4 (&p)[4], f32x2 (&a)[4]) {
  unsigned slo = 0x00003F80u, shi = 0x3F800000u;
  asm("" : "+s"(slo), "+s"(shi));
#pragma unroll
  for (int e = 0; e < 4; ++e) {
    float lo = bflo(p[0][e]), hi = bfhi(p[0][e]);
#pragma unroll
    for (int q = 1; q < 4; ++q) { lo = adds(p[q][e], slo, lo); hi = adds(p[q][e], shi, hi); }
    a[e] = f32x2{lo, hi};
  }
}
struct RowSet { u32x4 p[2][4]; u32x4 rv[2]; };
#define XAUX 16
__device__ __forceinline__ void row_issue2(RowSet& S, rsrc_t rp, rsrc_t rr, int row, unsigned l16) {
#pragma unroll
  for (int i = 0; i < 2; ++i) {
#pragma unroll
    for (int q = 0; q < 4; ++q) S.p[i][q] = TLP ? __builtin_amdgcn_raw_buffer_load_b128(rp, (l16 >> 6) * 2048 + (l16 & 48) + i * 32768, (unsigned)((row >> 5) * 32 * PROW + q * 65536 + (row & 31) * 64), 16)
                                                 : __builtin_amdgcn_raw_buffer_load_b128(rp, l16 + i * 1024, (unsigned)(row * PROW + q * 2048), 16);
    S.rv[i] = __builtin_amdgcn_raw_buffer_load_b128(rr, l16 + i * 1024, (unsigned)(row * DM) * 2, XAUX);
  }
}
__device__ __forceinline__ void row_a(const RowSet& S, f32x2 (&a)[2][4], u32x4 (&xv)[2]) {
#pragma unroll
  for (int i = 0; i < 2; ++i) { sum4(S.p[i], a[i]); xv[i] = S.rv[i]; }
#pragma unroll
  for (int i = 0; i < 2; ++i) asm volatile("" : "+v"(a[i][0]), "+v"(a[i][1]), "+v"(a[i][2]), "+v"(a[i][3]), "+v"(xv[i]));
}
__device__ __forceinline__ u32x4 resid8(const f32x2 (&a)[4], u32x4 xv, const char* mt, f32x2 (&v)[4]) {
  const f32x4 g0 = *(const f32x4*)mt, g1 = *(const f32x4*)(mt + 1024);
  u32x4 o;
  o[0] = [&] { const f32x2 s = pkfma(f32x2{g0[0], g0[1]}, a[0], f32x2{bflo(xv[0]), bfhi(xv[0])}); return pack2(s[0], s[1]); }();
  o[1] = [&] { const f32x2 s = pkfma(f32x2{g0[2], g0[3]}, a[1], f32x2{bflo(xv[1]), bfhi(xv[1])}); return pack2(s[0], s[1]); }();
  o[2] = [&] { const f32x2 s = pkfma(f32x2{g1[0], g1[1]}, a[2], f32x2{bflo(xv[2]), bfhi(xv[2])}); return pack2(s[0], s[1]); }();
  o[3] = [&] { const f32x2 s = pkfma(f32x2{g1[2], g1[3]}, a[3], f32x2{bflo(xv[3]), bfhi(xv[3])}); return pack2(s[0], s[1]); }();
#pragma unroll
  for (int e = 0; e < 4; ++e) v[e] = f32x2{bflo(o[e]), bfhi(o[e])};
  return o;
}
__device__ __forceinline__ u32x4 norm8(const f32x2 (&v)[4], const char* mt, float mb, float rden) {
  const f32x4 h0 = *(const f32x4*)(mt + 4096), h1 = *(const f32x4*)(mt + 4096 + 1024), c0 = *(const f32x4*)(mt + 8192), c1 = *(const f32x4*)(mt + 8192 + 1024);
  const f32x2 m2 = f32x2{mb, mb}, d2 = f32x2{rden, rden};
  const f32x2 r0 = pkfma(v[0] - m2, f32x2{c0[0], c0[1]} * d2, f32x2{h0[0], h0[1]}), r1 = pkfma(v[1] - m2, f32x2{c0[2], c0[3]} * d2, f32x2{h0[2], h0[3]});
  const f32x2 r2 = pkfma(v[2] - m2, f32x2{c1[0], c1[1]} * d2, f32x2{h1[0], h1[1]}), r3 = pkfma(v[3] - m2, f32x2{c1[2], c1[3]} * d2, f32x2{h1[2], h1[3]});
  return u32x4{pack2(r0[0], r0[1]), pack2(r1[0], r1[1]), pack2(r2[0], r2[1]), pack2(r3[0], r3[1])};
}
__device__ __forceinline__ void row_b(const f32x2 (&a)[2][4], const u32x4 (&xv)[2], const char* ml, rsrc_t rx1, rsrc_t rh2, int row, int lane) {
  f32x2 v[2][4];
  unsigned mo = lane * 16; asm volatile("" : "+v"(mo));
  ml += mo;
#pragma unroll
  for (int i = 0; i < 2; ++i) bst16(rx1, mo + i * 1024, (unsigned)(row * DM) * 2, resid8(a[i], xv[i], ml + i * 2048, v[i]));
  const f32x2 s2 = ((v[0][0] + v[0][1]) + (v[0][2] + v[0][3])) + ((v[1][0] + v[1][1]) + (v[1][2] + v[1][3]));
  const float mean = wavesum(s2[0] + s2[1]) * (1.0f / DM);
  f32x2 q2 = f32x2{0, 0};
#pragma unroll
  for (int i = 0; i < 8; ++i) { const f32x2 d = v[i >> 2][i & 3] - f32x2{mean, mean}; q2 = pkfma(d, d, q2); }
  const float q = wavesum(q2[0] + q2[1]);
  const float mb = bfr(mean), vb = bfr(q * (1.0f / DM));
  const float rden = 1.0f / bfr(sqrtf(bfr(vb + 1e-5f)));
  const int rb = row & 31;
  const unsigned hd = (unsigned)((((lane & 7) ^ (rb & 7)) << 4) + (lane >> 3) * 4096);
#pragma unroll
  for (int i = 0; i < 2; ++i) bst16(rh2, hd, (unsigned)((row >> 5) * 65536 + rb * 128 + i * 32768), norm8(v[i], ml + i * 2048, mb, rden));
}
__device__ __forceinline__ void half_load(const char* P, const u16* res, int row, int hf, int lane, f32x2 (&a)[4], u32x4& rv) {
  const rsrc_t rp = mkrs(P), rr = mkrs(res);
  const unsigned po = TLP ? (unsigned)((row >> 5) * 32 * PROW + (hf * 16 + (lane >> 2)) * 2048 + (row & 31) * 64 + (lane & 3) * 16) : (unsigned)(row * PROW + hf * 1024 + lane * 16), ro = (unsigned)(row * DM * 2 + hf * 1024 + lane * 16);
  u32x4 p[4];
#pragma unroll
  for (int q = 0; q < 4; ++q) p[q] = bld16(rp, po + q * (TLP ? 65536 : 2048));
  if (res) rv = bld16(rr, ro);
  sum4(p, a);
}
__device__ __forceinline__ void fin_half(const char* P, const u16* res, const char* gbt, u16* __restrict__ o1, int row, int hf, int lane) {
  f32x2 a[4]; u32x4 rv;
  half_load(P, res, row, hf, lane, a, rv);
  const u32x4 gv = *(const u32x4*)(gbt + hf * 1024 + lane * 16);
  u32x4 o;
#pragma unroll
  for (int e = 0; e < 4; ++e) { const f32x2 s = pkfma(f32x2{bflo(gv[e]), bfhi(gv[e])}, a[e], f32x2{bflo(rv[e]), bfhi(rv[e])}); o[e] = pack2(s[0], s[1]); }
  st16_sc1(o1 + (size_t)row * DM + hf * 512 + lane * 8, o);
}
template <int CTRL> __device__ __forceinline__ float dppf(float x) { return __builtin_bit_cast(float, dppu<CTRL>(__builtin_bit_cast(unsigned, x))); }
__device__ __forceinline__ void ln_row(const f32x2 (&a)[2][4], const f32x2 (&xf)[8], const f32x2 (&tg)[8], const f32x2 (&th)[8], const f32x2 (&tc)[8],
                                       const char* x1, const char* h2, unsigned xo, unsigned ho) {
  f32x2 v[8]; u32x4 o[2];
#pragma unroll
  for (int e = 0; e < 8; ++e) { const f32x2 t = pkfma(tg[e], a[e >> 2][e & 3], xf[e]); const unsigned u = pack2(t[0], t[1]); o[e >> 2][e & 3] = u; v[e] = f32x2{bflo(u), bfhi(u)}; }
  const float pv = __builtin_bit_cast(float, __builtin_amdgcn_readfirstlane(__float_as_int(v[0][0])));
  f32x2 d[8];
#pragma unroll
  for (int e = 0; e < 8; ++e) d[e] = v[e] - f32x2{pv, pv};
  const f32x2 s2 = ((d[0] + d[1]) + (d[2] + d[3])) + ((d[4] + d[5]) + (d[6] + d[7]));
  const f32x2 q2 = pkfma(d[3], d[3], pkfma(d[2], d[2], pkfma(d[1], d[1], d[0] * d[0]))) + pkfma(d[7], d[7], pkfma(d[6], d[6], pkfma(d[5], d[5], d[4] * d[4])));
  float s = s2[0] + s2[1], q = q2[0] + q2[1];
  s += dppf<0x128>(s); q += dppf<0x128>(q); s += dppf<0x124>(s); q += dppf<0x124>(q);
  s += dppf<0x122>(s); q += dppf<0x122>(q); s += dppf<0x121>(s); q += dppf<0x121>(q);
  asm volatile("s_nop 1\n\tv_permlane16_swap_b32 %0, %1" : "+v"(s), "+v"(q));
  s += q;
  { float x, y; sw32(s, x, y); s = x + y; }
  const float sh = __builtin_bit_cast(float, rdl(__builtin_bit_cast(unsigned, s), 0)) * (1.0f / DM), sq = __builtin_bit_cast(float, rdl(__builtin_bit_cast(unsigned, s), 16));
  const float mb = bfr(pv + sh), vb = bfr(fmaxf(sq * (1.0f / DM) - sh * sh, 0.0f));
  const float rden = __builtin_amdgcn_rcpf(bfr(__builtin_amdgcn_sqrtf(bfr(vb + 1e-5f))));
#pragma unroll
  for (int i = 0; i < 2; ++i) {
    u32x4 r;
#pragma unroll
    for (int e = 0; e < 4; ++e) { const f32x2 t = pkfma(v[4 * i + e] - f32x2{mb, mb}, tc[4 * i + e] * f32x2{rden, rden}, th[4 * i + e]); r[e] = pack2(t[0], t[1]); }
    st16s(h2, ho + i * 32768, r);
  }
  st16s(x1, xo, o[0]); st16s(x1, xo + 1024, o[1]);
}
__device__ __forceinline__ void row_table(char* tab, const float* mod, const unsigned* cnt, unsigned* tmo, int tid) {
  wait_l1(cnt + 1, 1u, tmo);
  const rsrc_t rm = mkrs((const char*)mod + OFF_MOD2);
#pragma unroll
  for (int i = 0; i < 2; ++i) {
    const int u = tid + 512 * i, a = u >> 8, c = u & 255;
    f32x4 v = __builtin_bit_cast(f32x4, bld16(rm, (unsigned)u * 16));
    v = f32x4{bfr(v[0]), bfr(v[1]), bfr(v[2]), bfr(v[3])};
    if (a == 2) v = f32x4{bfr(1.0f + v[0]), bfr(1.0f + v[1]), bfr(1.0f + v[2]), bfr(1.0f + v[3])};
    if (a < 3) *(f32x4*)(tab + a * 4096 + ((c >> 7) * 2 + (c & 1)) * 1024 + ((c >> 1) & 63) * 16) = v;
    else *(u32x2*)(tab + 12288 + c * 8) = u32x2{pack2(v[0], v[1]), pack2(v[2], v[3])};
  }
  __syncthreads();
}
#define LNB 48
__device__ __forceinline__ void row_role(char* smem, int wave_id_, int rbase, int par, const float* mod, const char* P, const u16* x, u16* x1, char* h2c,
                                         unsigned* cnt, unsigned* tmo, int slot, unsigned long long* mts) {
  const int tid = opaque_tid(), lane = tid & 63, wave = WAVE_ID;
  if (tid < 4) *(volatile LAS unsigned*)(smem + 16384 + tid * 4) = 0u;
  row_table(smem, mod, cnt, tmo, tid);
  const int rb = rbase + wave + 32 * par;
  const rsrc_t rx1 = mkrs(x1), rh2 = mkrs(h2c), rc = mkrs(cnt), rp = mkrs(P), rr = mkrs(x);
  const unsigned l16 = lane * 16;
  const unsigned pwo = (unsigned)(PWO + (lane & 31)) * 128;
  unsigned* myp = cnt + (PLN + (rb >> 3)) * 32 + wave;
  auto eval = [&](unsigned v) { v = rowmin16(v); return (int)((min(min(rdl(v, 0), rdl(v, 16)), (unsigned)LNB) + 1u - par) >> 1); };
  auto wmk = [&](int n) { return (unsigned)min(2 * n + par, LNB); };
  if (par && lane == 0) st4_sc1(myp, 1u);
  int b = 0, wa = 0;
  f32x2 a[2][4]; u32x4 xv[2];
  while (b < LNB / 2) {
    if (b >= wa) {
      for (int spins = 0;; ++spins) {
        wa = eval(ld4_sc1((const char*)cnt + pwo));
        if (b < wa) break;
        __builtin_amdgcn_s_sleep(2);
        if (give_up(spins, tmo, 11)) { wa = LNB / 2; break; }
      }
    }
    if ((b & 3) == 0 || b >= 28) { WTM(slot, 2 * b + par, 0); }
    if (wa >= b + 8) {
      RowSet S[4];
      const int b0 = b;
      asm volatile("; deep" ::: "memory");
#pragma unroll
      for (int i = 0; i < 4; ++i) row_issue2(S[i], rp, rr, (b + i) * 64 + rb, l16);
      while (b + 7 < wa) {
        const unsigned smp = __builtin_amdgcn_raw_buffer_load_b32(rc, pwo, 0, 16);
#pragma unroll
        for (int i = 0; i < 4; ++i) {
          row_a(S[i], a, xv);
          WAIT_V(42);
          if (lane == 0 && b > b0) st4_sc1(myp, wmk((b + i - 3)));
          __builtin_amdgcn_sched_barrier(0);
          row_issue2(S[i], rp, rr, (b + 4 + i) * 64 + rb, l16);
          __builtin_amdgcn_sched_barrier(0);
          row_b(a, xv, smem, rx1, rh2, (b + i) * 64 + rb, lane);
          __builtin_amdgcn_sched_barrier(0);
        }
        const int w = eval(smp);
        wa = w > wa ? w : wa;
        b += 4;
      }
#pragma unroll
      for (int i = 0; i < 4; ++i) {
        row_a(S[i], a, xv);
        __builtin_amdgcn_sched_barrier(0);
        row_b(a, xv, smem, rx1, rh2, (b + i) * 64 + rb, lane);
        WAIT_V(4);
        if (lane == 0) st4_sc1(myp, wmk((b + i)));
        __builtin_amdgcn_sched_barrier(0);
      }
      WAIT_V(0);
      b += 4;
      if (lane == 0) st4_sc1(myp, wmk(b));
      continue;
    }
    if (wa < b + 2) {
      RowSet T;
      asm volatile("; single" ::: "memory");
      row_issue2(T, rp, rr, b * 64 + rb, l16);
      row_a(T, a, xv);
      row_b(a, xv, smem, rx1, rh2, b * 64 + rb, lane);
      WAIT_V(0);
      ++b;
      if (lane == 0) st4_sc1(myp, wmk(b));
      WTM(slot, 2 * (b - 1) + par, 2);
      continue;
    }
    RowSet S0, S1;
    const int b0 = b;
    asm volatile("; multi" ::: "memory");
    row_issue2(S0, rp, rr, b * 64 + rb, l16);
    row_issue2(S1, rp, rr, (b + 1) * 64 + rb, l16);
    while (b + 3 < wa) {
      const unsigned smp = __builtin_amdgcn_raw_buffer_load_b32(rc, pwo, 0, 16);
      row_a(S0, a, xv);
      WAIT_V(15);
      if (lane == 0 && b > b0) st4_sc1(myp, wmk((b - 1)));
      __builtin_amdgcn_sched_barrier(0);
      row_issue2(S0, rp, rr, (b + 2) * 64 + rb, l16);
      __builtin_amdgcn_sched_barrier(0);
      row_b(a, xv, smem, rx1, rh2, b * 64 + rb, lane);
      __builtin_amdgcn_sched_barrier(0);
      row_a(S1, a, xv);
      WAIT_V(15);
      if (lane == 0 && b > b0) st4_sc1(myp, wmk(b));
      __builtin_amdgcn_sched_barrier(0);
      row_issue2(S1, rp, rr, (b + 3) * 64 + rb, l16);
      __builtin_amdgcn_sched_barrier(0);
      row_b(a, xv, smem, rx1, rh2, (b + 1) * 64 + rb, lane);
      __builtin_amdgcn_sched_barrier(0);
      const int w = eval(smp);
      wa = w > wa ? w : wa;
      b += 2;
    }
    row_a(S0, a, xv);
    row_b(a, xv, smem, rx1, rh2, b * 64 + rb, lane);
    row_a(S1, a, xv);
    WAIT_V(4 - 4);
    if (lane == 0) st4_sc1(myp, wmk((b + 1)));
    WTM(slot, 2 * b + par, 2);
    row_b(a, xv, smem, rx1, rh2, (b + 1) * 64 + rb, lane);
    WAIT_V(0);
    b += 2;
    if (lane == 0) st4_sc1(myp, wmk(b));
    WTM(slot, 2 * (b - 1) + par, 2);
  }
  static_assert(TLP && LNB % 2 == 0, "");
  const int gp = wave >> 2, r4 = rbase + 4 * par + (wave & 3);
  volatile LAS unsigned* lwb = (volatile LAS unsigned*)(smem + 16384 + gp * 8);
  if (gp && lane == 0) st4_sc1(myp, (unsigned)(LNB + 1));
  f32x2 tg[8], th[8], tc[8];
#pragma unroll
  for (int i = 0; i < 4; ++i) {
    const char* mt = smem + lane * 16 + i * 1024;
    const f32x4 g_ = *(const f32x4*)mt, h_ = *(const f32x4*)(mt + 4096), c_ = *(const f32x4*)(mt + 8192);
    tg[2 * i] = f32x2{g_[0], g_[1]}; tg[2 * i + 1] = f32x2{g_[2], g_[3]}; th[2 * i] = f32x2{h_[0], h_[1]}; th[2 * i + 1] = f32x2{h_[2], h_[3]};
    tc[2 * i] = f32x2{c_[0], c_[1]}; tc[2 * i + 1] = f32x2{c_[2], c_[3]};
  }
  const unsigned xo = (unsigned)(r4 * DM * 2 + lane * 16), ho = (unsigned)(r4 * 128 + (((lane & 7) ^ (r4 & 7)) << 4) + (lane >> 3) * 4096);
  const unsigned po = (l16 >> 6) * 2048 + (l16 & 48);
  for (int bb = LNB + gp; bb < NSEG; bb += 2) {
    f32x2 xf[8];
#pragma unroll
    for (int i = 0; i < 2; ++i) {
      const u32x4 rv = __builtin_amdgcn_raw_buffer_load_b128(rr, xo + i * 1024, (unsigned)bb * 65536u, 0);
#pragma unroll
      for (int e = 0; e < 4; ++e) xf[4 * i + e] = f32x2{bflo(rv[e]), bfhi(rv[e])};
    }
    asm volatile("" : "+v"(xf[0]), "+v"(xf[1]), "+v"(xf[2]), "+v"(xf[3]), "+v"(xf[4]), "+v"(xf[5]), "+v"(xf[6]), "+v"(xf[7]));
    {
      if ((wave & 3) == 0) {
        for (int spins = 0;; ++spins) {
          const unsigned v = ld4_sc1((const char*)cnt + pwo);
          if (__all(v > (unsigned)bb)) { lwb[0] = (unsigned)(bb + 1); break; }
          __builtin_amdgcn_s_sleep(2);
          if (give_up(spins, tmo, 11)) { lwb[0] = (unsigned)(bb + 1); break; }
        }
      } else {
        while (__builtin_amdgcn_readfirstlane(lwb[0]) <= (unsigned)bb) __builtin_amdgcn_s_sleep(1);
      }
    }
    WTM(slot, bb, 0);
    u32x4 pp[2][4];
#pragma unroll
    for (int i = 0; i < 2; ++i)
#pragma unroll
      for (int q = 0; q < 4; ++q) pp[i][q] = __builtin_amdgcn_raw_buffer_load_b128(rp, po + i * 32768, (unsigned)(bb * 32 * PROW + q * 65536 + r4 * 64), 16);
    sum4(pp[0], a[0]); sum4(pp[1], a[1]);
    ln_row(a, xf, tg, th, tc, uni((const char*)x1 + (size_t)bb * 65536), uni(h2c + (size_t)bb * 65536), xo, ho);
    WTM(slot, bb, 3);
    WAIT_V(0);
    unsigned o = 0;
    if (lane == 0) o = __hip_atomic_fetch_add((LAS unsigned*)(lwb + 1), 1u, __ATOMIC_RELAXED, __HIP_MEMORY_SCOPE_WORKGROUP);
    if ((__builtin_amdgcn_readfirstlane(o) & 3u) == 3u) {
      if (lane < 4) st4_sc1(myp - (wave & 3) + lane, (unsigned)(bb + 2));
      WTM(slot, bb, 2);
    }
  }
}
#define NCS 6
__device__ __forceinline__ void l1_role(char* smem, int wave_id_, int c, const u16* __restrict__ x, const float* __restrict__ mod,
                                        const u16* __restrict__ mem0, char* __restrict__ h1c, unsigned*& cnt, u64 tag, unsigned* tmo, unsigned long long* mts) {
  const int tid = opaque_tid(), lane = tid & 63, wave = WAVE_ID;
  char* ws = (char*)mod - OFF_MOD;
  u32x4 xr[8][2];
  const u16* src = x + (size_t)(8 * c + wave) * DM + lane * 8;
  xr[0][0] = ldnt<2>(src); xr[0][1] = ldnt<2>(src + 512);
  volatile LAS unsigned* sg = (volatile LAS unsigned*)(smem + L_SLOT + 784);
  if (wave >= 5) {
    char* cal = xch_base(ws) + ((size_t)(wave == 5 ? 0 : wave - 3) << 21) + (126 + (c >> 4)) * 16384 + (c & 15) * 512;
    st16_sc1(cal + (lane >> 5) * 8192 + (lane & 31) * 16, u32x4{SENT, SENT, SENT, SENT});
    WAIT_V(0);
    unsigned best[2] = {1u << 30, 1u << 30};
    const char* sb = uni(cal);
#pragma unroll
    for (int r = 1; r < NCS; ++r) {
      unsigned long long t0, t1; unsigned v;
      asm volatile("s_memtime %0\n\ts_waitcnt lgkmcnt(0)\n\ts_load_dword %2, %3, %4 glc\n\ts_waitcnt lgkmcnt(0)\n\ts_memtime %1\n\ts_waitcnt lgkmcnt(0)"
                   : "=&s"(t0), "=&s"(t1), "=&s"(v) : "s"(sb), "i"((r & 1) * 8192 + (r >> 1) * 128) : "memory");
      if (r > 1) best[r & 1] = min(best[r & 1], (unsigned)(t1 - t0));
    }
    unsigned xcc; asm volatile("s_getreg_b32 %0, hwreg(HW_REG_XCC_ID)" : "=s"(xcc));
    sg[wave - 5] = ((best[1] < best[0]) != ((xcc & 4) != 0)) ? 1u << (wave - 5) : 0u;
  } else if (wave < 3) {
    const int b = c + 32 * wave;
    if (b < 89) {
      const char* p = uni(xch_base(ws) + (b < 48 ? (size_t)((b >> 4) * 32 + (b & 15)) * 16384
                                        : b < 56 ? ((size_t)3 << 21) + (size_t)(8 + ((b - 48) >> 2) * 8 + (b & 3)) * 16384
                                        : b < 84 ? ((size_t)3 << 21) + (size_t)(b - 56 + 24) * 16384 : ((size_t)4 << 21) + (size_t)(b - 84) * 16384));
      const unsigned v = b < 80 ? SENT : 0u;
#pragma unroll
      for (int i = 0; i < 16; ++i) st16s(p, (unsigned)(i * 1024 + lane * 16), u32x4{v, v, v, v});
    } else if (b == 89 && lane == 0) st4_sc1(tmo, 0u);
    WAIT_V(0);
  } else {
    const int pc = (c & 7) * 2 + (wave & 1), g = lane >> 4, hf = (lane >> 2) & 1, row = ((lane >> 3) & 1) * 4 + (lane & 3);
    u32x4 v = *(const u32x4*)(mem0 + row * DM + 32 * (2 * pc + hf) + 8 * g);
    v.x = nosent(v.x); v.y = nosent(v.y); v.z = nosent(v.z); v.w = nosent(v.w);
    st16_sc1(xch_base(ws) + ((size_t)3 << 21) + (size_t)(((c >> 3) & 1) * 2 + (pc >> 3)) * 16384 + (c >> 4) * 8192 + (pc & 7) * 1024 + lane * 16, v);
    WAIT_V(0);
  }
  BAR();
  if (tid == 0) st8_sc1(ws + OFF_INIT + c * 128, u32x2{(unsigned)tag << 3 | sg[0] | sg[1] | sg[2], (unsigned)(tag >> 29)});
  if (wave == 0) STS(ws, 128 + (c >> 3) * 32 + 24 + (c & 7), 2);
  {
    f32x4 v;
    for (int spins = 0; spins < SPIN_CAP; ++spins) {
      u32x4 m[4];
      asm volatile("global_load_dwordx4 %0, %4, %6 sc1\n\tglobal_load_dwordx4 %1, %4, %6 offset:16 sc1\n\t"
                   "global_load_dwordx4 %2, %5, %6 sc1\n\tglobal_load_dwordx4 %3, %5, %6 offset:16 sc1\n\ts_waitcnt vmcnt(0)"
                   : "=&v"(m[0]), "=&v"(m[1]), "=&v"(m[2]), "=&v"(m[3]) : "v"((unsigned)tid * 32), "v"((unsigned)tid * 32 + 16384), "s"(mod) : "memory");
      bool ok = true;
#pragma unroll
      for (int i = 0; i < 4; ++i) ok = ok && m[i].y == (unsigned)tag && m[i].w == tag2(tag);
      v = f32x4{__uint_as_float(m[0].x) + __uint_as_float(m[2].x), __uint_as_float(m[0].z) + __uint_as_float(m[2].z),
                __uint_as_float(m[1].x) + __uint_as_float(m[3].x), __uint_as_float(m[1].z) + __uint_as_float(m[3].z)};
      if (__all(ok)) break;
      __builtin_amdgcn_s_sleep(4);
    }
    v = f32x4{bfr(v[0]), bfr(v[1]), bfr(v[2]), bfr(v[3])};
    const int a = tid >> 8, cc = tid & 255;
    if (a) v = f32x4{bfr(1.0f + v[0]), bfr(1.0f + v[1]), bfr(1.0f + v[2]), bfr(1.0f + v[3])};
    *(f32x4*)(smem + 4096 + a * 4096 + ((cc >> 7) * 2 + (cc & 1)) * 1024 + ((cc >> 1) & 63) * 16) = v;
  }
  BAR();
  if (wave == 0) STS(ws, 128 + (c >> 3) * 32 + 24 + (c & 7), 3);
#pragma unroll
  for (int p = 0; p < 8; ++p) {
    f32x2 v[2][4];
#pragma unroll
    for (int i = 0; i < 2; ++i)
#pragma unroll
      for (int e = 0; e < 4; ++e) v[i][e] = f32x2{bflo(xr[p][i][e]), bfhi(xr[p][i][e])};
    const f32x2 s2 = ((v[0][0] + v[0][1]) + (v[0][2] + v[0][3])) + ((v[1][0] + v[1][1]) + (v[1][2] + v[1][3]));
    const float mean = wavesum(s2[0] + s2[1]) * (1.0f / DM);
    f32x2 q2 = f32x2{0, 0};
#pragma unroll
    for (int i = 0; i < 8; ++i) { const f32x2 d = v[i >> 2][i & 3] - f32x2{mean, mean}; q2 = pkfma(d, d, q2); }
    const float q = wavesum(q2[0] + q2[1]);
    if (p == 0 && wave == 0) STS((char*)tmo - OFF_TMO, 128 + (c >> 3) * 32 + 24 + (c & 7), 5);
    const float mb = bfr(mean), vb = bfr(q * (1.0f / DM));
    const float rden = 1.0f / bfr(sqrtf(bfr(vb + 1e-5f)));
    const int row = 256 * p + 8 * c + wave, rb = row & 31;
    char* dst = h1c + (size_t)(row >> 5) * 65536 + (lane >> 3) * 4096 + rb * 128 + (((lane & 7) ^ (rb & 7)) << 4);
    const char* ml = smem + lane * 16;
#pragma unroll
    for (int i = 0; i < 2; ++i) st16_sc1(dst + i * 32768, norm8(v[i], ml + i * 2048, mb, rden));
    if (p == 0) cnt = cnt_of(ws, init_wait(ws, tag));
    if (p == 1) continue;
    if (p == 0 || p == 7) WAIT_V(0); else WAIT_V(2);
    __builtin_amdgcn_s_barrier();
    if (tid == 0) st4_sc1(cnt + (PL1 + c) * 32, (unsigned)(p == 0 || p == 7 ? p + 1 : p));
    if (p == 0) {
#pragma unroll
      for (int pp = 1; pp < 8; ++pp) { xr[pp][0] = ldnt<2>(src + (size_t)pp * 256 * DM); xr[pp][1] = ldnt<2>(src + (size_t)pp * 256 * DM + 512); }
    }
    if ((p == 0 || p == 7) && wave == 0) STS(ws, 128 + (c >> 3) * 32 + 24 + (c & 7), p ? 7 : 6);
  }
}
__device__ __forceinline__ void mod2_job(char* smem, int wave_id_, int c, const float* __restrict__ mod, const u16* __restrict__ cvec, const u16* __restrict__ wmod, unsigned* cnt) {
  const int tid = opaque_tid();
  {
    const float sva = bf2f(cvec[tid]), svb = bf2f(cvec[tid + 512]);
    float* sv = (float*)(smem + 12288);
    sv[tid] = bfr(sva / (1.0f + __expf(-sva))); sv[tid + 512] = bfr(svb / (1.0f + __expf(-svb)));
  }
  __syncthreads();
  {
    const int cg = tid & 15, r0 = tid >> 4;
    const u16* wp = wmod + (size_t)r0 * (6 * DM) + 2 * DM + 128 * c + cg * 8;
    const float* sv = (const float*)(smem + 12288) + r0;
    f32x2 acc[4] = {f32x2{0, 0}, f32x2{0, 0}, f32x2{0, 0}, f32x2{0, 0}};
#pragma unroll 1
    for (int o = 0; o < 4; ++o) {
      u32x4 w[8];
#pragma unroll
      for (int i = 0; i < 8; ++i) w[i] = ldnt<1>(wp + (size_t)(o * 8 + i) * 32 * (6 * DM));
#pragma unroll
      for (int i = 0; i < 8; ++i) {
        const float s = sv[(o * 8 + i) * 32];
#pragma unroll
        for (int e = 0; e < 4; ++e) acc[e] = pkfma(f32x2{s, s}, f32x2{bflo(w[i][e]), bfhi(w[i][e])}, acc[e]);
      }
    }
    float* red = (float*)(smem + 16384);
    *(f32x4*)(red + r0 * 128 + cg * 8) = f32x4{acc[0][0], acc[0][1], acc[1][0], acc[1][1]};
    *(f32x4*)(red + r0 * 128 + cg * 8 + 4) = f32x4{acc[2][0], acc[2][1], acc[3][0], acc[3][1]};
    __syncthreads();
    if (tid < 128) {
      float s = 0;
#pragma unroll
      for (int r = 0; r < 32; ++r) s += red[r * 128 + tid];
      st4_sc1((char*)mod + OFF_MOD2 + (128 * c + tid) * 4, __builtin_bit_cast(unsigned, s));
    }
    WAIT_V(0);
    __builtin_amdgcn_s_barrier();
    if (tid == 0) st4_sc1(cnt + (PL1 + c) * 32 + 1, 1u);
  }
}
enum { R_QKV = 0, R_PART = 1, R_F1 = 2, R_PART2 = 3 };
struct Dep {
  const char* base; unsigned voff; unsigned mul, add;
};
__device__ __forceinline__ unsigned dep_eval(unsigned v, const Dep& d) {
  v = rowmin16(v * d.mul + (d.add + 64u));
  return min(min(rdl(v, 0), rdl(v, 16)), min(rdl(v, 32), rdl(v, 48))) - 64u;
}
typedef unsigned u32x32 __attribute__((ext_vector_type(32)));
typedef unsigned u32x16 __attribute__((ext_vector_type(16)));
__device__ __forceinline__ void fast_ld0_2(const u32x32 (&W)[4], u32x16& pd, unsigned ea, unsigned da, int& b, int& lf, int cp, unsigned sa, unsigned mu, unsigned ad,
    unsigned rw, unsigned ab, const char* p1, const char* p0, unsigned l1, unsigned l0, const unsigned (&av)[8]) {
  asm volatile("s_setprio 0\n\t"
    "s_waitcnt vmcnt(4)\n\t"
    "s_barrier\n\t"
    "ds_read_b128 v[96:99], %[ea] offset:0\n\t"
    "ds_read_b128 v[100:103], %[ea] offset:2048\n\t"
    "ds_read_b128 v[104:107], %[da] offset:0\n\t"
    "ds_read_b128 v[108:111], %[da] offset:2048\n\t"
    "ds_read_b128 v[112:115], %[ea] offset:4096\n\t"
    "ds_read_b128 v[116:119], %[ea] offset:6144\n\t"
    "s_branch 2f\n\t"
    "1:\n\t"
    "s_waitcnt vmcnt(4)\n\t"
    "s_nop 1\n\t"
    "s_barrier\n\t"
    "2:\n\t"
    "s_waitcnt lgkmcnt(4)\n\t"
    "v_mfma_f32_16x16x32_bf16 v[64:67], v[128:131], v[96:99], 0\n\t"
    "ds_read_b128 v[120:123], %[da] offset:4096\n\t"
    "s_add_u32 m0, %[l1], 0\n\t"
    "ds_write_b128 %[rw], v[80:83] offset:0\n\t"
    "v_mfma_f32_16x16x32_bf16 v[68:71], v[132:135], v[96:99], 0\n\t"
    "ds_read_b128 v[124:127], %[da] offset:6144\n\t"
    "global_load_lds_dwordx4 %[av0], s[36:37] sc1\n\t"
    "ds_write_b128 %[rw], v[84:87] offset:1024\n\t"
    "v_mfma_f32_16x16x32_bf16 v[72:75], v[128:131], v[100:103], 0\n\t"
    "s_add_u32 m0, %[l1], 32768\n\t"
    "ds_write_b128 %[rw], v[88:91] offset:2048\n\t"
    "v_mfma_f32_16x16x32_bf16 v[76:79], v[132:135], v[100:103], 0\n\t"
    "global_load_lds_dwordx4 %[av1], s[36:37] sc1\n\t"
    "ds_write_b128 %[rw], v[92:95] offset:3072\n\t"
    "s_waitcnt lgkmcnt(8)\n\t"
    "v_mfma_f32_16x16x32_bf16 v[64:67], v[136:139], v[104:107], v[64:67]\n\t"
    "ds_read_b128 v[96:99], %[ea] offset:8192\n\t"
    "s_add_u32 m0, %[l1], 4096\n\t"
    "v_mfma_f32_16x16x32_bf16 v[68:71], v[140:143], v[104:107], v[68:71]\n\t"
    "ds_read_b128 v[100:103], %[ea] offset:10240\n\t"
    "global_load_lds_dwordx4 %[av2], s[36:37] sc1\n\t"
    "v_mfma_f32_16x16x32_bf16 v[72:75], v[136:139], v[108:111], v[72:75]\n\t"
    "s_add_u32 m0, %[l1], 36864\n\t"
    "v_mfma_f32_16x16x32_bf16 v[76:79], v[140:143], v[108:111], v[76:79]\n\t"
    "global_load_lds_dwordx4 %[av3], s[36:37] sc1\n\t"
    "s_waitcnt lgkmcnt(8)\n\t"
    "v_mfma_f32_16x16x32_bf16 v[64:67], v[144:147], v[112:115], v[64:67]\n\t"
    "ds_read_b128 v[104:107], %[da] offset:8192\n\t"
    "s_add_u32 m0, %[l1], 8192\n\t"
    "v_mfma_f32_16x16x32_bf16 v[68:71], v[148:151], v[112:115], v[68:71]\n\t"
    "ds_read_b128 v[108:111], %[da] offset:10240\n\t"
    "global_load_lds_dwordx4 %[av4], s[36:37] sc1\n\t"
    "v_mfma_f32_16x16x32_bf16 v[72:75], v[144:147], v[116:119], v[72:75]\n\t"
    "s_add_u32 m0, %[l1], 40960\n\t"
    "v_mfma_f32_16x16x32_bf16 v[76:79], v[148:151], v[116:119], v[76:79]\n\t"
    "global_load_lds_dwordx4 %[av5], s[36:37] sc1\n\t"
    "s_waitcnt lgkmcnt(7)\n\t"
    "v_mfma_f32_16x16x32_bf16 v[64:67], v[152:155], v[120:123], v[64:67]\n\t"
    "ds_read_b128 v[112:115], %[ea] offset:12288\n\t"
    "s_add_u32 m0, %[l1], 12288\n\t"
    "v_mfma_f32_16x16x32_bf16 v[68:71], v[156:159], v[120:123], v[68:71]\n\t"
    "ds_read_b128 v[116:119], %[ea] offset:14336\n\t"
    "global_load_lds_dwordx4 %[av6], s[36:37] sc1\n\t"
    "v_mfma_f32_16x16x32_bf16 v[72:75], v[152:155], v[124:127], v[72:75]\n\t"
    "s_add_u32 m0, %[l1], 45056\n\t"
    "v_mfma_f32_16x16x32_bf16 v[76:79], v[156:159], v[124:127], v[76:79]\n\t"
    "global_load_lds_dwordx4 %[av7], s[36:37] sc1\n\t"
    "s_waitcnt lgkmcnt(4)\n\t"
    "v_mfma_f32_16x16x32_bf16 v[64:67], v[160:163], v[96:99], v[64:67]\n\t"
    "ds_read_b128 v[120:123], %[da] offset:12288\n\t"
    "v_mfma_f32_16x16x32_bf16 v[68:71], v[164:167], v[96:99], v[68:71]\n\t"
    "ds_read_b128 v[124:127], %[da] offset:14336\n\t"
    "v_mfma_f32_16x16x32_bf16 v[72:75], v[160:163], v[100:103], v[72:75]\n\t"
    "v_mfma_f32_16x16x32_bf16 v[76:79], v[164:167], v[100:103], v[76:79]\n\t"
    "s_waitcnt lgkmcnt(4)\n\t"
    "v_mfma_f32_16x16x32_bf16 v[64:67], v[168:171], v[104:107], v[64:67]\n\t"
    "ds_read_b128 v[96:99], %[ea] offset:16384\n\t"
    "v_mfma_f32_16x16x32_bf16 v[68:71], v[172:175], v[104:107], v[68:71]\n\t"
    "ds_read_b128 v[100:103], %[ea] offset:18432\n\t"
    "v_mfma_f32_16x16x32_bf16 v[72:75], v[168:171], v[108:111], v[72:75]\n\t"
    "v_mfma_f32_16x16x32_bf16 v[76:79], v[172:175], v[108:111], v[76:79]\n\t"
    "s_waitcnt lgkmcnt(4)\n\t"
    "v_mfma_f32_16x16x32_bf16 v[64:67], v[176:179], v[112:115], v[64:67]\n\t"
    "ds_read_b128 v[104:107], %[da] offset:16384\n\t"
    "v_mfma_f32_16x16x32_bf16 v[68:71], v[180:183], v[112:115], v[68:71]\n\t"
    "ds_read_b128 v[108:111], %[da] offset:18432\n\t"
    "v_mfma_f32_16x16x32_bf16 v[72:75], v[176:179], v[116:119], v[72:75]\n\t"
    "v_mfma_f32_16x16x32_bf16 v[76:79], v[180:183], v[116:119], v[76:79]\n\t"
    "s_waitcnt lgkmcnt(4)\n\t"
    "v_mfma_f32_16x16x32_bf16 v[64:67], v[184:187], v[120:123], v[64:67]\n\t"
    "ds_read_b128 v[112:115], %[ea] offset:20480\n\t"
    "v_mfma_f32_16x16x32_bf16 v[68:71], v[188:191], v[120:123], v[68:71]\n\t"
    "ds_read_b128 v[116:119], %[ea] offset:22528\n\t"
    "v_mfma_f32_16x16x32_bf16 v[72:75], v[184:187], v[124:127], v[72:75]\n\t"
    "v_mfma_f32_16x16x32_bf16 v[76:79], v[188:191], v[124:127], v[76:79]\n\t"
    "s_waitcnt vmcnt(4)\n\t"
    "s_barrier\n\t"
    "s_waitcnt lgkmcnt(4)\n\t"
    "v_mfma_f32_16x16x32_bf16 v[64:67], v[192:195], v[96:99], v[64:67]\n\t"
    "ds_read_b128 v[120:123], %[da] offset:20480\n\t"
    "s_add_u32 m0, %[l0], 0\n\t"
    "v_mfma_f32_16x16x32_bf16 v[68:71], v[196:199], v[96:99], v[68:71]\n\t"
    "ds_read_b128 v[124:127], %[da] offset:22528\n\t"
    "global_load_lds_dwordx4 %[av0], s[38:39] sc1\n\t"
    "v_mfma_f32_16x16x32_bf16 v[72:75], v[192:195], v[100:103], v[72:75]\n\t"
    "s_add_u32 m0, %[l0], 32768\n\t"
    "v_mfma_f32_16x16x32_bf16 v[76:79], v[196:199], v[100:103], v[76:79]\n\t"
    "global_load_lds_dwordx4 %[av1], s[38:39] sc1\n\t"
    "s_waitcnt lgkmcnt(4)\n\t"
    "v_mfma_f32_16x16x32_bf16 v[64:67], v[200:203], v[104:107], v[64:67]\n\t"
    "ds_read_b128 v[96:99], %[ea] offset:24576\n\t"
    "s_add_u32 m0, %[l0], 4096\n\t"
    "v_mfma_f32_16x16x32_bf16 v[68:71], v[204:207], v[104:107], v[68:71]\n\t"
    "ds_read_b128 v[100:103], %[ea] offset:26624\n\t"
    "global_load_lds_dwordx4 %[av2], s[38:39] sc1\n\t"
    "v_mfma_f32_16x16x32_bf16 v[72:75], v[200:203], v[108:111], v[72:75]\n\t"
    "s_add_u32 m0, %[l0], 36864\n\t"
    "v_mfma_f32_16x16x32_bf16 v[76:79], v[204:207], v[108:111], v[76:79]\n\t"
    "global_load_lds_dwordx4 %[av3], s[38:39] sc1\n\t"
    "s_add_u32 s36, s36, %[ab]\n\t"
    "s_addc_u32 s37, s37, 0\n\t"
    "s_waitcnt lgkmcnt(4)\n\t"
    "v_mfma_f32_16x16x32_bf16 v[64:67], v[208:211], v[112:115], v[64:67]\n\t"
    "ds_read_b128 v[104:107], %[da] offset:24576\n\t"
    "s_add_u32 m0, %[l0], 8192\n\t"
    "v_mfma_f32_16x16x32_bf16 v[68:71], v[212:215], v[112:115], v[68:71]\n\t"
    "ds_read_b128 v[108:111], %[da] offset:26624\n\t"
    "global_load_lds_dwordx4 %[av4], s[38:39] sc1\n\t"
    "v_mfma_f32_16x16x32_bf16 v[72:75], v[208:211], v[116:119], v[72:75]\n\t"
    "ds_read_b32 v39, %[sa]\n\t"
    "s_add_u32 m0, %[l0], 40960\n\t"
    "v_mfma_f32_16x16x32_bf16 v[76:79], v[212:215], v[116:119], v[76:79]\n\t"
    "global_load_lds_dwordx4 %[av5], s[38:39] sc1\n\t"
    "s_xor_b32 %[l1], %[l1], 0x10000\n\t"
    "s_waitcnt lgkmcnt(5)\n\t"
    "v_mfma_f32_16x16x32_bf16 v[64:67], v[216:219], v[120:123], v[64:67]\n\t"
    "ds_read_b128 v[112:115], %[ea] offset:28672\n\t"
    "s_add_u32 m0, %[l0], 12288\n\t"
    "v_mfma_f32_16x16x32_bf16 v[68:71], v[220:223], v[120:123], v[68:71]\n\t"
    "ds_read_b128 v[116:119], %[ea] offset:30720\n\t"
    "global_load_lds_dwordx4 %[av6], s[38:39] sc1\n\t"
    "v_mfma_f32_16x16x32_bf16 v[72:75], v[216:219], v[124:127], v[72:75]\n\t"
    "v_xor_b32 %[ea], 0x10000, %[ea]\n\t"
    "s_add_u32 m0, %[l0], 45056\n\t"
    "v_mfma_f32_16x16x32_bf16 v[76:79], v[220:223], v[124:127], v[76:79]\n\t"
    "global_load_lds_dwordx4 %[av7], s[38:39] sc1\n\t"
    "s_waitcnt lgkmcnt(5)\n\t"
    "v_mfma_f32_16x16x32_bf16 v[64:67], v[224:227], v[96:99], v[64:67]\n\t"
    "ds_read_b128 v[120:123], %[da] offset:28672\n\t"
    "v_mfma_f32_16x16x32_bf16 v[68:71], v[228:231], v[96:99], v[68:71]\n\t"
    "ds_read_b128 v[124:127], %[da] offset:30720\n\t"
    "v_mfma_f32_16x16x32_bf16 v[72:75], v[224:227], v[100:103], v[72:75]\n\t"
    "v_xor_b32 %[da], 0x10000, %[da]\n\t"
    "v_mfma_f32_16x16x32_bf16 v[76:79], v[228:231], v[100:103], v[76:79]\n\t"
    "s_waitcnt lgkmcnt(5)\n\t"
    "v_mfma_f32_16x16x32_bf16 v[64:67], v[232:235], v[104:107], v[64:67]\n\t"
    "ds_read_b128 v[96:99], %[ea] offset:0\n\t"
    "v_mfma_f32_16x16x32_bf16 v[68:71], v[236:239], v[104:107], v[68:71]\n\t"
    "ds_read_b128 v[100:103], %[ea] offset:2048\n\t"
    "v_mfma_f32_16x16x32_bf16 v[72:75], v[232:235], v[108:111], v[72:75]\n\t"
    "v_mfma_f32_16x16x32_bf16 v[76:79], v[236:239], v[108:111], v[76:79]\n\t"
    "s_waitcnt lgkmcnt(4)\n\t"
    "v_mfma_f32_16x16x32_bf16 v[64:67], v[240:243], v[112:115], v[64:67]\n\t"
    "ds_read_b128 v[104:107], %[da] offset:0\n\t"
    "v_mad_u32_u24 v39, v39, %[mu], %[ad]\n\t"
    "v_mfma_f32_16x16x32_bf16 v[68:71], v[244:247], v[112:115], v[68:71]\n\t"
    "ds_read_b128 v[108:111], %[da] offset:2048\n\t"
    "v_cmp_ge_i32 vcc, %[lf], v39\n\t"
    "s_cmp_eq_u64 vcc, 0\n\t"
    "s_addc_u32 %[lf], %[lf], 0\n\t"
    "v_mfma_f32_16x16x32_bf16 v[72:75], v[240:243], v[116:119], v[72:75]\n\t"
    "v_mfma_f32_16x16x32_bf16 v[76:79], v[244:247], v[116:119], v[76:79]\n\t"
    "v_cmp_ge_i32 vcc, %[lf], v39\n\t"
    "s_cmp_eq_u64 vcc, 0\n\t"
    "s_addc_u32 %[lf], %[lf], 0\n\t"
    "s_waitcnt lgkmcnt(4)\n\t"
    "v_mfma_f32_16x16x32_bf16 v[80:83], v[248:251], v[120:123], v[64:67]\n\t"
    "ds_read_b128 v[112:115], %[ea] offset:4096\n\t"
    "s_min_i32 %[lf], %[lf], %[cp]\n\t"
    "s_add_u32 %[b], %[b], 1\n\t"
    "v_mfma_f32_16x16x32_bf16 v[84:87], v[252:255], v[120:123], v[68:71]\n\t"
    "ds_read_b128 v[116:119], %[ea] offset:6144\n\t"
    "v_mfma_f32_16x16x32_bf16 v[88:91], v[248:251], v[124:127], v[72:75]\n\t"
    "v_mfma_f32_16x16x32_bf16 v[92:95], v[252:255], v[124:127], v[76:79]\n\t"
    "s_add_u32 s38, s38, %[ab]\n\t"
    "s_addc_u32 s39, s39, 0\n\t"
    "s_xor_b32 %[l0], %[l0], 0x10000\n\t"
    "s_cmp_lt_i32 %[b], %[lf]\n\t"
    "s_cbranch_scc1 1b\n\t"
    "s_waitcnt lgkmcnt(0)\n\t"
    "s_nop 4"
    : "=&{v[80:95]}"(pd), [ea] "+&v"(ea), [da] "+&v"(da), [b] "+&s"(b), [lf] "+&s"(lf), "+{s[36:37]}"(p1), "+{s[38:39]}"(p0), [sa] "+&v"(sa), [l1] "+&s"(l1), [l0] "+&s"(l0)
    : [rw] "v"(rw), [ab] "s"(ab), [cp] "s"(cp), [mu] "s"(mu), [ad] "v"(ad), "{v[128:159]}"(W[0]), "{v[160:191]}"(W[1]), "{v[192:223]}"(W[2]), "{v[224:255]}"(W[3]), [av0] "v"(av[0]), [av1] "v"(av[1]), [av2] "v"(av[2]), [av3] "v"(av[3]), [av4] "v"(av[4]), [av5] "v"(av[5]), [av6] "v"(av[6]), [av7] "v"(av[7])
    : "v39", "v64", "v65", "v66", "v67", "v68", "v69", "v70", "v71", "v72", "v73", "v74", "v75", "v76", "v77", "v78", "v79", "v96", "v97", "v98", "v99", "v100", "v101", "v102", "v103", "v104", "v105", "v106", "v107", "v108", "v109", "v110", "v111", "v112", "v113", "v114", "v115", "v116", "v117", "v118", "v119", "v120", "v121", "v122", "v123", "v124", "v125", "v126", "v127", "vcc", "scc", "m0", "memory");
}
__device__ __forceinline__ void fast_ld2_2(const u32x32 (&W)[4], u32x16& pd, unsigned ea, unsigned da, int& b, int& lf, int cp, unsigned sa, unsigned mu, unsigned ad,
    unsigned rw, unsigned ab, const char* p1, const char* p0, unsigned l1, unsigned l0, const unsigned (&av)[8]) {
  asm volatile("s_setprio 0\n\t"
    "s_waitcnt vmcnt(2)\n\t"
    "s_barrier\n\t"
    "ds_read_b128 v[96:99], %[ea] offset:0\n\t"
    "ds_read_b128 v[100:103], %[ea] offset:2048\n\t"
    "ds_read_b128 v[104:107], %[da] offset:0\n\t"
    "ds_read_b128 v[108:111], %[da] offset:2048\n\t"
    "ds_read_b128 v[112:115], %[ea] offset:4096\n\t"
    "ds_read_b128 v[116:119], %[ea] offset:6144\n\t"
    "s_branch 2f\n\t"
    "1:\n\t"
    "s_waitcnt vmcnt(2)\n\t"
    "s_nop 1\n\t"
    "s_barrier\n\t"
    "2:\n\t"
    "s_waitcnt lgkmcnt(4)\n\t"
    "v_mfma_f32_16x16x32_bf16 v[64:67], v[128:131], v[96:99], 0\n\t"
    "ds_read_b128 v[120:123], %[da] offset:4096\n\t"
    "s_add_u32 m0, %[l1], 0\n\t"
    "ds_write_b128 %[rw], v[80:83] offset:0\n\t"
    "v_mfma_f32_16x16x32_bf16 v[68:71], v[132:135], v[96:99], 0\n\t"
    "ds_read_b128 v[124:127], %[da] offset:6144\n\t"
    "global_load_lds_dwordx4 %[av0], s[36:37] sc1\n\t"
    "ds_write_b128 %[rw], v[84:87] offset:1024\n\t"
    "v_mfma_f32_16x16x32_bf16 v[72:75], v[128:131], v[100:103], 0\n\t"
    "ds_write_b128 %[rw], v[88:91] offset:2048\n\t"
    "v_mfma_f32_16x16x32_bf16 v[76:79], v[132:135], v[100:103], 0\n\t"
    "ds_write_b128 %[rw], v[92:95] offset:3072\n\t"
    "s_waitcnt lgkmcnt(8)\n\t"
    "v_mfma_f32_16x16x32_bf16 v[64:67], v[136:139], v[104:107], v[64:67]\n\t"
    "ds_read_b128 v[96:99], %[ea] offset:8192\n\t"
    "s_add_u32 m0, %[l1], 4096\n\t"
    "v_mfma_f32_16x16x32_bf16 v[68:71], v[140:143], v[104:107], v[68:71]\n\t"
    "ds_read_b128 v[100:103], %[ea] offset:10240\n\t"
    "global_load_lds_dwordx4 %[av2], s[36:37] sc1\n\t"
    "v_mfma_f32_16x16x32_bf16 v[72:75], v[136:139], v[108:111], v[72:75]\n\t"
    "v_mfma_f32_16x16x32_bf16 v[76:79], v[140:143], v[108:111], v[76:79]\n\t"
    "s_waitcnt lgkmcnt(8)\n\t"
    "v_mfma_f32_16x16x32_bf16 v[64:67], v[144:147], v[112:115], v[64:67]\n\t"
    "ds_read_b128 v[104:107], %[da] offset:8192\n\t"
    "s_add_u32 m0, %[l1], 8192\n\t"
    "v_mfma_f32_16x16x32_bf16 v[68:71], v[148:151], v[112:115], v[68:71]\n\t"
    "ds_read_b128 v[108:111], %[da] offset:10240\n\t"
    "global_load_lds_dwordx4 %[av4], s[36:37] sc1\n\t"
    "v_mfma_f32_16x16x32_bf16 v[72:75], v[144:147], v[116:119], v[72:75]\n\t"
    "v_mfma_f32_16x16x32_bf16 v[76:79], v[148:151], v[116:119], v[76:79]\n\t"
    "s_waitcnt lgkmcnt(7)\n\t"
    "v_mfma_f32_16x16x32_bf16 v[64:67], v[152:155], v[120:123], v[64:67]\n\t"
    "ds_read_b128 v[112:115], %[ea] offset:12288\n\t"
    "s_add_u32 m0, %[l1], 12288\n\t"
    "v_mfma_f32_16x16x32_bf16 v[68:71], v[156:159], v[120:123], v[68:71]\n\t"
    "ds_read_b128 v[116:119], %[ea] offset:14336\n\t"
    "global_load_lds_dwordx4 %[av6], s[36:37] sc1\n\t"
    "v_mfma_f32_16x16x32_bf16 v[72:75], v[152:155], v[124:127], v[72:75]\n\t"
    "v_mfma_f32_16x16x32_bf16 v[76:79], v[156:159], v[124:127], v[76:79]\n\t"
    "s_waitcnt lgkmcnt(4)\n\t"
    "v_mfma_f32_16x16x32_bf16 v[64:67], v[160:163], v[96:99], v[64:67]\n\t"
    "ds_read_b128 v[120:123], %[da] offset:12288\n\t"
    "v_mfma_f32_16x16x32_bf16 v[68:71], v[164:167], v[96:99], v[68:71]\n\t"
    "ds_read_b128 v[124:127], %[da] offset:14336\n\t"
    "v_mfma_f32_16x16x32_bf16 v[72:75], v[160:163], v[100:103], v[72:75]\n\t"
    "v_mfma_f32_16x16x32_bf16 v[76:79], v[164:167], v[100:103], v[76:79]\n\t"
    "s_waitcnt lgkmcnt(4)\n\t"
    "v_mfma_f32_16x16x32_bf16 v[64:67], v[168:171], v[104:107], v[64:67]\n\t"
    "ds_read_b128 v[96:99], %[ea] offset:16384\n\t"
    "v_mfma_f32_16x16x32_bf16 v[68:71], v[172:175], v[104:107], v[68:71]\n\t"
    "ds_read_b128 v[100:103], %[ea] offset:18432\n\t"
    "v_mfma_f32_16x16x32_bf16 v[72:75], v[168:171], v[108:111], v[72:75]\n\t"
    "v_mfma_f32_16x16x32_bf16 v[76:79], v[172:175], v[108:111], v[76:79]\n\t"
    "s_waitcnt lgkmcnt(4)\n\t"
    "v_mfma_f32_16x16x32_bf16 v[64:67], v[176:179], v[112:115], v[64:67]\n\t"
    "ds_read_b128 v[104:107], %[da] offset:16384\n\t"
    "v_mfma_f32_16x16x32_bf16 v[68:71], v[180:183], v[112:115], v[68:71]\n\t"
    "ds_read_b128 v[108:111], %[da] offset:18432\n\t"
    "v_mfma_f32_16x16x32_bf16 v[72:75], v[176:179], v[116:119], v[72:75]\n\t"
    "v_mfma_f32_16x16x32_bf16 v[76:79], v[180:183], v[116:119], v[76:79]\n\t"
    "s_waitcnt lgkmcnt(4)\n\t"
    "v_mfma_f32_16x16x32_bf16 v[64:67], v[184:187], v[120:123], v[64:67]\n\t"
    "ds_read_b128 v[112:115], %[ea] offset:20480\n\t"
    "v_mfma_f32_16x16x32_bf16 v[68:71], v[188:191], v[120:123], v[68:71]\n\t"
    "ds_read_b128 v[116:119], %[ea] offset:22528\n\t"
    "v_mfma_f32_16x16x32_bf16 v[72:75], v[184:187], v[124:127], v[72:75]\n\t"
    "v_mfma_f32_16x16x32_bf16 v[76:79], v[188:191], v[124:127], v[76:79]\n\t"
    "s_waitcnt vmcnt(2)\n\t"
    "s_barrier\n\t"
    "s_waitcnt lgkmcnt(4)\n\t"
    "v_mfma_f32_16x16x32_bf16 v[64:67], v[192:195], v[96:99], v[64:67]\n\t"
    "ds_read_b128 v[120:123], %[da] offset:20480\n\t"
    "s_add_u32 m0, %[l0], 0\n\t"
    "v_mfma_f32_16x16x32_bf16 v[68:71], v[196:199], v[96:99], v[68:71]\n\t"
    "ds_read_b128 v[124:127], %[da] offset:22528\n\t"
    "global_load_lds_dwordx4 %[av0], s[38:39] sc1\n\t"
    "v_mfma_f32_16x16x32_bf16 v[72:75], v[192:195], v[100:103], v[72:75]\n\t"
    "v_mfma_f32_16x16x32_bf16 v[76:79], v[196:199], v[100:103], v[76:79]\n\t"
    "s_waitcnt lgkmcnt(4)\n\t"
    "v_mfma_f32_16x16x32_bf16 v[64:67], v[200:203], v[104:107], v[64:67]\n\t"
    "ds_read_b128 v[96:99], %[ea] offset:24576\n\t"
    "s_add_u32 m0, %[l0], 4096\n\t"
    "v_mfma_f32_16x16x32_bf16 v[68:71], v[204:207], v[104:107], v[68:71]\n\t"
    "ds_read_b128 v[100:103], %[ea] offset:26624\n\t"
    "global_load_lds_dwordx4 %[av2], s[38:39] sc1\n\t"
    "v_mfma_f32_16x16x32_bf16 v[72:75], v[200:203], v[108:111], v[72:75]\n\t"
    "v_mfma_f32_16x16x32_bf16 v[76:79], v[204:207], v[108:111], v[76:79]\n\t"
    "s_add_u32 s36, s36, %[ab]\n\t"
    "s_addc_u32 s37, s37, 0\n\t"
    "s_waitcnt lgkmcnt(4)\n\t"
    "v_mfma_f32_16x16x32_bf16 v[64:67], v[208:211], v[112:115], v[64:67]\n\t"
    "ds_read_b128 v[104:107], %[da] offset:24576\n\t"
    "s_add_u32 m0, %[l0], 8192\n\t"
    "v_mfma_f32_16x16x32_bf16 v[68:71], v[212:215], v[112:115], v[68:71]\n\t"
    "ds_read_b128 v[108:111], %[da] offset:26624\n\t"
    "global_load_lds_dwordx4 %[av4], s[38:39] sc1\n\t"
    "v_mfma_f32_16x16x32_bf16 v[72:75], v[208:211], v[116:119], v[72:75]\n\t"
    "ds_read_b32 v39, %[sa]\n\t"
    "v_mfma_f32_16x16x32_bf16 v[76:79], v[212:215], v[116:119], v[76:79]\n\t"
    "s_xor_b32 %[l1], %[l1], 0x10000\n\t"
    "s_waitcnt lgkmcnt(5)\n\t"
    "v_mfma_f32_16x16x32_bf16 v[64:67], v[216:219], v[120:123], v[64:67]\n\t"
    "ds_read_b128 v[112:115], %[ea] offset:28672\n\t"
    "s_add_u32 m0, %[l0], 12288\n\t"
    "v_mfma_f32_16x16x32_bf16 v[68:71], v[220:223], v[120:123], v[68:71]\n\t"
    "ds_read_b128 v[116:119], %[ea] offset:30720\n\t"
    "global_load_lds_dwordx4 %[av6], s[38:39] sc1\n\t"
    "v_mfma_f32_16x16x32_bf16 v[72:75], v[216:219], v[124:127], v[72:75]\n\t"
    "v_xor_b32 %[ea], 0x10000, %[ea]\n\t"
    "v_mfma_f32_16x16x32_bf16 v[76:79], v[220:223], v[124:127], v[76:79]\n\t"
    "s_waitcnt lgkmcnt(5)\n\t"
    "v_mfma_f32_16x16x32_bf16 v[64:67], v[224:227], v[96:99], v[64:67]\n\t"
    "ds_read_b128 v[120:123], %[da] offset:28672\n\t"
    "v_mfma_f32_16x16x32_bf16 v[68:71], v[228:231], v[96:99], v[68:71]\n\t"
    "ds_read_b128 v[124:127], %[da] offset:30720\n\t"
    "v_mfma_f32_16x16x32_bf16 v[72:75], v[224:227], v[100:103], v[72:75]\n\t"
    "v_xor_b32 %[da], 0x10000, %[da]\n\t"
    "v_mfma_f32_16x16x32_bf16 v[76:79], v[228:231], v[100:103], v[76:79]\n\t"
    "s_waitcnt lgkmcnt(5)\n\t"
    "v_mfma_f32_16x16x32_bf16 v[64:67], v[232:235], v[104:107], v[64:67]\n\t"
    "ds_read_b128 v[96:99], %[ea] offset:0\n\t"
    "v_mfma_f32_16x16x32_bf16 v[68:71], v[236:239], v[104:107], v[68:71]\n\t"
    "ds_read_b128 v[100:103], %[ea] offset:2048\n\t"
    "v_mfma_f32_16x16x32_bf16 v[72:75], v[232:235], v[108:111], v[72:75]\n\t"
    "v_mfma_f32_16x16x32_bf16 v[76:79], v[236:239], v[108:111], v[76:79]\n\t"
    "s_waitcnt lgkmcnt(4)\n\t"
    "v_mfma_f32_16x16x32_bf16 v[64:67], v[240:243], v[112:115], v[64:67]\n\t"
    "ds_read_b128 v[104:107], %[da] offset:0\n\t"
    "v_mad_u32_u24 v39, v39, %[mu], %[ad]\n\t"
    "v_mfma_f32_16x16x32_bf16 v[68:71], v[244:247], v[112:115], v[68:71]\n\t"
    "ds_read_b128 v[108:111], %[da] offset:2048\n\t"
    "v_cmp_ge_i32 vcc, %[lf], v39\n\t"
    "s_cmp_eq_u64 vcc, 0\n\t"
    "s_addc_u32 %[lf], %[lf], 0\n\t"
    "v_mfma_f32_16x16x32_bf16 v[72:75], v[240:243], v[116:119], v[72:75]\n\t"
    "v_mfma_f32_16x16x32_bf16 v[76:79], v[244:247], v[116:119], v[76:79]\n\t"
    "v_cmp_ge_i32 vcc, %[lf], v39\n\t"
    "s_cmp_eq_u64 vcc, 0\n\t"
    "s_addc_u32 %[lf], %[lf], 0\n\t"
    "s_waitcnt lgkmcnt(4)\n\t"
    "v_mfma_f32_16x16x32_bf16 v[80:83], v[248:251], v[120:123], v[64:67]\n\t"
    "ds_read_b128 v[112:115], %[ea] offset:4096\n\t"
    "s_min_i32 %[lf], %[lf], %[cp]\n\t"
    "s_add_u32 %[b], %[b], 1\n\t"
    "v_mfma_f32_16x16x32_bf16 v[84:87], v[252:255], v[120:123], v[68:71]\n\t"
    "ds_read_b128 v[116:119], %[ea] offset:6144\n\t"
    "v_mfma_f32_16x16x32_bf16 v[88:91], v[248:251], v[124:127], v[72:75]\n\t"
    "v_mfma_f32_16x16x32_bf16 v[92:95], v[252:255], v[124:127], v[76:79]\n\t"
    "s_add_u32 s38, s38, %[ab]\n\t"
    "s_addc_u32 s39, s39, 0\n\t"
    "s_xor_b32 %[l0], %[l0], 0x10000\n\t"
    "s_cmp_lt_i32 %[b], %[lf]\n\t"
    "s_cbranch_scc1 1b\n\t"
    "s_waitcnt lgkmcnt(0)\n\t"
    "s_nop 4"
    : "=&{v[80:95]}"(pd), [ea] "+&v"(ea), [da] "+&v"(da), [b] "+&s"(b), [lf] "+&s"(lf), "+{s[36:37]}"(p1), "+{s[38:39]}"(p0), [sa] "+&v"(sa), [l1] "+&s"(l1), [l0] "+&s"(l0)
    : [rw] "v"(rw), [ab] "s"(ab), [cp] "s"(cp), [mu] "s"(mu), [ad] "v"(ad), "{v[128:159]}"(W[0]), "{v[160:191]}"(W[1]), "{v[192:223]}"(W[2]), "{v[224:255]}"(W[3]), [av0] "v"(av[0]), [av1] "v"(av[1]), [av2] "v"(av[2]), [av3] "v"(av[3]), [av4] "v"(av[4]), [av5] "v"(av[5]), [av6] "v"(av[6]), [av7] "v"(av[7])
    : "v39", "v64", "v65", "v66", "v67", "v68", "v69", "v70", "v71", "v72", "v73", "v74", "v75", "v76", "v77", "v78", "v79", "v96", "v97", "v98", "v99", "v100", "v101", "v102", "v103", "v104", "v105", "v106", "v107", "v108", "v109", "v110", "v111", "v112", "v113", "v114", "v115", "v116", "v117", "v118", "v119", "v120", "v121", "v122", "v123", "v124", "v125", "v126", "v127", "vcc", "scc", "m0", "memory");
}
__device__ __forceinline__ void fast_ld0_3(const u32x32 (&W)[4], u32x16& pd, unsigned ea, unsigned da, int& b, int& lf, int cp, unsigned sa, unsigned mu, unsigned ad,
    unsigned rw, unsigned ab, const char* p1, const char* p0, unsigned l1, unsigned l0, const unsigned (&av)[8]) {
  asm volatile("s_setprio 0\n\t"
    "s_waitcnt vmcnt(12)\n\t"
    "s_barrier\n\t"
    "ds_read_b128 v[96:99], %[ea] offset:0\n\t"
    "ds_read_b128 v[100:103], %[ea] offset:2048\n\t"
    "ds_read_b128 v[104:107], %[da] offset:0\n\t"
    "ds_read_b128 v[108:111], %[da] offset:2048\n\t"
    "ds_read_b128 v[112:115], %[ea] offset:4096\n\t"
    "ds_read_b128 v[116:119], %[ea] offset:6144\n\t"
    "s_branch 2f\n\t"
    "1:\n\t"
    "s_waitcnt vmcnt(12)\n\t"
    "s_nop 1\n\t"
    "s_barrier\n\t"
    "2:\n\t"
    "s_waitcnt lgkmcnt(4)\n\t"
    "v_mfma_f32_16x16x32_bf16 v[64:67], v[128:131], v[96:99], 0\n\t"
    "ds_read_b128 v[120:123], %[da] offset:4096\n\t"
    "s_add_u32 m0, %[l1], 0\n\t"
    "ds_write_b128 %[rw], v[80:83] offset:0\n\t"
    "v_mfma_f32_16x16x32_bf16 v[68:71], v[132:135], v[96:99], 0\n\t"
    "ds_read_b128 v[124:127], %[da] offset:6144\n\t"
    "global_load_lds_dwordx4 %[av0], s[36:37] sc1\n\t"
    "ds_write_b128 %[rw], v[84:87] offset:1024\n\t"
    "v_mfma_f32_16x16x32_bf16 v[72:75], v[128:131], v[100:103], 0\n\t"
    "s_add_u32 m0, %[l1], 32768\n\t"
    "ds_write_b128 %[rw], v[88:91] offset:2048\n\t"
    "v_mfma_f32_16x16x32_bf16 v[76:79], v[132:135], v[100:103], 0\n\t"
    "global_load_lds_dwordx4 %[av1], s[36:37] sc1\n\t"
    "ds_write_b128 %[rw], v[92:95] offset:3072\n\t"
    "s_waitcnt lgkmcnt(8)\n\t"
    "v_mfma_f32_16x16x32_bf16 v[64:67], v[136:139], v[104:107], v[64:67]\n\t"
    "ds_read_b128 v[96:99], %[ea] offset:8192\n\t"
    "s_add_u32 m0, %[l1], 4096\n\t"
    "v_mfma_f32_16x16x32_bf16 v[68:71], v[140:143], v[104:107], v[68:71]\n\t"
    "ds_read_b128 v[100:103], %[ea] offset:10240\n\t"
    "global_load_lds_dwordx4 %[av2], s[36:37] sc1\n\t"
    "v_mfma_f32_16x16x32_bf16 v[72:75], v[136:139], v[108:111], v[72:75]\n\t"
    "s_add_u32 m0, %[l1], 36864\n\t"
    "v_mfma_f32_16x16x32_bf16 v[76:79], v[140:143], v[108:111], v[76:79]\n\t"
    "global_load_lds_dwordx4 %[av3], s[36:37] sc1\n\t"
    "s_waitcnt lgkmcnt(8)\n\t"
    "v_mfma_f32_16x16x32_bf16 v[64:67], v[144:147], v[112:115], v[64:67]\n\t"
    "ds_read_b128 v[104:107], %[da] offset:8192\n\t"
    "s_add_u32 m0, %[l1], 8192\n\t"
    "v_mfma_f32_16x16x32_bf16 v[68:71], v[148:151], v[112:115], v[68:71]\n\t"
    "ds_read_b128 v[108:111], %[da] offset:10240\n\t"
    "global_load_lds_dwordx4 %[av4], s[36:37] sc1\n\t"
    "v_mfma_f32_16x16x32_bf16 v[72:75], v[144:147], v[116:119], v[72:75]\n\t"
    "s_add_u32 m0, %[l1], 40960\n\t"
    "v_mfma_f32_16x16x32_bf16 v[76:79], v[148:151], v[116:119], v[76:79]\n\t"
    "global_load_lds_dwordx4 %[av5], s[36:37] sc1\n\t"
    "s_waitcnt lgkmcnt(7)\n\t"
    "v_mfma_f32_16x16x32_bf16 v[64:67], v[152:155], v[120:123], v[64:67]\n\t"
    "ds_read_b128 v[112:115], %[ea] offset:12288\n\t"
    "s_add_u32 m0, %[l1], 12288\n\t"
    "v_mfma_f32_16x16x32_bf16 v[68:71], v[156:159], v[120:123], v[68:71]\n\t"
    "ds_read_b128 v[116:119], %[ea] offset:14336\n\t"
    "global_load_lds_dwordx4 %[av6], s[36:37] sc1\n\t"
    "v_mfma_f32_16x16x32_bf16 v[72:75], v[152:155], v[124:127], v[72:75]\n\t"
    "s_add_u32 m0, %[l1], 45056\n\t"
    "v_mfma_f32_16x16x32_bf16 v[76:79], v[156:159], v[124:127], v[76:79]\n\t"
    "global_load_lds_dwordx4 %[av7], s[36:37] sc1\n\t"
    "s_waitcnt lgkmcnt(4)\n\t"
    "v_mfma_f32_16x16x32_bf16 v[64:67], v[160:163], v[96:99], v[64:67]\n\t"
    "ds_read_b128 v[120:123], %[da] offset:12288\n\t"
    "v_mfma_f32_16x16x32_bf16 v[68:71], v[164:167], v[96:99], v[68:71]\n\t"
    "ds_read_b128 v[124:127], %[da] offset:14336\n\t"
    "v_mfma_f32_16x16x32_bf16 v[72:75], v[160:163], v[100:103], v[72:75]\n\t"
    "v_mfma_f32_16x16x32_bf16 v[76:79], v[164:167], v[100:103], v[76:79]\n\t"
    "s_waitcnt lgkmcnt(4)\n\t"
    "v_mfma_f32_16x16x32_bf16 v[64:67], v[168:171], v[104:107], v[64:67]\n\t"
    "ds_read_b128 v[96:99], %[ea] offset:16384\n\t"
    "v_mfma_f32_16x16x32_bf16 v[68:71], v[172:175], v[104:107], v[68:71]\n\t"
    "ds_read_b128 v[100:103], %[ea] offset:18432\n\t"
    "v_mfma_f32_16x16x32_bf16 v[72:75], v[168:171], v[108:111], v[72:75]\n\t"
    "v_mfma_f32_16x16x32_bf16 v[76:79], v[172:175], v[108:111], v[76:79]\n\t"
    "s_waitcnt lgkmcnt(4)\n\t"
    "v_mfma_f32_16x16x32_bf16 v[64:67], v[176:179], v[112:115], v[64:67]\n\t"
    "ds_read_b128 v[104:107], %[da] offset:16384\n\t"
    "v_mfma_f32_16x16x32_bf16 v[68:71], v[180:183], v[112:115], v[68:71]\n\t"
    "ds_read_b128 v[108:111], %[da] offset:18432\n\t"
    "v_mfma_f32_16x16x32_bf16 v[72:75], v[176:179], v[116:119], v[72:75]\n\t"
    "v_mfma_f32_16x16x32_bf16 v[76:79], v[180:183], v[116:119], v[76:79]\n\t"
    "s_waitcnt lgkmcnt(4)\n\t"
    "v_mfma_f32_16x16x32_bf16 v[64:67], v[184:187], v[120:123], v[64:67]\n\t"
    "ds_read_b128 v[112:115], %[ea] offset:20480\n\t"
    "v_mfma_f32_16x16x32_bf16 v[68:71], v[188:191], v[120:123], v[68:71]\n\t"
    "ds_read_b128 v[116:119], %[ea] offset:22528\n\t"
    "v_mfma_f32_16x16x32_bf16 v[72:75], v[184:187], v[124:127], v[72:75]\n\t"
    "v_mfma_f32_16x16x32_bf16 v[76:79], v[188:191], v[124:127], v[76:79]\n\t"
    "s_waitcnt vmcnt(12)\n\t"
    "s_barrier\n\t"
    "s_waitcnt lgkmcnt(4)\n\t"
    "v_mfma_f32_16x16x32_bf16 v[64:67], v[192:195], v[96:99], v[64:67]\n\t"
    "ds_read_b128 v[120:123], %[da] offset:20480\n\t"
    "s_add_u32 m0, %[l0], 0\n\t"
    "v_mfma_f32_16x16x32_bf16 v[68:71], v[196:199], v[96:99], v[68:71]\n\t"
    "ds_read_b128 v[124:127], %[da] offset:22528\n\t"
    "global_load_lds_dwordx4 %[av0], s[38:39] sc1\n\t"
    "v_mfma_f32_16x16x32_bf16 v[72:75], v[192:195], v[100:103], v[72:75]\n\t"
    "s_add_u32 m0, %[l0], 32768\n\t"
    "v_mfma_f32_16x16x32_bf16 v[76:79], v[196:199], v[100:103], v[76:79]\n\t"
    "global_load_lds_dwordx4 %[av1], s[38:39] sc1\n\t"
    "s_waitcnt lgkmcnt(4)\n\t"
    "v_mfma_f32_16x16x32_bf16 v[64:67], v[200:203], v[104:107], v[64:67]\n\t"
    "ds_read_b128 v[96:99], %[ea] offset:24576\n\t"
    "s_add_u32 m0, %[l0], 4096\n\t"
    "v_mfma_f32_16x16x32_bf16 v[68:71], v[204:207], v[104:107], v[68:71]\n\t"
    "ds_read_b128 v[100:103], %[ea] offset:26624\n\t"
    "global_load_lds_dwordx4 %[av2], s[38:39] sc1\n\t"
    "v_mfma_f32_16x16x32_bf16 v[72:75], v[200:203], v[108:111], v[72:75]\n\t"
    "s_add_u32 m0, %[l0], 36864\n\t"
    "v_mfma_f32_16x16x32_bf16 v[76:79], v[204:207], v[108:111], v[76:79]\n\t"
    "global_load_lds_dwordx4 %[av3], s[38:39] sc1\n\t"
    "s_add_u32 s36, s36, %[ab]\n\t"
    "s_addc_u32 s37, s37, 0\n\t"
    "s_waitcnt lgkmcnt(4)\n\t"
    "v_mfma_f32_16x16x32_bf16 v[64:67], v[208:211], v[112:115], v[64:67]\n\t"
    "ds_read_b128 v[104:107], %[da] offset:24576\n\t"
    "s_add_u32 m0, %[l0], 8192\n\t"
    "v_mfma_f32_16x16x32_bf16 v[68:71], v[212:215], v[112:115], v[68:71]\n\t"
    "ds_read_b128 v[108:111], %[da] offset:26624\n\t"
    "global_load_lds_dwordx4 %[av4], s[38:39] sc1\n\t"
    "v_mfma_f32_16x16x32_bf16 v[72:75], v[208:211], v[116:119], v[72:75]\n\t"
    "ds_read_b32 v39, %[sa]\n\t"
    "s_add_u32 m0, %[l0], 40960\n\t"
    "v_mfma_f32_16x16x32_bf16 v[76:79], v[212:215], v[116:119], v[76:79]\n\t"
    "global_load_lds_dwordx4 %[av5], s[38:39] sc1\n\t"
    "s_xor_b32 %[l1], %[l1], 0x10000\n\t"
    "s_waitcnt lgkmcnt(5)\n\t"
    "v_mfma_f32_16x16x32_bf16 v[64:67], v[216:219], v[120:123], v[64:67]\n\t"
    "ds_read_b128 v[112:115], %[ea] offset:28672\n\t"
    "s_add_u32 m0, %[l0], 12288\n\t"
    "v_mfma_f32_16x16x32_bf16 v[68:71], v[220:223], v[120:123], v[68:71]\n\t"
    "ds_read_b128 v[116:119], %[ea] offset:30720\n\t"
    "global_load_lds_dwordx4 %[av6], s[38:39] sc1\n\t"
    "v_mfma_f32_16x16x32_bf16 v[72:75], v[216:219], v[124:127], v[72:75]\n\t"
    "v_xor_b32 %[ea], 0x10000, %[ea]\n\t"
    "s_add_u32 m0, %[l0], 45056\n\t"
    "v_mfma_f32_16x16x32_bf16 v[76:79], v[220:223], v[124:127], v[76:79]\n\t"
    "global_load_lds_dwordx4 %[av7], s[38:39] sc1\n\t"
    "s_waitcnt lgkmcnt(5)\n\t"
    "v_mfma_f32_16x16x32_bf16 v[64:67], v[224:227], v[96:99], v[64:67]\n\t"
    "ds_read_b128 v[120:123], %[da] offset:28672\n\t"
    "v_mfma_f32_16x16x32_bf16 v[68:71], v[228:231], v[96:99], v[68:71]\n\t"
    "ds_read_b128 v[124:127], %[da] offset:30720\n\t"
    "v_mfma_f32_16x16x32_bf16 v[72:75], v[224:227], v[100:103], v[72:75]\n\t"
    "v_xor_b32 %[da], 0x10000, %[da]\n\t"
    "v_mfma_f32_16x16x32_bf16 v[76:79], v[228:231], v[100:103], v[76:79]\n\t"
    "s_waitcnt lgkmcnt(5)\n\t"
    "v_mfma_f32_16x16x32_bf16 v[64:67], v[232:235], v[104:107], v[64:67]\n\t"
    "ds_read_b128 v[96:99], %[ea] offset:0\n\t"
    "v_mfma_f32_16x16x32_bf16 v[68:71], v[236:239], v[104:107], v[68:71]\n\t"
    "ds_read_b128 v[100:103], %[ea] offset:2048\n\t"
    "v_mfma_f32_16x16x32_bf16 v[72:75], v[232:235], v[108:111], v[72:75]\n\t"
    "v_mfma_f32_16x16x32_bf16 v[76:79], v[236:239], v[108:111], v[76:79]\n\t"
    "s_waitcnt lgkmcnt(4)\n\t"
    "v_mfma_f32_16x16x32_bf16 v[64:67], v[240:243], v[112:115], v[64:67]\n\t"
    "ds_read_b128 v[104:107], %[da] offset:0\n\t"
    "v_mad_u32_u24 v39, v39, %[mu], %[ad]\n\t"
    "v_mfma_f32_16x16x32_bf16 v[68:71], v[244:247], v[112:115], v[68:71]\n\t"
    "ds_read_b128 v[108:111], %[da] offset:2048\n\t"
    "v_cmp_ge_i32 vcc, %[lf], v39\n\t"
    "s_cmp_eq_u64 vcc, 0\n\t"
    "s_addc_u32 %[lf], %[lf], 0\n\t"
    "v_mfma_f32_16x16x32_bf16 v[72:75], v[240:243], v[116:119], v[72:75]\n\t"
    "v_mfma_f32_16x16x32_bf16 v[76:79], v[244:247], v[116:119], v[76:79]\n\t"
    "v_cmp_ge_i32 vcc, %[lf], v39\n\t"
    "s_cmp_eq_u64 vcc, 0\n\t"
    "s_addc_u32 %[lf], %[lf], 0\n\t"
    "s_waitcnt lgkmcnt(4)\n\t"
    "v_mfma_f32_16x16x32_bf16 v[80:83], v[248:251], v[120:123], v[64:67]\n\t"
    "ds_read_b128 v[112:115], %[ea] offset:4096\n\t"
    "s_min_i32 %[lf], %[lf], %[cp]\n\t"
    "s_add_u32 %[b], %[b], 1\n\t"
    "v_mfma_f32_16x16x32_bf16 v[84:87], v[252:255], v[120:123], v[68:71]\n\t"
    "ds_read_b128 v[116:119], %[ea] offset:6144\n\t"
    "v_mfma_f32_16x16x32_bf16 v[88:91], v[248:251], v[124:127], v[72:75]\n\t"
    "v_mfma_f32_16x16x32_bf16 v[92:95], v[252:255], v[124:127], v[76:79]\n\t"
    "s_add_u32 s38, s38, %[ab]\n\t"
    "s_addc_u32 s39, s39, 0\n\t"
    "s_xor_b32 %[l0], %[l0], 0x10000\n\t"
    "s_cmp_lt_i32 %[b], %[lf]\n\t"
    "s_cbranch_scc1 1b\n\t"
    "s_waitcnt lgkmcnt(0)\n\t"
    "s_nop 4"
    : "=&{v[80:95]}"(pd), [ea] "+&v"(ea), [da] "+&v"(da), [b] "+&s"(b), [lf] "+&s"(lf), "+{s[36:37]}"(p1), "+{s[38:39]}"(p0), [sa] "+&v"(sa), [l1] "+&s"(l1), [l0] "+&s"(l0)
    : [rw] "v"(rw), [ab] "s"(ab), [cp] "s"(cp), [mu] "s"(mu), [ad] "v"(ad), "{v[128:159]}"(W[0]), "{v[160:191]}"(W[1]), "{v[192:223]}"(W[2]), "{v[224:255]}"(W[3]), [av0] "v"(av[0]), [av1] "v"(av[1]), [av2] "v"(av[2]), [av3] "v"(av[3]), [av4] "v"(av[4]), [av5] "v"(av[5]), [av6] "v"(av[6]), [av7] "v"(av[7])
    : "v39", "v64", "v65", "v66", "v67", "v68", "v69", "v70", "v71", "v72", "v73", "v74", "v75", "v76", "v77", "v78", "v79", "v96", "v97", "v98", "v99", "v100", "v101", "v102", "v103", "v104", "v105", "v106", "v107", "v108", "v109", "v110", "v111", "v112", "v113", "v114", "v115", "v116", "v117", "v118", "v119", "v120", "v121", "v122", "v123", "v124", "v125", "v126", "v127", "vcc", "scc", "m0", "memory");
}
__device__ __forceinline__ void fast_ld2_3(const u32x32 (&W)[4], u32x16& pd, unsigned ea, unsigned da, int& b, int& lf, int cp, unsigned sa, unsigned mu, unsigned ad,
    unsigned rw, unsigned ab, const char* p1, const char* p0, unsigned l1, unsigned l0, const unsigned (&av)[8]) {
  asm volatile("s_setprio 0\n\t"
    "s_waitcnt vmcnt(6)\n\t"
    "s_barrier\n\t"
    "ds_read_b128 v[96:99], %[ea] offset:0\n\t"
    "ds_read_b128 v[100:103], %[ea] offset:2048\n\t"
    "ds_read_b128 v[104:107], %[da] offset:0\n\t"
    "ds_read_b128 v[108:111], %[da] offset:2048\n\t"
    "ds_read_b128 v[112:115], %[ea] offset:4096\n\t"
    "ds_read_b128 v[116:119], %[ea] offset:6144\n\t"
    "s_branch 2f\n\t"
    "1:\n\t"
    "s_waitcnt vmcnt(6)\n\t"
    "s_nop 1\n\t"
    "s_barrier\n\t"
    "2:\n\t"
    "s_waitcnt lgkmcnt(4)\n\t"
    "v_mfma_f32_16x16x32_bf16 v[64:67], v[128:131], v[96:99], 0\n\t"
    "ds_read_b128 v[120:123], %[da] offset:4096\n\t"
    "s_add_u32 m0, %[l1], 0\n\t"
    "ds_write_b128 %[rw], v[80:83] offset:0\n\t"
    "v_mfma_f32_16x16x32_bf16 v[68:71], v[132:135], v[96:99], 0\n\t"
    "ds_read_b128 v[124:127], %[da] offset:6144\n\t"
    "global_load_lds_dwordx4 %[av0], s[36:37] sc1\n\t"
    "ds_write_b128 %[rw], v[84:87] offset:1024\n\t"
    "v_mfma_f32_16x16x32_bf16 v[72:75], v[128:131], v[100:103], 0\n\t"
    "ds_write_b128 %[rw], v[88:91] offset:2048\n\t"
    "v_mfma_f32_16x16x32_bf16 v[76:79], v[132:135], v[100:103], 0\n\t"
    "ds_write_b128 %[rw], v[92:95] offset:3072\n\t"
    "s_waitcnt lgkmcnt(8)\n\t"
    "v_mfma_f32_16x16x32_bf16 v[64:67], v[136:139], v[104:107], v[64:67]\n\t"
    "ds_read_b128 v[96:99], %[ea] offset:8192\n\t"
    "s_add_u32 m0, %[l1], 4096\n\t"
    "v_mfma_f32_16x16x32_bf16 v[68:71], v[140:143], v[104:107], v[68:71]\n\t"
    "ds_read_b128 v[100:103], %[ea] offset:10240\n\t"
    "global_load_lds_dwordx4 %[av2], s[36:37] sc1\n\t"
    "v_mfma_f32_16x16x32_bf16 v[72:75], v[136:139], v[108:111], v[72:75]\n\t"
    "v_mfma_f32_16x16x32_bf16 v[76:79], v[140:143], v[108:111], v[76:79]\n\t"
    "s_waitcnt lgkmcnt(8)\n\t"
    "v_mfma_f32_16x16x32_bf16 v[64:67], v[144:147], v[112:115], v[64:67]\n\t"
    "ds_read_b128 v[104:107], %[da] offset:8192\n\t"
    "s_add_u32 m0, %[l1], 8192\n\t"
    "v_mfma_f32_16x16x32_bf16 v[68:71], v[148:151], v[112:115], v[68:71]\n\t"
    "ds_read_b128 v[108:111], %[da] offset:10240\n\t"
    "global_load_lds_dwordx4 %[av4], s[36:37] sc1\n\t"
    "v_mfma_f32_16x16x32_bf16 v[72:75], v[144:147], v[116:119], v[72:75]\n\t"
    "v_mfma_f32_16x16x32_bf16 v[76:79], v[148:151], v[116:119], v[76:79]\n\t"
    "s_waitcnt lgkmcnt(7)\n\t"
    "v_mfma_f32_16x16x32_bf16 v[64:67], v[152:155], v[120:123], v[64:67]\n\t"
    "ds_read_b128 v[112:115], %[ea] offset:12288\n\t"
    "s_add_u32 m0, %[l1], 12288\n\t"
    "v_mfma_f32_16x16x32_bf16 v[68:71], v[156:159], v[120:123], v[68:71]\n\t"
    "ds_read_b128 v[116:119], %[ea] offset:14336\n\t"
    "global_load_lds_dwordx4 %[av6], s[36:37] sc1\n\t"
    "v_mfma_f32_16x16x32_bf16 v[72:75], v[152:155], v[124:127], v[72:75]\n\t"
    "v_mfma_f32_16x16x32_bf16 v[76:79], v[156:159], v[124:127], v[76:79]\n\t"
    "s_waitcnt lgkmcnt(4)\n\t"
    "v_mfma_f32_16x16x32_bf16 v[64:67], v[160:163], v[96:99], v[64:67]\n\t"
    "ds_read_b128 v[120:123], %[da] offset:12288\n\t"
    "v_mfma_f32_16x16x32_bf16 v[68:71], v[164:167], v[96:99], v[68:71]\n\t"
    "ds_read_b128 v[124:127], %[da] offset:14336\n\t"
    "v_mfma_f32_16x16x32_bf16 v[72:75], v[160:163], v[100:103], v[72:75]\n\t"
    "v_mfma_f32_16x16x32_bf16 v[76:79], v[164:167], v[100:103], v[76:79]\n\t"
    "s_waitcnt lgkmcnt(4)\n\t"
    "v_mfma_f32_16x16x32_bf16 v[64:67], v[168:171], v[104:107], v[64:67]\n\t"
    "ds_read_b128 v[96:99], %[ea] offset:16384\n\t"
    "v_mfma_f32_16x16x32_bf16 v[68:71], v[172:175], v[104:107], v[68:71]\n\t"
    "ds_read_b128 v[100:103], %[ea] offset:18432\n\t"
    "v_mfma_f32_16x16x32_bf16 v[72:75], v[168:171], v[108:111], v[72:75]\n\t"
    "v_mfma_f32_16x16x32_bf16 v[76:79], v[172:175], v[108:111], v[76:79]\n\t"
    "s_waitcnt lgkmcnt(4)\n\t"
    "v_mfma_f32_16x16x32_bf16 v[64:67], v[176:179], v[112:115], v[64:67]\n\t"
    "ds_read_b128 v[104:107], %[da] offset:16384\n\t"
    "v_mfma_f32_16x16x32_bf16 v[68:71], v[180:183], v[112:115], v[68:71]\n\t"
    "ds_read_b128 v[108:111], %[da] offset:18432\n\t"
    "v_mfma_f32_16x16x32_bf16 v[72:75], v[176:179], v[116:119], v[72:75]\n\t"
    "v_mfma_f32_16x16x32_bf16 v[76:79], v[180:183], v[116:119], v[76:79]\n\t"
    "s_waitcnt lgkmcnt(4)\n\t"
    "v_mfma_f32_16x16x32_bf16 v[64:67], v[184:187], v[120:123], v[64:67]\n\t"
    "ds_read_b128 v[112:115], %[ea] offset:20480\n\t"
    "v_mfma_f32_16x16x32_bf16 v[68:71], v[188:191], v[120:123], v[68:71]\n\t"
    "ds_read_b128 v[116:119], %[ea] offset:22528\n\t"
    "v_mfma_f32_16x16x32_bf16 v[72:75], v[184:187], v[124:127], v[72:75]\n\t"
    "v_mfma_f32_16x16x32_bf16 v[76:79], v[188:191], v[124:127], v[76:79]\n\t"
    "s_waitcnt vmcnt(6)\n\t"
    "s_barrier\n\t"
    "s_waitcnt lgkmcnt(4)\n\t"
    "v_mfma_f32_16x16x32_bf16 v[64:67], v[192:195], v[96:99], v[64:67]\n\t"
    "ds_read_b128 v[120:123], %[da] offset:20480\n\t"
    "s_add_u32 m0, %[l0], 0\n\t"
    "v_mfma_f32_16x16x32_bf16 v[68:71], v[196:199], v[96:99], v[68:71]\n\t"
    "ds_read_b128 v[124:127], %[da] offset:22528\n\t"
    "global_load_lds_dwordx4 %[av0], s[38:39] sc1\n\t"
    "v_mfma_f32_16x16x32_bf16 v[72:75], v[192:195], v[100:103], v[72:75]\n\t"
    "v_mfma_f32_16x16x32_bf16 v[76:79], v[196:199], v[100:103], v[76:79]\n\t"
    "s_waitcnt lgkmcnt(4)\n\t"
    "v_mfma_f32_16x16x32_bf16 v[64:67], v[200:203], v[104:107], v[64:67]\n\t"
    "ds_read_b128 v[96:99], %[ea] offset:24576\n\t"
    "s_add_u32 m0, %[l0], 4096\n\t"
    "v_mfma_f32_16x16x32_bf16 v[68:71], v[204:207], v[104:107], v[68:71]\n\t"
    "ds_read_b128 v[100:103], %[ea] offset:26624\n\t"
    "global_load_lds_dwordx4 %[av2], s[38:39] sc1\n\t"
    "v_mfma_f32_16x16x32_bf16 v[72:75], v[200:203], v[108:111], v[72:75]\n\t"
    "v_mfma_f32_16x16x32_bf16 v[76:79], v[204:207], v[108:111], v[76:79]\n\t"
    "s_add_u32 s36, s36, %[ab]\n\t"
    "s_addc_u32 s37, s37, 0\n\t"
    "s_waitcnt lgkmcnt(4)\n\t"
    "v_mfma_f32_16x16x32_bf16 v[64:67], v[208:211], v[112:115], v[64:67]\n\t"
    "ds_read_b128 v[104:107], %[da] offset:24576\n\t"
    "s_add_u32 m0, %[l0], 8192\n\t"
    "v_mfma_f32_16x16x32_bf16 v[68:71], v[212:215], v[112:115], v[68:71]\n\t"
    "ds_read_b128 v[108:111], %[da] offset:26624\n\t"
    "global_load_lds_dwordx4 %[av4], s[38:39] sc1\n\t"
    "v_mfma_f32_16x16x32_bf16 v[72:75], v[208:211], v[116:119], v[72:75]\n\t"
    "ds_read_b32 v39, %[sa]\n\t"
    "v_mfma_f32_16x16x32_bf16 v[76:79], v[212:215], v[116:119], v[76:79]\n\t"
    "s_xor_b32 %[l1], %[l1], 0x10000\n\t"
    "s_waitcnt lgkmcnt(5)\n\t"
    "v_mfma_f32_16x16x32_bf16 v[64:67], v[216:219], v[120:123], v[64:67]\n\t"
    "ds_read_b128 v[112:115], %[ea] offset:28672\n\t"
    "s_add_u32 m0, %[l0], 12288\n\t"
    "v_mfma_f32_16x16x32_bf16 v[68:71], v[220:223], v[120:123], v[68:71]\n\t"
    "ds_read_b128 v[116:119], %[ea] offset:30720\n\t"
    "global_load_lds_dwordx4 %[av6], s[38:39] sc1\n\t"
    "v_mfma_f32_16x16x32_bf16 v[72:75], v[216:219], v[124:127], v[72:75]\n\t"
    "v_xor_b32 %[ea], 0x10000, %[ea]\n\t"
    "v_mfma_f32_16x16x32_bf16 v[76:79], v[220:223], v[124:127], v[76:79]\n\t"
    "s_waitcnt lgkmcnt(5)\n\t"
    "v_mfma_f32_16x16x32_bf16 v[64:67], v[224:227], v[96:99], v[64:67]\n\t"
    "ds_read_b128 v[120:123], %[da] offset:28672\n\t"
    "v_mfma_f32_16x16x32_bf16 v[68:71], v[228:231], v[96:99], v[68:71]\n\t"
    "ds_read_b128 v[124:127], %[da] offset:30720\n\t"
    "v_mfma_f32_16x16x32_bf16 v[72:75], v[224:227], v[100:103], v[72:75]\n\t"
    "v_xor_b32 %[da], 0x10000, %[da]\n\t"
    "v_mfma_f32_16x16x32_bf16 v[76:79], v[228:231], v[100:103], v[76:79]\n\t"
    "s_waitcnt lgkmcnt(5)\n\t"
    "v_mfma_f32_16x16x32_bf16 v[64:67], v[232:235], v[104:107], v[64:67]\n\t"
    "ds_read_b128 v[96:99], %[ea] offset:0\n\t"
    "v_mfma_f32_16x16x32_bf16 v[68:71], v[236:239], v[104:107], v[68:71]\n\t"
    "ds_read_b128 v[100:103], %[ea] offset:2048\n\t"
    "v_mfma_f32_16x16x32_bf16 v[72:75], v[232:235], v[108:111], v[72:75]\n\t"
    "v_mfma_f32_16x16x32_bf16 v[76:79], v[236:239], v[108:111], v[76:79]\n\t"
    "s_waitcnt lgkmcnt(4)\n\t"
    "v_mfma_f32_16x16x32_bf16 v[64:67], v[240:243], v[112:115], v[64:67]\n\t"
    "ds_read_b128 v[104:107], %[da] offset:0\n\t"
    "v_mad_u32_u24 v39, v39, %[mu], %[ad]\n\t"
    "v_mfma_f32_16x16x32_bf16 v[68:71], v[244:247], v[112:115], v[68:71]\n\t"
    "ds_read_b128 v[108:111], %[da] offset:2048\n\t"
    "v_cmp_ge_i32 vcc, %[lf], v39\n\t"
    "s_cmp_eq_u64 vcc, 0\n\t"
    "s_addc_u32 %[lf], %[lf], 0\n\t"
    "v_mfma_f32_16x16x32_bf16 v[72:75], v[240:243], v[116:119], v[72:75]\n\t"
    "v_mfma_f32_16x16x32_bf16 v[76:79], v[244:247], v[116:119], v[76:79]\n\t"
    "v_cmp_ge_i32 vcc, %[lf], v39\n\t"
    "s_cmp_eq_u64 vcc, 0\n\t"
    "s_addc_u32 %[lf], %[lf], 0\n\t"
    "s_waitcnt lgkmcnt(4)\n\t"
    "v_mfma_f32_16x16x32_bf16 v[80:83], v[248:251], v[120:123], v[64:67]\n\t"
    "ds_read_b128 v[112:115], %[ea] offset:4096\n\t"
    "s_min_i32 %[lf], %[lf], %[cp]\n\t"
    "s_add_u32 %[b], %[b], 1\n\t"
    "v_mfma_f32_16x16x32_bf16 v[84:87], v[252:255], v[120:123], v[68:71]\n\t"
    "ds_read_b128 v[116:119], %[ea] offset:6144\n\t"
    "v_mfma_f32_16x16x32_bf16 v[88:91], v[248:251], v[124:127], v[72:75]\n\t"
    "v_mfma_f32_16x16x32_bf16 v[92:95], v[252:255], v[124:127], v[76:79]\n\t"
    "s_add_u32 s38, s38, %[ab]\n\t"
    "s_addc_u32 s39, s39, 0\n\t"
    "s_xor_b32 %[l0], %[l0], 0x10000\n\t"
    "s_cmp_lt_i32 %[b], %[lf]\n\t"
    "s_cbranch_scc1 1b\n\t"
    "s_waitcnt lgkmcnt(0)\n\t"
    "s_nop 4"
    : "=&{v[80:95]}"(pd), [ea] "+&v"(ea), [da] "+&v"(da), [b] "+&s"(b), [lf] "+&s"(lf), "+{s[36:37]}"(p1), "+{s[38:39]}"(p0), [sa] "+&v"(sa), [l1] "+&s"(l1), [l0] "+&s"(l0)
    : [rw] "v"(rw), [ab] "s"(ab), [cp] "s"(cp), [mu] "s"(mu), [ad] "v"(ad), "{v[128:159]}"(W[0]), "{v[160:191]}"(W[1]), "{v[192:223]}"(W[2]), "{v[224:255]}"(W[3]), [av0] "v"(av[0]), [av1] "v"(av[1]), [av2] "v"(av[2]), [av3] "v"(av[3]), [av4] "v"(av[4]), [av5] "v"(av[5]), [av6] "v"(av[6]), [av7] "v"(av[7])
    : "v39", "v64", "v65", "v66", "v67", "v68", "v69", "v70", "v71", "v72", "v73", "v74", "v75", "v76", "v77", "v78", "v79", "v96", "v97", "v98", "v99", "v100", "v101", "v102", "v103", "v104", "v105", "v106", "v107", "v108", "v109", "v110", "v111", "v112", "v113", "v114", "v115", "v116", "v117", "v118", "v119", "v120", "v121", "v122", "v123", "v124", "v125", "v126", "v127", "vcc", "scc", "m0", "memory");
}
__device__ __forceinline__ void fast_st0(const u32x32 (&W)[4], u32x16& pd, unsigned ea, unsigned da, int& b, int& lf, int cp, unsigned sa, unsigned mu, unsigned ad,
    unsigned rw, unsigned ab, const char* p1, const char* p0, unsigned co0, unsigned co1, int po, int pf, unsigned w0, unsigned sl, unsigned dv, const char* db, const char* pg) {
  unsigned st;
  asm volatile("s_barrier\n\t"
    "ds_read_b128 v[96:99], %[ea] offset:0\n\t"
    "ds_read_b128 v[100:103], %[ea] offset:2048\n\t"
    "ds_read_b128 v[104:107], %[da] offset:0\n\t"
    "ds_read_b128 v[108:111], %[da] offset:2048\n\t"
    "ds_read_b128 v[112:115], %[ea] offset:4096\n\t"
    "ds_read_b128 v[116:119], %[ea] offset:6144\n\t"
    "s_branch 2f\n\t"
    "1:\n\t"
    "s_barrier\n\t"
    "2:\n\t"
    "s_waitcnt lgkmcnt(4)\n\t"
    "v_mfma_f32_16x16x32_bf16 v[64:67], v[128:131], v[96:99], 0\n\t"
    "ds_read_b128 v[120:123], %[da] offset:4096\n\t"
    "v_mfma_f32_16x16x32_bf16 v[68:71], v[132:135], v[96:99], 0\n\t"
    "ds_read_b128 v[124:127], %[da] offset:6144\n\t"
    "v_mfma_f32_16x16x32_bf16 v[72:75], v[128:131], v[100:103], 0\n\t"
    "v_mfma_f32_16x16x32_bf16 v[76:79], v[132:135], v[100:103], 0\n\t"
    "s_waitcnt lgkmcnt(4)\n\t"
    "v_mfma_f32_16x16x32_bf16 v[64:67], v[136:139], v[104:107], v[64:67]\n\t"
    "ds_read_b128 v[96:99], %[ea] offset:8192\n\t"
    "v_mfma_f32_16x16x32_bf16 v[68:71], v[140:143], v[104:107], v[68:71]\n\t"
    "ds_read_b128 v[100:103], %[ea] offset:10240\n\t"
    "v_mfma_f32_16x16x32_bf16 v[72:75], v[136:139], v[108:111], v[72:75]\n\t"
    "v_mfma_f32_16x16x32_bf16 v[76:79], v[140:143], v[108:111], v[76:79]\n\t"
    "s_cmp_eq_u32 %[w0], 1\n\t"
    "s_cbranch_scc1 7f\n\t"
    "8:\n\t"
    "s_waitcnt lgkmcnt(4)\n\t"
    "v_mfma_f32_16x16x32_bf16 v[64:67], v[144:147], v[112:115], v[64:67]\n\t"
    "ds_read_b128 v[104:107], %[da] offset:8192\n\t"
    "v_mfma_f32_16x16x32_bf16 v[68:71], v[148:151], v[112:115], v[68:71]\n\t"
    "ds_read_b128 v[108:111], %[da] offset:10240\n\t"
    "v_mfma_f32_16x16x32_bf16 v[72:75], v[144:147], v[116:119], v[72:75]\n\t"
    "v_mfma_f32_16x16x32_bf16 v[76:79], v[148:151], v[116:119], v[76:79]\n\t"
    "s_waitcnt lgkmcnt(4)\n\t"
    "v_mfma_f32_16x16x32_bf16 v[64:67], v[152:155], v[120:123], v[64:67]\n\t"
    "ds_read_b128 v[112:115], %[ea] offset:12288\n\t"
    "v_mfma_f32_16x16x32_bf16 v[68:71], v[156:159], v[120:123], v[68:71]\n\t"
    "ds_read_b128 v[116:119], %[ea] offset:14336\n\t"
    "v_mfma_f32_16x16x32_bf16 v[72:75], v[152:155], v[124:127], v[72:75]\n\t"
    "v_mfma_f32_16x16x32_bf16 v[76:79], v[156:159], v[124:127], v[76:79]\n\t"
    "s_waitcnt lgkmcnt(4)\n\t"
    "v_mfma_f32_16x16x32_bf16 v[64:67], v[160:163], v[96:99], v[64:67]\n\t"
    "ds_read_b128 v[120:123], %[da] offset:12288\n\t"
    "v_mfma_f32_16x16x32_bf16 v[68:71], v[164:167], v[96:99], v[68:71]\n\t"
    "ds_read_b128 v[124:127], %[da] offset:14336\n\t"
    "v_mfma_f32_16x16x32_bf16 v[72:75], v[160:163], v[100:103], v[72:75]\n\t"
    "v_mfma_f32_16x16x32_bf16 v[76:79], v[164:167], v[100:103], v[76:79]\n\t"
    "s_waitcnt lgkmcnt(4)\n\t"
    "v_mfma_f32_16x16x32_bf16 v[64:67], v[168:171], v[104:107], v[64:67]\n\t"
    "ds_read_b128 v[96:99], %[ea] offset:16384\n\t"
    "v_mfma_f32_16x16x32_bf16 v[68:71], v[172:175], v[104:107], v[68:71]\n\t"
    "ds_read_b128 v[100:103], %[ea] offset:18432\n\t"
    "v_mfma_f32_16x16x32_bf16 v[72:75], v[168:171], v[108:111], v[72:75]\n\t"
    "v_mfma_f32_16x16x32_bf16 v[76:79], v[172:175], v[108:111], v[76:79]\n\t"
    "s_waitcnt lgkmcnt(4)\n\t"
    "v_mfma_f32_16x16x32_bf16 v[64:67], v[176:179], v[112:115], v[64:67]\n\t"
    "ds_read_b128 v[104:107], %[da] offset:16384\n\t"
    "v_mfma_f32_16x16x32_bf16 v[68:71], v[180:183], v[112:115], v[68:71]\n\t"
    "ds_read_b128 v[108:111], %[da] offset:18432\n\t"
    "v_mfma_f32_16x16x32_bf16 v[72:75], v[176:179], v[116:119], v[72:75]\n\t"
    "v_mfma_f32_16x16x32_bf16 v[76:79], v[180:183], v[116:119], v[76:79]\n\t"
    "s_waitcnt lgkmcnt(4)\n\t"
    "v_mfma_f32_16x16x32_bf16 v[64:67], v[184:187], v[120:123], v[64:67]\n\t"
    "ds_read_b128 v[112:115], %[ea] offset:20480\n\t"
    "v_mfma_f32_16x16x32_bf16 v[68:71], v[188:191], v[120:123], v[68:71]\n\t"
    "ds_read_b128 v[116:119], %[ea] offset:22528\n\t"
    "v_mfma_f32_16x16x32_bf16 v[72:75], v[184:187], v[124:127], v[72:75]\n\t"
    "v_mfma_f32_16x16x32_bf16 v[76:79], v[188:191], v[124:127], v[76:79]\n\t"
    "s_waitcnt vmcnt(0)\n\t"
    "s_barrier\n\t"
    "s_waitcnt lgkmcnt(4)\n\t"
    "v_mfma_f32_16x16x32_bf16 v[64:67], v[192:195], v[96:99], v[64:67]\n\t"
    "ds_read_b128 v[120:123], %[da] offset:20480\n\t"
    "v_mfma_f32_16x16x32_bf16 v[68:71], v[196:199], v[96:99], v[68:71]\n\t"
    "ds_read_b128 v[124:127], %[da] offset:22528\n\t"
    "v_mfma_f32_16x16x32_bf16 v[72:75], v[192:195], v[100:103], v[72:75]\n\t"
    "ds_read_b128 v[48:51], %[rw] offset:0\n\t"
    "v_mfma_f32_16x16x32_bf16 v[76:79], v[196:199], v[100:103], v[76:79]\n\t"
    "ds_read_b128 v[52:55], %[rw] offset:1024\n\t"
    "s_cmp_eq_u32 %[w0], 0\n\t"
    "s_cbranch_scc1 5f\n\t"
    "6:\n\t"
    "s_waitcnt lgkmcnt(6)\n\t"
    "v_mfma_f32_16x16x32_bf16 v[64:67], v[200:203], v[104:107], v[64:67]\n\t"
    "ds_read_b128 v[96:99], %[ea] offset:24576\n\t"
    "v_mfma_f32_16x16x32_bf16 v[68:71], v[204:207], v[104:107], v[68:71]\n\t"
    "ds_read_b128 v[100:103], %[ea] offset:26624\n\t"
    "v_mfma_f32_16x16x32_bf16 v[72:75], v[200:203], v[108:111], v[72:75]\n\t"
    "ds_read_b128 v[56:59], %[rw] offset:2048\n\t"
    "v_mfma_f32_16x16x32_bf16 v[76:79], v[204:207], v[108:111], v[76:79]\n\t"
    "ds_read_b128 v[60:63], %[rw] offset:3072\n\t"
    "s_waitcnt lgkmcnt(0)\n\t"
    "v_mfma_f32_16x16x32_bf16 v[64:67], v[208:211], v[112:115], v[64:67]\n\t"
    "ds_read_b128 v[104:107], %[da] offset:24576\n\t"
    "v_add_f32 v48, v48, v80\n\t"
    "v_add_f32 v49, v49, v81\n\t"
    "v_mfma_f32_16x16x32_bf16 v[68:71], v[212:215], v[112:115], v[68:71]\n\t"
    "ds_read_b128 v[108:111], %[da] offset:26624\n\t"
    "v_add_f32 v50, v50, v82\n\t"
    "v_mfma_f32_16x16x32_bf16 v[72:75], v[208:211], v[116:119], v[72:75]\n\t"
    "ds_read_b32 v39, %[sa]\n\t"
    "v_add_f32 v51, v51, v83\n\t"
    "v_mfma_f32_16x16x32_bf16 v[76:79], v[212:215], v[116:119], v[76:79]\n\t"
    "v_add_f32 v52, v52, v84\n\t"
    "v_add_f32 v53, v53, v85\n\t"
    "v_mfma_f32_16x16x32_bf16 v[64:67], v[216:219], v[120:123], v[64:67]\n\t"
    "ds_read_b128 v[112:115], %[ea] offset:28672\n\t"
    "v_add_f32 v54, v54, v86\n\t"
    "v_mfma_f32_16x16x32_bf16 v[68:71], v[220:223], v[120:123], v[68:71]\n\t"
    "ds_read_b128 v[116:119], %[ea] offset:30720\n\t"
    "v_add_f32 v55, v55, v87\n\t"
    "v_mfma_f32_16x16x32_bf16 v[72:75], v[216:219], v[124:127], v[72:75]\n\t"
    "v_xor_b32 %[ea], 0x10000, %[ea]\n\t"
    "v_add_f32 v56, v56, v88\n\t"
    "v_add_f32 v57, v57, v89\n\t"
    "v_mfma_f32_16x16x32_bf16 v[76:79], v[220:223], v[124:127], v[76:79]\n\t"
    "v_add_f32 v58, v58, v90\n\t"
    "v_mfma_f32_16x16x32_bf16 v[64:67], v[224:227], v[96:99], v[64:67]\n\t"
    "ds_read_b128 v[120:123], %[da] offset:28672\n\t"
    "v_add_f32 v59, v59, v91\n\t"
    "v_mfma_f32_16x16x32_bf16 v[68:71], v[228:231], v[96:99], v[68:71]\n\t"
    "ds_read_b128 v[124:127], %[da] offset:30720\n\t"
    "v_add_f32 v60, v60, v92\n\t"
    "v_mfma_f32_16x16x32_bf16 v[72:75], v[224:227], v[100:103], v[72:75]\n\t"
    "v_xor_b32 %[da], 0x10000, %[da]\n\t"
    "v_add_f32 v61, v61, v93\n\t"
    "v_add_f32 v62, v62, v94\n\t"
    "v_mfma_f32_16x16x32_bf16 v[76:79], v[228:231], v[100:103], v[76:79]\n\t"
    "v_add_f32 v63, v63, v95\n\t"
    "s_waitcnt lgkmcnt(5)\n\t"
    "v_mfma_f32_16x16x32_bf16 v[64:67], v[232:235], v[104:107], v[64:67]\n\t"
    "ds_read_b128 v[96:99], %[ea] offset:0\n\t"
    "v_cvt_pk_bf16_f32 v40, v48, v49\n\t"
    "v_mfma_f32_16x16x32_bf16 v[68:71], v[236:239], v[104:107], v[68:71]\n\t"
    "ds_read_b128 v[100:103], %[ea] offset:2048\n\t"
    "v_cvt_pk_bf16_f32 v41, v50, v51\n\t"
    "v_cvt_pk_bf16_f32 v42, v52, v53\n\t"
    "v_mfma_f32_16x16x32_bf16 v[72:75], v[232:235], v[108:111], v[72:75]\n\t"
    "v_cvt_pk_bf16_f32 v43, v54, v55\n\t"
    "v_mfma_f32_16x16x32_bf16 v[76:79], v[236:239], v[108:111], v[76:79]\n\t"
    "global_store_dwordx4 %[co0], v[40:43], s[36:37] sc1\n\t"
    "s_waitcnt lgkmcnt(4)\n\t"
    "v_mfma_f32_16x16x32_bf16 v[64:67], v[240:243], v[112:115], v[64:67]\n\t"
    "ds_read_b128 v[104:107], %[da] offset:0\n\t"
    "v_mad_u32_u24 v39, v39, %[mu], %[ad]\n\t"
    "v_cvt_pk_bf16_f32 v44, v56, v57\n\t"
    "v_cvt_pk_bf16_f32 v45, v58, v59\n\t"
    "v_mfma_f32_16x16x32_bf16 v[68:71], v[244:247], v[112:115], v[68:71]\n\t"
    "ds_read_b128 v[108:111], %[da] offset:2048\n\t"
    "v_cmp_ge_i32 vcc, %[lf], v39\n\t"
    "s_cmp_eq_u64 vcc, 0\n\t"
    "s_addc_u32 %[lf], %[lf], 0\n\t"
    "v_cvt_pk_bf16_f32 v46, v60, v61\n\t"
    "v_mfma_f32_16x16x32_bf16 v[72:75], v[240:243], v[116:119], v[72:75]\n\t"
    "v_cvt_pk_bf16_f32 v47, v62, v63\n\t"
    "v_mfma_f32_16x16x32_bf16 v[76:79], v[244:247], v[116:119], v[76:79]\n\t"
    "v_cmp_ge_i32 vcc, %[lf], v39\n\t"
    "s_cmp_eq_u64 vcc, 0\n\t"
    "s_addc_u32 %[lf], %[lf], 0\n\t"
    "global_store_dwordx4 %[co1], v[44:47], s[36:37] sc1\n\t"
    "s_waitcnt lgkmcnt(4)\n\t"
    "v_mfma_f32_16x16x32_bf16 v[80:83], v[248:251], v[120:123], v[64:67]\n\t"
    "ds_read_b128 v[112:115], %[ea] offset:4096\n\t"
    "s_min_i32 %[lf], %[lf], %[cp]\n\t"
    "s_add_u32 %[b], %[b], 1\n\t"
    "v_mfma_f32_16x16x32_bf16 v[84:87], v[252:255], v[120:123], v[68:71]\n\t"
    "ds_read_b128 v[116:119], %[ea] offset:6144\n\t"
    "v_mfma_f32_16x16x32_bf16 v[88:91], v[248:251], v[124:127], v[72:75]\n\t"
    "v_mfma_f32_16x16x32_bf16 v[92:95], v[252:255], v[124:127], v[76:79]\n\t"
    "s_mov_b64 s[36:37], s[38:39]\n\t"
    "s_add_u32 s38, s38, %[ab]\n\t"
    "s_addc_u32 s39, s39, 0\n\t"
    "s_cmp_lt_i32 %[b], %[lf]\n\t"
    "s_cbranch_scc1 1b\n\t"
    "s_waitcnt lgkmcnt(0)\n\t"
    "s_nop 4\n\t"
    "s_branch 9f\n\t"
    "5:\n\t"
    "s_add_u32 %[st], %[b], %[po]\n\t"
    "s_max_i32 %[st], %[st], %[p0]\n\t"
    "v_mov_b32 v39, %[st]\n\t"
    "v_mov_b32 v38, 0\n\t"
    "s_mov_b64 exec, 1\n\t"
    "global_store_dword v38, v39, %[pg] sc1\n\t"
    "s_mov_b64 exec, -1\n\t"
    "s_mov_b32 %[p0], %[p1]\n\t"
    "s_branch 6b\n\t"
    "7:\n\t"
    "s_cmp_ge_i32 %[lf], %[cp]\n\t"
    "s_cbranch_scc1 8b\n\t"
    "s_mov_b32 m0, %[sl]\n\t"
    "s_nop 0\n\t"
    "global_load_lds_dword %[dv], %[db] sc1\n\t"
    "s_branch 8b\n\t"
    "9:"
    : "=&{v[80:95]}"(pd), [ea] "+&v"(ea), [da] "+&v"(da), [b] "+&s"(b), [lf] "+&s"(lf), "+{s[36:37]}"(p1), "+{s[38:39]}"(p0), [sa] "+&v"(sa), [st] "=&s"(st), [sl] "+&s"(sl), [p0] "+&s"(pf)
    : [rw] "v"(rw), [ab] "s"(ab), [cp] "s"(cp), [mu] "s"(mu), [ad] "v"(ad), "{v[128:159]}"(W[0]), "{v[160:191]}"(W[1]), "{v[192:223]}"(W[2]), "{v[224:255]}"(W[3]), [co0] "v"(co0), [w0] "s"(w0), [dv] "v"(dv), [db] "s"(db), [pg] "s"(pg), [po] "s"(po - 1), [p1] "s"(b + po), [co1] "v"(co1)
    : "v38", "v39", "v40", "v41", "v42", "v43", "v44", "v45", "v46", "v47", "v48", "v49", "v50", "v51", "v52", "v53", "v54", "v55", "v56", "v57", "v58", "v59", "v60", "v61", "v62", "v63", "v64", "v65", "v66", "v67", "v68", "v69", "v70", "v71", "v72", "v73", "v74", "v75", "v76", "v77", "v78", "v79", "v96", "v97", "v98", "v99", "v100", "v101", "v102", "v103", "v104", "v105", "v106", "v107", "v108", "v109", "v110", "v111", "v112", "v113", "v114", "v115", "v116", "v117", "v118", "v119", "v120", "v121", "v122", "v123", "v124", "v125", "v126", "v127", "vcc", "scc", "m0", "memory");
}
__device__ __forceinline__ void fast_st1(const u32x32 (&W)[4], u32x16& pd, unsigned ea, unsigned da, int& b, int& lf, int cp, unsigned sa, unsigned mu, unsigned ad,
    unsigned rw, unsigned ab, const char* p1, const char* p0, unsigned co0, unsigned co1, int po, int pf, unsigned w0, unsigned sl, unsigned dv, const char* db, const char* pg) {
  unsigned st;
  asm volatile("s_barrier\n\t"
    "ds_read_b128 v[96:99], %[ea] offset:0\n\t"
    "ds_read_b128 v[100:103], %[ea] offset:2048\n\t"
    "ds_read_b128 v[104:107], %[da] offset:0\n\t"
    "ds_read_b128 v[108:111], %[da] offset:2048\n\t"
    "ds_read_b128 v[112:115], %[ea] offset:4096\n\t"
    "ds_read_b128 v[116:119], %[ea] offset:6144\n\t"
    "s_branch 2f\n\t"
    "1:\n\t"
    "s_barrier\n\t"
    "2:\n\t"
    "s_waitcnt lgkmcnt(4)\n\t"
    "v_mfma_f32_16x16x32_bf16 v[64:67], v[128:131], v[96:99], 0\n\t"
    "ds_read_b128 v[120:123], %[da] offset:4096\n\t"
    "v_mfma_f32_16x16x32_bf16 v[68:71], v[132:135], v[96:99], 0\n\t"
    "ds_read_b128 v[124:127], %[da] offset:6144\n\t"
    "v_mfma_f32_16x16x32_bf16 v[72:75], v[128:131], v[100:103], 0\n\t"
    "v_mfma_f32_16x16x32_bf16 v[76:79], v[132:135], v[100:103], 0\n\t"
    "s_waitcnt lgkmcnt(4)\n\t"
    "v_mfma_f32_16x16x32_bf16 v[64:67], v[136:139], v[104:107], v[64:67]\n\t"
    "ds_read_b128 v[96:99], %[ea] offset:8192\n\t"
    "v_mfma_f32_16x16x32_bf16 v[68:71], v[140:143], v[104:107], v[68:71]\n\t"
    "ds_read_b128 v[100:103], %[ea] offset:10240\n\t"
    "v_mfma_f32_16x16x32_bf16 v[72:75], v[136:139], v[108:111], v[72:75]\n\t"
    "v_mfma_f32_16x16x32_bf16 v[76:79], v[140:143], v[108:111], v[76:79]\n\t"
    "s_cmp_eq_u32 %[w0], 1\n\t"
    "s_cbranch_scc1 7f\n\t"
    "8:\n\t"
    "s_waitcnt lgkmcnt(4)\n\t"
    "v_mfma_f32_16x16x32_bf16 v[64:67], v[144:147], v[112:115], v[64:67]\n\t"
    "ds_read_b128 v[104:107], %[da] offset:8192\n\t"
    "v_mfma_f32_16x16x32_bf16 v[68:71], v[148:151], v[112:115], v[68:71]\n\t"
    "ds_read_b128 v[108:111], %[da] offset:10240\n\t"
    "v_mfma_f32_16x16x32_bf16 v[72:75], v[144:147], v[116:119], v[72:75]\n\t"
    "v_mfma_f32_16x16x32_bf16 v[76:79], v[148:151], v[116:119], v[76:79]\n\t"
    "s_waitcnt lgkmcnt(4)\n\t"
    "v_mfma_f32_16x16x32_bf16 v[64:67], v[152:155], v[120:123], v[64:67]\n\t"
    "ds_read_b128 v[112:115], %[ea] offset:12288\n\t"
    "v_mfma_f32_16x16x32_bf16 v[68:71], v[156:159], v[120:123], v[68:71]\n\t"
    "ds_read_b128 v[116:119], %[ea] offset:14336\n\t"
    "v_mfma_f32_16x16x32_bf16 v[72:75], v[152:155], v[124:127], v[72:75]\n\t"
    "v_mfma_f32_16x16x32_bf16 v[76:79], v[156:159], v[124:127], v[76:79]\n\t"
    "s_waitcnt lgkmcnt(4)\n\t"
    "v_mfma_f32_16x16x32_bf16 v[64:67], v[160:163], v[96:99], v[64:67]\n\t"
    "ds_read_b128 v[120:123], %[da] offset:12288\n\t"
    "v_mfma_f32_16x16x32_bf16 v[68:71], v[164:167], v[96:99], v[68:71]\n\t"
    "ds_read_b128 v[124:127], %[da] offset:14336\n\t"
    "v_mfma_f32_16x16x32_bf16 v[72:75], v[160:163], v[100:103], v[72:75]\n\t"
    "v_mfma_f32_16x16x32_bf16 v[76:79], v[164:167], v[100:103], v[76:79]\n\t"
    "s_waitcnt lgkmcnt(4)\n\t"
    "v_mfma_f32_16x16x32_bf16 v[64:67], v[168:171], v[104:107], v[64:67]\n\t"
    "ds_read_b128 v[96:99], %[ea] offset:16384\n\t"
    "v_mfma_f32_16x16x32_bf16 v[68:71], v[172:175], v[104:107], v[68:71]\n\t"
    "ds_read_b128 v[100:103], %[ea] offset:18432\n\t"
    "v_mfma_f32_16x16x32_bf16 v[72:75], v[168:171], v[108:111], v[72:75]\n\t"
    "v_mfma_f32_16x16x32_bf16 v[76:79], v[172:175], v[108:111], v[76:79]\n\t"
    "s_waitcnt lgkmcnt(4)\n\t"
    "v_mfma_f32_16x16x32_bf16 v[64:67], v[176:179], v[112:115], v[64:67]\n\t"
    "ds_read_b128 v[104:107], %[da] offset:16384\n\t"
    "v_mfma_f32_16x16x32_bf16 v[68:71], v[180:183], v[112:115], v[68:71]\n\t"
    "ds_read_b128 v[108:111], %[da] offset:18432\n\t"
    "v_mfma_f32_16x16x32_bf16 v[72:75], v[176:179], v[116:119], v[72:75]\n\t"
    "v_mfma_f32_16x16x32_bf16 v[76:79], v[180:183], v[116:119], v[76:79]\n\t"
    "s_waitcnt lgkmcnt(4)\n\t"
    "v_mfma_f32_16x16x32_bf16 v[64:67], v[184:187], v[120:123], v[64:67]\n\t"
    "ds_read_b128 v[112:115], %[ea] offset:20480\n\t"
    "v_mfma_f32_16x16x32_bf16 v[68:71], v[188:191], v[120:123], v[68:71]\n\t"
    "ds_read_b128 v[116:119], %[ea] offset:22528\n\t"
    "v_mfma_f32_16x16x32_bf16 v[72:75], v[184:187], v[124:127], v[72:75]\n\t"
    "v_mfma_f32_16x16x32_bf16 v[76:79], v[188:191], v[124:127], v[76:79]\n\t"
    "s_waitcnt vmcnt(0)\n\t"
    "s_barrier\n\t"
    "s_waitcnt lgkmcnt(4)\n\t"
    "v_mfma_f32_16x16x32_bf16 v[64:67], v[192:195], v[96:99], v[64:67]\n\t"
    "ds_read_b128 v[120:123], %[da] offset:20480\n\t"
    "v_mfma_f32_16x16x32_bf16 v[68:71], v[196:199], v[96:99], v[68:71]\n\t"
    "ds_read_b128 v[124:127], %[da] offset:22528\n\t"
    "v_mfma_f32_16x16x32_bf16 v[72:75], v[192:195], v[100:103], v[72:75]\n\t"
    "ds_read_b128 v[48:51], %[rw] offset:0\n\t"
    "v_mfma_f32_16x16x32_bf16 v[76:79], v[196:199], v[100:103], v[76:79]\n\t"
    "ds_read_b128 v[52:55], %[rw] offset:1024\n\t"
    "s_cmp_eq_u32 %[w0], 0\n\t"
    "s_cbranch_scc1 5f\n\t"
    "6:\n\t"
    "s_waitcnt lgkmcnt(6)\n\t"
    "v_mfma_f32_16x16x32_bf16 v[64:67], v[200:203], v[104:107], v[64:67]\n\t"
    "ds_read_b128 v[96:99], %[ea] offset:24576\n\t"
    "v_mfma_f32_16x16x32_bf16 v[68:71], v[204:207], v[104:107], v[68:71]\n\t"
    "ds_read_b128 v[100:103], %[ea] offset:26624\n\t"
    "v_mfma_f32_16x16x32_bf16 v[72:75], v[200:203], v[108:111], v[72:75]\n\t"
    "ds_read_b128 v[56:59], %[rw] offset:2048\n\t"
    "v_mfma_f32_16x16x32_bf16 v[76:79], v[204:207], v[108:111], v[76:79]\n\t"
    "ds_read_b128 v[60:63], %[rw] offset:3072\n\t"
    "s_waitcnt lgkmcnt(0)\n\t"
    "v_mfma_f32_16x16x32_bf16 v[64:67], v[208:211], v[112:115], v[64:67]\n\t"
    "ds_read_b128 v[104:107], %[da] offset:24576\n\t"
    "v_add_f32 v48, v48, v80\n\t"
    "v_add_f32 v49, v49, v81\n\t"
    "v_add_f32 v50, v50, v82\n\t"
    "v_mfma_f32_16x16x32_bf16 v[68:71], v[212:215], v[112:115], v[68:71]\n\t"
    "ds_read_b128 v[108:111], %[da] offset:26624\n\t"
    "v_add_f32 v51, v51, v83\n\t"
    "v_add_f32 v52, v52, v84\n\t"
    "v_add_f32 v53, v53, v85\n\t"
    "v_mfma_f32_16x16x32_bf16 v[72:75], v[208:211], v[116:119], v[72:75]\n\t"
    "ds_read_b32 v39, %[sa]\n\t"
    "v_add_f32 v54, v54, v86\n\t"
    "v_add_f32 v55, v55, v87\n\t"
    "v_add_f32 v56, v56, v88\n\t"
    "v_mfma_f32_16x16x32_bf16 v[76:79], v[212:215], v[116:119], v[76:79]\n\t"
    "v_add_f32 v57, v57, v89\n\t"
    "v_add_f32 v58, v58, v90\n\t"
    "v_add_f32 v59, v59, v91\n\t"
    "v_mfma_f32_16x16x32_bf16 v[64:67], v[216:219], v[120:123], v[64:67]\n\t"
    "ds_read_b128 v[112:115], %[ea] offset:28672\n\t"
    "v_add_f32 v60, v60, v92\n\t"
    "v_add_f32 v61, v61, v93\n\t"
    "v_add_f32 v62, v62, v94\n\t"
    "v_mfma_f32_16x16x32_bf16 v[68:71], v[220:223], v[120:123], v[68:71]\n\t"
    "ds_read_b128 v[116:119], %[ea] offset:30720\n\t"
    "v_add_f32 v63, v63, v95\n\t"
    "v_mul_f32 v40, %[nl], v48\n\t"
    "v_mul_f32 v41, %[nl], v49\n\t"
    "v_mfma_f32_16x16x32_bf16 v[72:75], v[216:219], v[124:127], v[72:75]\n\t"
    "v_xor_b32 %[ea], 0x10000, %[ea]\n\t"
    "v_mul_f32 v42, %[nl], v50\n\t"
    "v_mul_f32 v43, %[nl], v51\n\t"
    "v_mul_f32 v44, %[nl], v56\n\t"
    "v_mfma_f32_16x16x32_bf16 v[76:79], v[220:223], v[124:127], v[76:79]\n\t"
    "v_mul_f32 v45, %[nl], v57\n\t"
    "v_mul_f32 v46, %[nl], v58\n\t"
    "v_mul_f32 v47, %[nl], v59\n\t"
    "v_mfma_f32_16x16x32_bf16 v[64:67], v[224:227], v[96:99], v[64:67]\n\t"
    "ds_read_b128 v[120:123], %[da] offset:28672\n\t"
    "v_exp_f32 v40, v40\n\t"
    "v_exp_f32 v41, v41\n\t"
    "v_exp_f32 v42, v42\n\t"
    "v_mfma_f32_16x16x32_bf16 v[68:71], v[228:231], v[96:99], v[68:71]\n\t"
    "ds_read_b128 v[124:127], %[da] offset:30720\n\t"
    "v_exp_f32 v43, v43\n\t"
    "v_exp_f32 v44, v44\n\t"
    "v_exp_f32 v45, v45\n\t"
    "v_mfma_f32_16x16x32_bf16 v[72:75], v[224:227], v[100:103], v[72:75]\n\t"
    "v_xor_b32 %[da], 0x10000, %[da]\n\t"
    "v_exp_f32 v46, v46\n\t"
    "v_exp_f32 v47, v47\n\t"
    "v_add_f32 v40, 1.0, v40\n\t"
    "v_mfma_f32_16x16x32_bf16 v[76:79], v[228:231], v[100:103], v[76:79]\n\t"
    "v_add_f32 v41, 1.0, v41\n\t"
    "v_add_f32 v42, 1.0, v42\n\t"
    "v_add_f32 v43, 1.0, v43\n\t"
    "s_waitcnt lgkmcnt(5)\n\t"
    "v_mfma_f32_16x16x32_bf16 v[64:67], v[232:235], v[104:107], v[64:67]\n\t"
    "ds_read_b128 v[96:99], %[ea] offset:0\n\t"
    "v_add_f32 v44, 1.0, v44\n\t"
    "v_add_f32 v45, 1.0, v45\n\t"
    "v_add_f32 v46, 1.0, v46\n\t"
    "v_mfma_f32_16x16x32_bf16 v[68:71], v[236:239], v[104:107], v[68:71]\n\t"
    "ds_read_b128 v[100:103], %[ea] offset:2048\n\t"
    "v_add_f32 v47, 1.0, v47\n\t"
    "v_rcp_f32 v40, v40\n\t"
    "v_rcp_f32 v41, v41\n\t"
    "v_mfma_f32_16x16x32_bf16 v[72:75], v[232:235], v[108:111], v[72:75]\n\t"
    "v_rcp_f32 v42, v42\n\t"
    "v_rcp_f32 v43, v43\n\t"
    "v_rcp_f32 v44, v44\n\t"
    "v_mfma_f32_16x16x32_bf16 v[76:79], v[236:239], v[108:111], v[76:79]\n\t"
    "v_rcp_f32 v45, v45\n\t"
    "v_rcp_f32 v46, v46\n\t"
    "v_rcp_f32 v47, v47\n\t"
    "s_waitcnt lgkmcnt(4)\n\t"
    "v_mfma_f32_16x16x32_bf16 v[64:67], v[240:243], v[112:115], v[64:67]\n\t"
    "ds_read_b128 v[104:107], %[da] offset:0\n\t"
    "v_mad_u32_u24 v39, v39, %[mu], %[ad]\n\t"
    "v_mul_f32 v40, v48, v40\n\t"
    "v_mul_f32 v41, v49, v41\n\t"
    "v_mul_f32 v42, v50, v42\n\t"
    "v_mfma_f32_16x16x32_bf16 v[68:71], v[244:247], v[112:115], v[68:71]\n\t"
    "ds_read_b128 v[108:111], %[da] offset:2048\n\t"
    "v_cmp_ge_i32 vcc, %[lf], v39\n\t"
    "s_cmp_eq_u64 vcc, 0\n\t"
    "s_addc_u32 %[lf], %[lf], 0\n\t"
    "v_mul_f32 v43, v51, v43\n\t"
    "v_mul_f32 v44, v56, v44\n\t"
    "v_mul_f32 v45, v57, v45\n\t"
    "v_mfma_f32_16x16x32_bf16 v[72:75], v[240:243], v[116:119], v[72:75]\n\t"
    "v_mul_f32 v46, v58, v46\n\t"
    "v_mul_f32 v47, v59, v47\n\t"
    "v_mul_f32 v40, v40, v52\n\t"
    "v_mfma_f32_16x16x32_bf16 v[76:79], v[244:247], v[116:119], v[76:79]\n\t"
    "v_cmp_ge_i32 vcc, %[lf], v39\n\t"
    "s_cmp_eq_u64 vcc, 0\n\t"
    "s_addc_u32 %[lf], %[lf], 0\n\t"
    "v_mul_f32 v41, v41, v53\n\t"
    "v_mul_f32 v42, v42, v54\n\t"
    "v_mul_f32 v43, v43, v55\n\t"
    "s_waitcnt lgkmcnt(4)\n\t"
    "v_mfma_f32_16x16x32_bf16 v[80:83], v[248:251], v[120:123], v[64:67]\n\t"
    "ds_read_b128 v[112:115], %[ea] offset:4096\n\t"
    "s_min_i32 %[lf], %[lf], %[cp]\n\t"
    "s_add_u32 %[b], %[b], 1\n\t"
    "v_mul_f32 v44, v44, v60\n\t"
    "v_mul_f32 v45, v45, v61\n\t"
    "v_mul_f32 v46, v46, v62\n\t"
    "v_mfma_f32_16x16x32_bf16 v[84:87], v[252:255], v[120:123], v[68:71]\n\t"
    "ds_read_b128 v[116:119], %[ea] offset:6144\n\t"
    "v_mul_f32 v47, v47, v63\n\t"
    "v_cvt_pk_bf16_f32 v48, v40, v41\n\t"
    "v_cvt_pk_bf16_f32 v49, v42, v43\n\t"
    "v_mfma_f32_16x16x32_bf16 v[88:91], v[248:251], v[124:127], v[72:75]\n\t"
    "v_cvt_pk_bf16_f32 v50, v44, v45\n\t"
    "v_cvt_pk_bf16_f32 v51, v46, v47\n\t"
    "s_nop 1\n\t"
    "v_mfma_f32_16x16x32_bf16 v[92:95], v[252:255], v[124:127], v[76:79]\n\t"
    "v_permlane16_swap_b32 v48, v50\n\t"
    "v_permlane16_swap_b32 v49, v51\n\t"
    "global_store_dwordx4 %[co0], v[48:51], s[36:37] sc1\n\t"
    "s_mov_b64 s[36:37], s[38:39]\n\t"
    "s_add_u32 s38, s38, %[ab]\n\t"
    "s_addc_u32 s39, s39, 0\n\t"
    "s_cmp_lt_i32 %[b], %[lf]\n\t"
    "s_cbranch_scc1 1b\n\t"
    "s_waitcnt lgkmcnt(0)\n\t"
    "s_nop 4\n\t"
    "s_branch 9f\n\t"
    "5:\n\t"
    "s_add_u32 %[st], %[b], %[po]\n\t"
    "s_max_i32 %[st], %[st], %[p0]\n\t"
    "v_mov_b32 v39, %[st]\n\t"
    "v_mov_b32 v38, 0\n\t"
    "s_mov_b64 exec, 1\n\t"
    "global_store_dword v38, v39, %[pg] sc1\n\t"
    "s_mov_b64 exec, -1\n\t"
    "s_mov_b32 %[p0], %[p1]\n\t"
    "s_branch 6b\n\t"
    "7:\n\t"
    "s_cmp_ge_i32 %[lf], %[cp]\n\t"
    "s_cbranch_scc1 8b\n\t"
    "s_mov_b32 m0, %[sl]\n\t"
    "s_nop 0\n\t"
    "global_load_lds_dword %[dv], %[db] sc1\n\t"
    "s_branch 8b\n\t"
    "9:"
    : "=&{v[80:95]}"(pd), [ea] "+&v"(ea), [da] "+&v"(da), [b] "+&s"(b), [lf] "+&s"(lf), "+{s[36:37]}"(p1), "+{s[38:39]}"(p0), [sa] "+&v"(sa), [st] "=&s"(st), [sl] "+&s"(sl), [p0] "+&s"(pf)
    : [rw] "v"(rw), [ab] "s"(ab), [cp] "s"(cp), [mu] "s"(mu), [ad] "v"(ad), "{v[128:159]}"(W[0]), "{v[160:191]}"(W[1]), "{v[192:223]}"(W[2]), "{v[224:255]}"(W[3]), [co0] "v"(co0), [w0] "s"(w0), [dv] "v"(dv), [db] "s"(db), [pg] "s"(pg), [po] "s"(po - 1), [p1] "s"(b + po), [nl] "s"(-LOG2E)
    : "v38", "v39", "v40", "v41", "v42", "v43", "v44", "v45", "v46", "v47", "v48", "v49", "v50", "v51", "v52", "v53", "v54", "v55", "v56", "v57", "v58", "v59", "v60", "v61", "v62", "v63", "v64", "v65", "v66", "v67", "v68", "v69", "v70", "v71", "v72", "v73", "v74", "v75", "v76", "v77", "v78", "v79", "v96", "v97", "v98", "v99", "v100", "v101", "v102", "v103", "v104", "v105", "v106", "v107", "v108", "v109", "v110", "v111", "v112", "v113", "v114", "v115", "v116", "v117", "v118", "v119", "v120", "v121", "v122", "v123", "v124", "v125", "v126", "v127", "vcc", "scc", "m0", "memory");
}
__device__ __forceinline__ void fast_st2(const u32x32 (&W)[4], u32x16& pd, unsigned ea, unsigned da, int& b, int& lf, int cp, unsigned sa, unsigned mu, unsigned ad,
    unsigned rw, unsigned ab, const char* p1, const char* p0, unsigned co0, unsigned co1, int po, int pf, unsigned w0, unsigned sl, unsigned dv, const char* db, const char* pg) {
  unsigned st;
  asm volatile("s_barrier\n\t"
    "ds_read_b128 v[96:99], %[ea] offset:0\n\t"
    "ds_read_b128 v[100:103], %[ea] offset:2048\n\t"
    "ds_read_b128 v[104:107], %[da] offset:0\n\t"
    "ds_read_b128 v[108:111], %[da] offset:2048\n\t"
    "ds_read_b128 v[112:115], %[ea] offset:4096\n\t"
    "ds_read_b128 v[116:119], %[ea] offset:6144\n\t"
    "s_branch 2f\n\t"
    "1:\n\t"
    "s_barrier\n\t"
    "2:\n\t"
    "s_waitcnt lgkmcnt(4)\n\t"
    "v_mfma_f32_16x16x32_bf16 v[64:67], v[128:131], v[96:99], 0\n\t"
    "ds_read_b128 v[120:123], %[da] offset:4096\n\t"
    "v_mfma_f32_16x16x32_bf16 v[68:71], v[132:135], v[96:99], 0\n\t"
    "ds_read_b128 v[124:127], %[da] offset:6144\n\t"
    "v_mfma_f32_16x16x32_bf16 v[72:75], v[128:131], v[100:103], 0\n\t"
    "v_mfma_f32_16x16x32_bf16 v[76:79], v[132:135], v[100:103], 0\n\t"
    "s_waitcnt lgkmcnt(4)\n\t"
    "v_mfma_f32_16x16x32_bf16 v[64:67], v[136:139], v[104:107], v[64:67]\n\t"
    "ds_read_b128 v[96:99], %[ea] offset:8192\n\t"
    "v_mfma_f32_16x16x32_bf16 v[68:71], v[140:143], v[104:107], v[68:71]\n\t"
    "ds_read_b128 v[100:103], %[ea] offset:10240\n\t"
    "v_mfma_f32_16x16x32_bf16 v[72:75], v[136:139], v[108:111], v[72:75]\n\t"
    "v_mfma_f32_16x16x32_bf16 v[76:79], v[140:143], v[108:111], v[76:79]\n\t"
    "s_cmp_eq_u32 %[w0], 1\n\t"
    "s_cbranch_scc1 7f\n\t"
    "8:\n\t"
    "s_waitcnt lgkmcnt(4)\n\t"
    "v_mfma_f32_16x16x32_bf16 v[64:67], v[144:147], v[112:115], v[64:67]\n\t"
    "ds_read_b128 v[104:107], %[da] offset:8192\n\t"
    "v_mfma_f32_16x16x32_bf16 v[68:71], v[148:151], v[112:115], v[68:71]\n\t"
    "ds_read_b128 v[108:111], %[da] offset:10240\n\t"
    "v_mfma_f32_16x16x32_bf16 v[72:75], v[144:147], v[116:119], v[72:75]\n\t"
    "v_mfma_f32_16x16x32_bf16 v[76:79], v[148:151], v[116:119], v[76:79]\n\t"
    "s_waitcnt lgkmcnt(4)\n\t"
    "v_mfma_f32_16x16x32_bf16 v[64:67], v[152:155], v[120:123], v[64:67]\n\t"
    "ds_read_b128 v[112:115], %[ea] offset:12288\n\t"
    "v_mfma_f32_16x16x32_bf16 v[68:71], v[156:159], v[120:123], v[68:71]\n\t"
    "ds_read_b128 v[116:119], %[ea] offset:14336\n\t"
    "v_mfma_f32_16x16x32_bf16 v[72:75], v[152:155], v[124:127], v[72:75]\n\t"
    "v_mfma_f32_16x16x32_bf16 v[76:79], v[156:159], v[124:127], v[76:79]\n\t"
    "s_waitcnt lgkmcnt(4)\n\t"
    "v_mfma_f32_16x16x32_bf16 v[64:67], v[160:163], v[96:99], v[64:67]\n\t"
    "ds_read_b128 v[120:123], %[da] offset:12288\n\t"
    "v_mfma_f32_16x16x32_bf16 v[68:71], v[164:167], v[96:99], v[68:71]\n\t"
    "ds_read_b128 v[124:127], %[da] offset:14336\n\t"
    "v_mfma_f32_16x16x32_bf16 v[72:75], v[160:163], v[100:103], v[72:75]\n\t"
    "v_mfma_f32_16x16x32_bf16 v[76:79], v[164:167], v[100:103], v[76:79]\n\t"
    "s_waitcnt lgkmcnt(4)\n\t"
    "v_mfma_f32_16x16x32_bf16 v[64:67], v[168:171], v[104:107], v[64:67]\n\t"
    "ds_read_b128 v[96:99], %[ea] offset:16384\n\t"
    "v_mfma_f32_16x16x32_bf16 v[68:71], v[172:175], v[104:107], v[68:71]\n\t"
    "ds_read_b128 v[100:103], %[ea] offset:18432\n\t"
    "v_mfma_f32_16x16x32_bf16 v[72:75], v[168:171], v[108:111], v[72:75]\n\t"
    "v_mfma_f32_16x16x32_bf16 v[76:79], v[172:175], v[108:111], v[76:79]\n\t"
    "s_waitcnt lgkmcnt(4)\n\t"
    "v_mfma_f32_16x16x32_bf16 v[64:67], v[176:179], v[112:115], v[64:67]\n\t"
    "ds_read_b128 v[104:107], %[da] offset:16384\n\t"
    "v_mfma_f32_16x16x32_bf16 v[68:71], v[180:183], v[112:115], v[68:71]\n\t"
    "ds_read_b128 v[108:111], %[da] offset:18432\n\t"
    "v_mfma_f32_16x16x32_bf16 v[72:75], v[176:179], v[116:119], v[72:75]\n\t"
    "v_mfma_f32_16x16x32_bf16 v[76:79], v[180:183], v[116:119], v[76:79]\n\t"
    "s_waitcnt lgkmcnt(4)\n\t"
    "v_mfma_f32_16x16x32_bf16 v[64:67], v[184:187], v[120:123], v[64:67]\n\t"
    "ds_read_b128 v[112:115], %[ea] offset:20480\n\t"
    "v_mfma_f32_16x16x32_bf16 v[68:71], v[188:191], v[120:123], v[68:71]\n\t"
    "ds_read_b128 v[116:119], %[ea] offset:22528\n\t"
    "v_mfma_f32_16x16x32_bf16 v[72:75], v[184:187], v[124:127], v[72:75]\n\t"
    "v_mfma_f32_16x16x32_bf16 v[76:79], v[188:191], v[124:127], v[76:79]\n\t"
    "s_waitcnt vmcnt(0)\n\t"
    "s_barrier\n\t"
    "s_waitcnt lgkmcnt(4)\n\t"
    "v_mfma_f32_16x16x32_bf16 v[64:67], v[192:195], v[96:99], v[64:67]\n\t"
    "ds_read_b128 v[120:123], %[da] offset:20480\n\t"
    "v_mfma_f32_16x16x32_bf16 v[68:71], v[196:199], v[96:99], v[68:71]\n\t"
    "ds_read_b128 v[124:127], %[da] offset:22528\n\t"
    "v_mfma_f32_16x16x32_bf16 v[72:75], v[192:195], v[100:103], v[72:75]\n\t"
    "ds_read_b128 v[48:51], %[rw] offset:0\n\t"
    "v_mfma_f32_16x16x32_bf16 v[76:79], v[196:199], v[100:103], v[76:79]\n\t"
    "ds_read_b128 v[52:55], %[rw] offset:1024\n\t"
    "s_cmp_eq_u32 %[w0], 0\n\t"
    "s_cbranch_scc1 5f\n\t"
    "6:\n\t"
    "s_waitcnt lgkmcnt(6)\n\t"
    "v_mfma_f32_16x16x32_bf16 v[64:67], v[200:203], v[104:107], v[64:67]\n\t"
    "ds_read_b128 v[96:99], %[ea] offset:24576\n\t"
    "v_mfma_f32_16x16x32_bf16 v[68:71], v[204:207], v[104:107], v[68:71]\n\t"
    "ds_read_b128 v[100:103], %[ea] offset:26624\n\t"
    "v_mfma_f32_16x16x32_bf16 v[72:75], v[200:203], v[108:111], v[72:75]\n\t"
    "ds_read_b128 v[56:59], %[rw] offset:2048\n\t"
    "v_mfma_f32_16x16x32_bf16 v[76:79], v[204:207], v[108:111], v[76:79]\n\t"
    "ds_read_b128 v[60:63], %[rw] offset:3072\n\t"
    "s_waitcnt lgkmcnt(0)\n\t"
    "v_mfma_f32_16x16x32_bf16 v[64:67], v[208:211], v[112:115], v[64:67]\n\t"
    "ds_read_b128 v[104:107], %[da] offset:24576\n\t"
    "v_cvt_pk_bf16_f32 v40, v80, v81\n\t"
    "v_mfma_f32_16x16x32_bf16 v[68:71], v[212:215], v[112:115], v[68:71]\n\t"
    "ds_read_b128 v[108:111], %[da] offset:26624\n\t"
    "v_cvt_pk_bf16_f32 v41, v82, v83\n\t"
    "v_mfma_f32_16x16x32_bf16 v[72:75], v[208:211], v[116:119], v[72:75]\n\t"
    "ds_read_b32 v39, %[sa]\n\t"
    "v_cvt_pk_bf16_f32 v42, v84, v85\n\t"
    "v_mfma_f32_16x16x32_bf16 v[76:79], v[212:215], v[116:119], v[76:79]\n\t"
    "v_cvt_pk_bf16_f32 v43, v86, v87\n\t"
    "v_mfma_f32_16x16x32_bf16 v[64:67], v[216:219], v[120:123], v[64:67]\n\t"
    "ds_read_b128 v[112:115], %[ea] offset:28672\n\t"
    "global_store_dwordx4 %[co0], v[40:43], s[36:37] sc1\n\t"
    "v_mfma_f32_16x16x32_bf16 v[68:71], v[220:223], v[120:123], v[68:71]\n\t"
    "ds_read_b128 v[116:119], %[ea] offset:30720\n\t"
    "v_cvt_pk_bf16_f32 v44, v88, v89\n\t"
    "v_mfma_f32_16x16x32_bf16 v[72:75], v[216:219], v[124:127], v[72:75]\n\t"
    "v_xor_b32 %[ea], 0x10000, %[ea]\n\t"
    "v_cvt_pk_bf16_f32 v45, v90, v91\n\t"
    "v_mfma_f32_16x16x32_bf16 v[76:79], v[220:223], v[124:127], v[76:79]\n\t"
    "v_cvt_pk_bf16_f32 v46, v92, v93\n\t"
    "v_mfma_f32_16x16x32_bf16 v[64:67], v[224:227], v[96:99], v[64:67]\n\t"
    "ds_read_b128 v[120:123], %[da] offset:28672\n\t"
    "v_cvt_pk_bf16_f32 v47, v94, v95\n\t"
    "v_mfma_f32_16x16x32_bf16 v[68:71], v[228:231], v[96:99], v[68:71]\n\t"
    "ds_read_b128 v[124:127], %[da] offset:30720\n\t"
    "global_store_dwordx4 %[co1], v[44:47], s[36:37] sc1\n\t"
    "v_mfma_f32_16x16x32_bf16 v[72:75], v[224:227], v[100:103], v[72:75]\n\t"
    "v_xor_b32 %[da], 0x10000, %[da]\n\t"
    "v_cvt_pk_bf16_f32 v48, v48, v49\n\t"
    "v_mfma_f32_16x16x32_bf16 v[76:79], v[228:231], v[100:103], v[76:79]\n\t"
    "v_cvt_pk_bf16_f32 v49, v50, v51\n\t"
    "s_waitcnt lgkmcnt(5)\n\t"
    "v_mfma_f32_16x16x32_bf16 v[64:67], v[232:235], v[104:107], v[64:67]\n\t"
    "ds_read_b128 v[96:99], %[ea] offset:0\n\t"
    "v_cvt_pk_bf16_f32 v50, v52, v53\n\t"
    "v_mfma_f32_16x16x32_bf16 v[68:71], v[236:239], v[104:107], v[68:71]\n\t"
    "ds_read_b128 v[100:103], %[ea] offset:2048\n\t"
    "v_cvt_pk_bf16_f32 v51, v54, v55\n\t"
    "v_mfma_f32_16x16x32_bf16 v[72:75], v[232:235], v[108:111], v[72:75]\n\t"
    "global_store_dwordx4 %[co0], v[48:51], s[36:37] offset:2048 sc1\n\t"
    "v_mfma_f32_16x16x32_bf16 v[76:79], v[236:239], v[108:111], v[76:79]\n\t"
    "v_cvt_pk_bf16_f32 v56, v56, v57\n\t"
    "s_waitcnt lgkmcnt(4)\n\t"
    "v_mfma_f32_16x16x32_bf16 v[64:67], v[240:243], v[112:115], v[64:67]\n\t"
    "ds_read_b128 v[104:107], %[da] offset:0\n\t"
    "v_mad_u32_u24 v39, v39, %[mu], %[ad]\n\t"
    "v_cvt_pk_bf16_f32 v57, v58, v59\n\t"
    "v_mfma_f32_16x16x32_bf16 v[68:71], v[244:247], v[112:115], v[68:71]\n\t"
    "ds_read_b128 v[108:111], %[da] offset:2048\n\t"
    "v_cmp_ge_i32 vcc, %[lf], v39\n\t"
    "s_cmp_eq_u64 vcc, 0\n\t"
    "s_addc_u32 %[lf], %[lf], 0\n\t"
    "v_cvt_pk_bf16_f32 v58, v60, v61\n\t"
    "v_mfma_f32_16x16x32_bf16 v[72:75], v[240:243], v[116:119], v[72:75]\n\t"
    "v_cvt_pk_bf16_f32 v59, v62, v63\n\t"
    "v_mfma_f32_16x16x32_bf16 v[76:79], v[244:247], v[116:119], v[76:79]\n\t"
    "v_cmp_ge_i32 vcc, %[lf], v39\n\t"
    "s_cmp_eq_u64 vcc, 0\n\t"
    "s_addc_u32 %[lf], %[lf], 0\n\t"
    "global_store_dwordx4 %[co1], v[56:59], s[36:37] offset:2048 sc1\n\t"
    "s_waitcnt lgkmcnt(4)\n\t"
    "v_mfma_f32_16x16x32_bf16 v[80:83], v[248:251], v[120:123], v[64:67]\n\t"
    "ds_read_b128 v[112:115], %[ea] offset:4096\n\t"
    "s_min_i32 %[lf], %[lf], %[cp]\n\t"
    "s_add_u32 %[b], %[b], 1\n\t"
    "v_mfma_f32_16x16x32_bf16 v[84:87], v[252:255], v[120:123], v[68:71]\n\t"
    "ds_read_b128 v[116:119], %[ea] offset:6144\n\t"
    "v_mfma_f32_16x16x32_bf16 v[88:91], v[248:251], v[124:127], v[72:75]\n\t"
    "v_mfma_f32_16x16x32_bf16 v[92:95], v[252:255], v[124:127], v[76:79]\n\t"
    "s_mov_b64 s[36:37], s[38:39]\n\t"
    "s_add_u32 s38, s38, %[ab]\n\t"
    "s_addc_u32 s39, s39, 0\n\t"
    "s_cmp_lt_i32 %[b], %[lf]\n\t"
    "s_cbranch_scc1 1b\n\t"
    "s_waitcnt lgkmcnt(0)\n\t"
    "s_nop 4\n\t"
    "s_branch 9f\n\t"
    "5:\n\t"
    "s_add_u32 %[st], %[b], %[po]\n\t"
    "s_max_i32 %[st], %[st], %[p0]\n\t"
    "v_mov_b32 v39, %[st]\n\t"
    "v_mov_b32 v38, 0\n\t"
    "s_mov_b64 exec, 1\n\t"
    "global_store_dword v38, v39, %[pg] sc1\n\t"
    "s_mov_b64 exec, -1\n\t"
    "s_mov_b32 %[p0], %[p1]\n\t"
    "s_branch 6b\n\t"
    "7:\n\t"
    "s_cmp_ge_i32 %[lf], %[cp]\n\t"
    "s_cbranch_scc1 8b\n\t"
    "s_mov_b32 m0, %[sl]\n\t"
    "s_nop 0\n\t"
    "global_load_lds_dword %[dv], %[db] sc1\n\t"
    "s_branch 8b\n\t"
    "9:"
    : "=&{v[80:95]}"(pd), [ea] "+&v"(ea), [da] "+&v"(da), [b] "+&s"(b), [lf] "+&s"(lf), "+{s[36:37]}"(p1), "+{s[38:39]}"(p0), [sa] "+&v"(sa), [st] "=&s"(st), [sl] "+&s"(sl), [p0] "+&s"(pf)
    : [rw] "v"(rw), [ab] "s"(ab), [cp] "s"(cp), [mu] "s"(mu), [ad] "v"(ad), "{v[128:159]}"(W[0]), "{v[160:191]}"(W[1]), "{v[192:223]}"(W[2]), "{v[224:255]}"(W[3]), [co0] "v"(co0), [w0] "s"(w0), [dv] "v"(dv), [db] "s"(db), [pg] "s"(pg), [po] "s"(po - 1), [p1] "s"(b + po), [co1] "v"(co1)
    : "v38", "v39", "v40", "v41", "v42", "v43", "v44", "v45", "v46", "v47", "v48", "v49", "v50", "v51", "v52", "v53", "v54", "v55", "v56", "v57", "v58", "v59", "v60", "v61", "v62", "v63", "v64", "v65", "v66", "v67", "v68", "v69", "v70", "v71", "v72", "v73", "v74", "v75", "v76", "v77", "v78", "v79", "v96", "v97", "v98", "v99", "v100", "v101", "v102", "v103", "v104", "v105", "v106", "v107", "v108", "v109", "v110", "v111", "v112", "v113", "v114", "v115", "v116", "v117", "v118", "v119", "v120", "v121", "v122", "v123", "v124", "v125", "v126", "v127", "vcc", "scc", "m0", "memory");
}
__device__ __forceinline__ void lean_ld0(const u32x32 (&W)[4], unsigned ea, unsigned da, unsigned x, unsigned lb, const char* s0, const char* s1, const unsigned (&av)[8], unsigned ps, unsigned dv, const char* db, unsigned rw, unsigned fa, unsigned fv,
    unsigned ga, unsigned mu, unsigned ad, int th, unsigned& sv, unsigned& got) {
  asm volatile("s_setprio 2\n\t"
    "ds_read_b128 v[96:99], %[ea] offset:0\n\t"
    "ds_read_b128 v[100:103], %[ea] offset:2048\n\t"
    "ds_read_b128 v[104:107], %[da] offset:0\n\t"
    "ds_read_b128 v[108:111], %[da] offset:2048\n\t"
    "ds_read_b128 v[112:115], %[ea] offset:4096\n\t"
    "ds_read_b128 v[116:119], %[ea] offset:6144\n\t"
    "s_waitcnt lgkmcnt(4)\n\t"
    "v_mfma_f32_16x16x32_bf16 v[80:83], v[128:131], v[96:99], 0\n\t"
    "ds_read_b128 v[120:123], %[da] offset:4096\n\t"
    "v_mfma_f32_16x16x32_bf16 v[84:87], v[132:135], v[96:99], 0\n\t"
    "ds_read_b128 v[124:127], %[da] offset:6144\n\t"
    "v_mfma_f32_16x16x32_bf16 v[88:91], v[128:131], v[100:103], 0\n\t"
    "v_mfma_f32_16x16x32_bf16 v[92:95], v[132:135], v[100:103], 0\n\t"
    "s_waitcnt vmcnt(24)\n\t"
    "s_cmp_lt_u32 %[x], 2\n\t"
    "s_cbranch_scc0 41f\n\t"
    "s_waitcnt vmcnt(8)\n\t"
    "41:\n\t"
    "s_barrier\n\t"
    "s_waitcnt lgkmcnt(4)\n\t"
    "v_mfma_f32_16x16x32_bf16 v[80:83], v[136:139], v[104:107], v[80:83]\n\t"
    "ds_read_b128 v[96:99], %[ea] offset:8192\n\t"
    "v_mfma_f32_16x16x32_bf16 v[84:87], v[140:143], v[104:107], v[84:87]\n\t"
    "ds_read_b128 v[100:103], %[ea] offset:10240\n\t"
    "v_mfma_f32_16x16x32_bf16 v[88:91], v[136:139], v[108:111], v[88:91]\n\t"
    "v_mfma_f32_16x16x32_bf16 v[92:95], v[140:143], v[108:111], v[92:95]\n\t"
    "s_waitcnt lgkmcnt(4)\n\t"
    "v_mfma_f32_16x16x32_bf16 v[80:83], v[144:147], v[112:115], v[80:83]\n\t"
    "ds_read_b128 v[104:107], %[da] offset:8192\n\t"
    "v_mfma_f32_16x16x32_bf16 v[84:87], v[148:151], v[112:115], v[84:87]\n\t"
    "ds_read_b128 v[108:111], %[da] offset:10240\n\t"
    "v_mfma_f32_16x16x32_bf16 v[88:91], v[144:147], v[116:119], v[88:91]\n\t"
    "v_mfma_f32_16x16x32_bf16 v[92:95], v[148:151], v[116:119], v[92:95]\n\t"
    "s_waitcnt lgkmcnt(4)\n\t"
    "v_mfma_f32_16x16x32_bf16 v[80:83], v[152:155], v[120:123], v[80:83]\n\t"
    "ds_read_b128 v[112:115], %[ea] offset:12288\n\t"
    "v_mfma_f32_16x16x32_bf16 v[84:87], v[156:159], v[120:123], v[84:87]\n\t"
    "ds_read_b128 v[116:119], %[ea] offset:14336\n\t"
    "v_mfma_f32_16x16x32_bf16 v[88:91], v[152:155], v[124:127], v[88:91]\n\t"
    "v_mfma_f32_16x16x32_bf16 v[92:95], v[156:159], v[124:127], v[92:95]\n\t"
    "s_waitcnt lgkmcnt(4)\n\t"
    "v_mfma_f32_16x16x32_bf16 v[80:83], v[160:163], v[96:99], v[80:83]\n\t"
    "ds_read_b128 v[120:123], %[da] offset:12288\n\t"
    "v_mfma_f32_16x16x32_bf16 v[84:87], v[164:167], v[96:99], v[84:87]\n\t"
    "ds_read_b128 v[124:127], %[da] offset:14336\n\t"
    "v_mfma_f32_16x16x32_bf16 v[88:91], v[160:163], v[100:103], v[88:91]\n\t"
    "v_mfma_f32_16x16x32_bf16 v[92:95], v[164:167], v[100:103], v[92:95]\n\t"
    "s_waitcnt vmcnt(20)\n\t"
    "s_cmp_lt_u32 %[x], 2\n\t"
    "s_cbranch_scc0 45f\n\t"
    "s_waitcnt vmcnt(4)\n\t"
    "45:\n\t"
    "s_barrier\n\t"
    "s_waitcnt lgkmcnt(4)\n\t"
    "v_mfma_f32_16x16x32_bf16 v[80:83], v[168:171], v[104:107], v[80:83]\n\t"
    "ds_read_b128 v[96:99], %[ea] offset:16384\n\t"
    "v_mfma_f32_16x16x32_bf16 v[84:87], v[172:175], v[104:107], v[84:87]\n\t"
    "ds_read_b128 v[100:103], %[ea] offset:18432\n\t"
    "v_mfma_f32_16x16x32_bf16 v[88:91], v[168:171], v[108:111], v[88:91]\n\t"
    "v_mfma_f32_16x16x32_bf16 v[92:95], v[172:175], v[108:111], v[92:95]\n\t"
    "s_waitcnt lgkmcnt(4)\n\t"
    "v_mfma_f32_16x16x32_bf16 v[80:83], v[176:179], v[112:115], v[80:83]\n\t"
    "ds_read_b128 v[104:107], %[da] offset:16384\n\t"
    "v_mfma_f32_16x16x32_bf16 v[84:87], v[180:183], v[112:115], v[84:87]\n\t"
    "ds_read_b128 v[108:111], %[da] offset:18432\n\t"
    "v_mfma_f32_16x16x32_bf16 v[88:91], v[176:179], v[116:119], v[88:91]\n\t"
    "v_mfma_f32_16x16x32_bf16 v[92:95], v[180:183], v[116:119], v[92:95]\n\t"
    "s_waitcnt lgkmcnt(4)\n\t"
    "v_mfma_f32_16x16x32_bf16 v[80:83], v[184:187], v[120:123], v[80:83]\n\t"
    "ds_read_b128 v[112:115], %[ea] offset:20480\n\t"
    "v_mfma_f32_16x16x32_bf16 v[84:87], v[188:191], v[120:123], v[84:87]\n\t"
    "ds_read_b128 v[116:119], %[ea] offset:22528\n\t"
    "v_mfma_f32_16x16x32_bf16 v[88:91], v[184:187], v[124:127], v[88:91]\n\t"
    "v_mfma_f32_16x16x32_bf16 v[92:95], v[188:191], v[124:127], v[92:95]\n\t"
    "s_waitcnt lgkmcnt(4)\n\t"
    "v_mfma_f32_16x16x32_bf16 v[80:83], v[192:195], v[96:99], v[80:83]\n\t"
    "ds_read_b128 v[120:123], %[da] offset:20480\n\t"
    "v_mfma_f32_16x16x32_bf16 v[84:87], v[196:199], v[96:99], v[84:87]\n\t"
    "ds_read_b128 v[124:127], %[da] offset:22528\n\t"
    "v_mfma_f32_16x16x32_bf16 v[88:91], v[192:195], v[100:103], v[88:91]\n\t"
    "v_mfma_f32_16x16x32_bf16 v[92:95], v[196:199], v[100:103], v[92:95]\n\t"
    "s_waitcnt vmcnt(16)\n\t"
    "s_cmp_lt_u32 %[x], 2\n\t"
    "s_cbranch_scc0 49f\n\t"
    "s_waitcnt vmcnt(0)\n\t"
    "49:\n\t"
    "s_barrier\n\t"
    "s_waitcnt lgkmcnt(4)\n\t"
    "v_mfma_f32_16x16x32_bf16 v[80:83], v[200:203], v[104:107], v[80:83]\n\t"
    "ds_read_b128 v[96:99], %[ea] offset:24576\n\t"
    "v_mfma_f32_16x16x32_bf16 v[84:87], v[204:207], v[104:107], v[84:87]\n\t"
    "ds_read_b128 v[100:103], %[ea] offset:26624\n\t"
    "v_mfma_f32_16x16x32_bf16 v[88:91], v[200:203], v[108:111], v[88:91]\n\t"
    "v_mfma_f32_16x16x32_bf16 v[92:95], v[204:207], v[108:111], v[92:95]\n\t"
    "s_waitcnt lgkmcnt(4)\n\t"
    "v_mfma_f32_16x16x32_bf16 v[80:83], v[208:211], v[112:115], v[80:83]\n\t"
    "ds_read_b128 v[104:107], %[da] offset:24576\n\t"
    "v_mfma_f32_16x16x32_bf16 v[84:87], v[212:215], v[112:115], v[84:87]\n\t"
    "ds_read_b128 v[108:111], %[da] offset:26624\n\t"
    "v_mfma_f32_16x16x32_bf16 v[88:91], v[208:211], v[116:119], v[88:91]\n\t"
    "v_mfma_f32_16x16x32_bf16 v[92:95], v[212:215], v[116:119], v[92:95]\n\t"
    "s_cmp_eq_u32 %[ps], 0\n\t"
    "s_cbranch_scc1 10f\n\t"
    "global_load_dword %[sv], %[dv], %[db] sc1\n\t"
    "10:\n\t"
    "s_waitcnt lgkmcnt(4)\n\t"
    "v_mfma_f32_16x16x32_bf16 v[80:83], v[216:219], v[120:123], v[80:83]\n\t"
    "ds_read_b128 v[112:115], %[ea] offset:28672\n\t"
    "v_mfma_f32_16x16x32_bf16 v[84:87], v[220:223], v[120:123], v[84:87]\n\t"
    "ds_read_b128 v[116:119], %[ea] offset:30720\n\t"
    "v_mfma_f32_16x16x32_bf16 v[88:91], v[216:219], v[124:127], v[88:91]\n\t"
    "v_mfma_f32_16x16x32_bf16 v[92:95], v[220:223], v[124:127], v[92:95]\n\t"
    "s_waitcnt lgkmcnt(4)\n\t"
    "v_mfma_f32_16x16x32_bf16 v[80:83], v[224:227], v[96:99], v[80:83]\n\t"
    "ds_read_b128 v[120:123], %[da] offset:28672\n\t"
    "v_mfma_f32_16x16x32_bf16 v[84:87], v[228:231], v[96:99], v[84:87]\n\t"
    "ds_read_b128 v[124:127], %[da] offset:30720\n\t"
    "v_mfma_f32_16x16x32_bf16 v[88:91], v[224:227], v[100:103], v[88:91]\n\t"
    "v_mfma_f32_16x16x32_bf16 v[92:95], v[228:231], v[100:103], v[92:95]\n\t"
    "s_waitcnt lgkmcnt(4)\n\t"
    "v_mfma_f32_16x16x32_bf16 v[80:83], v[232:235], v[104:107], v[80:83]\n\t"
    "v_mfma_f32_16x16x32_bf16 v[84:87], v[236:239], v[104:107], v[84:87]\n\t"
    "v_mfma_f32_16x16x32_bf16 v[88:91], v[232:235], v[108:111], v[88:91]\n\t"
    "v_mfma_f32_16x16x32_bf16 v[92:95], v[236:239], v[108:111], v[92:95]\n\t"
    "s_waitcnt lgkmcnt(2)\n\t"
    "v_mfma_f32_16x16x32_bf16 v[80:83], v[240:243], v[112:115], v[80:83]\n\t"
    "v_mfma_f32_16x16x32_bf16 v[84:87], v[244:247], v[112:115], v[84:87]\n\t"
    "v_mfma_f32_16x16x32_bf16 v[88:91], v[240:243], v[116:119], v[88:91]\n\t"
    "v_mfma_f32_16x16x32_bf16 v[92:95], v[244:247], v[116:119], v[92:95]\n\t"
    "s_waitcnt lgkmcnt(0)\n\t"
    "v_mfma_f32_16x16x32_bf16 v[80:83], v[248:251], v[120:123], v[80:83]\n\t"
    "v_mfma_f32_16x16x32_bf16 v[84:87], v[252:255], v[120:123], v[84:87]\n\t"
    "v_mfma_f32_16x16x32_bf16 v[88:91], v[248:251], v[124:127], v[88:91]\n\t"
    "v_mfma_f32_16x16x32_bf16 v[92:95], v[252:255], v[124:127], v[92:95]\n\t"
    "s_mov_b32 %[got], 0\n\t"
    "v_mov_b32 v76, %[fa]\n\t"
    "v_mov_b32 v77, %[fv]\n\t"
    "v_add_u32 v78, 1, v77\n\t"
    "v_mov_b32 v79, %[ga]\n\t"
    "ds_write_b128 %[rw], v[80:83] offset:0\n\t"
    "ds_write_b128 %[rw], v[84:87] offset:1024\n\t"
    "ds_write_b128 %[rw], v[88:91] offset:2048\n\t"
    "ds_write_b128 %[rw], v[92:95] offset:3072\n\t"
    "ds_write_b32 v76, v77\n\t"
    "s_cmp_eq_u32 %[ps], 0\n\t"
    "s_cbranch_scc1 9f\n\t"
    "s_waitcnt vmcnt(0)\n\t"
    "v_mad_u32_u24 v76, %[sv], %[mu], %[ad]\n\t"
    "v_cmp_ge_i32 vcc, %[th], v76\n\t"
    "s_cbranch_vccnz 9f\n\t"
    "ds_write2_b32 v79, v78, v78 offset1:1\n\t"
    "s_add_u32 m0, %[lb], 0\n\t"
    "s_nop 0\n\t"
    "global_load_lds_dwordx4 %[av0], %[s0] sc1\n\t"
    "s_add_u32 m0, %[lb], 32768\n\t"
    "s_nop 0\n\t"
    "global_load_lds_dwordx4 %[av1], %[s0] sc1\n\t"
    "s_add_u32 m0, %[lb], 4096\n\t"
    "s_nop 0\n\t"
    "global_load_lds_dwordx4 %[av2], %[s0] sc1\n\t"
    "s_add_u32 m0, %[lb], 36864\n\t"
    "s_nop 0\n\t"
    "global_load_lds_dwordx4 %[av3], %[s0] sc1\n\t"
    "s_add_u32 m0, %[lb], 8192\n\t"
    "s_nop 0\n\t"
    "global_load_lds_dwordx4 %[av4], %[s0] sc1\n\t"
    "s_add_u32 m0, %[lb], 40960\n\t"
    "s_nop 0\n\t"
    "global_load_lds_dwordx4 %[av5], %[s0] sc1\n\t"
    "s_add_u32 m0, %[lb], 12288\n\t"
    "s_nop 0\n\t"
    "global_load_lds_dwordx4 %[av6], %[s0] sc1\n\t"
    "s_add_u32 m0, %[lb], 45056\n\t"
    "s_nop 0\n\t"
    "global_load_lds_dwordx4 %[av7], %[s0] sc1\n\t"
    "s_add_u32 m0, %[lb], 16384\n\t"
    "s_nop 0\n\t"
    "global_load_lds_dwordx4 %[av0], %[s1] sc1\n\t"
    "s_add_u32 m0, %[lb], 49152\n\t"
    "s_nop 0\n\t"
    "global_load_lds_dwordx4 %[av1], %[s1] sc1\n\t"
    "s_add_u32 m0, %[lb], 20480\n\t"
    "s_nop 0\n\t"
    "global_load_lds_dwordx4 %[av2], %[s1] sc1\n\t"
    "s_add_u32 m0, %[lb], 53248\n\t"
    "s_nop 0\n\t"
    "global_load_lds_dwordx4 %[av3], %[s1] sc1\n\t"
    "s_add_u32 m0, %[lb], 24576\n\t"
    "s_nop 0\n\t"
    "global_load_lds_dwordx4 %[av4], %[s1] sc1\n\t"
    "s_add_u32 m0, %[lb], 57344\n\t"
    "s_nop 0\n\t"
    "global_load_lds_dwordx4 %[av5], %[s1] sc1\n\t"
    "s_add_u32 m0, %[lb], 28672\n\t"
    "s_nop 0\n\t"
    "global_load_lds_dwordx4 %[av6], %[s1] sc1\n\t"
    "s_add_u32 m0, %[lb], 61440\n\t"
    "s_nop 0\n\t"
    "global_load_lds_dwordx4 %[av7], %[s1] sc1\n\t"
    "s_mov_b32 %[got], 1\n\t"
    "9:"
    : [sv] "+&v"(sv), [got] "=&s"(got)
    : [ea] "v"(ea), [da] "v"(da), [x] "s"(x), "{v[128:159]}"(W[0]), "{v[160:191]}"(W[1]), "{v[192:223]}"(W[2]), "{v[224:255]}"(W[3]), [lb] "s"(lb), [s0] "s"(s0), [s1] "s"(s1), [av0] "v"(av[0]), [av1] "v"(av[1]), [av2] "v"(av[2]), [av3] "v"(av[3]), [av4] "v"(av[4]), [av5] "v"(av[5]), [av6] "v"(av[6]), [av7] "v"(av[7]), [ps] "s"(ps), [dv] "v"(dv), [db] "s"(db), [rw] "v"(rw), [fa] "s"(fa), [fv] "s"(fv), [ga] "s"(ga), [mu] "s"(mu), [ad] "v"(ad), [th] "s"(th)
    : "v96", "v97", "v98", "v99", "v100", "v101", "v102", "v103", "v104", "v105", "v106", "v107", "v108", "v109", "v110", "v111", "v112", "v113", "v114", "v115", "v116", "v117", "v118", "v119", "v120", "v121", "v122", "v123", "v124", "v125", "v126", "v127", "v76", "v77", "v78", "v79", "v80", "v81", "v82", "v83", "v84", "v85", "v86", "v87", "v88", "v89", "v90", "v91", "v92", "v93", "v94", "v95", "vcc", "scc", "m0", "memory");
}
__device__ __forceinline__ void lean_dm0(const u32x32 (&W)[4], unsigned ea, unsigned da, unsigned x, unsigned lb, const char* s0, const char* s1, const unsigned (&av)[8], unsigned ps, unsigned dv, const char* db, unsigned rw, unsigned fa, unsigned fv,
    unsigned ga, unsigned mu, unsigned ad, int th, unsigned& sv, unsigned& got) {
  asm volatile("s_setprio 2\n\t"
    "ds_read_b128 v[96:99], %[ea] offset:0\n\t"
    "ds_read_b128 v[100:103], %[ea] offset:2048\n\t"
    "ds_read_b128 v[104:107], %[da] offset:0\n\t"
    "ds_read_b128 v[108:111], %[da] offset:2048\n\t"
    "ds_read_b128 v[112:115], %[ea] offset:4096\n\t"
    "ds_read_b128 v[116:119], %[ea] offset:6144\n\t"
    "s_waitcnt lgkmcnt(4)\n\t"
    "v_mfma_f32_16x16x32_bf16 v[80:83], v[128:131], v[96:99], 0\n\t"
    "ds_read_b128 v[120:123], %[da] offset:4096\n\t"
    "s_add_u32 m0, %[lb], 0\n\t"
    "v_mfma_f32_16x16x32_bf16 v[84:87], v[132:135], v[96:99], 0\n\t"
    "ds_read_b128 v[124:127], %[da] offset:6144\n\t"
    "global_load_lds_dwordx4 %[av0], %[s0] sc1\n\t"
    "v_mfma_f32_16x16x32_bf16 v[88:91], v[128:131], v[100:103], 0\n\t"
    "s_add_u32 m0, %[lb], 32768\n\t"
    "v_mfma_f32_16x16x32_bf16 v[92:95], v[132:135], v[100:103], 0\n\t"
    "global_load_lds_dwordx4 %[av1], %[s0] sc1\n\t"
    "s_waitcnt vmcnt(10)\n\t"
    "s_barrier\n\t"
    "s_waitcnt lgkmcnt(4)\n\t"
    "v_mfma_f32_16x16x32_bf16 v[80:83], v[136:139], v[104:107], v[80:83]\n\t"
    "ds_read_b128 v[96:99], %[ea] offset:8192\n\t"
    "s_add_u32 m0, %[lb], 4096\n\t"
    "v_mfma_f32_16x16x32_bf16 v[84:87], v[140:143], v[104:107], v[84:87]\n\t"
    "ds_read_b128 v[100:103], %[ea] offset:10240\n\t"
    "global_load_lds_dwordx4 %[av2], %[s0] sc1\n\t"
    "v_mfma_f32_16x16x32_bf16 v[88:91], v[136:139], v[108:111], v[88:91]\n\t"
    "s_add_u32 m0, %[lb], 36864\n\t"
    "v_mfma_f32_16x16x32_bf16 v[92:95], v[140:143], v[108:111], v[92:95]\n\t"
    "global_load_lds_dwordx4 %[av3], %[s0] sc1\n\t"
    "s_waitcnt lgkmcnt(4)\n\t"
    "v_mfma_f32_16x16x32_bf16 v[80:83], v[144:147], v[112:115], v[80:83]\n\t"
    "ds_read_b128 v[104:107], %[da] offset:8192\n\t"
    "s_add_u32 m0, %[lb], 8192\n\t"
    "v_mfma_f32_16x16x32_bf16 v[84:87], v[148:151], v[112:115], v[84:87]\n\t"
    "ds_read_b128 v[108:111], %[da] offset:10240\n\t"
    "global_load_lds_dwordx4 %[av4], %[s0] sc1\n\t"
    "v_mfma_f32_16x16x32_bf16 v[88:91], v[144:147], v[116:119], v[88:91]\n\t"
    "s_add_u32 m0, %[lb], 40960\n\t"
    "v_mfma_f32_16x16x32_bf16 v[92:95], v[148:151], v[116:119], v[92:95]\n\t"
    "global_load_lds_dwordx4 %[av5], %[s0] sc1\n\t"
    "s_waitcnt lgkmcnt(4)\n\t"
    "v_mfma_f32_16x16x32_bf16 v[80:83], v[152:155], v[120:123], v[80:83]\n\t"
    "ds_read_b128 v[112:115], %[ea] offset:12288\n\t"
    "s_add_u32 m0, %[lb], 12288\n\t"
    "v_mfma_f32_16x16x32_bf16 v[84:87], v[156:159], v[120:123], v[84:87]\n\t"
    "ds_read_b128 v[116:119], %[ea] offset:14336\n\t"
    "global_load_lds_dwordx4 %[av6], %[s0] sc1\n\t"
    "v_mfma_f32_16x16x32_bf16 v[88:91], v[152:155], v[124:127], v[88:91]\n\t"
    "s_add_u32 m0, %[lb], 45056\n\t"
    "v_mfma_f32_16x16x32_bf16 v[92:95], v[156:159], v[124:127], v[92:95]\n\t"
    "global_load_lds_dwordx4 %[av7], %[s0] sc1\n\t"
    "s_waitcnt lgkmcnt(4)\n\t"
    "v_mfma_f32_16x16x32_bf16 v[80:83], v[160:163], v[96:99], v[80:83]\n\t"
    "ds_read_b128 v[120:123], %[da] offset:12288\n\t"
    "s_add_u32 m0, %[lb], 16384\n\t"
    "v_mfma_f32_16x16x32_bf16 v[84:87], v[164:167], v[96:99], v[84:87]\n\t"
    "ds_read_b128 v[124:127], %[da] offset:14336\n\t"
    "global_load_lds_dwordx4 %[av0], %[s1] sc1\n\t"
    "v_mfma_f32_16x16x32_bf16 v[88:91], v[160:163], v[100:103], v[88:91]\n\t"
    "s_add_u32 m0, %[lb], 49152\n\t"
    "v_mfma_f32_16x16x32_bf16 v[92:95], v[164:167], v[100:103], v[92:95]\n\t"
    "global_load_lds_dwordx4 %[av1], %[s1] sc1\n\t"
    "s_waitcnt vmcnt(14)\n\t"
    "s_barrier\n\t"
    "s_waitcnt lgkmcnt(4)\n\t"
    "v_mfma_f32_16x16x32_bf16 v[80:83], v[168:171], v[104:107], v[80:83]\n\t"
    "ds_read_b128 v[96:99], %[ea] offset:16384\n\t"
    "s_add_u32 m0, %[lb], 20480\n\t"
    "v_mfma_f32_16x16x32_bf16 v[84:87], v[172:175], v[104:107], v[84:87]\n\t"
    "ds_read_b128 v[100:103], %[ea] offset:18432\n\t"
    "global_load_lds_dwordx4 %[av2], %[s1] sc1\n\t"
    "v_mfma_f32_16x16x32_bf16 v[88:91], v[168:171], v[108:111], v[88:91]\n\t"
    "s_add_u32 m0, %[lb], 53248\n\t"
    "v_mfma_f32_16x16x32_bf16 v[92:95], v[172:175], v[108:111], v[92:95]\n\t"
    "global_load_lds_dwordx4 %[av3], %[s1] sc1\n\t"
    "s_waitcnt lgkmcnt(4)\n\t"
    "v_mfma_f32_16x16x32_bf16 v[80:83], v[176:179], v[112:115], v[80:83]\n\t"
    "ds_read_b128 v[104:107], %[da] offset:16384\n\t"
    "s_add_u32 m0, %[lb], 24576\n\t"
    "v_mfma_f32_16x16x32_bf16 v[84:87], v[180:183], v[112:115], v[84:87]\n\t"
    "ds_read_b128 v[108:111], %[da] offset:18432\n\t"
    "global_load_lds_dwordx4 %[av4], %[s1] sc1\n\t"
    "v_mfma_f32_16x16x32_bf16 v[88:91], v[176:179], v[116:119], v[88:91]\n\t"
    "s_add_u32 m0, %[lb], 57344\n\t"
    "v_mfma_f32_16x16x32_bf16 v[92:95], v[180:183], v[116:119], v[92:95]\n\t"
    "global_load_lds_dwordx4 %[av5], %[s1] sc1\n\t"
    "s_waitcnt lgkmcnt(4)\n\t"
    "v_mfma_f32_16x16x32_bf16 v[80:83], v[184:187], v[120:123], v[80:83]\n\t"
    "ds_read_b128 v[112:115], %[ea] offset:20480\n\t"
    "s_add_u32 m0, %[lb], 28672\n\t"
    "v_mfma_f32_16x16x32_bf16 v[84:87], v[188:191], v[120:123], v[84:87]\n\t"
    "ds_read_b128 v[116:119], %[ea] offset:22528\n\t"
    "global_load_lds_dwordx4 %[av6], %[s1] sc1\n\t"
    "v_mfma_f32_16x16x32_bf16 v[88:91], v[184:187], v[124:127], v[88:91]\n\t"
    "s_add_u32 m0, %[lb], 61440\n\t"
    "v_mfma_f32_16x16x32_bf16 v[92:95], v[188:191], v[124:127], v[92:95]\n\t"
    "global_load_lds_dwordx4 %[av7], %[s1] sc1\n\t"
    "s_waitcnt lgkmcnt(4)\n\t"
    "v_mfma_f32_16x16x32_bf16 v[80:83], v[192:195], v[96:99], v[80:83]\n\t"
    "ds_read_b128 v[120:123], %[da] offset:20480\n\t"
    "v_mfma_f32_16x16x32_bf16 v[84:87], v[196:199], v[96:99], v[84:87]\n\t"
    "ds_read_b128 v[124:127], %[da] offset:22528\n\t"
    "v_mfma_f32_16x16x32_bf16 v[88:91], v[192:195], v[100:103], v[88:91]\n\t"
    "v_mfma_f32_16x16x32_bf16 v[92:95], v[196:199], v[100:103], v[92:95]\n\t"
    "s_waitcnt vmcnt(16)\n\t"
    "s_barrier\n\t"
    "s_waitcnt lgkmcnt(4)\n\t"
    "v_mfma_f32_16x16x32_bf16 v[80:83], v[200:203], v[104:107], v[80:83]\n\t"
    "ds_read_b128 v[96:99], %[ea] offset:24576\n\t"
    "v_mfma_f32_16x16x32_bf16 v[84:87], v[204:207], v[104:107], v[84:87]\n\t"
    "ds_read_b128 v[100:103], %[ea] offset:26624\n\t"
    "v_mfma_f32_16x16x32_bf16 v[88:91], v[200:203], v[108:111], v[88:91]\n\t"
    "v_mfma_f32_16x16x32_bf16 v[92:95], v[204:207], v[108:111], v[92:95]\n\t"
    "s_waitcnt lgkmcnt(4)\n\t"
    "v_mfma_f32_16x16x32_bf16 v[80:83], v[208:211], v[112:115], v[80:83]\n\t"
    "ds_read_b128 v[104:107], %[da] offset:24576\n\t"
    "v_mfma_f32_16x16x32_bf16 v[84:87], v[212:215], v[112:115], v[84:87]\n\t"
    "ds_read_b128 v[108:111], %[da] offset:26624\n\t"
    "v_mfma_f32_16x16x32_bf16 v[88:91], v[208:211], v[116:119], v[88:91]\n\t"
    "v_mfma_f32_16x16x32_bf16 v[92:95], v[212:215], v[116:119], v[92:95]\n\t"
    "s_cmp_eq_u32 %[ps], 0\n\t"
    "s_cbranch_scc1 10f\n\t"
    "global_load_dword %[sv], %[dv], %[db] sc1\n\t"
    "10:\n\t"
    "s_waitcnt lgkmcnt(4)\n\t"
    "v_mfma_f32_16x16x32_bf16 v[80:83], v[216:219], v[120:123], v[80:83]\n\t"
    "ds_read_b128 v[112:115], %[ea] offset:28672\n\t"
    "v_mfma_f32_16x16x32_bf16 v[84:87], v[220:223], v[120:123], v[84:87]\n\t"
    "ds_read_b128 v[116:119], %[ea] offset:30720\n\t"
    "v_mfma_f32_16x16x32_bf16 v[88:91], v[216:219], v[124:127], v[88:91]\n\t"
    "v_mfma_f32_16x16x32_bf16 v[92:95], v[220:223], v[124:127], v[92:95]\n\t"
    "s_waitcnt lgkmcnt(4)\n\t"
    "v_mfma_f32_16x16x32_bf16 v[80:83], v[224:227], v[96:99], v[80:83]\n\t"
    "ds_read_b128 v[120:123], %[da] offset:28672\n\t"
    "v_mfma_f32_16x16x32_bf16 v[84:87], v[228:231], v[96:99], v[84:87]\n\t"
    "ds_read_b128 v[124:127], %[da] offset:30720\n\t"
    "v_mfma_f32_16x16x32_bf16 v[88:91], v[224:227], v[100:103], v[88:91]\n\t"
    "v_mfma_f32_16x16x32_bf16 v[92:95], v[228:231], v[100:103], v[92:95]\n\t"
    "s_waitcnt lgkmcnt(4)\n\t"
    "v_mfma_f32_16x16x32_bf16 v[80:83], v[232:235], v[104:107], v[80:83]\n\t"
    "v_mfma_f32_16x16x32_bf16 v[84:87], v[236:239], v[104:107], v[84:87]\n\t"
    "v_mfma_f32_16x16x32_bf16 v[88:91], v[232:235], v[108:111], v[88:91]\n\t"
    "v_mfma_f32_16x16x32_bf16 v[92:95], v[236:239], v[108:111], v[92:95]\n\t"
    "s_waitcnt lgkmcnt(2)\n\t"
    "v_mfma_f32_16x16x32_bf16 v[80:83], v[240:243], v[112:115], v[80:83]\n\t"
    "v_mfma_f32_16x16x32_bf16 v[84:87], v[244:247], v[112:115], v[84:87]\n\t"
    "v_mfma_f32_16x16x32_bf16 v[88:91], v[240:243], v[116:119], v[88:91]\n\t"
    "v_mfma_f32_16x16x32_bf16 v[92:95], v[244:247], v[116:119], v[92:95]\n\t"
    "s_waitcnt lgkmcnt(0)\n\t"
    "v_mfma_f32_16x16x32_bf16 v[80:83], v[248:251], v[120:123], v[80:83]\n\t"
    "v_mfma_f32_16x16x32_bf16 v[84:87], v[252:255], v[120:123], v[84:87]\n\t"
    "v_mfma_f32_16x16x32_bf16 v[88:91], v[248:251], v[124:127], v[88:91]\n\t"
    "v_mfma_f32_16x16x32_bf16 v[92:95], v[252:255], v[124:127], v[92:95]\n\t"
    "s_mov_b32 %[got], 0\n\t"
    "v_mov_b32 v76, %[fa]\n\t"
    "v_mov_b32 v77, %[fv]\n\t"
    "v_add_u32 v78, 1, v77\n\t"
    "v_mov_b32 v79, %[ga]\n\t"
    "ds_write_b128 %[rw], v[80:83] offset:0\n\t"
    "ds_write_b128 %[rw], v[84:87] offset:1024\n\t"
    "ds_write_b128 %[rw], v[88:91] offset:2048\n\t"
    "ds_write_b128 %[rw], v[92:95] offset:3072\n\t"
    "ds_write_b32 v76, v77\n\t"
    "s_cmp_eq_u32 %[ps], 0\n\t"
    "s_cbranch_scc1 9f\n\t"
    "s_waitcnt vmcnt(0)\n\t"
    "9:"
    : [sv] "+&v"(sv), [got] "=&s"(got)
    : [ea] "v"(ea), [da] "v"(da), [x] "s"(x), "{v[128:159]}"(W[0]), "{v[160:191]}"(W[1]), "{v[192:223]}"(W[2]), "{v[224:255]}"(W[3]), [lb] "s"(lb), [s0] "s"(s0), [s1] "s"(s1), [av0] "v"(av[0]), [av1] "v"(av[1]), [av2] "v"(av[2]), [av3] "v"(av[3]), [av4] "v"(av[4]), [av5] "v"(av[5]), [av6] "v"(av[6]), [av7] "v"(av[7]), [ps] "s"(ps), [dv] "v"(dv), [db] "s"(db), [rw] "v"(rw), [fa] "s"(fa), [fv] "s"(fv), [ga] "s"(ga), [mu] "s"(mu), [ad] "v"(ad), [th] "s"(th)
    : "v96", "v97", "v98", "v99", "v100", "v101", "v102", "v103", "v104", "v105", "v106", "v107", "v108", "v109", "v110", "v111", "v112", "v113", "v114", "v115", "v116", "v117", "v118", "v119", "v120", "v121", "v122", "v123", "v124", "v125", "v126", "v127", "v76", "v77", "v78", "v79", "v80", "v81", "v82", "v83", "v84", "v85", "v86", "v87", "v88", "v89", "v90", "v91", "v92", "v93", "v94", "v95", "vcc", "scc", "m0", "memory");
}
__device__ __forceinline__ void lean_ld1(const u32x32 (&W)[4], unsigned ea, unsigned da, unsigned x, unsigned lb, const char* s0, const char* s1, const unsigned (&av)[8], unsigned ps, unsigned dv, const char* db, unsigned rw, unsigned fa, unsigned fv,
    unsigned ga, unsigned mu, unsigned ad, int th, unsigned& sv, unsigned& got) {
  asm volatile("s_setprio 2\n\t"
    "ds_read_b128 v[96:99], %[ea] offset:0\n\t"
    "ds_read_b128 v[100:103], %[ea] offset:2048\n\t"
    "ds_read_b128 v[104:107], %[da] offset:0\n\t"
    "ds_read_b128 v[108:111], %[da] offset:2048\n\t"
    "ds_read_b128 v[112:115], %[ea] offset:4096\n\t"
    "ds_read_b128 v[116:119], %[ea] offset:6144\n\t"
    "s_waitcnt lgkmcnt(4)\n\t"
    "v_mfma_f32_16x16x32_bf16 v[80:83], v[128:131], v[96:99], 0\n\t"
    "ds_read_b128 v[120:123], %[da] offset:4096\n\t"
    "v_mfma_f32_16x16x32_bf16 v[84:87], v[132:135], v[96:99], 0\n\t"
    "ds_read_b128 v[124:127], %[da] offset:6144\n\t"
    "v_mfma_f32_16x16x32_bf16 v[88:91], v[128:131], v[100:103], 0\n\t"
    "v_mfma_f32_16x16x32_bf16 v[92:95], v[132:135], v[100:103], 0\n\t"
    "s_waitcnt vmcnt(12)\n\t"
    "s_cmp_lt_u32 %[x], 2\n\t"
    "s_cbranch_scc0 41f\n\t"
    "s_waitcnt vmcnt(4)\n\t"
    "41:\n\t"
    "s_barrier\n\t"
    "s_waitcnt lgkmcnt(4)\n\t"
    "v_mfma_f32_16x16x32_bf16 v[80:83], v[136:139], v[104:107], v[80:83]\n\t"
    "ds_read_b128 v[96:99], %[ea] offset:8192\n\t"
    "v_mfma_f32_16x16x32_bf16 v[84:87], v[140:143], v[104:107], v[84:87]\n\t"
    "ds_read_b128 v[100:103], %[ea] offset:10240\n\t"
    "v_mfma_f32_16x16x32_bf16 v[88:91], v[136:139], v[108:111], v[88:91]\n\t"
    "v_mfma_f32_16x16x32_bf16 v[92:95], v[140:143], v[108:111], v[92:95]\n\t"
    "s_waitcnt lgkmcnt(4)\n\t"
    "v_mfma_f32_16x16x32_bf16 v[80:83], v[144:147], v[112:115], v[80:83]\n\t"
    "ds_read_b128 v[104:107], %[da] offset:8192\n\t"
    "v_mfma_f32_16x16x32_bf16 v[84:87], v[148:151], v[112:115], v[84:87]\n\t"
    "ds_read_b128 v[108:111], %[da] offset:10240\n\t"
    "v_mfma_f32_16x16x32_bf16 v[88:91], v[144:147], v[116:119], v[88:91]\n\t"
    "v_mfma_f32_16x16x32_bf16 v[92:95], v[148:151], v[116:119], v[92:95]\n\t"
    "s_waitcnt lgkmcnt(4)\n\t"
    "v_mfma_f32_16x16x32_bf16 v[80:83], v[152:155], v[120:123], v[80:83]\n\t"
    "ds_read_b128 v[112:115], %[ea] offset:12288\n\t"
    "v_mfma_f32_16x16x32_bf16 v[84:87], v[156:159], v[120:123], v[84:87]\n\t"
    "ds_read_b128 v[116:119], %[ea] offset:14336\n\t"
    "v_mfma_f32_16x16x32_bf16 v[88:91], v[152:155], v[124:127], v[88:91]\n\t"
    "v_mfma_f32_16x16x32_bf16 v[92:95], v[156:159], v[124:127], v[92:95]\n\t"
    "s_waitcnt lgkmcnt(4)\n\t"
    "v_mfma_f32_16x16x32_bf16 v[80:83], v[160:163], v[96:99], v[80:83]\n\t"
    "ds_read_b128 v[120:123], %[da] offset:12288\n\t"
    "v_mfma_f32_16x16x32_bf16 v[84:87], v[164:167], v[96:99], v[84:87]\n\t"
    "ds_read_b128 v[124:127], %[da] offset:14336\n\t"
    "v_mfma_f32_16x16x32_bf16 v[88:91], v[160:163], v[100:103], v[88:91]\n\t"
    "v_mfma_f32_16x16x32_bf16 v[92:95], v[164:167], v[100:103], v[92:95]\n\t"
    "s_waitcnt vmcnt(10)\n\t"
    "s_cmp_lt_u32 %[x], 2\n\t"
    "s_cbranch_scc0 45f\n\t"
    "s_waitcnt vmcnt(2)\n\t"
    "45:\n\t"
    "s_barrier\n\t"
    "s_waitcnt lgkmcnt(4)\n\t"
    "v_mfma_f32_16x16x32_bf16 v[80:83], v[168:171], v[104:107], v[80:83]\n\t"
    "ds_read_b128 v[96:99], %[ea] offset:16384\n\t"
    "v_mfma_f32_16x16x32_bf16 v[84:87], v[172:175], v[104:107], v[84:87]\n\t"
    "ds_read_b128 v[100:103], %[ea] offset:18432\n\t"
    "v_mfma_f32_16x16x32_bf16 v[88:91], v[168:171], v[108:111], v[88:91]\n\t"
    "v_mfma_f32_16x16x32_bf16 v[92:95], v[172:175], v[108:111], v[92:95]\n\t"
    "s_waitcnt lgkmcnt(4)\n\t"
    "v_mfma_f32_16x16x32_bf16 v[80:83], v[176:179], v[112:115], v[80:83]\n\t"
    "ds_read_b128 v[104:107], %[da] offset:16384\n\t"
    "v_mfma_f32_16x16x32_bf16 v[84:87], v[180:183], v[112:115], v[84:87]\n\t"
    "ds_read_b128 v[108:111], %[da] offset:18432\n\t"
    "v_mfma_f32_16x16x32_bf16 v[88:91], v[176:179], v[116:119], v[88:91]\n\t"
    "v_mfma_f32_16x16x32_bf16 v[92:95], v[180:183], v[116:119], v[92:95]\n\t"
    "s_waitcnt lgkmcnt(4)\n\t"
    "v_mfma_f32_16x16x32_bf16 v[80:83], v[184:187], v[120:123], v[80:83]\n\t"
    "ds_read_b128 v[112:115], %[ea] offset:20480\n\t"
    "v_mfma_f32_16x16x32_bf16 v[84:87], v[188:191], v[120:123], v[84:87]\n\t"
    "ds_read_b128 v[116:119], %[ea] offset:22528\n\t"
    "v_mfma_f32_16x16x32_bf16 v[88:91], v[184:187], v[124:127], v[88:91]\n\t"
    "v_mfma_f32_16x16x32_bf16 v[92:95], v[188:191], v[124:127], v[92:95]\n\t"
    "s_waitcnt lgkmcnt(4)\n\t"
    "v_mfma_f32_16x16x32_bf16 v[80:83], v[192:195], v[96:99], v[80:83]\n\t"
    "ds_read_b128 v[120:123], %[da] offset:20480\n\t"
    "v_mfma_f32_16x16x32_bf16 v[84:87], v[196:199], v[96:99], v[84:87]\n\t"
    "ds_read_b128 v[124:127], %[da] offset:22528\n\t"
    "v_mfma_f32_16x16x32_bf16 v[88:91], v[192:195], v[100:103], v[88:91]\n\t"
    "v_mfma_f32_16x16x32_bf16 v[92:95], v[196:199], v[100:103], v[92:95]\n\t"
    "s_waitcnt vmcnt(8)\n\t"
    "s_cmp_lt_u32 %[x], 2\n\t"
    "s_cbranch_scc0 49f\n\t"
    "s_waitcnt vmcnt(0)\n\t"
    "49:\n\t"
    "s_barrier\n\t"
    "s_waitcnt lgkmcnt(4)\n\t"
    "v_mfma_f32_16x16x32_bf16 v[80:83], v[200:203], v[104:107], v[80:83]\n\t"
    "ds_read_b128 v[96:99], %[ea] offset:24576\n\t"
    "v_mfma_f32_16x16x32_bf16 v[84:87], v[204:207], v[104:107], v[84:87]\n\t"
    "ds_read_b128 v[100:103], %[ea] offset:26624\n\t"
    "v_mfma_f32_16x16x32_bf16 v[88:91], v[200:203], v[108:111], v[88:91]\n\t"
    "v_mfma_f32_16x16x32_bf16 v[92:95], v[204:207], v[108:111], v[92:95]\n\t"
    "s_waitcnt lgkmcnt(4)\n\t"
    "v_mfma_f32_16x16x32_bf16 v[80:83], v[208:211], v[112:115], v[80:83]\n\t"
    "ds_read_b128 v[104:107], %[da] offset:24576\n\t"
    "v_mfma_f32_16x16x32_bf16 v[84:87], v[212:215], v[112:115], v[84:87]\n\t"
    "ds_read_b128 v[108:111], %[da] offset:26624\n\t"
    "v_mfma_f32_16x16x32_bf16 v[88:91], v[208:211], v[116:119], v[88:91]\n\t"
    "v_mfma_f32_16x16x32_bf16 v[92:95], v[212:215], v[116:119], v[92:95]\n\t"
    "s_cmp_eq_u32 %[ps], 0\n\t"
    "s_cbranch_scc1 10f\n\t"
    "global_load_dword %[sv], %[dv], %[db] sc1\n\t"
    "10:\n\t"
    "s_waitcnt lgkmcnt(4)\n\t"
    "v_mfma_f32_16x16x32_bf16 v[80:83], v[216:219], v[120:123], v[80:83]\n\t"
    "ds_read_b128 v[112:115], %[ea] offset:28672\n\t"
    "v_mfma_f32_16x16x32_bf16 v[84:87], v[220:223], v[120:123], v[84:87]\n\t"
    "ds_read_b128 v[116:119], %[ea] offset:30720\n\t"
    "v_mfma_f32_16x16x32_bf16 v[88:91], v[216:219], v[124:127], v[88:91]\n\t"
    "v_mfma_f32_16x16x32_bf16 v[92:95], v[220:223], v[124:127], v[92:95]\n\t"
    "s_waitcnt lgkmcnt(4)\n\t"
    "v_mfma_f32_16x16x32_bf16 v[80:83], v[224:227], v[96:99], v[80:83]\n\t"
    "ds_read_b128 v[120:123], %[da] offset:28672\n\t"
    "v_mfma_f32_16x16x32_bf16 v[84:87], v[228:231], v[96:99], v[84:87]\n\t"
    "ds_read_b128 v[124:127], %[da] offset:30720\n\t"
    "v_mfma_f32_16x16x32_bf16 v[88:91], v[224:227], v[100:103], v[88:91]\n\t"
    "v_mfma_f32_16x16x32_bf16 v[92:95], v[228:231], v[100:103], v[92:95]\n\t"
    "s_waitcnt lgkmcnt(4)\n\t"
    "v_mfma_f32_16x16x32_bf16 v[80:83], v[232:235], v[104:107], v[80:83]\n\t"
    "v_mfma_f32_16x16x32_bf16 v[84:87], v[236:239], v[104:107], v[84:87]\n\t"
    "v_mfma_f32_16x16x32_bf16 v[88:91], v[232:235], v[108:111], v[88:91]\n\t"
    "v_mfma_f32_16x16x32_bf16 v[92:95], v[236:239], v[108:111], v[92:95]\n\t"
    "s_waitcnt lgkmcnt(2)\n\t"
    "v_mfma_f32_16x16x32_bf16 v[80:83], v[240:243], v[112:115], v[80:83]\n\t"
    "v_mfma_f32_16x16x32_bf16 v[84:87], v[244:247], v[112:115], v[84:87]\n\t"
    "v_mfma_f32_16x16x32_bf16 v[88:91], v[240:243], v[116:119], v[88:91]\n\t"
    "v_mfma_f32_16x16x32_bf16 v[92:95], v[244:247], v[116:119], v[92:95]\n\t"
    "s_waitcnt lgkmcnt(0)\n\t"
    "v_mfma_f32_16x16x32_bf16 v[80:83], v[248:251], v[120:123], v[80:83]\n\t"
    "v_mfma_f32_16x16x32_bf16 v[84:87], v[252:255], v[120:123], v[84:87]\n\t"
    "v_mfma_f32_16x16x32_bf16 v[88:91], v[248:251], v[124:127], v[88:91]\n\t"
    "v_mfma_f32_16x16x32_bf16 v[92:95], v[252:255], v[124:127], v[92:95]\n\t"
    "s_mov_b32 %[got], 0\n\t"
    "v_mov_b32 v76, %[fa]\n\t"
    "v_mov_b32 v77, %[fv]\n\t"
    "v_add_u32 v78, 1, v77\n\t"
    "v_mov_b32 v79, %[ga]\n\t"
    "ds_write_b128 %[rw], v[80:83] offset:0\n\t"
    "ds_write_b128 %[rw], v[84:87] offset:1024\n\t"
    "ds_write_b128 %[rw], v[88:91] offset:2048\n\t"
    "ds_write_b128 %[rw], v[92:95] offset:3072\n\t"
    "ds_write_b32 v76, v77\n\t"
    "s_cmp_eq_u32 %[ps], 0\n\t"
    "s_cbranch_scc1 9f\n\t"
    "s_waitcnt vmcnt(0)\n\t"
    "v_mad_u32_u24 v76, %[sv], %[mu], %[ad]\n\t"
    "v_cmp_ge_i32 vcc, %[th], v76\n\t"
    "s_cbranch_vccnz 9f\n\t"
    "ds_write2_b32 v79, v78, v78 offset1:1\n\t"
    "s_add_u32 m0, %[lb], 0\n\t"
    "s_nop 0\n\t"
    "global_load_lds_dwordx4 %[av0], %[s0] sc1\n\t"
    "s_add_u32 m0, %[lb], 4096\n\t"
    "s_nop 0\n\t"
    "global_load_lds_dwordx4 %[av2], %[s0] sc1\n\t"
    "s_add_u32 m0, %[lb], 8192\n\t"
    "s_nop 0\n\t"
    "global_load_lds_dwordx4 %[av4], %[s0] sc1\n\t"
    "s_add_u32 m0, %[lb], 12288\n\t"
    "s_nop 0\n\t"
    "global_load_lds_dwordx4 %[av6], %[s0] sc1\n\t"
    "s_add_u32 m0, %[lb], 16384\n\t"
    "s_nop 0\n\t"
    "global_load_lds_dwordx4 %[av0], %[s1] sc1\n\t"
    "s_add_u32 m0, %[lb], 20480\n\t"
    "s_nop 0\n\t"
    "global_load_lds_dwordx4 %[av2], %[s1] sc1\n\t"
    "s_add_u32 m0, %[lb], 24576\n\t"
    "s_nop 0\n\t"
    "global_load_lds_dwordx4 %[av4], %[s1] sc1\n\t"
    "s_add_u32 m0, %[lb], 28672\n\t"
    "s_nop 0\n\t"
    "global_load_lds_dwordx4 %[av6], %[s1] sc1\n\t"
    "s_mov_b32 %[got], 1\n\t"
    "9:"
    : [sv] "+&v"(sv), [got] "=&s"(got)
    : [ea] "v"(ea), [da] "v"(da), [x] "s"(x), "{v[128:159]}"(W[0]), "{v[160:191]}"(W[1]), "{v[192:223]}"(W[2]), "{v[224:255]}"(W[3]), [lb] "s"(lb), [s0] "s"(s0), [s1] "s"(s1), [av0] "v"(av[0]), [av1] "v"(av[1]), [av2] "v"(av[2]), [av3] "v"(av[3]), [av4] "v"(av[4]), [av5] "v"(av[5]), [av6] "v"(av[6]), [av7] "v"(av[7]), [ps] "s"(ps), [dv] "v"(dv), [db] "s"(db), [rw] "v"(rw), [fa] "s"(fa), [fv] "s"(fv), [ga] "s"(ga), [mu] "s"(mu), [ad] "v"(ad), [th] "s"(th)
    : "v96", "v97", "v98", "v99", "v100", "v101", "v102", "v103", "v104", "v105", "v106", "v107", "v108", "v109", "v110", "v111", "v112", "v113", "v114", "v115", "v116", "v117", "v118", "v119", "v120", "v121", "v122", "v123", "v124", "v125", "v126", "v127", "v76", "v77", "v78", "v79", "v80", "v81", "v82", "v83", "v84", "v85", "v86", "v87", "v88", "v89", "v90", "v91", "v92", "v93", "v94", "v95", "vcc", "scc", "m0", "memory");
}
__device__ __forceinline__ void lean_dm1(const u32x32 (&W)[4], unsigned ea, unsigned da, unsigned x, unsigned lb, const char* s0, const char* s1, const unsigned (&av)[8], unsigned ps, unsigned dv, const char* db, unsigned rw, unsigned fa, unsigned fv,
    unsigned ga, unsigned mu, unsigned ad, int th, unsigned& sv, unsigned& got) {
  asm volatile("s_setprio 2\n\t"
    "ds_read_b128 v[96:99], %[ea] offset:0\n\t"
    "ds_read_b128 v[100:103], %[ea] offset:2048\n\t"
    "ds_read_b128 v[104:107], %[da] offset:0\n\t"
    "ds_read_b128 v[108:111], %[da] offset:2048\n\t"
    "ds_read_b128 v[112:115], %[ea] offset:4096\n\t"
    "ds_read_b128 v[116:119], %[ea] offset:6144\n\t"
    "s_waitcnt lgkmcnt(4)\n\t"
    "v_mfma_f32_16x16x32_bf16 v[80:83], v[128:131], v[96:99], 0\n\t"
    "ds_read_b128 v[120:123], %[da] offset:4096\n\t"
    "s_add_u32 m0, %[lb], 0\n\t"
    "v_mfma_f32_16x16x32_bf16 v[84:87], v[132:135], v[96:99], 0\n\t"
    "ds_read_b128 v[124:127], %[da] offset:6144\n\t"
    "global_load_lds_dwordx4 %[av0], %[s0] sc1\n\t"
    "v_mfma_f32_16x16x32_bf16 v[88:91], v[128:131], v[100:103], 0\n\t"
    "v_mfma_f32_16x16x32_bf16 v[92:95], v[132:135], v[100:103], 0\n\t"
    "s_waitcnt vmcnt(5)\n\t"
    "s_barrier\n\t"
    "s_waitcnt lgkmcnt(4)\n\t"
    "v_mfma_f32_16x16x32_bf16 v[80:83], v[136:139], v[104:107], v[80:83]\n\t"
    "ds_read_b128 v[96:99], %[ea] offset:8192\n\t"
    "s_add_u32 m0, %[lb], 4096\n\t"
    "v_mfma_f32_16x16x32_bf16 v[84:87], v[140:143], v[104:107], v[84:87]\n\t"
    "ds_read_b128 v[100:103], %[ea] offset:10240\n\t"
    "global_load_lds_dwordx4 %[av2], %[s0] sc1\n\t"
    "v_mfma_f32_16x16x32_bf16 v[88:91], v[136:139], v[108:111], v[88:91]\n\t"
    "v_mfma_f32_16x16x32_bf16 v[92:95], v[140:143], v[108:111], v[92:95]\n\t"
    "s_waitcnt lgkmcnt(4)\n\t"
    "v_mfma_f32_16x16x32_bf16 v[80:83], v[144:147], v[112:115], v[80:83]\n\t"
    "ds_read_b128 v[104:107], %[da] offset:8192\n\t"
    "s_add_u32 m0, %[lb], 8192\n\t"
    "v_mfma_f32_16x16x32_bf16 v[84:87], v[148:151], v[112:115], v[84:87]\n\t"
    "ds_read_b128 v[108:111], %[da] offset:10240\n\t"
    "global_load_lds_dwordx4 %[av4], %[s0] sc1\n\t"
    "v_mfma_f32_16x16x32_bf16 v[88:91], v[144:147], v[116:119], v[88:91]\n\t"
    "v_mfma_f32_16x16x32_bf16 v[92:95], v[148:151], v[116:119], v[92:95]\n\t"
    "s_waitcnt lgkmcnt(4)\n\t"
    "v_mfma_f32_16x16x32_bf16 v[80:83], v[152:155], v[120:123], v[80:83]\n\t"
    "ds_read_b128 v[112:115], %[ea] offset:12288\n\t"
    "s_add_u32 m0, %[lb], 12288\n\t"
    "v_mfma_f32_16x16x32_bf16 v[84:87], v[156:159], v[120:123], v[84:87]\n\t"
    "ds_read_b128 v[116:119], %[ea] offset:14336\n\t"
    "global_load_lds_dwordx4 %[av6], %[s0] sc1\n\t"
    "v_mfma_f32_16x16x32_bf16 v[88:91], v[152:155], v[124:127], v[88:91]\n\t"
    "v_mfma_f32_16x16x32_bf16 v[92:95], v[156:159], v[124:127], v[92:95]\n\t"
    "s_waitcnt lgkmcnt(4)\n\t"
    "v_mfma_f32_16x16x32_bf16 v[80:83], v[160:163], v[96:99], v[80:83]\n\t"
    "ds_read_b128 v[120:123], %[da] offset:12288\n\t"
    "s_add_u32 m0, %[lb], 16384\n\t"
    "v_mfma_f32_16x16x32_bf16 v[84:87], v[164:167], v[96:99], v[84:87]\n\t"
    "ds_read_b128 v[124:127], %[da] offset:14336\n\t"
    "global_load_lds_dwordx4 %[av0], %[s1] sc1\n\t"
    "v_mfma_f32_16x16x32_bf16 v[88:91], v[160:163], v[100:103], v[88:91]\n\t"
    "v_mfma_f32_16x16x32_bf16 v[92:95], v[164:167], v[100:103], v[92:95]\n\t"
    "s_waitcnt vmcnt(7)\n\t"
    "s_barrier\n\t"
    "s_waitcnt lgkmcnt(4)\n\t"
    "v_mfma_f32_16x16x32_bf16 v[80:83], v[168:171], v[104:107], v[80:83]\n\t"
    "ds_read_b128 v[96:99], %[ea] offset:16384\n\t"
    "s_add_u32 m0, %[lb], 20480\n\t"
    "v_mfma_f32_16x16x32_bf16 v[84:87], v[172:175], v[104:107], v[84:87]\n\t"
    "ds_read_b128 v[100:103], %[ea] offset:18432\n\t"
    "global_load_lds_dwordx4 %[av2], %[s1] sc1\n\t"
    "v_mfma_f32_16x16x32_bf16 v[88:91], v[168:171], v[108:111], v[88:91]\n\t"
    "v_mfma_f32_16x16x32_bf16 v[92:95], v[172:175], v[108:111], v[92:95]\n\t"
    "s_waitcnt lgkmcnt(4)\n\t"
    "v_mfma_f32_16x16x32_bf16 v[80:83], v[176:179], v[112:115], v[80:83]\n\t"
    "ds_read_b128 v[104:107], %[da] offset:16384\n\t"
    "s_add_u32 m0, %[lb], 24576\n\t"
    "v_mfma_f32_16x16x32_bf16 v[84:87], v[180:183], v[112:115], v[84:87]\n\t"
    "ds_read_b128 v[108:111], %[da] offset:18432\n\t"
    "global_load_lds_dwordx4 %[av4], %[s1] sc1\n\t"
    "v_mfma_f32_16x16x32_bf16 v[88:91], v[176:179], v[116:119], v[88:91]\n\t"
    "v_mfma_f32_16x16x32_bf16 v[92:95], v[180:183], v[116:119], v[92:95]\n\t"
    "s_waitcnt lgkmcnt(4)\n\t"
    "v_mfma_f32_16x16x32_bf16 v[80:83], v[184:187], v[120:123], v[80:83]\n\t"
    "ds_read_b128 v[112:115], %[ea] offset:20480\n\t"
    "s_add_u32 m0, %[lb], 28672\n\t"
    "v_mfma_f32_16x16x32_bf16 v[84:87], v[188:191], v[120:123], v[84:87]\n\t"
    "ds_read_b128 v[116:119], %[ea] offset:22528\n\t"
    "global_load_lds_dwordx4 %[av6], %[s1] sc1\n\t"
    "v_mfma_f32_16x16x32_bf16 v[88:91], v[184:187], v[124:127], v[88:91]\n\t"
    "v_mfma_f32_16x16x32_bf16 v[92:95], v[188:191], v[124:127], v[92:95]\n\t"
    "s_waitcnt lgkmcnt(4)\n\t"
    "v_mfma_f32_16x16x32_bf16 v[80:83], v[192:195], v[96:99], v[80:83]\n\t"
    "ds_read_b128 v[120:123], %[da] offset:20480\n\t"
    "v_mfma_f32_16x16x32_bf16 v[84:87], v[196:199], v[96:99], v[84:87]\n\t"
    "ds_read_b128 v[124:127], %[da] offset:22528\n\t"
    "v_mfma_f32_16x16x32_bf16 v[88:91], v[192:195], v[100:103], v[88:91]\n\t"
    "v_mfma_f32_16x16x32_bf16 v[92:95], v[196:199], v[100:103], v[92:95]\n\t"
    "s_waitcnt vmcnt(8)\n\t"
    "s_barrier\n\t"
    "s_waitcnt lgkmcnt(4)\n\t"
    "v_mfma_f32_16x16x32_bf16 v[80:83], v[200:203], v[104:107], v[80:83]\n\t"
    "ds_read_b128 v[96:99], %[ea] offset:24576\n\t"
    "v_mfma_f32_16x16x32_bf16 v[84:87], v[204:207], v[104:107], v[84:87]\n\t"
    "ds_read_b128 v[100:103], %[ea] offset:26624\n\t"
    "v_mfma_f32_16x16x32_bf16 v[88:91], v[200:203], v[108:111], v[88:91]\n\t"
    "v_mfma_f32_16x16x32_bf16 v[92:95], v[204:207], v[108:111], v[92:95]\n\t"
    "s_waitcnt lgkmcnt(4)\n\t"
    "v_mfma_f32_16x16x32_bf16 v[80:83], v[208:211], v[112:115], v[80:83]\n\t"
    "ds_read_b128 v[104:107], %[da] offset:24576\n\t"
    "v_mfma_f32_16x16x32_bf16 v[84:87], v[212:215], v[112:115], v[84:87]\n\t"
    "ds_read_b128 v[108:111], %[da] offset:26624\n\t"
    "v_mfma_f32_16x16x32_bf16 v[88:91], v[208:211], v[116:119], v[88:91]\n\t"
    "v_mfma_f32_16x16x32_bf16 v[92:95], v[212:215], v[116:119], v[92:95]\n\t"
    "s_cmp_eq_u32 %[ps], 0\n\t"
    "s_cbranch_scc1 10f\n\t"
    "global_load_dword %[sv], %[dv], %[db] sc1\n\t"
    "10:\n\t"
    "s_waitcnt lgkmcnt(4)\n\t"
    "v_mfma_f32_16x16x32_bf16 v[80:83], v[216:219], v[120:123], v[80:83]\n\t"
    "ds_read_b128 v[112:115], %[ea] offset:28672\n\t"
    "v_mfma_f32_16x16x32_bf16 v[84:87], v[220:223], v[120:123], v[84:87]\n\t"
    "ds_read_b128 v[116:119], %[ea] offset:30720\n\t"
    "v_mfma_f32_16x16x32_bf16 v[88:91], v[216:219], v[124:127], v[88:91]\n\t"
    "v_mfma_f32_16x16x32_bf16 v[92:95], v[220:223], v[124:127], v[92:95]\n\t"
    "s_waitcnt lgkmcnt(4)\n\t"
    "v_mfma_f32_16x16x32_bf16 v[80:83], v[224:227], v[96:99], v[80:83]\n\t"
    "ds_read_b128 v[120:123], %[da] offset:28672\n\t"
    "v_mfma_f32_16x16x32_bf16 v[84:87], v[228:231], v[96:99], v[84:87]\n\t"
    "ds_read_b128 v[124:127], %[da] offset:30720\n\t"
    "v_mfma_f32_16x16x32_bf16 v[88:91], v[224:227], v[100:103], v[88:91]\n\t"
    "v_mfma_f32_16x16x32_bf16 v[92:95], v[228:231], v[100:103], v[92:95]\n\t"
    "s_waitcnt lgkmcnt(4)\n\t"
    "v_mfma_f32_16x16x32_bf16 v[80:83], v[232:235], v[104:107], v[80:83]\n\t"
    "v_mfma_f32_16x16x32_bf16 v[84:87], v[236:239], v[104:107], v[84:87]\n\t"
    "v_mfma_f32_16x16x32_bf16 v[88:91], v[232:235], v[108:111], v[88:91]\n\t"
    "v_mfma_f32_16x16x32_bf16 v[92:95], v[236:239], v[108:111], v[92:95]\n\t"
    "s_waitcnt lgkmcnt(2)\n\t"
    "v_mfma_f32_16x16x32_bf16 v[80:83], v[240:243], v[112:115], v[80:83]\n\t"
    "v_mfma_f32_16x16x32_bf16 v[84:87], v[244:247], v[112:115], v[84:87]\n\t"
    "v_mfma_f32_16x16x32_bf16 v[88:91], v[240:243], v[116:119], v[88:91]\n\t"
    "v_mfma_f32_16x16x32_bf16 v[92:95], v[244:247], v[116:119], v[92:95]\n\t"
    "s_waitcnt lgkmcnt(0)\n\t"
    "v_mfma_f32_16x16x32_bf16 v[80:83], v[248:251], v[120:123], v[80:83]\n\t"
    "v_mfma_f32_16x16x32_bf16 v[84:87], v[252:255], v[120:123], v[84:87]\n\t"
    "v_mfma_f32_16x16x32_bf16 v[88:91], v[248:251], v[124:127], v[88:91]\n\t"
    "v_mfma_f32_16x16x32_bf16 v[92:95], v[252:255], v[124:127], v[92:95]\n\t"
    "s_mov_b32 %[got], 0\n\t"
    "v_mov_b32 v76, %[fa]\n\t"
    "v_mov_b32 v77, %[fv]\n\t"
    "v_add_u32 v78, 1, v77\n\t"
    "v_mov_b32 v79, %[ga]\n\t"
    "ds_write_b128 %[rw], v[80:83] offset:0\n\t"
    "ds_write_b128 %[rw], v[84:87] offset:1024\n\t"
    "ds_write_b128 %[rw], v[88:91] offset:2048\n\t"
    "ds_write_b128 %[rw], v[92:95] offset:3072\n\t"
    "ds_write_b32 v76, v77\n\t"
    "s_cmp_eq_u32 %[ps], 0\n\t"
    "s_cbranch_scc1 9f\n\t"
    "s_waitcnt vmcnt(0)\n\t"
    "9:"
    : [sv] "+&v"(sv), [got] "=&s"(got)
    : [ea] "v"(ea), [da] "v"(da), [x] "s"(x), "{v[128:159]}"(W[0]), "{v[160:191]}"(W[1]), "{v[192:223]}"(W[2]), "{v[224:255]}"(W[3]), [lb] "s"(lb), [s0] "s"(s0), [s1] "s"(s1), [av0] "v"(av[0]), [av1] "v"(av[1]), [av2] "v"(av[2]), [av3] "v"(av[3]), [av4] "v"(av[4]), [av5] "v"(av[5]), [av6] "v"(av[6]), [av7] "v"(av[7]), [ps] "s"(ps), [dv] "v"(dv), [db] "s"(db), [rw] "v"(rw), [fa] "s"(fa), [fv] "s"(fv), [ga] "s"(ga), [mu] "s"(mu), [ad] "v"(ad), [th] "s"(th)
    : "v96", "v97", "v98", "v99", "v100", "v101", "v102", "v103", "v104", "v105", "v106", "v107", "v108", "v109", "v110", "v111", "v112", "v113", "v114", "v115", "v116", "v117", "v118", "v119", "v120", "v121", "v122", "v123", "v124", "v125", "v126", "v127", "v76", "v77", "v78", "v79", "v80", "v81", "v82", "v83", "v84", "v85", "v86", "v87", "v88", "v89", "v90", "v91", "v92", "v93", "v94", "v95", "vcc", "scc", "m0", "memory");
}
__device__ __forceinline__ void lean_st0(const u32x32 (&W)[4], unsigned ea, unsigned da, unsigned x, u32x16& pd, unsigned pv, unsigned cn, const char* pg) {
  unsigned st;
  asm volatile("ds_read_b128 v[96:99], %[ea] offset:0\n\t"
    "ds_read_b128 v[100:103], %[ea] offset:2048\n\t"
    "ds_read_b128 v[104:107], %[da] offset:0\n\t"
    "ds_read_b128 v[108:111], %[da] offset:2048\n\t"
    "ds_read_b128 v[112:115], %[ea] offset:4096\n\t"
    "ds_read_b128 v[116:119], %[ea] offset:6144\n\t"
    "s_waitcnt lgkmcnt(4)\n\t"
    "v_mfma_f32_16x16x32_bf16 v[80:83], v[128:131], v[96:99], 0\n\t"
    "ds_read_b128 v[120:123], %[da] offset:4096\n\t"
    "v_mfma_f32_16x16x32_bf16 v[84:87], v[132:135], v[96:99], 0\n\t"
    "ds_read_b128 v[124:127], %[da] offset:6144\n\t"
    "v_mfma_f32_16x16x32_bf16 v[88:91], v[128:131], v[100:103], 0\n\t"
    "v_mfma_f32_16x16x32_bf16 v[92:95], v[132:135], v[100:103], 0\n\t"
    "s_barrier\n\t"
    "s_bitcmp1_b32 %[x], 2\n\t"
    "s_cbranch_scc1 7f\n\t"
    "8:\n\t"
    "s_waitcnt lgkmcnt(4)\n\t"
    "v_mfma_f32_16x16x32_bf16 v[80:83], v[136:139], v[104:107], v[80:83]\n\t"
    "ds_read_b128 v[96:99], %[ea] offset:8192\n\t"
    "v_mfma_f32_16x16x32_bf16 v[84:87], v[140:143], v[104:107], v[84:87]\n\t"
    "ds_read_b128 v[100:103], %[ea] offset:10240\n\t"
    "v_mfma_f32_16x16x32_bf16 v[88:91], v[136:139], v[108:111], v[88:91]\n\t"
    "v_mfma_f32_16x16x32_bf16 v[92:95], v[140:143], v[108:111], v[92:95]\n\t"
    "s_waitcnt lgkmcnt(4)\n\t"
    "v_mfma_f32_16x16x32_bf16 v[80:83], v[144:147], v[112:115], v[80:83]\n\t"
    "ds_read_b128 v[104:107], %[da] offset:8192\n\t"
    "v_mfma_f32_16x16x32_bf16 v[84:87], v[148:151], v[112:115], v[84:87]\n\t"
    "ds_read_b128 v[108:111], %[da] offset:10240\n\t"
    "v_mfma_f32_16x16x32_bf16 v[88:91], v[144:147], v[116:119], v[88:91]\n\t"
    "v_mfma_f32_16x16x32_bf16 v[92:95], v[148:151], v[116:119], v[92:95]\n\t"
    "s_waitcnt lgkmcnt(4)\n\t"
    "v_mfma_f32_16x16x32_bf16 v[80:83], v[152:155], v[120:123], v[80:83]\n\t"
    "ds_read_b128 v[112:115], %[ea] offset:12288\n\t"
    "v_mfma_f32_16x16x32_bf16 v[84:87], v[156:159], v[120:123], v[84:87]\n\t"
    "ds_read_b128 v[116:119], %[ea] offset:14336\n\t"
    "v_mfma_f32_16x16x32_bf16 v[88:91], v[152:155], v[124:127], v[88:91]\n\t"
    "v_mfma_f32_16x16x32_bf16 v[92:95], v[156:159], v[124:127], v[92:95]\n\t"
    "s_waitcnt lgkmcnt(4)\n\t"
    "v_mfma_f32_16x16x32_bf16 v[80:83], v[160:163], v[96:99], v[80:83]\n\t"
    "ds_read_b128 v[120:123], %[da] offset:12288\n\t"
    "v_mfma_f32_16x16x32_bf16 v[84:87], v[164:167], v[96:99], v[84:87]\n\t"
    "ds_read_b128 v[124:127], %[da] offset:14336\n\t"
    "v_mfma_f32_16x16x32_bf16 v[88:91], v[160:163], v[100:103], v[88:91]\n\t"
    "v_mfma_f32_16x16x32_bf16 v[92:95], v[164:167], v[100:103], v[92:95]\n\t"
    "s_barrier\n\t"
    "s_waitcnt lgkmcnt(4)\n\t"
    "v_mfma_f32_16x16x32_bf16 v[80:83], v[168:171], v[104:107], v[80:83]\n\t"
    "ds_read_b128 v[96:99], %[ea] offset:16384\n\t"
    "v_mfma_f32_16x16x32_bf16 v[84:87], v[172:175], v[104:107], v[84:87]\n\t"
    "ds_read_b128 v[100:103], %[ea] offset:18432\n\t"
    "v_mfma_f32_16x16x32_bf16 v[88:91], v[168:171], v[108:111], v[88:91]\n\t"
    "v_mfma_f32_16x16x32_bf16 v[92:95], v[172:175], v[108:111], v[92:95]\n\t"
    "s_waitcnt lgkmcnt(4)\n\t"
    "v_mfma_f32_16x16x32_bf16 v[80:83], v[176:179], v[112:115], v[80:83]\n\t"
    "ds_read_b128 v[104:107], %[da] offset:16384\n\t"
    "v_mfma_f32_16x16x32_bf16 v[84:87], v[180:183], v[112:115], v[84:87]\n\t"
    "ds_read_b128 v[108:111], %[da] offset:18432\n\t"
    "v_mfma_f32_16x16x32_bf16 v[88:91], v[176:179], v[116:119], v[88:91]\n\t"
    "v_mfma_f32_16x16x32_bf16 v[92:95], v[180:183], v[116:119], v[92:95]\n\t"
    "s_waitcnt lgkmcnt(4)\n\t"
    "v_mfma_f32_16x16x32_bf16 v[80:83], v[184:187], v[120:123], v[80:83]\n\t"
    "ds_read_b128 v[112:115], %[ea] offset:20480\n\t"
    "v_mfma_f32_16x16x32_bf16 v[84:87], v[188:191], v[120:123], v[84:87]\n\t"
    "ds_read_b128 v[116:119], %[ea] offset:22528\n\t"
    "v_mfma_f32_16x16x32_bf16 v[88:91], v[184:187], v[124:127], v[88:91]\n\t"
    "v_mfma_f32_16x16x32_bf16 v[92:95], v[188:191], v[124:127], v[92:95]\n\t"
    "s_waitcnt lgkmcnt(4)\n\t"
    "v_mfma_f32_16x16x32_bf16 v[80:83], v[192:195], v[96:99], v[80:83]\n\t"
    "ds_read_b128 v[120:123], %[da] offset:20480\n\t"
    "v_mfma_f32_16x16x32_bf16 v[84:87], v[196:199], v[96:99], v[84:87]\n\t"
    "ds_read_b128 v[124:127], %[da] offset:22528\n\t"
    "v_mfma_f32_16x16x32_bf16 v[88:91], v[192:195], v[100:103], v[88:91]\n\t"
    "v_mfma_f32_16x16x32_bf16 v[92:95], v[196:199], v[100:103], v[92:95]\n\t"
    "s_barrier\n\t"
    "s_and_b32 %[st], %[x], 3\n\t"
    "s_cbranch_scc1 3f\n\t"
    "4:\n\t"
    "s_waitcnt lgkmcnt(4)\n\t"
    "v_mfma_f32_16x16x32_bf16 v[80:83], v[200:203], v[104:107], v[80:83]\n\t"
    "ds_read_b128 v[96:99], %[ea] offset:24576\n\t"
    "v_mfma_f32_16x16x32_bf16 v[84:87], v[204:207], v[104:107], v[84:87]\n\t"
    "ds_read_b128 v[100:103], %[ea] offset:26624\n\t"
    "v_mfma_f32_16x16x32_bf16 v[88:91], v[200:203], v[108:111], v[88:91]\n\t"
    "v_mfma_f32_16x16x32_bf16 v[92:95], v[204:207], v[108:111], v[92:95]\n\t"
    "s_waitcnt lgkmcnt(4)\n\t"
    "v_mfma_f32_16x16x32_bf16 v[80:83], v[208:211], v[112:115], v[80:83]\n\t"
    "ds_read_b128 v[104:107], %[da] offset:24576\n\t"
    "v_mfma_f32_16x16x32_bf16 v[84:87], v[212:215], v[112:115], v[84:87]\n\t"
    "ds_read_b128 v[108:111], %[da] offset:26624\n\t"
    "v_mfma_f32_16x16x32_bf16 v[88:91], v[208:211], v[116:119], v[88:91]\n\t"
    "v_mfma_f32_16x16x32_bf16 v[92:95], v[212:215], v[116:119], v[92:95]\n\t"
    "s_waitcnt lgkmcnt(4)\n\t"
    "v_mfma_f32_16x16x32_bf16 v[80:83], v[216:219], v[120:123], v[80:83]\n\t"
    "ds_read_b128 v[112:115], %[ea] offset:28672\n\t"
    "v_mfma_f32_16x16x32_bf16 v[84:87], v[220:223], v[120:123], v[84:87]\n\t"
    "ds_read_b128 v[116:119], %[ea] offset:30720\n\t"
    "v_mfma_f32_16x16x32_bf16 v[88:91], v[216:219], v[124:127], v[88:91]\n\t"
    "v_mfma_f32_16x16x32_bf16 v[92:95], v[220:223], v[124:127], v[92:95]\n\t"
    "s_waitcnt lgkmcnt(4)\n\t"
    "v_mfma_f32_16x16x32_bf16 v[80:83], v[224:227], v[96:99], v[80:83]\n\t"
    "ds_read_b128 v[120:123], %[da] offset:28672\n\t"
    "v_mfma_f32_16x16x32_bf16 v[84:87], v[228:231], v[96:99], v[84:87]\n\t"
    "ds_read_b128 v[124:127], %[da] offset:30720\n\t"
    "v_mfma_f32_16x16x32_bf16 v[88:91], v[224:227], v[100:103], v[88:91]\n\t"
    "v_mfma_f32_16x16x32_bf16 v[92:95], v[228:231], v[100:103], v[92:95]\n\t"
    "s_waitcnt lgkmcnt(4)\n\t"
    "v_mfma_f32_16x16x32_bf16 v[80:83], v[232:235], v[104:107], v[80:83]\n\t"
    "v_mfma_f32_16x16x32_bf16 v[84:87], v[236:239], v[104:107], v[84:87]\n\t"
    "v_mfma_f32_16x16x32_bf16 v[88:91], v[232:235], v[108:111], v[88:91]\n\t"
    "v_mfma_f32_16x16x32_bf16 v[92:95], v[236:239], v[108:111], v[92:95]\n\t"
    "s_waitcnt lgkmcnt(2)\n\t"
    "v_mfma_f32_16x16x32_bf16 v[80:83], v[240:243], v[112:115], v[80:83]\n\t"
    "v_mfma_f32_16x16x32_bf16 v[84:87], v[244:247], v[112:115], v[84:87]\n\t"
    "v_mfma_f32_16x16x32_bf16 v[88:91], v[240:243], v[116:119], v[88:91]\n\t"
    "v_mfma_f32_16x16x32_bf16 v[92:95], v[244:247], v[116:119], v[92:95]\n\t"
    "s_waitcnt lgkmcnt(0)\n\t"
    "v_mfma_f32_16x16x32_bf16 v[80:83], v[248:251], v[120:123], v[80:83]\n\t"
    "v_mfma_f32_16x16x32_bf16 v[84:87], v[252:255], v[120:123], v[84:87]\n\t"
    "v_mfma_f32_16x16x32_bf16 v[88:91], v[248:251], v[124:127], v[88:91]\n\t"
    "v_mfma_f32_16x16x32_bf16 v[92:95], v[252:255], v[124:127], v[92:95]\n\t"
    "s_nop 6\n\t"
    "s_branch 9f\n\t"
    "3:\n\t"
    "7:\n\t"
    "v_mov_b32 v76, %[cn]\n\t"
    "v_mov_b32 v77, 1\n\t"
    "v_mov_b32 v78, %[pv]\n\t"
    "v_mov_b32 v79, 0\n\t"
    "s_waitcnt vmcnt(0)\n\t"
    "s_mov_b64 exec, 1\n\t"
    "ds_add_rtn_u32 v77, v76, v77\n\t"
    "s_waitcnt lgkmcnt(0)\n\t"
    "v_readfirstlane_b32 %[st], v77\n\t"
    "s_and_b32 %[st], %[st], 3\n\t"
    "s_cmp_lg_u32 %[st], 3\n\t"
    "s_cbranch_scc1 6f\n\t"
    "global_store_dword v79, v78, %[pg] sc1\n\t"
    "6:\n\t"
    "s_mov_b64 exec, -1\n\t"
    "s_bitcmp1_b32 %[x], 2\n\t"
    "s_cbranch_scc1 8b\n\t"
    "s_branch 4b\n\t"
    "9:"
    : "=&{v[80:95]}"(pd), [st] "=&s"(st)
    : [ea] "v"(ea), [da] "v"(da), [x] "s"(x), "{v[128:159]}"(W[0]), "{v[160:191]}"(W[1]), "{v[192:223]}"(W[2]), "{v[224:255]}"(W[3]), [pv] "s"(pv), [cn] "s"(cn), [pg] "s"(pg)
    : "v96", "v97", "v98", "v99", "v100", "v101", "v102", "v103", "v104", "v105", "v106", "v107", "v108", "v109", "v110", "v111", "v112", "v113", "v114", "v115", "v116", "v117", "v118", "v119", "v120", "v121", "v122", "v123", "v124", "v125", "v126", "v127", "v76", "v77", "v78", "v79", "vcc", "scc", "m0", "memory");
}
#define LEAN_QB 1
#define PREK 10
#define NEARLY 16
__device__ __forceinline__ void fin_ld(const u32x16& pd, unsigned rw, unsigned fa, unsigned fv) {
  asm volatile("ds_write_b128 %[rw], v[80:83] offset:0\n\t"
    "ds_write_b128 %[rw], v[84:87] offset:1024\n\t"
    "ds_write_b128 %[rw], v[88:91] offset:2048\n\t"
    "ds_write_b128 %[rw], v[92:95] offset:3072\n\t"
    "ds_write_b32 %[fa], %[fv]"
    :
    : [rw] "v"(rw), "{v[80:95]}"(pd), [fa] "v"(fa), [fv] "v"(fv)
    : "memory");
}
__device__ __forceinline__ void fin_st0(const u32x16& pd, unsigned rw, unsigned fa, unsigned fv, const char* cb, unsigned co0, unsigned co1) {
  asm volatile("1:\n\t"
    "ds_read_b32 v39, %[fa]\n\t"
    "ds_read_b128 v[48:51], %[rw] offset:0\n\t"
    "ds_read_b128 v[52:55], %[rw] offset:1024\n\t"
    "ds_read_b128 v[56:59], %[rw] offset:2048\n\t"
    "ds_read_b128 v[60:63], %[rw] offset:3072\n\t"
    "s_waitcnt lgkmcnt(2)\n\t"
    "v_cmp_ne_u32 vcc, v39, %[fv]\n\t"
    "s_cbranch_vccz 2f\n\t"
    "s_waitcnt lgkmcnt(0)\n\t"
    "s_branch 1b\n\t"
    "2:\n\t"
    "v_add_f32 v48, v48, v80\n\t"
    "v_add_f32 v49, v49, v81\n\t"
    "v_add_f32 v50, v50, v82\n\t"
    "v_add_f32 v51, v51, v83\n\t"
    "v_add_f32 v52, v52, v84\n\t"
    "v_add_f32 v53, v53, v85\n\t"
    "v_add_f32 v54, v54, v86\n\t"
    "v_add_f32 v55, v55, v87\n\t"
    "v_cvt_pk_bf16_f32 v40, v48, v49\n\t"
    "v_cvt_pk_bf16_f32 v41, v50, v51\n\t"
    "v_cvt_pk_bf16_f32 v42, v52, v53\n\t"
    "v_cvt_pk_bf16_f32 v43, v54, v55\n\t"
    "global_store_dwordx4 %[co0], v[40:43], %[cb] sc1\n\t"
    "s_waitcnt lgkmcnt(0)\n\t"
    "v_add_f32 v56, v56, v88\n\t"
    "v_add_f32 v57, v57, v89\n\t"
    "v_add_f32 v58, v58, v90\n\t"
    "v_add_f32 v59, v59, v91\n\t"
    "v_add_f32 v60, v60, v92\n\t"
    "v_add_f32 v61, v61, v93\n\t"
    "v_add_f32 v62, v62, v94\n\t"
    "v_add_f32 v63, v63, v95\n\t"
    "v_cvt_pk_bf16_f32 v44, v56, v57\n\t"
    "v_cvt_pk_bf16_f32 v45, v58, v59\n\t"
    "v_cvt_pk_bf16_f32 v46, v60, v61\n\t"
    "v_cvt_pk_bf16_f32 v47, v62, v63\n\t"
    "global_store_dwordx4 %[co1], v[44:47], %[cb] sc1\n\t"
    "s_nop 1"
    :
    : [rw] "v"(rw), "{v[80:95]}"(pd), [fa] "v"(fa), [fv] "v"(fv), [cb] "s"(cb), [co0] "v"(co0), [co1] "v"(co1)
    : "v39", "v40", "v41", "v42", "v43", "v44", "v45", "v46", "v47", "v48", "v49", "v50", "v51", "v52", "v53", "v54", "v55", "v56", "v57", "v58", "v59", "v60", "v61", "v62", "v63", "vcc", "memory");
}
__device__ __forceinline__ void fin_st1(const u32x16& pd, unsigned rw, unsigned fa, unsigned fv, const char* cb, unsigned co0, unsigned co1) {
  asm volatile("1:\n\t"
    "ds_read_b32 v39, %[fa]\n\t"
    "ds_read_b128 v[48:51], %[rw] offset:0\n\t"
    "ds_read_b128 v[52:55], %[rw] offset:1024\n\t"
    "ds_read_b128 v[56:59], %[rw] offset:2048\n\t"
    "ds_read_b128 v[60:63], %[rw] offset:3072\n\t"
    "s_waitcnt lgkmcnt(2)\n\t"
    "v_cmp_ne_u32 vcc, v39, %[fv]\n\t"
    "s_cbranch_vccz 2f\n\t"
    "s_waitcnt lgkmcnt(0)\n\t"
    "s_branch 1b\n\t"
    "2:\n\t"
    "v_add_f32 v48, v48, v80\n\t"
    "v_add_f32 v49, v49, v81\n\t"
    "v_add_f32 v50, v50, v82\n\t"
    "v_add_f32 v51, v51, v83\n\t"
    "v_add_f32 v52, v52, v84\n\t"
    "v_add_f32 v53, v53, v85\n\t"
    "v_add_f32 v54, v54, v86\n\t"
    "v_add_f32 v55, v55, v87\n\t"
    "s_waitcnt lgkmcnt(0)\n\t"
    "v_add_f32 v56, v56, v88\n\t"
    "v_add_f32 v57, v57, v89\n\t"
    "v_add_f32 v58, v58, v90\n\t"
    "v_add_f32 v59, v59, v91\n\t"
    "v_add_f32 v60, v60, v92\n\t"
    "v_add_f32 v61, v61, v93\n\t"
    "v_add_f32 v62, v62, v94\n\t"
    "v_add_f32 v63, v63, v95\n\t"
    "v_mul_f32 v40, %[nl], v48\n\t"
    "v_mul_f32 v41, %[nl], v49\n\t"
    "v_mul_f32 v42, %[nl], v50\n\t"
    "v_mul_f32 v43, %[nl], v51\n\t"
    "v_mul_f32 v44, %[nl], v56\n\t"
    "v_mul_f32 v45, %[nl], v57\n\t"
    "v_mul_f32 v46, %[nl], v58\n\t"
    "v_mul_f32 v47, %[nl], v59\n\t"
    "v_exp_f32 v40, v40\n\t"
    "v_exp_f32 v41, v41\n\t"
    "v_exp_f32 v42, v42\n\t"
    "v_exp_f32 v43, v43\n\t"
    "v_exp_f32 v44, v44\n\t"
    "v_exp_f32 v45, v45\n\t"
    "v_exp_f32 v46, v46\n\t"
    "v_exp_f32 v47, v47\n\t"
    "v_add_f32 v40, 1.0, v40\n\t"
    "v_add_f32 v41, 1.0, v41\n\t"
    "v_add_f32 v42, 1.0, v42\n\t"
    "v_add_f32 v43, 1.0, v43\n\t"
    "v_add_f32 v44, 1.0, v44\n\t"
    "v_add_f32 v45, 1.0, v45\n\t"
    "v_add_f32 v46, 1.0, v46\n\t"
    "v_add_f32 v47, 1.0, v47\n\t"
    "v_rcp_f32 v40, v40\n\t"
    "v_rcp_f32 v41, v41\n\t"
    "v_rcp_f32 v42, v42\n\t"
    "v_rcp_f32 v43, v43\n\t"
    "v_rcp_f32 v44, v44\n\t"
    "v_rcp_f32 v45, v45\n\t"
    "v_rcp_f32 v46, v46\n\t"
    "v_rcp_f32 v47, v47\n\t"
    "v_mul_f32 v40, v48, v40\n\t"
    "v_mul_f32 v41, v49, v41\n\t"
    "v_mul_f32 v42, v50, v42\n\t"
    "v_mul_f32 v43, v51, v43\n\t"
    "v_mul_f32 v44, v56, v44\n\t"
    "v_mul_f32 v45, v57, v45\n\t"
    "v_mul_f32 v46, v58, v46\n\t"
    "v_mul_f32 v47, v59, v47\n\t"
    "v_mul_f32 v40, v40, v52\n\t"
    "v_mul_f32 v41, v41, v53\n\t"
    "v_mul_f32 v42, v42, v54\n\t"
    "v_mul_f32 v43, v43, v55\n\t"
    "v_mul_f32 v44, v44, v60\n\t"
    "v_mul_f32 v45, v45, v61\n\t"
    "v_mul_f32 v46, v46, v62\n\t"
    "v_mul_f32 v47, v47, v63\n\t"
    "v_cvt_pk_bf16_f32 v48, v40, v41\n\t"
    "v_cvt_pk_bf16_f32 v49, v42, v43\n\t"
    "v_cvt_pk_bf16_f32 v50, v44, v45\n\t"
    "v_cvt_pk_bf16_f32 v51, v46, v47\n\t"
    "s_nop 1\n\t"
    "v_permlane16_swap_b32 v48, v50\n\t"
    "v_permlane16_swap_b32 v49, v51\n\t"
    "global_store_dwordx4 %[co0], v[48:51], %[cb] sc1\n\t"
    "s_nop 1"
    :
    : [rw] "v"(rw), "{v[80:95]}"(pd), [fa] "v"(fa), [fv] "v"(fv), [cb] "s"(cb), [co0] "v"(co0), [nl] "s"(-LOG2E)
    : "v39", "v40", "v41", "v42", "v43", "v44", "v45", "v46", "v47", "v48", "v49", "v50", "v51", "v52", "v53", "v54", "v55", "v56", "v57", "v58", "v59", "v60", "v61", "v62", "v63", "vcc", "memory");
}
__device__ __forceinline__ void fin_st2(const u32x16& pd, unsigned rw, unsigned fa, unsigned fv, const char* cb, unsigned co0, unsigned co1) {
  asm volatile("s_nop 1\n\t"
    "v_cvt_pk_bf16_f32 v40, v80, v81\n\t"
    "v_cvt_pk_bf16_f32 v41, v82, v83\n\t"
    "v_cvt_pk_bf16_f32 v42, v84, v85\n\t"
    "v_cvt_pk_bf16_f32 v43, v86, v87\n\t"
    "global_store_dwordx4 %[co0], v[40:43], %[cb] sc1\n\t"
    "v_cvt_pk_bf16_f32 v44, v88, v89\n\t"
    "v_cvt_pk_bf16_f32 v45, v90, v91\n\t"
    "v_cvt_pk_bf16_f32 v46, v92, v93\n\t"
    "v_cvt_pk_bf16_f32 v47, v94, v95\n\t"
    "global_store_dwordx4 %[co1], v[44:47], %[cb] sc1\n\t"
    "1:\n\t"
    "ds_read_b32 v39, %[fa]\n\t"
    "ds_read_b128 v[48:51], %[rw] offset:0\n\t"
    "ds_read_b128 v[52:55], %[rw] offset:1024\n\t"
    "ds_read_b128 v[56:59], %[rw] offset:2048\n\t"
    "ds_read_b128 v[60:63], %[rw] offset:3072\n\t"
    "s_waitcnt lgkmcnt(2)\n\t"
    "v_cmp_ne_u32 vcc, v39, %[fv]\n\t"
    "s_cbranch_vccz 2f\n\t"
    "s_waitcnt lgkmcnt(0)\n\t"
    "s_branch 1b\n\t"
    "2:\n\t"
    "v_cvt_pk_bf16_f32 v48, v48, v49\n\t"
    "v_cvt_pk_bf16_f32 v49, v50, v51\n\t"
    "v_cvt_pk_bf16_f32 v50, v52, v53\n\t"
    "v_cvt_pk_bf16_f32 v51, v54, v55\n\t"
    "global_store_dwordx4 %[co0], v[48:51], %[cb] offset:2048 sc1\n\t"
    "s_waitcnt lgkmcnt(0)\n\t"
    "v_cvt_pk_bf16_f32 v56, v56, v57\n\t"
    "v_cvt_pk_bf16_f32 v57, v58, v59\n\t"
    "v_cvt_pk_bf16_f32 v58, v60, v61\n\t"
    "v_cvt_pk_bf16_f32 v59, v62, v63\n\t"
    "global_store_dwordx4 %[co1], v[56:59], %[cb] offset:2048 sc1\n\t"
    "s_nop 1"
    :
    : [rw] "v"(rw), "{v[80:95]}"(pd), [fa] "v"(fa), [fv] "v"(fv), [cb] "s"(cb), [co0] "v"(co0), [co1] "v"(co1)
    : "v39", "v40", "v41", "v42", "v43", "v44", "v45", "v46", "v47", "v48", "v49", "v50", "v51", "v52", "v53", "v54", "v55", "v56", "v57", "v58", "v59", "v60", "v61", "v62", "v63", "vcc", "memory");
}
template <int R> __device__ __forceinline__ void gw_load(u32x32& w, unsigned a0, unsigned a1);
template <> __device__ __forceinline__ void gw_load<0>(u32x32& w, unsigned a0, unsigned a1) {
  asm volatile("ds_read_b64_tr_b16 v[128:129], %1 offset:0\n\t"
    "ds_read_b64_tr_b16 v[130:131], %1 offset:512\n\t"
    "ds_read_b64_tr_b16 v[132:133], %2 offset:0\n\t"
    "ds_read_b64_tr_b16 v[134:135], %2 offset:512\n\t"
    "ds_read_b64_tr_b16 v[136:137], %1 offset:4096\n\t"
    "ds_read_b64_tr_b16 v[138:139], %1 offset:4608\n\t"
    "ds_read_b64_tr_b16 v[140:141], %2 offset:4096\n\t"
    "ds_read_b64_tr_b16 v[142:143], %2 offset:4608\n\t"
    "s_waitcnt lgkmcnt(0)\n\t"
    "ds_read_b64_tr_b16 v[144:145], %1 offset:16384\n\t"
    "ds_read_b64_tr_b16 v[146:147], %1 offset:16896\n\t"
    "ds_read_b64_tr_b16 v[148:149], %2 offset:16384\n\t"
    "ds_read_b64_tr_b16 v[150:151], %2 offset:16896\n\t"
    "ds_read_b64_tr_b16 v[152:153], %1 offset:20480\n\t"
    "ds_read_b64_tr_b16 v[154:155], %1 offset:20992\n\t"
    "ds_read_b64_tr_b16 v[156:157], %2 offset:20480\n\t"
    "ds_read_b64_tr_b16 v[158:159], %2 offset:20992\n\t"
    "s_waitcnt lgkmcnt(0)"
    : "=&{v[128:159]}"(w) : "v"(a0), "v"(a1) : "memory");
}
template <> __device__ __forceinline__ void gw_load<1>(u32x32& w, unsigned a0, unsigned a1) {
  asm volatile("ds_read_b64_tr_b16 v[160:161], %1 offset:0\n\t"
    "ds_read_b64_tr_b16 v[162:163], %1 offset:512\n\t"
    "ds_read_b64_tr_b16 v[164:165], %2 offset:0\n\t"
    "ds_read_b64_tr_b16 v[166:167], %2 offset:512\n\t"
    "ds_read_b64_tr_b16 v[168:169], %1 offset:4096\n\t"
    "ds_read_b64_tr_b16 v[170:171], %1 offset:4608\n\t"
    "ds_read_b64_tr_b16 v[172:173], %2 offset:4096\n\t"
    "ds_read_b64_tr_b16 v[174:175], %2 offset:4608\n\t"
    "s_waitcnt lgkmcnt(0)\n\t"
    "ds_read_b64_tr_b16 v[176:177], %1 offset:16384\n\t"
    "ds_read_b64_tr_b16 v[178:179], %1 offset:16896\n\t"
    "ds_read_b64_tr_b16 v[180:181], %2 offset:16384\n\t"
    "ds_read_b64_tr_b16 v[182:183], %2 offset:16896\n\t"
    "ds_read_b64_tr_b16 v[184:185], %1 offset:20480\n\t"
    "ds_read_b64_tr_b16 v[186:187], %1 offset:20992\n\t"
    "ds_read_b64_tr_b16 v[188:189], %2 offset:20480\n\t"
    "ds_read_b64_tr_b16 v[190:191], %2 offset:20992\n\t"
    "s_waitcnt lgkmcnt(0)"
    : "=&{v[160:191]}"(w) : "v"(a0), "v"(a1) : "memory");
}
template <> __device__ __forceinline__ void gw_load<2>(u32x32& w, unsigned a0, unsigned a1) {
  asm volatile("ds_read_b64_tr_b16 v[192:193], %1 offset:0\n\t"
    "ds_read_b64_tr_b16 v[194:195], %1 offset:512\n\t"
    "ds_read_b64_tr_b16 v[196:197], %2 offset:0\n\t"
    "ds_read_b64_tr_b16 v[198:199], %2 offset:512\n\t"
    "ds_read_b64_tr_b16 v[200:201], %1 offset:4096\n\t"
    "ds_read_b64_tr_b16 v[202:203], %1 offset:4608\n\t"
    "ds_read_b64_tr_b16 v[204:205], %2 offset:4096\n\t"
    "ds_read_b64_tr_b16 v[206:207], %2 offset:4608\n\t"
    "s_waitcnt lgkmcnt(0)\n\t"
    "ds_read_b64_tr_b16 v[208:209], %1 offset:16384\n\t"
    "ds_read_b64_tr_b16 v[210:211], %1 offset:16896\n\t"
    "ds_read_b64_tr_b16 v[212:213], %2 offset:16384\n\t"
    "ds_read_b64_tr_b16 v[214:215], %2 offset:16896\n\t"
    "ds_read_b64_tr_b16 v[216:217], %1 offset:20480\n\t"
    "ds_read_b64_tr_b16 v[218:219], %1 offset:20992\n\t"
    "ds_read_b64_tr_b16 v[220:221], %2 offset:20480\n\t"
    "ds_read_b64_tr_b16 v[222:223], %2 offset:20992\n\t"
    "s_waitcnt lgkmcnt(0)"
    : "=&{v[192:223]}"(w) : "v"(a0), "v"(a1) : "memory");
}
template <> __device__ __forceinline__ void gw_load<3>(u32x32& w, unsigned a0, unsigned a1) {
  asm volatile("ds_read_b64_tr_b16 v[224:225], %1 offset:0\n\t"
    "ds_read_b64_tr_b16 v[226:227], %1 offset:512\n\t"
    "ds_read_b64_tr_b16 v[228:229], %2 offset:0\n\t"
    "ds_read_b64_tr_b16 v[230:231], %2 offset:512\n\t"
    "ds_read_b64_tr_b16 v[232:233], %1 offset:4096\n\t"
    "ds_read_b64_tr_b16 v[234:235], %1 offset:4608\n\t"
    "ds_read_b64_tr_b16 v[236:237], %2 offset:4096\n\t"
    "ds_read_b64_tr_b16 v[238:239], %2 offset:4608\n\t"
    "s_waitcnt lgkmcnt(0)\n\t"
    "ds_read_b64_tr_b16 v[240:241], %1 offset:16384\n\t"
    "ds_read_b64_tr_b16 v[242:243], %1 offset:16896\n\t"
    "ds_read_b64_tr_b16 v[244:245], %2 offset:16384\n\t"
    "ds_read_b64_tr_b16 v[246:247], %2 offset:16896\n\t"
    "ds_read_b64_tr_b16 v[248:249], %1 offset:20480\n\t"
    "ds_read_b64_tr_b16 v[250:251], %1 offset:20992\n\t"
    "ds_read_b64_tr_b16 v[252:253], %2 offset:20480\n\t"
    "ds_read_b64_tr_b16 v[254:255], %2 offset:20992\n\t"
    "s_waitcnt lgkmcnt(0)"
    : "=&{v[224:255]}"(w) : "v"(a0), "v"(a1) : "memory");
}
template <int I, int E, class F> __device__ __forceinline__ void sfor(F&& f) { if constexpr (I < E) { f(std::integral_constant<int, I>{}); sfor<I + 1, E>(f); } }
template <int ROLE, int KQ, class F>
__device__ __forceinline__ void wload(char* smem, int wave_id_, const u16* W0, const u16* W1, int ldb, u32x32 (&bw)[4], int rot, F&& hook) {
  constexpr int ABUF = 65536;
  const int tid = opaque_tid(), lane = tid & 63, wave = WAVE_ID;
  const int kq = KQ == 2 ? wave >> 2 : 0, nq = KQ == 2 ? wave & 3 : wave;
  const int g = lane >> 4, r16 = lane & 15, q4 = r16 >> 2, p4 = lane & 3;
  const unsigned ldsb = __builtin_amdgcn_readfirstlane((unsigned)(size_t)(LAS char*)smem);
  {
    const int srow = 8 * wave + (lane >> 3), ch = (lane & 7) ^ bswz(srow);
    const unsigned voff = (unsigned)(srow * ldb + ch * 8) * 2;
    const char* w0 = uni((const char*)W0); const char* w1 = uni((const char*)(ROLE == R_F1 ? W1 : W0 + 64));
    const size_t kb = (size_t)64 * ldb * 2;
    auto wstage = [&](int r) {
      const unsigned ld = ldsb + (r & 1) * ABUF + wave * 1024;
#pragma unroll
      for (int i = 0; i < 8; ++i) {
        const int kbi = KQ == 2 ? (ROLE == R_PART2 ? 0 : (i >> 2) * 8) + ((2 * r + ((i >> 1) & 1)) ^ rot) : 2 * r + (i >> 2);
        const char* src = KQ == 2 ? ((i & 1) ? w1 : w0) + kbi * kb + (ROLE == R_PART2 ? (i >> 2) * 256 : 0) : w0 + kbi * kb + (i & 3) * 128;
        unsigned keep;
        asm volatile("s_mov_b32 %0, m0\n\ts_mov_b32 m0, %3\n\ts_nop 0\n\tglobal_load_lds_dwordx4 %1, %2" WNT_ "\n\ts_mov_b32 m0, %0"
                     : "=&s"(keep) : "v"(voff), "s"(src), "s"(ld + i * 8192) : "memory");
      }
    };
    const int brow = 8 * g + q4, bs = bswz(brow);
    unsigned la[2];
#pragma unroll
    for (int j = 0; j < 2; ++j) {
      int blk, c16, sub;
      if (ROLE == R_F1) { blk = j; c16 = nq * 2 + (p4 >> 1); sub = (p4 & 1) * 8; }
      else { blk = nq >> 1; c16 = (nq & 1) * 4 + p4; sub = j * 8; }
      int set = kq;
      if (ROLE == R_PART2 && TLP) { set = nq >> 1; blk = nq & 1; c16 = kq * 4 + p4; }
      la[j] = (unsigned)((KQ == 2 ? set * 4 + blk : blk) * 8192 + brow * 128 + ((c16 ^ bs) << 4) + sub);
    }
    hook(4);
    wstage(0); wstage(1);
    sfor<0, 4>([&](auto r_c) __attribute__((always_inline)) {
      constexpr int r = decltype(r_c)::value;
      if (r < 3) WAIT_V(8); else WAIT_V(0);
      __builtin_amdgcn_s_barrier(); asm volatile("" ::: "memory");
      gw_load<r>(bw[r], ldsb + (r & 1) * ABUF + la[0], ldsb + (r & 1) * ABUF + la[1]);
      BAR();
      if (r + 2 < 4) wstage(r + 2);
      hook(r);
    });
  }
}
#define DSR2(d0, d1, a0_, a1_, off) asm volatile("ds_read_b128 %0, %2 offset:%4\n\tds_read_b128 %1, %3 offset:%4" : "=&v"(d0), "=&v"(d1) : "v"(a0_), "v"(a1_), "i"(off))
template <int N> __device__ __forceinline__ void waitl(u32x4& a, u32x4& b) { asm volatile("s_waitcnt lgkmcnt(%2)" : "+v"(a), "+v"(b) : "i"(N)); }
__device__ __forceinline__ void glds1(const char* sbase, unsigned voff, unsigned lds) {
  unsigned keep;
  asm volatile("s_mov_b32 %0, m0\n\ts_mov_b32 m0, %3\n\ts_nop 0\n\tglobal_load_lds_dwordx4 %1, %2 sc1\n\ts_mov_b32 m0, %0"
               : "=&s"(keep) : "v"(voff), "s"(sbase), "s"(lds) : "memory");
}
#define XS(i) do { } while (0)
#define XFL(b_) do { } while (0)
#define XA_ 0
#define XF_ 0
#define XE_ 0
#define XP_ 0
#define PUBK 9
#define LATE 0
#define NOWP 0
#define PEEK 3
#define LAQ 2
#define LAP 2
#define PDM 2
#define PDEEP 4
template <int ROLE>
__device__ __forceinline__ void gemm7(char* smem, int wave_id_, const u16* W0, const u16* W1, int ldb, const char* A_, size_t a_blk, Dep dep,
                                      char* C_, size_t c_blk, unsigned*& cnt, int myw, unsigned* tmo, int slot, unsigned long long* mts,
                                      bool gl, int nimm = 0, int nseg = NSEG, int wm0 = 0, int pofs = 0, u64 tag = 0) {
  dep.base = (const char*)cnt;
  unsigned* myprog = cnt + myw;
  volatile LAS unsigned* wsl = (volatile LAS unsigned*)(smem + L_SLOT + 512);
  if (opaque_tid() < 144) *(volatile LAS unsigned*)(smem + L_SLOT + opaque_tid() * 4) = 0u;
  const int tid = opaque_tid(), lane = tid & 63, wave = WAVE_ID;
  const int kq = wave >> 2, nq = wave & 3;
  const unsigned ldsb = __builtin_amdgcn_readfirstlane((unsigned)(size_t)(LAS char*)smem);
  constexpr int rot = 0;
  const char* A = uni(A_) + nq * 1024;
  constexpr int LA = ROLE == R_QKV ? LAQ : LAP, PD = LA == 3 ? PDEEP : PDM;
  constexpr bool P2C = ROLE == R_PART2;
  int pw = 0;
  int wm = wm0, nh = 0;
  auto lgo = [&](int b, unsigned& v) {
    if (wave == 4) {
      for (int spins = 0; __builtin_amdgcn_readfirstlane(wsl[2]) != (unsigned)(b + 1); ++spins) {
        asm volatile("global_load_dword %0, %1, %2 sc1\n\ts_waitcnt vmcnt(0)" : "=&v"(v) : "v"(dep.voff), "s"(dep.base) : "memory");
        if (__all((int)(v * dep.mul + dep.add) > b)) break;
        __builtin_amdgcn_s_sleep(2);
        if (give_up(spins, tmo, 9)) { v = 255u; break; }
      }
      wsl[2] = (unsigned)(b + 1);
    } else {
      while (__builtin_amdgcn_readfirstlane(wsl[2]) != (unsigned)(b + 1)) __builtin_amdgcn_s_sleep(1);
    }
  };
  auto lwm = [&](int b, unsigned v) {
    unsigned w;
    if (wave == 4) {
      const int w2 = (int)dep_eval(v, dep);
      w = (unsigned)min(max(max(w2, wm), b + 1), 255);
      wsl[0] = ((unsigned)(b + 1) << 8) | w;
    } else {
      for (;;) {
        w = __builtin_amdgcn_readfirstlane(wsl[0]);
        if ((w >> 8) == (unsigned)(b + 1)) break;
        __builtin_amdgcn_s_sleep(1);
      }
      w &= 255u;
    }
    return (int)w;
  };
  auto pubw = [&](int n) {
    WAIT_V(0);
    if (lane == 0) {
      const unsigned o = __hip_atomic_fetch_add((LAS unsigned*)(smem + L_SLOT + 516), 1u, __ATOMIC_RELAXED, __HIP_MEMORY_SCOPE_WORKGROUP);
      if ((o & 3u) == 3u) {
        st4_sc1(myprog, (unsigned)(n + pofs));
      }
    }
    asm volatile("s_waitcnt lgkmcnt(0)" ::: "memory");
  };
  auto hsrc = [&](int hh) { return A + (size_t)(hh >> 1) * a_blk + ((hh ^ (rot >> 2)) & 1) * 16384; };
  auto hlds = [&](int hh) { return ldsb + (unsigned)(((hh >> 1) & 1) * 65536 + (hh & 1) * 16384 + nq * 1024); };
#define POFF(i_) (((i_) & 1) * 32768 + ((i_) >> 1) * 4096)
#define PIECE(s_, l_, i_) asm volatile("s_add_u32 m0, %2, %3\n\ts_nop 0\n\tglobal_load_lds_dwordx4 %0, %1 sc1" :: "v"(av[i_]), "s"(s_), "s"(l_), "i"(POFF(i_)) : "memory", "scc", "m0")
  u32x32 BW[4];
  wload<ROLE, 2>(smem, wave_id_, W0, W1, ldb, BW, rot, [&](int r) {
    if (ROLE == R_QKV && nimm > 0 && wave == 0) STS((char*)tmo - OFF_TMO, 128 + slot, 3 + r);
    if (ROLE == R_QKV && nimm > 0 && r == 0 && wave == 4) init_issue((char*)tmo - OFF_TMO, ldsb + L_RED);
    if (ROLE == R_QKV && nimm > 0 && r == 2) {
      cnt = cnt_of((char*)tmo - OFF_TMO, init_get((char*)tmo - OFF_TMO, tag, smem + L_RED));
      dep.base = (const char*)cnt; myprog = cnt + myw;
      if (kq) {
        unsigned v0 = 0;
        lgo(0, v0);
        wm = lwm(0, v0);
        const unsigned l16 = lane_id() * 16;
#pragma unroll
        for (int i = 0; i < 16; ++i) glds1(hsrc(i >> 3) + (i & 1) * 32768 + ((((i & 7) >> 1) ^ rot) & 3) * 4096, l16, hlds(i >> 3) + POFF(i & 7));
      }
      nh = 2;
    }
  });
  if (ROLE == R_QKV && nimm > 0 && !kq) wm = (int)(__builtin_amdgcn_readfirstlane(wsl[0]) & 255u);
  if (ROLE == R_QKV && nimm > 0 && opaque_tid() == 0) STS((char*)tmo - OFF_TMO, 128 + slot, 0);
  if (wave == 0) WFB();
  auto body = [&](auto ldr_c) __attribute__((always_inline)) {
    constexpr bool LDR = decltype(ldr_c)::value;
    if (XP_ == 0 ? !LDR : (XP_ == 1 ? LDR : false)) __builtin_amdgcn_s_setprio(1);
    unsigned av[8];
#pragma unroll
    for (int i = 0; i < 8; ++i) av[i] = lane * 16 + (P2C ? 0 : (i & 1) * 32768) + (((i >> 1) ^ rot) & 3) * 4096;
    auto issue_all = [&](int hh) {
      if (LDR) {
        const char* s_ = hsrc(hh); const unsigned l_ = hlds(hh);
#pragma unroll
        for (int i = 0; i < 8; ++i) if (!(P2C && (i & 1))) PIECE(s_, l_, i);
      }
    };
    int b = 0;
    bool pre = false;
    unsigned smp = 0, got = 0;
    while (b < nseg) {
      const bool fast = (wm < nseg ? wm : nseg) >= b + PD && b >= nimm && nh <= 2 * b + LA;
      const bool pl = wm <= b || pre, cold = nh < 2 * b + 2, nh3 = nh == 2 * b + 3;
      if (!fast) {
        XS(0);
        if (LDR && wm <= b && !got) lgo(b, smp);
        XS(1);
        if (got) nh += NEARLY / 8;
        { const int upto = nh3 ? 2 * b + 4 : 2 * b + 2; for (; nh < upto; ++nh) issue_all(nh); }
        if (LDR && pl) wm = lwm(b, smp);
        XS(2);
      }
      const int ln = lane_id(), g = ln >> 4, r16 = ln & 15;
      const unsigned e0 = ldsb + (unsigned)((P2C ? 0 : kq * 32768) + (b & 1) * 65536 + (gl ? (g >> 1) * 512 + r16 * 32 + (g & 1) * 16 : r16 * 128 + ((g ^ (r16 & 7)) << 4)));
      const unsigned d0 = e0 ^ (gl ? 1024u : 64u);
      const unsigned rw = ldsb + L_RED + nq * 4096 + ln * 16;
      const unsigned co0 = ROLE == R_F1 ? (unsigned)((g & 1) * 2048 + nq * 512 + r16 * 32 + (g >> 1) * 16)
                         : ((ROLE == R_QKV ? TLQ : TLP) ? (unsigned)(nq * (P2C ? 4096 : 2048) + r16 * 64 + g * 16)
                         : (ROLE == R_QKV ? (unsigned)(r16 * 256 + (((4 * nq + g) ^ r16) << 4)) : (unsigned)(r16 * PROW + (32 * nq + 8 * g) * 2)));
      const unsigned co1 = co0 + ((ROLE == R_QKV ? TLQ : TLP) ? 1024u : (ROLE == R_QKV ? 4096u : 16u * PROW));
      u32x16 pv_;
      bool pr2 = false;
      if (fast) {
        __builtin_amdgcn_s_barrier(); asm volatile("" ::: "memory");
        for (; nh < 2 * b + LA; ++nh) issue_all(nh);
        int lf = (wm < nseg ? wm : nseg) - (LA - 1), cp = nseg - (LA - 1);
        const unsigned sa = ldsb + L_SLOT + ln * 4, ad = dep.add - (unsigned)(LA - 1);
        if constexpr (LDR) (LA == 3 ? (P2C ? fast_ld2_3 : fast_ld0_3) : (P2C ? fast_ld2_2 : fast_ld0_2))(BW, pv_, e0, d0, b, lf, cp, sa, dep.mul, ad, rw, (unsigned)a_blk,
                                                                                                       hsrc(2 * b + LA), hsrc(2 * b + LA + 1), hlds(2 * b + LA), hlds(2 * b + LA + 1), av);
        else {
          const char* c0_ = uni((const char*)tmo - OFF_TMO + OFF_DUMP); const char* c1_ = uni(C_ + (size_t)b * c_blk);
          const int sf = b + pofs;
          const char* pg = uni((const char*)myprog);
          (ROLE == R_F1 ? fast_st1 : (P2C ? fast_st2 : fast_st0))(BW, pv_, e0, d0, b, lf, cp, sa, dep.mul, ad, rw, (unsigned)c_blk, c0_, c1_, co0, co1, pofs, sf, (unsigned)wave,
                                                                   ldsb + L_SLOT, dep.voff, dep.base, pg);
        }
        wm = lf + LA - 1 > wm ? lf + LA - 1 : wm;
        nh = 2 * b + LA;
        --b;
        __builtin_amdgcn_s_barrier(); asm volatile("" ::: "memory");
      } else {
        if (LDR) { if (nh >= 2 * b + 4) { if (P2C) WAIT_V(14); else WAIT_V(28); } else { if (P2C) WAIT_V(6); else WAIT_V(12); } }
        BAR();
        XS(3);
        if (ROLE == R_QKV && nimm > 0 && b < 2 && opaque_tid() == 0) STS((char*)tmo - OFF_TMO, 128 + slot, 1 + b);
        if constexpr (!LDR) {
          const unsigned wv = wsl[0];
          lean_st0(BW, e0, d0, __builtin_amdgcn_readfirstlane(pw > 0 ? (cold ? 4u : 1u) : 0u), pv_, (unsigned)(pw + pofs), ldsb + L_SLOT + 516, uni((const char*)myprog));
          if (pl) wm = (int)(__builtin_amdgcn_readfirstlane(wv) & 255u);
        }
        const bool dm = nh == 2 * b + 2 && wm > b + 1 && b + 1 < nseg;
        pr2 = b + 1 < nseg && wm < nseg;
        if constexpr (LDR) {
          const bool er = nh == 2 * b + 2 && !dm;
          const unsigned ps = pr2 && (wave == 4 || er) ? (unsigned)(b + 1) : 0u;
#define LARGS BW, e0, d0, (unsigned)(nh - 2 * b - 2), hlds(nh), hsrc(nh), hsrc(nh + 1), av, ps, dep.voff, dep.base, \
              rw, ldsb + L_SLOT + 528 + nq * 4, (unsigned)(b + 1), ldsb + L_SLOT + 520, dep.mul, dep.add, er ? b + 1 : 0x7FFFFFFF, smp, got
          if (dm) (P2C ? lean_dm1 : lean_dm0)(LARGS); else (P2C ? lean_ld1 : lean_ld0)(LARGS);
#undef LARGS
          got = __builtin_amdgcn_readfirstlane(got);
        }
        if (dm) nh += 2;
        XS(5);
      }
      if constexpr (LDR) { if (fast) fin_ld(pv_, rw, ldsb + L_SLOT + 528 + nq * 4, (unsigned)(b + 1)); }
      else (ROLE == R_F1 ? fin_st1 : (P2C ? fin_st2 : fin_st0))(pv_, rw, ldsb + L_SLOT + 528 + nq * 4, (unsigned)(b + 1), uni(C_ + (size_t)b * c_blk), co0, co1);
      XS(6);
      pre = pr2;
      bool now = (nh < 2 * b + 4 && wm < b + 1 + PD) || b + 1 >= nseg || b < nimm;
      if (!LDR && now) {
        if (NOWP == 0 && b >= nimm && __builtin_amdgcn_readfirstlane(wsl[PEEK]) == (unsigned)(b + 2)) now = false;
        else pubw(b + 1);
      }
      XS(7);
      XFL(b);
      pw = now ? 0 : b + 1;
      ++b;
    }
    __builtin_amdgcn_s_setprio(0);
  };
  if (kq) body(std::true_type{}); else body(std::false_type{});
#undef PIECE
#undef POFF
  WAIT_V(0);
  BAR();
}
__device__ __forceinline__ void fin_role(char* smem, int wave_id_, int fidx, const float* mod, const char* P2, const u16* x1, u16* out, unsigned* cnt, unsigned* tmo,
                                         unsigned long long* mts) {
  const int tid = opaque_tid(), lane = tid & 63, wave = WAVE_ID;
  row_table(smem + L_GB, mod, cnt, tmo, tid);
  volatile LAS unsigned* wsl = (volatile LAS unsigned*)(smem + L_SLOT + 512);
  int wf = 0, par = 0;
  for (int nf = fidx >> 3; nf < NSEG; nf += 2) {
    if (wf <= nf) {
      if (wave == 0) {
        unsigned w;
        for (int spins = 0;; ++spins) {
          const unsigned v = rowmin16(ld4_sc1(cnt + (PF2 + ((lane & 31) < 28 ? (lane & 31) : 0)) * 32));
          w = min(rdl(v, 0), rdl(v, 16));
          if ((int)w > nf) break;
          __builtin_amdgcn_s_sleep(2);
          if (give_up(spins, tmo, 9)) { w = NSEG; break; }
        }
        if (lane == 0) wsl[par] = w;
      }
      BAR();
      wf = (int)__builtin_amdgcn_readfirstlane(wsl[par]);
      par ^= 1;
    }
    int ln = lane; asm volatile("" : "+v"(ln));
    if (wave == 0) WTS(256 + 8 + fidx, nf / 2, 0);
    fin_half(P2, x1, smem + L_GB + 12288, out, nf * 32 + 4 * (fidx & 7) + (wave >> 1), wave & 1, ln);
    if (wave == 0) WTS(256 + 8 + fidx, nf / 2, 2);
  }
}
#define LD8(r, vo, sb) asm volatile("global_load_dwordx4 %0, %8, %12 sc1\n\tglobal_load_dwordx4 %1, %9, %12 sc1\n\t" \
    "global_load_dwordx4 %2, %10, %12 sc1\n\tglobal_load_dwordx4 %3, %11, %12 sc1\n\t" \
    "global_load_dwordx4 %4, %8, %13 sc1\n\tglobal_load_dwordx4 %5, %9, %13 sc1\n\t" \
    "global_load_dwordx4 %6, %10, %13 sc1\n\tglobal_load_dwordx4 %7, %11, %13 sc1" \
    : "=&v"(r[0][0]), "=&v"(r[0][1]), "=&v"(r[0][2]), "=&v"(r[0][3]), "=&v"(r[1][0]), "=&v"(r[1][1]), "=&v"(r[1][2]), "=&v"(r[1][3]) \
    : "v"(vo), "v"(vo ^ (TLQ ? 2048u : 64u)), "v"(vo ^ (TLQ ? 4096u : 128u)), "v"(vo ^ (TLQ ? 6144u : 192u)), "s"(sb), "s"((sb) + (TLQ ? 1024 : 4096)) : "memory")
#define TIE8(r) "+v"(r[0][0]), "+v"(r[0][1]), "+v"(r[0][2]), "+v"(r[0][3]), "+v"(r[1][0]), "+v"(r[1][1]), "+v"(r[1][2]), "+v"(r[1][3])
#define SADL 44
#define SASL 8
__device__ __forceinline__ void sa_role(char* smem, int wave_id_, int s0, int hbase, const char* __restrict__ Q, const char* __restrict__ Kx, const char* __restrict__ V,
                                        const char* kvm, char* __restrict__ O, unsigned* cnt, unsigned* tmo, int slot, unsigned long long* mts) {
  constexpr float LS = LOG2E * SCALE;
  const int tid = opaque_tid(), wave = WAVE_ID;
  const int h = hbase + wave;
  char* vl = smem + wave * 17408;
  char* ol = vl + 8192;
  const unsigned ldsw = __builtin_amdgcn_readfirstlane((unsigned)(size_t)(LAS char*)vl);
  unsigned avail = 0;
  for (int s = s0, it = 1; s < NSEG; s += 4, ++it) {
    u32x4 qv[2][4], kv[2][4];
    int lane = tid & 63; asm volatile("" : "+v"(lane));
    const int g = lane >> 4, r16 = lane & 15, q4 = r16 >> 2, p4 = lane & 3;
    auto request = [&](int sr) {
      const size_t o = ((size_t)sr * NH + h) * 8192;
      const char* qs = uni(Q + o); const char* ks = uni(Kx + o); const char* vs = uni(V + o);
      const unsigned fo = TLQ ? (unsigned)(r16 * 64 + g * 16) : (unsigned)(r16 * 256 + ((g ^ r16) << 4));
      LD8(qv, fo, qs); LD8(kv, fo, ks);
      head_to_lds(vs, lane, ldsw);
    };
    {
      for (int spins = 0; avail <= (unsigned)s; ++spins) {
        const unsigned p = ld4_sc1(cnt + (PQKV + h) * 32 + (lane & 15));
        avail = min(min(qkv_avail(p, 0), qkv_avail(p, 1)), qkv_avail(p, 2));
        if (avail > (unsigned)s) break;
        __builtin_amdgcn_s_sleep(32);
        if (give_up(spins, tmo, 10)) break;
      }
      request(s);
      asm volatile("s_waitcnt vmcnt(0)" : TIE8(qv), TIE8(kv) :: "memory");
    }
    if (wave == 0) WTS(slot, s, 0);
    f32x4 s00 = f32x4{0, 0, 0, 0}, s01 = s00, s11 = s00;
#pragma unroll
    for (int ks = 0; ks < 4; ++ks) {
      s00 = MFMA32(asbf8(kv[0][ks]), asbf8(qv[0][ks]), s00);
      s01 = MFMA32(asbf8(kv[0][ks]), asbf8(qv[1][ks]), s01);
      s11 = MFMA32(asbf8(kv[1][ks]), asbf8(qv[1][ks]), s11);
    }
    float mx[2], sum[2];
    u32x4 pseg[2];
    {
#pragma unroll
      for (int r = 0; r < 4; ++r) { const bool ok = 4 * g + r <= r16; s00[r] = ok ? s00[r] : -3.0e38f; s11[r] = ok ? s11[r] : -3.0e38f; }
      mx[0] = red16_32_max(max3(max3(s00[0], s00[1], s00[2]), s00[3], -1.0e38f));
      mx[1] = red16_32_max(max3(max3(fmaxf(fmaxf(s01[0], s01[1]), fmaxf(s01[2], s01[3])), s11[0], -1.0e38f), max3(s11[1], s11[2], s11[3]), -1.0e38f));
      const float n0 = -mx[0] * LS, n1 = -mx[1] * LS;
#pragma unroll
      for (int r = 0; r < 4; ++r) {
        s00[r] = __builtin_amdgcn_exp2f(s00[r] * LS + n0); s01[r] = __builtin_amdgcn_exp2f(s01[r] * LS + n1); s11[r] = __builtin_amdgcn_exp2f(s11[r] * LS + n1);
      }
      sum[0] = (s00[0] + s00[1]) + (s00[2] + s00[3]);
      sum[1] = ((s01[0] + s01[1]) + (s01[2] + s01[3])) + ((s11[0] + s11[1]) + (s11[2] + s11[3]));
      pseg[0] = u32x4{pack2(s00[0], s00[1]), pack2(s00[2], s00[3]), 0u, 0u};
      pseg[1] = u32x4{pack2(s01[0], s01[1]), pack2(s01[2], s01[3]), pack2(s11[0], s11[1]), pack2(s11[2], s11[3])};
    }
    f32x4 o[8][2];
#pragma unroll
    for (int dt = 0; dt < 8; ++dt) {
      const int row0 = 4 * g + q4;
      const unsigned xo = (unsigned)(row0 * 256 + (((4 * (dt >> 1) + p4) ^ row0) << 4) + (dt & 1) * 8);
      const u32x2 lo = trread(vl, xo), hi = trread(vl, xo + 4096);
      o[dt][0] = MFMA32(asbf8(u32x4{lo.x, lo.y, 0u, 0u}), asbf8(pseg[0]), (f32x4{0, 0, 0, 0}));
      o[dt][1] = MFMA32(asbf8(u32x4{lo.x, lo.y, hi.x, hi.y}), asbf8(pseg[1]), (f32x4{0, 0, 0, 0}));
    }
#pragma unroll
    for (int dt = 0; dt < 8; ++dt) asm volatile("" : "+v"(o[dt][0]), "+v"(o[dt][1]));
    asm volatile("s_waitcnt lgkmcnt(0)" ::: "memory");
    for (int spins = 0;; ++spins) {
      if (__all(ld4_sc1(cnt + (PROG + h) * 32 + ((lane & 4) ? 5 : (lane & 3))) + (s >= SDIR ? 1u : 0u) > (unsigned)s)) break;
      __builtin_amdgcn_s_sleep(SASL);
      if (give_up(spins, tmo, 6)) break;
    }
    if (s >= SDIR) delay64<SADL>();
    const char* mk = kvm + ((size_t)s * NH + h) * 4096;
    u32x4 kmv[4]; u32x2 vmv[8];
    {
      u32x4 k0, k1;
      const char* pk = mk + lane * 16;
      const char* pv = mk + 2048 + (8 * q4 + p4) * 16 + (g & 1) * 8;
      for (int spins = 0;; ++spins) {
      asm volatile("global_load_dwordx4 %0, %10, off sc1\n\tglobal_load_dwordx4 %1, %10, off offset:1024 sc1\n\t"
                   "global_load_dwordx2 %2, %11, off sc1\n\tglobal_load_dwordx2 %3, %11, off offset:64 sc1\n\t"
                   "global_load_dwordx2 %4, %11, off offset:512 sc1\n\tglobal_load_dwordx2 %5, %11, off offset:576 sc1\n\t"
                   "global_load_dwordx2 %6, %11, off offset:1024 sc1\n\tglobal_load_dwordx2 %7, %11, off offset:1088 sc1\n\t"
                   "global_load_dwordx2 %8, %11, off offset:1536 sc1\n\tglobal_load_dwordx2 %9, %11, off offset:1600 sc1\n\ts_waitcnt vmcnt(0)"
                   : "=&v"(k0), "=&v"(k1), "=&v"(vmv[0]), "=&v"(vmv[1]), "=&v"(vmv[2]), "=&v"(vmv[3]),
                     "=&v"(vmv[4]), "=&v"(vmv[5]), "=&v"(vmv[6]), "=&v"(vmv[7])
                   : "v"(pk), "v"(pv) : "memory");
      unsigned mx_ = max(max(max(k0.x, k0.y), max(k0.z, k0.w)), max(max(k1.x, k1.y), max(k1.z, k1.w)));
#pragma unroll
      for (int i = 0; i < 8; ++i) mx_ = max(mx_, max(vmv[i].x, vmv[i].y));
      if (__all(mx_ != SENT)) break;
      __builtin_amdgcn_s_sleep(2);
      if (give_up(spins, tmo, 6)) break;
      }
      WTM(slot, s, 1);
      *(u32x4*)(ol + lane * 16) = k0; *(u32x4*)(ol + 1024 + lane * 16) = k1;
      asm volatile("s_waitcnt lgkmcnt(0)" ::: "memory");
      __builtin_amdgcn_wave_barrier();
      const int a = (8 * g + q4) * 16 + (lane & 1) * 8;
#pragma unroll
      for (int ks = 0; ks < 4; ++ks) {
        const u32x2 lo = trread(ol, a + ks * 512), hi = trread(ol, a + ks * 512 + 64);
        kmv[ks] = u32x4{lo.x, lo.y, hi.x, hi.y};
      }
      asm volatile("s_waitcnt lgkmcnt(0)" ::: "memory");
      __builtin_amdgcn_wave_barrier();
    }
    u32x2 pm[2]; float cs[2], inv[2];
#pragma unroll
    for (int b = 0; b < 2; ++b) {
      f32x4 t = f32x4{0, 0, 0, 0};
#pragma unroll
      for (int ks = 0; ks < 4; ++ks) t = MFMA32(asbf8(kmv[ks]), asbf8(qv[b][ks]), t);
      float m = fmaxf(fmaxf(t[0], t[1]), fmaxf(fmaxf(t[2], t[3]), mx[b]));
      { float x, y; sw16(m, x, y); m = max2(x, y); }
      const float n = -m * LS;
      const float f = __builtin_amdgcn_exp2f(mx[b] * LS + n);
#pragma unroll
      for (int r = 0; r < 4; ++r) t[r] = g < 2 ? __builtin_amdgcn_exp2f(t[r] * LS + n) : 0.0f;
      inv[b] = __builtin_amdgcn_rcpf(red16_32_add(sum[b] * f + ((t[0] + t[1]) + (t[2] + t[3]))));
      cs[b] = f * inv[b];
      pm[b] = u32x2{pack2(t[0] * inv[b], t[1] * inv[b]), pack2(t[2] * inv[b], t[3] * inv[b])};
    }
    const char* od = uni(O + ((size_t)s * 64 + 2 * h) * 4096);
#pragma unroll
    for (int j = 0; j < 4; ++j) {
      u32x2 v0 = vmv[2 * j], v1 = vmv[2 * j + 1];
      if (g >= 2) { v0 = u32x2{0, 0}; v1 = v0; }
#pragma unroll
      for (int b = 0; b < 2; ++b) {
        const f32x4 r0 = __builtin_amdgcn_mfma_f32_16x16x16bf16_1k(ass4(v0), ass4(pm[b]), o[2 * j][b] * cs[b], 0, 0, 0);
        const f32x4 r1 = __builtin_amdgcn_mfma_f32_16x16x16bf16_1k(ass4(v1), ass4(pm[b]), o[2 * j + 1][b] * cs[b], 0, 0, 0);
        st16s(od, (unsigned)((j >> 1) * 4096 + (16 * b + r16) * 128 + (((4 * (j & 1) + g) ^ (r16 & 7)) << 4)),
              u32x4{pack2(r0[0], r0[1]), pack2(r0[2], r0[3]), pack2(r1[0], r1[1]), pack2(r1[2], r1[3])});
      }
    }
    WTM(slot, s, 3);
    WAIT_V(0);
    __builtin_amdgcn_s_barrier();
    if (wave == 0 && lane == 0) st4_sc1(cnt + (PSA + (hbase >> 1) + s0) * 32, (unsigned)it);
    WTM(slot, s, 2);
  }
}
#undef LD8
#undef TIE8

constexpr int Q_MEM = 0;
constexpr int Q_K   = 16384;
constexpr int Q_V   = 24576;
constexpr int Q_QK  = 32768;
constexpr int Q_TM  = 36864;
constexpr int Q_CB  = 40960;
constexpr int Q_RING = 32768;
constexpr int V_P1  = 0;
constexpr int V_P2  = 16384;
constexpr int NCHAIN = 96;
#define NQQ 6
#define NQK 4
#define E_AG 9
#define RDYI -2
#define ROTW 2
#define DQU 6
#define DQM 18
#define DQD 1
#define ORSEL (xg >> 1)
#define D_Q 105
#define D_O 4
#define E_B 6
#define E_O 1900
#define KDEL 5
#define VPOS 0

typedef __bf16 bf16x16 __attribute__((ext_vector_type(16)));
typedef unsigned u32x8 __attribute__((ext_vector_type(8)));
__device__ __forceinline__ f32x4 SMF(u32x4 a, u32x8 b, f32x4 c, int idx) {
  return __builtin_amdgcn_smfmac_f32_16x16x64_bf16(__builtin_bit_cast(bf16x8, a), __builtin_bit_cast(bf16x16, b), c, idx, 0, 0);
}
__device__ __forceinline__ float fold32(float a, float b) {
  const auto p = __builtin_amdgcn_permlane32_swap(__float_as_uint(a), __float_as_uint(b), false, false);
  return __uint_as_float(p[0]) + __uint_as_float(p[1]);
}
__device__ __forceinline__ f32x2 rowsum(f32x4 x) { return f32x2{fold32(x[0], x[2]), fold32(x[1], x[3])}; }
__device__ __forceinline__ unsigned nosent1(unsigned v) { return v < 0xFFFFFFFEu ? v : 0xFFFFFFFEu; }
__device__ __forceinline__ void st4s(const char* sb, unsigned vo, unsigned v) { asm volatile("global_store_dword %0, %1, %2 sc1\n\ts_nop 0" :: "v"(vo), "v"(v), "s"(sb) : "memory"); }
__device__ __forceinline__ void st8s(const char* sb, unsigned vo, u32x2 v) { asm volatile("global_store_dwordx2 %0, %1, %2 sc1\n\ts_nop 1" :: "v"(vo), "v"(v), "s"(sb) : "memory"); }
__device__ __forceinline__ void gldsw(const char* sbase, unsigned voff, unsigned lds) {
  unsigned keep;
  asm volatile("s_mov_b32 %0, m0\n\ts_mov_b32 m0, %3\n\ts_nop 0\n\tglobal_load_lds_dwordx4 %1, %2" CNT_ "\n\ts_mov_b32 m0, %0"
               : "=&s"(keep) : "v"(voff), "s"(sbase), "s"(lds) : "memory");
}
__device__ __forceinline__ void wait_clk(unsigned t0, unsigned d) {
  while ((unsigned)__builtin_amdgcn_s_memtime() - t0 < d) __builtin_amdgcn_s_sleep(1);
}
__device__ __forceinline__ unsigned bell_time(const char* a, unsigned vo, unsigned tag, unsigned cap) {
  unsigned v;
  for (unsigned spins = 0;; ++spins) {
    asm volatile("global_load_dword %0, %1, %2 sc1\n\ts_waitcnt vmcnt(0)" : "=&v"(v) : "v"(vo), "s"(a) : "memory");
    if (__all((v >> 24) >= tag) || spins >= cap) break;
  }
  const unsigned now = (unsigned)__builtin_amdgcn_s_memrealtime() + 250u;
  const unsigned age = rowmin16((now - v) & 0xFFFFFFu);
  return now - min(min(rdl(age, 0), rdl(age, 16)), 500u);
}
__device__ __forceinline__ void wait_rt(unsigned t0, int d) {
  while ((int)((unsigned)__builtin_amdgcn_s_memrealtime() - t0) < d) { }
}
#define WBAR() do { __builtin_amdgcn_s_barrier(); asm volatile("" ::: "memory"); } while (0)
#define SB() __builtin_amdgcn_sched_barrier(0)
#define TN(k, v) do { } while (0)
__device__ __forceinline__ void stage_proj(const char* src, unsigned lds, int tid, int wave) {
  const int row = tid >> 4, f = 2 * ((row & 1) | (((row >> 2) & 1) << 1));
#pragma unroll
  for (int j = 0; j < 4; ++j)
    gldsw(src + (size_t)j * 32 * HD * 2, (unsigned)(row * HD * 2 + (((tid & 15) ^ f ^ ((j & 1) << 3)) << 4)), lds + (wave * 64 + 512 * j) * 16);
}
__device__ __forceinline__ unsigned proj_base(int T, int lane) {
  const int g = lane >> 4, q4 = (lane & 15) >> 2, p4 = lane & 3, fs = 2 * ((q4 & 1) | ((g & 1) << 1) | ((q4 >> 1) << 2));
  return (unsigned)((4 * g + (q4 & 1) + 32 * (q4 >> 1)) * 256 + (((2 * T) ^ fs) << 4) + p4 * 8);
}
__device__ __forceinline__ u32x8 proj_frag(const char* smem, unsigned a) {
  const u32x2 t0 = trread(smem, a), t1 = trread(smem, a + 512), t2 = trread(smem, a + 4096), t3 = trread(smem, a + 4608);
  return u32x8{t0.x, t0.y, t1.x, t1.y, t2.x, t2.y, t3.x, t3.y};
}
__device__ __forceinline__ void flag_frags(unsigned fa, unsigned a, unsigned want, bf16x8 (&f)[4]) {
  u32x2 t[8];
  for (;;) {
    u32x4 fl;
    asm volatile("ds_read_b128 %0, %9\n\t"
                 "ds_read_b64_tr_b16 %1, %10\n\tds_read_b64_tr_b16 %2, %10 offset:64\n\t"
                 "ds_read_b64_tr_b16 %3, %10 offset:512\n\tds_read_b64_tr_b16 %4, %10 offset:576\n\t"
                 "ds_read_b64_tr_b16 %5, %10 offset:1024\n\tds_read_b64_tr_b16 %6, %10 offset:1088\n\t"
                 "ds_read_b64_tr_b16 %7, %10 offset:1536\n\tds_read_b64_tr_b16 %8, %10 offset:1600\n\ts_waitcnt lgkmcnt(0)"
                 : "=&v"(fl), "=&v"(t[0]), "=&v"(t[1]), "=&v"(t[2]), "=&v"(t[3]), "=&v"(t[4]), "=&v"(t[5]), "=&v"(t[6]), "=&v"(t[7])
                 : "v"(fa), "v"(a) : "memory");
    if (__builtin_amdgcn_readfirstlane(min(min(fl.x, fl.y), min(fl.z, fl.w))) >= want) break;
  }
#pragma unroll
  for (int ks = 0; ks < 4; ++ks) f[ks] = asbf8(u32x4{t[2 * ks].x, t[2 * ks].y, t[2 * ks + 1].x, t[2 * ks + 1].y});
}
__device__ __forceinline__ void wait_flags(const char* p, unsigned want) {
  for (;;) {
    const u32x4 fl = *(volatile LAS u32x4*)p;
    if (__builtin_amdgcn_readfirstlane(min(min(fl.x, fl.y), min(fl.z, fl.w))) >= want) break;
  }
}
__device__ __forceinline__ f32x4 zero4() { float z = 0.0f; asm volatile("" : "+v"(z)); return f32x4{z, z, z, z}; }
__device__ __forceinline__ unsigned lds_word(const char* p) { return __builtin_amdgcn_readfirstlane(*(volatile LAS unsigned*)p); }

#define MFMA16(a, b, c) __builtin_amdgcn_mfma_f32_16x16x16bf16_1k((a), (b), (c), 0, 0, 0)
#define UF(x) __uint_as_float(x)
__device__ __forceinline__ unsigned umax3(unsigned a, unsigned b, unsigned c) { return max(max(a, b), c); }
__device__ __forceinline__ f32x2 logit2(float a, float b) {
  return f32x2{a, b};
}
constexpr float LSC = LOG2E * SCALE;

struct QKW { u32x32 w0, w1, w2, w3, w4, w5; u32x8 x; };
template <int W_> __device__ __forceinline__ void qk_step(const QKW& W, const char* ag, const char* vs, const char* od, const char* kv, unsigned& cap, unsigned& sp,
                                                          unsigned long long& tc, unsigned long long* ts, unsigned& d0, unsigned& d1);
template <> __device__ __forceinline__ void qk_step<0>(const QKW& W, const char* ag, const char* vs, const char* od, const char* kv, unsigned& cap, unsigned& sp,
                                                        unsigned long long& tc, unsigned long long* ts, unsigned& d0, unsigned& d1) {
  unsigned st, bg;
  asm volatile("v_mbcnt_lo_u32_b32 v60, -1, 0\n\t"
    "v_mbcnt_hi_u32_b32 v60, -1, v60\n\t"
    "v_lshlrev_b32 v6, 4, v60\n\t"
    "s_mov_b32 %[sp], 0\n\t"
    "s_setprio 3\n\t"
    "global_load_dwordx4 v[40:43], v6, %[ag] sc1\n\t"
    "global_load_dwordx4 v[44:47], v6, %[ag] offset:1024 sc1\n\t"
    "s_memtime %[tc]\n\t"
    "v_add_u32 v7, 40960, v6\n\t"
    "v_add_u32 v8, 98304, v6\n\t"
    "ds_read_b128 v[24:27], v7 offset:0\n\t"
    "ds_read_b128 v[28:31], v7 offset:1024\n\t"
    "ds_read_b128 v[32:35], v7 offset:8192\n\t"
    "ds_read_b128 v[36:39], v7 offset:9216\n\t"
    "v_lshrrev_b32 v48, 4, v60\n\t"
    "v_and_b32 v49, 15, v60\n\t"
    "v_and_b32 v61, 4, v60\n\t"
    "v_mul_u32_u24 v61, 0x2aaa, v61\n\t"
    "v_lshrrev_b32 v9, 1, v60\n\t"
    "v_and_b32 v9, 2, v9\n\t"
    "v_or3_b32 v9, v9, v61, %[c44]\n\t"
    "v_and_b32 v61, 1, v48\n\t"
    "v_lshlrev_b32 v61, 3, v61\n\t"
    "v_lshl_or_b32 v14, v49, 4, v61\n\t"
    "v_add_u32 v14, 0, v14\n\t"
    "v_lshlrev_b32 v61, 2, v48\n\t"
    "v_lshl_or_b32 v10, v49, 4, v61\n\t"
    "v_lshrrev_b32 v50, 2, v49\n\t"
    "v_and_b32 v51, 3, v60\n\t"
    "v_and_b32 v61, 1, v60\n\t"
    "v_lshlrev_b32 v61, 3, v61\n\t"
    "v_lshl_or_b32 v11, v50, 4, v61\n\t"
    "v_lshl_or_b32 v11, v48, 7, v11\n\t"
    "v_xor_b32 v12, v48, v49\n\t"
    "v_lshlrev_b32 v12, 4, v12\n\t"
    "v_lshl_or_b32 v12, v49, 8, v12\n\t"
    "v_lshl_or_b32 v50, v48, 2, v50\n\t"
    "v_lshrrev_b32 v51, 1, v51\n\t"
    "v_or_b32 v51, 0, v51\n\t"
    "v_xor_b32 v51, v51, v50\n\t"
    "v_lshl_or_b32 v13, v51, 4, v61\n\t"
    "v_lshl_or_b32 v13, v50, 8, v13\n\t"
    "v_and_b32 v61, 3, v60\n\t"
    "v_lshlrev_b32 v61, 4, v61\n\t"
    "v_lshl_or_b32 v15, v48, 6, v61\n\t"
    "v_and_b32 v61, 8, v60\n\t"
    "v_lshl_or_b32 v15, v61, 14, v15\n\t"
    "v_and_b32 v61, 4, v60\n\t"
    "v_lshl_or_b32 v15, v61, 8, v15\n\t"
    "v_add_u32 v15, 0, v15\n\t"
    "3:\n\t"
    "v_mov_b32 v16, 0\n\t"
    "v_mov_b32 v17, 0\n\t"
    "v_mov_b32 v18, 0\n\t"
    "v_mov_b32 v19, 0\n\t"
    "v_mov_b32 v20, 0\n\t"
    "v_mov_b32 v21, 0\n\t"
    "v_mov_b32 v22, 0\n\t"
    "v_mov_b32 v23, 0\n\t"
    "s_waitcnt vmcnt(0) lgkmcnt(0)\n\t"
    "14:\n\t"
    "ds_write_b128 v6, v[40:43] offset:0\n\t"
    "ds_write_b128 v6, v[44:47] offset:1024\n\t"
    "v_smfmac_f32_16x16x64_bf16 v[16:19], v[40:43], v[64:71], v9\n\t"
    "v_max3_u32 v60, v40, v41, v42\n\t"
    "v_max3_u32 v61, v43, v44, v45\n\t"
    "v_smfmac_f32_16x16x64_bf16 v[20:23], v[40:43], v[72:79], v9\n\t"
    "v_max3_u32 v60, v60, v46, v47\n\t"
    "v_max_u32 v60, v60, v61\n\t"
    "v_cmp_eq_u32 vcc, -1, v60\n\t"
    "s_cbranch_vccnz 1f\n\t"
    "2:\n\t"
    "s_waitcnt lgkmcnt(0)\n\t"
    "s_barrier\n\t"
    "ds_read_b128 v[40:43], v6 offset:12288\n\t"
    "ds_read_b128 v[48:51], v6 offset:2048\n\t"
    "v_smfmac_f32_16x16x64_bf16 v[16:19], v[44:47], v[80:87], v9\n\t"
    "ds_read_b128 v[52:55], v6 offset:3072\n\t"
    "ds_read_b128 v[56:59], v6 offset:4096\n\t"
    "v_smfmac_f32_16x16x64_bf16 v[20:23], v[44:47], v[88:95], v9\n\t"
    "ds_read_b128 v[60:63], v6 offset:13312\n\t"
    "ds_read_b128 v[44:47], v6 offset:5120\n\t"
    "s_waitcnt lgkmcnt(5)\n\t"
    "v_smfmac_f32_16x16x64_bf16 v[16:19], v[40:43], v[24:31], v9\n\t"
    "ds_read_b128 v[24:27], v7 offset:16384\n\t"
    "ds_read_b128 v[28:31], v7 offset:17408\n\t"
    "v_smfmac_f32_16x16x64_bf16 v[20:23], v[40:43], v[32:39], v9\n\t"
    "ds_read_b128 v[32:35], v7 offset:24576\n\t"
    "ds_read_b128 v[36:39], v7 offset:25600\n\t"
    "ds_read_b128 v[40:43], v6 offset:6144\n\t"
    "s_waitcnt lgkmcnt(9)\n\t"
    "v_smfmac_f32_16x16x64_bf16 v[16:19], v[48:51], v[96:103], v9\n\t"
    "v_smfmac_f32_16x16x64_bf16 v[20:23], v[48:51], v[104:111], v9\n\t"
    "ds_read_b128 v[48:51], v6 offset:7168\n\t"
    "s_waitcnt lgkmcnt(9)\n\t"
    "v_smfmac_f32_16x16x64_bf16 v[16:19], v[52:55], v[112:119], v9\n\t"
    "v_smfmac_f32_16x16x64_bf16 v[20:23], v[52:55], v[120:127], v9\n\t"
    "ds_read_b128 v[52:55], v6 offset:14336\n\t"
    "s_waitcnt lgkmcnt(9)\n\t"
    "v_smfmac_f32_16x16x64_bf16 v[16:19], v[56:59], v[128:135], v9\n\t"
    "v_smfmac_f32_16x16x64_bf16 v[20:23], v[56:59], v[136:143], v9\n\t"
    "ds_read_b128 v[56:59], v6 offset:8192\n\t"
    "s_waitcnt lgkmcnt(6)\n\t"
    "v_smfmac_f32_16x16x64_bf16 v[16:19], v[60:63], v[24:31], v9\n\t"
    "ds_read_b128 v[24:27], v8 offset:0\n\t"
    "ds_read_b128 v[28:31], v8 offset:1024\n\t"
    "s_waitcnt lgkmcnt(6)\n\t"
    "v_smfmac_f32_16x16x64_bf16 v[20:23], v[60:63], v[32:39], v9\n\t"
    "ds_read_b128 v[32:35], v8 offset:8192\n\t"
    "ds_read_b128 v[36:39], v8 offset:9216\n\t"
    "ds_read_b128 v[60:63], v6 offset:9216\n\t"
    "v_smfmac_f32_16x16x64_bf16 v[16:19], v[44:47], v[144:151], v9\n\t"
    "v_smfmac_f32_16x16x64_bf16 v[20:23], v[44:47], v[152:159], v9\n\t"
    "ds_read_b128 v[44:47], v6 offset:10240\n\t"
    "s_waitcnt lgkmcnt(9)\n\t"
    "v_smfmac_f32_16x16x64_bf16 v[16:19], v[40:43], v[160:167], v9\n\t"
    "v_smfmac_f32_16x16x64_bf16 v[20:23], v[40:43], v[168:175], v9\n\t"
    "ds_read_b128 v[40:43], v6 offset:15360\n\t"
    "s_waitcnt lgkmcnt(9)\n\t"
    "v_smfmac_f32_16x16x64_bf16 v[16:19], v[48:51], v[176:183], v9\n\t"
    "v_smfmac_f32_16x16x64_bf16 v[20:23], v[48:51], v[184:191], v9\n\t"
    "ds_read_b128 v[48:51], v6 offset:11264\n\t"
    "s_waitcnt lgkmcnt(6)\n\t"
    "v_smfmac_f32_16x16x64_bf16 v[16:19], v[52:55], v[24:31], v9\n\t"
    "ds_read_b128 v[24:27], v8 offset:16384\n\t"
    "ds_read_b128 v[28:31], v8 offset:17408\n\t"
    "s_waitcnt lgkmcnt(6)\n\t"
    "v_smfmac_f32_16x16x64_bf16 v[20:23], v[52:55], v[32:39], v9\n\t"
    "ds_read_b128 v[32:35], v8 offset:24576\n\t"
    "ds_read_b128 v[36:39], v8 offset:25600\n\t"
    "v_smfmac_f32_16x16x64_bf16 v[16:19], v[56:59], v[192:199], v9\n\t"
    "v_smfmac_f32_16x16x64_bf16 v[20:23], v[56:59], v[200:207], v9\n\t"
    "s_waitcnt lgkmcnt(7)\n\t"
    "v_smfmac_f32_16x16x64_bf16 v[16:19], v[60:63], v[208:215], v9\n\t"
    "v_smfmac_f32_16x16x64_bf16 v[20:23], v[60:63], v[216:223], v9\n\t"
    "s_waitcnt lgkmcnt(6)\n\t"
    "v_smfmac_f32_16x16x64_bf16 v[16:19], v[44:47], v[224:231], v9\n\t"
    "v_smfmac_f32_16x16x64_bf16 v[20:23], v[44:47], v[232:239], v9\n\t"
    "s_waitcnt lgkmcnt(2)\n\t"
    "v_smfmac_f32_16x16x64_bf16 v[16:19], v[40:43], v[24:31], v9\n\t"
    "s_waitcnt lgkmcnt(0)\n\t"
    "v_smfmac_f32_16x16x64_bf16 v[20:23], v[40:43], v[32:39], v9\n\t"
    "v_smfmac_f32_16x16x64_bf16 v[16:19], v[48:51], v[240:247], v9\n\t"
    "v_smfmac_f32_16x16x64_bf16 v[20:23], v[48:51], v[248:255], v9\n\t"
    "v_xor_b32 v7, 64, v12\n\t"
    "v_xor_b32 v8, 0x80, v12\n\t"
    "v_xor_b32 v6, 0xc0, v12\n\t"
    "ds_read_b128 v[24:27], v12 offset:16384\n\t"
    "ds_read_b128 v[28:31], v12 offset:20480\n\t"
    "ds_read_b128 v[32:35], v7 offset:16384\n\t"
    "ds_read_b128 v[36:39], v7 offset:20480\n\t"
    "v_permlane16_swap_b32 v16, v18\n\t"
    "v_permlane16_swap_b32 v17, v19\n\t"
    "v_permlane16_swap_b32 v20, v22\n\t"
    "v_permlane16_swap_b32 v21, v23\n\t"
    "v_add_f32 v16, v16, v18\n\t"
    "v_add_f32 v17, v17, v19\n\t"
    "v_add_f32 v20, v20, v22\n\t"
    "v_mul_f32 v16, %[lsc], v16\n\t"
    "v_mul_f32 v17, %[lsc], v17\n\t"
    "v_cvt_pk_bf16_f32 v16, v16, v17\n\t"
    "v_add_f32 v21, v21, v23\n\t"
    "ds_write_b32 v10, v16 offset:32768\n\t"
    "v_mul_f32 v20, %[lsc], v20\n\t"
    "v_mul_f32 v21, %[lsc], v21\n\t"
    "v_cvt_pk_bf16_f32 v20, v20, v21\n\t"
    "ds_write_b32 v10, v20 offset:33024\n\t"
    "ds_read_b128 v[40:43], v8 offset:16384\n\t"
    "ds_read_b128 v[44:47], v8 offset:20480\n\t"
    "ds_read_b128 v[48:51], v6 offset:16384\n\t"
    "ds_read_b128 v[52:55], v6 offset:20480\n\t"
    "s_waitcnt lgkmcnt(4)\n\t"
    "s_barrier\n\t"
    "ds_read_b64_tr_b16 v[16:17], v11 offset:32768\n\t"
    "ds_read_b64_tr_b16 v[18:19], v11 offset:32832\n\t"
    "ds_read_b64_tr_b16 v[20:21], v11 offset:33280\n\t"
    "ds_read_b64_tr_b16 v[22:23], v11 offset:33344\n\t"
    "ds_read_b64_tr_b16 v[56:57], v11 offset:33792\n\t"
    "ds_read_b64_tr_b16 v[58:59], v11 offset:33856\n\t"
    "ds_read_b64_tr_b16 v[60:61], v11 offset:34304\n\t"
    "ds_read_b64_tr_b16 v[62:63], v11 offset:34368\n\t"
    "v_mov_b32 v6, 0\n\t"
    "v_mov_b32 v7, 0\n\t"
    "v_mov_b32 v8, 0\n\t"
    "v_mov_b32 v9, 0\n\t"
    "s_mov_b32 exec_hi, 0\n\t"
    "global_load_dwordx2 v[6:7], v14, %[vs] sc1\n\t"
    "global_load_dwordx2 v[8:9], v14, %[vs] offset:256 sc1\n\t"
    "s_mov_b32 exec_hi, -1\n\t"
    "s_waitcnt lgkmcnt(6)\n\t"
    "v_mfma_f32_16x16x32_bf16 v[24:27], v[24:27], v[16:19], 0\n\t"
    "v_mfma_f32_16x16x32_bf16 v[28:31], v[28:31], v[16:19], 0\n\t"
    "s_waitcnt lgkmcnt(4)\n\t"
    "v_mfma_f32_16x16x32_bf16 v[24:27], v[32:35], v[20:23], v[24:27]\n\t"
    "v_mfma_f32_16x16x32_bf16 v[28:31], v[36:39], v[20:23], v[28:31]\n\t"
    "v_xor_b32 v12, 32, v13\n\t"
    "ds_read_b64_tr_b16 v[32:33], v13 offset:24576\n\t"
    "ds_read_b64_tr_b16 v[34:35], v13 offset:28672\n\t"
    "ds_read_b64_tr_b16 v[36:37], v12 offset:24576\n\t"
    "ds_read_b64_tr_b16 v[38:39], v12 offset:28672\n\t"
    "s_waitcnt lgkmcnt(6)\n\t"
    "v_mfma_f32_16x16x32_bf16 v[24:27], v[40:43], v[56:59], v[24:27]\n\t"
    "v_mfma_f32_16x16x32_bf16 v[28:31], v[44:47], v[56:59], v[28:31]\n\t"
    "s_waitcnt lgkmcnt(4)\n\t"
    "v_mfma_f32_16x16x32_bf16 v[24:27], v[48:51], v[60:63], v[24:27]\n\t"
    "v_mfma_f32_16x16x32_bf16 v[28:31], v[52:55], v[60:63], v[28:31]\n\t"
    "s_setprio 1\n\t"
    "v_mov_b32 v52, %[one]\n\t"
    "v_mov_b32 v53, %[one]\n\t"
    "v_mov_b32 v54, %[one]\n\t"
    "v_mov_b32 v55, %[one]\n\t"
    "s_nop 2\n\t"
    "v_max3_f32 v10, |v24|, |v25|, |v26|\n\t"
    "v_max3_f32 v40, |v27|, |v28|, |v29|\n\t"
    "v_max3_f32 v10, v10, |v30|, |v31|\n\t"
    "v_max_f32 v10, v10, v40\n\t"
    "v_cmp_nge_f32 vcc, %[big], v10\n\t"
    "s_mov_b32 %[bg], %[big]\n\t"
    "v_mov_b32 v10, 0\n\t"
    "s_cbranch_vccnz 12f\n\t"
    "13:\n\t"
    "v_exp_f32 v24, v24\n\t"
    "v_exp_f32 v25, v25\n\t"
    "v_exp_f32 v26, v26\n\t"
    "v_exp_f32 v27, v27\n\t"
    "v_exp_f32 v28, v28\n\t"
    "v_exp_f32 v29, v29\n\t"
    "v_exp_f32 v30, v30\n\t"
    "v_exp_f32 v31, v31\n\t"
    "v_cvt_pk_bf16_f32 v40, v24, v25\n\t"
    "v_cvt_pk_bf16_f32 v41, v26, v27\n\t"
    "v_cvt_pk_bf16_f32 v42, v28, v29\n\t"
    "v_cvt_pk_bf16_f32 v43, v30, v31\n\t"
    "s_setprio 3\n\t"
    "s_waitcnt lgkmcnt(0)\n\t"
    "s_nop 0\n\t"
    "v_mfma_f32_16x16x32_bf16 v[44:47], v[32:35], v[40:43], 0\n\t"
    "v_mfma_f32_16x16x32_bf16 v[48:51], v[36:39], v[40:43], 0\n\t"
    "v_mfma_f32_16x16x32_bf16 v[52:55], v[52:55], v[40:43], 0\n\t"
    "v_mov_b32 v12, 0\n\t"
    "v_mov_b32 v13, 0\n\t"
    "s_mov_b32 exec_hi, 0\n\t"
    "v_mov_b32 v12, %[one]\n\t"
    "v_mov_b32 v13, %[one]\n\t"
    "s_mov_b32 exec_hi, -1\n\t"
    "s_barrier\n\t"
    "ds_read_b64_tr_b16 v[24:25], v11 offset:34816\n\t"
    "ds_read_b64_tr_b16 v[26:27], v11 offset:34880\n\t"
    "ds_read_b64_tr_b16 v[28:29], v11 offset:35328\n\t"
    "ds_read_b64_tr_b16 v[30:31], v11 offset:35392\n\t"
    "ds_read_b64_tr_b16 v[32:33], v11 offset:35840\n\t"
    "ds_read_b64_tr_b16 v[34:35], v11 offset:35904\n\t"
    "ds_read_b64_tr_b16 v[36:37], v11 offset:36352\n\t"
    "ds_read_b64_tr_b16 v[38:39], v11 offset:36416\n\t"
    "s_waitcnt lgkmcnt(6)\n\t"
    "v_mfma_f32_16x16x32_bf16 v[40:43], v[24:27], v[16:19], 0\n\t"
    "s_waitcnt lgkmcnt(4)\n\t"
    "v_mfma_f32_16x16x32_bf16 v[40:43], v[28:31], v[20:23], v[40:43]\n\t"
    "s_waitcnt lgkmcnt(2)\n\t"
    "v_mfma_f32_16x16x32_bf16 v[40:43], v[32:35], v[56:59], v[40:43]\n\t"
    "s_waitcnt lgkmcnt(0)\n\t"
    "v_mfma_f32_16x16x32_bf16 v[40:43], v[36:39], v[60:63], v[40:43]\n\t"
    "5:\n\t"
    "s_waitcnt vmcnt(0)\n\t"
    "v_max3_u32 v11, v6, v7, v8\n\t"
    "v_max_u32 v11, v11, v9\n\t"
    "v_cmp_eq_u32 vcc, -1, v11\n\t"
    "s_cbranch_vccnz 4f\n\t"
    "6:\n\t"
    "s_nop 2\n\t"
    "v_exp_f32 v28, v40\n\t"
    "v_max3_f32 v11, v40, v41, v42\n\t"
    "v_exp_f32 v29, v41\n\t"
    "v_max_f32 v11, v11, v43\n\t"
    "v_exp_f32 v30, v42\n\t"
    "v_cmp_nge_f32 vcc, %[bg], v11\n\t"
    "v_exp_f32 v31, v43\n\t"
    "s_cbranch_vccnz 10f\n\t"
    "11:\n\t"
    "v_cvt_pk_bf16_f32 v24, v28, v29\n\t"
    "v_cvt_pk_bf16_f32 v25, v30, v31\n\t"
    "s_nop 1\n\t"
    "v_mfma_f32_16x16x16_bf16 v[52:55], v[12:13], v[24:25], v[52:55]\n\t"
    "v_mfma_f32_16x16x16_bf16 v[44:47], v[6:7], v[24:25], v[44:47]\n\t"
    "v_mfma_f32_16x16x16_bf16 v[48:51], v[8:9], v[24:25], v[48:51]\n\t"
    "s_nop 5\n\t"
    "v_rcp_f32 v26, v52\n\t"
    "s_nop 0\n\t"
    "v_pk_mul_f32 v[44:45], v[44:45], v[26:27] op_sel_hi:[1,0]\n\t"
    "v_pk_mul_f32 v[46:47], v[46:47], v[26:27] op_sel_hi:[1,0]\n\t"
    "v_pk_mul_f32 v[48:49], v[48:49], v[26:27] op_sel_hi:[1,0]\n\t"
    "v_pk_mul_f32 v[50:51], v[50:51], v[26:27] op_sel_hi:[1,0]\n\t"
    "v_cvt_pk_bf16_f32 v36, v44, v45\n\t"
    "v_cvt_pk_bf16_f32 v37, v46, v47\n\t"
    "v_cvt_pk_bf16_f32 v38, v48, v49\n\t"
    "v_cvt_pk_bf16_f32 v39, v50, v51\n\t"
    "v_min_u32 v36, -2, v36\n\t"
    "v_min_u32 v37, -2, v37\n\t"
    "v_min_u32 v38, -2, v38\n\t"
    "v_min_u32 v39, -2, v39\n\t"
    "global_store_dwordx4 v15, v[36:39], %[od] sc1\n\t"
    "s_barrier\n\t"
    "v_permlane16_swap_b32 v6, v8\n\t"
    "v_permlane16_swap_b32 v7, v9\n\t"
    "v_and_b32 v11, 8, v14\n\t"
    "v_mul_u32_u24 v11, 31, v11\n\t"
    "v_add_u32 v14, v14, v11\n\t"
    "s_mov_b32 exec_hi, 0\n\t"
    "global_store_dwordx4 v14, v[6:9], %[kv] sc1\n\t"
    "s_mov_b32 exec_hi, -1\n\t"
    "s_nop 0\n\t"
    "s_waitcnt lgkmcnt(0)\n\t"
    "s_branch 9f\n\t"
    "1:\n\t"
    "s_add_u32 %[sp], %[sp], 1\n\t"
    "s_cmp_ge_u32 %[sp], %[cap]\n\t"
    "s_cbranch_scc1 7f\n\t"
    "global_load_dwordx4 v[40:43], v6, %[ag] sc1\n\t"
    "global_load_dwordx4 v[44:47], v6, %[ag] offset:1024 sc1\n\t"
    "s_waitcnt lgkmcnt(0)\n\t"
    "s_memtime %[tc]\n\t"
    "s_nop 7\n\t"
    "s_branch 3b\n\t"
    "7:\n\t"
    "s_mov_b32 %[cap], 0\n\t"
    "s_branch 2b\n\t"
    "4:\n\t"
    "s_add_u32 %[sp], %[sp], 0x10000\n\t"
    "s_lshr_b32 %[st], %[sp], 16\n\t"
    "s_cmp_ge_u32 %[st], %[cap]\n\t"
    "s_cbranch_scc1 8f\n\t"
    "s_mov_b32 exec_hi, 0\n\t"
    "global_load_dwordx2 v[6:7], v14, %[vs] sc1\n\t"
    "global_load_dwordx2 v[8:9], v14, %[vs] offset:256 sc1\n\t"
    "s_mov_b32 exec_hi, -1\n\t"
    "s_branch 5b\n\t"
    "8:\n\t"
    "s_mov_b32 %[cap], 0\n\t"
    "s_branch 6b\n\t"
    "10:\n\t"
    "v_max_f32 v11, v11, v10\n\t"
    "v_mov_b32 v26, v11\n\t"
    "s_nop 1\n\t"
    "v_permlane16_swap_b32 v11, v26\n\t"
    "v_max_f32 v11, v11, v26\n\t"
    "v_sub_f32 v28, v40, v11\n\t"
    "v_sub_f32 v29, v41, v11\n\t"
    "v_sub_f32 v30, v42, v11\n\t"
    "v_sub_f32 v31, v43, v11\n\t"
    "v_sub_f32 v11, v10, v11\n\t"
    "v_exp_f32 v11, v11\n\t"
    "v_exp_f32 v28, v28\n\t"
    "v_exp_f32 v29, v29\n\t"
    "v_exp_f32 v30, v30\n\t"
    "v_exp_f32 v31, v31\n\t"
    "v_mul_f32 v44, v44, v11\n\t"
    "v_mul_f32 v45, v45, v11\n\t"
    "v_mul_f32 v46, v46, v11\n\t"
    "v_mul_f32 v47, v47, v11\n\t"
    "v_mul_f32 v48, v48, v11\n\t"
    "v_mul_f32 v49, v49, v11\n\t"
    "v_mul_f32 v50, v50, v11\n\t"
    "v_mul_f32 v51, v51, v11\n\t"
    "v_mul_f32 v52, v52, v11\n\t"
    "v_mul_f32 v53, v53, v11\n\t"
    "v_mul_f32 v54, v54, v11\n\t"
    "v_mul_f32 v55, v55, v11\n\t"
    "s_branch 11b\n\t"
    "12:\n\t"
    "v_max3_f32 v10, v24, v25, v26\n\t"
    "v_max3_f32 v40, v27, v28, v29\n\t"
    "v_max3_f32 v10, v10, v30, v31\n\t"
    "v_max3_f32 v10, v10, v40, %[neg]\n\t"
    "v_mov_b32 v40, v10\n\t"
    "s_nop 1\n\t"
    "v_permlane16_swap_b32 v10, v40\n\t"
    "v_max_f32 v10, v10, v40\n\t"
    "v_mov_b32 v40, v10\n\t"
    "s_nop 1\n\t"
    "v_permlane32_swap_b32 v10, v40\n\t"
    "v_max_f32 v10, v10, v40\n\t"
    "v_sub_f32 v24, v24, v10\n\t"
    "v_sub_f32 v25, v25, v10\n\t"
    "v_sub_f32 v26, v26, v10\n\t"
    "v_sub_f32 v27, v27, v10\n\t"
    "v_sub_f32 v28, v28, v10\n\t"
    "v_sub_f32 v29, v29, v10\n\t"
    "v_sub_f32 v30, v30, v10\n\t"
    "v_sub_f32 v31, v31, v10\n\t"
    "s_mov_b32 %[bg], 0x7fc00000\n\t"
    "s_branch 13b\n\t"
    "9:"
    : [tc] "=&s"(tc), [sp] "=&s"(sp), [cap] "+s"(cap), [st] "=&s"(st), [bg] "=&s"(bg)
    : [ag] "s"(ag), [c44] "s"(0x4444u), [kv] "s"(kv), [vs] "s"(vs), [od] "s"(od), [od2] "s"(od + 262144), [neg] "s"(-1.0e38f), [lsc] "s"(LSC), [one] "s"(0x3F803F80u), [big] "s"(24.0f), "{v[64:95]}"(W.w0), "{v[96:127]}"(W.w1), "{v[128:159]}"(W.w2), "{v[160:191]}"(W.w3), "{v[192:223]}"(W.w4), "{v[224:255]}"(W.w5)
    : "v6", "v7", "v8", "v9", "v10", "v11", "v12", "v13", "v14", "v15", "v16", "v17", "v18", "v19", "v20", "v21", "v22", "v23", "v24", "v25", "v26", "v27", "v28", "v29", "v30", "v31", "v32", "v33", "v34", "v35", "v36", "v37", "v38", "v39", "v40", "v41", "v42", "v43", "v44", "v45", "v46", "v47", "v48", "v49", "v50", "v51", "v52", "v53", "v54", "v55", "v56", "v57", "v58", "v59", "v60", "v61", "v62", "v63", "vcc", "scc", "memory");
}
template <> __device__ __forceinline__ void qk_step<1>(const QKW& W, const char* ag, const char* vs, const char* od, const char* kv, unsigned& cap, unsigned& sp,
                                                        unsigned long long& tc, unsigned long long* ts, unsigned& d0, unsigned& d1) {
  unsigned st, bg;
  asm volatile("v_mbcnt_lo_u32_b32 v60, -1, 0\n\t"
    "v_mbcnt_hi_u32_b32 v60, -1, v60\n\t"
    "v_lshlrev_b32 v6, 4, v60\n\t"
    "s_mov_b32 %[sp], 0\n\t"
    "s_setprio 3\n\t"
    "global_load_dwordx4 v[40:43], v6, %[ag] sc1\n\t"
    "global_load_dwordx4 v[44:47], v6, %[ag] offset:1024 sc1\n\t"
    "s_memtime %[tc]\n\t"
    "v_add_u32 v7, 43008, v6\n\t"
    "v_add_u32 v8, 100352, v6\n\t"
    "ds_read_b128 v[24:27], v7 offset:0\n\t"
    "ds_read_b128 v[28:31], v7 offset:1024\n\t"
    "ds_read_b128 v[32:35], v7 offset:8192\n\t"
    "ds_read_b128 v[36:39], v7 offset:9216\n\t"
    "v_lshrrev_b32 v48, 4, v60\n\t"
    "v_and_b32 v49, 15, v60\n\t"
    "v_and_b32 v61, 4, v60\n\t"
    "v_mul_u32_u24 v61, 0x2aaa, v61\n\t"
    "v_lshrrev_b32 v9, 1, v60\n\t"
    "v_and_b32 v9, 2, v9\n\t"
    "v_or3_b32 v9, v9, v61, %[c44]\n\t"
    "v_and_b32 v61, 1, v48\n\t"
    "v_lshlrev_b32 v61, 3, v61\n\t"
    "v_lshl_or_b32 v14, v49, 4, v61\n\t"
    "v_add_u32 v14, 512, v14\n\t"
    "v_lshlrev_b32 v61, 2, v48\n\t"
    "v_lshl_or_b32 v10, v49, 4, v61\n\t"
    "v_lshrrev_b32 v50, 2, v49\n\t"
    "v_and_b32 v51, 3, v60\n\t"
    "v_and_b32 v61, 1, v60\n\t"
    "v_lshlrev_b32 v61, 3, v61\n\t"
    "v_lshl_or_b32 v11, v50, 4, v61\n\t"
    "v_lshl_or_b32 v11, v48, 7, v11\n\t"
    "v_xor_b32 v12, v48, v49\n\t"
    "v_lshlrev_b32 v12, 4, v12\n\t"
    "v_lshl_or_b32 v12, v49, 8, v12\n\t"
    "v_lshl_or_b32 v50, v48, 2, v50\n\t"
    "v_lshrrev_b32 v51, 1, v51\n\t"
    "v_or_b32 v51, 4, v51\n\t"
    "v_xor_b32 v51, v51, v50\n\t"
    "v_lshl_or_b32 v13, v51, 4, v61\n\t"
    "v_lshl_or_b32 v13, v50, 8, v13\n\t"
    "v_and_b32 v61, 3, v60\n\t"
    "v_lshlrev_b32 v61, 4, v61\n\t"
    "v_lshl_or_b32 v15, v48, 6, v61\n\t"
    "v_and_b32 v61, 8, v60\n\t"
    "v_lshl_or_b32 v15, v61, 14, v15\n\t"
    "v_and_b32 v61, 4, v60\n\t"
    "v_lshl_or_b32 v15, v61, 8, v15\n\t"
    "v_add_u32 v15, 256, v15\n\t"
    "3:\n\t"
    "v_mov_b32 v16, 0\n\t"
    "v_mov_b32 v17, 0\n\t"
    "v_mov_b32 v18, 0\n\t"
    "v_mov_b32 v19, 0\n\t"
    "v_mov_b32 v20, 0\n\t"
    "v_mov_b32 v21, 0\n\t"
    "v_mov_b32 v22, 0\n\t"
    "v_mov_b32 v23, 0\n\t"
    "s_waitcnt vmcnt(0) lgkmcnt(0)\n\t"
    "14:\n\t"
    "ds_write_b128 v6, v[40:43] offset:2048\n\t"
    "ds_write_b128 v6, v[44:47] offset:3072\n\t"
    "v_smfmac_f32_16x16x64_bf16 v[16:19], v[40:43], v[96:103], v9\n\t"
    "v_max3_u32 v60, v40, v41, v42\n\t"
    "v_max3_u32 v61, v43, v44, v45\n\t"
    "v_smfmac_f32_16x16x64_bf16 v[20:23], v[40:43], v[104:111], v9\n\t"
    "v_max3_u32 v60, v60, v46, v47\n\t"
    "v_max_u32 v60, v60, v61\n\t"
    "v_cmp_eq_u32 vcc, -1, v60\n\t"
    "s_cbranch_vccnz 1f\n\t"
    "2:\n\t"
    "s_waitcnt lgkmcnt(0)\n\t"
    "s_barrier\n\t"
    "ds_read_b128 v[40:43], v6 offset:12288\n\t"
    "ds_read_b128 v[48:51], v6 offset:0\n\t"
    "v_smfmac_f32_16x16x64_bf16 v[16:19], v[44:47], v[112:119], v9\n\t"
    "ds_read_b128 v[52:55], v6 offset:1024\n\t"
    "ds_read_b128 v[56:59], v6 offset:4096\n\t"
    "v_smfmac_f32_16x16x64_bf16 v[20:23], v[44:47], v[120:127], v9\n\t"
    "ds_read_b128 v[60:63], v6 offset:13312\n\t"
    "ds_read_b128 v[44:47], v6 offset:5120\n\t"
    "s_waitcnt lgkmcnt(5)\n\t"
    "v_smfmac_f32_16x16x64_bf16 v[16:19], v[40:43], v[24:31], v9\n\t"
    "ds_read_b128 v[24:27], v7 offset:16384\n\t"
    "ds_read_b128 v[28:31], v7 offset:17408\n\t"
    "v_smfmac_f32_16x16x64_bf16 v[20:23], v[40:43], v[32:39], v9\n\t"
    "ds_read_b128 v[32:35], v7 offset:24576\n\t"
    "ds_read_b128 v[36:39], v7 offset:25600\n\t"
    "ds_read_b128 v[40:43], v6 offset:6144\n\t"
    "s_waitcnt lgkmcnt(9)\n\t"
    "v_smfmac_f32_16x16x64_bf16 v[16:19], v[48:51], v[64:71], v9\n\t"
    "v_smfmac_f32_16x16x64_bf16 v[20:23], v[48:51], v[72:79], v9\n\t"
    "ds_read_b128 v[48:51], v6 offset:7168\n\t"
    "s_waitcnt lgkmcnt(9)\n\t"
    "v_smfmac_f32_16x16x64_bf16 v[16:19], v[52:55], v[80:87], v9\n\t"
    "v_smfmac_f32_16x16x64_bf16 v[20:23], v[52:55], v[88:95], v9\n\t"
    "ds_read_b128 v[52:55], v6 offset:14336\n\t"
    "s_waitcnt lgkmcnt(9)\n\t"
    "v_smfmac_f32_16x16x64_bf16 v[16:19], v[56:59], v[128:135], v9\n\t"
    "v_smfmac_f32_16x16x64_bf16 v[20:23], v[56:59], v[136:143], v9\n\t"
    "ds_read_b128 v[56:59], v6 offset:8192\n\t"
    "s_waitcnt lgkmcnt(6)\n\t"
    "v_smfmac_f32_16x16x64_bf16 v[16:19], v[60:63], v[24:31], v9\n\t"
    "ds_read_b128 v[24:27], v8 offset:0\n\t"
    "ds_read_b128 v[28:31], v8 offset:1024\n\t"
    "s_waitcnt lgkmcnt(6)\n\t"
    "v_smfmac_f32_16x16x64_bf16 v[20:23], v[60:63], v[32:39], v9\n\t"
    "ds_read_b128 v[32:35], v8 offset:8192\n\t"
    "ds_read_b128 v[36:39], v8 offset:9216\n\t"
    "ds_read_b128 v[60:63], v6 offset:9216\n\t"
    "v_smfmac_f32_16x16x64_bf16 v[16:19], v[44:47], v[144:151], v9\n\t"
    "v_smfmac_f32_16x16x64_bf16 v[20:23], v[44:47], v[152:159], v9\n\t"
    "ds_read_b128 v[44:47], v6 offset:10240\n\t"
    "s_waitcnt lgkmcnt(9)\n\t"
    "v_smfmac_f32_16x16x64_bf16 v[16:19], v[40:43], v[160:167], v9\n\t"
    "v_smfmac_f32_16x16x64_bf16 v[20:23], v[40:43], v[168:175], v9\n\t"
    "ds_read_b128 v[40:43], v6 offset:15360\n\t"
    "s_waitcnt lgkmcnt(9)\n\t"
    "v_smfmac_f32_16x16x64_bf16 v[16:19], v[48:51], v[176:183], v9\n\t"
    "v_smfmac_f32_16x16x64_bf16 v[20:23], v[48:51], v[184:191], v9\n\t"
    "ds_read_b128 v[48:51], v6 offset:11264\n\t"
    "s_waitcnt lgkmcnt(6)\n\t"
    "v_smfmac_f32_16x16x64_bf16 v[16:19], v[52:55], v[24:31], v9\n\t"
    "ds_read_b128 v[24:27], v8 offset:16384\n\t"
    "ds_read_b128 v[28:31], v8 offset:17408\n\t"
    "s_waitcnt lgkmcnt(6)\n\t"
    "v_smfmac_f32_16x16x64_bf16 v[20:23], v[52:55], v[32:39], v9\n\t"
    "ds_read_b128 v[32:35], v8 offset:24576\n\t"
    "ds_read_b128 v[36:39], v8 offset:25600\n\t"
    "v_smfmac_f32_16x16x64_bf16 v[16:19], v[56:59], v[192:199], v9\n\t"
    "v_smfmac_f32_16x16x64_bf16 v[20:23], v[56:59], v[200:207], v9\n\t"
    "s_waitcnt lgkmcnt(7)\n\t"
    "v_smfmac_f32_16x16x64_bf16 v[16:19], v[60:63], v[208:215], v9\n\t"
    "v_smfmac_f32_16x16x64_bf16 v[20:23], v[60:63], v[216:223], v9\n\t"
    "s_waitcnt lgkmcnt(6)\n\t"
    "v_smfmac_f32_16x16x64_bf16 v[16:19], v[44:47], v[224:231], v9\n\t"
    "v_smfmac_f32_16x16x64_bf16 v[20:23], v[44:47], v[232:239], v9\n\t"
    "s_waitcnt lgkmcnt(2)\n\t"
    "v_smfmac_f32_16x16x64_bf16 v[16:19], v[40:43], v[24:31], v9\n\t"
    "s_waitcnt lgkmcnt(0)\n\t"
    "v_smfmac_f32_16x16x64_bf16 v[20:23], v[40:43], v[32:39], v9\n\t"
    "v_smfmac_f32_16x16x64_bf16 v[16:19], v[48:51], v[240:247], v9\n\t"
    "v_smfmac_f32_16x16x64_bf16 v[20:23], v[48:51], v[248:255], v9\n\t"
    "v_xor_b32 v7, 64, v12\n\t"
    "v_xor_b32 v8, 0x80, v12\n\t"
    "v_xor_b32 v6, 0xc0, v12\n\t"
    "ds_read_b128 v[24:27], v12 offset:16384\n\t"
    "ds_read_b128 v[28:31], v12 offset:20480\n\t"
    "ds_read_b128 v[32:35], v7 offset:16384\n\t"
    "ds_read_b128 v[36:39], v7 offset:20480\n\t"
    "v_permlane16_swap_b32 v16, v18\n\t"
    "v_permlane16_swap_b32 v17, v19\n\t"
    "v_permlane16_swap_b32 v20, v22\n\t"
    "v_permlane16_swap_b32 v21, v23\n\t"
    "v_add_f32 v16, v16, v18\n\t"
    "v_add_f32 v17, v17, v19\n\t"
    "v_add_f32 v20, v20, v22\n\t"
    "v_mul_f32 v16, %[lsc], v16\n\t"
    "v_mul_f32 v17, %[lsc], v17\n\t"
    "v_cvt_pk_bf16_f32 v16, v16, v17\n\t"
    "v_add_f32 v21, v21, v23\n\t"
    "ds_write_b32 v10, v16 offset:33280\n\t"
    "v_mul_f32 v20, %[lsc], v20\n\t"
    "v_mul_f32 v21, %[lsc], v21\n\t"
    "v_cvt_pk_bf16_f32 v20, v20, v21\n\t"
    "ds_write_b32 v10, v20 offset:33536\n\t"
    "ds_read_b128 v[40:43], v8 offset:16384\n\t"
    "ds_read_b128 v[44:47], v8 offset:20480\n\t"
    "ds_read_b128 v[48:51], v6 offset:16384\n\t"
    "ds_read_b128 v[52:55], v6 offset:20480\n\t"
    "s_waitcnt lgkmcnt(4)\n\t"
    "s_barrier\n\t"
    "ds_read_b64_tr_b16 v[16:17], v11 offset:32768\n\t"
    "ds_read_b64_tr_b16 v[18:19], v11 offset:32832\n\t"
    "ds_read_b64_tr_b16 v[20:21], v11 offset:33280\n\t"
    "ds_read_b64_tr_b16 v[22:23], v11 offset:33344\n\t"
    "ds_read_b64_tr_b16 v[56:57], v11 offset:33792\n\t"
    "ds_read_b64_tr_b16 v[58:59], v11 offset:33856\n\t"
    "ds_read_b64_tr_b16 v[60:61], v11 offset:34304\n\t"
    "ds_read_b64_tr_b16 v[62:63], v11 offset:34368\n\t"
    "v_mov_b32 v6, 0\n\t"
    "v_mov_b32 v7, 0\n\t"
    "v_mov_b32 v8, 0\n\t"
    "v_mov_b32 v9, 0\n\t"
    "s_mov_b32 exec_hi, 0\n\t"
    "global_load_dwordx2 v[6:7], v14, %[vs] sc1\n\t"
    "global_load_dwordx2 v[8:9], v14, %[vs] offset:256 sc1\n\t"
    "s_mov_b32 exec_hi, -1\n\t"
    "s_waitcnt lgkmcnt(6)\n\t"
    "v_mfma_f32_16x16x32_bf16 v[24:27], v[24:27], v[16:19], 0\n\t"
    "v_mfma_f32_16x16x32_bf16 v[28:31], v[28:31], v[16:19], 0\n\t"
    "s_waitcnt lgkmcnt(4)\n\t"
    "v_mfma_f32_16x16x32_bf16 v[24:27], v[32:35], v[20:23], v[24:27]\n\t"
    "v_mfma_f32_16x16x32_bf16 v[28:31], v[36:39], v[20:23], v[28:31]\n\t"
    "v_xor_b32 v12, 32, v13\n\t"
    "ds_read_b64_tr_b16 v[32:33], v13 offset:24576\n\t"
    "ds_read_b64_tr_b16 v[34:35], v13 offset:28672\n\t"
    "ds_read_b64_tr_b16 v[36:37], v12 offset:24576\n\t"
    "ds_read_b64_tr_b16 v[38:39], v12 offset:28672\n\t"
    "s_waitcnt lgkmcnt(6)\n\t"
    "v_mfma_f32_16x16x32_bf16 v[24:27], v[40:43], v[56:59], v[24:27]\n\t"
    "v_mfma_f32_16x16x32_bf16 v[28:31], v[44:47], v[56:59], v[28:31]\n\t"
    "s_waitcnt lgkmcnt(4)\n\t"
    "v_mfma_f32_16x16x32_bf16 v[24:27], v[48:51], v[60:63], v[24:27]\n\t"
    "v_mfma_f32_16x16x32_bf16 v[28:31], v[52:55], v[60:63], v[28:31]\n\t"
    "s_setprio 1\n\t"
    "v_mov_b32 v52, %[one]\n\t"
    "v_mov_b32 v53, %[one]\n\t"
    "v_mov_b32 v54, %[one]\n\t"
    "v_mov_b32 v55, %[one]\n\t"
    "s_nop 2\n\t"
    "v_max3_f32 v10, |v24|, |v25|, |v26|\n\t"
    "v_max3_f32 v40, |v27|, |v28|, |v29|\n\t"
    "v_max3_f32 v10, v10, |v30|, |v31|\n\t"
    "v_max_f32 v10, v10, v40\n\t"
    "v_cmp_nge_f32 vcc, %[big], v10\n\t"
    "s_mov_b32 %[bg], %[big]\n\t"
    "v_mov_b32 v10, 0\n\t"
    "s_cbranch_vccnz 12f\n\t"
    "13:\n\t"
    "v_exp_f32 v24, v24\n\t"
    "v_exp_f32 v25, v25\n\t"
    "v_exp_f32 v26, v26\n\t"
    "v_exp_f32 v27, v27\n\t"
    "v_exp_f32 v28, v28\n\t"
    "v_exp_f32 v29, v29\n\t"
    "v_exp_f32 v30, v30\n\t"
    "v_exp_f32 v31, v31\n\t"
    "v_cvt_pk_bf16_f32 v40, v24, v25\n\t"
    "v_cvt_pk_bf16_f32 v41, v26, v27\n\t"
    "v_cvt_pk_bf16_f32 v42, v28, v29\n\t"
    "v_cvt_pk_bf16_f32 v43, v30, v31\n\t"
    "s_setprio 3\n\t"
    "s_waitcnt lgkmcnt(0)\n\t"
    "s_nop 0\n\t"
    "v_mfma_f32_16x16x32_bf16 v[44:47], v[32:35], v[40:43], 0\n\t"
    "v_mfma_f32_16x16x32_bf16 v[48:51], v[36:39], v[40:43], 0\n\t"
    "v_mfma_f32_16x16x32_bf16 v[52:55], v[52:55], v[40:43], 0\n\t"
    "v_mov_b32 v12, 0\n\t"
    "v_mov_b32 v13, 0\n\t"
    "s_mov_b32 exec_hi, 0\n\t"
    "v_mov_b32 v12, %[one]\n\t"
    "v_mov_b32 v13, %[one]\n\t"
    "s_mov_b32 exec_hi, -1\n\t"
    "s_barrier\n\t"
    "ds_read_b64_tr_b16 v[24:25], v11 offset:34816\n\t"
    "ds_read_b64_tr_b16 v[26:27], v11 offset:34880\n\t"
    "ds_read_b64_tr_b16 v[28:29], v11 offset:35328\n\t"
    "ds_read_b64_tr_b16 v[30:31], v11 offset:35392\n\t"
    "ds_read_b64_tr_b16 v[32:33], v11 offset:35840\n\t"
    "ds_read_b64_tr_b16 v[34:35], v11 offset:35904\n\t"
    "ds_read_b64_tr_b16 v[36:37], v11 offset:36352\n\t"
    "ds_read_b64_tr_b16 v[38:39], v11 offset:36416\n\t"
    "s_waitcnt lgkmcnt(6)\n\t"
    "v_mfma_f32_16x16x32_bf16 v[40:43], v[24:27], v[16:19], 0\n\t"
    "s_waitcnt lgkmcnt(4)\n\t"
    "v_mfma_f32_16x16x32_bf16 v[40:43], v[28:31], v[20:23], v[40:43]\n\t"
    "s_waitcnt lgkmcnt(2)\n\t"
    "v_mfma_f32_16x16x32_bf16 v[40:43], v[32:35], v[56:59], v[40:43]\n\t"
    "s_waitcnt lgkmcnt(0)\n\t"
    "v_mfma_f32_16x16x32_bf16 v[40:43], v[36:39], v[60:63], v[40:43]\n\t"
    "5:\n\t"
    "s_waitcnt vmcnt(0)\n\t"
    "v_max3_u32 v11, v6, v7, v8\n\t"
    "v_max_u32 v11, v11, v9\n\t"
    "v_cmp_eq_u32 vcc, -1, v11\n\t"
    "s_cbranch_vccnz 4f\n\t"
    "6:\n\t"
    "s_nop 2\n\t"
    "v_exp_f32 v28, v40\n\t"
    "v_max3_f32 v11, v40, v41, v42\n\t"
    "v_exp_f32 v29, v41\n\t"
    "v_max_f32 v11, v11, v43\n\t"
    "v_exp_f32 v30, v42\n\t"
    "v_cmp_nge_f32 vcc, %[bg], v11\n\t"
    "v_exp_f32 v31, v43\n\t"
    "s_cbranch_vccnz 10f\n\t"
    "11:\n\t"
    "v_cvt_pk_bf16_f32 v24, v28, v29\n\t"
    "v_cvt_pk_bf16_f32 v25, v30, v31\n\t"
    "s_nop 1\n\t"
    "v_mfma_f32_16x16x16_bf16 v[52:55], v[12:13], v[24:25], v[52:55]\n\t"
    "v_mfma_f32_16x16x16_bf16 v[44:47], v[6:7], v[24:25], v[44:47]\n\t"
    "v_mfma_f32_16x16x16_bf16 v[48:51], v[8:9], v[24:25], v[48:51]\n\t"
    "s_nop 5\n\t"
    "v_rcp_f32 v26, v52\n\t"
    "s_nop 0\n\t"
    "v_pk_mul_f32 v[44:45], v[44:45], v[26:27] op_sel_hi:[1,0]\n\t"
    "v_pk_mul_f32 v[46:47], v[46:47], v[26:27] op_sel_hi:[1,0]\n\t"
    "v_pk_mul_f32 v[48:49], v[48:49], v[26:27] op_sel_hi:[1,0]\n\t"
    "v_pk_mul_f32 v[50:51], v[50:51], v[26:27] op_sel_hi:[1,0]\n\t"
    "v_cvt_pk_bf16_f32 v36, v44, v45\n\t"
    "v_cvt_pk_bf16_f32 v37, v46, v47\n\t"
    "v_cvt_pk_bf16_f32 v38, v48, v49\n\t"
    "v_cvt_pk_bf16_f32 v39, v50, v51\n\t"
    "v_min_u32 v36, -2, v36\n\t"
    "v_min_u32 v37, -2, v37\n\t"
    "v_min_u32 v38, -2, v38\n\t"
    "v_min_u32 v39, -2, v39\n\t"
    "global_store_dwordx4 v15, v[36:39], %[od] sc1\n\t"
    "s_barrier\n\t"
    "v_permlane16_swap_b32 v6, v8\n\t"
    "v_permlane16_swap_b32 v7, v9\n\t"
    "v_and_b32 v11, 8, v14\n\t"
    "v_mul_u32_u24 v11, 31, v11\n\t"
    "v_add_u32 v14, v14, v11\n\t"
    "s_mov_b32 exec_hi, 0\n\t"
    "global_store_dwordx4 v14, v[6:9], %[kv] sc1\n\t"
    "s_mov_b32 exec_hi, -1\n\t"
    "s_nop 0\n\t"
    "s_waitcnt lgkmcnt(0)\n\t"
    "s_branch 9f\n\t"
    "1:\n\t"
    "s_add_u32 %[sp], %[sp], 1\n\t"
    "s_cmp_ge_u32 %[sp], %[cap]\n\t"
    "s_cbranch_scc1 7f\n\t"
    "global_load_dwordx4 v[40:43], v6, %[ag] sc1\n\t"
    "global_load_dwordx4 v[44:47], v6, %[ag] offset:1024 sc1\n\t"
    "s_waitcnt lgkmcnt(0)\n\t"
    "s_memtime %[tc]\n\t"
    "s_nop 7\n\t"
    "s_branch 3b\n\t"
    "7:\n\t"
    "s_mov_b32 %[cap], 0\n\t"
    "s_branch 2b\n\t"
    "4:\n\t"
    "s_add_u32 %[sp], %[sp], 0x10000\n\t"
    "s_lshr_b32 %[st], %[sp], 16\n\t"
    "s_cmp_ge_u32 %[st], %[cap]\n\t"
    "s_cbranch_scc1 8f\n\t"
    "s_mov_b32 exec_hi, 0\n\t"
    "global_load_dwordx2 v[6:7], v14, %[vs] sc1\n\t"
    "global_load_dwordx2 v[8:9], v14, %[vs] offset:256 sc1\n\t"
    "s_mov_b32 exec_hi, -1\n\t"
    "s_branch 5b\n\t"
    "8:\n\t"
    "s_mov_b32 %[cap], 0\n\t"
    "s_branch 6b\n\t"
    "10:\n\t"
    "v_max_f32 v11, v11, v10\n\t"
    "v_mov_b32 v26, v11\n\t"
    "s_nop 1\n\t"
    "v_permlane16_swap_b32 v11, v26\n\t"
    "v_max_f32 v11, v11, v26\n\t"
    "v_sub_f32 v28, v40, v11\n\t"
    "v_sub_f32 v29, v41, v11\n\t"
    "v_sub_f32 v30, v42, v11\n\t"
    "v_sub_f32 v31, v43, v11\n\t"
    "v_sub_f32 v11, v10, v11\n\t"
    "v_exp_f32 v11, v11\n\t"
    "v_exp_f32 v28, v28\n\t"
    "v_exp_f32 v29, v29\n\t"
    "v_exp_f32 v30, v30\n\t"
    "v_exp_f32 v31, v31\n\t"
    "v_mul_f32 v44, v44, v11\n\t"
    "v_mul_f32 v45, v45, v11\n\t"
    "v_mul_f32 v46, v46, v11\n\t"
    "v_mul_f32 v47, v47, v11\n\t"
    "v_mul_f32 v48, v48, v11\n\t"
    "v_mul_f32 v49, v49, v11\n\t"
    "v_mul_f32 v50, v50, v11\n\t"
    "v_mul_f32 v51, v51, v11\n\t"
    "v_mul_f32 v52, v52, v11\n\t"
    "v_mul_f32 v53, v53, v11\n\t"
    "v_mul_f32 v54, v54, v11\n\t"
    "v_mul_f32 v55, v55, v11\n\t"
    "s_branch 11b\n\t"
    "12:\n\t"
    "v_max3_f32 v10, v24, v25, v26\n\t"
    "v_max3_f32 v40, v27, v28, v29\n\t"
    "v_max3_f32 v10, v10, v30, v31\n\t"
    "v_max3_f32 v10, v10, v40, %[neg]\n\t"
    "v_mov_b32 v40, v10\n\t"
    "s_nop 1\n\t"
    "v_permlane16_swap_b32 v10, v40\n\t"
    "v_max_f32 v10, v10, v40\n\t"
    "v_mov_b32 v40, v10\n\t"
    "s_nop 1\n\t"
    "v_permlane32_swap_b32 v10, v40\n\t"
    "v_max_f32 v10, v10, v40\n\t"
    "v_sub_f32 v24, v24, v10\n\t"
    "v_sub_f32 v25, v25, v10\n\t"
    "v_sub_f32 v26, v26, v10\n\t"
    "v_sub_f32 v27, v27, v10\n\t"
    "v_sub_f32 v28, v28, v10\n\t"
    "v_sub_f32 v29, v29, v10\n\t"
    "v_sub_f32 v30, v30, v10\n\t"
    "v_sub_f32 v31, v31, v10\n\t"
    "s_mov_b32 %[bg], 0x7fc00000\n\t"
    "s_branch 13b\n\t"
    "9:"
    : [tc] "=&s"(tc), [sp] "=&s"(sp), [cap] "+s"(cap), [st] "=&s"(st), [bg] "=&s"(bg)
    : [ag] "s"(ag), [c44] "s"(0x4444u), [kv] "s"(kv), [vs] "s"(vs), [od] "s"(od), [od2] "s"(od + 262144), [neg] "s"(-1.0e38f), [lsc] "s"(LSC), [one] "s"(0x3F803F80u), [big] "s"(24.0f), "{v[64:95]}"(W.w0), "{v[96:127]}"(W.w1), "{v[128:159]}"(W.w2), "{v[160:191]}"(W.w3), "{v[192:223]}"(W.w4), "{v[224:255]}"(W.w5)
    : "v6", "v7", "v8", "v9", "v10", "v11", "v12", "v13", "v14", "v15", "v16", "v17", "v18", "v19", "v20", "v21", "v22", "v23", "v24", "v25", "v26", "v27", "v28", "v29", "v30", "v31", "v32", "v33", "v34", "v35", "v36", "v37", "v38", "v39", "v40", "v41", "v42", "v43", "v44", "v45", "v46", "v47", "v48", "v49", "v50", "v51", "v52", "v53", "v54", "v55", "v56", "v57", "v58", "v59", "v60", "v61", "v62", "v63", "vcc", "scc", "memory");
}
template <> __device__ __forceinline__ void qk_step<2>(const QKW& W, const char* ag, const char* vs, const char* od, const char* kv, unsigned& cap, unsigned& sp,
                                                        unsigned long long& tc, unsigned long long* ts, unsigned& d0, unsigned& d1) {
  unsigned st, bg;
  asm volatile("v_mbcnt_lo_u32_b32 v60, -1, 0\n\t"
    "v_mbcnt_hi_u32_b32 v60, -1, v60\n\t"
    "v_lshlrev_b32 v6, 4, v60\n\t"
    "s_mov_b32 %[sp], 0\n\t"
    "s_setprio 3\n\t"
    "global_load_dwordx4 v[40:43], v6, %[ag] sc1\n\t"
    "global_load_dwordx4 v[44:47], v6, %[ag] offset:1024 sc1\n\t"
    "s_memtime %[tc]\n\t"
    "v_add_u32 v7, 45056, v6\n\t"
    "v_add_u32 v8, 102400, v6\n\t"
    "ds_read_b128 v[24:27], v7 offset:0\n\t"
    "ds_read_b128 v[28:31], v7 offset:1024\n\t"
    "ds_read_b128 v[32:35], v7 offset:8192\n\t"
    "ds_read_b128 v[36:39], v7 offset:9216\n\t"
    "v_lshrrev_b32 v48, 4, v60\n\t"
    "v_and_b32 v49, 15, v60\n\t"
    "v_and_b32 v61, 4, v60\n\t"
    "v_mul_u32_u24 v61, 0x2aaa, v61\n\t"
    "v_lshrrev_b32 v9, 1, v60\n\t"
    "v_and_b32 v9, 2, v9\n\t"
    "v_or3_b32 v9, v9, v61, %[c44]\n\t"
    "v_and_b32 v61, 1, v48\n\t"
    "v_lshlrev_b32 v61, 3, v61\n\t"
    "v_lshl_or_b32 v14, v49, 4, v61\n\t"
    "v_add_u32 v14, 1024, v14\n\t"
    "v_lshlrev_b32 v61, 2, v48\n\t"
    "v_lshl_or_b32 v10, v49, 4, v61\n\t"
    "v_lshrrev_b32 v50, 2, v49\n\t"
    "v_and_b32 v51, 3, v60\n\t"
    "v_and_b32 v61, 1, v60\n\t"
    "v_lshlrev_b32 v61, 3, v61\n\t"
    "v_lshl_or_b32 v11, v50, 4, v61\n\t"
    "v_lshl_or_b32 v11, v48, 7, v11\n\t"
    "v_xor_b32 v12, v48, v49\n\t"
    "v_lshlrev_b32 v12, 4, v12\n\t"
    "v_lshl_or_b32 v12, v49, 8, v12\n\t"
    "v_lshl_or_b32 v50, v48, 2, v50\n\t"
    "v_lshrrev_b32 v51, 1, v51\n\t"
    "v_or_b32 v51, 8, v51\n\t"
    "v_xor_b32 v51, v51, v50\n\t"
    "v_lshl_or_b32 v13, v51, 4, v61\n\t"
    "v_lshl_or_b32 v13, v50, 8, v13\n\t"
    "v_and_b32 v61, 3, v60\n\t"
    "v_lshlrev_b32 v61, 4, v61\n\t"
    "v_lshl_or_b32 v15, v48, 6, v61\n\t"
    "v_and_b32 v61, 8, v60\n\t"
    "v_lshl_or_b32 v15, v61, 14, v15\n\t"
    "v_and_b32 v61, 4, v60\n\t"
    "v_lshl_or_b32 v15, v61, 8, v15\n\t"
    "v_add_u32 v15, 512, v15\n\t"
    "3:\n\t"
    "v_mov_b32 v16, 0\n\t"
    "v_mov_b32 v17, 0\n\t"
    "v_mov_b32 v18, 0\n\t"
    "v_mov_b32 v19, 0\n\t"
    "v_mov_b32 v20, 0\n\t"
    "v_mov_b32 v21, 0\n\t"
    "v_mov_b32 v22, 0\n\t"
    "v_mov_b32 v23, 0\n\t"
    "s_waitcnt vmcnt(0) lgkmcnt(0)\n\t"
    "14:\n\t"
    "ds_write_b128 v6, v[40:43] offset:4096\n\t"
    "ds_write_b128 v6, v[44:47] offset:5120\n\t"
    "v_smfmac_f32_16x16x64_bf16 v[16:19], v[40:43], v[128:135], v9\n\t"
    "v_max3_u32 v60, v40, v41, v42\n\t"
    "v_max3_u32 v61, v43, v44, v45\n\t"
    "v_smfmac_f32_16x16x64_bf16 v[20:23], v[40:43], v[136:143], v9\n\t"
    "v_max3_u32 v60, v60, v46, v47\n\t"
    "v_max_u32 v60, v60, v61\n\t"
    "v_cmp_eq_u32 vcc, -1, v60\n\t"
    "s_cbranch_vccnz 1f\n\t"
    "2:\n\t"
    "s_waitcnt lgkmcnt(0)\n\t"
    "s_barrier\n\t"
    "ds_read_b128 v[40:43], v6 offset:12288\n\t"
    "ds_read_b128 v[48:51], v6 offset:0\n\t"
    "v_smfmac_f32_16x16x64_bf16 v[16:19], v[44:47], v[144:151], v9\n\t"
    "ds_read_b128 v[52:55], v6 offset:1024\n\t"
    "ds_read_b128 v[56:59], v6 offset:2048\n\t"
    "v_smfmac_f32_16x16x64_bf16 v[20:23], v[44:47], v[152:159], v9\n\t"
    "ds_read_b128 v[60:63], v6 offset:13312\n\t"
    "ds_read_b128 v[44:47], v6 offset:3072\n\t"
    "s_waitcnt lgkmcnt(5)\n\t"
    "v_smfmac_f32_16x16x64_bf16 v[16:19], v[40:43], v[24:31], v9\n\t"
    "ds_read_b128 v[24:27], v7 offset:16384\n\t"
    "ds_read_b128 v[28:31], v7 offset:17408\n\t"
    "v_smfmac_f32_16x16x64_bf16 v[20:23], v[40:43], v[32:39], v9\n\t"
    "ds_read_b128 v[32:35], v7 offset:24576\n\t"
    "ds_read_b128 v[36:39], v7 offset:25600\n\t"
    "ds_read_b128 v[40:43], v6 offset:6144\n\t"
    "s_waitcnt lgkmcnt(9)\n\t"
    "v_smfmac_f32_16x16x64_bf16 v[16:19], v[48:51], v[64:71], v9\n\t"
    "v_smfmac_f32_16x16x64_bf16 v[20:23], v[48:51], v[72:79], v9\n\t"
    "ds_read_b128 v[48:51], v6 offset:7168\n\t"
    "s_waitcnt lgkmcnt(9)\n\t"
    "v_smfmac_f32_16x16x64_bf16 v[16:19], v[52:55], v[80:87], v9\n\t"
    "v_smfmac_f32_16x16x64_bf16 v[20:23], v[52:55], v[88:95], v9\n\t"
    "ds_read_b128 v[52:55], v6 offset:14336\n\t"
    "s_waitcnt lgkmcnt(9)\n\t"
    "v_smfmac_f32_16x16x64_bf16 v[16:19], v[56:59], v[96:103], v9\n\t"
    "v_smfmac_f32_16x16x64_bf16 v[20:23], v[56:59], v[104:111], v9\n\t"
    "ds_read_b128 v[56:59], v6 offset:8192\n\t"
    "s_waitcnt lgkmcnt(6)\n\t"
    "v_smfmac_f32_16x16x64_bf16 v[16:19], v[60:63], v[24:31], v9\n\t"
    "ds_read_b128 v[24:27], v8 offset:0\n\t"
    "ds_read_b128 v[28:31], v8 offset:1024\n\t"
    "s_waitcnt lgkmcnt(6)\n\t"
    "v_smfmac_f32_16x16x64_bf16 v[20:23], v[60:63], v[32:39], v9\n\t"
    "ds_read_b128 v[32:35], v8 offset:8192\n\t"
    "ds_read_b128 v[36:39], v8 offset:9216\n\t"
    "ds_read_b128 v[60:63], v6 offset:9216\n\t"
    "v_smfmac_f32_16x16x64_bf16 v[16:19], v[44:47], v[112:119], v9\n\t"
    "v_smfmac_f32_16x16x64_bf16 v[20:23], v[44:47], v[120:127], v9\n\t"
    "ds_read_b128 v[44:47], v6 offset:10240\n\t"
    "s_waitcnt lgkmcnt(9)\n\t"
    "v_smfmac_f32_16x16x64_bf16 v[16:19], v[40:43], v[160:167], v9\n\t"
    "v_smfmac_f32_16x16x64_bf16 v[20:23], v[40:43], v[168:175], v9\n\t"
    "ds_read_b128 v[40:43], v6 offset:15360\n\t"
    "s_waitcnt lgkmcnt(9)\n\t"
    "v_smfmac_f32_16x16x64_bf16 v[16:19], v[48:51], v[176:183], v9\n\t"
    "v_smfmac_f32_16x16x64_bf16 v[20:23], v[48:51], v[184:191], v9\n\t"
    "ds_read_b128 v[48:51], v6 offset:11264\n\t"
    "s_waitcnt lgkmcnt(6)\n\t"
    "v_smfmac_f32_16x16x64_bf16 v[16:19], v[52:55], v[24:31], v9\n\t"
    "ds_read_b128 v[24:27], v8 offset:16384\n\t"
    "ds_read_b128 v[28:31], v8 offset:17408\n\t"
    "s_waitcnt lgkmcnt(6)\n\t"
    "v_smfmac_f32_16x16x64_bf16 v[20:23], v[52:55], v[32:39], v9\n\t"
    "ds_read_b128 v[32:35], v8 offset:24576\n\t"
    "ds_read_b128 v[36:39], v8 offset:25600\n\t"
    "v_smfmac_f32_16x16x64_bf16 v[16:19], v[56:59], v[192:199], v9\n\t"
    "v_smfmac_f32_16x16x64_bf16 v[20:23], v[56:59], v[200:207], v9\n\t"
    "s_waitcnt lgkmcnt(7)\n\t"
    "v_smfmac_f32_16x16x64_bf16 v[16:19], v[60:63], v[208:215], v9\n\t"
    "v_smfmac_f32_16x16x64_bf16 v[20:23], v[60:63], v[216:223], v9\n\t"
    "s_waitcnt lgkmcnt(6)\n\t"
    "v_smfmac_f32_16x16x64_bf16 v[16:19], v[44:47], v[224:231], v9\n\t"
    "v_smfmac_f32_16x16x64_bf16 v[20:23], v[44:47], v[232:239], v9\n\t"
    "s_waitcnt lgkmcnt(2)\n\t"
    "v_smfmac_f32_16x16x64_bf16 v[16:19], v[40:43], v[24:31], v9\n\t"
    "s_waitcnt lgkmcnt(0)\n\t"
    "v_smfmac_f32_16x16x64_bf16 v[20:23], v[40:43], v[32:39], v9\n\t"
    "v_smfmac_f32_16x16x64_bf16 v[16:19], v[48:51], v[240:247], v9\n\t"
    "v_smfmac_f32_16x16x64_bf16 v[20:23], v[48:51], v[248:255], v9\n\t"
    "v_xor_b32 v7, 64, v12\n\t"
    "v_xor_b32 v8, 0x80, v12\n\t"
    "v_xor_b32 v6, 0xc0, v12\n\t"
    "ds_read_b128 v[24:27], v12 offset:16384\n\t"
    "ds_read_b128 v[28:31], v12 offset:20480\n\t"
    "ds_read_b128 v[32:35], v7 offset:16384\n\t"
    "ds_read_b128 v[36:39], v7 offset:20480\n\t"
    "v_permlane16_swap_b32 v16, v18\n\t"
    "v_permlane16_swap_b32 v17, v19\n\t"
    "v_permlane16_swap_b32 v20, v22\n\t"
    "v_permlane16_swap_b32 v21, v23\n\t"
    "v_add_f32 v16, v16, v18\n\t"
    "v_add_f32 v17, v17, v19\n\t"
    "v_add_f32 v20, v20, v22\n\t"
    "v_mul_f32 v16, %[lsc], v16\n\t"
    "v_mul_f32 v17, %[lsc], v17\n\t"
    "v_cvt_pk_bf16_f32 v16, v16, v17\n\t"
    "v_add_f32 v21, v21, v23\n\t"
    "ds_write_b32 v10, v16 offset:33792\n\t"
    "v_mul_f32 v20, %[lsc], v20\n\t"
    "v_mul_f32 v21, %[lsc], v21\n\t"
    "v_cvt_pk_bf16_f32 v20, v20, v21\n\t"
    "ds_write_b32 v10, v20 offset:34048\n\t"
    "ds_read_b128 v[40:43], v8 offset:16384\n\t"
    "ds_read_b128 v[44:47], v8 offset:20480\n\t"
    "ds_read_b128 v[48:51], v6 offset:16384\n\t"
    "ds_read_b128 v[52:55], v6 offset:20480\n\t"
    "s_waitcnt lgkmcnt(4)\n\t"
    "s_barrier\n\t"
    "ds_read_b64_tr_b16 v[16:17], v11 offset:32768\n\t"
    "ds_read_b64_tr_b16 v[18:19], v11 offset:32832\n\t"
    "ds_read_b64_tr_b16 v[20:21], v11 offset:33280\n\t"
    "ds_read_b64_tr_b16 v[22:23], v11 offset:33344\n\t"
    "ds_read_b64_tr_b16 v[56:57], v11 offset:33792\n\t"
    "ds_read_b64_tr_b16 v[58:59], v11 offset:33856\n\t"
    "ds_read_b64_tr_b16 v[60:61], v11 offset:34304\n\t"
    "ds_read_b64_tr_b16 v[62:63], v11 offset:34368\n\t"
    "v_mov_b32 v6, 0\n\t"
    "v_mov_b32 v7, 0\n\t"
    "v_mov_b32 v8, 0\n\t"
    "v_mov_b32 v9, 0\n\t"
    "s_mov_b32 exec_hi, 0\n\t"
    "global_load_dwordx2 v[6:7], v14, %[vs] sc1\n\t"
    "global_load_dwordx2 v[8:9], v14, %[vs] offset:256 sc1\n\t"
    "s_mov_b32 exec_hi, -1\n\t"
    "s_waitcnt lgkmcnt(6)\n\t"
    "v_mfma_f32_16x16x32_bf16 v[24:27], v[24:27], v[16:19], 0\n\t"
    "v_mfma_f32_16x16x32_bf16 v[28:31], v[28:31], v[16:19], 0\n\t"
    "s_waitcnt lgkmcnt(4)\n\t"
    "v_mfma_f32_16x16x32_bf16 v[24:27], v[32:35], v[20:23], v[24:27]\n\t"
    "v_mfma_f32_16x16x32_bf16 v[28:31], v[36:39], v[20:23], v[28:31]\n\t"
    "v_xor_b32 v12, 32, v13\n\t"
    "ds_read_b64_tr_b16 v[32:33], v13 offset:24576\n\t"
    "ds_read_b64_tr_b16 v[34:35], v13 offset:28672\n\t"
    "ds_read_b64_tr_b16 v[36:37], v12 offset:24576\n\t"
    "ds_read_b64_tr_b16 v[38:39], v12 offset:28672\n\t"
    "s_waitcnt lgkmcnt(6)\n\t"
    "v_mfma_f32_16x16x32_bf16 v[24:27], v[40:43], v[56:59], v[24:27]\n\t"
    "v_mfma_f32_16x16x32_bf16 v[28:31], v[44:47], v[56:59], v[28:31]\n\t"
    "s_waitcnt lgkmcnt(4)\n\t"
    "v_mfma_f32_16x16x32_bf16 v[24:27], v[48:51], v[60:63], v[24:27]\n\t"
    "v_mfma_f32_16x16x32_bf16 v[28:31], v[52:55], v[60:63], v[28:31]\n\t"
    "s_setprio 1\n\t"
    "v_mov_b32 v52, %[one]\n\t"
    "v_mov_b32 v53, %[one]\n\t"
    "v_mov_b32 v54, %[one]\n\t"
    "v_mov_b32 v55, %[one]\n\t"
    "s_nop 2\n\t"
    "v_max3_f32 v10, |v24|, |v25|, |v26|\n\t"
    "v_max3_f32 v40, |v27|, |v28|, |v29|\n\t"
    "v_max3_f32 v10, v10, |v30|, |v31|\n\t"
    "v_max_f32 v10, v10, v40\n\t"
    "v_cmp_nge_f32 vcc, %[big], v10\n\t"
    "s_mov_b32 %[bg], %[big]\n\t"
    "v_mov_b32 v10, 0\n\t"
    "s_cbranch_vccnz 12f\n\t"
    "13:\n\t"
    "v_exp_f32 v24, v24\n\t"
    "v_exp_f32 v25, v25\n\t"
    "v_exp_f32 v26, v26\n\t"
    "v_exp_f32 v27, v27\n\t"
    "v_exp_f32 v28, v28\n\t"
    "v_exp_f32 v29, v29\n\t"
    "v_exp_f32 v30, v30\n\t"
    "v_exp_f32 v31, v31\n\t"
    "v_cvt_pk_bf16_f32 v40, v24, v25\n\t"
    "v_cvt_pk_bf16_f32 v41, v26, v27\n\t"
    "v_cvt_pk_bf16_f32 v42, v28, v29\n\t"
    "v_cvt_pk_bf16_f32 v43, v30, v31\n\t"
    "s_setprio 3\n\t"
    "s_waitcnt lgkmcnt(0)\n\t"
    "s_nop 0\n\t"
    "v_mfma_f32_16x16x32_bf16 v[44:47], v[32:35], v[40:43], 0\n\t"
    "v_mfma_f32_16x16x32_bf16 v[48:51], v[36:39], v[40:43], 0\n\t"
    "v_mfma_f32_16x16x32_bf16 v[52:55], v[52:55], v[40:43], 0\n\t"
    "v_mov_b32 v12, 0\n\t"
    "v_mov_b32 v13, 0\n\t"
    "s_mov_b32 exec_hi, 0\n\t"
    "v_mov_b32 v12, %[one]\n\t"
    "v_mov_b32 v13, %[one]\n\t"
    "s_mov_b32 exec_hi, -1\n\t"
    "s_barrier\n\t"
    "ds_read_b64_tr_b16 v[24:25], v11 offset:34816\n\t"
    "ds_read_b64_tr_b16 v[26:27], v11 offset:34880\n\t"
    "ds_read_b64_tr_b16 v[28:29], v11 offset:35328\n\t"
    "ds_read_b64_tr_b16 v[30:31], v11 offset:35392\n\t"
    "ds_read_b64_tr_b16 v[32:33], v11 offset:35840\n\t"
    "ds_read_b64_tr_b16 v[34:35], v11 offset:35904\n\t"
    "ds_read_b64_tr_b16 v[36:37], v11 offset:36352\n\t"
    "ds_read_b64_tr_b16 v[38:39], v11 offset:36416\n\t"
    "s_waitcnt lgkmcnt(6)\n\t"
    "v_mfma_f32_16x16x32_bf16 v[40:43], v[24:27], v[16:19], 0\n\t"
    "s_waitcnt lgkmcnt(4)\n\t"
    "v_mfma_f32_16x16x32_bf16 v[40:43], v[28:31], v[20:23], v[40:43]\n\t"
    "s_waitcnt lgkmcnt(2)\n\t"
    "v_mfma_f32_16x16x32_bf16 v[40:43], v[32:35], v[56:59], v[40:43]\n\t"
    "s_waitcnt lgkmcnt(0)\n\t"
    "v_mfma_f32_16x16x32_bf16 v[40:43], v[36:39], v[60:63], v[40:43]\n\t"
    "5:\n\t"
    "s_waitcnt vmcnt(0)\n\t"
    "v_max3_u32 v11, v6, v7, v8\n\t"
    "v_max_u32 v11, v11, v9\n\t"
    "v_cmp_eq_u32 vcc, -1, v11\n\t"
    "s_cbranch_vccnz 4f\n\t"
    "6:\n\t"
    "s_nop 2\n\t"
    "v_exp_f32 v28, v40\n\t"
    "v_max3_f32 v11, v40, v41, v42\n\t"
    "v_exp_f32 v29, v41\n\t"
    "v_max_f32 v11, v11, v43\n\t"
    "v_exp_f32 v30, v42\n\t"
    "v_cmp_nge_f32 vcc, %[bg], v11\n\t"
    "v_exp_f32 v31, v43\n\t"
    "s_cbranch_vccnz 10f\n\t"
    "11:\n\t"
    "v_cvt_pk_bf16_f32 v24, v28, v29\n\t"
    "v_cvt_pk_bf16_f32 v25, v30, v31\n\t"
    "s_nop 1\n\t"
    "v_mfma_f32_16x16x16_bf16 v[52:55], v[12:13], v[24:25], v[52:55]\n\t"
    "v_mfma_f32_16x16x16_bf16 v[44:47], v[6:7], v[24:25], v[44:47]\n\t"
    "v_mfma_f32_16x16x16_bf16 v[48:51], v[8:9], v[24:25], v[48:51]\n\t"
    "s_nop 5\n\t"
    "v_rcp_f32 v26, v52\n\t"
    "s_nop 0\n\t"
    "v_pk_mul_f32 v[44:45], v[44:45], v[26:27] op_sel_hi:[1,0]\n\t"
    "v_pk_mul_f32 v[46:47], v[46:47], v[26:27] op_sel_hi:[1,0]\n\t"
    "v_pk_mul_f32 v[48:49], v[48:49], v[26:27] op_sel_hi:[1,0]\n\t"
    "v_pk_mul_f32 v[50:51], v[50:51], v[26:27] op_sel_hi:[1,0]\n\t"
    "v_cvt_pk_bf16_f32 v36, v44, v45\n\t"
    "v_cvt_pk_bf16_f32 v37, v46, v47\n\t"
    "v_cvt_pk_bf16_f32 v38, v48, v49\n\t"
    "v_cvt_pk_bf16_f32 v39, v50, v51\n\t"
    "v_min_u32 v36, -2, v36\n\t"
    "v_min_u32 v37, -2, v37\n\t"
    "v_min_u32 v38, -2, v38\n\t"
    "v_min_u32 v39, -2, v39\n\t"
    "global_store_dwordx4 v15, v[36:39], %[od] sc1\n\t"
    "s_barrier\n\t"
    "v_permlane16_swap_b32 v6, v8\n\t"
    "v_permlane16_swap_b32 v7, v9\n\t"
    "v_and_b32 v11, 8, v14\n\t"
    "v_mul_u32_u24 v11, 31, v11\n\t"
    "v_add_u32 v14, v14, v11\n\t"
    "s_mov_b32 exec_hi, 0\n\t"
    "global_store_dwordx4 v14, v[6:9], %[kv] sc1\n\t"
    "s_mov_b32 exec_hi, -1\n\t"
    "s_nop 0\n\t"
    "s_waitcnt lgkmcnt(0)\n\t"
    "s_branch 9f\n\t"
    "1:\n\t"
    "s_add_u32 %[sp], %[sp], 1\n\t"
    "s_cmp_ge_u32 %[sp], %[cap]\n\t"
    "s_cbranch_scc1 7f\n\t"
    "global_load_dwordx4 v[40:43], v6, %[ag] sc1\n\t"
    "global_load_dwordx4 v[44:47], v6, %[ag] offset:1024 sc1\n\t"
    "s_waitcnt lgkmcnt(0)\n\t"
    "s_memtime %[tc]\n\t"
    "s_nop 7\n\t"
    "s_branch 3b\n\t"
    "7:\n\t"
    "s_mov_b32 %[cap], 0\n\t"
    "s_branch 2b\n\t"
    "4:\n\t"
    "s_add_u32 %[sp], %[sp], 0x10000\n\t"
    "s_lshr_b32 %[st], %[sp], 16\n\t"
    "s_cmp_ge_u32 %[st], %[cap]\n\t"
    "s_cbranch_scc1 8f\n\t"
    "s_mov_b32 exec_hi, 0\n\t"
    "global_load_dwordx2 v[6:7], v14, %[vs] sc1\n\t"
    "global_load_dwordx2 v[8:9], v14, %[vs] offset:256 sc1\n\t"
    "s_mov_b32 exec_hi, -1\n\t"
    "s_branch 5b\n\t"
    "8:\n\t"
    "s_mov_b32 %[cap], 0\n\t"
    "s_branch 6b\n\t"
    "10:\n\t"
    "v_max_f32 v11, v11, v10\n\t"
    "v_mov_b32 v26, v11\n\t"
    "s_nop 1\n\t"
    "v_permlane16_swap_b32 v11, v26\n\t"
    "v_max_f32 v11, v11, v26\n\t"
    "v_sub_f32 v28, v40, v11\n\t"
    "v_sub_f32 v29, v41, v11\n\t"
    "v_sub_f32 v30, v42, v11\n\t"
    "v_sub_f32 v31, v43, v11\n\t"
    "v_sub_f32 v11, v10, v11\n\t"
    "v_exp_f32 v11, v11\n\t"
    "v_exp_f32 v28, v28\n\t"
    "v_exp_f32 v29, v29\n\t"
    "v_exp_f32 v30, v30\n\t"
    "v_exp_f32 v31, v31\n\t"
    "v_mul_f32 v44, v44, v11\n\t"
    "v_mul_f32 v45, v45, v11\n\t"
    "v_mul_f32 v46, v46, v11\n\t"
    "v_mul_f32 v47, v47, v11\n\t"
    "v_mul_f32 v48, v48, v11\n\t"
    "v_mul_f32 v49, v49, v11\n\t"
    "v_mul_f32 v50, v50, v11\n\t"
    "v_mul_f32 v51, v51, v11\n\t"
    "v_mul_f32 v52, v52, v11\n\t"
    "v_mul_f32 v53, v53, v11\n\t"
    "v_mul_f32 v54, v54, v11\n\t"
    "v_mul_f32 v55, v55, v11\n\t"
    "s_branch 11b\n\t"
    "12:\n\t"
    "v_max3_f32 v10, v24, v25, v26\n\t"
    "v_max3_f32 v40, v27, v28, v29\n\t"
    "v_max3_f32 v10, v10, v30, v31\n\t"
    "v_max3_f32 v10, v10, v40, %[neg]\n\t"
    "v_mov_b32 v40, v10\n\t"
    "s_nop 1\n\t"
    "v_permlane16_swap_b32 v10, v40\n\t"
    "v_max_f32 v10, v10, v40\n\t"
    "v_mov_b32 v40, v10\n\t"
    "s_nop 1\n\t"
    "v_permlane32_swap_b32 v10, v40\n\t"
    "v_max_f32 v10, v10, v40\n\t"
    "v_sub_f32 v24, v24, v10\n\t"
    "v_sub_f32 v25, v25, v10\n\t"
    "v_sub_f32 v26, v26, v10\n\t"
    "v_sub_f32 v27, v27, v10\n\t"
    "v_sub_f32 v28, v28, v10\n\t"
    "v_sub_f32 v29, v29, v10\n\t"
    "v_sub_f32 v30, v30, v10\n\t"
    "v_sub_f32 v31, v31, v10\n\t"
    "s_mov_b32 %[bg], 0x7fc00000\n\t"
    "s_branch 13b\n\t"
    "9:"
    : [tc] "=&s"(tc), [sp] "=&s"(sp), [cap] "+s"(cap), [st] "=&s"(st), [bg] "=&s"(bg)
    : [ag] "s"(ag), [c44] "s"(0x4444u), [kv] "s"(kv), [vs] "s"(vs), [od] "s"(od), [od2] "s"(od + 262144), [neg] "s"(-1.0e38f), [lsc] "s"(LSC), [one] "s"(0x3F803F80u), [big] "s"(24.0f), "{v[64:95]}"(W.w0), "{v[96:127]}"(W.w1), "{v[128:159]}"(W.w2), "{v[160:191]}"(W.w3), "{v[192:223]}"(W.w4), "{v[224:255]}"(W.w5)
    : "v6", "v7", "v8", "v9", "v10", "v11", "v12", "v13", "v14", "v15", "v16", "v17", "v18", "v19", "v20", "v21", "v22", "v23", "v24", "v25", "v26", "v27", "v28", "v29", "v30", "v31", "v32", "v33", "v34", "v35", "v36", "v37", "v38", "v39", "v40", "v41", "v42", "v43", "v44", "v45", "v46", "v47", "v48", "v49", "v50", "v51", "v52", "v53", "v54", "v55", "v56", "v57", "v58", "v59", "v60", "v61", "v62", "v63", "vcc", "scc", "memory");
}
template <> __device__ __forceinline__ void qk_step<3>(const QKW& W, const char* ag, const char* vs, const char* od, const char* kv, unsigned& cap, unsigned& sp,
                                                        unsigned long long& tc, unsigned long long* ts, unsigned& d0, unsigned& d1) {
  unsigned st, bg;
  asm volatile("v_mbcnt_lo_u32_b32 v60, -1, 0\n\t"
    "v_mbcnt_hi_u32_b32 v60, -1, v60\n\t"
    "v_lshlrev_b32 v6, 4, v60\n\t"
    "s_mov_b32 %[sp], 0\n\t"
    "s_setprio 3\n\t"
    "global_load_dwordx4 v[40:43], v6, %[ag] sc1\n\t"
    "global_load_dwordx4 v[44:47], v6, %[ag] offset:1024 sc1\n\t"
    "s_memtime %[tc]\n\t"
    "v_add_u32 v7, 47104, v6\n\t"
    "v_add_u32 v8, 104448, v6\n\t"
    "ds_read_b128 v[24:27], v7 offset:0\n\t"
    "ds_read_b128 v[28:31], v7 offset:1024\n\t"
    "ds_read_b128 v[32:35], v7 offset:8192\n\t"
    "ds_read_b128 v[36:39], v7 offset:9216\n\t"
    "v_lshrrev_b32 v48, 4, v60\n\t"
    "v_and_b32 v49, 15, v60\n\t"
    "v_and_b32 v61, 4, v60\n\t"
    "v_mul_u32_u24 v61, 0x2aaa, v61\n\t"
    "v_lshrrev_b32 v9, 1, v60\n\t"
    "v_and_b32 v9, 2, v9\n\t"
    "v_or3_b32 v9, v9, v61, %[c44]\n\t"
    "v_and_b32 v61, 1, v48\n\t"
    "v_lshlrev_b32 v61, 3, v61\n\t"
    "v_lshl_or_b32 v14, v49, 4, v61\n\t"
    "v_add_u32 v14, 1536, v14\n\t"
    "v_lshlrev_b32 v61, 2, v48\n\t"
    "v_lshl_or_b32 v10, v49, 4, v61\n\t"
    "v_lshrrev_b32 v50, 2, v49\n\t"
    "v_and_b32 v51, 3, v60\n\t"
    "v_and_b32 v61, 1, v60\n\t"
    "v_lshlrev_b32 v61, 3, v61\n\t"
    "v_lshl_or_b32 v11, v50, 4, v61\n\t"
    "v_lshl_or_b32 v11, v48, 7, v11\n\t"
    "v_xor_b32 v12, v48, v49\n\t"
    "v_lshlrev_b32 v12, 4, v12\n\t"
    "v_lshl_or_b32 v12, v49, 8, v12\n\t"
    "v_lshl_or_b32 v50, v48, 2, v50\n\t"
    "v_lshrrev_b32 v51, 1, v51\n\t"
    "v_or_b32 v51, 12, v51\n\t"
    "v_xor_b32 v51, v51, v50\n\t"
    "v_lshl_or_b32 v13, v51, 4, v61\n\t"
    "v_lshl_or_b32 v13, v50, 8, v13\n\t"
    "v_and_b32 v61, 3, v60\n\t"
    "v_lshlrev_b32 v61, 4, v61\n\t"
    "v_lshl_or_b32 v15, v48, 6, v61\n\t"
    "v_and_b32 v61, 8, v60\n\t"
    "v_lshl_or_b32 v15, v61, 14, v15\n\t"
    "v_and_b32 v61, 4, v60\n\t"
    "v_lshl_or_b32 v15, v61, 8, v15\n\t"
    "v_add_u32 v15, 768, v15\n\t"
    "3:\n\t"
    "v_mov_b32 v16, 0\n\t"
    "v_mov_b32 v17, 0\n\t"
    "v_mov_b32 v18, 0\n\t"
    "v_mov_b32 v19, 0\n\t"
    "v_mov_b32 v20, 0\n\t"
    "v_mov_b32 v21, 0\n\t"
    "v_mov_b32 v22, 0\n\t"
    "v_mov_b32 v23, 0\n\t"
    "s_waitcnt vmcnt(0) lgkmcnt(0)\n\t"
    "14:\n\t"
    "ds_write_b128 v6, v[40:43] offset:6144\n\t"
    "ds_write_b128 v6, v[44:47] offset:7168\n\t"
    "v_smfmac_f32_16x16x64_bf16 v[16:19], v[40:43], v[160:167], v9\n\t"
    "v_max3_u32 v60, v40, v41, v42\n\t"
    "v_max3_u32 v61, v43, v44, v45\n\t"
    "v_smfmac_f32_16x16x64_bf16 v[20:23], v[40:43], v[168:175], v9\n\t"
    "v_max3_u32 v60, v60, v46, v47\n\t"
    "v_max_u32 v60, v60, v61\n\t"
    "v_cmp_eq_u32 vcc, -1, v60\n\t"
    "s_cbranch_vccnz 1f\n\t"
    "2:\n\t"
    "s_waitcnt lgkmcnt(0)\n\t"
    "s_barrier\n\t"
    "ds_read_b128 v[40:43], v6 offset:12288\n\t"
    "ds_read_b128 v[48:51], v6 offset:0\n\t"
    "v_smfmac_f32_16x16x64_bf16 v[16:19], v[44:47], v[176:183], v9\n\t"
    "ds_read_b128 v[52:55], v6 offset:1024\n\t"
    "ds_read_b128 v[56:59], v6 offset:2048\n\t"
    "v_smfmac_f32_16x16x64_bf16 v[20:23], v[44:47], v[184:191], v9\n\t"
    "ds_read_b128 v[60:63], v6 offset:13312\n\t"
    "ds_read_b128 v[44:47], v6 offset:3072\n\t"
    "s_waitcnt lgkmcnt(5)\n\t"
    "v_smfmac_f32_16x16x64_bf16 v[16:19], v[40:43], v[24:31], v9\n\t"
    "ds_read_b128 v[24:27], v7 offset:16384\n\t"
    "ds_read_b128 v[28:31], v7 offset:17408\n\t"
    "v_smfmac_f32_16x16x64_bf16 v[20:23], v[40:43], v[32:39], v9\n\t"
    "ds_read_b128 v[32:35], v7 offset:24576\n\t"
    "ds_read_b128 v[36:39], v7 offset:25600\n\t"
    "ds_read_b128 v[40:43], v6 offset:4096\n\t"
    "s_waitcnt lgkmcnt(9)\n\t"
    "v_smfmac_f32_16x16x64_bf16 v[16:19], v[48:51], v[64:71], v9\n\t"
    "v_smfmac_f32_16x16x64_bf16 v[20:23], v[48:51], v[72:79], v9\n\t"
    "ds_read_b128 v[48:51], v6 offset:5120\n\t"
    "s_waitcnt lgkmcnt(9)\n\t"
    "v_smfmac_f32_16x16x64_bf16 v[16:19], v[52:55], v[80:87], v9\n\t"
    "v_smfmac_f32_16x16x64_bf16 v[20:23], v[52:55], v[88:95], v9\n\t"
    "ds_read_b128 v[52:55], v6 offset:14336\n\t"
    "s_waitcnt lgkmcnt(9)\n\t"
    "v_smfmac_f32_16x16x64_bf16 v[16:19], v[56:59], v[96:103], v9\n\t"
    "v_smfmac_f32_16x16x64_bf16 v[20:23], v[56:59], v[104:111], v9\n\t"
    "ds_read_b128 v[56:59], v6 offset:8192\n\t"
    "s_waitcnt lgkmcnt(6)\n\t"
    "v_smfmac_f32_16x16x64_bf16 v[16:19], v[60:63], v[24:31], v9\n\t"
    "ds_read_b128 v[24:27], v8 offset:0\n\t"
    "ds_read_b128 v[28:31], v8 offset:1024\n\t"
    "s_waitcnt lgkmcnt(6)\n\t"
    "v_smfmac_f32_16x16x64_bf16 v[20:23], v[60:63], v[32:39], v9\n\t"
    "ds_read_b128 v[32:35], v8 offset:8192\n\t"
    "ds_read_b128 v[36:39], v8 offset:9216\n\t"
    "ds_read_b128 v[60:63], v6 offset:9216\n\t"
    "v_smfmac_f32_16x16x64_bf16 v[16:19], v[44:47], v[112:119], v9\n\t"
    "v_smfmac_f32_16x16x64_bf16 v[20:23], v[44:47], v[120:127], v9\n\t"
    "ds_read_b128 v[44:47], v6 offset:10240\n\t"
    "s_waitcnt lgkmcnt(9)\n\t"
    "v_smfmac_f32_16x16x64_bf16 v[16:19], v[40:43], v[128:135], v9\n\t"
    "v_smfmac_f32_16x16x64_bf16 v[20:23], v[40:43], v[136:143], v9\n\t"
    "ds_read_b128 v[40:43], v6 offset:15360\n\t"
    "s_waitcnt lgkmcnt(9)\n\t"
    "v_smfmac_f32_16x16x64_bf16 v[16:19], v[48:51], v[144:151], v9\n\t"
    "v_smfmac_f32_16x16x64_bf16 v[20:23], v[48:51], v[152:159], v9\n\t"
    "ds_read_b128 v[48:51], v6 offset:11264\n\t"
    "s_waitcnt lgkmcnt(6)\n\t"
    "v_smfmac_f32_16x16x64_bf16 v[16:19], v[52:55], v[24:31], v9\n\t"
    "ds_read_b128 v[24:27], v8 offset:16384\n\t"
    "ds_read_b128 v[28:31], v8 offset:17408\n\t"
    "s_waitcnt lgkmcnt(6)\n\t"
    "v_smfmac_f32_16x16x64_bf16 v[20:23], v[52:55], v[32:39], v9\n\t"
    "ds_read_b128 v[32:35], v8 offset:24576\n\t"
    "ds_read_b128 v[36:39], v8 offset:25600\n\t"
    "v_smfmac_f32_16x16x64_bf16 v[16:19], v[56:59], v[192:199], v9\n\t"
    "v_smfmac_f32_16x16x64_bf16 v[20:23], v[56:59], v[200:207], v9\n\t"
    "s_waitcnt lgkmcnt(7)\n\t"
    "v_smfmac_f32_16x16x64_bf16 v[16:19], v[60:63], v[208:215], v9\n\t"
    "v_smfmac_f32_16x16x64_bf16 v[20:23], v[60:63], v[216:223], v9\n\t"
    "s_waitcnt lgkmcnt(6)\n\t"
    "v_smfmac_f32_16x16x64_bf16 v[16:19], v[44:47], v[224:231], v9\n\t"
    "v_smfmac_f32_16x16x64_bf16 v[20:23], v[44:47], v[232:239], v9\n\t"
    "s_waitcnt lgkmcnt(2)\n\t"
    "v_smfmac_f32_16x16x64_bf16 v[16:19], v[40:43], v[24:31], v9\n\t"
    "s_waitcnt lgkmcnt(0)\n\t"
    "v_smfmac_f32_16x16x64_bf16 v[20:23], v[40:43], v[32:39], v9\n\t"
    "v_smfmac_f32_16x16x64_bf16 v[16:19], v[48:51], v[240:247], v9\n\t"
    "v_smfmac_f32_16x16x64_bf16 v[20:23], v[48:51], v[248:255], v9\n\t"
    "v_xor_b32 v7, 64, v12\n\t"
    "v_xor_b32 v8, 0x80, v12\n\t"
    "v_xor_b32 v6, 0xc0, v12\n\t"
    "ds_read_b128 v[24:27], v12 offset:16384\n\t"
    "ds_read_b128 v[28:31], v12 offset:20480\n\t"
    "ds_read_b128 v[32:35], v7 offset:16384\n\t"
    "ds_read_b128 v[36:39], v7 offset:20480\n\t"
    "v_permlane16_swap_b32 v16, v18\n\t"
    "v_permlane16_swap_b32 v17, v19\n\t"
    "v_permlane16_swap_b32 v20, v22\n\t"
    "v_permlane16_swap_b32 v21, v23\n\t"
    "v_add_f32 v16, v16, v18\n\t"
    "v_add_f32 v17, v17, v19\n\t"
    "v_add_f32 v20, v20, v22\n\t"
    "v_mul_f32 v16, %[lsc], v16\n\t"
    "v_mul_f32 v17, %[lsc], v17\n\t"
    "v_cvt_pk_bf16_f32 v16, v16, v17\n\t"
    "v_add_f32 v21, v21, v23\n\t"
    "ds_write_b32 v10, v16 offset:34304\n\t"
    "v_mul_f32 v20, %[lsc], v20\n\t"
    "v_mul_f32 v21, %[lsc], v21\n\t"
    "v_cvt_pk_bf16_f32 v20, v20, v21\n\t"
    "ds_write_b32 v10, v20 offset:34560\n\t"
    "ds_read_b128 v[40:43], v8 offset:16384\n\t"
    "ds_read_b128 v[44:47], v8 offset:20480\n\t"
    "ds_read_b128 v[48:51], v6 offset:16384\n\t"
    "ds_read_b128 v[52:55], v6 offset:20480\n\t"
    "s_waitcnt lgkmcnt(4)\n\t"
    "s_barrier\n\t"
    "ds_read_b64_tr_b16 v[16:17], v11 offset:32768\n\t"
    "ds_read_b64_tr_b16 v[18:19], v11 offset:32832\n\t"
    "ds_read_b64_tr_b16 v[20:21], v11 offset:33280\n\t"
    "ds_read_b64_tr_b16 v[22:23], v11 offset:33344\n\t"
    "ds_read_b64_tr_b16 v[56:57], v11 offset:33792\n\t"
    "ds_read_b64_tr_b16 v[58:59], v11 offset:33856\n\t"
    "ds_read_b64_tr_b16 v[60:61], v11 offset:34304\n\t"
    "ds_read_b64_tr_b16 v[62:63], v11 offset:34368\n\t"
    "v_mov_b32 v6, 0\n\t"
    "v_mov_b32 v7, 0\n\t"
    "v_mov_b32 v8, 0\n\t"
    "v_mov_b32 v9, 0\n\t"
    "s_mov_b32 exec_hi, 0\n\t"
    "global_load_dwordx2 v[6:7], v14, %[vs] sc1\n\t"
    "global_load_dwordx2 v[8:9], v14, %[vs] offset:256 sc1\n\t"
    "s_mov_b32 exec_hi, -1\n\t"
    "s_waitcnt lgkmcnt(6)\n\t"
    "v_mfma_f32_16x16x32_bf16 v[24:27], v[24:27], v[16:19], 0\n\t"
    "v_mfma_f32_16x16x32_bf16 v[28:31], v[28:31], v[16:19], 0\n\t"
    "s_waitcnt lgkmcnt(4)\n\t"
    "v_mfma_f32_16x16x32_bf16 v[24:27], v[32:35], v[20:23], v[24:27]\n\t"
    "v_mfma_f32_16x16x32_bf16 v[28:31], v[36:39], v[20:23], v[28:31]\n\t"
    "v_xor_b32 v12, 32, v13\n\t"
    "ds_read_b64_tr_b16 v[32:33], v13 offset:24576\n\t"
    "ds_read_b64_tr_b16 v[34:35], v13 offset:28672\n\t"
    "ds_read_b64_tr_b16 v[36:37], v12 offset:24576\n\t"
    "ds_read_b64_tr_b16 v[38:39], v12 offset:28672\n\t"
    "s_waitcnt lgkmcnt(6)\n\t"
    "v_mfma_f32_16x16x32_bf16 v[24:27], v[40:43], v[56:59], v[24:27]\n\t"
    "v_mfma_f32_16x16x32_bf16 v[28:31], v[44:47], v[56:59], v[28:31]\n\t"
    "s_waitcnt lgkmcnt(4)\n\t"
    "v_mfma_f32_16x16x32_bf16 v[24:27], v[48:51], v[60:63], v[24:27]\n\t"
    "v_mfma_f32_16x16x32_bf16 v[28:31], v[52:55], v[60:63], v[28:31]\n\t"
    "s_setprio 1\n\t"
    "v_mov_b32 v52, %[one]\n\t"
    "v_mov_b32 v53, %[one]\n\t"
    "v_mov_b32 v54, %[one]\n\t"
    "v_mov_b32 v55, %[one]\n\t"
    "s_nop 2\n\t"
    "v_max3_f32 v10, |v24|, |v25|, |v26|\n\t"
    "v_max3_f32 v40, |v27|, |v28|, |v29|\n\t"
    "v_max3_f32 v10, v10, |v30|, |v31|\n\t"
    "v_max_f32 v10, v10, v40\n\t"
    "v_cmp_nge_f32 vcc, %[big], v10\n\t"
    "s_mov_b32 %[bg], %[big]\n\t"
    "v_mov_b32 v10, 0\n\t"
    "s_cbranch_vccnz 12f\n\t"
    "13:\n\t"
    "v_exp_f32 v24, v24\n\t"
    "v_exp_f32 v25, v25\n\t"
    "v_exp_f32 v26, v26\n\t"
    "v_exp_f32 v27, v27\n\t"
    "v_exp_f32 v28, v28\n\t"
    "v_exp_f32 v29, v29\n\t"
    "v_exp_f32 v30, v30\n\t"
    "v_exp_f32 v31, v31\n\t"
    "v_cvt_pk_bf16_f32 v40, v24, v25\n\t"
    "v_cvt_pk_bf16_f32 v41, v26, v27\n\t"
    "v_cvt_pk_bf16_f32 v42, v28, v29\n\t"
    "v_cvt_pk_bf16_f32 v43, v30, v31\n\t"
    "s_setprio 3\n\t"
    "s_waitcnt lgkmcnt(0)\n\t"
    "s_nop 0\n\t"
    "v_mfma_f32_16x16x32_bf16 v[44:47], v[32:35], v[40:43], 0\n\t"
    "v_mfma_f32_16x16x32_bf16 v[48:51], v[36:39], v[40:43], 0\n\t"
    "v_mfma_f32_16x16x32_bf16 v[52:55], v[52:55], v[40:43], 0\n\t"
    "v_mov_b32 v12, 0\n\t"
    "v_mov_b32 v13, 0\n\t"
    "s_mov_b32 exec_hi, 0\n\t"
    "v_mov_b32 v12, %[one]\n\t"
    "v_mov_b32 v13, %[one]\n\t"
    "s_mov_b32 exec_hi, -1\n\t"
    "s_barrier\n\t"
    "ds_read_b64_tr_b16 v[24:25], v11 offset:34816\n\t"
    "ds_read_b64_tr_b16 v[26:27], v11 offset:34880\n\t"
    "ds_read_b64_tr_b16 v[28:29], v11 offset:35328\n\t"
    "ds_read_b64_tr_b16 v[30:31], v11 offset:35392\n\t"
    "ds_read_b64_tr_b16 v[32:33], v11 offset:35840\n\t"
    "ds_read_b64_tr_b16 v[34:35], v11 offset:35904\n\t"
    "ds_read_b64_tr_b16 v[36:37], v11 offset:36352\n\t"
    "ds_read_b64_tr_b16 v[38:39], v11 offset:36416\n\t"
    "s_waitcnt lgkmcnt(6)\n\t"
    "v_mfma_f32_16x16x32_bf16 v[40:43], v[24:27], v[16:19], 0\n\t"
    "s_waitcnt lgkmcnt(4)\n\t"
    "v_mfma_f32_16x16x32_bf16 v[40:43], v[28:31], v[20:23], v[40:43]\n\t"
    "s_waitcnt lgkmcnt(2)\n\t"
    "v_mfma_f32_16x16x32_bf16 v[40:43], v[32:35], v[56:59], v[40:43]\n\t"
    "s_waitcnt lgkmcnt(0)\n\t"
    "v_mfma_f32_16x16x32_bf16 v[40:43], v[36:39], v[60:63], v[40:43]\n\t"
    "5:\n\t"
    "s_waitcnt vmcnt(0)\n\t"
    "v_max3_u32 v11, v6, v7, v8\n\t"
    "v_max_u32 v11, v11, v9\n\t"
    "v_cmp_eq_u32 vcc, -1, v11\n\t"
    "s_cbranch_vccnz 4f\n\t"
    "6:\n\t"
    "s_nop 2\n\t"
    "v_exp_f32 v28, v40\n\t"
    "v_max3_f32 v11, v40, v41, v42\n\t"
    "v_exp_f32 v29, v41\n\t"
    "v_max_f32 v11, v11, v43\n\t"
    "v_exp_f32 v30, v42\n\t"
    "v_cmp_nge_f32 vcc, %[bg], v11\n\t"
    "v_exp_f32 v31, v43\n\t"
    "s_cbranch_vccnz 10f\n\t"
    "11:\n\t"
    "v_cvt_pk_bf16_f32 v24, v28, v29\n\t"
    "v_cvt_pk_bf16_f32 v25, v30, v31\n\t"
    "s_nop 1\n\t"
    "v_mfma_f32_16x16x16_bf16 v[52:55], v[12:13], v[24:25], v[52:55]\n\t"
    "v_mfma_f32_16x16x16_bf16 v[44:47], v[6:7], v[24:25], v[44:47]\n\t"
    "v_mfma_f32_16x16x16_bf16 v[48:51], v[8:9], v[24:25], v[48:51]\n\t"
    "s_nop 5\n\t"
    "v_rcp_f32 v26, v52\n\t"
    "s_nop 0\n\t"
    "v_pk_mul_f32 v[44:45], v[44:45], v[26:27] op_sel_hi:[1,0]\n\t"
    "v_pk_mul_f32 v[46:47], v[46:47], v[26:27] op_sel_hi:[1,0]\n\t"
    "v_pk_mul_f32 v[48:49], v[48:49], v[26:27] op_sel_hi:[1,0]\n\t"
    "v_pk_mul_f32 v[50:51], v[50:51], v[26:27] op_sel_hi:[1,0]\n\t"
    "v_cvt_pk_bf16_f32 v36, v44, v45\n\t"
    "v_cvt_pk_bf16_f32 v37, v46, v47\n\t"
    "v_cvt_pk_bf16_f32 v38, v48, v49\n\t"
    "v_cvt_pk_bf16_f32 v39, v50, v51\n\t"
    "v_min_u32 v36, -2, v36\n\t"
    "v_min_u32 v37, -2, v37\n\t"
    "v_min_u32 v38, -2, v38\n\t"
    "v_min_u32 v39, -2, v39\n\t"
    "global_store_dwordx4 v15, v[36:39], %[od] sc1\n\t"
    "s_barrier\n\t"
    "v_permlane16_swap_b32 v6, v8\n\t"
    "v_permlane16_swap_b32 v7, v9\n\t"
    "v_and_b32 v11, 8, v14\n\t"
    "v_mul_u32_u24 v11, 31, v11\n\t"
    "v_add_u32 v14, v14, v11\n\t"
    "s_mov_b32 exec_hi, 0\n\t"
    "global_store_dwordx4 v14, v[6:9], %[kv] sc1\n\t"
    "s_mov_b32 exec_hi, -1\n\t"
    "s_nop 0\n\t"
    "s_waitcnt lgkmcnt(0)\n\t"
    "s_branch 9f\n\t"
    "1:\n\t"
    "s_add_u32 %[sp], %[sp], 1\n\t"
    "s_cmp_ge_u32 %[sp], %[cap]\n\t"
    "s_cbranch_scc1 7f\n\t"
    "global_load_dwordx4 v[40:43], v6, %[ag] sc1\n\t"
    "global_load_dwordx4 v[44:47], v6, %[ag] offset:1024 sc1\n\t"
    "s_waitcnt lgkmcnt(0)\n\t"
    "s_memtime %[tc]\n\t"
    "s_nop 7\n\t"
    "s_branch 3b\n\t"
    "7:\n\t"
    "s_mov_b32 %[cap], 0\n\t"
    "s_branch 2b\n\t"
    "4:\n\t"
    "s_add_u32 %[sp], %[sp], 0x10000\n\t"
    "s_lshr_b32 %[st], %[sp], 16\n\t"
    "s_cmp_ge_u32 %[st], %[cap]\n\t"
    "s_cbranch_scc1 8f\n\t"
    "s_mov_b32 exec_hi, 0\n\t"
    "global_load_dwordx2 v[6:7], v14, %[vs] sc1\n\t"
    "global_load_dwordx2 v[8:9], v14, %[vs] offset:256 sc1\n\t"
    "s_mov_b32 exec_hi, -1\n\t"
    "s_branch 5b\n\t"
    "8:\n\t"
    "s_mov_b32 %[cap], 0\n\t"
    "s_branch 6b\n\t"
    "10:\n\t"
    "v_max_f32 v11, v11, v10\n\t"
    "v_mov_b32 v26, v11\n\t"
    "s_nop 1\n\t"
    "v_permlane16_swap_b32 v11, v26\n\t"
    "v_max_f32 v11, v11, v26\n\t"
    "v_sub_f32 v28, v40, v11\n\t"
    "v_sub_f32 v29, v41, v11\n\t"
    "v_sub_f32 v30, v42, v11\n\t"
    "v_sub_f32 v31, v43, v11\n\t"
    "v_sub_f32 v11, v10, v11\n\t"
    "v_exp_f32 v11, v11\n\t"
    "v_exp_f32 v28, v28\n\t"
    "v_exp_f32 v29, v29\n\t"
    "v_exp_f32 v30, v30\n\t"
    "v_exp_f32 v31, v31\n\t"
    "v_mul_f32 v44, v44, v11\n\t"
    "v_mul_f32 v45, v45, v11\n\t"
    "v_mul_f32 v46, v46, v11\n\t"
    "v_mul_f32 v47, v47, v11\n\t"
    "v_mul_f32 v48, v48, v11\n\t"
    "v_mul_f32 v49, v49, v11\n\t"
    "v_mul_f32 v50, v50, v11\n\t"
    "v_mul_f32 v51, v51, v11\n\t"
    "v_mul_f32 v52, v52, v11\n\t"
    "v_mul_f32 v53, v53, v11\n\t"
    "v_mul_f32 v54, v54, v11\n\t"
    "v_mul_f32 v55, v55, v11\n\t"
    "s_branch 11b\n\t"
    "12:\n\t"
    "v_max3_f32 v10, v24, v25, v26\n\t"
    "v_max3_f32 v40, v27, v28, v29\n\t"
    "v_max3_f32 v10, v10, v30, v31\n\t"
    "v_max3_f32 v10, v10, v40, %[neg]\n\t"
    "v_mov_b32 v40, v10\n\t"
    "s_nop 1\n\t"
    "v_permlane16_swap_b32 v10, v40\n\t"
    "v_max_f32 v10, v10, v40\n\t"
    "v_mov_b32 v40, v10\n\t"
    "s_nop 1\n\t"
    "v_permlane32_swap_b32 v10, v40\n\t"
    "v_max_f32 v10, v10, v40\n\t"
    "v_sub_f32 v24, v24, v10\n\t"
    "v_sub_f32 v25, v25, v10\n\t"
    "v_sub_f32 v26, v26, v10\n\t"
    "v_sub_f32 v27, v27, v10\n\t"
    "v_sub_f32 v28, v28, v10\n\t"
    "v_sub_f32 v29, v29, v10\n\t"
    "v_sub_f32 v30, v30, v10\n\t"
    "v_sub_f32 v31, v31, v10\n\t"
    "s_mov_b32 %[bg], 0x7fc00000\n\t"
    "s_branch 13b\n\t"
    "9:"
    : [tc] "=&s"(tc), [sp] "=&s"(sp), [cap] "+s"(cap), [st] "=&s"(st), [bg] "=&s"(bg)
    : [ag] "s"(ag), [c44] "s"(0x4444u), [kv] "s"(kv), [vs] "s"(vs), [od] "s"(od), [od2] "s"(od + 262144), [neg] "s"(-1.0e38f), [lsc] "s"(LSC), [one] "s"(0x3F803F80u), [big] "s"(24.0f), "{v[64:95]}"(W.w0), "{v[96:127]}"(W.w1), "{v[128:159]}"(W.w2), "{v[160:191]}"(W.w3), "{v[192:223]}"(W.w4), "{v[224:255]}"(W.w5)
    : "v6", "v7", "v8", "v9", "v10", "v11", "v12", "v13", "v14", "v15", "v16", "v17", "v18", "v19", "v20", "v21", "v22", "v23", "v24", "v25", "v26", "v27", "v28", "v29", "v30", "v31", "v32", "v33", "v34", "v35", "v36", "v37", "v38", "v39", "v40", "v41", "v42", "v43", "v44", "v45", "v46", "v47", "v48", "v49", "v50", "v51", "v52", "v53", "v54", "v55", "v56", "v57", "v58", "v59", "v60", "v61", "v62", "v63", "vcc", "scc", "memory");
}
template <> __device__ __forceinline__ void qk_step<4>(const QKW& W, const char* ag, const char* vs, const char* od, const char* kv, unsigned& cap, unsigned& sp,
                                                        unsigned long long& tc, unsigned long long* ts, unsigned& d0, unsigned& d1) {
  unsigned st, bg;
  asm volatile("v_mbcnt_lo_u32_b32 v52, -1, 0\n\t"
    "v_mbcnt_hi_u32_b32 v52, -1, v52\n\t"
    "v_lshlrev_b32 v6, 4, v52\n\t"
    "s_mov_b32 %[sp], 0\n\t"
    "global_load_dwordx4 v[36:39], v6, %[ag] sc1\n\t"
    "global_load_dwordx4 v[40:43], v6, %[ag] offset:1024 sc1\n\t"
    "s_memtime %[tc]\n\t"
    "v_add_u32 v7, 73728, v6\n\t"
    "v_add_u32 v8, 131072, v6\n\t"
    "ds_read_b128 v[20:23], v7 offset:8192\n\t"
    "ds_read_b128 v[24:27], v7 offset:9216\n\t"
    "ds_read_b128 v[28:31], v7 offset:0\n\t"
    "ds_read_b128 v[32:35], v7 offset:1024\n\t"
    "v_lshrrev_b32 v44, 4, v52\n\t"
    "v_and_b32 v45, 15, v52\n\t"
    "v_and_b32 v53, 4, v52\n\t"
    "v_mul_u32_u24 v53, 0x2aaa, v53\n\t"
    "v_lshrrev_b32 v9, 1, v52\n\t"
    "v_and_b32 v9, 2, v9\n\t"
    "v_or3_b32 v9, v9, v53, %[c44]\n\t"
    "v_lshlrev_b32 v53, 2, v44\n\t"
    "v_lshl_or_b32 v10, v45, 4, v53\n\t"
    "3:\n\t"
    "v_mov_b32 v12, 0\n\t"
    "v_mov_b32 v13, 0\n\t"
    "v_mov_b32 v14, 0\n\t"
    "v_mov_b32 v15, 0\n\t"
    "v_mov_b32 v16, 0\n\t"
    "v_mov_b32 v17, 0\n\t"
    "v_mov_b32 v18, 0\n\t"
    "v_mov_b32 v19, 0\n\t"
    "s_waitcnt vmcnt(0) lgkmcnt(0)\n\t"
    "14:\n\t"
    "ds_write_b128 v6, v[36:39] offset:8192\n\t"
    "ds_write_b128 v6, v[40:43] offset:9216\n\t"
    "v_smfmac_f32_16x16x64_bf16 v[12:15], v[36:39], v[128:135], v9\n\t"
    "v_max3_u32 v52, v36, v37, v38\n\t"
    "v_max3_u32 v53, v39, v40, v41\n\t"
    "v_smfmac_f32_16x16x64_bf16 v[16:19], v[36:39], v[136:143], v9\n\t"
    "v_max3_u32 v52, v52, v42, v43\n\t"
    "v_max_u32 v52, v52, v53\n\t"
    "v_cmp_eq_u32 vcc, -1, v52\n\t"
    "s_cbranch_vccnz 1f\n\t"
    "2:\n\t"
    "s_waitcnt lgkmcnt(0)\n\t"
    "s_barrier\n\t"
    "s_memrealtime s[36:37]\n\t"
    "ds_read_b128 v[36:39], v6 offset:0\n\t"
    "ds_read_b128 v[44:47], v6 offset:4096\n\t"
    "v_smfmac_f32_16x16x64_bf16 v[12:15], v[40:43], v[144:151], v9\n\t"
    "ds_read_b128 v[48:51], v6 offset:5120\n\t"
    "ds_read_b128 v[52:55], v6 offset:6144\n\t"
    "v_smfmac_f32_16x16x64_bf16 v[16:19], v[40:43], v[152:159], v9\n\t"
    "ds_read_b128 v[40:43], v6 offset:1024\n\t"
    "s_waitcnt lgkmcnt(0)\n\t"
    "s_mov_b32 s38, s36\n\t"
    "s_min_u32 s37, %[d0], %[d1]\n\t"
    "s_add_u32 s36, s36, s37\n\t"
    "s_and_b32 s36, s36, 0xffffff\n\t"
    "s_or_b32 s36, s36, %[sn]\n\t"
    "v_lshlrev_b32 v11, 10, v6\n\t"
    "v_mov_b32 v5, s36\n\t"
    "s_mov_b64 exec, 15\n\t"
    "global_store_dword v11, v5, %[vs] offset:-4096 sc1\n\t"
    "s_mov_b64 exec, -1\n\t"
    "s_sleep 0\n\t"
    "v_smfmac_f32_16x16x64_bf16 v[12:15], v[36:39], v[56:63], v9\n\t"
    "v_smfmac_f32_16x16x64_bf16 v[16:19], v[36:39], v[28:35], v9\n\t"
    "ds_read_b128 v[28:31], v7 offset:16384\n\t"
    "ds_read_b128 v[32:35], v7 offset:17408\n\t"
    "ds_read_b128 v[36:39], v6 offset:7168\n\t"
    "v_smfmac_f32_16x16x64_bf16 v[12:15], v[44:47], v[64:71], v9\n\t"
    "v_smfmac_f32_16x16x64_bf16 v[16:19], v[44:47], v[72:79], v9\n\t"
    "ds_read_b128 v[44:47], v6 offset:10240\n\t"
    "v_smfmac_f32_16x16x64_bf16 v[12:15], v[48:51], v[80:87], v9\n\t"
    "v_smfmac_f32_16x16x64_bf16 v[16:19], v[48:51], v[88:95], v9\n\t"
    "ds_read_b128 v[48:51], v6 offset:11264\n\t"
    "v_smfmac_f32_16x16x64_bf16 v[12:15], v[52:55], v[96:103], v9\n\t"
    "v_smfmac_f32_16x16x64_bf16 v[16:19], v[52:55], v[104:111], v9\n\t"
    "ds_read_b128 v[52:55], v6 offset:2048\n\t"
    "v_smfmac_f32_16x16x64_bf16 v[12:15], v[40:43], v[20:27], v9\n\t"
    "ds_read_b128 v[20:23], v8 offset:0\n\t"
    "ds_read_b128 v[24:27], v8 offset:1024\n\t"
    "s_waitcnt lgkmcnt(6)\n\t"
    "v_smfmac_f32_16x16x64_bf16 v[16:19], v[40:43], v[28:35], v9\n\t"
    "ds_read_b128 v[28:31], v8 offset:8192\n\t"
    "ds_read_b128 v[32:35], v8 offset:9216\n\t"
    "ds_read_b128 v[40:43], v6 offset:12288\n\t"
    "s_waitcnt lgkmcnt(8)\n\t"
    "v_smfmac_f32_16x16x64_bf16 v[12:15], v[36:39], v[112:119], v9\n\t"
    "v_smfmac_f32_16x16x64_bf16 v[16:19], v[36:39], v[120:127], v9\n\t"
    "ds_read_b128 v[36:39], v6 offset:13312\n\t"
    "s_waitcnt lgkmcnt(8)\n\t"
    "v_smfmac_f32_16x16x64_bf16 v[12:15], v[44:47], v[160:167], v9\n\t"
    "v_smfmac_f32_16x16x64_bf16 v[16:19], v[44:47], v[168:175], v9\n\t"
    "ds_read_b128 v[44:47], v6 offset:14336\n\t"
    "s_barrier\n\t"
    "s_setprio 2\n\t"
    "s_waitcnt lgkmcnt(8)\n\t"
    "v_smfmac_f32_16x16x64_bf16 v[12:15], v[48:51], v[176:183], v9\n\t"
    "v_smfmac_f32_16x16x64_bf16 v[16:19], v[48:51], v[184:191], v9\n\t"
    "ds_read_b128 v[48:51], v6 offset:3072\n\t"
    "s_waitcnt lgkmcnt(6)\n\t"
    "v_smfmac_f32_16x16x64_bf16 v[12:15], v[52:55], v[20:27], v9\n\t"
    "ds_read_b128 v[20:23], v8 offset:16384\n\t"
    "ds_read_b128 v[24:27], v8 offset:17408\n\t"
    "s_waitcnt lgkmcnt(6)\n\t"
    "v_smfmac_f32_16x16x64_bf16 v[16:19], v[52:55], v[28:35], v9\n\t"
    "ds_read_b128 v[28:31], v8 offset:24576\n\t"
    "ds_read_b128 v[32:35], v8 offset:25600\n\t"
    "ds_read_b128 v[52:55], v6 offset:15360\n\t"
    "s_waitcnt lgkmcnt(8)\n\t"
    "v_smfmac_f32_16x16x64_bf16 v[12:15], v[40:43], v[192:199], v9\n\t"
    "v_smfmac_f32_16x16x64_bf16 v[16:19], v[40:43], v[200:207], v9\n\t"
    "s_waitcnt lgkmcnt(7)\n\t"
    "v_smfmac_f32_16x16x64_bf16 v[12:15], v[36:39], v[208:215], v9\n\t"
    "v_smfmac_f32_16x16x64_bf16 v[16:19], v[36:39], v[216:223], v9\n\t"
    "s_waitcnt lgkmcnt(6)\n\t"
    "v_smfmac_f32_16x16x64_bf16 v[12:15], v[44:47], v[224:231], v9\n\t"
    "v_smfmac_f32_16x16x64_bf16 v[16:19], v[44:47], v[232:239], v9\n\t"
    "s_waitcnt lgkmcnt(3)\n\t"
    "v_smfmac_f32_16x16x64_bf16 v[12:15], v[48:51], v[20:27], v9\n\t"
    "s_waitcnt lgkmcnt(1)\n\t"
    "v_smfmac_f32_16x16x64_bf16 v[16:19], v[48:51], v[28:35], v9\n\t"
    "s_waitcnt lgkmcnt(0)\n\t"
    "v_smfmac_f32_16x16x64_bf16 v[12:15], v[52:55], v[240:247], v9\n\t"
    "v_smfmac_f32_16x16x64_bf16 v[16:19], v[52:55], v[248:255], v9\n\t"
    "s_nop 6\n\t"
    "v_permlane16_swap_b32 v12, v14\n\t"
    "v_permlane16_swap_b32 v13, v15\n\t"
    "v_permlane16_swap_b32 v16, v18\n\t"
    "v_permlane16_swap_b32 v17, v19\n\t"
    "v_add_f32 v12, v12, v14\n\t"
    "v_add_f32 v13, v13, v15\n\t"
    "v_add_f32 v16, v16, v18\n\t"
    "v_cvt_pk_bf16_f32 v12, v12, v13\n\t"
    "v_add_f32 v17, v17, v19\n\t"
    "ds_write_b32 v10, v12 offset:34816\n\t"
    "v_cvt_pk_bf16_f32 v16, v16, v17\n\t"
    "ds_write_b32 v10, v16 offset:35072\n\t"
    "s_waitcnt lgkmcnt(0)\n\t"
    "s_setprio 0\n\t"
    "s_barrier\n\t"
    "s_memrealtime s[36:37]\n\t"
    "s_waitcnt lgkmcnt(0)\n\t"
    "s_and_b32 s36, s36, 0xffffff\n\t"
    "s_or_b32 s36, s36, %[sn]\n\t"
    "v_lshlrev_b32 v7, 10, v6\n\t"
    "v_mov_b32 v8, s36\n\t"
    "s_mov_b64 exec, 15\n\t"
    "global_store_dword v7, v8, %[vs] offset:0 sc1\n\t"
    "s_mov_b64 exec, -1\n\t"
    "s_barrier\n\t"
    "s_memrealtime s[36:37]\n\t"
    "s_mov_b32 %[d1], %[d0]\n\t"
    "s_waitcnt lgkmcnt(0)\n\t"
    "s_sub_u32 %[d0], s36, s38\n\t"
    "s_min_u32 %[d0], %[d0], 200\n\t"
    "s_waitcnt lgkmcnt(0)\n\t"
    "s_branch 9f\n\t"
    "1:\n\t"
    "s_add_u32 %[sp], %[sp], 1\n\t"
    "s_cmp_ge_u32 %[sp], %[cap]\n\t"
    "s_cbranch_scc1 7f\n\t"
    "global_load_dwordx4 v[36:39], v6, %[ag] sc1\n\t"
    "global_load_dwordx4 v[40:43], v6, %[ag] offset:1024 sc1\n\t"
    "s_waitcnt lgkmcnt(0)\n\t"
    "s_memtime %[tc]\n\t"
    "s_nop 7\n\t"
    "s_branch 3b\n\t"
    "7:\n\t"
    "s_mov_b32 %[cap], 0\n\t"
    "s_branch 2b\n\t"
    "9:"
    : [tc] "=&s"(tc), [sp] "=&s"(sp), [cap] "+s"(cap), [d0] "+s"(d0), [d1] "+s"(d1)
    : [ag] "s"(ag), [c44] "s"(0x4444u), [vs] "s"(vs), [sn] "s"((unsigned)(size_t)od), "{v[64:95]}"(W.w0), "{v[96:127]}"(W.w1), "{v[128:159]}"(W.w2), "{v[160:191]}"(W.w3), "{v[192:223]}"(W.w4), "{v[224:255]}"(W.w5), "{v[56:63]}"(W.x)
    : "v5", "v6", "v7", "v8", "v9", "v10", "v11", "v12", "v13", "v14", "v15", "v16", "v17", "v18", "v19", "v20", "v21", "v22", "v23", "v24", "v25", "v26", "v27", "v28", "v29", "v30", "v31", "v32", "v33", "v34", "v35", "v36", "v37", "v38", "v39", "v40", "v41", "v42", "v43", "v44", "v45", "v46", "v47", "v48", "v49", "v50", "v51", "v52", "v53", "v54", "v55", "vcc", "scc", "memory", "s36", "s37", "s38");
}
template <> __device__ __forceinline__ void qk_step<5>(const QKW& W, const char* ag, const char* vs, const char* od, const char* kv, unsigned& cap, unsigned& sp,
                                                        unsigned long long& tc, unsigned long long* ts, unsigned& d0, unsigned& d1) {
  unsigned st, bg;
  asm volatile("v_mbcnt_lo_u32_b32 v52, -1, 0\n\t"
    "v_mbcnt_hi_u32_b32 v52, -1, v52\n\t"
    "v_lshlrev_b32 v6, 4, v52\n\t"
    "s_mov_b32 %[sp], 0\n\t"
    "global_load_dwordx4 v[36:39], v6, %[ag] sc1\n\t"
    "global_load_dwordx4 v[40:43], v6, %[ag] offset:1024 sc1\n\t"
    "s_memtime %[tc]\n\t"
    "v_add_u32 v7, 75776, v6\n\t"
    "v_add_u32 v8, 133120, v6\n\t"
    "ds_read_b128 v[20:23], v7 offset:8192\n\t"
    "ds_read_b128 v[24:27], v7 offset:9216\n\t"
    "ds_read_b128 v[28:31], v7 offset:0\n\t"
    "ds_read_b128 v[32:35], v7 offset:1024\n\t"
    "v_lshrrev_b32 v44, 4, v52\n\t"
    "v_and_b32 v45, 15, v52\n\t"
    "v_and_b32 v53, 4, v52\n\t"
    "v_mul_u32_u24 v53, 0x2aaa, v53\n\t"
    "v_lshrrev_b32 v9, 1, v52\n\t"
    "v_and_b32 v9, 2, v9\n\t"
    "v_or3_b32 v9, v9, v53, %[c44]\n\t"
    "v_lshlrev_b32 v53, 2, v44\n\t"
    "v_lshl_or_b32 v10, v45, 4, v53\n\t"
    "3:\n\t"
    "v_mov_b32 v12, 0\n\t"
    "v_mov_b32 v13, 0\n\t"
    "v_mov_b32 v14, 0\n\t"
    "v_mov_b32 v15, 0\n\t"
    "v_mov_b32 v16, 0\n\t"
    "v_mov_b32 v17, 0\n\t"
    "v_mov_b32 v18, 0\n\t"
    "v_mov_b32 v19, 0\n\t"
    "s_waitcnt vmcnt(0) lgkmcnt(0)\n\t"
    "14:\n\t"
    "ds_write_b128 v6, v[36:39] offset:10240\n\t"
    "ds_write_b128 v6, v[40:43] offset:11264\n\t"
    "v_smfmac_f32_16x16x64_bf16 v[12:15], v[36:39], v[160:167], v9\n\t"
    "v_max3_u32 v52, v36, v37, v38\n\t"
    "v_max3_u32 v53, v39, v40, v41\n\t"
    "v_smfmac_f32_16x16x64_bf16 v[16:19], v[36:39], v[168:175], v9\n\t"
    "v_max3_u32 v52, v52, v42, v43\n\t"
    "v_max_u32 v52, v52, v53\n\t"
    "v_cmp_eq_u32 vcc, -1, v52\n\t"
    "s_cbranch_vccnz 1f\n\t"
    "2:\n\t"
    "s_waitcnt lgkmcnt(0)\n\t"
    "s_barrier\n\t"
    "ds_read_b128 v[36:39], v6 offset:0\n\t"
    "ds_read_b128 v[44:47], v6 offset:4096\n\t"
    "v_smfmac_f32_16x16x64_bf16 v[12:15], v[40:43], v[176:183], v9\n\t"
    "ds_read_b128 v[48:51], v6 offset:5120\n\t"
    "ds_read_b128 v[52:55], v6 offset:6144\n\t"
    "v_smfmac_f32_16x16x64_bf16 v[16:19], v[40:43], v[184:191], v9\n\t"
    "ds_read_b128 v[40:43], v6 offset:1024\n\t"
    "s_sleep 0\n\t"
    "s_waitcnt lgkmcnt(4)\n\t"
    "v_smfmac_f32_16x16x64_bf16 v[12:15], v[36:39], v[56:63], v9\n\t"
    "v_smfmac_f32_16x16x64_bf16 v[16:19], v[36:39], v[28:35], v9\n\t"
    "ds_read_b128 v[28:31], v7 offset:16384\n\t"
    "ds_read_b128 v[32:35], v7 offset:17408\n\t"
    "ds_read_b128 v[36:39], v6 offset:7168\n\t"
    "s_waitcnt lgkmcnt(6)\n\t"
    "v_smfmac_f32_16x16x64_bf16 v[12:15], v[44:47], v[64:71], v9\n\t"
    "v_smfmac_f32_16x16x64_bf16 v[16:19], v[44:47], v[72:79], v9\n\t"
    "ds_read_b128 v[44:47], v6 offset:8192\n\t"
    "s_waitcnt lgkmcnt(6)\n\t"
    "v_smfmac_f32_16x16x64_bf16 v[12:15], v[48:51], v[80:87], v9\n\t"
    "v_smfmac_f32_16x16x64_bf16 v[16:19], v[48:51], v[88:95], v9\n\t"
    "ds_read_b128 v[48:51], v6 offset:9216\n\t"
    "s_waitcnt lgkmcnt(6)\n\t"
    "v_smfmac_f32_16x16x64_bf16 v[12:15], v[52:55], v[96:103], v9\n\t"
    "v_smfmac_f32_16x16x64_bf16 v[16:19], v[52:55], v[104:111], v9\n\t"
    "ds_read_b128 v[52:55], v6 offset:2048\n\t"
    "s_waitcnt lgkmcnt(6)\n\t"
    "v_smfmac_f32_16x16x64_bf16 v[12:15], v[40:43], v[20:27], v9\n\t"
    "ds_read_b128 v[20:23], v8 offset:0\n\t"
    "ds_read_b128 v[24:27], v8 offset:1024\n\t"
    "s_waitcnt lgkmcnt(6)\n\t"
    "v_smfmac_f32_16x16x64_bf16 v[16:19], v[40:43], v[28:35], v9\n\t"
    "ds_read_b128 v[28:31], v8 offset:8192\n\t"
    "ds_read_b128 v[32:35], v8 offset:9216\n\t"
    "ds_read_b128 v[40:43], v6 offset:12288\n\t"
    "s_waitcnt lgkmcnt(8)\n\t"
    "v_smfmac_f32_16x16x64_bf16 v[12:15], v[36:39], v[112:119], v9\n\t"
    "v_smfmac_f32_16x16x64_bf16 v[16:19], v[36:39], v[120:127], v9\n\t"
    "ds_read_b128 v[36:39], v6 offset:13312\n\t"
    "s_waitcnt lgkmcnt(8)\n\t"
    "v_smfmac_f32_16x16x64_bf16 v[12:15], v[44:47], v[128:135], v9\n\t"
    "v_smfmac_f32_16x16x64_bf16 v[16:19], v[44:47], v[136:143], v9\n\t"
    "ds_read_b128 v[44:47], v6 offset:14336\n\t"
    "s_barrier\n\t"
    "s_setprio 2\n\t"
    "s_waitcnt lgkmcnt(8)\n\t"
    "v_smfmac_f32_16x16x64_bf16 v[12:15], v[48:51], v[144:151], v9\n\t"
    "v_smfmac_f32_16x16x64_bf16 v[16:19], v[48:51], v[152:159], v9\n\t"
    "ds_read_b128 v[48:51], v6 offset:3072\n\t"
    "s_waitcnt lgkmcnt(6)\n\t"
    "v_smfmac_f32_16x16x64_bf16 v[12:15], v[52:55], v[20:27], v9\n\t"
    "ds_read_b128 v[20:23], v8 offset:16384\n\t"
    "ds_read_b128 v[24:27], v8 offset:17408\n\t"
    "s_waitcnt lgkmcnt(6)\n\t"
    "v_smfmac_f32_16x16x64_bf16 v[16:19], v[52:55], v[28:35], v9\n\t"
    "ds_read_b128 v[28:31], v8 offset:24576\n\t"
    "ds_read_b128 v[32:35], v8 offset:25600\n\t"
    "ds_read_b128 v[52:55], v6 offset:15360\n\t"
    "s_waitcnt lgkmcnt(8)\n\t"
    "v_smfmac_f32_16x16x64_bf16 v[12:15], v[40:43], v[192:199], v9\n\t"
    "v_smfmac_f32_16x16x64_bf16 v[16:19], v[40:43], v[200:207], v9\n\t"
    "s_waitcnt lgkmcnt(7)\n\t"
    "v_smfmac_f32_16x16x64_bf16 v[12:15], v[36:39], v[208:215], v9\n\t"
    "v_smfmac_f32_16x16x64_bf16 v[16:19], v[36:39], v[216:223], v9\n\t"
    "s_waitcnt lgkmcnt(6)\n\t"
    "v_smfmac_f32_16x16x64_bf16 v[12:15], v[44:47], v[224:231], v9\n\t"
    "v_smfmac_f32_16x16x64_bf16 v[16:19], v[44:47], v[232:239], v9\n\t"
    "s_waitcnt lgkmcnt(3)\n\t"
    "v_smfmac_f32_16x16x64_bf16 v[12:15], v[48:51], v[20:27], v9\n\t"
    "s_waitcnt lgkmcnt(1)\n\t"
    "v_smfmac_f32_16x16x64_bf16 v[16:19], v[48:51], v[28:35], v9\n\t"
    "s_waitcnt lgkmcnt(0)\n\t"
    "v_smfmac_f32_16x16x64_bf16 v[12:15], v[52:55], v[240:247], v9\n\t"
    "v_smfmac_f32_16x16x64_bf16 v[16:19], v[52:55], v[248:255], v9\n\t"
    "s_nop 6\n\t"
    "v_permlane16_swap_b32 v12, v14\n\t"
    "v_permlane16_swap_b32 v13, v15\n\t"
    "v_permlane16_swap_b32 v16, v18\n\t"
    "v_permlane16_swap_b32 v17, v19\n\t"
    "v_add_f32 v12, v12, v14\n\t"
    "v_add_f32 v13, v13, v15\n\t"
    "v_add_f32 v16, v16, v18\n\t"
    "v_cvt_pk_bf16_f32 v12, v12, v13\n\t"
    "v_add_f32 v17, v17, v19\n\t"
    "ds_write_b32 v10, v12 offset:35328\n\t"
    "v_cvt_pk_bf16_f32 v16, v16, v17\n\t"
    "ds_write_b32 v10, v16 offset:35584\n\t"
    "s_waitcnt lgkmcnt(0)\n\t"
    "s_setprio 0\n\t"
    "s_barrier\n\t"
    "ds_read_b128 v[36:39], v6 offset:34816\n\t"
    "ds_read_b128 v[40:43], v6 offset:35840\n\t"
    "s_waitcnt lgkmcnt(0)\n\t"
    "v_min_u32 v36, -2, v36\n\t"
    "v_min_u32 v37, -2, v37\n\t"
    "v_min_u32 v38, -2, v38\n\t"
    "v_min_u32 v39, -2, v39\n\t"
    "v_min_u32 v40, -2, v40\n\t"
    "v_min_u32 v41, -2, v41\n\t"
    "v_min_u32 v42, -2, v42\n\t"
    "v_min_u32 v43, -2, v43\n\t"
    "global_store_dwordx4 v6, v[36:39], %[vs] sc1\n\t"
    "global_store_dwordx4 v6, v[40:43], %[vs] offset:1024 sc1\n\t"
    "s_nop 1\n\t"
    "s_barrier\n\t"
    "s_waitcnt lgkmcnt(0)\n\t"
    "s_branch 9f\n\t"
    "1:\n\t"
    "s_add_u32 %[sp], %[sp], 1\n\t"
    "s_cmp_ge_u32 %[sp], %[cap]\n\t"
    "s_cbranch_scc1 7f\n\t"
    "global_load_dwordx4 v[36:39], v6, %[ag] sc1\n\t"
    "global_load_dwordx4 v[40:43], v6, %[ag] offset:1024 sc1\n\t"
    "s_waitcnt lgkmcnt(0)\n\t"
    "s_memtime %[tc]\n\t"
    "s_nop 7\n\t"
    "s_branch 3b\n\t"
    "7:\n\t"
    "s_mov_b32 %[cap], 0\n\t"
    "s_branch 2b\n\t"
    "9:"
    : [tc] "=&s"(tc), [sp] "=&s"(sp), [cap] "+s"(cap)
    : [ag] "s"(ag), [c44] "s"(0x4444u), [vs] "s"(vs), "{v[64:95]}"(W.w0), "{v[96:127]}"(W.w1), "{v[128:159]}"(W.w2), "{v[160:191]}"(W.w3), "{v[192:223]}"(W.w4), "{v[224:255]}"(W.w5), "{v[56:63]}"(W.x)
    : "v6", "v7", "v8", "v9", "v10", "v11", "v12", "v13", "v14", "v15", "v16", "v17", "v18", "v19", "v20", "v21", "v22", "v23", "v24", "v25", "v26", "v27", "v28", "v29", "v30", "v31", "v32", "v33", "v34", "v35", "v36", "v37", "v38", "v39", "v40", "v41", "v42", "v43", "v44", "v45", "v46", "v47", "v48", "v49", "v50", "v51", "v52", "v53", "v54", "v55", "vcc", "scc", "memory");
}
template <> __device__ __forceinline__ void qk_step<6>(const QKW& W, const char* ag, const char* vs, const char* od, const char* kv, unsigned& cap, unsigned& sp,
                                                        unsigned long long& tc, unsigned long long* ts, unsigned& d0, unsigned& d1) {
  unsigned st, bg;
  asm volatile("v_mbcnt_lo_u32_b32 v52, -1, 0\n\t"
    "v_mbcnt_hi_u32_b32 v52, -1, v52\n\t"
    "v_lshlrev_b32 v6, 4, v52\n\t"
    "s_mov_b32 %[sp], 0\n\t"
    "global_load_dwordx4 v[36:39], v6, %[ag] sc1\n\t"
    "global_load_dwordx4 v[40:43], v6, %[ag] offset:1024 sc1\n\t"
    "s_memtime %[tc]\n\t"
    "v_add_u32 v7, 77824, v6\n\t"
    "v_add_u32 v8, 135168, v6\n\t"
    "ds_read_b128 v[20:23], v7 offset:8192\n\t"
    "ds_read_b128 v[24:27], v7 offset:9216\n\t"
    "ds_read_b128 v[28:31], v7 offset:0\n\t"
    "ds_read_b128 v[32:35], v7 offset:1024\n\t"
    "v_lshrrev_b32 v44, 4, v52\n\t"
    "v_and_b32 v45, 15, v52\n\t"
    "v_and_b32 v53, 4, v52\n\t"
    "v_mul_u32_u24 v53, 0x2aaa, v53\n\t"
    "v_lshrrev_b32 v9, 1, v52\n\t"
    "v_and_b32 v9, 2, v9\n\t"
    "v_or3_b32 v9, v9, v53, %[c44]\n\t"
    "v_lshlrev_b32 v53, 2, v44\n\t"
    "v_lshl_or_b32 v10, v45, 4, v53\n\t"
    "3:\n\t"
    "v_mov_b32 v12, 0\n\t"
    "v_mov_b32 v13, 0\n\t"
    "v_mov_b32 v14, 0\n\t"
    "v_mov_b32 v15, 0\n\t"
    "v_mov_b32 v16, 0\n\t"
    "v_mov_b32 v17, 0\n\t"
    "v_mov_b32 v18, 0\n\t"
    "v_mov_b32 v19, 0\n\t"
    "s_waitcnt vmcnt(0) lgkmcnt(0)\n\t"
    "14:\n\t"
    "ds_write_b128 v6, v[36:39] offset:12288\n\t"
    "ds_write_b128 v6, v[40:43] offset:13312\n\t"
    "v_smfmac_f32_16x16x64_bf16 v[12:15], v[36:39], v[192:199], v9\n\t"
    "v_max3_u32 v52, v36, v37, v38\n\t"
    "v_max3_u32 v53, v39, v40, v41\n\t"
    "v_smfmac_f32_16x16x64_bf16 v[16:19], v[36:39], v[200:207], v9\n\t"
    "v_max3_u32 v52, v52, v42, v43\n\t"
    "v_max_u32 v52, v52, v53\n\t"
    "v_cmp_eq_u32 vcc, -1, v52\n\t"
    "s_cbranch_vccnz 1f\n\t"
    "2:\n\t"
    "s_waitcnt lgkmcnt(0)\n\t"
    "s_barrier\n\t"
    "ds_read_b128 v[36:39], v6 offset:0\n\t"
    "ds_read_b128 v[44:47], v6 offset:4096\n\t"
    "v_smfmac_f32_16x16x64_bf16 v[12:15], v[40:43], v[208:215], v9\n\t"
    "ds_read_b128 v[48:51], v6 offset:5120\n\t"
    "ds_read_b128 v[52:55], v6 offset:6144\n\t"
    "v_smfmac_f32_16x16x64_bf16 v[16:19], v[40:43], v[216:223], v9\n\t"
    "ds_read_b128 v[40:43], v6 offset:1024\n\t"
    "s_sleep 0\n\t"
    "s_waitcnt lgkmcnt(4)\n\t"
    "v_smfmac_f32_16x16x64_bf16 v[12:15], v[36:39], v[56:63], v9\n\t"
    "v_smfmac_f32_16x16x64_bf16 v[16:19], v[36:39], v[28:35], v9\n\t"
    "ds_read_b128 v[28:31], v7 offset:16384\n\t"
    "ds_read_b128 v[32:35], v7 offset:17408\n\t"
    "ds_read_b128 v[36:39], v6 offset:7168\n\t"
    "s_waitcnt lgkmcnt(6)\n\t"
    "v_smfmac_f32_16x16x64_bf16 v[12:15], v[44:47], v[64:71], v9\n\t"
    "v_smfmac_f32_16x16x64_bf16 v[16:19], v[44:47], v[72:79], v9\n\t"
    "ds_read_b128 v[44:47], v6 offset:8192\n\t"
    "s_waitcnt lgkmcnt(6)\n\t"
    "v_smfmac_f32_16x16x64_bf16 v[12:15], v[48:51], v[80:87], v9\n\t"
    "v_smfmac_f32_16x16x64_bf16 v[16:19], v[48:51], v[88:95], v9\n\t"
    "ds_read_b128 v[48:51], v6 offset:9216\n\t"
    "s_waitcnt lgkmcnt(6)\n\t"
    "v_smfmac_f32_16x16x64_bf16 v[12:15], v[52:55], v[96:103], v9\n\t"
    "v_smfmac_f32_16x16x64_bf16 v[16:19], v[52:55], v[104:111], v9\n\t"
    "ds_read_b128 v[52:55], v6 offset:2048\n\t"
    "s_waitcnt lgkmcnt(6)\n\t"
    "v_smfmac_f32_16x16x64_bf16 v[12:15], v[40:43], v[20:27], v9\n\t"
    "ds_read_b128 v[20:23], v8 offset:0\n\t"
    "ds_read_b128 v[24:27], v8 offset:1024\n\t"
    "s_waitcnt lgkmcnt(6)\n\t"
    "v_smfmac_f32_16x16x64_bf16 v[16:19], v[40:43], v[28:35], v9\n\t"
    "ds_read_b128 v[28:31], v8 offset:8192\n\t"
    "ds_read_b128 v[32:35], v8 offset:9216\n\t"
    "ds_read_b128 v[40:43], v6 offset:10240\n\t"
    "s_waitcnt lgkmcnt(8)\n\t"
    "v_smfmac_f32_16x16x64_bf16 v[12:15], v[36:39], v[112:119], v9\n\t"
    "v_smfmac_f32_16x16x64_bf16 v[16:19], v[36:39], v[120:127], v9\n\t"
    "ds_read_b128 v[36:39], v6 offset:11264\n\t"
    "s_waitcnt lgkmcnt(8)\n\t"
    "v_smfmac_f32_16x16x64_bf16 v[12:15], v[44:47], v[128:135], v9\n\t"
    "v_smfmac_f32_16x16x64_bf16 v[16:19], v[44:47], v[136:143], v9\n\t"
    "ds_read_b128 v[44:47], v6 offset:14336\n\t"
    "s_barrier\n\t"
    "s_setprio 2\n\t"
    "s_waitcnt lgkmcnt(8)\n\t"
    "v_smfmac_f32_16x16x64_bf16 v[12:15], v[48:51], v[144:151], v9\n\t"
    "v_smfmac_f32_16x16x64_bf16 v[16:19], v[48:51], v[152:159], v9\n\t"
    "ds_read_b128 v[48:51], v6 offset:3072\n\t"
    "s_waitcnt lgkmcnt(6)\n\t"
    "v_smfmac_f32_16x16x64_bf16 v[12:15], v[52:55], v[20:27], v9\n\t"
    "ds_read_b128 v[20:23], v8 offset:16384\n\t"
    "ds_read_b128 v[24:27], v8 offset:17408\n\t"
    "s_waitcnt lgkmcnt(6)\n\t"
    "v_smfmac_f32_16x16x64_bf16 v[16:19], v[52:55], v[28:35], v9\n\t"
    "ds_read_b128 v[28:31], v8 offset:24576\n\t"
    "ds_read_b128 v[32:35], v8 offset:25600\n\t"
    "ds_read_b128 v[52:55], v6 offset:15360\n\t"
    "s_waitcnt lgkmcnt(8)\n\t"
    "v_smfmac_f32_16x16x64_bf16 v[12:15], v[40:43], v[160:167], v9\n\t"
    "v_smfmac_f32_16x16x64_bf16 v[16:19], v[40:43], v[168:175], v9\n\t"
    "s_waitcnt lgkmcnt(7)\n\t"
    "v_smfmac_f32_16x16x64_bf16 v[12:15], v[36:39], v[176:183], v9\n\t"
    "v_smfmac_f32_16x16x64_bf16 v[16:19], v[36:39], v[184:191], v9\n\t"
    "s_waitcnt lgkmcnt(6)\n\t"
    "v_smfmac_f32_16x16x64_bf16 v[12:15], v[44:47], v[224:231], v9\n\t"
    "v_smfmac_f32_16x16x64_bf16 v[16:19], v[44:47], v[232:239], v9\n\t"
    "s_waitcnt lgkmcnt(3)\n\t"
    "v_smfmac_f32_16x16x64_bf16 v[12:15], v[48:51], v[20:27], v9\n\t"
    "s_waitcnt lgkmcnt(1)\n\t"
    "v_smfmac_f32_16x16x64_bf16 v[16:19], v[48:51], v[28:35], v9\n\t"
    "s_waitcnt lgkmcnt(0)\n\t"
    "v_smfmac_f32_16x16x64_bf16 v[12:15], v[52:55], v[240:247], v9\n\t"
    "v_smfmac_f32_16x16x64_bf16 v[16:19], v[52:55], v[248:255], v9\n\t"
    "s_nop 6\n\t"
    "v_permlane16_swap_b32 v12, v14\n\t"
    "v_permlane16_swap_b32 v13, v15\n\t"
    "v_permlane16_swap_b32 v16, v18\n\t"
    "v_permlane16_swap_b32 v17, v19\n\t"
    "v_add_f32 v12, v12, v14\n\t"
    "v_add_f32 v13, v13, v15\n\t"
    "v_add_f32 v16, v16, v18\n\t"
    "v_cvt_pk_bf16_f32 v12, v12, v13\n\t"
    "v_add_f32 v17, v17, v19\n\t"
    "ds_write_b32 v10, v12 offset:35840\n\t"
    "v_cvt_pk_bf16_f32 v16, v16, v17\n\t"
    "ds_write_b32 v10, v16 offset:36096\n\t"
    "s_waitcnt lgkmcnt(0)\n\t"
    "s_setprio 0\n\t"
    "s_barrier\n\t"
    "s_barrier\n\t"
    "s_waitcnt lgkmcnt(0)\n\t"
    "s_branch 9f\n\t"
    "1:\n\t"
    "s_add_u32 %[sp], %[sp], 1\n\t"
    "s_cmp_ge_u32 %[sp], %[cap]\n\t"
    "s_cbranch_scc1 7f\n\t"
    "global_load_dwordx4 v[36:39], v6, %[ag] sc1\n\t"
    "global_load_dwordx4 v[40:43], v6, %[ag] offset:1024 sc1\n\t"
    "s_waitcnt lgkmcnt(0)\n\t"
    "s_memtime %[tc]\n\t"
    "s_nop 7\n\t"
    "s_branch 3b\n\t"
    "7:\n\t"
    "s_mov_b32 %[cap], 0\n\t"
    "s_branch 2b\n\t"
    "9:"
    : [tc] "=&s"(tc), [sp] "=&s"(sp), [cap] "+s"(cap)
    : [ag] "s"(ag), [c44] "s"(0x4444u), "{v[64:95]}"(W.w0), "{v[96:127]}"(W.w1), "{v[128:159]}"(W.w2), "{v[160:191]}"(W.w3), "{v[192:223]}"(W.w4), "{v[224:255]}"(W.w5), "{v[56:63]}"(W.x)
    : "v6", "v7", "v8", "v9", "v10", "v11", "v12", "v13", "v14", "v15", "v16", "v17", "v18", "v19", "v20", "v21", "v22", "v23", "v24", "v25", "v26", "v27", "v28", "v29", "v30", "v31", "v32", "v33", "v34", "v35", "v36", "v37", "v38", "v39", "v40", "v41", "v42", "v43", "v44", "v45", "v46", "v47", "v48", "v49", "v50", "v51", "v52", "v53", "v54", "v55", "vcc", "scc", "memory");
}
template <> __device__ __forceinline__ void qk_step<7>(const QKW& W, const char* ag, const char* vs, const char* od, const char* kv, unsigned& cap, unsigned& sp,
                                                        unsigned long long& tc, unsigned long long* ts, unsigned& d0, unsigned& d1) {
  unsigned st, bg;
  asm volatile("v_mbcnt_lo_u32_b32 v52, -1, 0\n\t"
    "v_mbcnt_hi_u32_b32 v52, -1, v52\n\t"
    "v_lshlrev_b32 v6, 4, v52\n\t"
    "s_mov_b32 %[sp], 0\n\t"
    "global_load_dwordx4 v[36:39], v6, %[ag] sc1\n\t"
    "global_load_dwordx4 v[40:43], v6, %[ag] offset:1024 sc1\n\t"
    "s_memtime %[tc]\n\t"
    "v_add_u32 v7, 79872, v6\n\t"
    "v_add_u32 v8, 137216, v6\n\t"
    "ds_read_b128 v[20:23], v7 offset:8192\n\t"
    "ds_read_b128 v[24:27], v7 offset:9216\n\t"
    "ds_read_b128 v[28:31], v7 offset:0\n\t"
    "ds_read_b128 v[32:35], v7 offset:1024\n\t"
    "v_lshrrev_b32 v44, 4, v52\n\t"
    "v_and_b32 v45, 15, v52\n\t"
    "v_and_b32 v53, 4, v52\n\t"
    "v_mul_u32_u24 v53, 0x2aaa, v53\n\t"
    "v_lshrrev_b32 v9, 1, v52\n\t"
    "v_and_b32 v9, 2, v9\n\t"
    "v_or3_b32 v9, v9, v53, %[c44]\n\t"
    "v_lshlrev_b32 v53, 2, v44\n\t"
    "v_lshl_or_b32 v10, v45, 4, v53\n\t"
    "3:\n\t"
    "v_mov_b32 v12, 0\n\t"
    "v_mov_b32 v13, 0\n\t"
    "v_mov_b32 v14, 0\n\t"
    "v_mov_b32 v15, 0\n\t"
    "v_mov_b32 v16, 0\n\t"
    "v_mov_b32 v17, 0\n\t"
    "v_mov_b32 v18, 0\n\t"
    "v_mov_b32 v19, 0\n\t"
    "s_waitcnt vmcnt(0) lgkmcnt(0)\n\t"
    "14:\n\t"
    "ds_write_b128 v6, v[36:39] offset:14336\n\t"
    "ds_write_b128 v6, v[40:43] offset:15360\n\t"
    "v_smfmac_f32_16x16x64_bf16 v[12:15], v[36:39], v[224:231], v9\n\t"
    "v_max3_u32 v52, v36, v37, v38\n\t"
    "v_max3_u32 v53, v39, v40, v41\n\t"
    "v_smfmac_f32_16x16x64_bf16 v[16:19], v[36:39], v[232:239], v9\n\t"
    "v_max3_u32 v52, v52, v42, v43\n\t"
    "v_max_u32 v52, v52, v53\n\t"
    "v_cmp_eq_u32 vcc, -1, v52\n\t"
    "s_cbranch_vccnz 1f\n\t"
    "2:\n\t"
    "s_waitcnt lgkmcnt(0)\n\t"
    "s_barrier\n\t"
    "ds_read_b128 v[36:39], v6 offset:0\n\t"
    "ds_read_b128 v[44:47], v6 offset:4096\n\t"
    "v_smfmac_f32_16x16x64_bf16 v[12:15], v[40:43], v[240:247], v9\n\t"
    "ds_read_b128 v[48:51], v6 offset:5120\n\t"
    "ds_read_b128 v[52:55], v6 offset:6144\n\t"
    "v_smfmac_f32_16x16x64_bf16 v[16:19], v[40:43], v[248:255], v9\n\t"
    "ds_read_b128 v[40:43], v6 offset:1024\n\t"
    "s_sleep 0\n\t"
    "s_waitcnt lgkmcnt(4)\n\t"
    "v_smfmac_f32_16x16x64_bf16 v[12:15], v[36:39], v[56:63], v9\n\t"
    "v_smfmac_f32_16x16x64_bf16 v[16:19], v[36:39], v[28:35], v9\n\t"
    "ds_read_b128 v[28:31], v7 offset:16384\n\t"
    "ds_read_b128 v[32:35], v7 offset:17408\n\t"
    "ds_read_b128 v[36:39], v6 offset:7168\n\t"
    "s_waitcnt lgkmcnt(6)\n\t"
    "v_smfmac_f32_16x16x64_bf16 v[12:15], v[44:47], v[64:71], v9\n\t"
    "v_smfmac_f32_16x16x64_bf16 v[16:19], v[44:47], v[72:79], v9\n\t"
    "ds_read_b128 v[44:47], v6 offset:8192\n\t"
    "s_waitcnt lgkmcnt(6)\n\t"
    "v_smfmac_f32_16x16x64_bf16 v[12:15], v[48:51], v[80:87], v9\n\t"
    "v_smfmac_f32_16x16x64_bf16 v[16:19], v[48:51], v[88:95], v9\n\t"
    "ds_read_b128 v[48:51], v6 offset:9216\n\t"
    "s_waitcnt lgkmcnt(6)\n\t"
    "v_smfmac_f32_16x16x64_bf16 v[12:15], v[52:55], v[96:103], v9\n\t"
    "v_smfmac_f32_16x16x64_bf16 v[16:19], v[52:55], v[104:111], v9\n\t"
    "ds_read_b128 v[52:55], v6 offset:2048\n\t"
    "s_waitcnt lgkmcnt(6)\n\t"
    "v_smfmac_f32_16x16x64_bf16 v[12:15], v[40:43], v[20:27], v9\n\t"
    "ds_read_b128 v[20:23], v8 offset:0\n\t"
    "ds_read_b128 v[24:27], v8 offset:1024\n\t"
    "s_waitcnt lgkmcnt(6)\n\t"
    "v_smfmac_f32_16x16x64_bf16 v[16:19], v[40:43], v[28:35], v9\n\t"
    "ds_read_b128 v[28:31], v8 offset:8192\n\t"
    "ds_read_b128 v[32:35], v8 offset:9216\n\t"
    "ds_read_b128 v[40:43], v6 offset:10240\n\t"
    "s_waitcnt lgkmcnt(8)\n\t"
    "v_smfmac_f32_16x16x64_bf16 v[12:15], v[36:39], v[112:119], v9\n\t"
    "v_smfmac_f32_16x16x64_bf16 v[16:19], v[36:39], v[120:127], v9\n\t"
    "ds_read_b128 v[36:39], v6 offset:11264\n\t"
    "s_waitcnt lgkmcnt(8)\n\t"
    "v_smfmac_f32_16x16x64_bf16 v[12:15], v[44:47], v[128:135], v9\n\t"
    "v_smfmac_f32_16x16x64_bf16 v[16:19], v[44:47], v[136:143], v9\n\t"
    "ds_read_b128 v[44:47], v6 offset:12288\n\t"
    "s_barrier\n\t"
    "s_setprio 2\n\t"
    "s_waitcnt lgkmcnt(8)\n\t"
    "v_smfmac_f32_16x16x64_bf16 v[12:15], v[48:51], v[144:151], v9\n\t"
    "v_smfmac_f32_16x16x64_bf16 v[16:19], v[48:51], v[152:159], v9\n\t"
    "ds_read_b128 v[48:51], v6 offset:3072\n\t"
    "s_waitcnt lgkmcnt(6)\n\t"
    "v_smfmac_f32_16x16x64_bf16 v[12:15], v[52:55], v[20:27], v9\n\t"
    "ds_read_b128 v[20:23], v8 offset:16384\n\t"
    "ds_read_b128 v[24:27], v8 offset:17408\n\t"
    "s_waitcnt lgkmcnt(6)\n\t"
    "v_smfmac_f32_16x16x64_bf16 v[16:19], v[52:55], v[28:35], v9\n\t"
    "ds_read_b128 v[28:31], v8 offset:24576\n\t"
    "ds_read_b128 v[32:35], v8 offset:25600\n\t"
    "ds_read_b128 v[52:55], v6 offset:13312\n\t"
    "s_waitcnt lgkmcnt(8)\n\t"
    "v_smfmac_f32_16x16x64_bf16 v[12:15], v[40:43], v[160:167], v9\n\t"
    "v_smfmac_f32_16x16x64_bf16 v[16:19], v[40:43], v[168:175], v9\n\t"
    "s_waitcnt lgkmcnt(7)\n\t"
    "v_smfmac_f32_16x16x64_bf16 v[12:15], v[36:39], v[176:183], v9\n\t"
    "v_smfmac_f32_16x16x64_bf16 v[16:19], v[36:39], v[184:191], v9\n\t"
    "s_waitcnt lgkmcnt(6)\n\t"
    "v_smfmac_f32_16x16x64_bf16 v[12:15], v[44:47], v[192:199], v9\n\t"
    "v_smfmac_f32_16x16x64_bf16 v[16:19], v[44:47], v[200:207], v9\n\t"
    "s_waitcnt lgkmcnt(3)\n\t"
    "v_smfmac_f32_16x16x64_bf16 v[12:15], v[48:51], v[20:27], v9\n\t"
    "s_waitcnt lgkmcnt(1)\n\t"
    "v_smfmac_f32_16x16x64_bf16 v[16:19], v[48:51], v[28:35], v9\n\t"
    "s_waitcnt lgkmcnt(0)\n\t"
    "v_smfmac_f32_16x16x64_bf16 v[12:15], v[52:55], v[208:215], v9\n\t"
    "v_smfmac_f32_16x16x64_bf16 v[16:19], v[52:55], v[216:223], v9\n\t"
    "s_nop 6\n\t"
    "v_permlane16_swap_b32 v12, v14\n\t"
    "v_permlane16_swap_b32 v13, v15\n\t"
    "v_permlane16_swap_b32 v16, v18\n\t"
    "v_permlane16_swap_b32 v17, v19\n\t"
    "v_add_f32 v12, v12, v14\n\t"
    "v_add_f32 v13, v13, v15\n\t"
    "v_add_f32 v16, v16, v18\n\t"
    "v_cvt_pk_bf16_f32 v12, v12, v13\n\t"
    "v_add_f32 v17, v17, v19\n\t"
    "ds_write_b32 v10, v12 offset:36352\n\t"
    "v_cvt_pk_bf16_f32 v16, v16, v17\n\t"
    "ds_write_b32 v10, v16 offset:36608\n\t"
    "s_waitcnt lgkmcnt(0)\n\t"
    "s_setprio 0\n\t"
    "s_barrier\n\t"
    "s_barrier\n\t"
    "s_waitcnt lgkmcnt(0)\n\t"
    "s_branch 9f\n\t"
    "1:\n\t"
    "s_add_u32 %[sp], %[sp], 1\n\t"
    "s_cmp_ge_u32 %[sp], %[cap]\n\t"
    "s_cbranch_scc1 7f\n\t"
    "global_load_dwordx4 v[36:39], v6, %[ag] sc1\n\t"
    "global_load_dwordx4 v[40:43], v6, %[ag] offset:1024 sc1\n\t"
    "s_waitcnt lgkmcnt(0)\n\t"
    "s_memtime %[tc]\n\t"
    "s_nop 7\n\t"
    "s_branch 3b\n\t"
    "7:\n\t"
    "s_mov_b32 %[cap], 0\n\t"
    "s_branch 2b\n\t"
    "9:"
    : [tc] "=&s"(tc), [sp] "=&s"(sp), [cap] "+s"(cap)
    : [ag] "s"(ag), [c44] "s"(0x4444u), "{v[64:95]}"(W.w0), "{v[96:127]}"(W.w1), "{v[128:159]}"(W.w2), "{v[160:191]}"(W.w3), "{v[192:223]}"(W.w4), "{v[224:255]}"(W.w5), "{v[56:63]}"(W.x)
    : "v6", "v7", "v8", "v9", "v10", "v11", "v12", "v13", "v14", "v15", "v16", "v17", "v18", "v19", "v20", "v21", "v22", "v23", "v24", "v25", "v26", "v27", "v28", "v29", "v30", "v31", "v32", "v33", "v34", "v35", "v36", "v37", "v38", "v39", "v40", "v41", "v42", "v43", "v44", "v45", "v46", "v47", "v48", "v49", "v50", "v51", "v52", "v53", "v54", "v55", "vcc", "scc", "memory");
}
template <int I> __device__ __forceinline__ void qk_wload(u32x32& w, unsigned a0, unsigned a1);
template <> __device__ __forceinline__ void qk_wload<0>(u32x32& w, unsigned a0, unsigned a1) {
  asm volatile("ds_read_b64_tr_b16 v[64:65], %1 offset:0\n\t"
    "ds_read_b64_tr_b16 v[66:67], %1 offset:512\n\t"
    "ds_read_b64_tr_b16 v[68:69], %1 offset:4096\n\t"
    "ds_read_b64_tr_b16 v[70:71], %1 offset:4608\n\t"
    "ds_read_b64_tr_b16 v[72:73], %2 offset:0\n\t"
    "ds_read_b64_tr_b16 v[74:75], %2 offset:512\n\t"
    "ds_read_b64_tr_b16 v[76:77], %2 offset:4096\n\t"
    "ds_read_b64_tr_b16 v[78:79], %2 offset:4608\n\t"
    "s_waitcnt lgkmcnt(0)\n\t"
    "ds_read_b64_tr_b16 v[80:81], %1 offset:16384\n\t"
    "ds_read_b64_tr_b16 v[82:83], %1 offset:16896\n\t"
    "ds_read_b64_tr_b16 v[84:85], %1 offset:20480\n\t"
    "ds_read_b64_tr_b16 v[86:87], %1 offset:20992\n\t"
    "ds_read_b64_tr_b16 v[88:89], %2 offset:16384\n\t"
    "ds_read_b64_tr_b16 v[90:91], %2 offset:16896\n\t"
    "ds_read_b64_tr_b16 v[92:93], %2 offset:20480\n\t"
    "ds_read_b64_tr_b16 v[94:95], %2 offset:20992\n\t"
    "s_waitcnt lgkmcnt(0)"
    : "=&{v[64:95]}"(w) : "v"(a0), "v"(a1) : "memory");
}
template <> __device__ __forceinline__ void qk_wload<1>(u32x32& w, unsigned a0, unsigned a1) {
  asm volatile("ds_read_b64_tr_b16 v[96:97], %1 offset:0\n\t"
    "ds_read_b64_tr_b16 v[98:99], %1 offset:512\n\t"
    "ds_read_b64_tr_b16 v[100:101], %1 offset:4096\n\t"
    "ds_read_b64_tr_b16 v[102:103], %1 offset:4608\n\t"
    "ds_read_b64_tr_b16 v[104:105], %2 offset:0\n\t"
    "ds_read_b64_tr_b16 v[106:107], %2 offset:512\n\t"
    "ds_read_b64_tr_b16 v[108:109], %2 offset:4096\n\t"
    "ds_read_b64_tr_b16 v[110:111], %2 offset:4608\n\t"
    "s_waitcnt lgkmcnt(0)\n\t"
    "ds_read_b64_tr_b16 v[112:113], %1 offset:16384\n\t"
    "ds_read_b64_tr_b16 v[114:115], %1 offset:16896\n\t"
    "ds_read_b64_tr_b16 v[116:117], %1 offset:20480\n\t"
    "ds_read_b64_tr_b16 v[118:119], %1 offset:20992\n\t"
    "ds_read_b64_tr_b16 v[120:121], %2 offset:16384\n\t"
    "ds_read_b64_tr_b16 v[122:123], %2 offset:16896\n\t"
    "ds_read_b64_tr_b16 v[124:125], %2 offset:20480\n\t"
    "ds_read_b64_tr_b16 v[126:127], %2 offset:20992\n\t"
    "s_waitcnt lgkmcnt(0)"
    : "=&{v[96:127]}"(w) : "v"(a0), "v"(a1) : "memory");
}
template <> __device__ __forceinline__ void qk_wload<2>(u32x32& w, unsigned a0, unsigned a1) {
  asm volatile("ds_read_b64_tr_b16 v[128:129], %1 offset:0\n\t"
    "ds_read_b64_tr_b16 v[130:131], %1 offset:512\n\t"
    "ds_read_b64_tr_b16 v[132:133], %1 offset:4096\n\t"
    "ds_read_b64_tr_b16 v[134:135], %1 offset:4608\n\t"
    "ds_read_b64_tr_b16 v[136:137], %2 offset:0\n\t"
    "ds_read_b64_tr_b16 v[138:139], %2 offset:512\n\t"
    "ds_read_b64_tr_b16 v[140:141], %2 offset:4096\n\t"
    "ds_read_b64_tr_b16 v[142:143], %2 offset:4608\n\t"
    "s_waitcnt lgkmcnt(0)\n\t"
    "ds_read_b64_tr_b16 v[144:145], %1 offset:16384\n\t"
    "ds_read_b64_tr_b16 v[146:147], %1 offset:16896\n\t"
    "ds_read_b64_tr_b16 v[148:149], %1 offset:20480\n\t"
    "ds_read_b64_tr_b16 v[150:151], %1 offset:20992\n\t"
    "ds_read_b64_tr_b16 v[152:153], %2 offset:16384\n\t"
    "ds_read_b64_tr_b16 v[154:155], %2 offset:16896\n\t"
    "ds_read_b64_tr_b16 v[156:157], %2 offset:20480\n\t"
    "ds_read_b64_tr_b16 v[158:159], %2 offset:20992\n\t"
    "s_waitcnt lgkmcnt(0)"
    : "=&{v[128:159]}"(w) : "v"(a0), "v"(a1) : "memory");
}
template <> __device__ __forceinline__ void qk_wload<3>(u32x32& w, unsigned a0, unsigned a1) {
  asm volatile("ds_read_b64_tr_b16 v[160:161], %1 offset:0\n\t"
    "ds_read_b64_tr_b16 v[162:163], %1 offset:512\n\t"
    "ds_read_b64_tr_b16 v[164:165], %1 offset:4096\n\t"
    "ds_read_b64_tr_b16 v[166:167], %1 offset:4608\n\t"
    "ds_read_b64_tr_b16 v[168:169], %2 offset:0\n\t"
    "ds_read_b64_tr_b16 v[170:171], %2 offset:512\n\t"
    "ds_read_b64_tr_b16 v[172:173], %2 offset:4096\n\t"
    "ds_read_b64_tr_b16 v[174:175], %2 offset:4608\n\t"
    "s_waitcnt lgkmcnt(0)\n\t"
    "ds_read_b64_tr_b16 v[176:177], %1 offset:16384\n\t"
    "ds_read_b64_tr_b16 v[178:179], %1 offset:16896\n\t"
    "ds_read_b64_tr_b16 v[180:181], %1 offset:20480\n\t"
    "ds_read_b64_tr_b16 v[182:183], %1 offset:20992\n\t"
    "ds_read_b64_tr_b16 v[184:185], %2 offset:16384\n\t"
    "ds_read_b64_tr_b16 v[186:187], %2 offset:16896\n\t"
    "ds_read_b64_tr_b16 v[188:189], %2 offset:20480\n\t"
    "ds_read_b64_tr_b16 v[190:191], %2 offset:20992\n\t"
    "s_waitcnt lgkmcnt(0)"
    : "=&{v[160:191]}"(w) : "v"(a0), "v"(a1) : "memory");
}
template <> __device__ __forceinline__ void qk_wload<4>(u32x32& w, unsigned a0, unsigned a1) {
  asm volatile("ds_read_b64_tr_b16 v[192:193], %1 offset:0\n\t"
    "ds_read_b64_tr_b16 v[194:195], %1 offset:512\n\t"
    "ds_read_b64_tr_b16 v[196:197], %1 offset:4096\n\t"
    "ds_read_b64_tr_b16 v[198:199], %1 offset:4608\n\t"
    "ds_read_b64_tr_b16 v[200:201], %2 offset:0\n\t"
    "ds_read_b64_tr_b16 v[202:203], %2 offset:512\n\t"
    "ds_read_b64_tr_b16 v[204:205], %2 offset:4096\n\t"
    "ds_read_b64_tr_b16 v[206:207], %2 offset:4608\n\t"
    "s_waitcnt lgkmcnt(0)\n\t"
    "ds_read_b64_tr_b16 v[208:209], %1 offset:16384\n\t"
    "ds_read_b64_tr_b16 v[210:211], %1 offset:16896\n\t"
    "ds_read_b64_tr_b16 v[212:213], %1 offset:20480\n\t"
    "ds_read_b64_tr_b16 v[214:215], %1 offset:20992\n\t"
    "ds_read_b64_tr_b16 v[216:217], %2 offset:16384\n\t"
    "ds_read_b64_tr_b16 v[218:219], %2 offset:16896\n\t"
    "ds_read_b64_tr_b16 v[220:221], %2 offset:20480\n\t"
    "ds_read_b64_tr_b16 v[222:223], %2 offset:20992\n\t"
    "s_waitcnt lgkmcnt(0)"
    : "=&{v[192:223]}"(w) : "v"(a0), "v"(a1) : "memory");
}
template <> __device__ __forceinline__ void qk_wload<5>(u32x32& w, unsigned a0, unsigned a1) {
  asm volatile("ds_read_b64_tr_b16 v[224:225], %1 offset:0\n\t"
    "ds_read_b64_tr_b16 v[226:227], %1 offset:512\n\t"
    "ds_read_b64_tr_b16 v[228:229], %1 offset:4096\n\t"
    "ds_read_b64_tr_b16 v[230:231], %1 offset:4608\n\t"
    "ds_read_b64_tr_b16 v[232:233], %2 offset:0\n\t"
    "ds_read_b64_tr_b16 v[234:235], %2 offset:512\n\t"
    "ds_read_b64_tr_b16 v[236:237], %2 offset:4096\n\t"
    "ds_read_b64_tr_b16 v[238:239], %2 offset:4608\n\t"
    "s_waitcnt lgkmcnt(0)\n\t"
    "ds_read_b64_tr_b16 v[240:241], %1 offset:16384\n\t"
    "ds_read_b64_tr_b16 v[242:243], %1 offset:16896\n\t"
    "ds_read_b64_tr_b16 v[244:245], %1 offset:20480\n\t"
    "ds_read_b64_tr_b16 v[246:247], %1 offset:20992\n\t"
    "ds_read_b64_tr_b16 v[248:249], %2 offset:16384\n\t"
    "ds_read_b64_tr_b16 v[250:251], %2 offset:16896\n\t"
    "ds_read_b64_tr_b16 v[252:253], %2 offset:20480\n\t"
    "ds_read_b64_tr_b16 v[254:255], %2 offset:20992\n\t"
    "s_waitcnt lgkmcnt(0)"
    : "=&{v[224:255]}"(w) : "v"(a0), "v"(a1) : "memory");
}
template <int I> __device__ __forceinline__ void vr_wload(u32x32& w, unsigned a);
template <> __device__ __forceinline__ void vr_wload<0>(u32x32& w, unsigned a) {
  asm volatile("ds_read_b64_tr_b16 v[64:65], %1 offset:0\n\t"
    "ds_read_b64_tr_b16 v[66:67], %1 offset:256\n\t"
    "ds_read_b64_tr_b16 v[68:69], %1 offset:2048\n\t"
    "ds_read_b64_tr_b16 v[70:71], %1 offset:2304\n\t"
    "ds_read_b64_tr_b16 v[72:73], %1 offset:32\n\t"
    "ds_read_b64_tr_b16 v[74:75], %1 offset:288\n\t"
    "ds_read_b64_tr_b16 v[76:77], %1 offset:2080\n\t"
    "ds_read_b64_tr_b16 v[78:79], %1 offset:2336\n\t"
    "s_waitcnt lgkmcnt(0)\n\t"
    "ds_read_b64_tr_b16 v[80:81], %1 offset:64\n\t"
    "ds_read_b64_tr_b16 v[82:83], %1 offset:320\n\t"
    "ds_read_b64_tr_b16 v[84:85], %1 offset:2112\n\t"
    "ds_read_b64_tr_b16 v[86:87], %1 offset:2368\n\t"
    "ds_read_b64_tr_b16 v[88:89], %1 offset:96\n\t"
    "ds_read_b64_tr_b16 v[90:91], %1 offset:352\n\t"
    "ds_read_b64_tr_b16 v[92:93], %1 offset:2144\n\t"
    "ds_read_b64_tr_b16 v[94:95], %1 offset:2400\n\t"
    "s_waitcnt lgkmcnt(0)"
    : "=&{v[64:95]}"(w) : "v"(a) : "memory");
}
template <> __device__ __forceinline__ void vr_wload<1>(u32x32& w, unsigned a) {
  asm volatile("ds_read_b64_tr_b16 v[96:97], %1 offset:8192\n\t"
    "ds_read_b64_tr_b16 v[98:99], %1 offset:8448\n\t"
    "ds_read_b64_tr_b16 v[100:101], %1 offset:10240\n\t"
    "ds_read_b64_tr_b16 v[102:103], %1 offset:10496\n\t"
    "ds_read_b64_tr_b16 v[104:105], %1 offset:8224\n\t"
    "ds_read_b64_tr_b16 v[106:107], %1 offset:8480\n\t"
    "ds_read_b64_tr_b16 v[108:109], %1 offset:10272\n\t"
    "ds_read_b64_tr_b16 v[110:111], %1 offset:10528\n\t"
    "s_waitcnt lgkmcnt(0)\n\t"
    "ds_read_b64_tr_b16 v[112:113], %1 offset:8256\n\t"
    "ds_read_b64_tr_b16 v[114:115], %1 offset:8512\n\t"
    "ds_read_b64_tr_b16 v[116:117], %1 offset:10304\n\t"
    "ds_read_b64_tr_b16 v[118:119], %1 offset:10560\n\t"
    "ds_read_b64_tr_b16 v[120:121], %1 offset:8288\n\t"
    "ds_read_b64_tr_b16 v[122:123], %1 offset:8544\n\t"
    "ds_read_b64_tr_b16 v[124:125], %1 offset:10336\n\t"
    "ds_read_b64_tr_b16 v[126:127], %1 offset:10592\n\t"
    "s_waitcnt lgkmcnt(0)"
    : "=&{v[96:127]}"(w) : "v"(a) : "memory");
}
template <> __device__ __forceinline__ void vr_wload<2>(u32x32& w, unsigned a) {
  asm volatile("ds_read_b64_tr_b16 v[128:129], %1 offset:0\n\t"
    "ds_read_b64_tr_b16 v[130:131], %1 offset:128\n\t"
    "ds_read_b64_tr_b16 v[132:133], %1 offset:1024\n\t"
    "ds_read_b64_tr_b16 v[134:135], %1 offset:1152\n\t"
    "ds_read_b64_tr_b16 v[136:137], %1 offset:32\n\t"
    "ds_read_b64_tr_b16 v[138:139], %1 offset:160\n\t"
    "ds_read_b64_tr_b16 v[140:141], %1 offset:1056\n\t"
    "ds_read_b64_tr_b16 v[142:143], %1 offset:1184\n\t"
    "s_waitcnt lgkmcnt(0)\n\t"
    "ds_read_b64_tr_b16 v[144:145], %1 offset:4096\n\t"
    "ds_read_b64_tr_b16 v[146:147], %1 offset:4224\n\t"
    "ds_read_b64_tr_b16 v[148:149], %1 offset:5120\n\t"
    "ds_read_b64_tr_b16 v[150:151], %1 offset:5248\n\t"
    "ds_read_b64_tr_b16 v[152:153], %1 offset:4128\n\t"
    "ds_read_b64_tr_b16 v[154:155], %1 offset:4256\n\t"
    "ds_read_b64_tr_b16 v[156:157], %1 offset:5152\n\t"
    "ds_read_b64_tr_b16 v[158:159], %1 offset:5280\n\t"
    "s_waitcnt lgkmcnt(0)"
    : "=&{v[128:159]}"(w) : "v"(a) : "memory");
}
template <> __device__ __forceinline__ void vr_wload<3>(u32x32& w, unsigned a) {
  asm volatile("ds_read_b64_tr_b16 v[160:161], %1 offset:8192\n\t"
    "ds_read_b64_tr_b16 v[162:163], %1 offset:8320\n\t"
    "ds_read_b64_tr_b16 v[164:165], %1 offset:9216\n\t"
    "ds_read_b64_tr_b16 v[166:167], %1 offset:9344\n\t"
    "ds_read_b64_tr_b16 v[168:169], %1 offset:8224\n\t"
    "ds_read_b64_tr_b16 v[170:171], %1 offset:8352\n\t"
    "ds_read_b64_tr_b16 v[172:173], %1 offset:9248\n\t"
    "ds_read_b64_tr_b16 v[174:175], %1 offset:9376\n\t"
    "s_waitcnt lgkmcnt(0)\n\t"
    "ds_read_b64_tr_b16 v[176:177], %1 offset:12288\n\t"
    "ds_read_b64_tr_b16 v[178:179], %1 offset:12416\n\t"
    "ds_read_b64_tr_b16 v[180:181], %1 offset:13312\n\t"
    "ds_read_b64_tr_b16 v[182:183], %1 offset:13440\n\t"
    "ds_read_b64_tr_b16 v[184:185], %1 offset:12320\n\t"
    "ds_read_b64_tr_b16 v[186:187], %1 offset:12448\n\t"
    "ds_read_b64_tr_b16 v[188:189], %1 offset:13344\n\t"
    "ds_read_b64_tr_b16 v[190:191], %1 offset:13472\n\t"
    "s_waitcnt lgkmcnt(0)"
    : "=&{v[160:191]}"(w) : "v"(a) : "memory");
}
template <> __device__ __forceinline__ void vr_wload<4>(u32x32& w, unsigned a) {
  asm volatile("ds_read_b64_tr_b16 v[192:193], %1 offset:0\n\t"
    "ds_read_b64_tr_b16 v[194:195], %1 offset:128\n\t"
    "ds_read_b64_tr_b16 v[196:197], %1 offset:1024\n\t"
    "ds_read_b64_tr_b16 v[198:199], %1 offset:1152\n\t"
    "ds_read_b64_tr_b16 v[200:201], %1 offset:32\n\t"
    "ds_read_b64_tr_b16 v[202:203], %1 offset:160\n\t"
    "ds_read_b64_tr_b16 v[204:205], %1 offset:1056\n\t"
    "ds_read_b64_tr_b16 v[206:207], %1 offset:1184\n\t"
    "s_waitcnt lgkmcnt(0)\n\t"
    "ds_read_b64_tr_b16 v[208:209], %1 offset:4096\n\t"
    "ds_read_b64_tr_b16 v[210:211], %1 offset:4224\n\t"
    "ds_read_b64_tr_b16 v[212:213], %1 offset:5120\n\t"
    "ds_read_b64_tr_b16 v[214:215], %1 offset:5248\n\t"
    "ds_read_b64_tr_b16 v[216:217], %1 offset:4128\n\t"
    "ds_read_b64_tr_b16 v[218:219], %1 offset:4256\n\t"
    "ds_read_b64_tr_b16 v[220:221], %1 offset:5152\n\t"
    "ds_read_b64_tr_b16 v[222:223], %1 offset:5280\n\t"
    "s_waitcnt lgkmcnt(0)"
    : "=&{v[192:223]}"(w) : "v"(a) : "memory");
}
template <> __device__ __forceinline__ void vr_wload<5>(u32x32& w, unsigned a) {
  asm volatile("ds_read_b64_tr_b16 v[224:225], %1 offset:8192\n\t"
    "ds_read_b64_tr_b16 v[226:227], %1 offset:8320\n\t"
    "ds_read_b64_tr_b16 v[228:229], %1 offset:9216\n\t"
    "ds_read_b64_tr_b16 v[230:231], %1 offset:9344\n\t"
    "ds_read_b64_tr_b16 v[232:233], %1 offset:8224\n\t"
    "ds_read_b64_tr_b16 v[234:235], %1 offset:8352\n\t"
    "ds_read_b64_tr_b16 v[236:237], %1 offset:9248\n\t"
    "ds_read_b64_tr_b16 v[238:239], %1 offset:9376\n\t"
    "s_waitcnt lgkmcnt(0)\n\t"
    "ds_read_b64_tr_b16 v[240:241], %1 offset:12288\n\t"
    "ds_read_b64_tr_b16 v[242:243], %1 offset:12416\n\t"
    "ds_read_b64_tr_b16 v[244:245], %1 offset:13312\n\t"
    "ds_read_b64_tr_b16 v[246:247], %1 offset:13440\n\t"
    "ds_read_b64_tr_b16 v[248:249], %1 offset:12320\n\t"
    "ds_read_b64_tr_b16 v[250:251], %1 offset:12448\n\t"
    "ds_read_b64_tr_b16 v[252:253], %1 offset:13344\n\t"
    "ds_read_b64_tr_b16 v[254:255], %1 offset:13472\n\t"
    "s_waitcnt lgkmcnt(0)"
    : "=&{v[224:255]}"(w) : "v"(a) : "memory");
}
#define OREP 2
#define AREP 2
struct VRW { u32x32 v0, v1, o0, o1, o2, o3; };
__device__ __forceinline__ void vr_v(const VRW& W, const char* ag, const char* vd, unsigned wa, unsigned ra, unsigned& cap, unsigned& sp, unsigned long long& tc, unsigned long long* ts) {
  asm volatile("v_mbcnt_lo_u32_b32 v16, -1, 0\n\t"
    "v_mbcnt_hi_u32_b32 v16, -1, v16\n\t"
    "v_lshlrev_b32 v17, 4, v16\n\t"
    "s_mov_b32 %[sp], 0\n\t"
    "global_load_dwordx4 v[24:27], v17, %[ag] sc1\n\t"
    "global_load_dwordx4 v[28:31], v17, %[ag] offset:1024 sc1\n\t"
    "s_memtime %[tc]\n\t"
    "v_and_b32 v22, 4, v16\n\t"
    "v_mul_u32_u24 v22, 0x2aaa, v22\n\t"
    "v_lshrrev_b32 v18, 1, v16\n\t"
    "v_and_b32 v18, 2, v18\n\t"
    "v_or3_b32 v18, v18, v22, %[c44]\n\t"
    "v_lshl_add_u32 v19, v16, 3, %[wa]\n\t"
    "v_lshl_add_u32 v20, v16, 3, %[ra]\n\t"
    "v_and_b32 v22, 15, v16\n\t"
    "v_lshlrev_b32 v21, 4, v22\n\t"
    "v_lshrrev_b32 v22, 2, v16\n\t"
    "v_and_or_b32 v21, v22, 12, v21\n\t"
    "3:\n\t"
    "v_mov_b32 v32, 0\n\t"
    "v_mov_b32 v33, 0\n\t"
    "v_mov_b32 v34, 0\n\t"
    "v_mov_b32 v35, 0\n\t"
    "v_mov_b32 v36, 0\n\t"
    "v_mov_b32 v37, 0\n\t"
    "v_mov_b32 v38, 0\n\t"
    "v_mov_b32 v39, 0\n\t"
    "v_mov_b32 v40, 0\n\t"
    "v_mov_b32 v41, 0\n\t"
    "v_mov_b32 v42, 0\n\t"
    "v_mov_b32 v43, 0\n\t"
    "v_mov_b32 v44, 0\n\t"
    "v_mov_b32 v45, 0\n\t"
    "v_mov_b32 v46, 0\n\t"
    "v_mov_b32 v47, 0\n\t"
    "s_waitcnt vmcnt(0) lgkmcnt(0)\n\t"
    "v_smfmac_f32_16x16x64_bf16 v[32:35], v[24:27], v[64:71], v18\n\t"
    "v_smfmac_f32_16x16x64_bf16 v[32:35], v[28:31], v[96:103], v18\n\t"
    "v_max3_u32 v22, v24, v25, v26\n\t"
    "v_max3_u32 v23, v27, v28, v29\n\t"
    "v_smfmac_f32_16x16x64_bf16 v[36:39], v[24:27], v[72:79], v18\n\t"
    "v_max3_u32 v22, v22, v30, v31\n\t"
    "v_max_u32 v22, v22, v23\n\t"
    "v_smfmac_f32_16x16x64_bf16 v[36:39], v[28:31], v[104:111], v18\n\t"
    "v_cmp_eq_u32 vcc, -1, v22\n\t"
    "s_cbranch_vccnz 1f\n\t"
    "2:\n\t"
    "v_smfmac_f32_16x16x64_bf16 v[40:43], v[24:27], v[80:87], v18\n\t"
    "v_permlane16_swap_b32 v32, v34\n\t"
    "v_permlane16_swap_b32 v33, v35\n\t"
    "v_smfmac_f32_16x16x64_bf16 v[40:43], v[28:31], v[112:119], v18\n\t"
    "v_pk_add_f32 v[32:33], v[32:33], v[34:35]\n\t"
    "ds_write_b64 v19, v[32:33] offset:0\n\t"
    "v_smfmac_f32_16x16x64_bf16 v[44:47], v[24:27], v[88:95], v18\n\t"
    "s_nop 0\n\t"
    "v_permlane16_swap_b32 v36, v38\n\t"
    "v_permlane16_swap_b32 v37, v39\n\t"
    "v_smfmac_f32_16x16x64_bf16 v[44:47], v[28:31], v[120:127], v18\n\t"
    "v_pk_add_f32 v[36:37], v[36:37], v[38:39]\n\t"
    "ds_write_b64 v19, v[36:37] offset:512\n\t"
    "v_permlane16_swap_b32 v40, v42\n\t"
    "v_permlane16_swap_b32 v41, v43\n\t"
    "v_pk_add_f32 v[40:41], v[40:41], v[42:43]\n\t"
    "ds_write_b64 v19, v[40:41] offset:1024\n\t"
    "s_nop 1\n\t"
    "v_permlane16_swap_b32 v44, v46\n\t"
    "v_permlane16_swap_b32 v45, v47\n\t"
    "v_pk_add_f32 v[44:45], v[44:45], v[46:47]\n\t"
    "ds_write_b64 v19, v[44:45] offset:1536\n\t"
    "s_cmp_ge_u32 %[wa], 8192\n\t"
    "s_waitcnt lgkmcnt(0)\n\t"
    "s_barrier\n\t"
    "s_cbranch_scc1 9f\n\t"
    "ds_read_b64 v[48:49], v20 offset:0\n\t"
    "ds_read_b64 v[50:51], v20 offset:2048\n\t"
    "ds_read_b64 v[52:53], v20 offset:4096\n\t"
    "ds_read_b64 v[54:55], v20 offset:6144\n\t"
    "ds_read_b64 v[56:57], v20 offset:8192\n\t"
    "ds_read_b64 v[58:59], v20 offset:10240\n\t"
    "ds_read_b64 v[60:61], v20 offset:12288\n\t"
    "ds_read_b64 v[62:63], v20 offset:14336\n\t"
    "s_waitcnt lgkmcnt(6)\n\t"
    "v_pk_add_f32 v[48:49], v[48:49], v[50:51]\n\t"
    "s_waitcnt lgkmcnt(4)\n\t"
    "v_pk_add_f32 v[52:53], v[52:53], v[54:55]\n\t"
    "v_pk_add_f32 v[48:49], v[48:49], v[52:53]\n\t"
    "s_waitcnt lgkmcnt(2)\n\t"
    "v_pk_add_f32 v[56:57], v[56:57], v[58:59]\n\t"
    "s_waitcnt lgkmcnt(0)\n\t"
    "v_pk_add_f32 v[60:61], v[60:61], v[62:63]\n\t"
    "v_pk_add_f32 v[56:57], v[56:57], v[60:61]\n\t"
    "v_pk_add_f32 v[48:49], v[48:49], v[56:57]\n\t"
    "v_cvt_pk_bf16_f32 v22, v48, v49\n\t"
    "v_min_u32 v22, -2, v22\n\t"
    "global_store_dword v21, v22, %[vd] sc1\n\t"
    "s_waitcnt lgkmcnt(0)\n\t"
    "s_branch 9f\n\t"
    "1:\n\t"
    "s_add_u32 %[sp], %[sp], 1\n\t"
    "s_cmp_ge_u32 %[sp], %[cap]\n\t"
    "s_cbranch_scc1 7f\n\t"
    "global_load_dwordx4 v[24:27], v17, %[ag] sc1\n\t"
    "global_load_dwordx4 v[28:31], v17, %[ag] offset:1024 sc1\n\t"
    "s_waitcnt lgkmcnt(0)\n\t"
    "s_memtime %[tc]\n\t"
    "s_nop 7\n\t"
    "s_branch 3b\n\t"
    "7:\n\t"
    "s_mov_b32 %[cap], 0\n\t"
    "s_branch 2b\n\t"
    "9:"
    : [tc] "=&s"(tc), [sp] "=&s"(sp), [cap] "+s"(cap)
    : [ag] "s"(ag), [vd] "s"(vd), [wa] "s"(wa), [ra] "s"(ra), [c44] "s"(0x4444u), "{v[64:95]}"(W.v0), "{v[96:127]}"(W.v1), "{v[128:159]}"(W.o0), "{v[160:191]}"(W.o1), "{v[192:223]}"(W.o2), "{v[224:255]}"(W.o3)
    : "v16", "v17", "v18", "v19", "v20", "v21", "v22", "v23", "v24", "v25", "v26", "v27", "v28", "v29", "v30", "v31", "v32", "v33", "v34", "v35", "v36", "v37", "v38", "v39", "v40", "v41", "v42", "v43", "v44", "v45", "v46", "v47", "v48", "v49", "v50", "v51", "v52", "v53", "v54", "v55", "v56", "v57", "v58", "v59", "v60", "v61", "v62", "v63", "vcc", "scc", "memory");
}
__device__ __forceinline__ void vr_r(const VRW& W, const char* os, const char* ad, unsigned wa, unsigned& cap, unsigned& sp, unsigned long long* ts) {
  asm volatile("v_mbcnt_lo_u32_b32 v16, -1, 0\n\t"
    "v_mbcnt_hi_u32_b32 v16, -1, v16\n\t"
    "v_lshlrev_b32 v17, 4, v16\n\t"
    "v_add_u32 v18, 0x1000, v17\n\t"
    "global_load_dwordx4 v[24:27], v17, %[os] offset:0 sc1\n\t"
    "global_load_dwordx4 v[28:31], v17, %[os] offset:2048 sc1\n\t"
    "global_load_dwordx4 v[32:35], v18, %[os] offset:0 sc1\n\t"
    "global_load_dwordx4 v[36:39], v18, %[os] offset:2048 sc1\n\t"
    "s_mov_b32 %[sp], 0\n\t"
    "v_and_b32 v23, 4, v16\n\t"
    "v_mul_u32_u24 v23, 0x2aaa, v23\n\t"
    "v_lshrrev_b32 v19, 1, v16\n\t"
    "v_and_b32 v19, 2, v19\n\t"
    "v_or3_b32 v19, v19, v23, %[c44]\n\t"
    "v_and_b32 v23, 15, v16\n\t"
    "v_lshl_add_u32 v20, v23, 2, %[wa]\n\t"
    "v_and_b32 v23, 48, v16\n\t"
    "v_lshl_add_u32 v20, v23, 3, v20\n\t"
    "v_and_b32 v23, 32, v16\n\t"
    "v_and_b32 v21, 28, v16\n\t"
    "v_lshlrev_b32 v21, 4, v21\n\t"
    "v_or_b32 v21, v21, v23\n\t"
    "v_lshlrev_b32 v22, 3, v23\n\t"
    "v_and_b32 v23, 3, v16\n\t"
    "v_lshl_or_b32 v21, v23, 3, v21\n\t"
    "v_and_b32 v23, 31, v16\n\t"
    "v_lshl_or_b32 v22, v23, 2, v22\n\t"
    "3:\n\t"
    "v_mov_b32 v40, 0\n\t"
    "v_mov_b32 v41, 0\n\t"
    "v_mov_b32 v42, 0\n\t"
    "v_mov_b32 v43, 0\n\t"
    "v_mov_b32 v44, 0\n\t"
    "v_mov_b32 v45, 0\n\t"
    "v_mov_b32 v46, 0\n\t"
    "v_mov_b32 v47, 0\n\t"
    "v_mov_b32 v48, 0\n\t"
    "v_mov_b32 v49, 0\n\t"
    "v_mov_b32 v50, 0\n\t"
    "v_mov_b32 v51, 0\n\t"
    "v_mov_b32 v52, 0\n\t"
    "v_mov_b32 v53, 0\n\t"
    "v_mov_b32 v54, 0\n\t"
    "v_mov_b32 v55, 0\n\t"
    "v_mov_b32 v56, 0\n\t"
    "s_waitcnt vmcnt(3)\n\t"
    "v_smfmac_f32_16x16x64_bf16 v[44:47], v[24:27], v[144:151], v19\n\t"
    "v_max3_u32 v56, v56, v24, v25\n\t"
    "v_smfmac_f32_16x16x64_bf16 v[48:51], v[24:27], v[136:143], v19\n\t"
    "v_max3_u32 v56, v56, v26, v27\n\t"
    "v_smfmac_f32_16x16x64_bf16 v[40:43], v[24:27], v[128:135], v19\n\t"
    "v_smfmac_f32_16x16x64_bf16 v[52:55], v[24:27], v[152:159], v19\n\t"
    "s_waitcnt vmcnt(2)\n\t"
    "v_smfmac_f32_16x16x64_bf16 v[44:47], v[28:31], v[176:183], v19\n\t"
    "v_max3_u32 v56, v56, v28, v29\n\t"
    "v_smfmac_f32_16x16x64_bf16 v[48:51], v[28:31], v[168:175], v19\n\t"
    "v_max3_u32 v56, v56, v30, v31\n\t"
    "v_smfmac_f32_16x16x64_bf16 v[40:43], v[28:31], v[160:167], v19\n\t"
    "v_smfmac_f32_16x16x64_bf16 v[52:55], v[28:31], v[184:191], v19\n\t"
    "s_waitcnt vmcnt(1)\n\t"
    "v_smfmac_f32_16x16x64_bf16 v[44:47], v[32:35], v[208:215], v19\n\t"
    "v_max3_u32 v56, v56, v32, v33\n\t"
    "v_smfmac_f32_16x16x64_bf16 v[48:51], v[32:35], v[200:207], v19\n\t"
    "v_max3_u32 v56, v56, v34, v35\n\t"
    "v_smfmac_f32_16x16x64_bf16 v[40:43], v[32:35], v[192:199], v19\n\t"
    "v_smfmac_f32_16x16x64_bf16 v[52:55], v[32:35], v[216:223], v19\n\t"
    "s_waitcnt vmcnt(0)\n\t"
    "v_smfmac_f32_16x16x64_bf16 v[44:47], v[36:39], v[240:247], v19\n\t"
    "v_max3_u32 v56, v56, v36, v37\n\t"
    "v_smfmac_f32_16x16x64_bf16 v[48:51], v[36:39], v[232:239], v19\n\t"
    "v_max3_u32 v56, v56, v38, v39\n\t"
    "v_smfmac_f32_16x16x64_bf16 v[40:43], v[36:39], v[224:231], v19\n\t"
    "v_cmp_eq_u32 vcc, -1, v56\n\t"
    "v_smfmac_f32_16x16x64_bf16 v[52:55], v[36:39], v[248:255], v19\n\t"
    "s_cbranch_vccnz 4f\n\t"
    "5:\n\t"
    "s_nop 2\n\t"
    "v_permlane32_swap_b32 v44, v48\n\t"
    "v_permlane32_swap_b32 v45, v49\n\t"
    "v_permlane32_swap_b32 v46, v50\n\t"
    "v_permlane32_swap_b32 v47, v51\n\t"
    "s_mov_b32 exec_hi, 0\n\t"
    "v_pk_add_f32 v[40:41], v[40:41], v[48:49]\n\t"
    "v_pk_add_f32 v[42:43], v[42:43], v[50:51]\n\t"
    "s_mov_b64 exec, %[hi]\n\t"
    "v_pk_add_f32 v[40:41], v[44:45], v[52:53]\n\t"
    "v_pk_add_f32 v[42:43], v[46:47], v[54:55]\n\t"
    "s_mov_b32 exec_lo, -1\n\t"
    "s_nop 0\n\t"
    "v_permlane16_swap_b32 v40, v42\n\t"
    "v_permlane16_swap_b32 v41, v43\n\t"
    "v_add_f32 v40, v40, v42\n\t"
    "v_add_f32 v41, v41, v43\n\t"
    "ds_write_b32 v20, v40 offset:16384\n\t"
    "ds_write_b32 v20, v41 offset:16448\n\t"
    "s_cmp_ge_u32 %[wa], 1024\n\t"
    "s_waitcnt lgkmcnt(0)\n\t"
    "s_barrier\n\t"
    "s_cbranch_scc1 9f\n\t"
    "ds_read_b64 v[24:25], v21 offset:16384\n\t"
    "ds_read_b64 v[26:27], v21 offset:16896\n\t"
    "ds_read_b64 v[28:29], v21 offset:17408\n\t"
    "ds_read_b64 v[30:31], v21 offset:17920\n\t"
    "ds_read_b64 v[32:33], v21 offset:18432\n\t"
    "ds_read_b64 v[34:35], v21 offset:18944\n\t"
    "ds_read_b64 v[36:37], v21 offset:19456\n\t"
    "ds_read_b64 v[38:39], v21 offset:19968\n\t"
    "s_waitcnt lgkmcnt(6)\n\t"
    "v_pk_add_f32 v[24:25], v[24:25], v[26:27]\n\t"
    "s_waitcnt lgkmcnt(4)\n\t"
    "v_pk_add_f32 v[28:29], v[28:29], v[30:31]\n\t"
    "v_pk_add_f32 v[24:25], v[24:25], v[28:29]\n\t"
    "s_waitcnt lgkmcnt(2)\n\t"
    "v_pk_add_f32 v[32:33], v[32:33], v[34:35]\n\t"
    "s_waitcnt lgkmcnt(0)\n\t"
    "v_pk_add_f32 v[36:37], v[36:37], v[38:39]\n\t"
    "v_pk_add_f32 v[32:33], v[32:33], v[36:37]\n\t"
    "v_pk_add_f32 v[24:25], v[24:25], v[32:33]\n\t"
    "v_cvt_pk_bf16_f32 v23, v24, v25\n\t"
    "v_min_u32 v23, -2, v23\n\t"
    "global_store_dword v22, v23, %[ad] sc1\n\t"
    "s_waitcnt lgkmcnt(0)\n\t"
    "s_branch 9f\n\t"
    "4:\n\t"
    "s_add_u32 %[sp], %[sp], 1\n\t"
    "s_cmp_ge_u32 %[sp], %[cap]\n\t"
    "s_cbranch_scc1 7f\n\t"
    "global_load_dwordx4 v[24:27], v17, %[os] offset:0 sc1\n\t"
    "global_load_dwordx4 v[28:31], v17, %[os] offset:2048 sc1\n\t"
    "global_load_dwordx4 v[32:35], v18, %[os] offset:0 sc1\n\t"
    "global_load_dwordx4 v[36:39], v18, %[os] offset:2048 sc1\n\t"
    "s_nop 7\n\t"
    "s_branch 3b\n\t"
    "7:\n\t"
    "s_mov_b32 %[cap], 0\n\t"
    "s_branch 5b\n\t"
    "9:"
    : [sp] "=&s"(sp), [cap] "+s"(cap)
    : [os] "s"(os), [ad] "s"(ad), [wa] "s"(wa), [c44] "s"(0x4444u), [hi] "s"(0xFFFFFFFF00000000ull), "{v[64:95]}"(W.v0), "{v[96:127]}"(W.v1), "{v[128:159]}"(W.o0), "{v[160:191]}"(W.o1), "{v[192:223]}"(W.o2), "{v[224:255]}"(W.o3)
    : "v16", "v17", "v18", "v19", "v20", "v21", "v22", "v23", "v24", "v25", "v26", "v27", "v28", "v29", "v30", "v31", "v32", "v33", "v34", "v35", "v36", "v37", "v38", "v39", "v40", "v41", "v42", "v43", "v44", "v45", "v46", "v47", "v48", "v49", "v50", "v51", "v52", "v53", "v54", "v55", "v56", "v57", "v58", "v59", "v60", "v61", "v62", "v63", "vcc", "scc", "memory");
}
__device__ __forceinline__ void qk_body(char* smem, int wave_id_, int xg, int h, int wg, const u16* __restrict__ Wq, const u16* __restrict__ Wk,
                                        const char* __restrict__ Kx, const char* __restrict__ V, char* ws, unsigned* tmo, u64 tag) {
  const int wave = WAVE_ID;
  const int ty = wave >> 2, w4 = wave & 3;
  const char* wq = uni((const char*)(Wq + h * DH)); const char* wk = uni((const char*)(Wk + h * DH));
  auto stage = [&](int u) {
    stage_proj((u & 1) ? wk + (size_t)(((u >> 1) + 2) & 7) * 128 * HD * 2 : wq + (size_t)(u >> 1) * 128 * HD * 2, ((u + 4) % 5) * 32768, wave * 64 + lane_id(), wave);
  };
  QKW W;
  if (wave == 0) STS(ws, wg, 7);
  stage(0); stage(1); stage(2); stage(3);
#define WLD(i, w) do { WAIT_V(12); WBAR(); stage(2 * (i) + 4); if (ty) { WAIT_V(12); WBAR(); stage(2 * (i) + 5); } \
    { const int l_ = lane_id(); const unsigned o_ = ty ? ((2 * (i) + 5) % 5) * 32768 : ((2 * (i) + 4) % 5) * 32768; qk_wload<i>(w, proj_base(2 * w4, l_) + o_, proj_base(2 * w4 + 1, l_) + o_); } \
    if (!ty) { WAIT_V(12); WBAR(); stage(2 * (i) + 5); } if (wave == 0) STS(ws, wg, 1 + (i)); } while (0)
  WLD(0, W.w0); WLD(1, W.w1); WLD(2, W.w2); WLD(3, W.w3); WLD(4, W.w4); WLD(5, W.w5);
#undef WLD
  unsigned in = 0;
#pragma unroll
  for (int i = 0; i < 2; ++i) {
    if (i == 0) WAIT_V(8); else WAIT_V(0);
    WBAR();
    if (i == 0 && wave == 1) init_issue(ws, Q_V);
    if (i == 1) in = init_get(ws, tag, smem + Q_V);
    const int lane = lane_id();
    const unsigned o = Q_RING + ty * 32768 + i * 65536, f0 = proj_base(2 * w4, lane) + o, f1 = proj_base(2 * w4 + 1, lane) + o;
    u32x8 x[4] = {proj_frag(smem, f0), proj_frag(smem, f1), proj_frag(smem, f0 + 16384), proj_frag(smem, f1 + 16384)};
    asm volatile("" : "+v"(x[0]), "+v"(x[1]), "+v"(x[2]), "+v"(x[3]));
    BAR();
    char* d = smem + Q_CB + i * 57344 + ty * (i ? 32768 : 24576) + w4 * 2048 + lane * 16;
#pragma unroll
    for (int j = 0; j < 4; ++j)
      if (i || j || !ty) { *(u32x4*)(d + j * 8192) = u32x4{x[j][0], x[j][1], x[j][2], x[j][3]}; *(u32x4*)(d + j * 8192 + 1024) = u32x4{x[j][4], x[j][5], x[j][6], x[j][7]}; }
    if (i == 0 && ty) { *(u32x4*)(smem + Q_MEM + wave * 2048 + lane * 16) = u32x4{x[0][0], x[0][1], x[0][2], x[0][3]}; *(u32x4*)(smem + Q_MEM + wave * 2048 + 1024 + lane * 16) = u32x4{x[0][4], x[0][5], x[0][6], x[0][7]}; }
  }
  if (wave == 4) *(volatile LAS u32x2*)(smem + Q_TM) = u32x2{0u, 0u};
  BAR();
  if (wave == 0) STS(ws, wg, 0);
  unsigned* cnt = cnt_of(ws, in);
  const char* xb = uni(xch_base(ws) + ((size_t)3 << 21) + (size_t)((in & 2u) << 12));
  const char* oh = uni(xch_base(ws) + (size_t)((in & 1u) << 13) + (size_t)(h >> 2) * 16384 + (h & 3) * 2048);
  auto rearm = [&](int ps) {
    unsigned z = SENT; asm volatile("" : "+v"(z));
    const unsigned l16 = lane_id() * 16;
#pragma unroll
    for (int r = 0; r < 2 * OREP; ++r) st16s(oh + (size_t)(ps * 4 + (r >> 1)) * 131072, (r & 1) * 1024u + l16, u32x4{z, z, z, z});
  };
  const char* dbw = xb + (size_t)48 * 16384 + 4096 + h * 128;
  unsigned avail = 0;
  auto stage_kv = [&](int s) {
    if (avail <= (unsigned)s) {
      for (int spins = 0;; ++spins) {
        const unsigned p = ld4_sc1(cnt + (PQKV + h) * 32 + (lane_id() & 15));
        avail = min(qkv_avail(p, 1), qkv_avail(p, 2));
        if (avail > (unsigned)s) break;
        __builtin_amdgcn_s_sleep(8);
        if (give_up(spins, tmo, 5)) break;
      }
    }
    const char* ks = uni(Kx + ((size_t)s * NH + h) * 8192); const char* vs = uni(V + ((size_t)s * NH + h) * 8192);
    const int lane = lane_id();
    head_to_lds(ks, lane, Q_K); head_to_lds(vs, lane, Q_V);
  };
  auto loop = [&](auto w_c) __attribute__((always_inline)) {
    constexpr int WV = decltype(w_c)::value;
    if (WV >= 4) asm volatile("ds_read_b128 v[56:59], %1\n\tds_read_b128 v[60:63], %1 offset:1024\n\ts_waitcnt lgkmcnt(0)" : "=&{v[56:63]}"(W.x) : "v"(Q_MEM + WV * 2048 + lane_id() * 16) : "memory");
    if (WV == 6) stage_kv(0);
    if (WV < 4) __builtin_amdgcn_s_setprio(2);
    unsigned t0 = 0, cap = 60000, d0 = 105, d1 = 200;
    int dq = D_Q;
    int sl = 0;
    for (int s = 0; s < NSEG; ++s) {
      TSDECL;
      const char* ag = xb + (size_t)((sl * 4 + (AREP == 4 ? xg : xg >> 1)) * 2 + (WV >> 2)) * 16384 + (WV & 3) * 2048;
      const char* vs = WV == 4 ? dbw : WV == 5 ? ws + OFF_KVM + ((size_t)s * NH + h) * 4096 : xb + (size_t)(24 + sl * 8 + (h >> 2)) * 16384 + (h & 3) * 2048;
      const char* od = WV == 4 ? (const char*)(size_t)((unsigned)(s + 1) << 24) : oh + (size_t)sl * 524288;
      if (s > 0) wait_rt(t0, dq);
      TS(0);
      unsigned long long tc; unsigned sp;
      qk_step<WV>(W, ag, vs, od, ws + OFF_KVM + ((size_t)s * NH + h) * 4096 + 2048, cap, sp, tc, nullptr, d0, d1);
      dq = (sp & 0xFFFFu) ? min(dq + DQU, D_Q + DQM) : max(dq - DQD, D_Q);
      if (WV < 4 || WV == 5) { WAIT_V(0); if (lane_id() == 0) st4_sc1(cnt + (PROG + h) * 32 + WV, (unsigned)(s + 1)); }
      if (WV == 6 && s + 1 < NSEG - 1) stage_kv(s + 1);
      if (WV == 7 && s > 0 && s < NSEG - 1) rearm(sl == 0 ? 2 : sl - 1);
      if (WV == 7 && s >= 8 && s < 8 + NSEG - SDIR) {
        unsigned z = SENT; asm volatile("" : "+v"(z));
        const char* kd = ws + OFF_KVM + ((size_t)(s - 8 + SDIR) * NH + h) * 4096;
#pragma unroll
        for (int r = 0; r < 4; ++r) st16s(kd, r * 1024u + lane_id() * 16, u32x4{z, z, z, z});
      }
      if (s < NSEG - 1) {
        if (WV == 4) {
          delay64<E_B>();
          t0 = bell_time(xb + (size_t)(48 + xg) * 16384 + 4096, (lane_id() & 31) * 128, (unsigned)(s + 1), cap);
          *(volatile LAS u32x2*)(smem + Q_TM) = u32x2{(unsigned)(s + 1), t0};
        } else {
          for (;;) {
            const u32x2 v = *(volatile LAS u32x2*)(smem + Q_TM);
            t0 = __builtin_amdgcn_readfirstlane(v.y);
            if (__builtin_amdgcn_readfirstlane(v.x) == (unsigned)(s + 1)) break;
            __builtin_amdgcn_s_sleep(1);
          }
        }
      }
      sl = sl == 2 ? 0 : sl + 1;
    }
    if (cap == 0 && lane_id() == 0) st4_sc1(tmo, 1u);
  };
  switch (wave) {
    case 0: loop(std::integral_constant<int, 0>{}); break;
    case 1: loop(std::integral_constant<int, 1>{}); break;
    case 2: loop(std::integral_constant<int, 2>{}); break;
    case 3: loop(std::integral_constant<int, 3>{}); break;
    case 4: loop(std::integral_constant<int, 4>{}); break;
    case 5: loop(std::integral_constant<int, 5>{}); break;
    case 6: loop(std::integral_constant<int, 6>{}); break;
    default: loop(std::integral_constant<int, 7>{}); break;
  }
  __builtin_amdgcn_s_setprio(0);
}

__device__ __forceinline__ void vr_body(char* smem, int wave_id_, int xg, int h, int c, int r, int wg, const u16* __restrict__ Wv, const u16* __restrict__ Wo,
                                        char* ws, unsigned* tmo, u64 tag) {
  const int wave = WAVE_ID;
  const unsigned po = (unsigned)(r >> 5) * 16384u + ((r >> 2) & 7) * 1024 + (r & 1) * 512 + ((r >> 1) & 1) * 128;
  const unsigned vo_ = (unsigned)(24 + (h >> 2)) * 16384u + (h & 3) * 2048 + c * 1024;
  const int hg = (wave + ROTW * (r & 3)) & 7;
  VRW W;
  {
    const int lane = lane_id();
    const int g = lane >> 4, q4 = (lane & 15) >> 2, p4 = lane & 3, fr = 4 * g + (q4 & 1) + 32 * (q4 >> 1);
    const unsigned lw = wave * 16384;
    const char* sv_ = uni((const char*)(Wv + (size_t)(128 * wave) * HD + h * DH + 64 * c));
    const char* so = uni((const char*)(Wo + (size_t)(512 * hg) * DM + 64 * (r >> 2) + 16 * (r & 1)));
    const unsigned vo = (unsigned)((32 * (lane >> 5) + 16 * ((lane >> 4) & 1) + ((lane >> 2) & 3)) * DM * 2 + ((lane >> 1) & 1) * 64 + (lane & 1) * 16);
    auto issue = [&](int p) {
#pragma unroll
      for (int i = 0; i < 8; ++i) {
        if (p >= 2 && (NTM & 8)) gldsw(so + (size_t)(128 * (p - 2) + 64 * (i & 1) + 8 * (i >> 2) + 4 * ((i >> 1) & 1)) * DM * 2, vo, lw + (p & 1) * 8192 + i * 1024);
        else if (p >= 2) glds16s<0>(so + (size_t)(128 * (p - 2) + 64 * (i & 1) + 8 * (i >> 2) + 4 * ((i >> 1) & 1)) * DM * 2, vo, lw + (p & 1) * 8192 + i * 1024);
        else gldsw(sv_ + (size_t)(8 * p + i) * 8 * HD * 2, (unsigned)((lane >> 3) * HD * 2 + (lane & 7) * 16), lw + (p & 1) * 8192 + i * 1024);
      }
    };
#define VTS(p) do { } while (0)
    if (wave == 0) STS(ws, wg, 7);
    issue(0); issue(1);
    const unsigned av = lw + (unsigned)(fr * 128 + p4 * 8), ao = lw + (unsigned)(fr * 64 + p4 * 8);
    WAIT_V(8); VTS(0); vr_wload<0>(W.v0, av); issue(2);
    WAIT_V(8); VTS(1); vr_wload<1>(W.v1, av); issue(3);
    WAIT_V(8); VTS(2); vr_wload<2>(W.o0, ao); issue(4);
    WAIT_V(8); vr_wload<3>(W.o1, ao); issue(5);
    if (wave == 0) init_issue(ws, 131072);
    WAIT_V(8); vr_wload<4>(W.o2, ao);
    WAIT_V(0); VTS(3); vr_wload<5>(W.o3, ao);
#undef VTS
  }
  if (wave == 0) STS(ws, wg, 5);
  if (wave == 0) STS(ws, wg, 6);
  BAR();
  if (wave == 0) STS(ws, wg, 0);
  const unsigned in = init_get(ws, tag, smem + 131072);
  const char* xb = uni(xch_base(ws) + ((size_t)3 << 21) + (size_t)((in & 2u) << 12));
  const char* ob = uni(xch_base(ws) + (size_t)((in & 1u) << 13));
  auto rearm = [&](int ps, bool ag) {
    const int lane = lane_id();
    unsigned z = SENT; asm volatile("" : "+v"(z));
    const u32x4 sv = u32x4{z, z, z, z};
    if (ag && lane < 16 * AREP) st16s(xb, (unsigned)(ps * 8 + (lane >> 4) * 2) * 16384u + po + ((lane >> 3) & 1) * 256 + (lane & 7) * 16, sv);
    st16s(xb, (unsigned)ps * 131072u + vo_ + lane * 16, sv);
  };
  __builtin_amdgcn_s_setprio(2);
  unsigned cap = 60000;
  int sl = 0;
  for (int s = 0; s < NSEG; ++s) {
    TSDECL;
    const int sn = sl == 2 ? 0 : sl + 1;
    const char* ag = xb + (size_t)((sl * 4 + (AREP == 4 ? xg : xg >> 1)) * 2 + (wave >> 2)) * 16384 + (wave & 3) * 2048;
    const char* vd = xb + (size_t)sl * 131072 + vo_ + (wave & 3) * 256;
    const char* os = ob + (size_t)((sl * 4 + (OREP == 4 ? xg : ORSEL)) * 8 + hg) * 16384 + ((r >> 1) & 1) * 1024;
    const char* ad = xb + (size_t)((sn * 4 + (wave & 3)) * 2) * 16384 + po;
    if (s > 0) delay64<E_AG>();
    TS(0);
    unsigned long long tc; unsigned sp;
    unsigned long long* tsp = nullptr;
    vr_v(W, ag, vd, wave * 2048, (wave & 3) * 512, cap, sp, tc, tsp);
    TN(12, sp + 1);
    if (s == NSEG - 1) break;
    if (wave == 7 && s > 0) rearm(sl == 0 ? 2 : sl - 1, true);
    wait_clk((unsigned)tc, E_O);
    TS(3);
    const unsigned tb = bell_time(xb + (size_t)(48 + xg) * 16384 + hg * 512, (lane_id() & 3) * 128, (unsigned)(s + 1), cap);
    wait_rt(tb, D_O);
    TS(4);
    vr_r(W, os, ad, wave * 512, cap, sp, tsp);
    TN(14, (sp & 0xFFFF) + 1);
    if (wave == 1) TSFLUSH(0x6078);
    sl = sn;
  }
  __builtin_amdgcn_s_setprio(0);
  if (cap == 0 && lane_id() == 0) st4_sc1(tmo, 2u);
}
struct ModIn { u32x4 w[8]; unsigned cv; };
__device__ __forceinline__ void mod_issue(ModIn& I, int wave_id_, int j, const u16* __restrict__ c, const u16* __restrict__ wmod) {
  const int tid = opaque_tid(), k0 = (j >> 5) * 512 + (tid >> 3);
  I.cv = c[k0 + 64 * (tid & 7)];
#pragma unroll
  for (int i = 0; i < 8; ++i) I.w[i] = ldnt<1>(wmod + (size_t)(k0 + 64 * i) * (6 * DM) + (j & 31) * 64 + (tid & 7) * 8);
}
__device__ __forceinline__ void mod_job(char* smem, int wave_id_, int j, u64 tag, const ModIn& I, char* ws) {
  const int tid = opaque_tid(), lane = tid & 63, wave = WAVE_ID, cg = tid & 7;
  const float cv = bflo(I.cv), sl = bfr(cv / (1.0f + __expf(-cv)));
  float acc[8] = {0, 0, 0, 0, 0, 0, 0, 0};
#pragma unroll
  for (int i = 0; i < 8; ++i) {
    const float sv = __shfl(sl, (lane & 56) + i);
#pragma unroll
    for (int e = 0; e < 4; ++e) { acc[2 * e] += sv * bflo(I.w[i][e]); acc[2 * e + 1] += sv * bfhi(I.w[i][e]); }
  }
#pragma unroll
  for (int e = 0; e < 8; ++e) acc[e] = red16_32_add(add_ror8(acc[e]));
  float* red = (float*)(smem + L_GB);
  if (lane < 8) { *(f32x4*)(red + wave * 64 + cg * 8) = f32x4{acc[0], acc[1], acc[2], acc[3]}; *(f32x4*)(red + wave * 64 + cg * 8 + 4) = f32x4{acc[4], acc[5], acc[6], acc[7]}; }
  __syncthreads();
  if (tid < 32) {
    f32x2 s = f32x2{0, 0};
#pragma unroll
    for (int r = 0; r < 8; ++r) s += *(const f32x2*)(red + r * 64 + tid * 2);
    st16_sc1(ws + OFF_MOD + (j * 32 + tid) * 16, u32x4{__float_as_uint(s[0]), (unsigned)tag, __float_as_uint(s[1]), tag2(tag)});
  }
  BAR();
}
#define NIMM 3
constexpr unsigned TK(int m, int hl, int part, int bo, int be) { return (unsigned)(m | hl << 2 | part << 5 | bo << 8 | be << 16); }
#define PLAN 1
#define QX 30
#define C1 43
#define C2 46
constexpr unsigned task_tab(int t, int it) {
  return t < 8 ? (it == 0 ? TK(0, t, 1, QX, 64) : 0)
       : t < 13 ? (it == 0 ? TK(1, t - 8, 0, 0, 64) : it == 1 ? TK(2, t - 8, 1, C1, 64) : 0)
       : t < 16 ? (it == 0 ? TK(1, t - 8, 0, 0, C2) : 0)
       : t < 24 ? (it == 0 ? TK(2, t - 16, 0, 0, t < 22 ? C1 : C2) : 0)
       : it > 0 ? 0
       : t < 26 ? TK(2, t - 18, 1, C2, 64)
       : t == 26 ? TK(1, 6, 1, C2, (C2 + 64) / 2) : t == 27 ? TK(1, 6, 2, (C2 + 64) / 2, 64)
       : t == 28 ? TK(2, 5, 1, C1, 64) : t == 29 ? TK(1, 5, 1, C2, 64)
       : t == 30 ? TK(1, 7, 1, C2, (C2 + 64) / 2) : t == 31 ? TK(1, 7, 2, (C2 + 64) / 2, 64)
       : TK(0, t - 32, 0, 0, QX);
}
template <int IT, int I = 0> __device__ __forceinline__ unsigned task_sel(int t) {
  if constexpr (I == 40) return 0u;
  else { unsigned r = task_sel<IT, I + 1>(t); constexpr unsigned c = task_tab(I, IT); if (c) asm("s_cmp_eq_u32 %1, %2\n\ts_cselect_b32 %0, %3, %0" : "+s"(r) : "s"(t), "i"(I), "i"(c) : "scc"); return r; }
}
__device__ __forceinline__ unsigned task_of(int t, int it) { return it == 0 ? task_sel<0>(t) : (it == 1 ? task_sel<1>(t) : task_sel<2>(t)); }
__global__ void __launch_bounds__(512, 2) k_mega(const u16* __restrict__ Wq, const u16* __restrict__ Wk, const u16* __restrict__ Wv,
                                                 const u16* __restrict__ Wo, const u16* __restrict__ w1, const u16* __restrict__ w3,
                                                 const u16* __restrict__ w2, const u16* __restrict__ x, const u16* __restrict__ cvec,
                                                 const u16* __restrict__ wmod, const u16* __restrict__ mem0, char* ws, u16* out) {
  extern __shared__ __attribute__((aligned(16))) char smem[];
  int wave_id_ = __builtin_amdgcn_readfirstlane((int)threadIdx.x >> 6);
  unsigned bid = blockIdx.x;
  asm volatile("" : "+s"(wave_id_), "+s"(bid));
  unsigned* tmo = (unsigned*)(ws + OFF_TMO);
  const unsigned tst = (unsigned)__builtin_amdgcn_s_memrealtime();
  unsigned long long* mts = nullptr;
  const float* mod = (const float*)(ws + OFF_MOD);
  volatile LAS unsigned* tslot = (volatile LAS unsigned*)(smem + L_SLOT + 768);
#define CPF 131072
  if (CPF && wave_id_ == 1) {
    u64 pc; asm volatile("s_getpc_b64 %0" : "=s"(pc));
    const unsigned o = (((bid >> 3) & 31) * 64 + lane_id()) * 64;
    if (o < CPF) asm volatile("s_mov_b32 m0, %0\n\ts_nop 0\n\tglobal_load_lds_dword %1, %2" :: "s"(155648 + 1024), "v"(o), "s"(pc & ~(u64)127) : "memory", "m0");
  }
  unsigned xcc; asm volatile("s_getreg_b32 %0, hwreg(HW_REG_XCC_ID)" : "=s"(xcc));
  xcc &= 7;
  const unsigned pred = xcc * 32 + ((bid >> 3) & 31);
  ModIn mi;
  auto modj = [](unsigned sl) { return sl >= 128 && ((sl + 8) & 31) < 16 ? (int)(((sl >> 5) & 3) * 16 + (sl & 15)) : -1; };
  if (modj(pred) >= 0) mod_issue(mi, wave_id_, modj(pred), cvec, wmod);
  u64 tag;
  asm volatile("s_load_dwordx2 %0, %1, 0x0\n\ts_waitcnt lgkmcnt(0)" : "=s"(tag) : "s"(ws + OFF_TAG) : "memory");
  tag += 1;
  if (opaque_tid() == 0) {
    unsigned sl = pred;
    const unsigned s0 = sl;
    for (unsigned i = 1; i <= 256; ++i) {
      if (__hip_atomic_exchange((u64*)(ws + OFF_CLAIM + sl * 128), tag, __ATOMIC_RELAXED, __HIP_MEMORY_SCOPE_AGENT) != tag) break;
      sl = i == 256 ? 0xFFFFu : (((s0 + (i & ~31u)) & 0xE0u) | ((s0 + i) & 31u));
    }
    *tslot = sl;
  }
  BAR();
  const int slot = (int)__builtin_amdgcn_readfirstlane(*tslot);
  BAR();
  if (slot >= 256) return;
  if (modj(slot) >= 0) {
    if (slot != (int)pred) mod_issue(mi, wave_id_, modj(slot), cvec, wmod);
    mod_job(smem, wave_id_, modj(slot), tag, mi, ws);
  }
  unsigned* cnt = nullptr;
  const int xg = slot >> 5, t = xg == 3 ? 24 + (slot & 7) : slot & 31;
  if (xg < 3) {
    const int nq = xg == 2 ? 8 : 12;
    if (t < nq) { const int h = xg * 12 + t; qk_body(smem, wave_id_, xg, h, (h >> 3) * 32 + (h & 7), Wq, Wk, ws + OFF_K, ws + OFF_V, ws, tmo, tag); }
    else { const int r = xg * 20 + t - nq; vr_body(smem, wave_id_, xg, r >> 1, r & 1, r, (r >> 4) * 32 + 8 + (r & 15), Wv, Wo, ws, tmo, tag); }
    return;
  }
  const bool cx = xg == 3;
  const int xq = cx ? (slot >> 3) & 3 : xg & 3, wsl = xq * 32 + t;
#define GDEL 1200
  if (cx) l1_role(smem, wave_id_, slot & 31, x, mod, mem0, ws + OFF_H1, cnt, tag, tmo, mts);
  else if (t < 8 || t >= 24) {
    if (wave_id_ == 0) STS(ws, 128 + wsl, 4);
    while ((unsigned)__builtin_amdgcn_s_memrealtime() - tst < GDEL) __builtin_amdgcn_s_sleep(8);
    cnt = cnt_of(ws, init_wait(ws, tag));
    if (t >= 24) mod2_job(smem, wave_id_, xq * 8 + t - 24, mod, cvec, wmod, cnt);
  }
  for (int it = 0; it < 3; ++it) {
    const int ti = cx ? t + 8 : t;
    const unsigned tk = task_of(ti, it);
    if (tk == 0) break;
    const int m = tk & 3, hl = (tk >> 2) & 7, part = (tk >> 5) & 3, bo = (tk >> 8) & 255, ns = (int)(tk >> 16) - bo, h = 8 * xq + hl;
    const bool first = m != 0 && bo == 0, late = it > 0;
    const int lane = lane_id();
    const Dep d{nullptr, (unsigned)(PL1 + (lane & 31)) * 128, 8u, (unsigned)-bo};
    if (wave_id_ == 0 && late) wait_l1(cnt, 8u, tmo);
    BAR();
    gemm7<R_QKV>(smem, wave_id_, (m == 0 ? Wq : (m == 1 ? Wk : Wv)) + h * DH, nullptr, HD, ws + OFF_H1 + (size_t)bo * 65536, 65536, d,
                    ws + (m == 0 ? OFF_Q : (m == 1 ? OFF_K : OFF_V)) + h * 8192 + (size_t)bo * 32 * 8192, 32 * 8192,
                    cnt, (PQKV + h) * 32 + 4 * part + m, tmo, wsl, mts, false, first ? NIMM : 0, ns, late ? ns : 0, bo * 257, tag);
  }
  __syncthreads();
  const int u2 = cx ? -1 : (t == 22 ? 0 : (t == 23 ? 6 : (t >= 13 && t < 16 ? t - 11 : (t == 24 ? 5 : (t == 25 ? 1 : -1)))));
  if (cx || (u2 >= 0 && (xq < 3 || u2 < 3))) {
    const int ks = cx || xq < 3 ? xq : u2, cn = cx ? (t + 1) & 7 : (xq < 3 ? u2 : 7);
    const int lane = lane_id();
    const Dep d{nullptr, (unsigned)(cx ? PSA + 4 * xq + (lane & 3) : PF1 + 16 * ks + (lane & 15)) * 128, cx ? 4u : 1u, cx ? (unsigned)(lane & 3) : 0u};
    gemm7<R_PART>(smem, wave_id_, (cx ? Wo : w2) + (size_t)ks * 1024 * DM + cn * 128, nullptr, DM, ws + (cx ? OFF_O : OFF_G) + ks * 16 * 4096, (cx ? 64 : 56) * 4096, d,
                  ws + (cx ? OFF_P : OFF_P2) + (ks * DM + cn * 128) * (TLP ? 64 : 2), (size_t)32 * PROW, cnt, ((cx ? PWO : PF2) + ks * 8 + cn) * 32, tmo,
                  128 + xq * 32 + (cx ? cn : 22 + u2), mts, !cx);
  } else if (u2 >= 0) {
    const int c4 = u2 - 3;
    const int lane = lane_id();
    const Dep d{nullptr, (unsigned)(PF1 + 48 + (lane & 7)) * 128, 1u, 0u};
    gemm7<R_PART2>(smem, wave_id_, w2 + (size_t)3072 * DM + c4 * 256, nullptr, DM, ws + OFF_G + 48 * 4096, 56 * 4096, d,
                   ws + OFF_P2 + (3 * DM + c4 * 256) * (TLP ? 64 : 2), (size_t)32 * PROW, cnt, (PF2 + 24 + c4) * 32, tmo, 128 + xq * 32 + 22 + u2, mts, true);
  } else if (t < 8 || (t >= 16 && t < 22)) {
    int fl = t < 8 ? 6 + t : t - 16;
    {
      const int sa = (3 + 2 * xq) & 7, sb = sa + 8 < 14 ? sa + 8 : 13;
      if (fl == 12) fl = sa; else if (fl == sa) fl = 12;
      else if (fl == 13) fl = sb; else if (fl == sb) fl = 13;
    }
    const int f = 14 * xq + fl;
    const int lane = lane_id();
    const Dep d{nullptr, (unsigned)((PLN + (lane >> 3)) * 128 + (lane & 7) * 4), 1u, 0u};
    gemm7<R_F1>(smem, wave_id_, w1 + 64 * f, w3 + 64 * f, FF, ws + OFF_H2, 65536, d, ws + OFF_G + f * 4096, 56 * 4096, cnt, (PF1 + f) * 32, tmo, 128 + xq * 32 + 8 + fl, mts, false);
  } else if (t >= 9 && t < 13) {
    fin_role(smem, wave_id_, xq * 4 + t - 9, mod, ws + OFF_P2, (const u16*)(ws + OFF_X1), out, cnt, tmo, mts);
    if (xq == 3 && t == 12 && opaque_tid() == 0) {
      const u32x4 v = ld16_sc1(ws + OFF_TAG);
      const u64 tn = (((u64)v.y << 32) | v.x) + 1;
      st8_sc1(ws + OFF_TAG, u32x2{(unsigned)tn, (unsigned)(tn >> 32)});
    }
  } else if (t == 28 || t == 29) {
    row_role(smem, wave_id_, 8 * xq, t - 28, mod, ws + OFF_P, x, (u16*)(ws + OFF_X1), ws + OFF_H2, cnt, tmo, 256 + xq, mts);
  } else if (t >= 26) {
    sa_role(smem, wave_id_, t >= 30 ? t - 30 : t - 24, 8 * xq, ws + OFF_Q, ws + OFF_K, ws + OFF_V, ws + OFF_KVM, ws + OFF_O, cnt, tmo, 128 + xq * 32 + 30 + (t & 1), mts);
  }
}

extern "C" void kernel_launch(void* const* d_in, const int* in_sizes, int n_in, void* d_out, int out_size, void* d_ws, size_t ws_size,
                              hipStream_t stream) {
  const u16* x    = (const u16*)d_in[0];
  const u16* mem0 = (const u16*)d_in[1];
  const u16* Wq   = (const u16*)d_in[2];
  const u16* Wk   = (const u16*)d_in[3];
  const u16* Wv   = (const u16*)d_in[4];
  const u16* Wo   = (const u16*)d_in[5];
  const u16* w1   = (const u16*)d_in[8];
  const u16* w3   = (const u16*)d_in[9];
  const u16* w2   = (const u16*)d_in[10];
  const u16* c    = (const u16*)d_in[11];
  const u16* wmod = (const u16*)d_in[12];
#define WSHIFT 0
  char* ws = (char*)d_ws + ((size_t)WSHIFT << 20);
  hipFuncSetAttribute((const void*)k_mega, hipFuncAttributeMaxDynamicSharedMemorySize, MEGA_LDS);
  k_mega<<<256, 512, MEGA_LDS, stream>>>(Wq, Wk, Wv, Wo, w1, w3, w2, x, c, wmod, mem0, ws, (u16*)d_out);
}
```

```cpp
#include <hip/hip_runtime.h>
#include <hip/hip_bf16.h>
#include <stdint.h>
#include <type_traits>
#pragma clang diagnostic ignored "-Winline-asm"
#pragma clang diagnostic ignored "-Wunused-value"

typedef unsigned short u16;
typedef short s16x4 __attribute__((ext_vector_type(4)));
typedef __bf16 bf16x8 __attribute__((ext_vector_type(8)));
typedef float f32x4 __attribute__((ext_vector_type(4)));
typedef float f32x2 __attribute__((ext_vector_type(2)));
typedef unsigned u32x4 __attribute__((ext_vector_type(4)));
typedef unsigned u32x2 __attribute__((ext_vector_type(2)));
#define LAS __attribute__((address_space(3)))

constexpr int SEQ = 2048, DM = 1024, NH = 32, DH = 128, HD = 4096, NMEM = 8, SEG = 32, NSEG = 64, FF = 3584;
constexpr unsigned SENT = 0xFFFFFFFFu;

__device__ __forceinline__ int lane_id() { int l; asm volatile("v_mbcnt_lo_u32_b32 %0, -1, 0\n\tv_mbcnt_hi_u32_b32 %0, -1, %0" : "=v"(l)); return l; }
#define WAVE_ID wave_id_
#define opaque_tid() (WAVE_ID * 64 + lane_id())
__device__ __forceinline__ float bf2f(u16 v) { return __builtin_bit_cast(float, (unsigned)v << 16); }
__device__ __forceinline__ float bflo(unsigned v) { return __builtin_bit_cast(float, v << 16); }
__device__ __forceinline__ float bfhi(unsigned v) { return __builtin_bit_cast(float, v & 0xffff0000u); }
__device__ __forceinline__ float bfr(float x) { return (float)(__bf16)x; }
__device__ __forceinline__ unsigned pack2(float a, float b) {
  typedef __bf16 bf16x2 __attribute__((ext_vector_type(2)));
  bf16x2 v; v[0] = (__bf16)a; v[1] = (__bf16)b; return __builtin_bit_cast(unsigned, v);
}
__device__ __forceinline__ unsigned nosent(unsigned v) { return v == SENT ? 0x7FC07FC0u : v; }
__device__ __forceinline__ bf16x8 asbf8(u32x4 v) { return __builtin_bit_cast(bf16x8, v); }
__device__ __forceinline__ s16x4 ass4(u32x2 v) { return __builtin_bit_cast(s16x4, v); }
__device__ __forceinline__ u32x2 trread(const char* lds_base, unsigned off) {
  s16x4 v = __builtin_amdgcn_ds_read_tr16_b64_v4i16((LAS s16x4*)(lds_base + off));
  return __builtin_bit_cast(u32x2, v);
}
__device__ __forceinline__ void glds16(const void* gsrc, unsigned lds_dst) {
  unsigned keep;
  asm volatile("s_mov_b32 %0, m0\n\ts_mov_b32 m0, %2\n\ts_nop 0\n\tglobal_load_lds_dwordx4 %1, off\n\ts_mov_b32 m0, %0"
               : "=&s"(keep) : "v"(gsrc), "s"(lds_dst) : "memory");
}
#define CNT_ " nt"
#define WNT_ " nt"
__device__ __forceinline__ void glds16_w(const void* gsrc, unsigned lds_dst) {
  unsigned keep;
  asm volatile("s_mov_b32 %0, m0\n\ts_mov_b32 m0, %2\n\ts_nop 0\n\tglobal_load_lds_dwordx4 %1, off" CNT_ "\n\ts_mov_b32 m0, %0"
               : "=&s"(keep) : "v"(gsrc), "s"(lds_dst) : "memory");
}
__device__ __forceinline__ void glds16_sc1(const void* gsrc, unsigned lds_dst) {
  unsigned keep;
  asm volatile("s_mov_b32 %0, m0\n\ts_mov_b32 m0, %2\n\ts_nop 0\n\tglobal_load_lds_dwordx4 %1, off sc1\n\ts_mov_b32 m0, %0"
               : "=&s"(keep) : "v"(gsrc), "s"(lds_dst) : "memory");
}
#define NTM 1
template <int B> __device__ __forceinline__ u32x4 ldnt(const void* p) { if (NTM & B) return __builtin_nontemporal_load((const u32x4*)p); else return *(const u32x4*)p; }
#define MFMA32(a, b, c) __builtin_amdgcn_mfma_f32_16x16x32_bf16((a), (b), (c), 0, 0, 0)

__device__ __forceinline__ void st16_sc1(void* p, u32x4 v) { asm volatile("global_store_dwordx4 %0, %1, off sc1\n\ts_nop 1" :: "v"(p), "v"(v) : "memory"); }
__device__ __forceinline__ void st8_sc1(void* p, u32x2 v) { asm volatile("global_store_dwordx2 %0, %1, off sc1\n\ts_nop 1" :: "v"(p), "v"(v) : "memory"); }
template <bool WT> __device__ __forceinline__ void st8(void* p, u32x2 v) { if (WT) st8_sc1(p, v); else *(u32x2*)p = v; }
template <bool WT> __device__ __forceinline__ void st16(void* p, u32x4 v) { if (WT) st16_sc1(p, v); else *(u32x4*)p = v; }
__device__ __forceinline__ u32x4 ld16_sc1(const void* p) {
  u32x4 r; asm volatile("global_load_dwordx4 %0, %1, off sc1\n\ts_waitcnt vmcnt(0)" : "=&v"(r) : "v"(p) : "memory"); return r;
}
__device__ __forceinline__ void ld16x4_sc1(const void* p, u32x4& a, u32x4& b, u32x4& c, u32x4& d) {
  asm volatile("global_load_dwordx4 %0, %4, off sc1\n\tglobal_load_dwordx4 %1, %4, off offset:1024 sc1\n\t"
               "global_load_dwordx4 %2, %4, off offset:2048 sc1\n\tglobal_load_dwordx4 %3, %4, off offset:3072 sc1\n\ts_waitcnt vmcnt(0)"
               : "=&v"(a), "=&v"(b), "=&v"(c), "=&v"(d) : "v"(p) : "memory");
}
typedef __amdgpu_buffer_rsrc_t rsrc_t;
__device__ __forceinline__ rsrc_t mkrs(const void* p) { return __builtin_amdgcn_make_buffer_rsrc((void*)p, 0, 0x7FFFFFFF, 0x00020000); }
__device__ __forceinline__ u32x4 bld16(rsrc_t r, unsigned off) { return __builtin_amdgcn_raw_buffer_load_b128(r, off, 0, 16); }
__device__ __forceinline__ u32x2 bld8(rsrc_t r, unsigned off) { return __builtin_amdgcn_raw_buffer_load_b64(r, off, 0, 16); }
__device__ __forceinline__ bool ok4(u32x4 v) { return v.x != SENT && v.y != SENT && v.z != SENT && v.w != SENT; }
constexpr int SPIN_CAP = 2000000;
__device__ __forceinline__ bool give_up(int spins, unsigned* tmo, unsigned code) {
  if ((spins & 4095) != 4095) return false;
  if (spins > SPIN_CAP) { __hip_atomic_store(tmo, code, __ATOMIC_RELAXED, __HIP_MEMORY_SCOPE_AGENT); return true; }
  return __hip_atomic_load(tmo, __ATOMIC_RELAXED, __HIP_MEMORY_SCOPE_AGENT) != 0u;
}
template <bool ACQ = true>
__device__ __forceinline__ void wait_cnt(unsigned* c, unsigned n, unsigned* tmo) {
  for (int spins = 0;; ++spins) {
    const unsigned v = __hip_atomic_load(c, __ATOMIC_RELAXED, __HIP_MEMORY_SCOPE_AGENT);
    if (v >= n) break;
    __builtin_amdgcn_s_sleep(8);
    if (give_up(spins, tmo, 5)) break;
  }
  if (ACQ) {
    __builtin_amdgcn_fence(__ATOMIC_ACQUIRE, "agent");
    asm volatile("s_waitcnt vmcnt(0)" ::: "memory");
  }
}

__device__ __forceinline__ const char* uni(const char* p) {
  const unsigned long long v = (unsigned long long)p;
  const unsigned lo = __builtin_amdgcn_readfirstlane((unsigned)v), hi = __builtin_amdgcn_readfirstlane((unsigned)(v >> 32));
  return (const char*)(((unsigned long long)hi << 32) | lo);
}
#define TSCLK "s_memrealtime"
#define TC(k) do { } while (0)
#define TS(k) do { } while (0)
#define TSDECL do { } while (0)
#define TSFLUSH(mask) do { } while (0)

constexpr size_t OFF_MOD  = 0;
constexpr size_t OFF_MOD2 = 65536;
constexpr size_t OFF_TMO  = 98304;
constexpr size_t OFF_TAG  = 131072;
constexpr size_t OFF_CLAIM = 135168;
constexpr size_t OFF_INIT = 196608;
constexpr size_t OFF_H1   = 262144;
constexpr size_t OFF_Q    = OFF_H1 + (size_t)SEQ * DM * 2;
constexpr size_t OFF_K    = OFF_Q + (size_t)SEQ * HD * 2;
constexpr size_t OFF_V    = OFF_K + (size_t)SEQ * HD * 2;
constexpr size_t OFF_O    = OFF_V + (size_t)SEQ * HD * 2;
constexpr size_t OFF_X1   = OFF_O + (size_t)SEQ * HD * 2;
constexpr size_t OFF_H2   = OFF_X1 + (size_t)SEQ * DM * 2;
constexpr size_t OFF_G    = OFF_H2 + (size_t)SEQ * DM * 2;
constexpr size_t OFF_KVM  = (size_t)92 << 20;
constexpr size_t OFF_XCH  = (size_t)100 << 20;
constexpr size_t OFF_P    = (size_t)112 << 20;
constexpr size_t OFF_P2   = (size_t)128 << 20;
constexpr size_t OFF_DUMP = (size_t)144 << 20;
constexpr int PROW = 4 * DM * 2;
#define TLQ 1
#define TLP 1
static_assert(OFF_G + (size_t)SEQ * FF * 2 <= OFF_KVM, "layout");
__device__ __forceinline__ char* xch_base(char* ws) { return (char*)(((size_t)(ws + OFF_XCH) + ((2u << 20) - 1)) & ~(size_t)((2u << 20) - 1)); }
#define XPAR 0
constexpr int PROG = 16;
constexpr int PQKV = 64;
constexpr int PL1 = 96;
constexpr int PL1B = 350;
constexpr int PSA = 128;
constexpr int PWO = 192;
constexpr int PLN = 224;
constexpr int PF2 = 320;
constexpr int PF1 = 448;
#define SDIR 40
#define SB 56
#define MARG 20
#define MARL 15
#define MARF 8
#define SAW 0
constexpr int L_RED = 131072;
constexpr int L_SLOT = L_RED + 16384;
constexpr int L_GB = L_SLOT + 1024;
constexpr int MEGA_LDS = 163840;
static_assert(L_GB + 12288 + 2048 <= MEGA_LDS, "lds");
#define XPARC 0

__device__ __forceinline__ int bswz(int row) { return (((row >> 1) & 1) | (((row >> 3) & 1) << 1)) << 1; }
template <int OFF, bool SC1 = false>
__device__ __forceinline__ void glds16s(const char* sbase, unsigned voff, unsigned lds_base) {
  unsigned keep;
  if (SC1)
    asm volatile("s_mov_b32 %0, m0\n\ts_add_u32 m0, %3, %4\n\ts_nop 2\n\tglobal_load_lds_dwordx4 %1, %2 sc1\n\ts_mov_b32 m0, %0"
                 : "=&s"(keep) : "v"(voff), "s"(sbase), "s"(lds_base), "i"(OFF) : "memory", "scc");
  else
    asm volatile("s_mov_b32 %0, m0\n\ts_add_u32 m0, %3, %4\n\ts_nop 2\n\tglobal_load_lds_dwordx4 %1, %2\n\ts_mov_b32 m0, %0"
                 : "=&s"(keep) : "v"(voff), "s"(sbase), "s"(lds_base), "i"(OFF) : "memory", "scc");
}
template <int OFF>
__device__ __forceinline__ void glds16x2s(const char* sbase, unsigned voff, unsigned lds_base) {
  unsigned keep;
  asm volatile("s_mov_b32 %0, m0\n\ts_add_u32 m0, %3, %4\n\ts_nop 2\n\tglobal_load_lds_dwordx4 %1, %2 sc1\n\tglobal_load_lds_dwordx4 %1, %2 offset:1024 sc1\n\ts_mov_b32 m0, %0"
               : "=&s"(keep) : "v"(voff), "s"(sbase), "s"(lds_base), "i"(OFF) : "memory", "scc");
}
__device__ __forceinline__ void st16s(const char* sb, unsigned vo, u32x4 v) { asm volatile("global_store_dwordx4 %0, %1, %2 sc1\n\ts_nop 1" :: "v"(vo), "v"(v), "s"(sb) : "memory"); }
__device__ __forceinline__ void head_to_lds(const char* src, int lane, unsigned lds) {
  if (TLQ) {
    const int c = (lane & 15) ^ (lane >> 4);
    const unsigned v0 = (unsigned)((c >> 2) * 2048 + (lane >> 4) * 64 + (c & 3) * 16);
#pragma unroll
    for (int i = 0; i < 8; ++i) glds16s<0, true>(src + i * 256, v0 ^ ((i & 3) * 2048), lds + i * 1024);
  } else {
#pragma unroll
    for (int i = 0; i < 4; ++i) glds16x2s<0>(src + i * 2048, (unsigned)lane * 16, lds + i * 2048);
  }
}
#define TCHP ""
__device__ __forceinline__ void touch(const void* sb, unsigned vo, unsigned lds) {
  asm volatile("s_mov_b32 m0, %0\n\ts_nop 0\n\tglobal_load_lds_dword %1, %2" TCHP :: "s"(lds), "v"(vo), "s"(sb) : "memory", "m0");
}
#define WAIT_V(n) asm volatile("s_waitcnt vmcnt(" #n ")" ::: "memory")
#define BAR() do { asm volatile("s_waitcnt lgkmcnt(0)" ::: "memory"); __builtin_amdgcn_s_barrier(); asm volatile("" ::: "memory"); } while (0)
__device__ __forceinline__ void st4_sc1(void* p, unsigned v) { asm volatile("global_store_dword %0, %1, off sc1" :: "v"(p), "v"(v) : "memory"); }
__device__ __forceinline__ unsigned ld4_sc1(const void* p) {
  unsigned r; asm volatile("global_load_dword %0, %1, off sc1\n\ts_waitcnt vmcnt(0)" : "=&v"(r) : "v"(p) : "memory"); return r;
}
template <int CTRL> __device__ __forceinline__ unsigned dppu(unsigned x) { return (unsigned)__builtin_amdgcn_update_dpp(0, (int)x, CTRL, 0xf, 0xf, false); }
__device__ __forceinline__ unsigned rowmin16(unsigned v) {
  v = min(v, dppu<0x128>(v)); v = min(v, dppu<0x124>(v)); v = min(v, dppu<0x122>(v)); v = min(v, dppu<0x121>(v));
  return v;
}
__device__ __forceinline__ unsigned rdl(unsigned v, int l) { return (unsigned)__builtin_amdgcn_readlane((int)v, l); }
__device__ __forceinline__ unsigned qkv_avail(unsigned v, int m) {
  unsigned a = rdl(v, m);
#pragma unroll
  for (int j = 1; j < 4; ++j) { const unsigned p = rdl(v, 4 * j + m); if ((p >> 8) <= a) a = max(a, p & 255u); }
  return a;
}
constexpr float SCALE = 0.08838834764831845f;
constexpr float LOG2E = 1.4426950408889634f;
template <int N> __device__ __forceinline__ void delay64() {
  if (N >= 16) { __builtin_amdgcn_s_sleep(15); delay64<(N >= 16 ? N - 15 : 0)>(); } else if (N > 0) __builtin_amdgcn_s_sleep(N);
}
__device__ __forceinline__ void sw16(float x, float& a, float& b) {
  a = x; b = x; asm volatile("s_nop 1\n\tv_permlane16_swap_b32 %0, %1" : "+v"(a), "+v"(b));
}
__device__ __forceinline__ void sw32(float x, float& a, float& b) {
  a = x; b = x; asm volatile("s_nop 1\n\tv_permlane32_swap_b32 %0, %1" : "+v"(a), "+v"(b));
}
__device__ __forceinline__ float red16_32_max(float x) {
  float a, b; sw16(x, a, b); x = fmaxf(a, b); sw32(x, a, b); return fmaxf(a, b);
}
__device__ __forceinline__ float red16_32_add(float x) {
  float a, b; sw16(x, a, b); x = a + b; sw32(x, a, b); return a + b;
}
__device__ __forceinline__ unsigned cvtpk(float a, float b) { unsigned r; asm("v_cvt_pk_bf16_f32 %0, %1, %2" : "=v"(r) : "v"(a), "v"(b)); return r; }
__device__ __forceinline__ float max3(float a, float b, float c) { float r; asm("v_max3_f32 %0, %1, %2, %3" : "=v"(r) : "v"(a), "v"(b), "v"(c)); return r; }
__device__ __forceinline__ float max2(float a, float b) { float r; asm("v_max_f32 %0, %1, %2" : "=v"(r) : "v"(a), "v"(b)); return r; }
__device__ __forceinline__ unsigned ror8u(unsigned x) { return (unsigned)__builtin_amdgcn_update_dpp(0, (int)x, 0x128, 0xf, 0xf, false); }
__device__ __forceinline__ float ror8(float x) { return __builtin_bit_cast(float, ror8u(__builtin_bit_cast(unsigned, x))); }
__device__ __forceinline__ float add_ror8(float x) {
  return x + __builtin_bit_cast(float, __builtin_amdgcn_update_dpp(0, __builtin_bit_cast(int, x), 0x128, 0xf, 0xf, false));
}

__device__ __forceinline__ unsigned rt_now() { return (unsigned)__builtin_amdgcn_s_memrealtime(); }
__device__ __forceinline__ unsigned latest(unsigned v) {
  const unsigned now = rt_now() + 400u, age = rowmin16((now - v) & 0xFFFFFFu);
  return now - min(min(min(rdl(age, 0), rdl(age, 16)), min(rdl(age, 32), rdl(age, 48))), 900u);
}
__device__ __forceinline__ void wait_rt(unsigned t0, int d) {
  while ((int)(rt_now() - t0) < d) { }
}
__device__ __forceinline__ bool bells(bool ok, unsigned u, int b, int marg) {
  if (b < SB || !__all(ok || (int)(u >> 24) > b)) return false;
  wait_rt(latest(!ok && (int)(u >> 24) == b + 1 ? u : rt_now() - 400u), marg);
  return true;
}
__device__ __forceinline__ void ld_fb(unsigned& v, unsigned& u, const void* sb, unsigned vo) {
  asm volatile("global_load_dword %0, %2, %3 sc1\n\tglobal_load_dword %1, %2, %3 offset:64 sc1\n\ts_waitcnt vmcnt(0)" : "=&v"(v), "=&v"(u) : "v"(vo), "s"(sb) : "memory");
}
__device__ __forceinline__ unsigned bell(int b, unsigned t) { return (unsigned)(b + 1) << 24 | (t & 0xFFFFFFu); }
typedef unsigned long long u64;
__device__ __forceinline__ unsigned tag2(u64 tag) { return (unsigned)(tag >> 32) ^ ((unsigned)tag * 0x9E3779B1u); }
__device__ __forceinline__ unsigned init_eval(u32x4 v, u64 tag) {
  if (!__all((v.x & ~63u) == (unsigned)tag << 6 && v.y == (unsigned)(tag >> 26))) return 0u;
  unsigned r = 8u;
#pragma unroll
  for (int c = 0; c < 3; ++c) r |= __popcll(__ballot(v.x & (1u << c))) > __popcll(__ballot(v.x & (8u << c))) ? 1u << c : 0u;
  return r ^ (XPAR * 3u + XPARC * 4u);
}
__device__ __forceinline__ unsigned init_wait(const char* ws, u64 tag) {
  for (int spins = 0; spins < SPIN_CAP; ++spins) {
    const unsigned r = init_eval(ld16_sc1(ws + OFF_INIT + (lane_id() & 31) * 128), tag);
    if (r) return r;
    __builtin_amdgcn_s_sleep(4);
  }
  return 8u;
}
__device__ __forceinline__ void init_issue(const char* ws, unsigned lds) { glds16s<0, true>(uni(ws + OFF_INIT), (unsigned)(lane_id() & 31) * 128u, lds); }
__device__ __forceinline__ unsigned init_get(const char* ws, u64 tag, const char* copy) {
  const unsigned r = init_eval(*(volatile LAS u32x4*)(copy + lane_id() * 16), tag);
  return r ? r : init_wait(ws, tag);
}
__device__ __forceinline__ unsigned* cnt_of(char* ws, unsigned in) { return (unsigned*)uni(xch_base(ws) + ((size_t)4 << 21) + (size_t)((in & 4u) << 11)); }
__device__ __forceinline__ void wait_l1(const unsigned* cnt, unsigned n, unsigned* tmo) {
  const int lane = lane_id();
  for (int spins = 0;; ++spins) {
    const unsigned v = ld4_sc1(cnt + (PL1 + (lane & 31)) * 32);
    if (__all(v >= n)) break;
    __builtin_amdgcn_s_sleep(8);
    if (give_up(spins, tmo, 12)) break;
  }
}
#define GKV 1
__device__ __forceinline__ void wait_k0(const unsigned* cnt, int h, unsigned* tmo) {
  for (int spins = 0;; ++spins) {
    const int l = lane_id();
    const unsigned v = ld4_sc1(cnt + (PQKV + h) * 32 + 1 + (l & 1));
    if (__all(v >= (unsigned)GKV)) break;
    __builtin_amdgcn_s_sleep(32);
    if (give_up(spins, tmo, 13)) break;
  }
}
#define WCLK(slot, b) do { } while (0)
#define WTM(slot, b, k) do { } while (0)
#define WTS(slot, b, k) do { } while (0)
#define WFB() do { } while (0)
#define STS(ws_, slot_, k_) do { } while (0)
typedef __bf16 bf16x2 __attribute__((ext_vector_type(2)));
__device__ __forceinline__ f32x2 pkfma(f32x2 a, f32x2 b, f32x2 c) { return __builtin_elementwise_fma(a, b, c); }
__device__ __forceinline__ float wavesum(float v) {
  v += __builtin_bit_cast(float, dppu<0x128>(__builtin_bit_cast(unsigned, v))); v += __builtin_bit_cast(float, dppu<0x124>(__builtin_bit_cast(unsigned, v)));
  v += __builtin_bit_cast(float, dppu<0x122>(__builtin_bit_cast(unsigned, v))); v += __builtin_bit_cast(float, dppu<0x121>(__builtin_bit_cast(unsigned, v)));
  return red16_32_add(v);
}
__device__ __forceinline__ float adds(unsigned v, unsigned sel, float c) { return __builtin_amdgcn_fdot2_f32_bf16(__builtin_bit_cast(bf16x2, v), __builtin_bit_cast(bf16x2, sel), c, false); }
__device__ __forceinline__ void sum4(const u32x4 (&p)[4], f32x2 (&a)[4]) {
  unsigned slo = 0x00003F80u, shi = 0x3F800000u;
  asm("" : "+s"(slo), "+s"(shi));
#pragma unroll
  for (int e = 0; e < 4; ++e) {
    float lo = bflo(p[0][e]), hi = bfhi(p[0][e]);
#pragma unroll
    for (int q = 1; q < 4; ++q) { lo = adds(p[q][e], slo, lo); hi = adds(p[q][e], shi, hi); }
    a[e] = f32x2{lo, hi};
  }
}
__device__ __forceinline__ u32x4 norm8(const f32x2 (&v)[4], const char* mt, float mb, float rden) {
  const f32x4 h0 = *(const f32x4*)(mt + 4096), h1 = *(const f32x4*)(mt + 4096 + 1024), c0 = *(const f32x4*)(mt + 8192), c1 = *(const f32x4*)(mt + 8192 + 1024);
  const f32x2 m2 = f32x2{mb, mb}, d2 = f32x2{rden, rden};
  const f32x2 r0 = pkfma(v[0] - m2, f32x2{c0[0], c0[1]} * d2, f32x2{h0[0], h0[1]}), r1 = pkfma(v[1] - m2, f32x2{c0[2], c0[3]} * d2, f32x2{h0[2], h0[3]});
  const f32x2 r2 = pkfma(v[2] - m2, f32x2{c1[0], c1[1]} * d2, f32x2{h1[0], h1[1]}), r3 = pkfma(v[3] - m2, f32x2{c1[2], c1[3]} * d2, f32x2{h1[2], h1[3]});
  return u32x4{pack2(r0[0], r0[1]), pack2(r1[0], r1[1]), pack2(r2[0], r2[1]), pack2(r3[0], r3[1])};
}
__device__ __forceinline__ void half_load(const char* P, const u16* res, int row, int hf, int lane, f32x2 (&a)[4], u32x4& rv) {
  const rsrc_t rp = mkrs(P), rr = mkrs(res);
  const unsigned po = TLP ? (unsigned)((row >> 5) * 32 * PROW + (hf * 16 + (lane >> 2)) * 2048 + (row & 31) * 64 + (lane & 3) * 16) : (unsigned)(row * PROW + hf * 1024 + lane * 16), ro = (unsigned)(row * DM * 2 + hf * 1024 + lane * 16);
  u32x4 p[4];
#pragma unroll
  for (int q = 0; q < 4; ++q) p[q] = bld16(rp, po + q * (TLP ? 65536 : 2048));
  if (res) rv = bld16(rr, ro);
  sum4(p, a);
}
__device__ __forceinline__ bool fin_half(bool fin, const char* P, const u16* res, const char* gbt, u16* __restrict__ o1, int row, int hf, int lane) {
  f32x2 a[4]; u32x4 rv;
  half_load(P, res, row, hf, lane, a, rv);
  const u32x4 gv = *(const u32x4*)(gbt + hf * 1024 + lane * 16);
  u32x4 o; f32x2 t = f32x2{0, 0};
#pragma unroll
  for (int e = 0; e < 4; ++e) { const f32x2 s = pkfma(f32x2{bflo(gv[e]), bfhi(gv[e])}, a[e], f32x2{bflo(rv[e]), bfhi(rv[e])}); o[e] = pack2(s[0], s[1]); t += s; }
  if (!fin && !__all(t[0] == t[0] && t[1] == t[1])) return false;
  st16_sc1(o1 + (size_t)row * DM + hf * 512 + lane * 8, o);
  return true;
}
#define DLN 10
#define LNE 1
template <int CTRL> __device__ __forceinline__ float dppf(float x) { return __builtin_bit_cast(float, dppu<CTRL>(__builtin_bit_cast(unsigned, x))); }
__device__ __forceinline__ bool ln_row(bool fin, int bb, unsigned* bl, const f32x2 (&a)[2][4], const f32x2 (&xf)[8], const f32x2 (&tg)[8], const f32x2 (&th)[8], const f32x2 (&tc)[8],
                                       const char* x1, const char* h2, unsigned xo, unsigned ho) {
  f32x2 v[8]; u32x4 o[2];
#pragma unroll
  for (int e = 0; e < 8; ++e) { const f32x2 t = pkfma(tg[e], a[e >> 2][e & 3], xf[e]); const unsigned u = pack2(t[0], t[1]); o[e >> 2][e & 3] = u; v[e] = f32x2{bflo(u), bfhi(u)}; }
  const float pv = __builtin_bit_cast(float, __builtin_amdgcn_readfirstlane(__float_as_int(v[0][0])));
  f32x2 d[8];
#pragma unroll
  for (int e = 0; e < 8; ++e) d[e] = v[e] - f32x2{pv, pv};
  const f32x2 s2 = ((d[0] + d[1]) + (d[2] + d[3])) + ((d[4] + d[5]) + (d[6] + d[7]));
  const f32x2 q2 = pkfma(d[3], d[3], pkfma(d[2], d[2], pkfma(d[1], d[1], d[0] * d[0]))) + pkfma(d[7], d[7], pkfma(d[6], d[6], pkfma(d[5], d[5], d[4] * d[4])));
  float s = s2[0] + s2[1], q = q2[0] + q2[1];
  if (!fin && !__all(s == s)) return false;
  if (LNE && bb >= SB && lane_id() == 0) st4_sc1(bl, bell(bb, rt_now() + DLN + 30));
  s += dppf<0x128>(s); q += dppf<0x128>(q); s += dppf<0x124>(s); q += dppf<0x124>(q);
  s += dppf<0x122>(s); q += dppf<0x122>(q); s += dppf<0x121>(s); q += dppf<0x121>(q);
  asm volatile("s_nop 1\n\tv_permlane16_swap_b32 %0, %1" : "+v"(s), "+v"(q));
  s += q;
  { float x, y; sw32(s, x, y); s = x + y; }
  const float sh = __builtin_bit_cast(float, rdl(__builtin_bit_cast(unsigned, s), 0)) * (1.0f / DM), sq = __builtin_bit_cast(float, rdl(__builtin_bit_cast(unsigned, s), 16));
  const float mb = bfr(pv + sh), vb = bfr(fmaxf(sq * (1.0f / DM) - sh * sh, 0.0f));
  const float rden = __builtin_amdgcn_rcpf(bfr(__builtin_amdgcn_sqrtf(bfr(vb + 1e-5f))));
#pragma unroll
  for (int i = 0; i < 2; ++i) {
    u32x4 r;
#pragma unroll
    for (int e = 0; e < 4; ++e) { const f32x2 t = pkfma(v[4 * i + e] - f32x2{mb, mb}, tc[4 * i + e] * f32x2{rden, rden}, th[4 * i + e]); r[e] = pack2(t[0], t[1]); }
    st16s(h2, ho + i * 32768, r);
  }
  if (!LNE && bb >= SB && lane_id() == 0) st4_sc1(bl, bell(bb, rt_now() + DLN));
  st16s(x1, xo, o[0]); st16s(x1, xo + 1024, o[1]);
  return true;
}
__device__ __forceinline__ void row_table(char* tab, const float* mod, const unsigned* cnt, unsigned* tmo, int tid) {
  wait_l1(cnt + 1, 1u, tmo);
  const rsrc_t rm = mkrs((const char*)mod + OFF_MOD2);
#pragma unroll
  for (int i = 0; i < 2; ++i) {
    const int u = tid + 512 * i, a = u >> 8, c = u & 255;
    f32x4 v = __builtin_bit_cast(f32x4, bld16(rm, (unsigned)u * 16));
    v = f32x4{bfr(v[0]), bfr(v[1]), bfr(v[2]), bfr(v[3])};
    if (a == 2) v = f32x4{bfr(1.0f + v[0]), bfr(1.0f + v[1]), bfr(1.0f + v[2]), bfr(1.0f + v[3])};
    if (a < 3) *(f32x4*)(tab + a * 4096 + ((c >> 7) * 2 + (c & 1)) * 1024 + ((c >> 1) & 63) * 16) = v;
    else *(u32x2*)(tab + 12288 + c * 8) = u32x2{pack2(v[0], v[1]), pack2(v[2], v[3])};
  }
  __syncthreads();
}
__device__ __forceinline__ void row_role(char* smem, int wave_id_, int rbase, int par, const float* mod, const char* P, const u16* x, u16* x1, char* h2c,
                                         unsigned* cnt, unsigned* tmo, int slot, unsigned long long* mts) {
  const int tid = opaque_tid(), lane = tid & 63, wave = WAVE_ID;
  if (tid < 80) *(volatile LAS unsigned*)(smem + 16384 + tid * 4) = 0u;
  {
    static_assert(TLP && SB % 2 == 0, "");
    unsigned z = SENT; asm volatile("" : "+v"(z));
    const int r4 = rbase + 4 * par + (wave & 3);
    for (int bb = SB + (wave >> 2); bb < NSEG; bb += 2)
#pragma unroll
      for (int i = 0; i < 2; ++i) {
        st16s(uni((const char*)x1 + (size_t)bb * 65536), (unsigned)(r4 * DM * 2 + i * 1024 + lane * 16), u32x4{z, z, z, z});
        st16s(uni(h2c + (size_t)bb * 65536), (unsigned)(r4 * 128 + (lane & 7) * 16 + (lane >> 3) * 4096 + i * 32768), u32x4{z, z, z, z});
      }
    WAIT_V(0);
  }
  row_table(smem, mod, cnt, tmo, tid);
  const int rb = rbase + wave + 32 * par;
  const rsrc_t rp = mkrs(P), rr = mkrs(x);
  const unsigned l16 = lane * 16;
  const unsigned pwo = (unsigned)(PWO + (lane & 31)) * 128;
  unsigned* myp = cnt + (PLN + (rb >> 3)) * 32 + wave;
  f32x2 a[2][4];
  const int gp = wave >> 2, r4 = rbase + 4 * par + (wave & 3);
  volatile LAS unsigned* lwb = (volatile LAS unsigned*)(smem + 16384 + gp * 8);
  if (gp && lane == 0) st4_sc1(myp, 1u);
  f32x2 tg[8], th[8], tc[8];
#pragma unroll
  for (int i = 0; i < 4; ++i) {
    const char* mt = smem + lane * 16 + i * 1024;
    const f32x4 g_ = *(const f32x4*)mt, h_ = *(const f32x4*)(mt + 4096), c_ = *(const f32x4*)(mt + 8192);
    tg[2 * i] = f32x2{g_[0], g_[1]}; tg[2 * i + 1] = f32x2{g_[2], g_[3]}; th[2 * i] = f32x2{h_[0], h_[1]}; th[2 * i + 1] = f32x2{h_[2], h_[3]};
    tc[2 * i] = f32x2{c_[0], c_[1]}; tc[2 * i + 1] = f32x2{c_[2], c_[3]};
  }
  const unsigned xo = (unsigned)(r4 * DM * 2 + lane * 16), ho = (unsigned)(r4 * 128 + (((lane & 7) ^ (r4 & 7)) << 4) + (lane >> 3) * 4096);
  const unsigned po = (l16 >> 6) * 2048 + (l16 & 48);
  u32x4 rv[2];
#pragma unroll
  for (int i = 0; i < 2; ++i) rv[i] = __builtin_amdgcn_raw_buffer_load_b128(rr, xo + i * 1024, (unsigned)gp * 65536u, 0);
  unsigned wb = 0;
  for (int bb = gp; bb < NSEG; bb += 2) {
    f32x2 xf[8];
#pragma unroll
    for (int e = 0; e < 8; ++e) xf[e] = f32x2{bflo(rv[e >> 2][e & 3]), bfhi(rv[e >> 2][e & 3])};
    asm volatile("" : "+v"(xf[0]), "+v"(xf[1]), "+v"(xf[2]), "+v"(xf[3]), "+v"(xf[4]), "+v"(xf[5]), "+v"(xf[6]), "+v"(xf[7]));
    if (wb <= (unsigned)bb) {
      if ((wave & 3) == 0) {
        wb = (unsigned)(bb + 1);
        for (int spins = 0;; ++spins) {
          unsigned v, u;
          ld_fb(v, u, cnt, pwo);
          if (__all(v > (unsigned)bb + 2u)) { v = rowmin16(v); wb = min(rdl(v, 0), rdl(v, 16)); break; }
          if (__all(v > (unsigned)bb) || bells(v > (unsigned)bb, u, bb, MARL)) break;
          __builtin_amdgcn_s_sleep(2);
          if (give_up(spins, tmo, 11)) break;
        }
        lwb[0] = wb;
      } else {
        while ((wb = __builtin_amdgcn_readfirstlane(lwb[0])) <= (unsigned)bb) __builtin_amdgcn_s_sleep(1);
      }
    }
    WTM(slot, bb, 0);
    bool fin = bb < SB;
    for (int spins = 0;; ++spins) {
      u32x4 pp[2][4];
#pragma unroll
      for (int i = 0; i < 2; ++i)
#pragma unroll
        for (int q = 0; q < 4; ++q) pp[i][q] = __builtin_amdgcn_raw_buffer_load_b128(rp, po + i * 32768, (unsigned)(bb * 32 * PROW + q * 65536 + r4 * 64), 16);
      sum4(pp[0], a[0]); sum4(pp[1], a[1]);
      if (ln_row(fin, bb, myp + 16, a, xf, tg, th, tc, uni((const char*)x1 + (size_t)bb * 65536), uni(h2c + (size_t)bb * 65536), xo, ho)) break;
      fin = __all(ld4_sc1((const char*)cnt + pwo) > (unsigned)bb) || give_up(spins, tmo, 11);
    }
    WTM(slot, bb, 3);
#pragma unroll
    for (int i = 0; i < 2; ++i) rv[i] = __builtin_amdgcn_raw_buffer_load_b128(rr, xo + i * 1024, (unsigned)min(bb + 2, NSEG - 1) * 65536u, 0);
    WAIT_V(2);
    unsigned o = 0;
    if (lane == 0) o = __hip_atomic_fetch_add((LAS unsigned*)(smem + 16448 + bb * 4), 1u, __ATOMIC_RELAXED, __HIP_MEMORY_SCOPE_WORKGROUP);
    if (__builtin_amdgcn_readfirstlane(o) == 3u) {
      if (lane < 4) st4_sc1(myp - (wave & 3) + lane, (unsigned)(bb + 2));
      WTM(slot, bb, 2);
    }
  }
}
#define NCS 8
__device__ __forceinline__ void l1_role(char* smem, int wave_id_, bool fst, int c, const u16* __restrict__ x, const float* __restrict__ mod,
                                        const u16* __restrict__ mem0, char* __restrict__ h1c, unsigned*& cnt, u64 tag, unsigned* tmo, unsigned long long* mts) {
  const int tid = opaque_tid(), lane = tid & 63, wave = WAVE_ID;
  char* ws = (char*)mod - OFF_MOD;
  u32x4 xr[7][2];
  const int row0 = (fst ? 0 : 256) + 8 * c + wave;
  const u16* src = x + (size_t)row0 * DM + lane * 8;
  xr[0][0] = ldnt<2>(src); xr[0][1] = ldnt<2>(src + 512);
  volatile LAS unsigned* sg = (volatile LAS unsigned*)(smem + L_SLOT + 784);
  if (!fst) {
#pragma unroll
    for (int pp = 1; pp < 7; ++pp) { xr[pp][0] = ldnt<2>(src + (size_t)pp * 256 * DM); xr[pp][1] = ldnt<2>(src + (size_t)pp * 256 * DM + 512); }
  } else if (wave >= 5) {
    char* cal = xch_base(ws) + ((size_t)(wave == 5 ? 0 : wave - 3) << 21) + (126 + (c >> 4)) * 16384 + (c & 15) * 512;
    st16_sc1(cal + (lane >> 5) * 8192 + (lane & 31) * 16, u32x4{SENT, SENT, SENT, SENT});
    WAIT_V(0);
    unsigned best[2] = {1u << 30, 1u << 30};
    const char* sb = uni(cal);
#pragma unroll
    for (int r = 1; r < NCS; ++r) {
      unsigned long long t0, t1; unsigned v;
      asm volatile("s_memtime %0\n\ts_waitcnt lgkmcnt(0)\n\ts_load_dword %2, %3, %4 glc\n\ts_waitcnt lgkmcnt(0)\n\ts_memtime %1\n\ts_waitcnt lgkmcnt(0)"
                   : "=&s"(t0), "=&s"(t1), "=&s"(v) : "s"(sb), "i"((r & 1) * 8192 + (r >> 1) * 128) : "memory");
      if (r > 1) best[r & 1] = min(best[r & 1], (unsigned)(t1 - t0));
    }
    unsigned xcc; asm volatile("s_getreg_b32 %0, hwreg(HW_REG_XCC_ID)" : "=s"(xcc));
    const unsigned df = max(best[0], best[1]) - min(best[0], best[1]);
    sg[wave - 5] = df < 110u || df > 400u ? 0u : (((best[1] < best[0]) != ((xcc & 4) != 0)) ? 1u : 8u) << (wave - 5);
  } else if (wave < 3) {
    const int b = c + 32 * wave;
    if (b < 89) {
      const char* p = uni(xch_base(ws) + (b < 48 ? (size_t)((b >> 4) * 32 + (b & 15)) * 16384
                                        : b < 56 ? ((size_t)3 << 21) + (size_t)(8 + ((b - 48) >> 2) * 8 + (b & 3)) * 16384
                                        : b < 84 ? ((size_t)3 << 21) + (size_t)(b - 56 + 24) * 16384 : ((size_t)4 << 21) + (size_t)(b - 84) * 16384));
      const unsigned v = b < 80 ? SENT : 0u;
#pragma unroll
      for (int i = 0; i < 16; ++i) st16s(p, (unsigned)(i * 1024 + lane * 16), u32x4{v, v, v, v});
    } else if (b == 89 && lane == 0) st4_sc1(tmo, 0u);
    WAIT_V(0);
  } else {
    const int pc = (c & 7) * 2 + (wave & 1), g = lane >> 4, hf = (lane >> 2) & 1, row = ((lane >> 3) & 1) * 4 + (lane & 3);
    u32x4 v = *(const u32x4*)(mem0 + row * DM + 32 * (2 * pc + hf) + 8 * g);
    v.x = nosent(v.x); v.y = nosent(v.y); v.z = nosent(v.z); v.w = nosent(v.w);
    st16_sc1(xch_base(ws) + ((size_t)3 << 21) + (size_t)(((c >> 3) & 1) * 2 + (pc >> 3)) * 16384 + (c >> 4) * 8192 + (pc & 7) * 1024 + lane * 16, v);
    WAIT_V(0);
  }
  BAR();
  if (fst && tid == 0) st8_sc1(ws + OFF_INIT + c * 128, u32x2{(unsigned)tag << 6 | sg[0] | sg[1] | sg[2], (unsigned)(tag >> 26)});
  if (fst && wave == 0) STS(ws, 128 + (c >> 3) * 32 + 24 + (c & 7), 2);
  {
    f32x4 v;
    for (int spins = 0; spins < SPIN_CAP; ++spins) {
      u32x4 m[4];
      asm volatile("s_nop 4\n\tglobal_load_dwordx4 %0, %4, %6 sc1\n\tglobal_load_dwordx4 %1, %4, %6 offset:16 sc1\n\t"
                   "global_load_dwordx4 %2, %5, %6 sc1\n\tglobal_load_dwordx4 %3, %5, %6 offset:16 sc1\n\ts_waitcnt vmcnt(0)"
                   : "=&v"(m[0]), "=&v"(m[1]), "=&v"(m[2]), "=&v"(m[3]) : "v"((unsigned)tid * 32), "v"((unsigned)tid * 32 + 16384), "s"(mod) : "memory");
      bool ok = true;
#pragma unroll
      for (int i = 0; i < 4; ++i) ok = ok && m[i].y == (unsigned)tag && m[i].w == tag2(tag);
      v = f32x4{__uint_as_float(m[0].x) + __uint_as_float(m[2].x), __uint_as_float(m[0].z) + __uint_as_float(m[2].z),
                __uint_as_float(m[1].x) + __uint_as_float(m[3].x), __uint_as_float(m[1].z) + __uint_as_float(m[3].z)};
      if (__all(ok)) break;
      __builtin_amdgcn_s_sleep(4);
    }
    v = f32x4{bfr(v[0]), bfr(v[1]), bfr(v[2]), bfr(v[3])};
    const int a = tid >> 8, cc = tid & 255;
    if (a) v = f32x4{bfr(1.0f + v[0]), bfr(1.0f + v[1]), bfr(1.0f + v[2]), bfr(1.0f + v[3])};
    *(f32x4*)(smem + 4096 + a * 4096 + ((cc >> 7) * 2 + (cc & 1)) * 1024 + ((cc >> 1) & 63) * 16) = v;
  }
  BAR();
  if (fst && wave == 0) STS(ws, 128 + (c >> 3) * 32 + 24 + (c & 7), 3);
#pragma unroll
  for (int p = 0; p < 7; ++p) {
    if (p > 0 && fst) break;
    f32x2 v[2][4];
#pragma unroll
    for (int i = 0; i < 2; ++i)
#pragma unroll
      for (int e = 0; e < 4; ++e) v[i][e] = f32x2{bflo(xr[p][i][e]), bfhi(xr[p][i][e])};
    const f32x2 s2 = ((v[0][0] + v[0][1]) + (v[0][2] + v[0][3])) + ((v[1][0] + v[1][1]) + (v[1][2] + v[1][3]));
    const float mean = wavesum(s2[0] + s2[1]) * (1.0f / DM);
    f32x2 q2 = f32x2{0, 0};
#pragma unroll
    for (int i = 0; i < 8; ++i) { const f32x2 d = v[i >> 2][i & 3] - f32x2{mean, mean}; q2 = pkfma(d, d, q2); }
    const float q = wavesum(q2[0] + q2[1]);
    if (p == 0 && fst && wave == 0) STS((char*)tmo - OFF_TMO, 128 + (c >> 3) * 32 + 24 + (c & 7), 5);
    const float mb = bfr(mean), vb = bfr(q * (1.0f / DM));
    const float rden = 1.0f / bfr(sqrtf(bfr(vb + 1e-5f)));
    const int row = 256 * p + row0, rb = row & 31;
    char* dst = h1c + (size_t)(row >> 5) * 65536 + (lane >> 3) * 4096 + rb * 128 + (((lane & 7) ^ (rb & 7)) << 4);
    const char* ml = smem + lane * 16;
#pragma unroll
    for (int i = 0; i < 2; ++i) st16_sc1(dst + i * 32768, norm8(v[i], ml + i * 2048, mb, rden));
  }
  cnt = cnt_of(ws, init_wait(ws, tag));
  WAIT_V(0);
  __builtin_amdgcn_s_barrier();
  if (tid == 0) st4_sc1(cnt + ((fst ? PL1 : PL1B) + c) * 32, fst ? 8u : 7u);
  if (wave == 0) STS(ws, 128 + (c >> 3) * 32 + 24 + (c & 7), fst ? 6 : 7);
}
__device__ __forceinline__ void mod2_job(char* smem, int wave_id_, int c, const float* __restrict__ mod, const u16* __restrict__ cvec, const u16* __restrict__ wmod, unsigned* cnt) {
  const int tid = opaque_tid();
  {
    const float sva = bf2f(cvec[tid]), svb = bf2f(cvec[tid + 512]);
    float* sv = (float*)(smem + 12288);
    sv[tid] = bfr(sva / (1.0f + __expf(-sva))); sv[tid + 512] = bfr(svb / (1.0f + __expf(-svb)));
  }
  __syncthreads();
  {
    const int cg = tid & 15, r0 = tid >> 4;
    const u16* wp = wmod + (size_t)r0 * (6 * DM) + 2 * DM + 128 * c + cg * 8;
    const float* sv = (const float*)(smem + 12288) + r0;
    f32x2 acc[4] = {f32x2{0, 0}, f32x2{0, 0}, f32x2{0, 0}, f32x2{0, 0}};
#pragma unroll 1
    for (int o = 0; o < 4; ++o) {
      u32x4 w[8];
#pragma unroll
      for (int i = 0; i < 8; ++i) w[i] = ldnt<1>(wp + (size_t)(o * 8 + i) * 32 * (6 * DM));
#pragma unroll
      for (int i = 0; i < 8; ++i) {
        const float s = sv[(o * 8 + i) * 32];
#pragma unroll
        for (int e = 0; e < 4; ++e) acc[e] = pkfma(f32x2{s, s}, f32x2{bflo(w[i][e]), bfhi(w[i][e])}, acc[e]);
      }
    }
    float* red = (float*)(smem + 16384);
    *(f32x4*)(red + r0 * 128 + cg * 8) = f32x4{acc[0][0], acc[0][1], acc[1][0], acc[1][1]};
    *(f32x4*)(red + r0 * 128 + cg * 8 + 4) = f32x4{acc[2][0], acc[2][1], acc[3][0], acc[3][1]};
    __syncthreads();
    if (tid < 128) {
      float s = 0;
#pragma unroll
      for (int r = 0; r < 32; ++r) s += red[r * 128 + tid];
      st4_sc1((char*)mod + OFF_MOD2 + (128 * c + tid) * 4, __builtin_bit_cast(unsigned, s));
    }
    WAIT_V(0);
    __builtin_amdgcn_s_barrier();
    if (tid == 0) st4_sc1(cnt + (PL1 + c) * 32 + 1, 1u);
  }
}
enum { R_QKV = 0, R_PART = 1, R_F1 = 2, R_PART2 = 3 };
struct Dep {
  const char* base; unsigned voff; unsigned mul, add;
};
__device__ __forceinline__ unsigned dep_eval(unsigned v, const Dep& d) {
  v = rowmin16(v * d.mul + (d.add + 64u));
  return min(min(rdl(v, 0), rdl(v, 16)), min(rdl(v, 32), rdl(v, 48))) - 64u;
}
typedef unsigned u32x32 __attribute__((ext_vector_type(32)));
typedef unsigned u32x16 __attribute__((ext_vector_type(16)));
__device__ __forceinline__ void fast_ld0(const u32x32 (&W)[4], u32x16& pd, unsigned ea, unsigned da, int& b, int& lf, int cp, unsigned up, unsigned sa, unsigned mu, unsigned ad,
    unsigned rw, unsigned ab, const char* p1, const char* p0, unsigned l1, unsigned l0, const unsigned (&av)[8], int la) {
  asm volatile("s_setprio 0\n\t"
    "s_cmp_eq_u32 %[la], 3\n\t"
    "s_waitcnt vmcnt(12)\n\t"
    "s_cbranch_scc1 20f\n\t"
    "s_waitcnt vmcnt(4)\n\t"
    "20:\n\t"
    "s_barrier\n\t"
    "ds_read_b128 v[96:99], %[ea] offset:0\n\t"
    "ds_read_b128 v[100:103], %[ea] offset:2048\n\t"
    "ds_read_b128 v[104:107], %[da] offset:0\n\t"
    "ds_read_b128 v[108:111], %[da] offset:2048\n\t"
    "ds_read_b128 v[112:115], %[ea] offset:4096\n\t"
    "ds_read_b128 v[116:119], %[ea] offset:6144\n\t"
    "s_branch 2f\n\t"
    "1:\n\t"
    "s_cmp_eq_u32 %[la], 3\n\t"
    "s_waitcnt vmcnt(12)\n\t"
    "s_cbranch_scc1 21f\n\t"
    "s_waitcnt vmcnt(4)\n\t"
    "21:\n\t"
    "s_barrier\n\t"
    "2:\n\t"
    "s_waitcnt lgkmcnt(4)\n\t"
    "v_mfma_f32_16x16x32_bf16 v[64:67], v[128:131], v[96:99], 0\n\t"
    "ds_read_b128 v[120:123], %[da] offset:4096\n\t"
    "s_add_u32 m0, %[l1], 0\n\t"
    "ds_write_b128 %[rw], v[80:83] offset:0\n\t"
    "v_mfma_f32_16x16x32_bf16 v[68:71], v[132:135], v[96:99], 0\n\t"
    "ds_read_b128 v[124:127], %[da] offset:6144\n\t"
    "global_load_lds_dwordx4 %[av0], s[36:37] sc1\n\t"
    "ds_write_b128 %[rw], v[84:87] offset:1024\n\t"
    "v_mfma_f32_16x16x32_bf16 v[72:75], v[128:131], v[100:103], 0\n\t"
    "s_add_u32 m0, %[l1], 32768\n\t"
    "ds_write_b128 %[rw], v[88:91] offset:2048\n\t"
    "v_mfma_f32_16x16x32_bf16 v[76:79], v[132:135], v[100:103], 0\n\t"
    "global_load_lds_dwordx4 %[av1], s[36:37] sc1\n\t"
    "ds_write_b128 %[rw], v[92:95] offset:3072\n\t"
    "s_waitcnt lgkmcnt(8)\n\t"
    "v_mfma_f32_16x16x32_bf16 v[64:67], v[136:139], v[104:107], v[64:67]\n\t"
    "ds_read_b128 v[96:99], %[ea] offset:8192\n\t"
    "s_add_u32 m0, %[l1], 4096\n\t"
    "v_mfma_f32_16x16x32_bf16 v[68:71], v[140:143], v[104:107], v[68:71]\n\t"
    "ds_read_b128 v[100:103], %[ea] offset:10240\n\t"
    "global_load_lds_dwordx4 %[av2], s[36:37] sc1\n\t"
    "v_mfma_f32_16x16x32_bf16 v[72:75], v[136:139], v[108:111], v[72:75]\n\t"
    "s_add_u32 m0, %[l1], 36864\n\t"
    "v_mfma_f32_16x16x32_bf16 v[76:79], v[140:143], v[108:111], v[76:79]\n\t"
    "global_load_lds_dwordx4 %[av3], s[36:37] sc1\n\t"
    "s_waitcnt lgkmcnt(8)\n\t"
    "v_mfma_f32_16x16x32_bf16 v[64:67], v[144:147], v[112:115], v[64:67]\n\t"
    "ds_read_b128 v[104:107], %[da] offset:8192\n\t"
    "s_add_u32 m0, %[l1], 8192\n\t"
    "v_mfma_f32_16x16x32_bf16 v[68:71], v[148:151], v[112:115], v[68:71]\n\t"
    "ds_read_b128 v[108:111], %[da] offset:10240\n\t"
    "global_load_lds_dwordx4 %[av4], s[36:37] sc1\n\t"
    "v_mfma_f32_16x16x32_bf16 v[72:75], v[144:147], v[116:119], v[72:75]\n\t"
    "s_add_u32 m0, %[l1], 40960\n\t"
    "v_mfma_f32_16x16x32_bf16 v[76:79], v[148:151], v[116:119], v[76:79]\n\t"
    "global_load_lds_dwordx4 %[av5], s[36:37] sc1\n\t"
    "s_waitcnt lgkmcnt(7)\n\t"
    "v_mfma_f32_16x16x32_bf16 v[64:67], v[152:155], v[120:123], v[64:67]\n\t"
    "ds_read_b128 v[112:115], %[ea] offset:12288\n\t"
    "s_add_u32 m0, %[l1], 12288\n\t"
    "v_mfma_f32_16x16x32_bf16 v[68:71], v[156:159], v[120:123], v[68:71]\n\t"
    "ds_read_b128 v[116:119], %[ea] offset:14336\n\t"
    "global_load_lds_dwordx4 %[av6], s[36:37] sc1\n\t"
    "v_mfma_f32_16x16x32_bf16 v[72:75], v[152:155], v[124:127], v[72:75]\n\t"
    "s_add_u32 m0, %[l1], 45056\n\t"
    "v_mfma_f32_16x16x32_bf16 v[76:79], v[156:159], v[124:127], v[76:79]\n\t"
    "global_load_lds_dwordx4 %[av7], s[36:37] sc1\n\t"
    "s_waitcnt lgkmcnt(4)\n\t"
    "v_mfma_f32_16x16x32_bf16 v[64:67], v[160:163], v[96:99], v[64:67]\n\t"
    "ds_read_b128 v[120:123], %[da] offset:12288\n\t"
    "v_mfma_f32_16x16x32_bf16 v[68:71], v[164:167], v[96:99], v[68:71]\n\t"
    "ds_read_b128 v[124:127], %[da] offset:14336\n\t"
    "v_mfma_f32_16x16x32_bf16 v[72:75], v[160:163], v[100:103], v[72:75]\n\t"
    "v_mfma_f32_16x16x32_bf16 v[76:79], v[164:167], v[100:103], v[76:79]\n\t"
    "s_waitcnt lgkmcnt(4)\n\t"
    "v_mfma_f32_16x16x32_bf16 v[64:67], v[168:171], v[104:107], v[64:67]\n\t"
    "ds_read_b128 v[96:99], %[ea] offset:16384\n\t"
    "v_mfma_f32_16x16x32_bf16 v[68:71], v[172:175], v[104:107], v[68:71]\n\t"
    "ds_read_b128 v[100:103], %[ea] offset:18432\n\t"
    "v_mfma_f32_16x16x32_bf16 v[72:75], v[168:171], v[108:111], v[72:75]\n\t"
    "v_mfma_f32_16x16x32_bf16 v[76:79], v[172:175], v[108:111], v[76:79]\n\t"
    "s_waitcnt lgkmcnt(4)\n\t"
    "v_mfma_f32_16x16x32_bf16 v[64:67], v[176:179], v[112:115], v[64:67]\n\t"
    "ds_read_b128 v[104:107], %[da] offset:16384\n\t"
    "v_mfma_f32_16x16x32_bf16 v[68:71], v[180:183], v[112:115], v[68:71]\n\t"
    "ds_read_b128 v[108:111], %[da] offset:18432\n\t"
    "v_mfma_f32_16x16x32_bf16 v[72:75], v[176:179], v[116:119], v[72:75]\n\t"
    "v_mfma_f32_16x16x32_bf16 v[76:79], v[180:183], v[116:119], v[76:79]\n\t"
    "s_waitcnt lgkmcnt(4)\n\t"
    "v_mfma_f32_16x16x32_bf16 v[64:67], v[184:187], v[120:123], v[64:67]\n\t"
    "ds_read_b128 v[112:115], %[ea] offset:20480\n\t"
    "v_mfma_f32_16x16x32_bf16 v[68:71], v[188:191], v[120:123], v[68:71]\n\t"
    "ds_read_b128 v[116:119], %[ea] offset:22528\n\t"
    "v_mfma_f32_16x16x32_bf16 v[72:75], v[184:187], v[124:127], v[72:75]\n\t"
    "v_mfma_f32_16x16x32_bf16 v[76:79], v[188:191], v[124:127], v[76:79]\n\t"
    "s_cmp_eq_u32 %[la], 3\n\t"
    "s_waitcnt vmcnt(12)\n\t"
    "s_cbranch_scc1 22f\n\t"
    "s_waitcnt vmcnt(4)\n\t"
    "22:\n\t"
    "s_barrier\n\t"
    "s_waitcnt lgkmcnt(4)\n\t"
    "v_mfma_f32_16x16x32_bf16 v[64:67], v[192:195], v[96:99], v[64:67]\n\t"
    "ds_read_b128 v[120:123], %[da] offset:20480\n\t"
    "s_add_u32 m0, %[l0], 0\n\t"
    "v_mfma_f32_16x16x32_bf16 v[68:71], v[196:199], v[96:99], v[68:71]\n\t"
    "ds_read_b128 v[124:127], %[da] offset:22528\n\t"
    "global_load_lds_dwordx4 %[av0], s[38:39] sc1\n\t"
    "v_mfma_f32_16x16x32_bf16 v[72:75], v[192:195], v[100:103], v[72:75]\n\t"
    "s_add_u32 m0, %[l0], 32768\n\t"
    "v_mfma_f32_16x16x32_bf16 v[76:79], v[196:199], v[100:103], v[76:79]\n\t"
    "global_load_lds_dwordx4 %[av1], s[38:39] sc1\n\t"
    "s_waitcnt lgkmcnt(4)\n\t"
    "v_mfma_f32_16x16x32_bf16 v[64:67], v[200:203], v[104:107], v[64:67]\n\t"
    "ds_read_b128 v[96:99], %[ea] offset:24576\n\t"
    "s_add_u32 m0, %[l0], 4096\n\t"
    "v_mfma_f32_16x16x32_bf16 v[68:71], v[204:207], v[104:107], v[68:71]\n\t"
    "ds_read_b128 v[100:103], %[ea] offset:26624\n\t"
    "global_load_lds_dwordx4 %[av2], s[38:39] sc1\n\t"
    "v_mfma_f32_16x16x32_bf16 v[72:75], v[200:203], v[108:111], v[72:75]\n\t"
    "s_add_u32 m0, %[l0], 36864\n\t"
    "v_mfma_f32_16x16x32_bf16 v[76:79], v[204:207], v[108:111], v[76:79]\n\t"
    "global_load_lds_dwordx4 %[av3], s[38:39] sc1\n\t"
    "s_add_u32 s36, s36, %[ab]\n\t"
    "s_addc_u32 s37, s37, 0\n\t"
    "s_waitcnt lgkmcnt(4)\n\t"
    "v_mfma_f32_16x16x32_bf16 v[64:67], v[208:211], v[112:115], v[64:67]\n\t"
    "ds_read_b128 v[104:107], %[da] offset:24576\n\t"
    "s_add_u32 m0, %[l0], 8192\n\t"
    "v_mfma_f32_16x16x32_bf16 v[68:71], v[212:215], v[112:115], v[68:71]\n\t"
    "ds_read_b128 v[108:111], %[da] offset:26624\n\t"
    "global_load_lds_dwordx4 %[av4], s[38:39] sc1\n\t"
    "v_mfma_f32_16x16x32_bf16 v[72:75], v[208:211], v[116:119], v[72:75]\n\t"
    "ds_read_b32 v39, %[sa]\n\t"
    "s_add_u32 m0, %[l0], 40960\n\t"
    "v_mfma_f32_16x16x32_bf16 v[76:79], v[212:215], v[116:119], v[76:79]\n\t"
    "global_load_lds_dwordx4 %[av5], s[38:39] sc1\n\t"
    "s_xor_b32 %[l1], %[l1], 0x10000\n\t"
    "s_waitcnt lgkmcnt(5)\n\t"
    "v_mfma_f32_16x16x32_bf16 v[64:67], v[216:219], v[120:123], v[64:67]\n\t"
    "ds_read_b128 v[112:115], %[ea] offset:28672\n\t"
    "s_add_u32 m0, %[l0], 12288\n\t"
    "v_mfma_f32_16x16x32_bf16 v[68:71], v[220:223], v[120:123], v[68:71]\n\t"
    "ds_read_b128 v[116:119], %[ea] offset:30720\n\t"
    "global_load_lds_dwordx4 %[av6], s[38:39] sc1\n\t"
    "v_mfma_f32_16x16x32_bf16 v[72:75], v[216:219], v[124:127], v[72:75]\n\t"
    "v_xor_b32 %[ea], 0x10000, %[ea]\n\t"
    "s_add_u32 m0, %[l0], 45056\n\t"
    "v_mfma_f32_16x16x32_bf16 v[76:79], v[220:223], v[124:127], v[76:79]\n\t"
    "global_load_lds_dwordx4 %[av7], s[38:39] sc1\n\t"
    "s_waitcnt lgkmcnt(5)\n\t"
    "v_mfma_f32_16x16x32_bf16 v[64:67], v[224:227], v[96:99], v[64:67]\n\t"
    "ds_read_b128 v[120:123], %[da] offset:28672\n\t"
    "v_mfma_f32_16x16x32_bf16 v[68:71], v[228:231], v[96:99], v[68:71]\n\t"
    "ds_read_b128 v[124:127], %[da] offset:30720\n\t"
    "v_mfma_f32_16x16x32_bf16 v[72:75], v[224:227], v[100:103], v[72:75]\n\t"
    "v_xor_b32 %[da], 0x10000, %[da]\n\t"
    "v_mfma_f32_16x16x32_bf16 v[76:79], v[228:231], v[100:103], v[76:79]\n\t"
    "s_waitcnt lgkmcnt(5)\n\t"
    "v_mfma_f32_16x16x32_bf16 v[64:67], v[232:235], v[104:107], v[64:67]\n\t"
    "ds_read_b128 v[96:99], %[ea] offset:0\n\t"
    "v_mfma_f32_16x16x32_bf16 v[68:71], v[236:239], v[104:107], v[68:71]\n\t"
    "ds_read_b128 v[100:103], %[ea] offset:2048\n\t"
    "v_mfma_f32_16x16x32_bf16 v[72:75], v[232:235], v[108:111], v[72:75]\n\t"
    "v_mfma_f32_16x16x32_bf16 v[76:79], v[236:239], v[108:111], v[76:79]\n\t"
    "s_waitcnt lgkmcnt(4)\n\t"
    "v_mfma_f32_16x16x32_bf16 v[64:67], v[240:243], v[112:115], v[64:67]\n\t"
    "ds_read_b128 v[104:107], %[da] offset:0\n\t"
    "v_mad_u32_u24 v39, v39, %[mu], %[ad]\n\t"
    "v_mfma_f32_16x16x32_bf16 v[68:71], v[244:247], v[112:115], v[68:71]\n\t"
    "ds_read_b128 v[108:111], %[da] offset:2048\n\t"
    "v_cmp_ge_i32 vcc, %[lf], v39\n\t"
    "s_cmp_eq_u64 vcc, 0\n\t"
    "s_addc_u32 %[lf], %[lf], 0\n\t"
    "v_mfma_f32_16x16x32_bf16 v[72:75], v[240:243], v[116:119], v[72:75]\n\t"
    "v_mfma_f32_16x16x32_bf16 v[76:79], v[244:247], v[116:119], v[76:79]\n\t"
    "v_cmp_ge_i32 vcc, %[lf], v39\n\t"
    "s_cmp_eq_u64 vcc, 0\n\t"
    "s_addc_u32 %[lf], %[lf], 0\n\t"
    "s_waitcnt lgkmcnt(4)\n\t"
    "v_mfma_f32_16x16x32_bf16 v[80:83], v[248:251], v[120:123], v[64:67]\n\t"
    "ds_read_b128 v[112:115], %[ea] offset:4096\n\t"
    "s_min_i32 %[lf], %[lf], %[cp]\n\t"
    "s_add_u32 %[b], %[b], 1\n\t"
    "v_mfma_f32_16x16x32_bf16 v[84:87], v[252:255], v[120:123], v[68:71]\n\t"
    "ds_read_b128 v[116:119], %[ea] offset:6144\n\t"
    "v_mfma_f32_16x16x32_bf16 v[88:91], v[248:251], v[124:127], v[72:75]\n\t"
    "v_mfma_f32_16x16x32_bf16 v[92:95], v[252:255], v[124:127], v[76:79]\n\t"
    "s_add_u32 s38, s38, %[ab]\n\t"
    "s_addc_u32 s39, s39, 0\n\t"
    "s_xor_b32 %[l0], %[l0], 0x10000\n\t"
    "s_sub_u32 m0, %[lf], %[b]\n\t"
    "s_sub_u32 m0, m0, 1\n\t"
    "s_cmp_lt_u32 m0, %[up]\n\t"
    "s_cbranch_scc1 1b\n\t"
    "s_waitcnt lgkmcnt(0)\n\t"
    "s_nop 4"
    : "=&{v[80:95]}"(pd), [ea] "+&v"(ea), [da] "+&v"(da), [b] "+&s"(b), [lf] "+&s"(lf), "+{s[36:37]}"(p1), "+{s[38:39]}"(p0), [sa] "+&v"(sa), [l1] "+&s"(l1), [l0] "+&s"(l0)
    : [rw] "v"(rw), [ab] "s"(ab), [cp] "s"(cp), [mu] "s"(mu), [ad] "v"(ad), [up] "s"(up), "{v[128:159]}"(W[0]), "{v[160:191]}"(W[1]), "{v[192:223]}"(W[2]), "{v[224:255]}"(W[3]), [av0] "v"(av[0]), [av1] "v"(av[1]), [av2] "v"(av[2]), [av3] "v"(av[3]), [av4] "v"(av[4]), [av5] "v"(av[5]), [av6] "v"(av[6]), [av7] "v"(av[7]), [la] "s"(la)
    : "v39", "v64", "v65", "v66", "v67", "v68", "v69", "v70", "v71", "v72", "v73", "v74", "v75", "v76", "v77", "v78", "v79", "v96", "v97", "v98", "v99", "v100", "v101", "v102", "v103", "v104", "v105", "v106", "v107", "v108", "v109", "v110", "v111", "v112", "v113", "v114", "v115", "v116", "v117", "v118", "v119", "v120", "v121", "v122", "v123", "v124", "v125", "v126", "v127", "vcc", "scc", "m0", "memory");
}
__device__ __forceinline__ void fast_ld2(const u32x32 (&W)[4], u32x16& pd, unsigned ea, unsigned da, int& b, int& lf, int cp, unsigned up, unsigned sa, unsigned mu, unsigned ad,
    unsigned rw, unsigned ab, const char* p1, const char* p0, unsigned l1, unsigned l0, const unsigned (&av)[8], int la) {
  asm volatile("s_setprio 0\n\t"
    "s_cmp_eq_u32 %[la], 3\n\t"
    "s_waitcnt vmcnt(6)\n\t"
    "s_cbranch_scc1 20f\n\t"
    "s_waitcnt vmcnt(2)\n\t"
    "20:\n\t"
    "s_barrier\n\t"
    "ds_read_b128 v[96:99], %[ea] offset:0\n\t"
    "ds_read_b128 v[100:103], %[ea] offset:2048\n\t"
    "ds_read_b128 v[104:107], %[da] offset:0\n\t"
    "ds_read_b128 v[108:111], %[da] offset:2048\n\t"
    "ds_read_b128 v[112:115], %[ea] offset:4096\n\t"
    "ds_read_b128 v[116:119], %[ea] offset:6144\n\t"
    "s_branch 2f\n\t"
    "1:\n\t"
    "s_cmp_eq_u32 %[la], 3\n\t"
    "s_waitcnt vmcnt(6)\n\t"
    "s_cbranch_scc1 21f\n\t"
    "s_waitcnt vmcnt(2)\n\t"
    "21:\n\t"
    "s_barrier\n\t"
    "2:\n\t"
    "s_waitcnt lgkmcnt(4)\n\t"
    "v_mfma_f32_16x16x32_bf16 v[64:67], v[128:131], v[96:99], 0\n\t"
    "ds_read_b128 v[120:123], %[da] offset:4096\n\t"
    "s_add_u32 m0, %[l1], 0\n\t"
    "ds_write_b128 %[rw], v[80:83] offset:0\n\t"
    "v_mfma_f32_16x16x32_bf16 v[68:71], v[132:135], v[96:99], 0\n\t"
    "ds_read_b128 v[124:127], %[da] offset:6144\n\t"
    "global_load_lds_dwordx4 %[av0], s[36:37] sc1\n\t"
    "ds_write_b128 %[rw], v[84:87] offset:1024\n\t"
    "v_mfma_f32_16x16x32_bf16 v[72:75], v[128:131], v[100:103], 0\n\t"
    "ds_write_b128 %[rw], v[88:91] offset:2048\n\t"
    "v_mfma_f32_16x16x32_bf16 v[76:79], v[132:135], v[100:103], 0\n\t"
    "ds_write_b128 %[rw], v[92:95] offset:3072\n\t"
    "s_waitcnt lgkmcnt(8)\n\t"
    "v_mfma_f32_16x16x32_bf16 v[64:67], v[136:139], v[104:107], v[64:67]\n\t"
    "ds_read_b128 v[96:99], %[ea] offset:8192\n\t"
    "s_add_u32 m0, %[l1], 4096\n\t"
    "v_mfma_f32_16x16x32_bf16 v[68:71], v[140:143], v[104:107], v[68:71]\n\t"
    "ds_read_b128 v[100:103], %[ea] offset:10240\n\t"
    "global_load_lds_dwordx4 %[av2], s[36:37] sc1\n\t"
    "v_mfma_f32_16x16x32_bf16 v[72:75], v[136:139], v[108:111], v[72:75]\n\t"
    "v_mfma_f32_16x16x32_bf16 v[76:79], v[140:143], v[108:111], v[76:79]\n\t"
    "s_waitcnt lgkmcnt(8)\n\t"
    "v_mfma_f32_16x16x32_bf16 v[64:67], v[144:147], v[112:115], v[64:67]\n\t"
    "ds_read_b128 v[104:107], %[da] offset:8192\n\t"
    "s_add_u32 m0, %[l1], 8192\n\t"
    "v_mfma_f32_16x16x32_bf16 v[68:71], v[148:151], v[112:115], v[68:71]\n\t"
    "ds_read_b128 v[108:111], %[da] offset:10240\n\t"
    "global_load_lds_dwordx4 %[av4], s[36:37] sc1\n\t"
    "v_mfma_f32_16x16x32_bf16 v[72:75], v[144:147], v[116:119], v[72:75]\n\t"
    "v_mfma_f32_16x16x32_bf16 v[76:79], v[148:151], v[116:119], v[76:79]\n\t"
    "s_waitcnt lgkmcnt(7)\n\t"
    "v_mfma_f32_16x16x32_bf16 v[64:67], v[152:155], v[120:123], v[64:67]\n\t"
    "ds_read_b128 v[112:115], %[ea] offset:12288\n\t"
    "s_add_u32 m0, %[l1], 12288\n\t"
    "v_mfma_f32_16x16x32_bf16 v[68:71], v[156:159], v[120:123], v[68:71]\n\t"
    "ds_read_b128 v[116:119], %[ea] offset:14336\n\t"
    "global_load_lds_dwordx4 %[av6], s[36:37] sc1\n\t"
    "v_mfma_f32_16x16x32_bf16 v[72:75], v[152:155], v[124:127], v[72:75]\n\t"
    "v_mfma_f32_16x16x32_bf16 v[76:79], v[156:159], v[124:127], v[76:79]\n\t"
    "s_waitcnt lgkmcnt(4)\n\t"
    "v_mfma_f32_16x16x32_bf16 v[64:67], v[160:163], v[96:99], v[64:67]\n\t"
    "ds_read_b128 v[120:123], %[da] offset:12288\n\t"
    "v_mfma_f32_16x16x32_bf16 v[68:71], v[164:167], v[96:99], v[68:71]\n\t"
    "ds_read_b128 v[124:127], %[da] offset:14336\n\t"
    "v_mfma_f32_16x16x32_bf16 v[72:75], v[160:163], v[100:103], v[72:75]\n\t"
    "v_mfma_f32_16x16x32_bf16 v[76:79], v[164:167], v[100:103], v[76:79]\n\t"
    "s_waitcnt lgkmcnt(4)\n\t"
    "v_mfma_f32_16x16x32_bf16 v[64:67], v[168:171], v[104:107], v[64:67]\n\t"
    "ds_read_b128 v[96:99], %[ea] offset:16384\n\t"
    "v_mfma_f32_16x16x32_bf16 v[68:71], v[172:175], v[104:107], v[68:71]\n\t"
    "ds_read_b128 v[100:103], %[ea] offset:18432\n\t"
    "v_mfma_f32_16x16x32_bf16 v[72:75], v[168:171], v[108:111], v[72:75]\n\t"
    "v_mfma_f32_16x16x32_bf16 v[76:79], v[172:175], v[108:111], v[76:79]\n\t"
    "s_waitcnt lgkmcnt(4)\n\t"
    "v_mfma_f32_16x16x32_bf16 v[64:67], v[176:179], v[112:115], v[64:67]\n\t"
    "ds_read_b128 v[104:107], %[da] offset:16384\n\t"
    "v_mfma_f32_16x16x32_bf16 v[68:71], v[180:183], v[112:115], v[68:71]\n\t"
    "ds_read_b128 v[108:111], %[da] offset:18432\n\t"
    "v_mfma_f32_16x16x32_bf16 v[72:75], v[176:179], v[116:119], v[72:75]\n\t"
    "v_mfma_f32_16x16x32_bf16 v[76:79], v[180:183], v[116:119], v[76:79]\n\t"
    "s_waitcnt lgkmcnt(4)\n\t"
    "v_mfma_f32_16x16x32_bf16 v[64:67], v[184:187], v[120:123], v[64:67]\n\t"
    "ds_read_b128 v[112:115], %[ea] offset:20480\n\t"
    "v_mfma_f32_16x16x32_bf16 v[68:71], v[188:191], v[120:123], v[68:71]\n\t"
    "ds_read_b128 v[116:119], %[ea] offset:22528\n\t"
    "v_mfma_f32_16x16x32_bf16 v[72:75], v[184:187], v[124:127], v[72:75]\n\t"
    "v_mfma_f32_16x16x32_bf16 v[76:79], v[188:191], v[124:127], v[76:79]\n\t"
    "s_cmp_eq_u32 %[la], 3\n\t"
    "s_waitcnt vmcnt(6)\n\t"
    "s_cbranch_scc1 22f\n\t"
    "s_waitcnt vmcnt(2)\n\t"
    "22:\n\t"
    "s_barrier\n\t"
    "s_waitcnt lgkmcnt(4)\n\t"
    "v_mfma_f32_16x16x32_bf16 v[64:67], v[192:195], v[96:99], v[64:67]\n\t"
    "ds_read_b128 v[120:123], %[da] offset:20480\n\t"
    "s_add_u32 m0, %[l0], 0\n\t"
    "v_mfma_f32_16x16x32_bf16 v[68:71], v[196:199], v[96:99], v[68:71]\n\t"
    "ds_read_b128 v[124:127], %[da] offset:22528\n\t"
    "global_load_lds_dwordx4 %[av0], s[38:39] sc1\n\t"
    "v_mfma_f32_16x16x32_bf16 v[72:75], v[192:195], v[100:103], v[72:75]\n\t"
    "v_mfma_f32_16x16x32_bf16 v[76:79], v[196:199], v[100:103], v[76:79]\n\t"
    "s_waitcnt lgkmcnt(4)\n\t"
    "v_mfma_f32_16x16x32_bf16 v[64:67], v[200:203], v[104:107], v[64:67]\n\t"
    "ds_read_b128 v[96:99], %[ea] offset:24576\n\t"
    "s_add_u32 m0, %[l0], 4096\n\t"
    "v_mfma_f32_16x16x32_bf16 v[68:71], v[204:207], v[104:107], v[68:71]\n\t"
    "ds_read_b128 v[100:103], %[ea] offset:26624\n\t"
    "global_load_lds_dwordx4 %[av2], s[38:39] sc1\n\t"
    "v_mfma_f32_16x16x32_bf16 v[72:75], v[200:203], v[108:111], v[72:75]\n\t"
    "v_mfma_f32_16x16x32_bf16 v[76:79], v[204:207], v[108:111], v[76:79]\n\t"
    "s_add_u32 s36, s36, %[ab]\n\t"
    "s_addc_u32 s37, s37, 0\n\t"
    "s_waitcnt lgkmcnt(4)\n\t"
    "v_mfma_f32_16x16x32_bf16 v[64:67], v[208:211], v[112:115], v[64:67]\n\t"
    "ds_read_b128 v[104:107], %[da] offset:24576\n\t"
    "s_add_u32 m0, %[l0], 8192\n\t"
    "v_mfma_f32_16x16x32_bf16 v[68:71], v[212:215], v[112:115], v[68:71]\n\t"
    "ds_read_b128 v[108:111], %[da] offset:26624\n\t"
    "global_load_lds_dwordx4 %[av4], s[38:39] sc1\n\t"
    "v_mfma_f32_16x16x32_bf16 v[72:75], v[208:211], v[116:119], v[72:75]\n\t"
    "ds_read_b32 v39, %[sa]\n\t"
    "v_mfma_f32_16x16x32_bf16 v[76:79], v[212:215], v[116:119], v[76:79]\n\t"
    "s_xor_b32 %[l1], %[l1], 0x10000\n\t"
    "s_waitcnt lgkmcnt(5)\n\t"
    "v_mfma_f32_16x16x32_bf16 v[64:67], v[216:219], v[120:123], v[64:67]\n\t"
    "ds_read_b128 v[112:115], %[ea] offset:28672\n\t"
    "s_add_u32 m0, %[l0], 12288\n\t"
    "v_mfma_f32_16x16x32_bf16 v[68:71], v[220:223], v[120:123], v[68:71]\n\t"
    "ds_read_b128 v[116:119], %[ea] offset:30720\n\t"
    "global_load_lds_dwordx4 %[av6], s[38:39] sc1\n\t"
    "v_mfma_f32_16x16x32_bf16 v[72:75], v[216:219], v[124:127], v[72:75]\n\t"
    "v_xor_b32 %[ea], 0x10000, %[ea]\n\t"
    "v_mfma_f32_16x16x32_bf16 v[76:79], v[220:223], v[124:127], v[76:79]\n\t"
    "s_waitcnt lgkmcnt(5)\n\t"
    "v_mfma_f32_16x16x32_bf16 v[64:67], v[224:227], v[96:99], v[64:67]\n\t"
    "ds_read_b128 v[120:123], %[da] offset:28672\n\t"
    "v_mfma_f32_16x16x32_bf16 v[68:71], v[228:231], v[96:99], v[68:71]\n\t"
    "ds_read_b128 v[124:127], %[da] offset:30720\n\t"
    "v_mfma_f32_16x16x32_bf16 v[72:75], v[224:227], v[100:103], v[72:75]\n\t"
    "v_xor_b32 %[da], 0x10000, %[da]\n\t"
    "v_mfma_f32_16x16x32_bf16 v[76:79], v[228:231], v[100:103], v[76:79]\n\t"
    "s_waitcnt lgkmcnt(5)\n\t"
    "v_mfma_f32_16x16x32_bf16 v[64:67], v[232:235], v[104:107], v[64:67]\n\t"
    "ds_read_b128 v[96:99], %[ea] offset:0\n\t"
    "v_mfma_f32_16x16x32_bf16 v[68:71], v[236:239], v[104:107], v[68:71]\n\t"
    "ds_read_b128 v[100:103], %[ea] offset:2048\n\t"
    "v_mfma_f32_16x16x32_bf16 v[72:75], v[232:235], v[108:111], v[72:75]\n\t"
    "v_mfma_f32_16x16x32_bf16 v[76:79], v[236:239], v[108:111], v[76:79]\n\t"
    "s_waitcnt lgkmcnt(4)\n\t"
    "v_mfma_f32_16x16x32_bf16 v[64:67], v[240:243], v[112:115], v[64:67]\n\t"
    "ds_read_b128 v[104:107], %[da] offset:0\n\t"
    "v_mad_u32_u24 v39, v39, %[mu], %[ad]\n\t"
    "v_mfma_f32_16x16x32_bf16 v[68:71], v[244:247], v[112:115], v[68:71]\n\t"
    "ds_read_b128 v[108:111], %[da] offset:2048\n\t"
    "v_cmp_ge_i32 vcc, %[lf], v39\n\t"
    "s_cmp_eq_u64 vcc, 0\n\t"
    "s_addc_u32 %[lf], %[lf], 0\n\t"
    "v_mfma_f32_16x16x32_bf16 v[72:75], v[240:243], v[116:119], v[72:75]\n\t"
    "v_mfma_f32_16x16x32_bf16 v[76:79], v[244:247], v[116:119], v[76:79]\n\t"
    "v_cmp_ge_i32 vcc, %[lf], v39\n\t"
    "s_cmp_eq_u64 vcc, 0\n\t"
    "s_addc_u32 %[lf], %[lf], 0\n\t"
    "s_waitcnt lgkmcnt(4)\n\t"
    "v_mfma_f32_16x16x32_bf16 v[80:83], v[248:251], v[120:123], v[64:67]\n\t"
    "ds_read_b128 v[112:115], %[ea] offset:4096\n\t"
    "s_min_i32 %[lf], %[lf], %[cp]\n\t"
    "s_add_u32 %[b], %[b], 1\n\t"
    "v_mfma_f32_16x16x32_bf16 v[84:87], v[252:255], v[120:123], v[68:71]\n\t"
    "ds_read_b128 v[116:119], %[ea] offset:6144\n\t"
    "v_mfma_f32_16x16x32_bf16 v[88:91], v[248:251], v[124:127], v[72:75]\n\t"
    "v_mfma_f32_16x16x32_bf16 v[92:95], v[252:255], v[124:127], v[76:79]\n\t"
    "s_add_u32 s38, s38, %[ab]\n\t"
    "s_addc_u32 s39, s39, 0\n\t"
    "s_xor_b32 %[l0], %[l0], 0x10000\n\t"
    "s_sub_u32 m0, %[lf], %[b]\n\t"
    "s_sub_u32 m0, m0, 1\n\t"
    "s_cmp_lt_u32 m0, %[up]\n\t"
    "s_cbranch_scc1 1b\n\t"
    "s_waitcnt lgkmcnt(0)\n\t"
    "s_nop 4"
    : "=&{v[80:95]}"(pd), [ea] "+&v"(ea), [da] "+&v"(da), [b] "+&s"(b), [lf] "+&s"(lf), "+{s[36:37]}"(p1), "+{s[38:39]}"(p0), [sa] "+&v"(sa), [l1] "+&s"(l1), [l0] "+&s"(l0)
    : [rw] "v"(rw), [ab] "s"(ab), [cp] "s"(cp), [mu] "s"(mu), [ad] "v"(ad), [up] "s"(up), "{v[128:159]}"(W[0]), "{v[160:191]}"(W[1]), "{v[192:223]}"(W[2]), "{v[224:255]}"(W[3]), [av0] "v"(av[0]), [av1] "v"(av[1]), [av2] "v"(av[2]), [av3] "v"(av[3]), [av4] "v"(av[4]), [av5] "v"(av[5]), [av6] "v"(av[6]), [av7] "v"(av[7]), [la] "s"(la)
    : "v39", "v64", "v65", "v66", "v67", "v68", "v69", "v70", "v71", "v72", "v73", "v74", "v75", "v76", "v77", "v78", "v79", "v96", "v97", "v98", "v99", "v100", "v101", "v102", "v103", "v104", "v105", "v106", "v107", "v108", "v109", "v110", "v111", "v112", "v113", "v114", "v115", "v116", "v117", "v118", "v119", "v120", "v121", "v122", "v123", "v124", "v125", "v126", "v127", "vcc", "scc", "m0", "memory");
}
__device__ __forceinline__ void fast_st0(const u32x32 (&W)[4], u32x16& pd, unsigned ea, unsigned da, int& b, int& lf, int cp, unsigned up, unsigned sa, unsigned mu, unsigned ad,
    unsigned rw, unsigned ab, const char* p1, const char* p0, unsigned co0, unsigned co1, int po, int pf, unsigned w0, unsigned sl, unsigned dv, const char* db, const char* pg) {
  unsigned st;
  asm volatile("s_barrier\n\t"
    "ds_read_b128 v[96:99], %[ea] offset:0\n\t"
    "ds_read_b128 v[100:103], %[ea] offset:2048\n\t"
    "ds_read_b128 v[104:107], %[da] offset:0\n\t"
    "ds_read_b128 v[108:111], %[da] offset:2048\n\t"
    "ds_read_b128 v[112:115], %[ea] offset:4096\n\t"
    "ds_read_b128 v[116:119], %[ea] offset:6144\n\t"
    "s_branch 2f\n\t"
    "1:\n\t"
    "s_barrier\n\t"
    "2:\n\t"
    "s_waitcnt lgkmcnt(4)\n\t"
    "v_mfma_f32_16x16x32_bf16 v[64:67], v[128:131], v[96:99], 0\n\t"
    "ds_read_b128 v[120:123], %[da] offset:4096\n\t"
    "v_mfma_f32_16x16x32_bf16 v[68:71], v[132:135], v[96:99], 0\n\t"
    "ds_read_b128 v[124:127], %[da] offset:6144\n\t"
    "v_mfma_f32_16x16x32_bf16 v[72:75], v[128:131], v[100:103], 0\n\t"
    "v_mfma_f32_16x16x32_bf16 v[76:79], v[132:135], v[100:103], 0\n\t"
    "s_waitcnt lgkmcnt(4)\n\t"
    "v_mfma_f32_16x16x32_bf16 v[64:67], v[136:139], v[104:107], v[64:67]\n\t"
    "ds_read_b128 v[96:99], %[ea] offset:8192\n\t"
    "v_mfma_f32_16x16x32_bf16 v[68:71], v[140:143], v[104:107], v[68:71]\n\t"
    "ds_read_b128 v[100:103], %[ea] offset:10240\n\t"
    "v_mfma_f32_16x16x32_bf16 v[72:75], v[136:139], v[108:111], v[72:75]\n\t"
    "v_mfma_f32_16x16x32_bf16 v[76:79], v[140:143], v[108:111], v[76:79]\n\t"
    "s_cmp_eq_u32 %[w0], 1\n\t"
    "s_cbranch_scc1 7f\n\t"
    "8:\n\t"
    "s_waitcnt lgkmcnt(4)\n\t"
    "v_mfma_f32_16x16x32_bf16 v[64:67], v[144:147], v[112:115], v[64:67]\n\t"
    "ds_read_b128 v[104:107], %[da] offset:8192\n\t"
    "v_mfma_f32_16x16x32_bf16 v[68:71], v[148:151], v[112:115], v[68:71]\n\t"
    "ds_read_b128 v[108:111], %[da] offset:10240\n\t"
    "v_mfma_f32_16x16x32_bf16 v[72:75], v[144:147], v[116:119], v[72:75]\n\t"
    "v_mfma_f32_16x16x32_bf16 v[76:79], v[148:151], v[116:119], v[76:79]\n\t"
    "s_waitcnt lgkmcnt(4)\n\t"
    "v_mfma_f32_16x16x32_bf16 v[64:67], v[152:155], v[120:123], v[64:67]\n\t"
    "ds_read_b128 v[112:115], %[ea] offset:12288\n\t"
    "v_mfma_f32_16x16x32_bf16 v[68:71], v[156:159], v[120:123], v[68:71]\n\t"
    "ds_read_b128 v[116:119], %[ea] offset:14336\n\t"
    "v_mfma_f32_16x16x32_bf16 v[72:75], v[152:155], v[124:127], v[72:75]\n\t"
    "v_mfma_f32_16x16x32_bf16 v[76:79], v[156:159], v[124:127], v[76:79]\n\t"
    "s_waitcnt lgkmcnt(4)\n\t"
    "v_mfma_f32_16x16x32_bf16 v[64:67], v[160:163], v[96:99], v[64:67]\n\t"
    "ds_read_b128 v[120:123], %[da] offset:12288\n\t"
    "v_mfma_f32_16x16x32_bf16 v[68:71], v[164:167], v[96:99], v[68:71]\n\t"
    "ds_read_b128 v[124:127], %[da] offset:14336\n\t"
    "v_mfma_f32_16x16x32_bf16 v[72:75], v[160:163], v[100:103], v[72:75]\n\t"
    "v_mfma_f32_16x16x32_bf16 v[76:79], v[164:167], v[100:103], v[76:79]\n\t"
    "s_waitcnt lgkmcnt(4)\n\t"
    "v_mfma_f32_16x16x32_bf16 v[64:67], v[168:171], v[104:107], v[64:67]\n\t"
    "ds_read_b128 v[96:99], %[ea] offset:16384\n\t"
    "v_mfma_f32_16x16x32_bf16 v[68:71], v[172:175], v[104:107], v[68:71]\n\t"
    "ds_read_b128 v[100:103], %[ea] offset:18432\n\t"
    "v_mfma_f32_16x16x32_bf16 v[72:75], v[168:171], v[108:111], v[72:75]\n\t"
    "v_mfma_f32_16x16x32_bf16 v[76:79], v[172:175], v[108:111], v[76:79]\n\t"
    "s_waitcnt lgkmcnt(4)\n\t"
    "v_mfma_f32_16x16x32_bf16 v[64:67], v[176:179], v[112:115], v[64:67]\n\t"
    "ds_read_b128 v[104:107], %[da] offset:16384\n\t"
    "v_mfma_f32_16x16x32_bf16 v[68:71], v[180:183], v[112:115], v[68:71]\n\t"
    "ds_read_b128 v[108:111], %[da] offset:18432\n\t"
    "v_mfma_f32_16x16x32_bf16 v[72:75], v[176:179], v[116:119], v[72:75]\n\t"
    "v_mfma_f32_16x16x32_bf16 v[76:79], v[180:183], v[116:119], v[76:79]\n\t"
    "s_waitcnt lgkmcnt(4)\n\t"
    "v_mfma_f32_16x16x32_bf16 v[64:67], v[184:187], v[120:123], v[64:67]\n\t"
    "ds_read_b128 v[112:115], %[ea] offset:20480\n\t"
    "v_mfma_f32_16x16x32_bf16 v[68:71], v[188:191], v[120:123], v[68:71]\n\t"
    "ds_read_b128 v[116:119], %[ea] offset:22528\n\t"
    "v_mfma_f32_16x16x32_bf16 v[72:75], v[184:187], v[124:127], v[72:75]\n\t"
    "v_mfma_f32_16x16x32_bf16 v[76:79], v[188:191], v[124:127], v[76:79]\n\t"
    "s_waitcnt vmcnt(0)\n\t"
    "s_barrier\n\t"
    "s_waitcnt lgkmcnt(4)\n\t"
    "v_mfma_f32_16x16x32_bf16 v[64:67], v[192:195], v[96:99], v[64:67]\n\t"
    "ds_read_b128 v[120:123], %[da] offset:20480\n\t"
    "v_mfma_f32_16x16x32_bf16 v[68:71], v[196:199], v[96:99], v[68:71]\n\t"
    "ds_read_b128 v[124:127], %[da] offset:22528\n\t"
    "v_mfma_f32_16x16x32_bf16 v[72:75], v[192:195], v[100:103], v[72:75]\n\t"
    "ds_read_b128 v[48:51], %[rw] offset:0\n\t"
    "v_mfma_f32_16x16x32_bf16 v[76:79], v[196:199], v[100:103], v[76:79]\n\t"
    "ds_read_b128 v[52:55], %[rw] offset:1024\n\t"
    "s_cmp_eq_u32 %[w0], 0\n\t"
    "s_cbranch_scc1 5f\n\t"
    "6:\n\t"
    "s_waitcnt lgkmcnt(6)\n\t"
    "v_mfma_f32_16x16x32_bf16 v[64:67], v[200:203], v[104:107], v[64:67]\n\t"
    "ds_read_b128 v[96:99], %[ea] offset:24576\n\t"
    "v_mfma_f32_16x16x32_bf16 v[68:71], v[204:207], v[104:107], v[68:71]\n\t"
    "ds_read_b128 v[100:103], %[ea] offset:26624\n\t"
    "v_mfma_f32_16x16x32_bf16 v[72:75], v[200:203], v[108:111], v[72:75]\n\t"
    "ds_read_b128 v[56:59], %[rw] offset:2048\n\t"
    "v_mfma_f32_16x16x32_bf16 v[76:79], v[204:207], v[108:111], v[76:79]\n\t"
    "ds_read_b128 v[60:63], %[rw] offset:3072\n\t"
    "s_waitcnt lgkmcnt(0)\n\t"
    "v_mfma_f32_16x16x32_bf16 v[64:67], v[208:211], v[112:115], v[64:67]\n\t"
    "ds_read_b128 v[104:107], %[da] offset:24576\n\t"
    "v_add_f32 v48, v48, v80\n\t"
    "v_add_f32 v49, v49, v81\n\t"
    "v_mfma_f32_16x16x32_bf16 v[68:71], v[212:215], v[112:115], v[68:71]\n\t"
    "ds_read_b128 v[108:111], %[da] offset:26624\n\t"
    "v_add_f32 v50, v50, v82\n\t"
    "v_mfma_f32_16x16x32_bf16 v[72:75], v[208:211], v[116:119], v[72:75]\n\t"
    "ds_read_b32 v39, %[sa]\n\t"
    "v_add_f32 v51, v51, v83\n\t"
    "v_mfma_f32_16x16x32_bf16 v[76:79], v[212:215], v[116:119], v[76:79]\n\t"
    "v_add_f32 v52, v52, v84\n\t"
    "v_add_f32 v53, v53, v85\n\t"
    "v_mfma_f32_16x16x32_bf16 v[64:67], v[216:219], v[120:123], v[64:67]\n\t"
    "ds_read_b128 v[112:115], %[ea] offset:28672\n\t"
    "v_add_f32 v54, v54, v86\n\t"
    "v_mfma_f32_16x16x32_bf16 v[68:71], v[220:223], v[120:123], v[68:71]\n\t"
    "ds_read_b128 v[116:119], %[ea] offset:30720\n\t"
    "v_add_f32 v55, v55, v87\n\t"
    "v_mfma_f32_16x16x32_bf16 v[72:75], v[216:219], v[124:127], v[72:75]\n\t"
    "v_xor_b32 %[ea], 0x10000, %[ea]\n\t"
    "v_add_f32 v56, v56, v88\n\t"
    "v_add_f32 v57, v57, v89\n\t"
    "v_mfma_f32_16x16x32_bf16 v[76:79], v[220:223], v[124:127], v[76:79]\n\t"
    "v_add_f32 v58, v58, v90\n\t"
    "v_mfma_f32_16x16x32_bf16 v[64:67], v[224:227], v[96:99], v[64:67]\n\t"
    "ds_read_b128 v[120:123], %[da] offset:28672\n\t"
    "v_add_f32 v59, v59, v91\n\t"
    "v_mfma_f32_16x16x32_bf16 v[68:71], v[228:231], v[96:99], v[68:71]\n\t"
    "ds_read_b128 v[124:127], %[da] offset:30720\n\t"
    "v_add_f32 v60, v60, v92\n\t"
    "v_mfma_f32_16x16x32_bf16 v[72:75], v[224:227], v[100:103], v[72:75]\n\t"
    "v_xor_b32 %[da], 0x10000, %[da]\n\t"
    "v_add_f32 v61, v61, v93\n\t"
    "v_add_f32 v62, v62, v94\n\t"
    "v_mfma_f32_16x16x32_bf16 v[76:79], v[228:231], v[100:103], v[76:79]\n\t"
    "v_add_f32 v63, v63, v95\n\t"
    "s_waitcnt lgkmcnt(5)\n\t"
    "v_mfma_f32_16x16x32_bf16 v[64:67], v[232:235], v[104:107], v[64:67]\n\t"
    "ds_read_b128 v[96:99], %[ea] offset:0\n\t"
    "v_cvt_pk_bf16_f32 v40, v48, v49\n\t"
    "v_mfma_f32_16x16x32_bf16 v[68:71], v[236:239], v[104:107], v[68:71]\n\t"
    "ds_read_b128 v[100:103], %[ea] offset:2048\n\t"
    "v_cvt_pk_bf16_f32 v41, v50, v51\n\t"
    "v_cvt_pk_bf16_f32 v42, v52, v53\n\t"
    "v_mfma_f32_16x16x32_bf16 v[72:75], v[232:235], v[108:111], v[72:75]\n\t"
    "v_cvt_pk_bf16_f32 v43, v54, v55\n\t"
    "v_mfma_f32_16x16x32_bf16 v[76:79], v[236:239], v[108:111], v[76:79]\n\t"
    "global_store_dwordx4 %[co0], v[40:43], s[36:37] sc1\n\t"
    "s_waitcnt lgkmcnt(4)\n\t"
    "v_mfma_f32_16x16x32_bf16 v[64:67], v[240:243], v[112:115], v[64:67]\n\t"
    "ds_read_b128 v[104:107], %[da] offset:0\n\t"
    "v_mad_u32_u24 v39, v39, %[mu], %[ad]\n\t"
    "v_cvt_pk_bf16_f32 v44, v56, v57\n\t"
    "v_cvt_pk_bf16_f32 v45, v58, v59\n\t"
    "v_mfma_f32_16x16x32_bf16 v[68:71], v[244:247], v[112:115], v[68:71]\n\t"
    "ds_read_b128 v[108:111], %[da] offset:2048\n\t"
    "v_cmp_ge_i32 vcc, %[lf], v39\n\t"
    "s_cmp_eq_u64 vcc, 0\n\t"
    "s_addc_u32 %[lf], %[lf], 0\n\t"
    "v_cvt_pk_bf16_f32 v46, v60, v61\n\t"
    "v_mfma_f32_16x16x32_bf16 v[72:75], v[240:243], v[116:119], v[72:75]\n\t"
    "v_cvt_pk_bf16_f32 v47, v62, v63\n\t"
    "v_mfma_f32_16x16x32_bf16 v[76:79], v[244:247], v[116:119], v[76:79]\n\t"
    "v_cmp_ge_i32 vcc, %[lf], v39\n\t"
    "s_cmp_eq_u64 vcc, 0\n\t"
    "s_addc_u32 %[lf], %[lf], 0\n\t"
    "global_store_dwordx4 %[co1], v[44:47], s[36:37] sc1\n\t"
    "s_waitcnt lgkmcnt(4)\n\t"
    "v_mfma_f32_16x16x32_bf16 v[80:83], v[248:251], v[120:123], v[64:67]\n\t"
    "ds_read_b128 v[112:115], %[ea] offset:4096\n\t"
    "s_min_i32 %[lf], %[lf], %[cp]\n\t"
    "s_add_u32 %[b], %[b], 1\n\t"
    "v_mfma_f32_16x16x32_bf16 v[84:87], v[252:255], v[120:123], v[68:71]\n\t"
    "ds_read_b128 v[116:119], %[ea] offset:6144\n\t"
    "v_mfma_f32_16x16x32_bf16 v[88:91], v[248:251], v[124:127], v[72:75]\n\t"
    "v_mfma_f32_16x16x32_bf16 v[92:95], v[252:255], v[124:127], v[76:79]\n\t"
    "s_mov_b64 s[36:37], s[38:39]\n\t"
    "s_add_u32 s38, s38, %[ab]\n\t"
    "s_addc_u32 s39, s39, 0\n\t"
    "s_sub_u32 m0, %[lf], %[b]\n\t"
    "s_sub_u32 m0, m0, 1\n\t"
    "s_cmp_lt_u32 m0, %[up]\n\t"
    "s_cbranch_scc1 1b\n\t"
    "s_waitcnt lgkmcnt(0)\n\t"
    "s_nop 4\n\t"
    "s_branch 9f\n\t"
    "5:\n\t"
    "s_add_u32 %[st], %[b], %[po]\n\t"
    "s_max_i32 %[st], %[st], %[p0]\n\t"
    "v_mov_b32 v39, %[st]\n\t"
    "v_mov_b32 v38, 0\n\t"
    "s_mov_b64 exec, 1\n\t"
    "global_store_dword v38, v39, %[pg] sc1\n\t"
    "s_mov_b64 exec, -1\n\t"
    "s_mov_b32 %[p0], %[p1]\n\t"
    "s_branch 6b\n\t"
    "7:\n\t"
    "s_cmp_ge_i32 %[lf], %[cp]\n\t"
    "s_cbranch_scc1 8b\n\t"
    "s_mov_b32 m0, %[sl]\n\t"
    "s_nop 0\n\t"
    "global_load_lds_dword %[dv], %[db] sc1\n\t"
    "s_branch 8b\n\t"
    "9:"
    : "=&{v[80:95]}"(pd), [ea] "+&v"(ea), [da] "+&v"(da), [b] "+&s"(b), [lf] "+&s"(lf), "+{s[36:37]}"(p1), "+{s[38:39]}"(p0), [sa] "+&v"(sa), [st] "=&s"(st), [sl] "+&s"(sl), [p0] "+&s"(pf)
    : [rw] "v"(rw), [ab] "s"(ab), [cp] "s"(cp), [mu] "s"(mu), [ad] "v"(ad), [up] "s"(up), "{v[128:159]}"(W[0]), "{v[160:191]}"(W[1]), "{v[192:223]}"(W[2]), "{v[224:255]}"(W[3]), [co0] "v"(co0), [w0] "s"(w0), [dv] "v"(dv), [db] "s"(db), [pg] "s"(pg), [po] "s"(po - 1), [p1] "s"(b + po), [co1] "v"(co1)
    : "v38", "v39", "v40", "v41", "v42", "v43", "v44", "v45", "v46", "v47", "v48", "v49", "v50", "v51", "v52", "v53", "v54", "v55", "v56", "v57", "v58", "v59", "v60", "v61", "v62", "v63", "v64", "v65", "v66", "v67", "v68", "v69", "v70", "v71", "v72", "v73", "v74", "v75", "v76", "v77", "v78", "v79", "v96", "v97", "v98", "v99", "v100", "v101", "v102", "v103", "v104", "v105", "v106", "v107", "v108", "v109", "v110", "v111", "v112", "v113", "v114", "v115", "v116", "v117", "v118", "v119", "v120", "v121", "v122", "v123", "v124", "v125", "v126", "v127", "vcc", "scc", "m0", "memory");
}
__device__ __forceinline__ void fast_st1(const u32x32 (&W)[4], u32x16& pd, unsigned ea, unsigned da, int& b, int& lf, int cp, unsigned up, unsigned sa, unsigned mu, unsigned ad,
    unsigned rw, unsigned ab, const char* p1, const char* p0, unsigned co0, unsigned co1, int po, int pf, unsigned w0, unsigned sl, unsigned dv, const char* db, const char* pg) {
  unsigned st;
  asm volatile("s_barrier\n\t"
    "ds_read_b128 v[96:99], %[ea] offset:0\n\t"
    "ds_read_b128 v[100:103], %[ea] offset:2048\n\t"
    "ds_read_b128 v[104:107], %[da] offset:0\n\t"
    "ds_read_b128 v[108:111], %[da] offset:2048\n\t"
    "ds_read_b128 v[112:115], %[ea] offset:4096\n\t"
    "ds_read_b128 v[116:119], %[ea] offset:6144\n\t"
    "s_branch 2f\n\t"
    "1:\n\t"
    "s_barrier\n\t"
    "2:\n\t"
    "s_waitcnt lgkmcnt(4)\n\t"
    "v_mfma_f32_16x16x32_bf16 v[64:67], v[128:131], v[96:99], 0\n\t"
    "ds_read_b128 v[120:123], %[da] offset:4096\n\t"
    "v_mfma_f32_16x16x32_bf16 v[68:71], v[132:135], v[96:99], 0\n\t"
    "ds_read_b128 v[124:127], %[da] offset:6144\n\t"
    "v_mfma_f32_16x16x32_bf16 v[72:75], v[128:131], v[100:103], 0\n\t"
    "v_mfma_f32_16x16x32_bf16 v[76:79], v[132:135], v[100:103], 0\n\t"
    "s_waitcnt lgkmcnt(4)\n\t"
    "v_mfma_f32_16x16x32_bf16 v[64:67], v[136:139], v[104:107], v[64:67]\n\t"
    "ds_read_b128 v[96:99], %[ea] offset:8192\n\t"
    "v_mfma_f32_16x16x32_bf16 v[68:71], v[140:143], v[104:107], v[68:71]\n\t"
    "ds_read_b128 v[100:103], %[ea] offset:10240\n\t"
    "v_mfma_f32_16x16x32_bf16 v[72:75], v[136:139], v[108:111], v[72:75]\n\t"
    "v_mfma_f32_16x16x32_bf16 v[76:79], v[140:143], v[108:111], v[76:79]\n\t"
    "s_cmp_eq_u32 %[w0], 1\n\t"
    "s_cbranch_scc1 7f\n\t"
    "8:\n\t"
    "s_waitcnt lgkmcnt(4)\n\t"
    "v_mfma_f32_16x16x32_bf16 v[64:67], v[144:147], v[112:115], v[64:67]\n\t"
    "ds_read_b128 v[104:107], %[da] offset:8192\n\t"
    "v_mfma_f32_16x16x32_bf16 v[68:71], v[148:151], v[112:115], v[68:71]\n\t"
    "ds_read_b128 v[108:111], %[da] offset:10240\n\t"
    "v_mfma_f32_16x16x32_bf16 v[72:75], v[144:147], v[116:119], v[72:75]\n\t"
    "v_mfma_f32_16x16x32_bf16 v[76:79], v[148:151], v[116:119], v[76:79]\n\t"
    "s_waitcnt lgkmcnt(4)\n\t"
    "v_mfma_f32_16x16x32_bf16 v[64:67], v[152:155], v[120:123], v[64:67]\n\t"
    "ds_read_b128 v[112:115], %[ea] offset:12288\n\t"
    "v_mfma_f32_16x16x32_bf16 v[68:71], v[156:159], v[120:123], v[68:71]\n\t"
    "ds_read_b128 v[116:119], %[ea] offset:14336\n\t"
    "v_mfma_f32_16x16x32_bf16 v[72:75], v[152:155], v[124:127], v[72:75]\n\t"
    "v_mfma_f32_16x16x32_bf16 v[76:79], v[156:159], v[124:127], v[76:79]\n\t"
    "s_waitcnt lgkmcnt(4)\n\t"
    "v_mfma_f32_16x16x32_bf16 v[64:67], v[160:163], v[96:99], v[64:67]\n\t"
    "ds_read_b128 v[120:123], %[da] offset:12288\n\t"
    "v_mfma_f32_16x16x32_bf16 v[68:71], v[164:167], v[96:99], v[68:71]\n\t"
    "ds_read_b128 v[124:127], %[da] offset:14336\n\t"
    "v_mfma_f32_16x16x32_bf16 v[72:75], v[160:163], v[100:103], v[72:75]\n\t"
    "v_mfma_f32_16x16x32_bf16 v[76:79], v[164:167], v[100:103], v[76:79]\n\t"
    "s_waitcnt lgkmcnt(4)\n\t"
    "v_mfma_f32_16x16x32_bf16 v[64:67], v[168:171], v[104:107], v[64:67]\n\t"
    "ds_read_b128 v[96:99], %[ea] offset:16384\n\t"
    "v_mfma_f32_16x16x32_bf16 v[68:71], v[172:175], v[104:107], v[68:71]\n\t"
    "ds_read_b128 v[100:103], %[ea] offset:18432\n\t"
    "v_mfma_f32_16x16x32_bf16 v[72:75], v[168:171], v[108:111], v[72:75]\n\t"
    "v_mfma_f32_16x16x32_bf16 v[76:79], v[172:175], v[108:111], v[76:79]\n\t"
    "s_waitcnt lgkmcnt(4)\n\t"
    "v_mfma_f32_16x16x32_bf16 v[64:67], v[176:179], v[112:115], v[64:67]\n\t"
    "ds_read_b128 v[104:107], %[da] offset:16384\n\t"
    "v_mfma_f32_16x16x32_bf16 v[68:71], v[180:183], v[112:115], v[68:71]\n\t"
    "ds_read_b128 v[108:111], %[da] offset:18432\n\t"
    "v_mfma_f32_16x16x32_bf16 v[72:75], v[176:179], v[116:119], v[72:75]\n\t"
    "v_mfma_f32_16x16x32_bf16 v[76:79], v[180:183], v[116:119], v[76:79]\n\t"
    "s_waitcnt lgkmcnt(4)\n\t"
    "v_mfma_f32_16x16x32_bf16 v[64:67], v[184:187], v[120:123], v[64:67]\n\t"
    "ds_read_b128 v[112:115], %[ea] offset:20480\n\t"
    "v_mfma_f32_16x16x32_bf16 v[68:71], v[188:191], v[120:123], v[68:71]\n\t"
    "ds_read_b128 v[116:119], %[ea] offset:22528\n\t"
    "v_mfma_f32_16x16x32_bf16 v[72:75], v[184:187], v[124:127], v[72:75]\n\t"
    "v_mfma_f32_16x16x32_bf16 v[76:79], v[188:191], v[124:127], v[76:79]\n\t"
    "s_waitcnt vmcnt(0)\n\t"
    "s_barrier\n\t"
    "s_waitcnt lgkmcnt(4)\n\t"
    "v_mfma_f32_16x16x32_bf16 v[64:67], v[192:195], v[96:99], v[64:67]\n\t"
    "ds_read_b128 v[120:123], %[da] offset:20480\n\t"
    "v_mfma_f32_16x16x32_bf16 v[68:71], v[196:199], v[96:99], v[68:71]\n\t"
    "ds_read_b128 v[124:127], %[da] offset:22528\n\t"
    "v_mfma_f32_16x16x32_bf16 v[72:75], v[192:195], v[100:103], v[72:75]\n\t"
    "ds_read_b128 v[48:51], %[rw] offset:0\n\t"
    "v_mfma_f32_16x16x32_bf16 v[76:79], v[196:199], v[100:103], v[76:79]\n\t"
    "ds_read_b128 v[52:55], %[rw] offset:1024\n\t"
    "s_cmp_eq_u32 %[w0], 0\n\t"
    "s_cbranch_scc1 5f\n\t"
    "6:\n\t"
    "s_waitcnt lgkmcnt(6)\n\t"
    "v_mfma_f32_16x16x32_bf16 v[64:67], v[200:203], v[104:107], v[64:67]\n\t"
    "ds_read_b128 v[96:99], %[ea] offset:24576\n\t"
    "v_mfma_f32_16x16x32_bf16 v[68:71], v[204:207], v[104:107], v[68:71]\n\t"
    "ds_read_b128 v[100:103], %[ea] offset:26624\n\t"
    "v_mfma_f32_16x16x32_bf16 v[72:75], v[200:203], v[108:111], v[72:75]\n\t"
    "ds_read_b128 v[56:59], %[rw] offset:2048\n\t"
    "v_mfma_f32_16x16x32_bf16 v[76:79], v[204:207], v[108:111], v[76:79]\n\t"
    "ds_read_b128 v[60:63], %[rw] offset:3072\n\t"
    "s_waitcnt lgkmcnt(0)\n\t"
    "v_mfma_f32_16x16x32_bf16 v[64:67], v[208:211], v[112:115], v[64:67]\n\t"
    "ds_read_b128 v[104:107], %[da] offset:24576\n\t"
    "v_add_f32 v48, v48, v80\n\t"
    "v_add_f32 v49, v49, v81\n\t"
    "v_add_f32 v50, v50, v82\n\t"
    "v_mfma_f32_16x16x32_bf16 v[68:71], v[212:215], v[112:115], v[68:71]\n\t"
    "ds_read_b128 v[108:111], %[da] offset:26624\n\t"
    "v_add_f32 v51, v51, v83\n\t"
    "v_add_f32 v52, v52, v84\n\t"
    "v_add_f32 v53, v53, v85\n\t"
    "v_mfma_f32_16x16x32_bf16 v[72:75], v[208:211], v[116:119], v[72:75]\n\t"
    "ds_read_b32 v39, %[sa]\n\t"
    "v_add_f32 v54, v54, v86\n\t"
    "v_add_f32 v55, v55, v87\n\t"
    "v_add_f32 v56, v56, v88\n\t"
    "v_mfma_f32_16x16x32_bf16 v[76:79], v[212:215], v[116:119], v[76:79]\n\t"
    "v_add_f32 v57, v57, v89\n\t"
    "v_add_f32 v58, v58, v90\n\t"
    "v_add_f32 v59, v59, v91\n\t"
    "v_mfma_f32_16x16x32_bf16 v[64:67], v[216:219], v[120:123], v[64:67]\n\t"
    "ds_read_b128 v[112:115], %[ea] offset:28672\n\t"
    "v_add_f32 v60, v60, v92\n\t"
    "v_add_f32 v61, v61, v93\n\t"
    "v_add_f32 v62, v62, v94\n\t"
    "v_mfma_f32_16x16x32_bf16 v[68:71], v[220:223], v[120:123], v[68:71]\n\t"
    "ds_read_b128 v[116:119], %[ea] offset:30720\n\t"
    "v_add_f32 v63, v63, v95\n\t"
    "v_mul_f32 v40, %[nl], v48\n\t"
    "v_mul_f32 v41, %[nl], v49\n\t"
    "v_mfma_f32_16x16x32_bf16 v[72:75], v[216:219], v[124:127], v[72:75]\n\t"
    "v_xor_b32 %[ea], 0x10000, %[ea]\n\t"
    "v_mul_f32 v42, %[nl], v50\n\t"
    "v_mul_f32 v43, %[nl], v51\n\t"
    "v_mul_f32 v44, %[nl], v56\n\t"
    "v_mfma_f32_16x16x32_bf16 v[76:79], v[220:223], v[124:127], v[76:79]\n\t"
    "v_mul_f32 v45, %[nl], v57\n\t"
    "v_mul_f32 v46, %[nl], v58\n\t"
    "v_mul_f32 v47, %[nl], v59\n\t"
    "v_mfma_f32_16x16x32_bf16 v[64:67], v[224:227], v[96:99], v[64:67]\n\t"
    "ds_read_b128 v[120:123], %[da] offset:28672\n\t"
    "v_exp_f32 v40, v40\n\t"
    "v_exp_f32 v41, v41\n\t"
    "v_exp_f32 v42, v42\n\t"
    "v_mfma_f32_16x16x32_bf16 v[68:71], v[228:231], v[96:99], v[68:71]\n\t"
    "ds_read_b128 v[124:127], %[da] offset:30720\n\t"
    "v_exp_f32 v43, v43\n\t"
    "v_exp_f32 v44, v44\n\t"
    "v_exp_f32 v45, v45\n\t"
    "v_mfma_f32_16x16x32_bf16 v[72:75], v[224:227], v[100:103], v[72:75]\n\t"
    "v_xor_b32 %[da], 0x10000, %[da]\n\t"
    "v_exp_f32 v46, v46\n\t"
    "v_exp_f32 v47, v47\n\t"
    "v_add_f32 v40, 1.0, v40\n\t"
    "v_mfma_f32_16x16x32_bf16 v[76:79], v[228:231], v[100:103], v[76:79]\n\t"
    "v_add_f32 v41, 1.0, v41\n\t"
    "v_add_f32 v42, 1.0, v42\n\t"
    "v_add_f32 v43, 1.0, v43\n\t"
    "s_waitcnt lgkmcnt(5)\n\t"
    "v_mfma_f32_16x16x32_bf16 v[64:67], v[232:235], v[104:107], v[64:67]\n\t"
    "ds_read_b128 v[96:99], %[ea] offset:0\n\t"
    "v_add_f32 v44, 1.0, v44\n\t"
    "v_add_f32 v45, 1.0, v45\n\t"
    "v_add_f32 v46, 1.0, v46\n\t"
    "v_mfma_f32_16x16x32_bf16 v[68:71], v[236:239], v[104:107], v[68:71]\n\t"
    "ds_read_b128 v[100:103], %[ea] offset:2048\n\t"
    "v_add_f32 v47, 1.0, v47\n\t"
    "v_rcp_f32 v40, v40\n\t"
    "v_rcp_f32 v41, v41\n\t"
    "v_mfma_f32_16x16x32_bf16 v[72:75], v[232:235], v[108:111], v[72:75]\n\t"
    "v_rcp_f32 v42, v42\n\t"
    "v_rcp_f32 v43, v43\n\t"
    "v_rcp_f32 v44, v44\n\t"
    "v_mfma_f32_16x16x32_bf16 v[76:79], v[236:239], v[108:111], v[76:79]\n\t"
    "v_rcp_f32 v45, v45\n\t"
    "v_rcp_f32 v46, v46\n\t"
    "v_rcp_f32 v47, v47\n\t"
    "s_waitcnt lgkmcnt(4)\n\t"
    "v_mfma_f32_16x16x32_bf16 v[64:67], v[240:243], v[112:115], v[64:67]\n\t"
    "ds_read_b128 v[104:107], %[da] offset:0\n\t"
    "v_mad_u32_u24 v39, v39, %[mu], %[ad]\n\t"
    "v_mul_f32 v40, v48, v40\n\t"
    "v_mul_f32 v41, v49, v41\n\t"
    "v_mul_f32 v42, v50, v42\n\t"
    "v_mfma_f32_16x16x32_bf16 v[68:71], v[244:247], v[112:115], v[68:71]\n\t"
    "ds_read_b128 v[108:111], %[da] offset:2048\n\t"
    "v_cmp_ge_i32 vcc, %[lf], v39\n\t"
    "s_cmp_eq_u64 vcc, 0\n\t"
    "s_addc_u32 %[lf], %[lf], 0\n\t"
    "v_mul_f32 v43, v51, v43\n\t"
    "v_mul_f32 v44, v56, v44\n\t"
    "v_mul_f32 v45, v57, v45\n\t"
    "v_mfma_f32_16x16x32_bf16 v[72:75], v[240:243], v[116:119], v[72:75]\n\t"
    "v_mul_f32 v46, v58, v46\n\t"
    "v_mul_f32 v47, v59, v47\n\t"
    "v_mul_f32 v40, v40, v52\n\t"
    "v_mfma_f32_16x16x32_bf16 v[76:79], v[244:247], v[116:119], v[76:79]\n\t"
    "v_cmp_ge_i32 vcc, %[lf], v39\n\t"
    "s_cmp_eq_u64 vcc, 0\n\t"
    "s_addc_u32 %[lf], %[lf], 0\n\t"
    "v_mul_f32 v41, v41, v53\n\t"
    "v_mul_f32 v42, v42, v54\n\t"
    "v_mul_f32 v43, v43, v55\n\t"
    "s_waitcnt lgkmcnt(4)\n\t"
    "v_mfma_f32_16x16x32_bf16 v[80:83], v[248:251], v[120:123], v[64:67]\n\t"
    "ds_read_b128 v[112:115], %[ea] offset:4096\n\t"
    "s_min_i32 %[lf], %[lf], %[cp]\n\t"
    "s_add_u32 %[b], %[b], 1\n\t"
    "v_mul_f32 v44, v44, v60\n\t"
    "v_mul_f32 v45, v45, v61\n\t"
    "v_mul_f32 v46, v46, v62\n\t"
    "v_mfma_f32_16x16x32_bf16 v[84:87], v[252:255], v[120:123], v[68:71]\n\t"
    "ds_read_b128 v[116:119], %[ea] offset:6144\n\t"
    "v_mul_f32 v47, v47, v63\n\t"
    "v_cvt_pk_bf16_f32 v48, v40, v41\n\t"
    "v_cvt_pk_bf16_f32 v49, v42, v43\n\t"
    "v_mfma_f32_16x16x32_bf16 v[88:91], v[248:251], v[124:127], v[72:75]\n\t"
    "v_cvt_pk_bf16_f32 v50, v44, v45\n\t"
    "v_cvt_pk_bf16_f32 v51, v46, v47\n\t"
    "s_nop 1\n\t"
    "v_mfma_f32_16x16x32_bf16 v[92:95], v[252:255], v[124:127], v[76:79]\n\t"
    "v_permlane16_swap_b32 v48, v50\n\t"
    "v_permlane16_swap_b32 v49, v51\n\t"
    "global_store_dwordx4 %[co0], v[48:51], s[36:37] sc1\n\t"
    "s_mov_b64 s[36:37], s[38:39]\n\t"
    "s_add_u32 s38, s38, %[ab]\n\t"
    "s_addc_u32 s39, s39, 0\n\t"
    "s_sub_u32 m0, %[lf], %[b]\n\t"
    "s_sub_u32 m0, m0, 1\n\t"
    "s_cmp_lt_u32 m0, %[up]\n\t"
    "s_cbranch_scc1 1b\n\t"
    "s_waitcnt lgkmcnt(0)\n\t"
    "s_nop 4\n\t"
    "s_branch 9f\n\t"
    "5:\n\t"
    "s_add_u32 %[st], %[b], %[po]\n\t"
    "s_max_i32 %[st], %[st], %[p0]\n\t"
    "v_mov_b32 v39, %[st]\n\t"
    "v_mov_b32 v38, 0\n\t"
    "s_mov_b64 exec, 1\n\t"
    "global_store_dword v38, v39, %[pg] sc1\n\t"
    "s_mov_b64 exec, -1\n\t"
    "s_mov_b32 %[p0], %[p1]\n\t"
    "s_branch 6b\n\t"
    "7:\n\t"
    "s_cmp_ge_i32 %[lf], %[cp]\n\t"
    "s_cbranch_scc1 8b\n\t"
    "s_mov_b32 m0, %[sl]\n\t"
    "s_nop 0\n\t"
    "global_load_lds_dword %[dv], %[db] sc1\n\t"
    "s_branch 8b\n\t"
    "9:"
    : "=&{v[80:95]}"(pd), [ea] "+&v"(ea), [da] "+&v"(da), [b] "+&s"(b), [lf] "+&s"(lf), "+{s[36:37]}"(p1), "+{s[38:39]}"(p0), [sa] "+&v"(sa), [st] "=&s"(st), [sl] "+&s"(sl), [p0] "+&s"(pf)
    : [rw] "v"(rw), [ab] "s"(ab), [cp] "s"(cp), [mu] "s"(mu), [ad] "v"(ad), [up] "s"(up), "{v[128:159]}"(W[0]), "{v[160:191]}"(W[1]), "{v[192:223]}"(W[2]), "{v[224:255]}"(W[3]), [co0] "v"(co0), [w0] "s"(w0), [dv] "v"(dv), [db] "s"(db), [pg] "s"(pg), [po] "s"(po - 1), [p1] "s"(b + po), [nl] "s"(-LOG2E)
    : "v38", "v39", "v40", "v41", "v42", "v43", "v44", "v45", "v46", "v47", "v48", "v49", "v50", "v51", "v52", "v53", "v54", "v55", "v56", "v57", "v58", "v59", "v60", "v61", "v62", "v63", "v64", "v65", "v66", "v67", "v68", "v69", "v70", "v71", "v72", "v73", "v74", "v75", "v76", "v77", "v78", "v79", "v96", "v97", "v98", "v99", "v100", "v101", "v102", "v103", "v104", "v105", "v106", "v107", "v108", "v109", "v110", "v111", "v112", "v113", "v114", "v115", "v116", "v117", "v118", "v119", "v120", "v121", "v122", "v123", "v124", "v125", "v126", "v127", "vcc", "scc", "m0", "memory");
}
__device__ __forceinline__ void fast_st2(const u32x32 (&W)[4], u32x16& pd, unsigned ea, unsigned da, int& b, int& lf, int cp, unsigned up, unsigned sa, unsigned mu, unsigned ad,
    unsigned rw, unsigned ab, const char* p1, const char* p0, unsigned co0, unsigned co1, int po, int pf, unsigned w0, unsigned sl, unsigned dv, const char* db, const char* pg) {
  unsigned st;
  asm volatile("s_barrier\n\t"
    "ds_read_b128 v[96:99], %[ea] offset:0\n\t"
    "ds_read_b128 v[100:103], %[ea] offset:2048\n\t"
    "ds_read_b128 v[104:107], %[da] offset:0\n\t"
    "ds_read_b128 v[108:111], %[da] offset:2048\n\t"
    "ds_read_b128 v[112:115], %[ea] offset:4096\n\t"
    "ds_read_b128 v[116:119], %[ea] offset:6144\n\t"
    "s_branch 2f\n\t"
    "1:\n\t"
    "s_barrier\n\t"
    "2:\n\t"
    "s_waitcnt lgkmcnt(4)\n\t"
    "v_mfma_f32_16x16x32_bf16 v[64:67], v[128:131], v[96:99], 0\n\t"
    "ds_read_b128 v[120:123], %[da] offset:4096\n\t"
    "v_mfma_f32_16x16x32_bf16 v[68:71], v[132:135], v[96:99], 0\n\t"
    "ds_read_b128 v[124:127], %[da] offset:6144\n\t"
    "v_mfma_f32_16x16x32_bf16 v[72:75], v[128:131], v[100:103], 0\n\t"
    "v_mfma_f32_16x16x32_bf16 v[76:79], v[132:135], v[100:103], 0\n\t"
    "s_waitcnt lgkmcnt(4)\n\t"
    "v_mfma_f32_16x16x32_bf16 v[64:67], v[136:139], v[104:107], v[64:67]\n\t"
    "ds_read_b128 v[96:99], %[ea] offset:8192\n\t"
    "v_mfma_f32_16x16x32_bf16 v[68:71], v[140:143], v[104:107], v[68:71]\n\t"
    "ds_read_b128 v[100:103], %[ea] offset:10240\n\t"
    "v_mfma_f32_16x16x32_bf16 v[72:75], v[136:139], v[108:111], v[72:75]\n\t"
    "v_mfma_f32_16x16x32_bf16 v[76:79], v[140:143], v[108:111], v[76:79]\n\t"
    "s_cmp_eq_u32 %[w0], 1\n\t"
    "s_cbranch_scc1 7f\n\t"
    "8:\n\t"
    "s_waitcnt lgkmcnt(4)\n\t"
    "v_mfma_f32_16x16x32_bf16 v[64:67], v[144:147], v[112:115], v[64:67]\n\t"
    "ds_read_b128 v[104:107], %[da] offset:8192\n\t"
    "v_mfma_f32_16x16x32_bf16 v[68:71], v[148:151], v[112:115], v[68:71]\n\t"
    "ds_read_b128 v[108:111], %[da] offset:10240\n\t"
    "v_mfma_f32_16x16x32_bf16 v[72:75], v[144:147], v[116:119], v[72:75]\n\t"
    "v_mfma_f32_16x16x32_bf16 v[76:79], v[148:151], v[116:119], v[76:79]\n\t"
    "s_waitcnt lgkmcnt(4)\n\t"
    "v_mfma_f32_16x16x32_bf16 v[64:67], v[152:155], v[120:123], v[64:67]\n\t"
    "ds_read_b128 v[112:115], %[ea] offset:12288\n\t"
    "v_mfma_f32_16x16x32_bf16 v[68:71], v[156:159], v[120:123], v[68:71]\n\t"
    "ds_read_b128 v[116:119], %[ea] offset:14336\n\t"
    "v_mfma_f32_16x16x32_bf16 v[72:75], v[152:155], v[124:127], v[72:75]\n\t"
    "v_mfma_f32_16x16x32_bf16 v[76:79], v[156:159], v[124:127], v[76:79]\n\t"
    "s_waitcnt lgkmcnt(4)\n\t"
    "v_mfma_f32_16x16x32_bf16 v[64:67], v[160:163], v[96:99], v[64:67]\n\t"
    "ds_read_b128 v[120:123], %[da] offset:12288\n\t"
    "v_mfma_f32_16x16x32_bf16 v[68:71], v[164:167], v[96:99], v[68:71]\n\t"
    "ds_read_b128 v[124:127], %[da] offset:14336\n\t"
    "v_mfma_f32_16x16x32_bf16 v[72:75], v[160:163], v[100:103], v[72:75]\n\t"
    "v_mfma_f32_16x16x32_bf16 v[76:79], v[164:167], v[100:103], v[76:79]\n\t"
    "s_waitcnt lgkmcnt(4)\n\t"
    "v_mfma_f32_16x16x32_bf16 v[64:67], v[168:171], v[104:107], v[64:67]\n\t"
    "ds_read_b128 v[96:99], %[ea] offset:16384\n\t"
    "v_mfma_f32_16x16x32_bf16 v[68:71], v[172:175], v[104:107], v[68:71]\n\t"
    "ds_read_b128 v[100:103], %[ea] offset:18432\n\t"
    "v_mfma_f32_16x16x32_bf16 v[72:75], v[168:171], v[108:111], v[72:75]\n\t"
    "v_mfma_f32_16x16x32_bf16 v[76:79], v[172:175], v[108:111], v[76:79]\n\t"
    "s_waitcnt lgkmcnt(4)\n\t"
    "v_mfma_f32_16x16x32_bf16 v[64:67], v[176:179], v[112:115], v[64:67]\n\t"
    "ds_read_b128 v[104:107], %[da] offset:16384\n\t"
    "v_mfma_f32_16x16x32_bf16 v[68:71], v[180:183], v[112:115], v[68:71]\n\t"
    "ds_read_b128 v[108:111], %[da] offset:18432\n\t"
    "v_mfma_f32_16x16x32_bf16 v[72:75], v[176:179], v[116:119], v[72:75]\n\t"
    "v_mfma_f32_16x16x32_bf16 v[76:79], v[180:183], v[116:119], v[76:79]\n\t"
    "s_waitcnt lgkmcnt(4)\n\t"
    "v_mfma_f32_16x16x32_bf16 v[64:67], v[184:187], v[120:123], v[64:67]\n\t"
    "ds_read_b128 v[112:115], %[ea] offset:20480\n\t"
    "v_mfma_f32_16x16x32_bf16 v[68:71], v[188:191], v[120:123], v[68:71]\n\t"
    "ds_read_b128 v[116:119], %[ea] offset:22528\n\t"
    "v_mfma_f32_16x16x32_bf16 v[72:75], v[184:187], v[124:127], v[72:75]\n\t"
    "v_mfma_f32_16x16x32_bf16 v[76:79], v[188:191], v[124:127], v[76:79]\n\t"
    "s_waitcnt vmcnt(0)\n\t"
    "s_barrier\n\t"
    "s_waitcnt lgkmcnt(4)\n\t"
    "v_mfma_f32_16x16x32_bf16 v[64:67], v[192:195], v[96:99], v[64:67]\n\t"
    "ds_read_b128 v[120:123], %[da] offset:20480\n\t"
    "v_mfma_f32_16x16x32_bf16 v[68:71], v[196:199], v[96:99], v[68:71]\n\t"
    "ds_read_b128 v[124:127], %[da] offset:22528\n\t"
    "v_mfma_f32_16x16x32_bf16 v[72:75], v[192:195], v[100:103], v[72:75]\n\t"
    "ds_read_b128 v[48:51], %[rw] offset:0\n\t"
    "v_mfma_f32_16x16x32_bf16 v[76:79], v[196:199], v[100:103], v[76:79]\n\t"
    "ds_read_b128 v[52:55], %[rw] offset:1024\n\t"
    "s_cmp_eq_u32 %[w0], 0\n\t"
    "s_cbranch_scc1 5f\n\t"
    "6:\n\t"
    "s_waitcnt lgkmcnt(6)\n\t"
    "v_mfma_f32_16x16x32_bf16 v[64:67], v[200:203], v[104:107], v[64:67]\n\t"
    "ds_read_b128 v[96:99], %[ea] offset:24576\n\t"
    "v_mfma_f32_16x16x32_bf16 v[68:71], v[204:207], v[104:107], v[68:71]\n\t"
    "ds_read_b128 v[100:103], %[ea] offset:26624\n\t"
    "v_mfma_f32_16x16x32_bf16 v[72:75], v[200:203], v[108:111], v[72:75]\n\t"
    "ds_read_b128 v[56:59], %[rw] offset:2048\n\t"
    "v_mfma_f32_16x16x32_bf16 v[76:79], v[204:207], v[108:111], v[76:79]\n\t"
    "ds_read_b128 v[60:63], %[rw] offset:3072\n\t"
    "s_waitcnt lgkmcnt(0)\n\t"
    "v_mfma_f32_16x16x32_bf16 v[64:67], v[208:211], v[112:115], v[64:67]\n\t"
    "ds_read_b128 v[104:107], %[da] offset:24576\n\t"
    "v_cvt_pk_bf16_f32 v40, v80, v81\n\t"
    "v_mfma_f32_16x16x32_bf16 v[68:71], v[212:215], v[112:115], v[68:71]\n\t"
    "ds_read_b128 v[108:111], %[da] offset:26624\n\t"
    "v_cvt_pk_bf16_f32 v41, v82, v83\n\t"
    "v_mfma_f32_16x16x32_bf16 v[72:75], v[208:211], v[116:119], v[72:75]\n\t"
    "ds_read_b32 v39, %[sa]\n\t"
    "v_cvt_pk_bf16_f32 v42, v84, v85\n\t"
    "v_mfma_f32_16x16x32_bf16 v[76:79], v[212:215], v[116:119], v[76:79]\n\t"
    "v_cvt_pk_bf16_f32 v43, v86, v87\n\t"
    "v_mfma_f32_16x16x32_bf16 v[64:67], v[216:219], v[120:123], v[64:67]\n\t"
    "ds_read_b128 v[112:115], %[ea] offset:28672\n\t"
    "global_store_dwordx4 %[co0], v[40:43], s[36:37] sc1\n\t"
    "v_mfma_f32_16x16x32_bf16 v[68:71], v[220:223], v[120:123], v[68:71]\n\t"
    "ds_read_b128 v[116:119], %[ea] offset:30720\n\t"
    "v_cvt_pk_bf16_f32 v44, v88, v89\n\t"
    "v_mfma_f32_16x16x32_bf16 v[72:75], v[216:219], v[124:127], v[72:75]\n\t"
    "v_xor_b32 %[ea], 0x10000, %[ea]\n\t"
    "v_cvt_pk_bf16_f32 v45, v90, v91\n\t"
    "v_mfma_f32_16x16x32_bf16 v[76:79], v[220:223], v[124:127], v[76:79]\n\t"
    "v_cvt_pk_bf16_f32 v46, v92, v93\n\t"
    "v_mfma_f32_16x16x32_bf16 v[64:67], v[224:227], v[96:99], v[64:67]\n\t"
    "ds_read_b128 v[120:123], %[da] offset:28672\n\t"
    "v_cvt_pk_bf16_f32 v47, v94, v95\n\t"
    "v_mfma_f32_16x16x32_bf16 v[68:71], v[228:231], v[96:99], v[68:71]\n\t"
    "ds_read_b128 v[124:127], %[da] offset:30720\n\t"
    "global_store_dwordx4 %[co1], v[44:47], s[36:37] sc1\n\t"
    "v_mfma_f32_16x16x32_bf16 v[72:75], v[224:227], v[100:103], v[72:75]\n\t"
    "v_xor_b32 %[da], 0x10000, %[da]\n\t"
    "v_cvt_pk_bf16_f32 v48, v48, v49\n\t"
    "v_mfma_f32_16x16x32_bf16 v[76:79], v[228:231], v[100:103], v[76:79]\n\t"
    "v_cvt_pk_bf16_f32 v49, v50, v51\n\t"
    "s_waitcnt lgkmcnt(5)\n\t"
    "v_mfma_f32_16x16x32_bf16 v[64:67], v[232:235], v[104:107], v[64:67]\n\t"
    "ds_read_b128 v[96:99], %[ea] offset:0\n\t"
    "v_cvt_pk_bf16_f32 v50, v52, v53\n\t"
    "v_mfma_f32_16x16x32_bf16 v[68:71], v[236:239], v[104:107], v[68:71]\n\t"
    "ds_read_b128 v[100:103], %[ea] offset:2048\n\t"
    "v_cvt_pk_bf16_f32 v51, v54, v55\n\t"
    "v_mfma_f32_16x16x32_bf16 v[72:75], v[232:235], v[108:111], v[72:75]\n\t"
    "global_store_dwordx4 %[co0], v[48:51], s[36:37] offset:2048 sc1\n\t"
    "v_mfma_f32_16x16x32_bf16 v[76:79], v[236:239], v[108:111], v[76:79]\n\t"
    "v_cvt_pk_bf16_f32 v56, v56, v57\n\t"
    "s_waitcnt lgkmcnt(4)\n\t"
    "v_mfma_f32_16x16x32_bf16 v[64:67], v[240:243], v[112:115], v[64:67]\n\t"
    "ds_read_b128 v[104:107], %[da] offset:0\n\t"
    "v_mad_u32_u24 v39, v39, %[mu], %[ad]\n\t"
    "v_cvt_pk_bf16_f32 v57, v58, v59\n\t"
    "v_mfma_f32_16x16x32_bf16 v[68:71], v[244:247], v[112:115], v[68:71]\n\t"
    "ds_read_b128 v[108:111], %[da] offset:2048\n\t"
    "v_cmp_ge_i32 vcc, %[lf], v39\n\t"
    "s_cmp_eq_u64 vcc, 0\n\t"
    "s_addc_u32 %[lf], %[lf], 0\n\t"
    "v_cvt_pk_bf16_f32 v58, v60, v61\n\t"
    "v_mfma_f32_16x16x32_bf16 v[72:75], v[240:243], v[116:119], v[72:75]\n\t"
    "v_cvt_pk_bf16_f32 v59, v62, v63\n\t"
    "v_mfma_f32_16x16x32_bf16 v[76:79], v[244:247], v[116:119], v[76:79]\n\t"
    "v_cmp_ge_i32 vcc, %[lf], v39\n\t"
    "s_cmp_eq_u64 vcc, 0\n\t"
    "s_addc_u32 %[lf], %[lf], 0\n\t"
    "global_store_dwordx4 %[co1], v[56:59], s[36:37] offset:2048 sc1\n\t"
    "s_waitcnt lgkmcnt(4)\n\t"
    "v_mfma_f32_16x16x32_bf16 v[80:83], v[248:251], v[120:123], v[64:67]\n\t"
    "ds_read_b128 v[112:115], %[ea] offset:4096\n\t"
    "s_min_i32 %[lf], %[lf], %[cp]\n\t"
    "s_add_u32 %[b], %[b], 1\n\t"
    "v_mfma_f32_16x16x32_bf16 v[84:87], v[252:255], v[120:123], v[68:71]\n\t"
    "ds_read_b128 v[116:119], %[ea] offset:6144\n\t"
    "v_mfma_f32_16x16x32_bf16 v[88:91], v[248:251], v[124:127], v[72:75]\n\t"
    "v_mfma_f32_16x16x32_bf16 v[92:95], v[252:255], v[124:127], v[76:79]\n\t"
    "s_mov_b64 s[36:37], s[38:39]\n\t"
    "s_add_u32 s38, s38, %[ab]\n\t"
    "s_addc_u32 s39, s39, 0\n\t"
    "s_sub_u32 m0, %[lf], %[b]\n\t"
    "s_sub_u32 m0, m0, 1\n\t"
    "s_cmp_lt_u32 m0, %[up]\n\t"
    "s_cbranch_scc1 1b\n\t"
    "s_waitcnt lgkmcnt(0)\n\t"
    "s_nop 4\n\t"
    "s_branch 9f\n\t"
    "5:\n\t"
    "s_add_u32 %[st], %[b], %[po]\n\t"
    "s_max_i32 %[st], %[st], %[p0]\n\t"
    "v_mov_b32 v39, %[st]\n\t"
    "v_mov_b32 v38, 0\n\t"
    "s_mov_b64 exec, 1\n\t"
    "global_store_dword v38, v39, %[pg] sc1\n\t"
    "s_mov_b64 exec, -1\n\t"
    "s_mov_b32 %[p0], %[p1]\n\t"
    "s_branch 6b\n\t"
    "7:\n\t"
    "s_cmp_ge_i32 %[lf], %[cp]\n\t"
    "s_cbranch_scc1 8b\n\t"
    "s_mov_b32 m0, %[sl]\n\t"
    "s_nop 0\n\t"
    "global_load_lds_dword %[dv], %[db] sc1\n\t"
    "s_branch 8b\n\t"
    "9:"
    : "=&{v[80:95]}"(pd), [ea] "+&v"(ea), [da] "+&v"(da), [b] "+&s"(b), [lf] "+&s"(lf), "+{s[36:37]}"(p1), "+{s[38:39]}"(p0), [sa] "+&v"(sa), [st] "=&s"(st), [sl] "+&s"(sl), [p0] "+&s"(pf)
    : [rw] "v"(rw), [ab] "s"(ab), [cp] "s"(cp), [mu] "s"(mu), [ad] "v"(ad), [up] "s"(up), "{v[128:159]}"(W[0]), "{v[160:191]}"(W[1]), "{v[192:223]}"(W[2]), "{v[224:255]}"(W[3]), [co0] "v"(co0), [w0] "s"(w0), [dv] "v"(dv), [db] "s"(db), [pg] "s"(pg), [po] "s"(po - 1), [p1] "s"(b + po), [co1] "v"(co1)
    : "v38", "v39", "v40", "v41", "v42", "v43", "v44", "v45", "v46", "v47", "v48", "v49", "v50", "v51", "v52", "v53", "v54", "v55", "v56", "v57", "v58", "v59", "v60", "v61", "v62", "v63", "v64", "v65", "v66", "v67", "v68", "v69", "v70", "v71", "v72", "v73", "v74", "v75", "v76", "v77", "v78", "v79", "v96", "v97", "v98", "v99", "v100", "v101", "v102", "v103", "v104", "v105", "v106", "v107", "v108", "v109", "v110", "v111", "v112", "v113", "v114", "v115", "v116", "v117", "v118", "v119", "v120", "v121", "v122", "v123", "v124", "v125", "v126", "v127", "vcc", "scc", "m0", "memory");
}
__device__ __forceinline__ void lean_ld0(const u32x32 (&W)[4], unsigned ea, unsigned da, unsigned x, unsigned lb, const char* s0, const char* s1, const unsigned (&av)[8], unsigned ps, unsigned dv, const char* db, unsigned rw, unsigned fa, unsigned fv,
    unsigned ga, unsigned mu, unsigned ad, int th, int tb, unsigned& sv, unsigned& su, unsigned& got) {
  asm volatile("s_setprio 2\n\t"
    "v_mov_b32 v76, %[ga]\n\t"
    "ds_read_b32 v76, v76 offset:24\n\t"
    "ds_read_b128 v[96:99], %[ea] offset:0\n\t"
    "ds_read_b128 v[100:103], %[ea] offset:2048\n\t"
    "ds_read_b128 v[104:107], %[da] offset:0\n\t"
    "ds_read_b128 v[108:111], %[da] offset:2048\n\t"
    "ds_read_b128 v[112:115], %[ea] offset:4096\n\t"
    "ds_read_b128 v[116:119], %[ea] offset:6144\n\t"
    "s_waitcnt lgkmcnt(6)\n\t"
    "v_cmp_ne_u32 vcc, 0, v76\n\t"
    "s_mov_b32 %[got], -1\n\t"
    "s_cbranch_vccnz 12f\n\t"
    "s_waitcnt lgkmcnt(4)\n\t"
    "v_mfma_f32_16x16x32_bf16 v[80:83], v[128:131], v[96:99], 0\n\t"
    "ds_read_b128 v[120:123], %[da] offset:4096\n\t"
    "v_mfma_f32_16x16x32_bf16 v[84:87], v[132:135], v[96:99], 0\n\t"
    "ds_read_b128 v[124:127], %[da] offset:6144\n\t"
    "v_mfma_f32_16x16x32_bf16 v[88:91], v[128:131], v[100:103], 0\n\t"
    "v_mfma_f32_16x16x32_bf16 v[92:95], v[132:135], v[100:103], 0\n\t"
    "s_waitcnt vmcnt(24)\n\t"
    "s_cmp_lt_u32 %[x], 2\n\t"
    "s_cbranch_scc0 41f\n\t"
    "s_waitcnt vmcnt(8)\n\t"
    "41:\n\t"
    "s_barrier\n\t"
    "s_waitcnt lgkmcnt(4)\n\t"
    "v_mfma_f32_16x16x32_bf16 v[80:83], v[136:139], v[104:107], v[80:83]\n\t"
    "ds_read_b128 v[96:99], %[ea] offset:8192\n\t"
    "v_mfma_f32_16x16x32_bf16 v[84:87], v[140:143], v[104:107], v[84:87]\n\t"
    "ds_read_b128 v[100:103], %[ea] offset:10240\n\t"
    "v_mfma_f32_16x16x32_bf16 v[88:91], v[136:139], v[108:111], v[88:91]\n\t"
    "v_mfma_f32_16x16x32_bf16 v[92:95], v[140:143], v[108:111], v[92:95]\n\t"
    "s_waitcnt lgkmcnt(4)\n\t"
    "v_mfma_f32_16x16x32_bf16 v[80:83], v[144:147], v[112:115], v[80:83]\n\t"
    "ds_read_b128 v[104:107], %[da] offset:8192\n\t"
    "v_mfma_f32_16x16x32_bf16 v[84:87], v[148:151], v[112:115], v[84:87]\n\t"
    "ds_read_b128 v[108:111], %[da] offset:10240\n\t"
    "v_mfma_f32_16x16x32_bf16 v[88:91], v[144:147], v[116:119], v[88:91]\n\t"
    "v_mfma_f32_16x16x32_bf16 v[92:95], v[148:151], v[116:119], v[92:95]\n\t"
    "s_waitcnt lgkmcnt(4)\n\t"
    "v_mfma_f32_16x16x32_bf16 v[80:83], v[152:155], v[120:123], v[80:83]\n\t"
    "ds_read_b128 v[112:115], %[ea] offset:12288\n\t"
    "v_mfma_f32_16x16x32_bf16 v[84:87], v[156:159], v[120:123], v[84:87]\n\t"
    "ds_read_b128 v[116:119], %[ea] offset:14336\n\t"
    "v_mfma_f32_16x16x32_bf16 v[88:91], v[152:155], v[124:127], v[88:91]\n\t"
    "v_mfma_f32_16x16x32_bf16 v[92:95], v[156:159], v[124:127], v[92:95]\n\t"
    "s_waitcnt lgkmcnt(4)\n\t"
    "v_mfma_f32_16x16x32_bf16 v[80:83], v[160:163], v[96:99], v[80:83]\n\t"
    "ds_read_b128 v[120:123], %[da] offset:12288\n\t"
    "v_mfma_f32_16x16x32_bf16 v[84:87], v[164:167], v[96:99], v[84:87]\n\t"
    "ds_read_b128 v[124:127], %[da] offset:14336\n\t"
    "v_mfma_f32_16x16x32_bf16 v[88:91], v[160:163], v[100:103], v[88:91]\n\t"
    "v_mfma_f32_16x16x32_bf16 v[92:95], v[164:167], v[100:103], v[92:95]\n\t"
    "s_waitcnt vmcnt(20)\n\t"
    "s_cmp_lt_u32 %[x], 2\n\t"
    "s_cbranch_scc0 45f\n\t"
    "s_waitcnt vmcnt(4)\n\t"
    "45:\n\t"
    "s_barrier\n\t"
    "s_waitcnt lgkmcnt(4)\n\t"
    "v_mfma_f32_16x16x32_bf16 v[80:83], v[168:171], v[104:107], v[80:83]\n\t"
    "ds_read_b128 v[96:99], %[ea] offset:16384\n\t"
    "v_mfma_f32_16x16x32_bf16 v[84:87], v[172:175], v[104:107], v[84:87]\n\t"
    "ds_read_b128 v[100:103], %[ea] offset:18432\n\t"
    "v_mfma_f32_16x16x32_bf16 v[88:91], v[168:171], v[108:111], v[88:91]\n\t"
    "v_mfma_f32_16x16x32_bf16 v[92:95], v[172:175], v[108:111], v[92:95]\n\t"
    "s_waitcnt lgkmcnt(4)\n\t"
    "v_mfma_f32_16x16x32_bf16 v[80:83], v[176:179], v[112:115], v[80:83]\n\t"
    "ds_read_b128 v[104:107], %[da] offset:16384\n\t"
    "v_mfma_f32_16x16x32_bf16 v[84:87], v[180:183], v[112:115], v[84:87]\n\t"
    "ds_read_b128 v[108:111], %[da] offset:18432\n\t"
    "v_mfma_f32_16x16x32_bf16 v[88:91], v[176:179], v[116:119], v[88:91]\n\t"
    "v_mfma_f32_16x16x32_bf16 v[92:95], v[180:183], v[116:119], v[92:95]\n\t"
    "s_waitcnt lgkmcnt(4)\n\t"
    "v_mfma_f32_16x16x32_bf16 v[80:83], v[184:187], v[120:123], v[80:83]\n\t"
    "ds_read_b128 v[112:115], %[ea] offset:20480\n\t"
    "v_mfma_f32_16x16x32_bf16 v[84:87], v[188:191], v[120:123], v[84:87]\n\t"
    "ds_read_b128 v[116:119], %[ea] offset:22528\n\t"
    "v_mfma_f32_16x16x32_bf16 v[88:91], v[184:187], v[124:127], v[88:91]\n\t"
    "v_mfma_f32_16x16x32_bf16 v[92:95], v[188:191], v[124:127], v[92:95]\n\t"
    "s_waitcnt lgkmcnt(4)\n\t"
    "v_mfma_f32_16x16x32_bf16 v[80:83], v[192:195], v[96:99], v[80:83]\n\t"
    "ds_read_b128 v[120:123], %[da] offset:20480\n\t"
    "v_mfma_f32_16x16x32_bf16 v[84:87], v[196:199], v[96:99], v[84:87]\n\t"
    "ds_read_b128 v[124:127], %[da] offset:22528\n\t"
    "v_mfma_f32_16x16x32_bf16 v[88:91], v[192:195], v[100:103], v[88:91]\n\t"
    "v_mfma_f32_16x16x32_bf16 v[92:95], v[196:199], v[100:103], v[92:95]\n\t"
    "s_waitcnt vmcnt(16)\n\t"
    "s_cmp_lt_u32 %[x], 2\n\t"
    "s_cbranch_scc0 49f\n\t"
    "s_waitcnt vmcnt(0)\n\t"
    "49:\n\t"
    "s_barrier\n\t"
    "s_waitcnt lgkmcnt(4)\n\t"
    "v_mfma_f32_16x16x32_bf16 v[80:83], v[200:203], v[104:107], v[80:83]\n\t"
    "ds_read_b128 v[96:99], %[ea] offset:24576\n\t"
    "v_mfma_f32_16x16x32_bf16 v[84:87], v[204:207], v[104:107], v[84:87]\n\t"
    "ds_read_b128 v[100:103], %[ea] offset:26624\n\t"
    "v_mfma_f32_16x16x32_bf16 v[88:91], v[200:203], v[108:111], v[88:91]\n\t"
    "v_mfma_f32_16x16x32_bf16 v[92:95], v[204:207], v[108:111], v[92:95]\n\t"
    "s_waitcnt lgkmcnt(4)\n\t"
    "v_mfma_f32_16x16x32_bf16 v[80:83], v[208:211], v[112:115], v[80:83]\n\t"
    "ds_read_b128 v[104:107], %[da] offset:24576\n\t"
    "v_mfma_f32_16x16x32_bf16 v[84:87], v[212:215], v[112:115], v[84:87]\n\t"
    "ds_read_b128 v[108:111], %[da] offset:26624\n\t"
    "v_mfma_f32_16x16x32_bf16 v[88:91], v[208:211], v[116:119], v[88:91]\n\t"
    "v_mfma_f32_16x16x32_bf16 v[92:95], v[212:215], v[116:119], v[92:95]\n\t"
    "s_cmp_eq_u32 %[ps], 0\n\t"
    "s_cbranch_scc1 10f\n\t"
    "global_load_dword %[sv], %[dv], %[db] sc1\n\t"
    "global_load_dword %[su], %[dv], %[db] offset:64 sc1\n\t"
    "10:\n\t"
    "s_waitcnt lgkmcnt(4)\n\t"
    "v_mfma_f32_16x16x32_bf16 v[80:83], v[216:219], v[120:123], v[80:83]\n\t"
    "ds_read_b128 v[112:115], %[ea] offset:28672\n\t"
    "v_mfma_f32_16x16x32_bf16 v[84:87], v[220:223], v[120:123], v[84:87]\n\t"
    "ds_read_b128 v[116:119], %[ea] offset:30720\n\t"
    "v_mfma_f32_16x16x32_bf16 v[88:91], v[216:219], v[124:127], v[88:91]\n\t"
    "v_mfma_f32_16x16x32_bf16 v[92:95], v[220:223], v[124:127], v[92:95]\n\t"
    "s_waitcnt lgkmcnt(4)\n\t"
    "v_mfma_f32_16x16x32_bf16 v[80:83], v[224:227], v[96:99], v[80:83]\n\t"
    "ds_read_b128 v[120:123], %[da] offset:28672\n\t"
    "v_mfma_f32_16x16x32_bf16 v[84:87], v[228:231], v[96:99], v[84:87]\n\t"
    "ds_read_b128 v[124:127], %[da] offset:30720\n\t"
    "v_mfma_f32_16x16x32_bf16 v[88:91], v[224:227], v[100:103], v[88:91]\n\t"
    "v_mfma_f32_16x16x32_bf16 v[92:95], v[228:231], v[100:103], v[92:95]\n\t"
    "s_waitcnt lgkmcnt(4)\n\t"
    "v_mfma_f32_16x16x32_bf16 v[80:83], v[232:235], v[104:107], v[80:83]\n\t"
    "v_mfma_f32_16x16x32_bf16 v[84:87], v[236:239], v[104:107], v[84:87]\n\t"
    "v_mfma_f32_16x16x32_bf16 v[88:91], v[232:235], v[108:111], v[88:91]\n\t"
    "v_mfma_f32_16x16x32_bf16 v[92:95], v[236:239], v[108:111], v[92:95]\n\t"
    "s_waitcnt lgkmcnt(2)\n\t"
    "v_mfma_f32_16x16x32_bf16 v[80:83], v[240:243], v[112:115], v[80:83]\n\t"
    "v_mfma_f32_16x16x32_bf16 v[84:87], v[244:247], v[112:115], v[84:87]\n\t"
    "v_mfma_f32_16x16x32_bf16 v[88:91], v[240:243], v[116:119], v[88:91]\n\t"
    "v_mfma_f32_16x16x32_bf16 v[92:95], v[244:247], v[116:119], v[92:95]\n\t"
    "s_waitcnt lgkmcnt(0)\n\t"
    "v_mfma_f32_16x16x32_bf16 v[80:83], v[248:251], v[120:123], v[80:83]\n\t"
    "s_memrealtime s[36:37]\n\t"
    "v_mfma_f32_16x16x32_bf16 v[84:87], v[252:255], v[120:123], v[84:87]\n\t"
    "v_mfma_f32_16x16x32_bf16 v[88:91], v[248:251], v[124:127], v[88:91]\n\t"
    "v_mfma_f32_16x16x32_bf16 v[92:95], v[252:255], v[124:127], v[92:95]\n\t"
    "s_mov_b32 %[got], 0\n\t"
    "v_mov_b32 v76, %[fa]\n\t"
    "v_mov_b32 v77, %[fv]\n\t"
    "v_add_u32 v78, 1, v77\n\t"
    "v_mov_b32 v79, %[ga]\n\t"
    "s_waitcnt lgkmcnt(0)\n\t"
    "ds_write_b128 %[rw], v[80:83] offset:0\n\t"
    "ds_write_b128 %[rw], v[84:87] offset:1024\n\t"
    "ds_write_b128 %[rw], v[88:91] offset:2048\n\t"
    "ds_write_b128 %[rw], v[92:95] offset:3072\n\t"
    "ds_write_b32 v76, v77\n\t"
    "s_cmp_eq_u32 %[ps], 0\n\t"
    "s_cbranch_scc1 9f\n\t"
    "s_waitcnt vmcnt(0)\n\t"
    "v_mad_u32_u24 v76, %[sv], %[mu], %[ad]\n\t"
    "v_cmp_ge_i32 vcc, %[th], v76\n\t"
    "s_cbranch_vccz 11f\n\t"
    "s_sub_u32 s36, s36, 20\n\t"
    "v_sub_u32 v76, s36, %[su]\n\t"
    "v_lshrrev_b32 v77, 24, %[su]\n\t"
    "v_lshlrev_b32 v76, 8, v76\n\t"
    "v_ashrrev_i32 v76, 31, v76\n\t"
    "v_add_u32 v77, v77, v76\n\t"
    "v_cmp_lt_i32 s[36:37], %[tb], v77\n\t"
    "s_andn2_b64 vcc, vcc, s[36:37]\n\t"
    "s_cbranch_scc1 9f\n\t"
    "11:\n\t"
    "ds_write2_b32 v79, v78, v78 offset1:1\n\t"
    "s_add_u32 m0, %[lb], 0\n\t"
    "s_nop 0\n\t"
    "global_load_lds_dwordx4 %[av0], %[s0] sc1\n\t"
    "s_add_u32 m0, %[lb], 32768\n\t"
    "s_nop 0\n\t"
    "global_load_lds_dwordx4 %[av1], %[s0] sc1\n\t"
    "s_add_u32 m0, %[lb], 4096\n\t"
    "s_nop 0\n\t"
    "global_load_lds_dwordx4 %[av2], %[s0] sc1\n\t"
    "s_add_u32 m0, %[lb], 36864\n\t"
    "s_nop 0\n\t"
    "global_load_lds_dwordx4 %[av3], %[s0] sc1\n\t"
    "s_add_u32 m0, %[lb], 8192\n\t"
    "s_nop 0\n\t"
    "global_load_lds_dwordx4 %[av4], %[s0] sc1\n\t"
    "s_add_u32 m0, %[lb], 40960\n\t"
    "s_nop 0\n\t"
    "global_load_lds_dwordx4 %[av5], %[s0] sc1\n\t"
    "s_add_u32 m0, %[lb], 12288\n\t"
    "s_nop 0\n\t"
    "global_load_lds_dwordx4 %[av6], %[s0] sc1\n\t"
    "s_add_u32 m0, %[lb], 45056\n\t"
    "s_nop 0\n\t"
    "global_load_lds_dwordx4 %[av7], %[s0] sc1\n\t"
    "s_add_u32 m0, %[lb], 16384\n\t"
    "s_nop 0\n\t"
    "global_load_lds_dwordx4 %[av0], %[s1] sc1\n\t"
    "s_add_u32 m0, %[lb], 49152\n\t"
    "s_nop 0\n\t"
    "global_load_lds_dwordx4 %[av1], %[s1] sc1\n\t"
    "s_add_u32 m0, %[lb], 20480\n\t"
    "s_nop 0\n\t"
    "global_load_lds_dwordx4 %[av2], %[s1] sc1\n\t"
    "s_add_u32 m0, %[lb], 53248\n\t"
    "s_nop 0\n\t"
    "global_load_lds_dwordx4 %[av3], %[s1] sc1\n\t"
    "s_add_u32 m0, %[lb], 24576\n\t"
    "s_nop 0\n\t"
    "global_load_lds_dwordx4 %[av4], %[s1] sc1\n\t"
    "s_add_u32 m0, %[lb], 57344\n\t"
    "s_nop 0\n\t"
    "global_load_lds_dwordx4 %[av5], %[s1] sc1\n\t"
    "s_add_u32 m0, %[lb], 28672\n\t"
    "s_nop 0\n\t"
    "global_load_lds_dwordx4 %[av6], %[s1] sc1\n\t"
    "s_add_u32 m0, %[lb], 61440\n\t"
    "s_nop 0\n\t"
    "global_load_lds_dwordx4 %[av7], %[s1] sc1\n\t"
    "s_mov_b32 %[got], 1\n\t"
    "s_branch 9f\n\t"
    "12:\n\t"
    "s_waitcnt lgkmcnt(0)\n\t"
    "9:"
    : [sv] "+&v"(sv), [su] "+&v"(su), [got] "=&s"(got)
    : [ea] "v"(ea), [da] "v"(da), [x] "s"(x), "{v[128:159]}"(W[0]), "{v[160:191]}"(W[1]), "{v[192:223]}"(W[2]), "{v[224:255]}"(W[3]), [lb] "s"(lb), [s0] "s"(s0), [s1] "s"(s1), [av0] "v"(av[0]), [av1] "v"(av[1]), [av2] "v"(av[2]), [av3] "v"(av[3]), [av4] "v"(av[4]), [av5] "v"(av[5]), [av6] "v"(av[6]), [av7] "v"(av[7]), [ps] "s"(ps), [dv] "v"(dv), [db] "s"(db), [rw] "v"(rw), [fa] "s"(fa), [fv] "s"(fv), [ga] "s"(ga), [mu] "s"(mu), [ad] "v"(ad), [th] "s"(th), [tb] "s"(tb)
    : "v96", "v97", "v98", "v99", "v100", "v101", "v102", "v103", "v104", "v105", "v106", "v107", "v108", "v109", "v110", "v111", "v112", "v113", "v114", "v115", "v116", "v117", "v118", "v119", "v120", "v121", "v122", "v123", "v124", "v125", "v126", "v127", "v76", "v77", "v78", "v79", "v80", "v81", "v82", "v83", "v84", "v85", "v86", "v87", "v88", "v89", "v90", "v91", "v92", "v93", "v94", "v95", "vcc", "scc", "m0", "memory", "s36", "s37", "s38", "s39");
}
__device__ __forceinline__ void lean_dm0(const u32x32 (&W)[4], unsigned ea, unsigned da, unsigned x, unsigned lb, const char* s0, const char* s1, const unsigned (&av)[8], unsigned ps, unsigned dv, const char* db, unsigned rw, unsigned fa, unsigned fv,
    unsigned ga, unsigned mu, unsigned ad, int th, int tb, unsigned& sv, unsigned& su, unsigned& got) {
  asm volatile("s_setprio 2\n\t"
    "ds_read_b128 v[96:99], %[ea] offset:0\n\t"
    "ds_read_b128 v[100:103], %[ea] offset:2048\n\t"
    "ds_read_b128 v[104:107], %[da] offset:0\n\t"
    "ds_read_b128 v[108:111], %[da] offset:2048\n\t"
    "ds_read_b128 v[112:115], %[ea] offset:4096\n\t"
    "ds_read_b128 v[116:119], %[ea] offset:6144\n\t"
    "s_waitcnt lgkmcnt(4)\n\t"
    "v_mfma_f32_16x16x32_bf16 v[80:83], v[128:131], v[96:99], 0\n\t"
    "ds_read_b128 v[120:123], %[da] offset:4096\n\t"
    "s_add_u32 m0, %[lb], 0\n\t"
    "v_mfma_f32_16x16x32_bf16 v[84:87], v[132:135], v[96:99], 0\n\t"
    "ds_read_b128 v[124:127], %[da] offset:6144\n\t"
    "global_load_lds_dwordx4 %[av0], %[s0] sc1\n\t"
    "v_mfma_f32_16x16x32_bf16 v[88:91], v[128:131], v[100:103], 0\n\t"
    "s_add_u32 m0, %[lb], 32768\n\t"
    "v_mfma_f32_16x16x32_bf16 v[92:95], v[132:135], v[100:103], 0\n\t"
    "global_load_lds_dwordx4 %[av1], %[s0] sc1\n\t"
    "s_waitcnt vmcnt(10)\n\t"
    "s_barrier\n\t"
    "s_waitcnt lgkmcnt(4)\n\t"
    "v_mfma_f32_16x16x32_bf16 v[80:83], v[136:139], v[104:107], v[80:83]\n\t"
    "ds_read_b128 v[96:99], %[ea] offset:8192\n\t"
    "s_add_u32 m0, %[lb], 4096\n\t"
    "v_mfma_f32_16x16x32_bf16 v[84:87], v[140:143], v[104:107], v[84:87]\n\t"
    "ds_read_b128 v[100:103], %[ea] offset:10240\n\t"
    "global_load_lds_dwordx4 %[av2], %[s0] sc1\n\t"
    "v_mfma_f32_16x16x32_bf16 v[88:91], v[136:139], v[108:111], v[88:91]\n\t"
    "s_add_u32 m0, %[lb], 36864\n\t"
    "v_mfma_f32_16x16x32_bf16 v[92:95], v[140:143], v[108:111], v[92:95]\n\t"
    "global_load_lds_dwordx4 %[av3], %[s0] sc1\n\t"
    "s_waitcnt lgkmcnt(4)\n\t"
    "v_mfma_f32_16x16x32_bf16 v[80:83], v[144:147], v[112:115], v[80:83]\n\t"
    "ds_read_b128 v[104:107], %[da] offset:8192\n\t"
    "s_add_u32 m0, %[lb], 8192\n\t"
    "v_mfma_f32_16x16x32_bf16 v[84:87], v[148:151], v[112:115], v[84:87]\n\t"
    "ds_read_b128 v[108:111], %[da] offset:10240\n\t"
    "global_load_lds_dwordx4 %[av4], %[s0] sc1\n\t"
    "v_mfma_f32_16x16x32_bf16 v[88:91], v[144:147], v[116:119], v[88:91]\n\t"
    "s_add_u32 m0, %[lb], 40960\n\t"
    "v_mfma_f32_16x16x32_bf16 v[92:95], v[148:151], v[116:119], v[92:95]\n\t"
    "global_load_lds_dwordx4 %[av5], %[s0] sc1\n\t"
    "s_waitcnt lgkmcnt(4)\n\t"
    "v_mfma_f32_16x16x32_bf16 v[80:83], v[152:155], v[120:123], v[80:83]\n\t"
    "ds_read_b128 v[112:115], %[ea] offset:12288\n\t"
    "s_add_u32 m0, %[lb], 12288\n\t"
    "v_mfma_f32_16x16x32_bf16 v[84:87], v[156:159], v[120:123], v[84:87]\n\t"
    "ds_read_b128 v[116:119], %[ea] offset:14336\n\t"
    "global_load_lds_dwordx4 %[av6], %[s0] sc1\n\t"
    "v_mfma_f32_16x16x32_bf16 v[88:91], v[152:155], v[124:127], v[88:91]\n\t"
    "s_add_u32 m0, %[lb], 45056\n\t"
    "v_mfma_f32_16x16x32_bf16 v[92:95], v[156:159], v[124:127], v[92:95]\n\t"
    "global_load_lds_dwordx4 %[av7], %[s0] sc1\n\t"
    "s_waitcnt lgkmcnt(4)\n\t"
    "v_mfma_f32_16x16x32_bf16 v[80:83], v[160:163], v[96:99], v[80:83]\n\t"
    "ds_read_b128 v[120:123], %[da] offset:12288\n\t"
    "s_add_u32 m0, %[lb], 16384\n\t"
    "v_mfma_f32_16x16x32_bf16 v[84:87], v[164:167], v[96:99], v[84:87]\n\t"
    "ds_read_b128 v[124:127], %[da] offset:14336\n\t"
    "global_load_lds_dwordx4 %[av0], %[s1] sc1\n\t"
    "v_mfma_f32_16x16x32_bf16 v[88:91], v[160:163], v[100:103], v[88:91]\n\t"
    "s_add_u32 m0, %[lb], 49152\n\t"
    "v_mfma_f32_16x16x32_bf16 v[92:95], v[164:167], v[100:103], v[92:95]\n\t"
    "global_load_lds_dwordx4 %[av1], %[s1] sc1\n\t"
    "s_waitcnt vmcnt(14)\n\t"
    "s_barrier\n\t"
    "s_waitcnt lgkmcnt(4)\n\t"
    "v_mfma_f32_16x16x32_bf16 v[80:83], v[168:171], v[104:107], v[80:83]\n\t"
    "ds_read_b128 v[96:99], %[ea] offset:16384\n\t"
    "s_add_u32 m0, %[lb], 20480\n\t"
    "v_mfma_f32_16x16x32_bf16 v[84:87], v[172:175], v[104:107], v[84:87]\n\t"
    "ds_read_b128 v[100:103], %[ea] offset:18432\n\t"
    "global_load_lds_dwordx4 %[av2], %[s1] sc1\n\t"
    "v_mfma_f32_16x16x32_bf16 v[88:91], v[168:171], v[108:111], v[88:91]\n\t"
    "s_add_u32 m0, %[lb], 53248\n\t"
    "v_mfma_f32_16x16x32_bf16 v[92:95], v[172:175], v[108:111], v[92:95]\n\t"
    "global_load_lds_dwordx4 %[av3], %[s1] sc1\n\t"
    "s_waitcnt lgkmcnt(4)\n\t"
    "v_mfma_f32_16x16x32_bf16 v[80:83], v[176:179], v[112:115], v[80:83]\n\t"
    "ds_read_b128 v[104:107], %[da] offset:16384\n\t"
    "s_add_u32 m0, %[lb], 24576\n\t"
    "v_mfma_f32_16x16x32_bf16 v[84:87], v[180:183], v[112:115], v[84:87]\n\t"
    "ds_read_b128 v[108:111], %[da] offset:18432\n\t"
    "global_load_lds_dwordx4 %[av4], %[s1] sc1\n\t"
    "v_mfma_f32_16x16x32_bf16 v[88:91], v[176:179], v[116:119], v[88:91]\n\t"
    "s_add_u32 m0, %[lb], 57344\n\t"
    "v_mfma_f32_16x16x32_bf16 v[92:95], v[180:183], v[116:119], v[92:95]\n\t"
    "global_load_lds_dwordx4 %[av5], %[s1] sc1\n\t"
    "s_waitcnt lgkmcnt(4)\n\t"
    "v_mfma_f32_16x16x32_bf16 v[80:83], v[184:187], v[120:123], v[80:83]\n\t"
    "ds_read_b128 v[112:115], %[ea] offset:20480\n\t"
    "s_add_u32 m0, %[lb], 28672\n\t"
    "v_mfma_f32_16x16x32_bf16 v[84:87], v[188:191], v[120:123], v[84:87]\n\t"
    "ds_read_b128 v[116:119], %[ea] offset:22528\n\t"
    "global_load_lds_dwordx4 %[av6], %[s1] sc1\n\t"
    "v_mfma_f32_16x16x32_bf16 v[88:91], v[184:187], v[124:127], v[88:91]\n\t"
    "s_add_u32 m0, %[lb], 61440\n\t"
    "v_mfma_f32_16x16x32_bf16 v[92:95], v[188:191], v[124:127], v[92:95]\n\t"
    "global_load_lds_dwordx4 %[av7], %[s1] sc1\n\t"
    "s_waitcnt lgkmcnt(4)\n\t"
    "v_mfma_f32_16x16x32_bf16 v[80:83], v[192:195], v[96:99], v[80:83]\n\t"
    "ds_read_b128 v[120:123], %[da] offset:20480\n\t"
    "v_mfma_f32_16x16x32_bf16 v[84:87], v[196:199], v[96:99], v[84:87]\n\t"
    "ds_read_b128 v[124:127], %[da] offset:22528\n\t"
    "v_mfma_f32_16x16x32_bf16 v[88:91], v[192:195], v[100:103], v[88:91]\n\t"
    "v_mfma_f32_16x16x32_bf16 v[92:95], v[196:199], v[100:103], v[92:95]\n\t"
    "s_waitcnt vmcnt(16)\n\t"
    "s_barrier\n\t"
    "s_waitcnt lgkmcnt(4)\n\t"
    "v_mfma_f32_16x16x32_bf16 v[80:83], v[200:203], v[104:107], v[80:83]\n\t"
    "ds_read_b128 v[96:99], %[ea] offset:24576\n\t"
    "v_mfma_f32_16x16x32_bf16 v[84:87], v[204:207], v[104:107], v[84:87]\n\t"
    "ds_read_b128 v[100:103], %[ea] offset:26624\n\t"
    "v_mfma_f32_16x16x32_bf16 v[88:91], v[200:203], v[108:111], v[88:91]\n\t"
    "v_mfma_f32_16x16x32_bf16 v[92:95], v[204:207], v[108:111], v[92:95]\n\t"
    "s_waitcnt lgkmcnt(4)\n\t"
    "v_mfma_f32_16x16x32_bf16 v[80:83], v[208:211], v[112:115], v[80:83]\n\t"
    "ds_read_b128 v[104:107], %[da] offset:24576\n\t"
    "v_mfma_f32_16x16x32_bf16 v[84:87], v[212:215], v[112:115], v[84:87]\n\t"
    "ds_read_b128 v[108:111], %[da] offset:26624\n\t"
    "v_mfma_f32_16x16x32_bf16 v[88:91], v[208:211], v[116:119], v[88:91]\n\t"
    "v_mfma_f32_16x16x32_bf16 v[92:95], v[212:215], v[116:119], v[92:95]\n\t"
    "s_cmp_eq_u32 %[ps], 0\n\t"
    "s_cbranch_scc1 10f\n\t"
    "global_load_dword %[sv], %[dv], %[db] sc1\n\t"
    "global_load_dword %[su], %[dv], %[db] offset:64 sc1\n\t"
    "10:\n\t"
    "s_waitcnt lgkmcnt(4)\n\t"
    "v_mfma_f32_16x16x32_bf16 v[80:83], v[216:219], v[120:123], v[80:83]\n\t"
    "ds_read_b128 v[112:115], %[ea] offset:28672\n\t"
    "v_mfma_f32_16x16x32_bf16 v[84:87], v[220:223], v[120:123], v[84:87]\n\t"
    "ds_read_b128 v[116:119], %[ea] offset:30720\n\t"
    "v_mfma_f32_16x16x32_bf16 v[88:91], v[216:219], v[124:127], v[88:91]\n\t"
    "v_mfma_f32_16x16x32_bf16 v[92:95], v[220:223], v[124:127], v[92:95]\n\t"
    "s_waitcnt lgkmcnt(4)\n\t"
    "v_mfma_f32_16x16x32_bf16 v[80:83], v[224:227], v[96:99], v[80:83]\n\t"
    "ds_read_b128 v[120:123], %[da] offset:28672\n\t"
    "v_mfma_f32_16x16x32_bf16 v[84:87], v[228:231], v[96:99], v[84:87]\n\t"
    "ds_read_b128 v[124:127], %[da] offset:30720\n\t"
    "v_mfma_f32_16x16x32_bf16 v[88:91], v[224:227], v[100:103], v[88:91]\n\t"
    "v_mfma_f32_16x16x32_bf16 v[92:95], v[228:231], v[100:103], v[92:95]\n\t"
    "s_waitcnt lgkmcnt(4)\n\t"
    "v_mfma_f32_16x16x32_bf16 v[80:83], v[232:235], v[104:107], v[80:83]\n\t"
    "v_mfma_f32_16x16x32_bf16 v[84:87], v[236:239], v[104:107], v[84:87]\n\t"
    "v_mfma_f32_16x16x32_bf16 v[88:91], v[232:235], v[108:111], v[88:91]\n\t"
    "v_mfma_f32_16x16x32_bf16 v[92:95], v[236:239], v[108:111], v[92:95]\n\t"
    "s_waitcnt lgkmcnt(2)\n\t"
    "v_mfma_f32_16x16x32_bf16 v[80:83], v[240:243], v[112:115], v[80:83]\n\t"
    "v_mfma_f32_16x16x32_bf16 v[84:87], v[244:247], v[112:115], v[84:87]\n\t"
    "v_mfma_f32_16x16x32_bf16 v[88:91], v[240:243], v[116:119], v[88:91]\n\t"
    "v_mfma_f32_16x16x32_bf16 v[92:95], v[244:247], v[116:119], v[92:95]\n\t"
    "s_waitcnt lgkmcnt(0)\n\t"
    "v_mfma_f32_16x16x32_bf16 v[80:83], v[248:251], v[120:123], v[80:83]\n\t"
    "v_mfma_f32_16x16x32_bf16 v[84:87], v[252:255], v[120:123], v[84:87]\n\t"
    "v_mfma_f32_16x16x32_bf16 v[88:91], v[248:251], v[124:127], v[88:91]\n\t"
    "v_mfma_f32_16x16x32_bf16 v[92:95], v[252:255], v[124:127], v[92:95]\n\t"
    "s_mov_b32 %[got], 0\n\t"
    "v_mov_b32 v76, %[fa]\n\t"
    "v_mov_b32 v77, %[fv]\n\t"
    "v_add_u32 v78, 1, v77\n\t"
    "v_mov_b32 v79, %[ga]\n\t"
    "ds_write_b128 %[rw], v[80:83] offset:0\n\t"
    "ds_write_b128 %[rw], v[84:87] offset:1024\n\t"
    "ds_write_b128 %[rw], v[88:91] offset:2048\n\t"
    "ds_write_b128 %[rw], v[92:95] offset:3072\n\t"
    "ds_write_b32 v76, v77\n\t"
    "s_cmp_eq_u32 %[ps], 0\n\t"
    "s_cbranch_scc1 9f\n\t"
    "s_waitcnt vmcnt(0)\n\t"
    "9:"
    : [sv] "+&v"(sv), [su] "+&v"(su), [got] "=&s"(got)
    : [ea] "v"(ea), [da] "v"(da), [x] "s"(x), "{v[128:159]}"(W[0]), "{v[160:191]}"(W[1]), "{v[192:223]}"(W[2]), "{v[224:255]}"(W[3]), [lb] "s"(lb), [s0] "s"(s0), [s1] "s"(s1), [av0] "v"(av[0]), [av1] "v"(av[1]), [av2] "v"(av[2]), [av3] "v"(av[3]), [av4] "v"(av[4]), [av5] "v"(av[5]), [av6] "v"(av[6]), [av7] "v"(av[7]), [ps] "s"(ps), [dv] "v"(dv), [db] "s"(db), [rw] "v"(rw), [fa] "s"(fa), [fv] "s"(fv), [ga] "s"(ga), [mu] "s"(mu), [ad] "v"(ad), [th] "s"(th), [tb] "s"(tb)
    : "v96", "v97", "v98", "v99", "v100", "v101", "v102", "v103", "v104", "v105", "v106", "v107", "v108", "v109", "v110", "v111", "v112", "v113", "v114", "v115", "v116", "v117", "v118", "v119", "v120", "v121", "v122", "v123", "v124", "v125", "v126", "v127", "v76", "v77", "v78", "v79", "v80", "v81", "v82", "v83", "v84", "v85", "v86", "v87", "v88", "v89", "v90", "v91", "v92", "v93", "v94", "v95", "vcc", "scc", "m0", "memory");
}
__device__ __forceinline__ void lean_ld1(const u32x32 (&W)[4], unsigned ea, unsigned da, unsigned x, unsigned lb, const char* s0, const char* s1, const unsigned (&av)[8], unsigned ps, unsigned dv, const char* db, unsigned rw, unsigned fa, unsigned fv,
    unsigned ga, unsigned mu, unsigned ad, int th, int tb, unsigned& sv, unsigned& su, unsigned& got) {
  asm volatile("s_setprio 2\n\t"
    "v_mov_b32 v76, %[ga]\n\t"
    "ds_read_b32 v76, v76 offset:24\n\t"
    "ds_read_b128 v[96:99], %[ea] offset:0\n\t"
    "ds_read_b128 v[100:103], %[ea] offset:2048\n\t"
    "ds_read_b128 v[104:107], %[da] offset:0\n\t"
    "ds_read_b128 v[108:111], %[da] offset:2048\n\t"
    "ds_read_b128 v[112:115], %[ea] offset:4096\n\t"
    "ds_read_b128 v[116:119], %[ea] offset:6144\n\t"
    "s_waitcnt lgkmcnt(6)\n\t"
    "v_cmp_ne_u32 vcc, 0, v76\n\t"
    "s_mov_b32 %[got], -1\n\t"
    "s_cbranch_vccnz 12f\n\t"
    "s_waitcnt lgkmcnt(4)\n\t"
    "v_mfma_f32_16x16x32_bf16 v[80:83], v[128:131], v[96:99], 0\n\t"
    "ds_read_b128 v[120:123], %[da] offset:4096\n\t"
    "v_mfma_f32_16x16x32_bf16 v[84:87], v[132:135], v[96:99], 0\n\t"
    "ds_read_b128 v[124:127], %[da] offset:6144\n\t"
    "v_mfma_f32_16x16x32_bf16 v[88:91], v[128:131], v[100:103], 0\n\t"
    "v_mfma_f32_16x16x32_bf16 v[92:95], v[132:135], v[100:103], 0\n\t"
    "s_waitcnt vmcnt(12)\n\t"
    "s_cmp_lt_u32 %[x], 2\n\t"
    "s_cbranch_scc0 41f\n\t"
    "s_waitcnt vmcnt(4)\n\t"
    "41:\n\t"
    "s_barrier\n\t"
    "s_waitcnt lgkmcnt(4)\n\t"
    "v_mfma_f32_16x16x32_bf16 v[80:83], v[136:139], v[104:107], v[80:83]\n\t"
    "ds_read_b128 v[96:99], %[ea] offset:8192\n\t"
    "v_mfma_f32_16x16x32_bf16 v[84:87], v[140:143], v[104:107], v[84:87]\n\t"
    "ds_read_b128 v[100:103], %[ea] offset:10240\n\t"
    "v_mfma_f32_16x16x32_bf16 v[88:91], v[136:139], v[108:111], v[88:91]\n\t"
    "v_mfma_f32_16x16x32_bf16 v[92:95], v[140:143], v[108:111], v[92:95]\n\t"
    "s_waitcnt lgkmcnt(4)\n\t"
    "v_mfma_f32_16x16x32_bf16 v[80:83], v[144:147], v[112:115], v[80:83]\n\t"
    "ds_read_b128 v[104:107], %[da] offset:8192\n\t"
    "v_mfma_f32_16x16x32_bf16 v[84:87], v[148:151], v[112:115], v[84:87]\n\t"
    "ds_read_b128 v[108:111], %[da] offset:10240\n\t"
    "v_mfma_f32_16x16x32_bf16 v[88:91], v[144:147], v[116:119], v[88:91]\n\t"
    "v_mfma_f32_16x16x32_bf16 v[92:95], v[148:151], v[116:119], v[92:95]\n\t"
    "s_waitcnt lgkmcnt(4)\n\t"
    "v_mfma_f32_16x16x32_bf16 v[80:83], v[152:155], v[120:123], v[80:83]\n\t"
    "ds_read_b128 v[112:115], %[ea] offset:12288\n\t"
    "v_mfma_f32_16x16x32_bf16 v[84:87], v[156:159], v[120:123], v[84:87]\n\t"
    "ds_read_b128 v[116:119], %[ea] offset:14336\n\t"
    "v_mfma_f32_16x16x32_bf16 v[88:91], v[152:155], v[124:127], v[88:91]\n\t"
    "v_mfma_f32_16x16x32_bf16 v[92:95], v[156:159], v[124:127], v[92:95]\n\t"
    "s_waitcnt lgkmcnt(4)\n\t"
    "v_mfma_f32_16x16x32_bf16 v[80:83], v[160:163], v[96:99], v[80:83]\n\t"
    "ds_read_b128 v[120:123], %[da] offset:12288\n\t"
    "v_mfma_f32_16x16x32_bf16 v[84:87], v[164:167], v[96:99], v[84:87]\n\t"
    "ds_read_b128 v[124:127], %[da] offset:14336\n\t"
    "v_mfma_f32_16x16x32_bf16 v[88:91], v[160:163], v[100:103], v[88:91]\n\t"
    "v_mfma_f32_16x16x32_bf16 v[92:95], v[164:167], v[100:103], v[92:95]\n\t"
    "s_waitcnt vmcnt(10)\n\t"
    "s_cmp_lt_u32 %[x], 2\n\t"
    "s_cbranch_scc0 45f\n\t"
    "s_waitcnt vmcnt(2)\n\t"
    "45:\n\t"
    "s_barrier\n\t"
    "s_waitcnt lgkmcnt(4)\n\t"
    "v_mfma_f32_16x16x32_bf16 v[80:83], v[168:171], v[104:107], v[80:83]\n\t"
    "ds_read_b128 v[96:99], %[ea] offset:16384\n\t"
    "v_mfma_f32_16x16x32_bf16 v[84:87], v[172:175], v[104:107], v[84:87]\n\t"
    "ds_read_b128 v[100:103], %[ea] offset:18432\n\t"
    "v_mfma_f32_16x16x32_bf16 v[88:91], v[168:171], v[108:111], v[88:91]\n\t"
    "v_mfma_f32_16x16x32_bf16 v[92:95], v[172:175], v[108:111], v[92:95]\n\t"
    "s_waitcnt lgkmcnt(4)\n\t"
    "v_mfma_f32_16x16x32_bf16 v[80:83], v[176:179], v[112:115], v[80:83]\n\t"
    "ds_read_b128 v[104:107], %[da] offset:16384\n\t"
    "v_mfma_f32_16x16x32_bf16 v[84:87], v[180:183], v[112:115], v[84:87]\n\t"
    "ds_read_b128 v[108:111], %[da] offset:18432\n\t"
    "v_mfma_f32_16x16x32_bf16 v[88:91], v[176:179], v[116:119], v[88:91]\n\t"
    "v_mfma_f32_16x16x32_bf16 v[92:95], v[180:183], v[116:119], v[92:95]\n\t"
    "s_waitcnt lgkmcnt(4)\n\t"
    "v_mfma_f32_16x16x32_bf16 v[80:83], v[184:187], v[120:123], v[80:83]\n\t"
    "ds_read_b128 v[112:115], %[ea] offset:20480\n\t"
    "v_mfma_f32_16x16x32_bf16 v[84:87], v[188:191], v[120:123], v[84:87]\n\t"
    "ds_read_b128 v[116:119], %[ea] offset:22528\n\t"
    "v_mfma_f32_16x16x32_bf16 v[88:91], v[184:187], v[124:127], v[88:91]\n\t"
    "v_mfma_f32_16x16x32_bf16 v[92:95], v[188:191], v[124:127], v[92:95]\n\t"
    "s_waitcnt lgkmcnt(4)\n\t"
    "v_mfma_f32_16x16x32_bf16 v[80:83], v[192:195], v[96:99], v[80:83]\n\t"
    "ds_read_b128 v[120:123], %[da] offset:20480\n\t"
    "v_mfma_f32_16x16x32_bf16 v[84:87], v[196:199], v[96:99], v[84:87]\n\t"
    "ds_read_b128 v[124:127], %[da] offset:22528\n\t"
    "v_mfma_f32_16x16x32_bf16 v[88:91], v[192:195], v[100:103], v[88:91]\n\t"
    "v_mfma_f32_16x16x32_bf16 v[92:95], v[196:199], v[100:103], v[92:95]\n\t"
    "s_waitcnt vmcnt(8)\n\t"
    "s_cmp_lt_u32 %[x], 2\n\t"
    "s_cbranch_scc0 49f\n\t"
    "s_waitcnt vmcnt(0)\n\t"
    "49:\n\t"
    "s_barrier\n\t"
    "s_waitcnt lgkmcnt(4)\n\t"
    "v_mfma_f32_16x16x32_bf16 v[80:83], v[200:203], v[104:107], v[80:83]\n\t"
    "ds_read_b128 v[96:99], %[ea] offset:24576\n\t"
    "v_mfma_f32_16x16x32_bf16 v[84:87], v[204:207], v[104:107], v[84:87]\n\t"
    "ds_read_b128 v[100:103], %[ea] offset:26624\n\t"
    "v_mfma_f32_16x16x32_bf16 v[88:91], v[200:203], v[108:111], v[88:91]\n\t"
    "v_mfma_f32_16x16x32_bf16 v[92:95], v[204:207], v[108:111], v[92:95]\n\t"
    "s_waitcnt lgkmcnt(4)\n\t"
    "v_mfma_f32_16x16x32_bf16 v[80:83], v[208:211], v[112:115], v[80:83]\n\t"
    "ds_read_b128 v[104:107], %[da] offset:24576\n\t"
    "v_mfma_f32_16x16x32_bf16 v[84:87], v[212:215], v[112:115], v[84:87]\n\t"
    "ds_read_b128 v[108:111], %[da] offset:26624\n\t"
    "v_mfma_f32_16x16x32_bf16 v[88:91], v[208:211], v[116:119], v[88:91]\n\t"
    "v_mfma_f32_16x16x32_bf16 v[92:95], v[212:215], v[116:119], v[92:95]\n\t"
    "s_cmp_eq_u32 %[ps], 0\n\t"
    "s_cbranch_scc1 10f\n\t"
    "global_load_dword %[sv], %[dv], %[db] sc1\n\t"
    "global_load_dword %[su], %[dv], %[db] offset:64 sc1\n\t"
    "10:\n\t"
    "s_waitcnt lgkmcnt(4)\n\t"
    "v_mfma_f32_16x16x32_bf16 v[80:83], v[216:219], v[120:123], v[80:83]\n\t"
    "ds_read_b128 v[112:115], %[ea] offset:28672\n\t"
    "v_mfma_f32_16x16x32_bf16 v[84:87], v[220:223], v[120:123], v[84:87]\n\t"
    "ds_read_b128 v[116:119], %[ea] offset:30720\n\t"
    "v_mfma_f32_16x16x32_bf16 v[88:91], v[216:219], v[124:127], v[88:91]\n\t"
    "v_mfma_f32_16x16x32_bf16 v[92:95], v[220:223], v[124:127], v[92:95]\n\t"
    "s_waitcnt lgkmcnt(4)\n\t"
    "v_mfma_f32_16x16x32_bf16 v[80:83], v[224:227], v[96:99], v[80:83]\n\t"
    "ds_read_b128 v[120:123], %[da] offset:28672\n\t"
    "v_mfma_f32_16x16x32_bf16 v[84:87], v[228:231], v[96:99], v[84:87]\n\t"
    "ds_read_b128 v[124:127], %[da] offset:30720\n\t"
    "v_mfma_f32_16x16x32_bf16 v[88:91], v[224:227], v[100:103], v[88:91]\n\t"
    "v_mfma_f32_16x16x32_bf16 v[92:95], v[228:231], v[100:103], v[92:95]\n\t"
    "s_waitcnt lgkmcnt(4)\n\t"
    "v_mfma_f32_16x16x32_bf16 v[80:83], v[232:235], v[104:107], v[80:83]\n\t"
    "v_mfma_f32_16x16x32_bf16 v[84:87], v[236:239], v[104:107], v[84:87]\n\t"
    "v_mfma_f32_16x16x32_bf16 v[88:91], v[232:235], v[108:111], v[88:91]\n\t"
    "v_mfma_f32_16x16x32_bf16 v[92:95], v[236:239], v[108:111], v[92:95]\n\t"
    "s_waitcnt lgkmcnt(2)\n\t"
    "v_mfma_f32_16x16x32_bf16 v[80:83], v[240:243], v[112:115], v[80:83]\n\t"
    "v_mfma_f32_16x16x32_bf16 v[84:87], v[244:247], v[112:115], v[84:87]\n\t"
    "v_mfma_f32_16x16x32_bf16 v[88:91], v[240:243], v[116:119], v[88:91]\n\t"
    "v_mfma_f32_16x16x32_bf16 v[92:95], v[244:247], v[116:119], v[92:95]\n\t"
    "s_waitcnt lgkmcnt(0)\n\t"
    "v_mfma_f32_16x16x32_bf16 v[80:83], v[248:251], v[120:123], v[80:83]\n\t"
    "s_memrealtime s[36:37]\n\t"
    "v_mfma_f32_16x16x32_bf16 v[84:87], v[252:255], v[120:123], v[84:87]\n\t"
    "v_mfma_f32_16x16x32_bf16 v[88:91], v[248:251], v[124:127], v[88:91]\n\t"
    "v_mfma_f32_16x16x32_bf16 v[92:95], v[252:255], v[124:127], v[92:95]\n\t"
    "s_mov_b32 %[got], 0\n\t"
    "v_mov_b32 v76, %[fa]\n\t"
    "v_mov_b32 v77, %[fv]\n\t"
    "v_add_u32 v78, 1, v77\n\t"
    "v_mov_b32 v79, %[ga]\n\t"
    "s_waitcnt lgkmcnt(0)\n\t"
    "ds_write_b128 %[rw], v[80:83] offset:0\n\t"
    "ds_write_b128 %[rw], v[84:87] offset:1024\n\t"
    "ds_write_b128 %[rw], v[88:91] offset:2048\n\t"
    "ds_write_b128 %[rw], v[92:95] offset:3072\n\t"
    "ds_write_b32 v76, v77\n\t"
    "s_cmp_eq_u32 %[ps], 0\n\t"
    "s_cbranch_scc1 9f\n\t"
    "s_waitcnt vmcnt(0)\n\t"
    "v_mad_u32_u24 v76, %[sv], %[mu], %[ad]\n\t"
    "v_cmp_ge_i32 vcc, %[th], v76\n\t"
    "s_cbranch_vccz 11f\n\t"
    "s_sub_u32 s36, s36, 20\n\t"
    "v_sub_u32 v76, s36, %[su]\n\t"
    "v_lshrrev_b32 v77, 24, %[su]\n\t"
    "v_lshlrev_b32 v76, 8, v76\n\t"
    "v_ashrrev_i32 v76, 31, v76\n\t"
    "v_add_u32 v77, v77, v76\n\t"
    "v_cmp_lt_i32 s[36:37], %[tb], v77\n\t"
    "s_andn2_b64 vcc, vcc, s[36:37]\n\t"
    "s_cbranch_scc1 9f\n\t"
    "11:\n\t"
    "ds_write2_b32 v79, v78, v78 offset1:1\n\t"
    "s_add_u32 m0, %[lb], 0\n\t"
    "s_nop 0\n\t"
    "global_load_lds_dwordx4 %[av0], %[s0] sc1\n\t"
    "s_add_u32 m0, %[lb], 4096\n\t"
    "s_nop 0\n\t"
    "global_load_lds_dwordx4 %[av2], %[s0] sc1\n\t"
    "s_add_u32 m0, %[lb], 8192\n\t"
    "s_nop 0\n\t"
    "global_load_lds_dwordx4 %[av4], %[s0] sc1\n\t"
    "s_add_u32 m0, %[lb], 12288\n\t"
    "s_nop 0\n\t"
    "global_load_lds_dwordx4 %[av6], %[s0] sc1\n\t"
    "s_add_u32 m0, %[lb], 16384\n\t"
    "s_nop 0\n\t"
    "global_load_lds_dwordx4 %[av0], %[s1] sc1\n\t"
    "s_add_u32 m0, %[lb], 20480\n\t"
    "s_nop 0\n\t"
    "global_load_lds_dwordx4 %[av2], %[s1] sc1\n\t"
    "s_add_u32 m0, %[lb], 24576\n\t"
    "s_nop 0\n\t"
    "global_load_lds_dwordx4 %[av4], %[s1] sc1\n\t"
    "s_add_u32 m0, %[lb], 28672\n\t"
    "s_nop 0\n\t"
    "global_load_lds_dwordx4 %[av6], %[s1] sc1\n\t"
    "s_mov_b32 %[got], 1\n\t"
    "s_branch 9f\n\t"
    "12:\n\t"
    "s_waitcnt lgkmcnt(0)\n\t"
    "9:"
    : [sv] "+&v"(sv), [su] "+&v"(su), [got] "=&s"(got)
    : [ea] "v"(ea), [da] "v"(da), [x] "s"(x), "{v[128:159]}"(W[0]), "{v[160:191]}"(W[1]), "{v[192:223]}"(W[2]), "{v[224:255]}"(W[3]), [lb] "s"(lb), [s0] "s"(s0), [s1] "s"(s1), [av0] "v"(av[0]), [av1] "v"(av[1]), [av2] "v"(av[2]), [av3] "v"(av[3]), [av4] "v"(av[4]), [av5] "v"(av[5]), [av6] "v"(av[6]), [av7] "v"(av[7]), [ps] "s"(ps), [dv] "v"(dv), [db] "s"(db), [rw] "v"(rw), [fa] "s"(fa), [fv] "s"(fv), [ga] "s"(ga), [mu] "s"(mu), [ad] "v"(ad), [th] "s"(th), [tb] "s"(tb)
    : "v96", "v97", "v98", "v99", "v100", "v101", "v102", "v103", "v104", "v105", "v106", "v107", "v108", "v109", "v110", "v111", "v112", "v113", "v114", "v115", "v116", "v117", "v118", "v119", "v120", "v121", "v122", "v123", "v124", "v125", "v126", "v127", "v76", "v77", "v78", "v79", "v80", "v81", "v82", "v83", "v84", "v85", "v86", "v87", "v88", "v89", "v90", "v91", "v92", "v93", "v94", "v95", "vcc", "scc", "m0", "memory", "s36", "s37", "s38", "s39");
}
__device__ __forceinline__ void lean_dm1(const u32x32 (&W)[4], unsigned ea, unsigned da, unsigned x, unsigned lb, const char* s0, const char* s1, const unsigned (&av)[8], unsigned ps, unsigned dv, const char* db, unsigned rw, unsigned fa, unsigned fv,
    unsigned ga, unsigned mu, unsigned ad, int th, int tb, unsigned& sv, unsigned& su, unsigned& got) {
  asm volatile("s_setprio 2\n\t"
    "ds_read_b128 v[96:99], %[ea] offset:0\n\t"
    "ds_read_b128 v[100:103], %[ea] offset:2048\n\t"
    "ds_read_b128 v[104:107], %[da] offset:0\n\t"
    "ds_read_b128 v[108:111], %[da] offset:2048\n\t"
    "ds_read_b128 v[112:115], %[ea] offset:4096\n\t"
    "ds_read_b128 v[116:119], %[ea] offset:6144\n\t"
    "s_waitcnt lgkmcnt(4)\n\t"
    "v_mfma_f32_16x16x32_bf16 v[80:83], v[128:131], v[96:99], 0\n\t"
    "ds_read_b128 v[120:123], %[da] offset:4096\n\t"
    "s_add_u32 m0, %[lb], 0\n\t"
    "v_mfma_f32_16x16x32_bf16 v[84:87], v[132:135], v[96:99], 0\n\t"
    "ds_read_b128 v[124:127], %[da] offset:6144\n\t"
    "global_load_lds_dwordx4 %[av0], %[s0] sc1\n\t"
    "v_mfma_f32_16x16x32_bf16 v[88:91], v[128:131], v[100:103], 0\n\t"
    "v_mfma_f32_16x16x32_bf16 v[92:95], v[132:135], v[100:103], 0\n\t"
    "s_waitcnt vmcnt(5)\n\t"
    "s_barrier\n\t"
    "s_waitcnt lgkmcnt(4)\n\t"
    "v_mfma_f32_16x16x32_bf16 v[80:83], v[136:139], v[104:107], v[80:83]\n\t"
    "ds_read_b128 v[96:99], %[ea] offset:8192\n\t"
    "s_add_u32 m0, %[lb], 4096\n\t"
    "v_mfma_f32_16x16x32_bf16 v[84:87], v[140:143], v[104:107], v[84:87]\n\t"
    "ds_read_b128 v[100:103], %[ea] offset:10240\n\t"
    "global_load_lds_dwordx4 %[av2], %[s0] sc1\n\t"
    "v_mfma_f32_16x16x32_bf16 v[88:91], v[136:139], v[108:111], v[88:91]\n\t"
    "v_mfma_f32_16x16x32_bf16 v[92:95], v[140:143], v[108:111], v[92:95]\n\t"
    "s_waitcnt lgkmcnt(4)\n\t"
    "v_mfma_f32_16x16x32_bf16 v[80:83], v[144:147], v[112:115], v[80:83]\n\t"
    "ds_read_b128 v[104:107], %[da] offset:8192\n\t"
    "s_add_u32 m0, %[lb], 8192\n\t"
    "v_mfma_f32_16x16x32_bf16 v[84:87], v[148:151], v[112:115], v[84:87]\n\t"
    "ds_read_b128 v[108:111], %[da] offset:10240\n\t"
    "global_load_lds_dwordx4 %[av4], %[s0] sc1\n\t"
    "v_mfma_f32_16x16x32_bf16 v[88:91], v[144:147], v[116:119], v[88:91]\n\t"
    "v_mfma_f32_16x16x32_bf16 v[92:95], v[148:151], v[116:119], v[92:95]\n\t"
    "s_waitcnt lgkmcnt(4)\n\t"
    "v_mfma_f32_16x16x32_bf16 v[80:83], v[152:155], v[120:123], v[80:83]\n\t"
    "ds_read_b128 v[112:115], %[ea] offset:12288\n\t"
    "s_add_u32 m0, %[lb], 12288\n\t"
    "v_mfma_f32_16x16x32_bf16 v[84:87], v[156:159], v[120:123], v[84:87]\n\t"
    "ds_read_b128 v[116:119], %[ea] offset:14336\n\t"
    "global_load_lds_dwordx4 %[av6], %[s0] sc1\n\t"
    "v_mfma_f32_16x16x32_bf16 v[88:91], v[152:155], v[124:127], v[88:91]\n\t"
    "v_mfma_f32_16x16x32_bf16 v[92:95], v[156:159], v[124:127], v[92:95]\n\t"
    "s_waitcnt lgkmcnt(4)\n\t"
    "v_mfma_f32_16x16x32_bf16 v[80:83], v[160:163], v[96:99], v[80:83]\n\t"
    "ds_read_b128 v[120:123], %[da] offset:12288\n\t"
    "s_add_u32 m0, %[lb], 16384\n\t"
    "v_mfma_f32_16x16x32_bf16 v[84:87], v[164:167], v[96:99], v[84:87]\n\t"
    "ds_read_b128 v[124:127], %[da] offset:14336\n\t"
    "global_load_lds_dwordx4 %[av0], %[s1] sc1\n\t"
    "v_mfma_f32_16x16x32_bf16 v[88:91], v[160:163], v[100:103], v[88:91]\n\t"
    "v_mfma_f32_16x16x32_bf16 v[92:95], v[164:167], v[100:103], v[92:95]\n\t"
    "s_waitcnt vmcnt(7)\n\t"
    "s_barrier\n\t"
    "s_waitcnt lgkmcnt(4)\n\t"
    "v_mfma_f32_16x16x32_bf16 v[80:83], v[168:171], v[104:107], v[80:83]\n\t"
    "ds_read_b128 v[96:99], %[ea] offset:16384\n\t"
    "s_add_u32 m0, %[lb], 20480\n\t"
    "v_mfma_f32_16x16x32_bf16 v[84:87], v[172:175], v[104:107], v[84:87]\n\t"
    "ds_read_b128 v[100:103], %[ea] offset:18432\n\t"
    "global_load_lds_dwordx4 %[av2], %[s1] sc1\n\t"
    "v_mfma_f32_16x16x32_bf16 v[88:91], v[168:171], v[108:111], v[88:91]\n\t"
    "v_mfma_f32_16x16x32_bf16 v[92:95], v[172:175], v[108:111], v[92:95]\n\t"
    "s_waitcnt lgkmcnt(4)\n\t"
    "v_mfma_f32_16x16x32_bf16 v[80:83], v[176:179], v[112:115], v[80:83]\n\t"
    "ds_read_b128 v[104:107], %[da] offset:16384\n\t"
    "s_add_u32 m0, %[lb], 24576\n\t"
    "v_mfma_f32_16x16x32_bf16 v[84:87], v[180:183], v[112:115], v[84:87]\n\t"
    "ds_read_b128 v[108:111], %[da] offset:18432\n\t"
    "global_load_lds_dwordx4 %[av4], %[s1] sc1\n\t"
    "v_mfma_f32_16x16x32_bf16 v[88:91], v[176:179], v[116:119], v[88:91]\n\t"
    "v_mfma_f32_16x16x32_bf16 v[92:95], v[180:183], v[116:119], v[92:95]\n\t"
    "s_waitcnt lgkmcnt(4)\n\t"
    "v_mfma_f32_16x16x32_bf16 v[80:83], v[184:187], v[120:123], v[80:83]\n\t"
    "ds_read_b128 v[112:115], %[ea] offset:20480\n\t"
    "s_add_u32 m0, %[lb], 28672\n\t"
    "v_mfma_f32_16x16x32_bf16 v[84:87], v[188:191], v[120:123], v[84:87]\n\t"
    "ds_read_b128 v[116:119], %[ea] offset:22528\n\t"
    "global_load_lds_dwordx4 %[av6], %[s1] sc1\n\t"
    "v_mfma_f32_16x16x32_bf16 v[88:91], v[184:187], v[124:127], v[88:91]\n\t"
    "v_mfma_f32_16x16x32_bf16 v[92:95], v[188:191], v[124:127], v[92:95]\n\t"
    "s_waitcnt lgkmcnt(4)\n\t"
    "v_mfma_f32_16x16x32_bf16 v[80:83], v[192:195], v[96:99], v[80:83]\n\t"
    "ds_read_b128 v[120:123], %[da] offset:20480\n\t"
    "v_mfma_f32_16x16x32_bf16 v[84:87], v[196:199], v[96:99], v[84:87]\n\t"
    "ds_read_b128 v[124:127], %[da] offset:22528\n\t"
    "v_mfma_f32_16x16x32_bf16 v[88:91], v[192:195], v[100:103], v[88:91]\n\t"
    "v_mfma_f32_16x16x32_bf16 v[92:95], v[196:199], v[100:103], v[92:95]\n\t"
    "s_waitcnt vmcnt(8)\n\t"
    "s_barrier\n\t"
    "s_waitcnt lgkmcnt(4)\n\t"
    "v_mfma_f32_16x16x32_bf16 v[80:83], v[200:203], v[104:107], v[80:83]\n\t"
    "ds_read_b128 v[96:99], %[ea] offset:24576\n\t"
    "v_mfma_f32_16x16x32_bf16 v[84:87], v[204:207], v[104:107], v[84:87]\n\t"
    "ds_read_b128 v[100:103], %[ea] offset:26624\n\t"
    "v_mfma_f32_16x16x32_bf16 v[88:91], v[200:203], v[108:111], v[88:91]\n\t"
    "v_mfma_f32_16x16x32_bf16 v[92:95], v[204:207], v[108:111], v[92:95]\n\t"
    "s_waitcnt lgkmcnt(4)\n\t"
    "v_mfma_f32_16x16x32_bf16 v[80:83], v[208:211], v[112:115], v[80:83]\n\t"
    "ds_read_b128 v[104:107], %[da] offset:24576\n\t"
    "v_mfma_f32_16x16x32_bf16 v[84:87], v[212:215], v[112:115], v[84:87]\n\t"
    "ds_read_b128 v[108:111], %[da] offset:26624\n\t"
    "v_mfma_f32_16x16x32_bf16 v[88:91], v[208:211], v[116:119], v[88:91]\n\t"
    "v_mfma_f32_16x16x32_bf16 v[92:95], v[212:215], v[116:119], v[92:95]\n\t"
    "s_cmp_eq_u32 %[ps], 0\n\t"
    "s_cbranch_scc1 10f\n\t"
    "global_load_dword %[sv], %[dv], %[db] sc1\n\t"
    "global_load_dword %[su], %[dv], %[db] offset:64 sc1\n\t"
    "10:\n\t"
    "s_waitcnt lgkmcnt(4)\n\t"
    "v_mfma_f32_16x16x32_bf16 v[80:83], v[216:219], v[120:123], v[80:83]\n\t"
    "ds_read_b128 v[112:115], %[ea] offset:28672\n\t"
    "v_mfma_f32_16x16x32_bf16 v[84:87], v[220:223], v[120:123], v[84:87]\n\t"
    "ds_read_b128 v[116:119], %[ea] offset:30720\n\t"
    "v_mfma_f32_16x16x32_bf16 v[88:91], v[216:219], v[124:127], v[88:91]\n\t"
    "v_mfma_f32_16x16x32_bf16 v[92:95], v[220:223], v[124:127], v[92:95]\n\t"
    "s_waitcnt lgkmcnt(4)\n\t"
    "v_mfma_f32_16x16x32_bf16 v[80:83], v[224:227], v[96:99], v[80:83]\n\t"
    "ds_read_b128 v[120:123], %[da] offset:28672\n\t"
    "v_mfma_f32_16x16x32_bf16 v[84:87], v[228:231], v[96:99], v[84:87]\n\t"
    "ds_read_b128 v[124:127], %[da] offset:30720\n\t"
    "v_mfma_f32_16x16x32_bf16 v[88:91], v[224:227], v[100:103], v[88:91]\n\t"
    "v_mfma_f32_16x16x32_bf16 v[92:95], v[228:231], v[100:103], v[92:95]\n\t"
    "s_waitcnt lgkmcnt(4)\n\t"
    "v_mfma_f32_16x16x32_bf16 v[80:83], v[232:235], v[104:107], v[80:83]\n\t"
    "v_mfma_f32_16x16x32_bf16 v[84:87], v[236:239], v[104:107], v[84:87]\n\t"
    "v_mfma_f32_16x16x32_bf16 v[88:91], v[232:235], v[108:111], v[88:91]\n\t"
    "v_mfma_f32_16x16x32_bf16 v[92:95], v[236:239], v[108:111], v[92:95]\n\t"
    "s_waitcnt lgkmcnt(2)\n\t"
    "v_mfma_f32_16x16x32_bf16 v[80:83], v[240:243], v[112:115], v[80:83]\n\t"
    "v_mfma_f32_16x16x32_bf16 v[84:87], v[244:247], v[112:115], v[84:87]\n\t"
    "v_mfma_f32_16x16x32_bf16 v[88:91], v[240:243], v[116:119], v[88:91]\n\t"
    "v_mfma_f32_16x16x32_bf16 v[92:95], v[244:247], v[116:119], v[92:95]\n\t"
    "s_waitcnt lgkmcnt(0)\n\t"
    "v_mfma_f32_16x16x32_bf16 v[80:83], v[248:251], v[120:123], v[80:83]\n\t"
    "v_mfma_f32_16x16x32_bf16 v[84:87], v[252:255], v[120:123], v[84:87]\n\t"
    "v_mfma_f32_16x16x32_bf16 v[88:91], v[248:251], v[124:127], v[88:91]\n\t"
    "v_mfma_f32_16x16x32_bf16 v[92:95], v[252:255], v[124:127], v[92:95]\n\t"
    "s_mov_b32 %[got], 0\n\t"
    "v_mov_b32 v76, %[fa]\n\t"
    "v_mov_b32 v77, %[fv]\n\t"
    "v_add_u32 v78, 1, v77\n\t"
    "v_mov_b32 v79, %[ga]\n\t"
    "ds_write_b128 %[rw], v[80:83] offset:0\n\t"
    "ds_write_b128 %[rw], v[84:87] offset:1024\n\t"
    "ds_write_b128 %[rw], v[88:91] offset:2048\n\t"
    "ds_write_b128 %[rw], v[92:95] offset:3072\n\t"
    "ds_write_b32 v76, v77\n\t"
    "s_cmp_eq_u32 %[ps], 0\n\t"
    "s_cbranch_scc1 9f\n\t"
    "s_waitcnt vmcnt(0)\n\t"
    "9:"
    : [sv] "+&v"(sv), [su] "+&v"(su), [got] "=&s"(got)
    : [ea] "v"(ea), [da] "v"(da), [x] "s"(x), "{v[128:159]}"(W[0]), "{v[160:191]}"(W[1]), "{v[192:223]}"(W[2]), "{v[224:255]}"(W[3]), [lb] "s"(lb), [s0] "s"(s0), [s1] "s"(s1), [av0] "v"(av[0]), [av1] "v"(av[1]), [av2] "v"(av[2]), [av3] "v"(av[3]), [av4] "v"(av[4]), [av5] "v"(av[5]), [av6] "v"(av[6]), [av7] "v"(av[7]), [ps] "s"(ps), [dv] "v"(dv), [db] "s"(db), [rw] "v"(rw), [fa] "s"(fa), [fv] "s"(fv), [ga] "s"(ga), [mu] "s"(mu), [ad] "v"(ad), [th] "s"(th), [tb] "s"(tb)
    : "v96", "v97", "v98", "v99", "v100", "v101", "v102", "v103", "v104", "v105", "v106", "v107", "v108", "v109", "v110", "v111", "v112", "v113", "v114", "v115", "v116", "v117", "v118", "v119", "v120", "v121", "v122", "v123", "v124", "v125", "v126", "v127", "v76", "v77", "v78", "v79", "v80", "v81", "v82", "v83", "v84", "v85", "v86", "v87", "v88", "v89", "v90", "v91", "v92", "v93", "v94", "v95", "vcc", "scc", "m0", "memory");
}
__device__ __forceinline__ void lean_st0(const u32x32 (&W)[4], unsigned ea, unsigned da, unsigned x, u32x16& pd, unsigned pv, unsigned cn, const char* pg, unsigned bl, unsigned& tk, unsigned& st) {
  asm volatile("v_mov_b32 v76, %[cn]\n\t"
    "ds_read_b32 v76, v76 offset:28\n\t"
    "ds_read_b128 v[96:99], %[ea] offset:0\n\t"
    "ds_read_b128 v[100:103], %[ea] offset:2048\n\t"
    "ds_read_b128 v[104:107], %[da] offset:0\n\t"
    "ds_read_b128 v[108:111], %[da] offset:2048\n\t"
    "ds_read_b128 v[112:115], %[ea] offset:4096\n\t"
    "ds_read_b128 v[116:119], %[ea] offset:6144\n\t"
    "s_waitcnt lgkmcnt(6)\n\t"
    "v_cmp_ne_u32 vcc, 0, v76\n\t"
    "s_mov_b32 %[st], -1\n\t"
    "s_cbranch_vccnz 12f\n\t"
    "s_waitcnt lgkmcnt(4)\n\t"
    "v_mfma_f32_16x16x32_bf16 v[80:83], v[128:131], v[96:99], 0\n\t"
    "ds_read_b128 v[120:123], %[da] offset:4096\n\t"
    "v_mfma_f32_16x16x32_bf16 v[84:87], v[132:135], v[96:99], 0\n\t"
    "ds_read_b128 v[124:127], %[da] offset:6144\n\t"
    "v_mfma_f32_16x16x32_bf16 v[88:91], v[128:131], v[100:103], 0\n\t"
    "v_mfma_f32_16x16x32_bf16 v[92:95], v[132:135], v[100:103], 0\n\t"
    "s_barrier\n\t"
    "s_bitcmp1_b32 %[x], 2\n\t"
    "s_cbranch_scc1 7f\n\t"
    "8:\n\t"
    "s_bitcmp1_b32 %[x], 3\n\t"
    "s_cbranch_scc1 13f\n\t"
    "14:\n\t"
    "s_waitcnt lgkmcnt(4)\n\t"
    "v_mfma_f32_16x16x32_bf16 v[80:83], v[136:139], v[104:107], v[80:83]\n\t"
    "ds_read_b128 v[96:99], %[ea] offset:8192\n\t"
    "v_mfma_f32_16x16x32_bf16 v[84:87], v[140:143], v[104:107], v[84:87]\n\t"
    "ds_read_b128 v[100:103], %[ea] offset:10240\n\t"
    "v_mfma_f32_16x16x32_bf16 v[88:91], v[136:139], v[108:111], v[88:91]\n\t"
    "v_mfma_f32_16x16x32_bf16 v[92:95], v[140:143], v[108:111], v[92:95]\n\t"
    "s_waitcnt lgkmcnt(4)\n\t"
    "v_mfma_f32_16x16x32_bf16 v[80:83], v[144:147], v[112:115], v[80:83]\n\t"
    "ds_read_b128 v[104:107], %[da] offset:8192\n\t"
    "v_mfma_f32_16x16x32_bf16 v[84:87], v[148:151], v[112:115], v[84:87]\n\t"
    "ds_read_b128 v[108:111], %[da] offset:10240\n\t"
    "v_mfma_f32_16x16x32_bf16 v[88:91], v[144:147], v[116:119], v[88:91]\n\t"
    "v_mfma_f32_16x16x32_bf16 v[92:95], v[148:151], v[116:119], v[92:95]\n\t"
    "s_waitcnt lgkmcnt(4)\n\t"
    "v_mfma_f32_16x16x32_bf16 v[80:83], v[152:155], v[120:123], v[80:83]\n\t"
    "ds_read_b128 v[112:115], %[ea] offset:12288\n\t"
    "v_mfma_f32_16x16x32_bf16 v[84:87], v[156:159], v[120:123], v[84:87]\n\t"
    "ds_read_b128 v[116:119], %[ea] offset:14336\n\t"
    "v_mfma_f32_16x16x32_bf16 v[88:91], v[152:155], v[124:127], v[88:91]\n\t"
    "v_mfma_f32_16x16x32_bf16 v[92:95], v[156:159], v[124:127], v[92:95]\n\t"
    "s_waitcnt lgkmcnt(4)\n\t"
    "v_mfma_f32_16x16x32_bf16 v[80:83], v[160:163], v[96:99], v[80:83]\n\t"
    "ds_read_b128 v[120:123], %[da] offset:12288\n\t"
    "v_mfma_f32_16x16x32_bf16 v[84:87], v[164:167], v[96:99], v[84:87]\n\t"
    "ds_read_b128 v[124:127], %[da] offset:14336\n\t"
    "v_mfma_f32_16x16x32_bf16 v[88:91], v[160:163], v[100:103], v[88:91]\n\t"
    "v_mfma_f32_16x16x32_bf16 v[92:95], v[164:167], v[100:103], v[92:95]\n\t"
    "s_barrier\n\t"
    "s_waitcnt lgkmcnt(4)\n\t"
    "v_mfma_f32_16x16x32_bf16 v[80:83], v[168:171], v[104:107], v[80:83]\n\t"
    "ds_read_b128 v[96:99], %[ea] offset:16384\n\t"
    "v_mfma_f32_16x16x32_bf16 v[84:87], v[172:175], v[104:107], v[84:87]\n\t"
    "ds_read_b128 v[100:103], %[ea] offset:18432\n\t"
    "v_mfma_f32_16x16x32_bf16 v[88:91], v[168:171], v[108:111], v[88:91]\n\t"
    "v_mfma_f32_16x16x32_bf16 v[92:95], v[172:175], v[108:111], v[92:95]\n\t"
    "s_waitcnt lgkmcnt(4)\n\t"
    "v_mfma_f32_16x16x32_bf16 v[80:83], v[176:179], v[112:115], v[80:83]\n\t"
    "ds_read_b128 v[104:107], %[da] offset:16384\n\t"
    "v_mfma_f32_16x16x32_bf16 v[84:87], v[180:183], v[112:115], v[84:87]\n\t"
    "ds_read_b128 v[108:111], %[da] offset:18432\n\t"
    "v_mfma_f32_16x16x32_bf16 v[88:91], v[176:179], v[116:119], v[88:91]\n\t"
    "v_mfma_f32_16x16x32_bf16 v[92:95], v[180:183], v[116:119], v[92:95]\n\t"
    "s_waitcnt lgkmcnt(4)\n\t"
    "v_mfma_f32_16x16x32_bf16 v[80:83], v[184:187], v[120:123], v[80:83]\n\t"
    "ds_read_b128 v[112:115], %[ea] offset:20480\n\t"
    "v_mfma_f32_16x16x32_bf16 v[84:87], v[188:191], v[120:123], v[84:87]\n\t"
    "ds_read_b128 v[116:119], %[ea] offset:22528\n\t"
    "v_mfma_f32_16x16x32_bf16 v[88:91], v[184:187], v[124:127], v[88:91]\n\t"
    "v_mfma_f32_16x16x32_bf16 v[92:95], v[188:191], v[124:127], v[92:95]\n\t"
    "s_waitcnt lgkmcnt(4)\n\t"
    "v_mfma_f32_16x16x32_bf16 v[80:83], v[192:195], v[96:99], v[80:83]\n\t"
    "ds_read_b128 v[120:123], %[da] offset:20480\n\t"
    "v_mfma_f32_16x16x32_bf16 v[84:87], v[196:199], v[96:99], v[84:87]\n\t"
    "ds_read_b128 v[124:127], %[da] offset:22528\n\t"
    "v_mfma_f32_16x16x32_bf16 v[88:91], v[192:195], v[100:103], v[88:91]\n\t"
    "v_mfma_f32_16x16x32_bf16 v[92:95], v[196:199], v[100:103], v[92:95]\n\t"
    "s_barrier\n\t"
    "s_and_b32 %[st], %[x], 3\n\t"
    "s_cbranch_scc1 3f\n\t"
    "4:\n\t"
    "s_waitcnt lgkmcnt(4)\n\t"
    "v_mfma_f32_16x16x32_bf16 v[80:83], v[200:203], v[104:107], v[80:83]\n\t"
    "ds_read_b128 v[96:99], %[ea] offset:24576\n\t"
    "v_mfma_f32_16x16x32_bf16 v[84:87], v[204:207], v[104:107], v[84:87]\n\t"
    "ds_read_b128 v[100:103], %[ea] offset:26624\n\t"
    "v_mfma_f32_16x16x32_bf16 v[88:91], v[200:203], v[108:111], v[88:91]\n\t"
    "v_mfma_f32_16x16x32_bf16 v[92:95], v[204:207], v[108:111], v[92:95]\n\t"
    "s_waitcnt lgkmcnt(4)\n\t"
    "v_mfma_f32_16x16x32_bf16 v[80:83], v[208:211], v[112:115], v[80:83]\n\t"
    "ds_read_b128 v[104:107], %[da] offset:24576\n\t"
    "v_mfma_f32_16x16x32_bf16 v[84:87], v[212:215], v[112:115], v[84:87]\n\t"
    "ds_read_b128 v[108:111], %[da] offset:26624\n\t"
    "v_mfma_f32_16x16x32_bf16 v[88:91], v[208:211], v[116:119], v[88:91]\n\t"
    "v_mfma_f32_16x16x32_bf16 v[92:95], v[212:215], v[116:119], v[92:95]\n\t"
    "s_waitcnt lgkmcnt(4)\n\t"
    "v_mfma_f32_16x16x32_bf16 v[80:83], v[216:219], v[120:123], v[80:83]\n\t"
    "ds_read_b128 v[112:115], %[ea] offset:28672\n\t"
    "v_mfma_f32_16x16x32_bf16 v[84:87], v[220:223], v[120:123], v[84:87]\n\t"
    "ds_read_b128 v[116:119], %[ea] offset:30720\n\t"
    "v_mfma_f32_16x16x32_bf16 v[88:91], v[216:219], v[124:127], v[88:91]\n\t"
    "v_mfma_f32_16x16x32_bf16 v[92:95], v[220:223], v[124:127], v[92:95]\n\t"
    "s_waitcnt lgkmcnt(4)\n\t"
    "v_mfma_f32_16x16x32_bf16 v[80:83], v[224:227], v[96:99], v[80:83]\n\t"
    "ds_read_b128 v[120:123], %[da] offset:28672\n\t"
    "v_mfma_f32_16x16x32_bf16 v[84:87], v[228:231], v[96:99], v[84:87]\n\t"
    "ds_read_b128 v[124:127], %[da] offset:30720\n\t"
    "v_mfma_f32_16x16x32_bf16 v[88:91], v[224:227], v[100:103], v[88:91]\n\t"
    "v_mfma_f32_16x16x32_bf16 v[92:95], v[228:231], v[100:103], v[92:95]\n\t"
    "s_waitcnt lgkmcnt(4)\n\t"
    "v_mfma_f32_16x16x32_bf16 v[80:83], v[232:235], v[104:107], v[80:83]\n\t"
    "v_mfma_f32_16x16x32_bf16 v[84:87], v[236:239], v[104:107], v[84:87]\n\t"
    "v_mfma_f32_16x16x32_bf16 v[88:91], v[232:235], v[108:111], v[88:91]\n\t"
    "v_mfma_f32_16x16x32_bf16 v[92:95], v[236:239], v[108:111], v[92:95]\n\t"
    "s_waitcnt lgkmcnt(2)\n\t"
    "v_mfma_f32_16x16x32_bf16 v[80:83], v[240:243], v[112:115], v[80:83]\n\t"
    "v_mfma_f32_16x16x32_bf16 v[84:87], v[244:247], v[112:115], v[84:87]\n\t"
    "v_mfma_f32_16x16x32_bf16 v[88:91], v[240:243], v[116:119], v[88:91]\n\t"
    "v_mfma_f32_16x16x32_bf16 v[92:95], v[244:247], v[116:119], v[92:95]\n\t"
    "s_waitcnt lgkmcnt(0)\n\t"
    "v_mfma_f32_16x16x32_bf16 v[80:83], v[248:251], v[120:123], v[80:83]\n\t"
    "v_mfma_f32_16x16x32_bf16 v[84:87], v[252:255], v[120:123], v[84:87]\n\t"
    "v_mfma_f32_16x16x32_bf16 v[88:91], v[248:251], v[124:127], v[88:91]\n\t"
    "v_mfma_f32_16x16x32_bf16 v[92:95], v[252:255], v[124:127], v[92:95]\n\t"
    "s_mov_b32 %[st], 0\n\t"
    "s_nop 5\n\t"
    "s_branch 9f\n\t"
    "13:\n\t"
    "s_memrealtime s[36:37]\n\t"
    "v_mov_b32 v79, 0\n\t"
    "s_waitcnt lgkmcnt(0)\n\t"
    "s_mov_b32 %[tk], s36\n\t"
    "s_add_u32 s36, s36, %[bl]\n\t"
    "s_and_b32 s36, s36, 0xffffff\n\t"
    "s_and_b32 s37, %[bl], 0xff000000\n\t"
    "s_or_b32 s36, s36, s37\n\t"
    "v_mov_b32 v78, s36\n\t"
    "s_mov_b64 exec, 1\n\t"
    "global_store_dword v79, v78, %[pg] offset:64 sc1\n\t"
    "s_mov_b64 exec, -1\n\t"
    "s_branch 14b\n\t"
    "3:\n\t"
    "7:\n\t"
    "v_mov_b32 v76, %[cn]\n\t"
    "v_mov_b32 v77, 1\n\t"
    "v_mov_b32 v78, %[pv]\n\t"
    "v_mov_b32 v79, 0\n\t"
    "s_waitcnt vmcnt(0)\n\t"
    "s_mov_b64 exec, 1\n\t"
    "ds_add_rtn_u32 v77, v76, v77\n\t"
    "s_waitcnt lgkmcnt(0)\n\t"
    "v_readfirstlane_b32 %[st], v77\n\t"
    "s_and_b32 %[st], %[st], 3\n\t"
    "s_cmp_lg_u32 %[st], 3\n\t"
    "s_cbranch_scc1 6f\n\t"
    "global_store_dword v79, v78, %[pg] sc1\n\t"
    "6:\n\t"
    "s_mov_b64 exec, -1\n\t"
    "s_bitcmp1_b32 %[x], 2\n\t"
    "s_cbranch_scc1 8b\n\t"
    "s_branch 4b\n\t"
    "12:\n\t"
    "s_waitcnt lgkmcnt(0)\n\t"
    "9:"
    : "=&{v[80:95]}"(pd), [st] "=&s"(st), [tk] "+&s"(tk)
    : [ea] "v"(ea), [da] "v"(da), [x] "s"(x), "{v[128:159]}"(W[0]), "{v[160:191]}"(W[1]), "{v[192:223]}"(W[2]), "{v[224:255]}"(W[3]), [pv] "s"(pv), [cn] "s"(cn), [pg] "s"(pg), [bl] "s"(bl)
    : "v96", "v97", "v98", "v99", "v100", "v101", "v102", "v103", "v104", "v105", "v106", "v107", "v108", "v109", "v110", "v111", "v112", "v113", "v114", "v115", "v116", "v117", "v118", "v119", "v120", "v121", "v122", "v123", "v124", "v125", "v126", "v127", "v76", "v77", "v78", "v79", "vcc", "scc", "m0", "memory", "s36", "s37", "s38", "s39");
}
#define LEAN_QB 1
#define PREK 10
#define NEARLY 16
__device__ __forceinline__ void fin_ld(const u32x16& pd, unsigned rw, unsigned fa, unsigned fv) {
  asm volatile("ds_write_b128 %[rw], v[80:83] offset:0\n\t"
    "ds_write_b128 %[rw], v[84:87] offset:1024\n\t"
    "ds_write_b128 %[rw], v[88:91] offset:2048\n\t"
    "ds_write_b128 %[rw], v[92:95] offset:3072\n\t"
    "ds_write_b32 %[fa], %[fv]"
    :
    : [rw] "v"(rw), "{v[80:95]}"(pd), [fa] "v"(fa), [fv] "v"(fv)
    : "memory");
}
__device__ __forceinline__ void fin_st0(const u32x16& pd, unsigned rw, unsigned fa, unsigned fv, const char* cb, unsigned co0, unsigned co1, unsigned pa, unsigned& bad, unsigned& pk) {
  asm volatile("1:\n\t"
    "ds_read_b32 v39, %[fa]\n\t"
    "ds_read_b128 v[48:51], %[rw] offset:0\n\t"
    "ds_read_b128 v[52:55], %[rw] offset:1024\n\t"
    "ds_read_b128 v[56:59], %[rw] offset:2048\n\t"
    "ds_read_b128 v[60:63], %[rw] offset:3072\n\t"
    "s_waitcnt lgkmcnt(2)\n\t"
    "v_cmp_ne_u32 vcc, v39, %[fv]\n\t"
    "s_cbranch_vccz 2f\n\t"
    "s_waitcnt lgkmcnt(0)\n\t"
    "s_branch 1b\n\t"
    "2:\n\t"
    "v_add_f32 v48, v48, v80\n\t"
    "v_add_f32 v49, v49, v81\n\t"
    "v_add_f32 v50, v50, v82\n\t"
    "v_add_f32 v51, v51, v83\n\t"
    "v_add_f32 v52, v52, v84\n\t"
    "v_add_f32 v53, v53, v85\n\t"
    "v_add_f32 v54, v54, v86\n\t"
    "v_add_f32 v55, v55, v87\n\t"
    "v_cvt_pk_bf16_f32 v40, v48, v49\n\t"
    "v_cvt_pk_bf16_f32 v41, v50, v51\n\t"
    "v_cvt_pk_bf16_f32 v42, v52, v53\n\t"
    "v_cvt_pk_bf16_f32 v43, v54, v55\n\t"
    "global_store_dwordx4 %[co0], v[40:43], %[cb] sc1\n\t"
    "s_waitcnt lgkmcnt(0)\n\t"
    "v_mov_b32 v39, %[pa]\n\t"
    "ds_read_b32 v39, v39\n\t"
    "v_add_f32 v56, v56, v88\n\t"
    "v_add_f32 v57, v57, v89\n\t"
    "v_add_f32 v58, v58, v90\n\t"
    "v_add_f32 v59, v59, v91\n\t"
    "v_add_f32 v60, v60, v92\n\t"
    "v_add_f32 v61, v61, v93\n\t"
    "v_add_f32 v62, v62, v94\n\t"
    "v_add_f32 v63, v63, v95\n\t"
    "v_cvt_pk_bf16_f32 v44, v56, v57\n\t"
    "v_cvt_pk_bf16_f32 v45, v58, v59\n\t"
    "v_cvt_pk_bf16_f32 v46, v60, v61\n\t"
    "v_cvt_pk_bf16_f32 v47, v62, v63\n\t"
    "global_store_dwordx4 %[co1], v[44:47], %[cb] sc1\n\t"
    "v_cmp_u_f32 vcc, v48, v56\n\t"
    "s_waitcnt lgkmcnt(0)\n\t"
    "s_or_b32 %[bad], vcc_lo, vcc_hi\n\t"
    "v_readfirstlane_b32 %[pk], v39"
    : [bad] "=&s"(bad), [pk] "=&s"(pk)
    : [rw] "v"(rw), "{v[80:95]}"(pd), [fa] "v"(fa), [fv] "v"(fv), [cb] "s"(cb), [co0] "v"(co0), [co1] "v"(co1), [pa] "s"(pa)
    : "v39", "v40", "v41", "v42", "v43", "v44", "v45", "v46", "v47", "v48", "v49", "v50", "v51", "v52", "v53", "v54", "v55", "v56", "v57", "v58", "v59", "v60", "v61", "v62", "v63", "vcc", "scc", "memory");
}
__device__ __forceinline__ void fin_st1(const u32x16& pd, unsigned rw, unsigned fa, unsigned fv, const char* cb, unsigned co0, unsigned co1, unsigned pa, unsigned& bad, unsigned& pk) {
  asm volatile("1:\n\t"
    "ds_read_b32 v39, %[fa]\n\t"
    "ds_read_b128 v[48:51], %[rw] offset:0\n\t"
    "ds_read_b128 v[52:55], %[rw] offset:1024\n\t"
    "ds_read_b128 v[56:59], %[rw] offset:2048\n\t"
    "ds_read_b128 v[60:63], %[rw] offset:3072\n\t"
    "s_waitcnt lgkmcnt(2)\n\t"
    "v_cmp_ne_u32 vcc, v39, %[fv]\n\t"
    "s_cbranch_vccz 2f\n\t"
    "s_waitcnt lgkmcnt(0)\n\t"
    "s_branch 1b\n\t"
    "2:\n\t"
    "v_add_f32 v48, v48, v80\n\t"
    "v_add_f32 v49, v49, v81\n\t"
    "v_add_f32 v50, v50, v82\n\t"
    "v_add_f32 v51, v51, v83\n\t"
    "v_add_f32 v52, v52, v84\n\t"
    "v_add_f32 v53, v53, v85\n\t"
    "v_add_f32 v54, v54, v86\n\t"
    "v_add_f32 v55, v55, v87\n\t"
    "s_waitcnt lgkmcnt(0)\n\t"
    "v_mov_b32 v39, %[pa]\n\t"
    "ds_read_b32 v39, v39\n\t"
    "v_add_f32 v56, v56, v88\n\t"
    "v_add_f32 v57, v57, v89\n\t"
    "v_add_f32 v58, v58, v90\n\t"
    "v_add_f32 v59, v59, v91\n\t"
    "v_add_f32 v60, v60, v92\n\t"
    "v_add_f32 v61, v61, v93\n\t"
    "v_add_f32 v62, v62, v94\n\t"
    "v_add_f32 v63, v63, v95\n\t"
    "v_mul_f32 v40, %[nl], v48\n\t"
    "v_mul_f32 v41, %[nl], v49\n\t"
    "v_mul_f32 v42, %[nl], v50\n\t"
    "v_mul_f32 v43, %[nl], v51\n\t"
    "v_mul_f32 v44, %[nl], v56\n\t"
    "v_mul_f32 v45, %[nl], v57\n\t"
    "v_mul_f32 v46, %[nl], v58\n\t"
    "v_mul_f32 v47, %[nl], v59\n\t"
    "v_exp_f32 v40, v40\n\t"
    "v_exp_f32 v41, v41\n\t"
    "v_exp_f32 v42, v42\n\t"
    "v_exp_f32 v43, v43\n\t"
    "v_exp_f32 v44, v44\n\t"
    "v_exp_f32 v45, v45\n\t"
    "v_exp_f32 v46, v46\n\t"
    "v_exp_f32 v47, v47\n\t"
    "v_add_f32 v40, 1.0, v40\n\t"
    "v_add_f32 v41, 1.0, v41\n\t"
    "v_add_f32 v42, 1.0, v42\n\t"
    "v_add_f32 v43, 1.0, v43\n\t"
    "v_add_f32 v44, 1.0, v44\n\t"
    "v_add_f32 v45, 1.0, v45\n\t"
    "v_add_f32 v46, 1.0, v46\n\t"
    "v_add_f32 v47, 1.0, v47\n\t"
    "v_rcp_f32 v40, v40\n\t"
    "v_rcp_f32 v41, v41\n\t"
    "v_rcp_f32 v42, v42\n\t"
    "v_rcp_f32 v43, v43\n\t"
    "v_rcp_f32 v44, v44\n\t"
    "v_rcp_f32 v45, v45\n\t"
    "v_rcp_f32 v46, v46\n\t"
    "v_rcp_f32 v47, v47\n\t"
    "v_mul_f32 v40, v48, v40\n\t"
    "v_mul_f32 v41, v49, v41\n\t"
    "v_mul_f32 v42, v50, v42\n\t"
    "v_mul_f32 v43, v51, v43\n\t"
    "v_mul_f32 v44, v56, v44\n\t"
    "v_mul_f32 v45, v57, v45\n\t"
    "v_mul_f32 v46, v58, v46\n\t"
    "v_mul_f32 v47, v59, v47\n\t"
    "v_mul_f32 v40, v40, v52\n\t"
    "v_mul_f32 v41, v41, v53\n\t"
    "v_mul_f32 v42, v42, v54\n\t"
    "v_mul_f32 v43, v43, v55\n\t"
    "v_mul_f32 v44, v44, v60\n\t"
    "v_mul_f32 v45, v45, v61\n\t"
    "v_mul_f32 v46, v46, v62\n\t"
    "v_mul_f32 v47, v47, v63\n\t"
    "v_cvt_pk_bf16_f32 v48, v40, v41\n\t"
    "v_cvt_pk_bf16_f32 v49, v42, v43\n\t"
    "v_cvt_pk_bf16_f32 v50, v44, v45\n\t"
    "v_cvt_pk_bf16_f32 v51, v46, v47\n\t"
    "s_nop 1\n\t"
    "v_permlane16_swap_b32 v48, v50\n\t"
    "v_permlane16_swap_b32 v49, v51\n\t"
    "global_store_dwordx4 %[co0], v[48:51], %[cb] sc1\n\t"
    "v_cmp_u_f32 vcc, v52, v60\n\t"
    "s_waitcnt lgkmcnt(0)\n\t"
    "s_or_b32 %[bad], vcc_lo, vcc_hi\n\t"
    "v_readfirstlane_b32 %[pk], v39"
    : [bad] "=&s"(bad), [pk] "=&s"(pk)
    : [rw] "v"(rw), "{v[80:95]}"(pd), [fa] "v"(fa), [fv] "v"(fv), [cb] "s"(cb), [co0] "v"(co0), [nl] "s"(-LOG2E), [pa] "s"(pa)
    : "v39", "v40", "v41", "v42", "v43", "v44", "v45", "v46", "v47", "v48", "v49", "v50", "v51", "v52", "v53", "v54", "v55", "v56", "v57", "v58", "v59", "v60", "v61", "v62", "v63", "vcc", "scc", "memory");
}
__device__ __forceinline__ void fin_st2(const u32x16& pd, unsigned rw, unsigned fa, unsigned fv, const char* cb, unsigned co0, unsigned co1, unsigned pa, unsigned& bad, unsigned& pk) {
  asm volatile("s_nop 1\n\t"
    "v_cvt_pk_bf16_f32 v40, v80, v81\n\t"
    "v_cvt_pk_bf16_f32 v41, v82, v83\n\t"
    "v_cvt_pk_bf16_f32 v42, v84, v85\n\t"
    "v_cvt_pk_bf16_f32 v43, v86, v87\n\t"
    "global_store_dwordx4 %[co0], v[40:43], %[cb] sc1\n\t"
    "v_cvt_pk_bf16_f32 v44, v88, v89\n\t"
    "v_cvt_pk_bf16_f32 v45, v90, v91\n\t"
    "v_cvt_pk_bf16_f32 v46, v92, v93\n\t"
    "v_cvt_pk_bf16_f32 v47, v94, v95\n\t"
    "global_store_dwordx4 %[co1], v[44:47], %[cb] sc1\n\t"
    "1:\n\t"
    "ds_read_b32 v39, %[fa]\n\t"
    "ds_read_b128 v[48:51], %[rw] offset:0\n\t"
    "ds_read_b128 v[52:55], %[rw] offset:1024\n\t"
    "ds_read_b128 v[56:59], %[rw] offset:2048\n\t"
    "ds_read_b128 v[60:63], %[rw] offset:3072\n\t"
    "s_waitcnt lgkmcnt(2)\n\t"
    "v_cmp_ne_u32 vcc, v39, %[fv]\n\t"
    "s_cbranch_vccz 2f\n\t"
    "s_waitcnt lgkmcnt(0)\n\t"
    "s_branch 1b\n\t"
    "2:\n\t"
    "v_cvt_pk_bf16_f32 v48, v48, v49\n\t"
    "v_cvt_pk_bf16_f32 v49, v50, v51\n\t"
    "v_cvt_pk_bf16_f32 v50, v52, v53\n\t"
    "v_cvt_pk_bf16_f32 v51, v54, v55\n\t"
    "global_store_dwordx4 %[co0], v[48:51], %[cb] offset:2048 sc1\n\t"
    "s_waitcnt lgkmcnt(0)\n\t"
    "v_mov_b32 v39, %[pa]\n\t"
    "ds_read_b32 v39, v39\n\t"
    "v_cvt_pk_bf16_f32 v56, v56, v57\n\t"
    "v_cvt_pk_bf16_f32 v57, v58, v59\n\t"
    "v_cvt_pk_bf16_f32 v58, v60, v61\n\t"
    "v_cvt_pk_bf16_f32 v59, v62, v63\n\t"
    "global_store_dwordx4 %[co1], v[56:59], %[cb] offset:2048 sc1\n\t"
    "v_cmp_u_f32 vcc, v80, v88\n\t"
    "s_waitcnt lgkmcnt(0)\n\t"
    "s_or_b32 %[bad], vcc_lo, vcc_hi\n\t"
    "v_readfirstlane_b32 %[pk], v39"
    : [bad] "=&s"(bad), [pk] "=&s"(pk)
    : [rw] "v"(rw), "{v[80:95]}"(pd), [fa] "v"(fa), [fv] "v"(fv), [cb] "s"(cb), [co0] "v"(co0), [co1] "v"(co1), [pa] "s"(pa)
    : "v39", "v40", "v41", "v42", "v43", "v44", "v45", "v46", "v47", "v48", "v49", "v50", "v51", "v52", "v53", "v54", "v55", "v56", "v57", "v58", "v59", "v60", "v61", "v62", "v63", "vcc", "scc", "memory");
}
template <int R> __device__ __forceinline__ void gw_load(u32x32& w, unsigned a0, unsigned a1);
template <> __device__ __forceinline__ void gw_load<0>(u32x32& w, unsigned a0, unsigned a1) {
  asm volatile("ds_read_b64_tr_b16 v[128:129], %1 offset:0\n\t"
    "ds_read_b64_tr_b16 v[130:131], %1 offset:512\n\t"
    "ds_read_b64_tr_b16 v[132:133], %2 offset:0\n\t"
    "ds_read_b64_tr_b16 v[134:135], %2 offset:512\n\t"
    "ds_read_b64_tr_b16 v[136:137], %1 offset:4096\n\t"
    "ds_read_b64_tr_b16 v[138:139], %1 offset:4608\n\t"
    "ds_read_b64_tr_b16 v[140:141], %2 offset:4096\n\t"
    "ds_read_b64_tr_b16 v[142:143], %2 offset:4608\n\t"
    "s_waitcnt lgkmcnt(0)\n\t"
    "ds_read_b64_tr_b16 v[144:145], %1 offset:16384\n\t"
    "ds_read_b64_tr_b16 v[146:147], %1 offset:16896\n\t"
    "ds_read_b64_tr_b16 v[148:149], %2 offset:16384\n\t"
    "ds_read_b64_tr_b16 v[150:151], %2 offset:16896\n\t"
    "ds_read_b64_tr_b16 v[152:153], %1 offset:20480\n\t"
    "ds_read_b64_tr_b16 v[154:155], %1 offset:20992\n\t"
    "ds_read_b64_tr_b16 v[156:157], %2 offset:20480\n\t"
    "ds_read_b64_tr_b16 v[158:159], %2 offset:20992\n\t"
    "s_waitcnt lgkmcnt(0)"
    : "=&{v[128:159]}"(w) : "v"(a0), "v"(a1) : "memory");
}
template <> __device__ __forceinline__ void gw_load<1>(u32x32& w, unsigned a0, unsigned a1) {
  asm volatile("ds_read_b64_tr_b16 v[160:161], %1 offset:0\n\t"
    "ds_read_b64_tr_b16 v[162:163], %1 offset:512\n\t"
    "ds_read_b64_tr_b16 v[164:165], %2 offset:0\n\t"
    "ds_read_b64_tr_b16 v[166:167], %2 offset:512\n\t"
    "ds_read_b64_tr_b16 v[168:169], %1 offset:4096\n\t"
    "ds_read_b64_tr_b16 v[170:171], %1 offset:4608\n\t"
    "ds_read_b64_tr_b16 v[172:173], %2 offset:4096\n\t"
    "ds_read_b64_tr_b16 v[174:175], %2 offset:4608\n\t"
    "s_waitcnt lgkmcnt(0)\n\t"
    "ds_read_b64_tr_b16 v[176:177], %1 offset:16384\n\t"
    "ds_read_b64_tr_b16 v[178:179], %1 offset:16896\n\t"
    "ds_read_b64_tr_b16 v[180:181], %2 offset:16384\n\t"
    "ds_read_b64_tr_b16 v[182:183], %2 offset:16896\n\t"
    "ds_read_b64_tr_b16 v[184:185], %1 offset:20480\n\t"
    "ds_read_b64_tr_b16 v[186:187], %1 offset:20992\n\t"
    "ds_read_b64_tr_b16 v[188:189], %2 offset:20480\n\t"
    "ds_read_b64_tr_b16 v[190:191], %2 offset:20992\n\t"
    "s_waitcnt lgkmcnt(0)"
    : "=&{v[160:191]}"(w) : "v"(a0), "v"(a1) : "memory");
}
template <> __device__ __forceinline__ void gw_load<2>(u32x32& w, unsigned a0, unsigned a1) {
  asm volatile("ds_read_b64_tr_b16 v[192:193], %1 offset:0\n\t"
    "ds_read_b64_tr_b16 v[194:195], %1 offset:512\n\t"
    "ds_read_b64_tr_b16 v[196:197], %2 offset:0\n\t"
    "ds_read_b64_tr_b16 v[198:199], %2 offset:512\n\t"
    "ds_read_b64_tr_b16 v[200:201], %1 offset:4096\n\t"
    "ds_read_b64_tr_b16 v[202:203], %1 offset:4608\n\t"
    "ds_read_b64_tr_b16 v[204:205], %2 offset:4096\n\t"
    "ds_read_b64_tr_b16 v[206:207], %2 offset:4608\n\t"
    "s_waitcnt lgkmcnt(0)\n\t"
    "ds_read_b64_tr_b16 v[208:209], %1 offset:16384\n\t"
    "ds_read_b64_tr_b16 v[210:211], %1 offset:16896\n\t"
    "ds_read_b64_tr_b16 v[212:213], %2 offset:16384\n\t"
    "ds_read_b64_tr_b16 v[214:215], %2 offset:16896\n\t"
    "ds_read_b64_tr_b16 v[216:217], %1 offset:20480\n\t"
    "ds_read_b64_tr_b16 v[218:219], %1 offset:20992\n\t"
    "ds_read_b64_tr_b16 v[220:221], %2 offset:20480\n\t"
    "ds_read_b64_tr_b16 v[222:223], %2 offset:20992\n\t"
    "s_waitcnt lgkmcnt(0)"
    : "=&{v[192:223]}"(w) : "v"(a0), "v"(a1) : "memory");
}
template <> __device__ __forceinline__ void gw_load<3>(u32x32& w, unsigned a0, unsigned a1) {
  asm volatile("ds_read_b64_tr_b16 v[224:225], %1 offset:0\n\t"
    "ds_read_b64_tr_b16 v[226:227], %1 offset:512\n\t"
    "ds_read_b64_tr_b16 v[228:229], %2 offset:0\n\t"
    "ds_read_b64_tr_b16 v[230:231], %2 offset:512\n\t"
    "ds_read_b64_tr_b16 v[232:233], %1 offset:4096\n\t"
    "ds_read_b64_tr_b16 v[234:235], %1 offset:4608\n\t"
    "ds_read_b64_tr_b16 v[236:237], %2 offset:4096\n\t"
    "ds_read_b64_tr_b16 v[238:239], %2 offset:4608\n\t"
    "s_waitcnt lgkmcnt(0)\n\t"
    "ds_read_b64_tr_b16 v[240:241], %1 offset:16384\n\t"
    "ds_read_b64_tr_b16 v[242:243], %1 offset:16896\n\t"
    "ds_read_b64_tr_b16 v[244:245], %2 offset:16384\n\t"
    "ds_read_b64_tr_b16 v[246:247], %2 offset:16896\n\t"
    "ds_read_b64_tr_b16 v[248:249], %1 offset:20480\n\t"
    "ds_read_b64_tr_b16 v[250:251], %1 offset:20992\n\t"
    "ds_read_b64_tr_b16 v[252:253], %2 offset:20480\n\t"
    "ds_read_b64_tr_b16 v[254:255], %2 offset:20992\n\t"
    "s_waitcnt lgkmcnt(0)"
    : "=&{v[224:255]}"(w) : "v"(a0), "v"(a1) : "memory");
}
template <int I, int E, class F> __device__ __forceinline__ void sfor(F&& f) { if constexpr (I < E) { f(std::integral_constant<int, I>{}); sfor<I + 1, E>(f); } }
template <int ROLE, int KQ, class F>
__device__ __forceinline__ void wload(char* smem, int wave_id_, const u16* W0, const u16* W1, int ldb, u32x32 (&bw)[4], int rot, F&& hook) {
  constexpr int ABUF = 65536;
  const int tid = opaque_tid(), lane = tid & 63, wave = WAVE_ID;
  const int kq = KQ == 2 ? wave >> 2 : 0, nq = KQ == 2 ? wave & 3 : wave;
  const int g = lane >> 4, r16 = lane & 15, q4 = r16 >> 2, p4 = lane & 3;
  const unsigned ldsb = __builtin_amdgcn_readfirstlane((unsigned)(size_t)(LAS char*)smem);
  {
    const int srow = 8 * wave + (lane >> 3), ch = (lane & 7) ^ bswz(srow);
    const unsigned voff = (unsigned)(srow * ldb + ch * 8) * 2;
    const char* w0 = uni((const char*)W0); const char* w1 = uni((const char*)(ROLE == R_F1 ? W1 : W0 + 64));
    const size_t kb = (size_t)64 * ldb * 2;
    auto wstage = [&](int r) {
      const unsigned ld = ldsb + (r & 1) * ABUF + wave * 1024;
#pragma unroll
      for (int i = 0; i < 8; ++i) {
        const int kbi = KQ == 2 ? (ROLE == R_PART2 ? 0 : (i >> 2) * 8) + ((2 * r + ((i >> 1) & 1)) ^ rot) : 2 * r + (i >> 2);
        const char* src = KQ == 2 ? ((i & 1) ? w1 : w0) + kbi * kb + (ROLE == R_PART2 ? (i >> 2) * 256 : 0) : w0 + kbi * kb + (i & 3) * 128;
        unsigned keep;
        asm volatile("s_mov_b32 %0, m0\n\ts_mov_b32 m0, %3\n\ts_nop 2\n\tglobal_load_lds_dwordx4 %1, %2" WNT_ "\n\ts_mov_b32 m0, %0"
                     : "=&s"(keep) : "v"(voff), "s"(src), "s"(ld + i * 8192) : "memory");
      }
    };
    const int brow = 8 * g + q4, bs = bswz(brow);
    unsigned la[2];
#pragma unroll
    for (int j = 0; j < 2; ++j) {
      int blk, c16, sub;
      if (ROLE == R_F1) { blk = j; c16 = nq * 2 + (p4 >> 1); sub = (p4 & 1) * 8; }
      else { blk = nq >> 1; c16 = (nq & 1) * 4 + p4; sub = j * 8; }
      int set = kq;
      if (ROLE == R_PART2 && TLP) { set = nq >> 1; blk = nq & 1; c16 = kq * 4 + p4; }
      la[j] = (unsigned)((KQ == 2 ? set * 4 + blk : blk) * 8192 + brow * 128 + ((c16 ^ bs) << 4) + sub);
    }
    hook(4);
    wstage(0); wstage(1);
    sfor<0, 4>([&](auto r_c) __attribute__((always_inline)) {
      constexpr int r = decltype(r_c)::value;
      if (r < 3) WAIT_V(8); else WAIT_V(0);
      __builtin_amdgcn_s_barrier(); asm volatile("" ::: "memory");
      gw_load<r>(bw[r], ldsb + (r & 1) * ABUF + la[0], ldsb + (r & 1) * ABUF + la[1]);
      BAR();
      if (r + 2 < 4) wstage(r + 2);
      hook(r);
    });
  }
}
#define DSR2(d0, d1, a0_, a1_, off) asm volatile("ds_read_b128 %0, %2 offset:%4\n\tds_read_b128 %1, %3 offset:%4" : "=&v"(d0), "=&v"(d1) : "v"(a0_), "v"(a1_), "i"(off))
template <int N> __device__ __forceinline__ void waitl(u32x4& a, u32x4& b) { asm volatile("s_waitcnt lgkmcnt(%2)" : "+v"(a), "+v"(b) : "i"(N)); }
__device__ __forceinline__ void glds1(const char* sbase, unsigned voff, unsigned lds) {
  unsigned keep;
  asm volatile("s_mov_b32 %0, m0\n\ts_mov_b32 m0, %3\n\ts_nop 2\n\tglobal_load_lds_dwordx4 %1, %2 sc1\n\ts_mov_b32 m0, %0"
               : "=&s"(keep) : "v"(voff), "s"(sbase), "s"(lds) : "memory");
}
#define XS(i) do { } while (0)
#define XFL(b_) do { } while (0)
#define XA_ 0
#define XF_ 0
#define XE_ 0
#define XP_ 0
#define PUBK 9
#define LATE 0
#define NOWP 0
#define TB 48
#define TPUB 1
#define DUR0 160
#define PUP 4
#define PDQ 5
#define PDM 2
#define PDEEP 5
template <int ROLE>
__device__ __forceinline__ void gemm7(char* smem, int wave_id_, const u16* W0, const u16* W1, int ldb, const char* A_, size_t a_blk, Dep dep,
                                      char* C_, size_t c_blk, unsigned*& cnt, int myw, unsigned* tmo, int slot, unsigned long long* mts,
                                      bool gl, int nimm = 0, int nseg = NSEG, int wm0 = 0, int pofs = 0, u64 tag = 0) {
  dep.base = (const char*)cnt;
  unsigned* myprog = cnt + myw;
  volatile LAS unsigned* wsl = (volatile LAS unsigned*)(smem + L_SLOT + 512);
  if (opaque_tid() < 144) *(volatile LAS unsigned*)(smem + L_SLOT + opaque_tid() * 4) = 0u;
  const int tid = opaque_tid(), lane = tid & 63, wave = WAVE_ID;
  const int kq = wave >> 2, nq = wave & 3;
  const unsigned ldsb = __builtin_amdgcn_readfirstlane((unsigned)(size_t)(LAS char*)smem);
  constexpr int rot = 0;
  const char* A = uni(A_) + nq * 1024;
  const char* Cu = uni(C_);
  constexpr int PD = PDM;
  constexpr bool P2C = ROLE == R_PART2;
  int pw = 0;
  int wm = wm0, nh = 0;
  constexpr bool SPEC = ROLE != R_QKV;
  auto lgo = [&](int b, unsigned& v, unsigned& u, bool pre, bool force) {
    if (wave == 4) {
      for (int spins = 0; __builtin_amdgcn_readfirstlane(wsl[2]) != (unsigned)(b + 1); ++spins) {
        if (!pre) ld_fb(v, u, dep.base, dep.voff);
        const bool ok = (int)(v * dep.mul + dep.add) > b;
        if (__all(ok)) break;
        if (SPEC && !force && bells(ok, u, b, MARG)) break;
        if (!pre) __builtin_amdgcn_s_sleep(2);
        pre = false;
        if (give_up(spins, tmo, 9)) { v = 255u; break; }
      }
      wsl[2] = (unsigned)(b + 1);
    } else {
      while (__builtin_amdgcn_readfirstlane(wsl[2]) != (unsigned)(b + 1)) __builtin_amdgcn_s_sleep(1);
    }
  };
  auto lwm = [&](int b, unsigned v) {
    unsigned w;
    if (wave == 4) {
      const int w2 = (int)dep_eval(v, dep);
      w = (unsigned)min(max(max(w2, wm), b + 1), 255);
      wsl[0] = ((unsigned)(b + 1) << 8) | w;
    } else {
      for (;;) {
        w = __builtin_amdgcn_readfirstlane(wsl[0]);
        if ((w >> 8) == (unsigned)(b + 1)) break;
        __builtin_amdgcn_s_sleep(1);
      }
      w &= 255u;
    }
    return (int)w;
  };
  auto pubw = [&](int n) {
    WAIT_V(0);
    if (lane == 0) {
      const unsigned o = __hip_atomic_fetch_add((LAS unsigned*)(smem + L_SLOT + 516), 1u, __ATOMIC_RELAXED, __HIP_MEMORY_SCOPE_WORKGROUP);
      if ((o & 3u) == 3u) {
        st4_sc1(myprog, (unsigned)(n + pofs));
      }
    }
    asm volatile("s_waitcnt lgkmcnt(0)" ::: "memory");
  };
  auto hsrc = [&](int hh) { return A + (size_t)(hh >> 1) * a_blk + ((hh ^ (rot >> 2)) & 1) * 16384; };
  auto hlds = [&](int hh) { return ldsb + (unsigned)(((hh >> 1) & 1) * 65536 + (hh & 1) * 16384 + nq * 1024); };
#define POFF(i_) (((i_) & 1) * 32768 + ((i_) >> 1) * 4096)
#define PIECE(s_, l_, i_) asm volatile("s_add_u32 m0, %2, %3\n\ts_nop 0\n\tglobal_load_lds_dwordx4 %0, %1 sc1" :: "v"(av[i_]), "s"(s_), "s"(l_), "i"(POFF(i_)) : "memory", "scc", "m0")
  int na = SB;
  auto arm = [&](int bb) {
    unsigned z = SENT; asm volatile("" : "+v"(z));
    constexpr int N = ROLE == R_F1 ? 1 : (P2C ? 4 : 2);
    const unsigned o = nq * N * 1024 + lane_id() * 16;
    const char* p = Cu + (size_t)bb * c_blk;
#pragma unroll
    for (int i = 0; i < N; ++i) st16s(p, o + i * 1024, u32x4{z, z, z, z});
  };
  u32x32 BW[4];
  wload<ROLE, 2>(smem, wave_id_, W0, W1, ldb, BW, rot, [&](int r) {
    if (ROLE == R_QKV && nimm > 0 && wave == 0) STS((char*)tmo - OFF_TMO, 128 + slot, 3 + r);
    if (SPEC && r == 2 && (ROLE != R_PART || gl)) { for (na += kq; na < NSEG; na += 2) arm(na); na = NSEG; }
    if (ROLE == R_QKV && nimm > 0 && r == 0 && wave == 4) init_issue((char*)tmo - OFF_TMO, ldsb + L_RED);
    if (ROLE == R_QKV && nimm > 0 && r == 2) {
      cnt = cnt_of((char*)tmo - OFF_TMO, init_get((char*)tmo - OFF_TMO, tag, smem + L_RED));
      dep.base = (const char*)cnt; myprog = cnt + myw;
      if (kq) {
        unsigned v0 = 0, u0;
        lgo(0, v0, u0, false, true);
        wm = lwm(0, v0);
        const unsigned l16 = lane_id() * 16;
#pragma unroll
        for (int i = 0; i < 16; ++i) glds1(hsrc(i >> 3) + (i & 1) * 32768 + ((((i & 7) >> 1) ^ rot) & 3) * 4096, l16, hlds(i >> 3) + POFF(i & 7));
      }
      nh = 2;
    }
  });
  if (ROLE == R_QKV && nimm > 0 && !kq) wm = (int)(__builtin_amdgcn_readfirstlane(wsl[0]) & 255u);
  if (ROLE == R_QKV && nimm > 0 && opaque_tid() == 0) STS((char*)tmo - OFF_TMO, 128 + slot, 0);
  if (wave == 0) WFB();
  auto body = [&](auto ldr_c) __attribute__((always_inline)) {
    constexpr bool LDR = decltype(ldr_c)::value;
    if (XP_ == 0 ? !LDR : (XP_ == 1 ? LDR : false)) __builtin_amdgcn_s_setprio(1);
    unsigned av[8];
#pragma unroll
    for (int i = 0; i < 8; ++i) av[i] = lane * 16 + (P2C ? 0 : (i & 1) * 32768) + (((i >> 1) ^ rot) & 3) * 4096;
    auto issue_all = [&](int hh) {
      if (LDR) {
        const char* s_ = hsrc(hh); const unsigned l_ = hlds(hh);
#pragma unroll
        for (int i = 0; i < 8; ++i) if (!(P2C && (i & 1))) PIECE(s_, l_, i);
      }
    };
    int b = 0;
    bool pre = false;
    unsigned smu = 0, tk = 0;
    unsigned smp = 0, got = 0;
    while (b < nseg) {
      const bool fast = (wm < nseg ? wm : nseg) >= b + PD && b >= nimm && nh <= 2 * b + 2;
      if (SPEC && b >= TB && !fast) break;
      const bool pl = wm <= b || pre, cold = nh < 2 * b + 2, nh3 = nh == 2 * b + 3;
      if (!fast) {
        XS(0);
        if (LDR && wm <= b && !got) lgo(b, smp, smu, false, true);
        XS(1);
        if (got) nh += NEARLY / 8;
        { const int upto = nh3 ? 2 * b + 4 : 2 * b + 2; for (; nh < upto; ++nh) issue_all(nh); }
        if (LDR && pl) wm = lwm(b, smp);
        XS(2);
      }
      const int ln = lane_id(), g = ln >> 4, r16 = ln & 15;
      const unsigned e0 = ldsb + (unsigned)((P2C ? 0 : kq * 32768) + (b & 1) * 65536 + (gl ? (g >> 1) * 512 + r16 * 32 + (g & 1) * 16 : r16 * 128 + ((g ^ (r16 & 7)) << 4)));
      const unsigned d0 = e0 ^ (gl ? 1024u : 64u);
      const unsigned rw = ldsb + L_RED + nq * 4096 + ln * 16;
      const unsigned co0 = ROLE == R_F1 ? (unsigned)((g & 1) * 2048 + nq * 512 + r16 * 32 + (g >> 1) * 16)
                         : ((ROLE == R_QKV ? TLQ : TLP) ? (unsigned)(nq * (P2C ? 4096 : 2048) + r16 * 64 + g * 16)
                         : (ROLE == R_QKV ? (unsigned)(r16 * 256 + (((4 * nq + g) ^ r16) << 4)) : (unsigned)(r16 * PROW + (32 * nq + 8 * g) * 2)));
      const unsigned co1 = co0 + ((ROLE == R_QKV ? TLQ : TLP) ? 1024u : (ROLE == R_QKV ? 4096u : 16u * PROW));
      u32x16 pv_;
      bool pr2 = false;

      if (fast) {
        __builtin_amdgcn_s_barrier(); asm volatile("" ::: "memory");
        const int LA = (wm < nseg ? wm : nseg) >= b + (ROLE == R_QKV ? PDQ : PDEEP) ? 3 : 2;
        for (; nh < 2 * b + LA; ++nh) issue_all(nh);
        int lf = (wm < nseg ? wm : nseg) - (LA - 1), cp = nseg - (LA - 1);
        const unsigned sa = ldsb + L_SLOT + ln * 4, ad = dep.add - (unsigned)(LA - 1);
        const unsigned up = LA == 3 ? 1000u : (unsigned)PUP;
#define FARGS BW, pv_, e0, d0, b, lf, cp, up, sa, dep.mul, ad, rw, (unsigned)a_blk, hsrc(2 * b + LA), hsrc(2 * b + LA + 1), hlds(2 * b + LA), hlds(2 * b + LA + 1), av
        if constexpr (LDR) (P2C ? fast_ld2 : fast_ld0)(FARGS, LA);
#undef FARGS
        else {
          const char* c0_ = uni((const char*)tmo - OFF_TMO + OFF_DUMP); const char* c1_ = Cu + (size_t)b * c_blk;
          const int sf = b + pofs;
          const char* pg = uni((const char*)myprog);
          (ROLE == R_F1 ? fast_st1 : (P2C ? fast_st2 : fast_st0))(BW, pv_, e0, d0, b, lf, cp, up, sa, dep.mul, ad, rw, (unsigned)c_blk, c0_, c1_, co0, co1, pofs, sf, (unsigned)wave,
                                                                   ldsb + L_SLOT, dep.voff, dep.base, pg);
        }
        wm = lf + LA - 1 > wm ? lf + LA - 1 : wm;
        nh = 2 * b + 2;
        --b;
        __builtin_amdgcn_s_barrier(); asm volatile("" ::: "memory");
      } else {
        if (LDR) { if (nh >= 2 * b + 4) { if (P2C) WAIT_V(14); else WAIT_V(28); } else { if (P2C) WAIT_V(6); else WAIT_V(12); } }
        BAR();
        XS(3);
        if (ROLE == R_QKV && nimm > 0 && b < 2 && opaque_tid() == 0) STS((char*)tmo - OFF_TMO, 128 + slot, 1 + b);
        if constexpr (!LDR) {
          const unsigned wv = wsl[0];
          unsigned st;
          lean_st0(BW, e0, d0, __builtin_amdgcn_readfirstlane(pw > 0 ? (cold ? 4u : 1u) : 0u), pv_, (unsigned)(pw + pofs), ldsb + L_SLOT + 516, uni((const char*)myprog), 0u, tk, st);
          if (pl) wm = (int)(__builtin_amdgcn_readfirstlane(wv) & 255u);
        }
        const bool dm = nh == 2 * b + 2 && wm > b + 1 && b + 1 < nseg;
        pr2 = b + 1 < nseg && wm < nseg;
        if constexpr (LDR) {
          const bool er = nh == 2 * b + 2 && !dm;
          const unsigned ps = pr2 && (wave == 4 || er) ? (unsigned)(b + 1) : 0u;
#define LARGS BW, e0, d0, (unsigned)(nh - 2 * b - 2), hlds(nh), hsrc(nh), hsrc(nh + 1), av, ps, dep.voff, dep.base, \
              rw, ldsb + L_SLOT + 528 + nq * 4, (unsigned)(b + 1), ldsb + L_SLOT + 520, dep.mul, dep.add, er ? b + 1 : 0x7FFFFFFF, 0x7FFFFFFF, smp, smu, got
          if (dm) (P2C ? lean_dm1 : lean_dm0)(LARGS); else (P2C ? lean_ld1 : lean_ld0)(LARGS);
#undef LARGS
          got = __builtin_amdgcn_readfirstlane(got);
        }
        if (dm) nh += 2;
        XS(5);
      }
      unsigned bad = 0, pk = 0;
      if constexpr (LDR) { if (fast) fin_ld(pv_, rw, ldsb + L_SLOT + 528 + nq * 4, (unsigned)(b + 1)); }
      else (ROLE == R_F1 ? fin_st1 : (P2C ? fin_st2 : fin_st0))(pv_, rw, ldsb + L_SLOT + 528 + nq * 4, (unsigned)(b + 1), Cu + (size_t)b * c_blk, co0, co1, ldsb + L_SLOT + 524, bad, pk);
      XS(6);
      pre = pr2;
      bool now = (nh < 2 * b + 4 && wm < b + 1 + PD) || b + 1 >= nseg || b < nimm;
      if (!LDR && now) {
        if (NOWP == 0 && b >= nimm && pk == (unsigned)(b + 2)) now = false;
        else pubw(b + 1);
      }
      XS(7);
      XFL(b);
      pw = now ? 0 : b + 1;
      if (SPEC && !LDR && now && na < NSEG && na > b + 1) arm(na++);
      ++b;
    }
    if (SPEC && b < nseg) {
      static_assert(!SPEC || (NEARLY == 16 && TLP), "");
      const int ln = lane_id(), g = ln >> 4, r16 = ln & 15;
      const unsigned e0 = ldsb + (unsigned)((P2C ? 0 : kq * 32768) + (gl ? (g >> 1) * 512 + r16 * 32 + (g & 1) * 16 : r16 * 128 + ((g ^ (r16 & 7)) << 4)));
      const unsigned rw = ldsb + L_RED + nq * 4096 + ln * 16;
      const unsigned co0 = ROLE == R_F1 ? (unsigned)((g & 1) * 2048 + nq * 512 + r16 * 32 + (g >> 1) * 16) : (unsigned)(nq * (P2C ? 4096 : 2048) + r16 * 64 + g * 16);
      const char* pg = uni((const char*)myprog);
      bool force = false;
      unsigned dur = DUR0, dp = 0, x8 = __builtin_amdgcn_readfirstlane(wave == 0 ? 8u : 0u);
      if (nh >= 2 * b + 2) got = 1;
      pre = false;
      for (;;) {
        unsigned rd;
        XS(0);
        if (LDR && b < nseg) {
          if (!got) { lgo(b, smp, smu, pre, force); issue_all(2 * b); issue_all(2 * b + 1); }
          XS(2);
          if (P2C) WAIT_V(6); else WAIT_V(12);
        }
        if (!LDR && b >= SB && na <= NSEG) { for (na = max(na, b); na < NSEG; ++na) arm(na); WAIT_V(0); ++na; }
        const unsigned ea = e0 + (b & 1) * 65536;
        BAR();
        XS(3);
        if (b >= nseg) rd = __builtin_amdgcn_readfirstlane(wsl[8]);
        else if constexpr (LDR) {
          const int th = b + 1 < nseg ? b + 1 : 0;
          (P2C ? lean_ld1 : lean_ld0)(BW, ea, ea ^ (gl ? 1024u : 64u), 0u, hlds(2 * b + 2), hsrc(2 * b + 2), hsrc(2 * b + 3), av, (unsigned)th, dep.voff, dep.base,
                                      rw, ldsb + L_SLOT + 528 + nq * 4, (unsigned)(b + 1), ldsb + L_SLOT + 520, dep.mul, dep.add, th, th >= SB ? th : 0x7FFFFFFF, smp, smu, got);
          got = __builtin_amdgcn_readfirstlane(got);
          rd = got >> 1;
          pre = true;
        } else {
          u32x16 pv_;
          lean_st0(BW, ea, ea ^ (gl ? 1024u : 64u), b >= SB - 2 ? (unsigned)min(pw, 1) | x8 : (unsigned)min(pw, 1) * TPUB, pv_, (unsigned)pw, ldsb + L_SLOT + 516, pg, (unsigned)(b + 1) << 24 | dur, tk, rd);
          rd = __builtin_amdgcn_readfirstlane(rd);
          XS(5);
          if (!rd) {
            unsigned bad, pk;
            (ROLE == R_F1 ? fin_st1 : (P2C ? fin_st2 : fin_st0))(pv_, rw, ldsb + L_SLOT + 528 + nq * 4, (unsigned)(b + 1), Cu + (size_t)b * c_blk, co0, co0 + 1024u,
                                                                 ldsb + L_SLOT + 524, bad, pk);
            XS(6);
            pw = b + 1;
            if (x8 && b >= SB - 2) { const unsigned d = min(rt_now() - __builtin_amdgcn_readfirstlane(tk), 300u); dur = max(d, dp); dp = d; }
            if (bad && !force && b >= SB) wsl[8] = 1u;
            else if (b >= SB - 1 ? b + 1 >= nseg : pk != (unsigned)(b + 2)) {
              pubw(b + 1);
              pw = 0;
              if (na < NSEG && na > b + 1) arm(na++);
            }
          }
        }
        XS(7);
        if (rd) {
          WAIT_V(0);
          BAR();
          if (wave == 4 && ln < 9) wsl[ln] = 0u;
          BAR();
          --b; pw = 0; pre = false; got = 0; force = true;
          continue;
        }
        if (b >= nseg) break;
        force = false;
        ++b;
      }
    }
    __builtin_amdgcn_s_setprio(0);
  };
  if (kq) body(std::true_type{}); else body(std::false_type{});
#undef PIECE
#undef POFF
  WAIT_V(0);
  BAR();
}
__device__ __forceinline__ void fin_role(char* smem, int wave_id_, int fidx, const float* mod, const char* P2, const u16* x1, u16* out, unsigned* cnt, unsigned* tmo,
                                         unsigned long long* mts) {
  const int tid = opaque_tid(), lane = tid & 63, wave = WAVE_ID;
  row_table(smem + L_GB, mod, cnt, tmo, tid);
  volatile LAS unsigned* wsl = (volatile LAS unsigned*)(smem + L_SLOT + 512);
  int wf = 0, par = 0;
  const unsigned pf2 = (unsigned)(PF2 + ((lane & 31) < 28 ? (lane & 31) : 0)) * 128, pln = (unsigned)((PLN + (lane >> 3)) * 128 + (lane & 7) * 4);
  for (int nf = fidx >> 3; nf < NSEG; nf += 2) {
    if (wf <= nf) {
      if (wave == 0) {
        unsigned w;
        for (int spins = 0;; ++spins) {
          unsigned v, u;
          ld_fb(v, u, cnt, pf2);
          if (!__all(v > (unsigned)nf) && bells(v > (unsigned)nf, u, nf, MARF)) { w = 0; break; }
          v = rowmin16(v);
          w = min(rdl(v, 0), rdl(v, 16));
          if ((int)w > nf) break;
          __builtin_amdgcn_s_sleep(2);
          if (give_up(spins, tmo, 9)) { w = NSEG; break; }
        }
        if (lane == 0) wsl[par] = w;
      }
      BAR();
      wf = (int)__builtin_amdgcn_readfirstlane(wsl[par]);
      par ^= 1;
    }
    int ln = lane; asm volatile("" : "+v"(ln));
    if (wave == 0) WTS(256 + 8 + fidx, nf / 2, 0);
    bool fin = nf < SB;
    for (int spins = 0; !fin_half(fin, P2, x1, smem + L_GB + 12288, out, nf * 32 + 4 * (fidx & 7) + (wave >> 1), wave & 1, ln); ++spins)
      fin = __all(ld4_sc1((const char*)cnt + pf2) > (unsigned)nf && ld4_sc1((const char*)cnt + pln) > (unsigned)nf) || give_up(spins, tmo, 9);
    if (wave == 0) WTS(256 + 8 + fidx, nf / 2, 2);
  }
}
#define LD8(r, vo, sb) asm volatile("global_load_dwordx4 %0, %8, %12 sc1\n\tglobal_load_dwordx4 %1, %9, %12 sc1\n\t" \
    "global_load_dwordx4 %2, %10, %12 sc1\n\tglobal_load_dwordx4 %3, %11, %12 sc1\n\t" \
    "global_load_dwordx4 %4, %8, %13 sc1\n\tglobal_load_dwordx4 %5, %9, %13 sc1\n\t" \
    "global_load_dwordx4 %6, %10, %13 sc1\n\tglobal_load_dwordx4 %7, %11, %13 sc1" \
    : "=&v"(r[0][0]), "=&v"(r[0][1]), "=&v"(r[0][2]), "=&v"(r[0][3]), "=&v"(r[1][0]), "=&v"(r[1][1]), "=&v"(r[1][2]), "=&v"(r[1][3]) \
    : "v"(vo), "v"(vo ^ (TLQ ? 2048u : 64u)), "v"(vo ^ (TLQ ? 4096u : 128u)), "v"(vo ^ (TLQ ? 6144u : 192u)), "s"(sb), "s"((sb) + (TLQ ? 1024 : 4096)) : "memory")
#define TIE8(r) "+v"(r[0][0]), "+v"(r[0][1]), "+v"(r[0][2]), "+v"(r[0][3]), "+v"(r[1][0]), "+v"(r[1][1]), "+v"(r[1][2]), "+v"(r[1][3])
#define SADL 255
#define DSA 20
#define SASL 8
__device__ __forceinline__ void sa_role(char* smem, int wave_id_, int hbase, const char* __restrict__ Q, const char* __restrict__ Kx, const char* __restrict__ V,
                                        const char* kvm, char* __restrict__ O, unsigned* cnt, unsigned* tmo, int slot, unsigned long long* mts) {
  constexpr float LS = LOG2E * SCALE;
  const int tid = opaque_tid(), wave = WAVE_ID;
  const int h = hbase + (wave & 1), s0 = wave >> 1;
  char* vl = smem + wave * 17408;
  const unsigned ldsw = __builtin_amdgcn_readfirstlane((unsigned)(size_t)(LAS char*)vl);
  unsigned avail = 0;
  {
    static_assert(SB % 4 == 0, "");
    unsigned z = SENT; asm volatile("" : "+v"(z));
    for (int s = SB + s0; s < NSEG; s += 4)
#pragma unroll
      for (int i = 0; i < 8; ++i) st16s(uni(O + ((size_t)s * 64 + 2 * h) * 4096), (unsigned)(i * 1024 + (tid & 63) * 16), u32x4{z, z, z, z});
  }
  for (int s = s0, it = 1; s < NSEG; s += 4, ++it) {
    u32x4 qv[2][4], kv[2][4];
    int lane = tid & 63; asm volatile("" : "+v"(lane));
    const int g = lane >> 4, r16 = lane & 15, q4 = r16 >> 2, p4 = lane & 3;
    auto request = [&](int sr) {
      const size_t o = ((size_t)sr * NH + h) * 8192;
      const char* qs = uni(Q + o); const char* ks = uni(Kx + o); const char* vs = uni(V + o);
      const unsigned fo = TLQ ? (unsigned)(r16 * 64 + g * 16) : (unsigned)(r16 * 256 + ((g ^ r16) << 4));
      LD8(qv, fo, qs); LD8(kv, fo, ks);
      head_to_lds(vs, lane, ldsw);
    };
    {
      for (int spins = 0; avail <= (unsigned)s; ++spins) {
        const unsigned p = ld4_sc1(cnt + (PQKV + h) * 32 + (lane & 15));
        avail = min(min(qkv_avail(p, 0), qkv_avail(p, 1)), qkv_avail(p, 2));
        if (avail > (unsigned)s) break;
        __builtin_amdgcn_s_sleep(32);
        if (give_up(spins, tmo, 10)) break;
      }
      request(s);
      asm volatile("s_waitcnt vmcnt(0)" : TIE8(qv), TIE8(kv) :: "memory");
    }
    WTM(slot, s, 0);
    f32x4 s00 = f32x4{0, 0, 0, 0}, s01 = s00, s11 = s00;
#pragma unroll
    for (int ks = 0; ks < 4; ++ks) {
      s00 = MFMA32(asbf8(kv[0][ks]), asbf8(qv[0][ks]), s00);
      s01 = MFMA32(asbf8(kv[0][ks]), asbf8(qv[1][ks]), s01);
      s11 = MFMA32(asbf8(kv[1][ks]), asbf8(qv[1][ks]), s11);
    }
    float mx[2], sum[2];
    u32x4 pseg[2];
    {
#pragma unroll
      for (int r = 0; r < 4; ++r) { const bool ok = 4 * g + r <= r16; s00[r] = ok ? s00[r] : -3.0e38f; s11[r] = ok ? s11[r] : -3.0e38f; }
      mx[0] = red16_32_max(max3(max3(s00[0], s00[1], s00[2]), s00[3], -1.0e38f));
      mx[1] = red16_32_max(max3(max3(fmaxf(fmaxf(s01[0], s01[1]), fmaxf(s01[2], s01[3])), s11[0], -1.0e38f), max3(s11[1], s11[2], s11[3]), -1.0e38f));
      const float n0 = -mx[0] * LS, n1 = -mx[1] * LS;
#pragma unroll
      for (int r = 0; r < 4; ++r) {
        s00[r] = __builtin_amdgcn_exp2f(s00[r] * LS + n0); s01[r] = __builtin_amdgcn_exp2f(s01[r] * LS + n1); s11[r] = __builtin_amdgcn_exp2f(s11[r] * LS + n1);
      }
      sum[0] = (s00[0] + s00[1]) + (s00[2] + s00[3]);
      sum[1] = ((s01[0] + s01[1]) + (s01[2] + s01[3])) + ((s11[0] + s11[1]) + (s11[2] + s11[3]));
      pseg[0] = u32x4{pack2(s00[0], s00[1]), pack2(s00[2], s00[3]), 0u, 0u};
      pseg[1] = u32x4{pack2(s01[0], s01[1]), pack2(s01[2], s01[3]), pack2(s11[0], s11[1]), pack2(s11[2], s11[3])};
    }
    f32x4 o[8][2];
#pragma unroll
    for (int dt = 0; dt < 8; ++dt) {
      const int row0 = 4 * g + q4;
      const unsigned xo = (unsigned)(row0 * 256 + (((4 * (dt >> 1) + p4) ^ row0) << 4) + (dt & 1) * 8);
      const u32x2 lo = trread(vl, xo), hi = trread(vl, xo + 4096);
      o[dt][0] = MFMA32(asbf8(u32x4{lo.x, lo.y, 0u, 0u}), asbf8(pseg[0]), (f32x4{0, 0, 0, 0}));
      o[dt][1] = MFMA32(asbf8(u32x4{lo.x, lo.y, hi.x, hi.y}), asbf8(pseg[1]), (f32x4{0, 0, 0, 0}));
    }
    float n0[2]; f32x4 sm[2];
#pragma unroll
    for (int b = 0; b < 2; ++b) { const float t = red16_32_add(sum[b]); sm[b] = f32x4{t, t, t, t}; n0[b] = -mx[b] * LS; }
    unsigned oa = (unsigned)(r16 * 128 + ((g ^ (r16 & 7)) << 4)), ka = (unsigned)(g * 256 + (r16 & 7) * 16), va = (unsigned)lane * 16;
    unsigned one = g < 2 ? 0x3F803F80u : 0u;
    u32x4 kmv[4], vmv[4];
#pragma unroll
    for (int i = 0; i < 4; ++i) vmv[i] = u32x4{0u, 0u, 0u, 0u};
#pragma unroll
    for (int dt = 0; dt < 8; ++dt) asm volatile("" : "+v"(o[dt][0]), "+v"(o[dt][1]), "+v"(vmv[dt >> 1]));
    asm volatile("s_waitcnt lgkmcnt(0)" : "+v"(sm[0]), "+v"(sm[1]), "+v"(n0[0]), "+v"(n0[1]), "+v"(oa), "+v"(ka), "+v"(va), "+v"(one) :: "memory");
    unsigned pg;
    for (int spins = 0;; ++spins) {
      pg = ld4_sc1(cnt + (PROG + h) * 32 + ((lane & 4) ? 5 : (lane & 3)));
      if (__all((pg >> 24) + (s >= SDIR ? 1u : 0u) > (unsigned)s)) break;
      __builtin_amdgcn_s_sleep(SASL);
      if (give_up(spins, tmo, 6)) break;
    }
    if (__all((pg >> 24) == (unsigned)s)) wait_rt(latest(pg), SADL);
    const char* mk = uni(kvm + ((size_t)s * NH + h) * 4096);
    for (int spins = 0;; ++spins) {
      asm volatile("global_load_dwordx4 %0, %8, %10 sc1\n\tglobal_load_dwordx4 %1, %8, %10 offset:128 sc1\n\t"
                   "global_load_dwordx4 %2, %8, %10 offset:1024 sc1\n\tglobal_load_dwordx4 %3, %8, %10 offset:1152 sc1\n\ts_mov_b32 exec_hi, 0\n\t"
                   "global_load_dwordx4 %4, %9, %10 offset:2048 sc1\n\tglobal_load_dwordx4 %5, %9, %10 offset:2560 sc1\n\t"
                   "global_load_dwordx4 %6, %9, %10 offset:3072 sc1\n\tglobal_load_dwordx4 %7, %9, %10 offset:3584 sc1\n\ts_mov_b32 exec_hi, -1\n\ts_waitcnt vmcnt(0)"
                   : "=&v"(kmv[0]), "=&v"(kmv[1]), "=&v"(kmv[2]), "=&v"(kmv[3]), "+v"(vmv[0]), "+v"(vmv[1]), "+v"(vmv[2]), "+v"(vmv[3])
                   : "v"(ka), "v"(va), "s"(mk) : "memory");
      unsigned mx_ = 0;
#pragma unroll
      for (int i = 0; i < 4; ++i) mx_ = max(max(max(max(mx_, kmv[i].x), kmv[i].y), max(kmv[i].z, kmv[i].w)), max(max(vmv[i].x, vmv[i].y), max(vmv[i].z, vmv[i].w)));
      if (__all(mx_ != SENT)) break;
      __builtin_amdgcn_s_sleep(2);
      if (give_up(spins, tmo, 6)) break;
    }
    WTM(slot, s, 1);
    f32x4 tt[2] = {f32x4{0, 0, 0, 0}, f32x4{0, 0, 0, 0}};
#pragma unroll
    for (int ks = 0; ks < 4; ++ks) { tt[0] = MFMA32(asbf8(kmv[ks]), asbf8(qv[0][ks]), tt[0]); tt[1] = MFMA32(asbf8(kmv[ks]), asbf8(qv[1][ks]), tt[1]); }
#pragma unroll
    for (int b = 0; b < 2; ++b)
#pragma unroll
      for (int r = 0; r < 4; ++r) tt[b][r] = tt[b][r] * LS + n0[b];
    if (!__all(max3(max3(tt[0][0], tt[0][1], tt[0][2]), max3(tt[0][3], tt[1][0], tt[1][1]), max2(tt[1][2], tt[1][3])) <= 40.0f)) {
#pragma unroll
      for (int b = 0; b < 2; ++b) {
        float m = max3(max3(tt[b][0], tt[b][1], tt[b][2]), tt[b][3], 0.0f);
        { float x, y; sw16(m, x, y); m = max2(x, y); }
        const float f = __builtin_amdgcn_exp2f(-m);
        tt[b] -= m; sm[b] *= f;
#pragma unroll
        for (int dt = 0; dt < 8; ++dt) o[dt][b] *= f;
      }
    }
    u32x2 pm[2]; float inv[2];
#pragma unroll
    for (int b = 0; b < 2; ++b) {
      pm[b] = u32x2{pack2(__builtin_amdgcn_exp2f(tt[b][0]), __builtin_amdgcn_exp2f(tt[b][1])), pack2(__builtin_amdgcn_exp2f(tt[b][2]), __builtin_amdgcn_exp2f(tt[b][3]))};
      sm[b] = __builtin_amdgcn_mfma_f32_16x16x16bf16_1k(ass4(u32x2{one, one}), ass4(pm[b]), sm[b], 0, 0, 0);
    }
#pragma unroll
    for (int dt = 0; dt < 8; ++dt)
#pragma unroll
      for (int b = 0; b < 2; ++b) o[dt][b] = __builtin_amdgcn_mfma_f32_16x16x16bf16_1k(ass4((dt & 1) ? u32x2{vmv[dt >> 1].z, vmv[dt >> 1].w} : u32x2{vmv[dt >> 1].x, vmv[dt >> 1].y}), ass4(pm[b]), o[dt][b], 0, 0, 0);
    inv[0] = __builtin_amdgcn_rcpf(sm[0][0]); inv[1] = __builtin_amdgcn_rcpf(sm[1][0]);
    const char* od = uni(O + ((size_t)s * 64 + 2 * h) * 4096);
#pragma unroll
    for (int j = 0; j < 4; ++j) {
#pragma unroll
      for (int b = 0; b < 2; ++b) {
        const f32x4 r0 = o[2 * j][b] * inv[b], r1 = o[2 * j + 1][b] * inv[b];
        st16s(od + (j >> 1) * 4096 + b * 2048, oa ^ ((j & 1) * 64u), u32x4{pack2(r0[0], r0[1]), pack2(r0[2], r0[3]), pack2(r1[0], r1[1]), pack2(r1[2], r1[3])});
      }
    }
    if (s >= SB && lane == 0) st4_sc1(cnt + (PSA + (hbase >> 1)) * 32 + 16 + wave, bell(s, rt_now() + DSA));
    WTM(slot, s, 3);
    WAIT_V(0);
    if (lane == 0) st4_sc1(cnt + (PSA + (hbase >> 1)) * 32 + wave, (unsigned)it);
    WTM(slot, s, 2);
  }
}
#undef LD8
#undef TIE8

constexpr int Q_MEM = 0;
constexpr int Q_K   = 16384;
constexpr int Q_V   = 24576;
constexpr int Q_QK  = 32768;
constexpr int Q_TM  = 36864;
constexpr int Q_CB  = 40960;
constexpr int Q_RING = 32768;
constexpr int V_P1  = 0;
constexpr int V_P2  = 16384;
constexpr int NCHAIN = 96;
#define NQQ 6
#define NQK 4
#define E_AG 9
#define RDYI -2
#define ROTW 2
#define DQU 6
#define DQM 18
#define DQD 1
#define ORSEL (xg >> 1)
#define D_Q 105
#define D_O 4
#define E_B 6
#define E_O 1900
#define KDEL 5
#define VPOS 0

typedef __bf16 bf16x16 __attribute__((ext_vector_type(16)));
typedef unsigned u32x8 __attribute__((ext_vector_type(8)));
__device__ __forceinline__ f32x4 SMF(u32x4 a, u32x8 b, f32x4 c, int idx) {
  return __builtin_amdgcn_smfmac_f32_16x16x64_bf16(__builtin_bit_cast(bf16x8, a), __builtin_bit_cast(bf16x16, b), c, idx, 0, 0);
}
__device__ __forceinline__ float fold32(float a, float b) {
  const auto p = __builtin_amdgcn_permlane32_swap(__float_as_uint(a), __float_as_uint(b), false, false);
  return __uint_as_float(p[0]) + __uint_as_float(p[1]);
}
__device__ __forceinline__ f32x2 rowsum(f32x4 x) { return f32x2{fold32(x[0], x[2]), fold32(x[1], x[3])}; }
__device__ __forceinline__ unsigned nosent1(unsigned v) { return v < 0xFFFFFFFEu ? v : 0xFFFFFFFEu; }
__device__ __forceinline__ void st4s(const char* sb, unsigned vo, unsigned v) { asm volatile("global_store_dword %0, %1, %2 sc1\n\ts_nop 0" :: "v"(vo), "v"(v), "s"(sb) : "memory"); }
__device__ __forceinline__ void st8s(const char* sb, unsigned vo, u32x2 v) { asm volatile("global_store_dwordx2 %0, %1, %2 sc1\n\ts_nop 1" :: "v"(vo), "v"(v), "s"(sb) : "memory"); }
__device__ __forceinline__ void gldsw(const char* sbase, unsigned voff, unsigned lds) {
  unsigned keep;
  asm volatile("s_mov_b32 %0, m0\n\ts_mov_b32 m0, %3\n\ts_nop 2\n\tglobal_load_lds_dwordx4 %1, %2" CNT_ "\n\ts_mov_b32 m0, %0"
               : "=&s"(keep) : "v"(voff), "s"(sbase), "s"(lds) : "memory");
}
__device__ __forceinline__ void wait_clk(unsigned t0, unsigned d) {
  while ((unsigned)__builtin_amdgcn_s_memtime() - t0 < d) __builtin_amdgcn_s_sleep(1);
}
__device__ __forceinline__ unsigned bell_time(const char* a, unsigned vo, unsigned tag, unsigned cap) {
  unsigned v;
  for (unsigned spins = 0;; ++spins) {
    asm volatile("global_load_dword %0, %1, %2 sc1\n\ts_waitcnt vmcnt(0)" : "=&v"(v) : "v"(vo), "s"(a) : "memory");
    if (__all((v >> 24) >= tag) || spins >= cap) break;
  }
  const unsigned now = (unsigned)__builtin_amdgcn_s_memrealtime() + 250u;
  const unsigned age = rowmin16((now - v) & 0xFFFFFFu);
  return now - min(min(rdl(age, 0), rdl(age, 16)), 500u);
}
#define WBAR() do { __builtin_amdgcn_s_barrier(); asm volatile("" ::: "memory"); } while (0)
#define TN(k, v) do { } while (0)
__device__ __forceinline__ void stage_proj(const char* src, unsigned lds, int tid, int wave) {
  const int row = tid >> 4, f = 2 * ((row & 1) | (((row >> 2) & 1) << 1));
#pragma unroll
  for (int j = 0; j < 4; ++j)
    gldsw(src + (size_t)j * 32 * HD * 2, (unsigned)(row * HD * 2 + (((tid & 15) ^ f ^ ((j & 1) << 3)) << 4)), lds + (wave * 64 + 512 * j) * 16);
}
__device__ __forceinline__ unsigned proj_base(int T, int lane) {
  const int g = lane >> 4, q4 = (lane & 15) >> 2, p4 = lane & 3, fs = 2 * ((q4 & 1) | ((g & 1) << 1) | ((q4 >> 1) << 2));
  return (unsigned)((4 * g + (q4 & 1) + 32 * (q4 >> 1)) * 256 + (((2 * T) ^ fs) << 4) + p4 * 8);
}
__device__ __forceinline__ u32x8 proj_frag(const char* smem, unsigned a) {
  const u32x2 t0 = trread(smem, a), t1 = trread(smem, a + 512), t2 = trread(smem, a + 4096), t3 = trread(smem, a + 4608);
  return u32x8{t0.x, t0.y, t1.x, t1.y, t2.x, t2.y, t3.x, t3.y};
}
__device__ __forceinline__ void flag_frags(unsigned fa, unsigned a, unsigned want, bf16x8 (&f)[4]) {
  u32x2 t[8];
  for (;;) {
    u32x4 fl;
    asm volatile("ds_read_b128 %0, %9\n\t"
                 "ds_read_b64_tr_b16 %1, %10\n\tds_read_b64_tr_b16 %2, %10 offset:64\n\t"
                 "ds_read_b64_tr_b16 %3, %10 offset:512\n\tds_read_b64_tr_b16 %4, %10 offset:576\n\t"
                 "ds_read_b64_tr_b16 %5, %10 offset:1024\n\tds_read_b64_tr_b16 %6, %10 offset:1088\n\t"
                 "ds_read_b64_tr_b16 %7, %10 offset:1536\n\tds_read_b64_tr_b16 %8, %10 offset:1600\n\ts_waitcnt lgkmcnt(0)"
                 : "=&v"(fl), "=&v"(t[0]), "=&v"(t[1]), "=&v"(t[2]), "=&v"(t[3]), "=&v"(t[4]), "=&v"(t[5]), "=&v"(t[6]), "=&v"(t[7])
                 : "v"(fa), "v"(a) : "memory");
    if (__builtin_amdgcn_readfirstlane(min(min(fl.x, fl.y), min(fl.z, fl.w))) >= want) break;
  }
#pragma unroll
  for (int ks = 0; ks < 4; ++ks) f[ks] = asbf8(u32x4{t[2 * ks].x, t[2 * ks].y, t[2 * ks + 1].x, t[2 * ks + 1].y});
}
__device__ __forceinline__ void wait_flags(const char* p, unsigned want) {
  for (;;) {
    const u32x4 fl = *(volatile LAS u32x4*)p;
    if (__builtin_amdgcn_readfirstlane(min(min(fl.x, fl.y), min(fl.z, fl.w))) >= want) break;
  }
}
__device__ __forceinline__ f32x4 zero4() { float z = 0.0f; asm volatile("" : "+v"(z)); return f32x4{z, z, z, z}; }
__device__ __forceinline__ unsigned lds_word(const char* p) { return __builtin_amdgcn_readfirstlane(*(volatile LAS unsigned*)p); }

#define MFMA16(a, b, c) __builtin_amdgcn_mfma_f32_16x16x16bf16_1k((a), (b), (c), 0, 0, 0)
#define UF(x) __uint_as_float(x)
__device__ __forceinline__ unsigned umax3(unsigned a, unsigned b, unsigned c) { return max(max(a, b), c); }
__device__ __forceinline__ f32x2 logit2(float a, float b) {
  return f32x2{a, b};
}
constexpr float LSC = LOG2E * SCALE;

struct QKW { u32x32 w0, w1, w2, w3, w4, w5; u32x8 x; };
template <int W_> __device__ __forceinline__ void qk_step(const QKW& W, const char* ag, const char* vs, const char* od, const char* kv, unsigned& cap, unsigned& sp,
                                                          unsigned long long& tc, unsigned long long* ts, unsigned& d0, unsigned& d1);
template <> __device__ __forceinline__ void qk_step<0>(const QKW& W, const char* ag, const char* vs, const char* od, const char* kv, unsigned& cap, unsigned& sp,
                                                        unsigned long long& tc, unsigned long long* ts, unsigned& d0, unsigned& d1) {
  unsigned st, bg;
  asm volatile("v_mbcnt_lo_u32_b32 v60, -1, 0\n\t"
    "v_mbcnt_hi_u32_b32 v60, -1, v60\n\t"
    "v_lshlrev_b32 v6, 4, v60\n\t"
    "s_mov_b32 %[sp], 0\n\t"
    "s_setprio 3\n\t"
    "global_load_dwordx4 v[40:43], v6, %[ag] sc1\n\t"
    "global_load_dwordx4 v[44:47], v6, %[ag] offset:1024 sc1\n\t"
    "s_memtime %[tc]\n\t"
    "v_add_u32 v7, 40960, v6\n\t"
    "v_add_u32 v8, 98304, v6\n\t"
    "ds_read_b128 v[24:27], v7 offset:0\n\t"
    "ds_read_b128 v[28:31], v7 offset:1024\n\t"
    "ds_read_b128 v[32:35], v7 offset:8192\n\t"
    "ds_read_b128 v[36:39], v7 offset:9216\n\t"
    "v_lshrrev_b32 v48, 4, v60\n\t"
    "v_and_b32 v49, 15, v60\n\t"
    "v_and_b32 v61, 4, v60\n\t"
    "v_mul_u32_u24 v61, 0x2aaa, v61\n\t"
    "v_lshrrev_b32 v9, 1, v60\n\t"
    "v_and_b32 v9, 2, v9\n\t"
    "v_or3_b32 v9, v9, v61, %[c44]\n\t"
    "v_add_u32 v14, 0, v6\n\t"
    "v_lshlrev_b32 v61, 2, v48\n\t"
    "v_lshl_or_b32 v10, v49, 4, v61\n\t"
    "v_lshrrev_b32 v50, 2, v49\n\t"
    "v_and_b32 v51, 3, v60\n\t"
    "v_and_b32 v61, 1, v60\n\t"
    "v_lshlrev_b32 v61, 3, v61\n\t"
    "v_lshl_or_b32 v11, v50, 4, v61\n\t"
    "v_lshl_or_b32 v11, v48, 7, v11\n\t"
    "v_xor_b32 v12, v48, v49\n\t"
    "v_lshlrev_b32 v12, 4, v12\n\t"
    "v_lshl_or_b32 v12, v49, 8, v12\n\t"
    "v_lshl_or_b32 v50, v48, 2, v50\n\t"
    "v_or_b32 v51, 0, v51\n\t"
    "v_xor_b32 v51, v51, v50\n\t"
    "v_lshlrev_b32 v13, 4, v51\n\t"
    "v_lshl_or_b32 v13, v50, 8, v13\n\t"
    "v_and_b32 v61, 3, v60\n\t"
    "v_lshlrev_b32 v61, 4, v61\n\t"
    "v_lshl_or_b32 v15, v48, 6, v61\n\t"
    "v_and_b32 v61, 8, v60\n\t"
    "v_lshl_or_b32 v15, v61, 14, v15\n\t"
    "v_and_b32 v61, 4, v60\n\t"
    "v_lshl_or_b32 v15, v61, 8, v15\n\t"
    "v_add_u32 v15, 0, v15\n\t"
    "3:\n\t"
    "v_mov_b32 v16, 0\n\t"
    "v_mov_b32 v17, 0\n\t"
    "v_mov_b32 v18, 0\n\t"
    "v_mov_b32 v19, 0\n\t"
    "v_mov_b32 v20, 0\n\t"
    "v_mov_b32 v21, 0\n\t"
    "v_mov_b32 v22, 0\n\t"
    "v_mov_b32 v23, 0\n\t"
    "s_waitcnt vmcnt(0) lgkmcnt(0)\n\t"
    "14:\n\t"
    "ds_write_b128 v6, v[40:43] offset:0\n\t"
    "ds_write_b128 v6, v[44:47] offset:1024\n\t"
    "v_smfmac_f32_16x16x64_bf16 v[16:19], v[40:43], v[64:71], v9\n\t"
    "v_max3_u32 v60, v40, v41, v42\n\t"
    "v_max3_u32 v61, v43, v44, v45\n\t"
    "v_smfmac_f32_16x16x64_bf16 v[20:23], v[40:43], v[72:79], v9\n\t"
    "v_max3_u32 v60, v60, v46, v47\n\t"
    "v_max_u32 v60, v60, v61\n\t"
    "v_cmp_eq_u32 vcc, -1, v60\n\t"
    "s_cbranch_vccnz 1f\n\t"
    "2:\n\t"
    "s_waitcnt lgkmcnt(0)\n\t"
    "s_barrier\n\t"
    "ds_read_b128 v[40:43], v6 offset:12288\n\t"
    "ds_read_b128 v[48:51], v6 offset:2048\n\t"
    "v_smfmac_f32_16x16x64_bf16 v[16:19], v[44:47], v[80:87], v9\n\t"
    "ds_read_b128 v[52:55], v6 offset:3072\n\t"
    "ds_read_b128 v[56:59], v6 offset:4096\n\t"
    "v_smfmac_f32_16x16x64_bf16 v[20:23], v[44:47], v[88:95], v9\n\t"
    "ds_read_b128 v[60:63], v6 offset:13312\n\t"
    "ds_read_b128 v[44:47], v6 offset:5120\n\t"
    "s_waitcnt lgkmcnt(5)\n\t"
    "v_smfmac_f32_16x16x64_bf16 v[16:19], v[40:43], v[24:31], v9\n\t"
    "ds_read_b128 v[24:27], v7 offset:16384\n\t"
    "ds_read_b128 v[28:31], v7 offset:17408\n\t"
    "v_smfmac_f32_16x16x64_bf16 v[20:23], v[40:43], v[32:39], v9\n\t"
    "ds_read_b128 v[32:35], v7 offset:24576\n\t"
    "ds_read_b128 v[36:39], v7 offset:25600\n\t"
    "ds_read_b128 v[40:43], v6 offset:6144\n\t"
    "s_waitcnt lgkmcnt(9)\n\t"
    "v_smfmac_f32_16x16x64_bf16 v[16:19], v[48:51], v[96:103], v9\n\t"
    "v_smfmac_f32_16x16x64_bf16 v[20:23], v[48:51], v[104:111], v9\n\t"
    "ds_read_b128 v[48:51], v6 offset:7168\n\t"
    "s_waitcnt lgkmcnt(9)\n\t"
    "v_smfmac_f32_16x16x64_bf16 v[16:19], v[52:55], v[112:119], v9\n\t"
    "v_smfmac_f32_16x16x64_bf16 v[20:23], v[52:55], v[120:127], v9\n\t"
    "ds_read_b128 v[52:55], v6 offset:14336\n\t"
    "s_waitcnt lgkmcnt(9)\n\t"
    "v_smfmac_f32_16x16x64_bf16 v[16:19], v[56:59], v[128:135], v9\n\t"
    "v_smfmac_f32_16x16x64_bf16 v[20:23], v[56:59], v[136:143], v9\n\t"
    "ds_read_b128 v[56:59], v6 offset:8192\n\t"
    "s_waitcnt lgkmcnt(6)\n\t"
    "v_smfmac_f32_16x16x64_bf16 v[16:19], v[60:63], v[24:31], v9\n\t"
    "ds_read_b128 v[24:27], v8 offset:0\n\t"
    "ds_read_b128 v[28:31], v8 offset:1024\n\t"
    "s_waitcnt lgkmcnt(6)\n\t"
    "v_smfmac_f32_16x16x64_bf16 v[20:23], v[60:63], v[32:39], v9\n\t"
    "ds_read_b128 v[32:35], v8 offset:8192\n\t"
    "ds_read_b128 v[36:39], v8 offset:9216\n\t"
    "ds_read_b128 v[60:63], v6 offset:9216\n\t"
    "v_smfmac_f32_16x16x64_bf16 v[16:19], v[44:47], v[144:151], v9\n\t"
    "v_smfmac_f32_16x16x64_bf16 v[20:23], v[44:47], v[152:159], v9\n\t"
    "ds_read_b128 v[44:47], v6 offset:10240\n\t"
    "s_waitcnt lgkmcnt(9)\n\t"
    "v_smfmac_f32_16x16x64_bf16 v[16:19], v[40:43], v[160:167], v9\n\t"
    "v_smfmac_f32_16x16x64_bf16 v[20:23], v[40:43], v[168:175], v9\n\t"
    "ds_read_b128 v[40:43], v6 offset:15360\n\t"
    "s_waitcnt lgkmcnt(9)\n\t"
    "v_smfmac_f32_16x16x64_bf16 v[16:19], v[48:51], v[176:183], v9\n\t"
    "v_smfmac_f32_16x16x64_bf16 v[20:23], v[48:51], v[184:191], v9\n\t"
    "ds_read_b128 v[48:51], v6 offset:11264\n\t"
    "s_waitcnt lgkmcnt(6)\n\t"
    "v_smfmac_f32_16x16x64_bf16 v[16:19], v[52:55], v[24:31], v9\n\t"
    "ds_read_b128 v[24:27], v8 offset:16384\n\t"
    "ds_read_b128 v[28:31], v8 offset:17408\n\t"
    "s_waitcnt lgkmcnt(6)\n\t"
    "v_smfmac_f32_16x16x64_bf16 v[20:23], v[52:55], v[32:39], v9\n\t"
    "ds_read_b128 v[32:35], v8 offset:24576\n\t"
    "ds_read_b128 v[36:39], v8 offset:25600\n\t"
    "v_smfmac_f32_16x16x64_bf16 v[16:19], v[56:59], v[192:199], v9\n\t"
    "v_smfmac_f32_16x16x64_bf16 v[20:23], v[56:59], v[200:207], v9\n\t"
    "s_waitcnt lgkmcnt(7)\n\t"
    "v_smfmac_f32_16x16x64_bf16 v[16:19], v[60:63], v[208:215], v9\n\t"
    "v_smfmac_f32_16x16x64_bf16 v[20:23], v[60:63], v[216:223], v9\n\t"
    "s_waitcnt lgkmcnt(6)\n\t"
    "v_smfmac_f32_16x16x64_bf16 v[16:19], v[44:47], v[224:231], v9\n\t"
    "v_smfmac_f32_16x16x64_bf16 v[20:23], v[44:47], v[232:239], v9\n\t"
    "s_waitcnt lgkmcnt(2)\n\t"
    "v_smfmac_f32_16x16x64_bf16 v[16:19], v[40:43], v[24:31], v9\n\t"
    "s_waitcnt lgkmcnt(0)\n\t"
    "v_smfmac_f32_16x16x64_bf16 v[20:23], v[40:43], v[32:39], v9\n\t"
    "v_smfmac_f32_16x16x64_bf16 v[16:19], v[48:51], v[240:247], v9\n\t"
    "v_smfmac_f32_16x16x64_bf16 v[20:23], v[48:51], v[248:255], v9\n\t"
    "v_xor_b32 v7, 64, v12\n\t"
    "v_xor_b32 v8, 0x80, v12\n\t"
    "v_xor_b32 v6, 0xc0, v12\n\t"
    "ds_read_b128 v[24:27], v12 offset:16384\n\t"
    "ds_read_b128 v[28:31], v12 offset:20480\n\t"
    "ds_read_b128 v[32:35], v7 offset:16384\n\t"
    "ds_read_b128 v[36:39], v7 offset:20480\n\t"
    "v_permlane16_swap_b32 v16, v18\n\t"
    "v_permlane16_swap_b32 v17, v19\n\t"
    "v_permlane16_swap_b32 v20, v22\n\t"
    "v_permlane16_swap_b32 v21, v23\n\t"
    "v_add_f32 v16, v16, v18\n\t"
    "v_add_f32 v17, v17, v19\n\t"
    "v_add_f32 v20, v20, v22\n\t"
    "v_mul_f32 v16, %[lsc], v16\n\t"
    "v_mul_f32 v17, %[lsc], v17\n\t"
    "v_cvt_pk_bf16_f32 v16, v16, v17\n\t"
    "v_add_f32 v21, v21, v23\n\t"
    "ds_write_b32 v10, v16 offset:32768\n\t"
    "v_mul_f32 v20, %[lsc], v20\n\t"
    "v_mul_f32 v21, %[lsc], v21\n\t"
    "v_cvt_pk_bf16_f32 v20, v20, v21\n\t"
    "ds_write_b32 v10, v20 offset:33024\n\t"
    "ds_read_b128 v[40:43], v8 offset:16384\n\t"
    "ds_read_b128 v[44:47], v8 offset:20480\n\t"
    "ds_read_b128 v[48:51], v6 offset:16384\n\t"
    "ds_read_b128 v[52:55], v6 offset:20480\n\t"
    "s_waitcnt lgkmcnt(4)\n\t"
    "s_barrier\n\t"
    "ds_read_b64_tr_b16 v[16:17], v11 offset:32768\n\t"
    "ds_read_b64_tr_b16 v[18:19], v11 offset:32832\n\t"
    "ds_read_b64_tr_b16 v[20:21], v11 offset:33280\n\t"
    "ds_read_b64_tr_b16 v[22:23], v11 offset:33344\n\t"
    "ds_read_b64_tr_b16 v[56:57], v11 offset:33792\n\t"
    "ds_read_b64_tr_b16 v[58:59], v11 offset:33856\n\t"
    "ds_read_b64_tr_b16 v[60:61], v11 offset:34304\n\t"
    "ds_read_b64_tr_b16 v[62:63], v11 offset:34368\n\t"
    "v_mov_b32 v6, 0\n\t"
    "v_mov_b32 v7, 0\n\t"
    "v_mov_b32 v8, 0\n\t"
    "v_mov_b32 v9, 0\n\t"
    "s_mov_b32 exec_hi, 0\n\t"
    "global_load_dwordx4 v[6:9], v14, %[vs] sc1\n\t"
    "s_mov_b32 exec_hi, -1\n\t"
    "s_waitcnt lgkmcnt(6)\n\t"
    "v_mfma_f32_16x16x32_bf16 v[24:27], v[24:27], v[16:19], 0\n\t"
    "v_mfma_f32_16x16x32_bf16 v[28:31], v[28:31], v[16:19], 0\n\t"
    "s_waitcnt lgkmcnt(4)\n\t"
    "v_mfma_f32_16x16x32_bf16 v[24:27], v[32:35], v[20:23], v[24:27]\n\t"
    "v_mfma_f32_16x16x32_bf16 v[28:31], v[36:39], v[20:23], v[28:31]\n\t"
    "ds_read_b64_tr_b16 v[32:33], v13 offset:24576\n\t"
    "ds_read_b64_tr_b16 v[34:35], v13 offset:28672\n\t"
    "ds_read_b64_tr_b16 v[36:37], v13 offset:24584\n\t"
    "ds_read_b64_tr_b16 v[38:39], v13 offset:28680\n\t"
    "s_waitcnt lgkmcnt(6)\n\t"
    "v_mfma_f32_16x16x32_bf16 v[24:27], v[40:43], v[56:59], v[24:27]\n\t"
    "v_mfma_f32_16x16x32_bf16 v[28:31], v[44:47], v[56:59], v[28:31]\n\t"
    "s_waitcnt lgkmcnt(4)\n\t"
    "v_mfma_f32_16x16x32_bf16 v[24:27], v[48:51], v[60:63], v[24:27]\n\t"
    "v_mfma_f32_16x16x32_bf16 v[28:31], v[52:55], v[60:63], v[28:31]\n\t"
    "s_setprio 1\n\t"
    "v_mov_b32 v52, %[one]\n\t"
    "v_mov_b32 v53, %[one]\n\t"
    "v_mov_b32 v54, %[one]\n\t"
    "v_mov_b32 v55, %[one]\n\t"
    "s_nop 2\n\t"
    "v_max3_f32 v10, |v24|, |v25|, |v26|\n\t"
    "v_max3_f32 v40, |v27|, |v28|, |v29|\n\t"
    "v_max3_f32 v10, v10, |v30|, |v31|\n\t"
    "v_max_f32 v10, v10, v40\n\t"
    "v_cmp_nge_f32 vcc, %[big], v10\n\t"
    "s_mov_b32 %[bg], %[big]\n\t"
    "v_mov_b32 v10, 0\n\t"
    "s_cbranch_vccnz 12f\n\t"
    "13:\n\t"
    "v_exp_f32 v24, v24\n\t"
    "v_exp_f32 v25, v25\n\t"
    "v_exp_f32 v26, v26\n\t"
    "v_exp_f32 v27, v27\n\t"
    "v_exp_f32 v28, v28\n\t"
    "v_exp_f32 v29, v29\n\t"
    "v_exp_f32 v30, v30\n\t"
    "v_exp_f32 v31, v31\n\t"
    "v_cvt_pk_bf16_f32 v40, v24, v25\n\t"
    "v_cvt_pk_bf16_f32 v41, v26, v27\n\t"
    "v_cvt_pk_bf16_f32 v42, v28, v29\n\t"
    "v_cvt_pk_bf16_f32 v43, v30, v31\n\t"
    "s_setprio 3\n\t"
    "s_waitcnt lgkmcnt(0)\n\t"
    "s_nop 0\n\t"
    "v_mfma_f32_16x16x32_bf16 v[44:47], v[32:35], v[40:43], 0\n\t"
    "v_mfma_f32_16x16x32_bf16 v[48:51], v[36:39], v[40:43], 0\n\t"
    "v_mfma_f32_16x16x32_bf16 v[52:55], v[52:55], v[40:43], 0\n\t"
    "v_mov_b32 v12, 0\n\t"
    "v_mov_b32 v13, 0\n\t"
    "s_mov_b32 exec_hi, 0\n\t"
    "v_mov_b32 v12, %[one]\n\t"
    "v_mov_b32 v13, %[one]\n\t"
    "s_mov_b32 exec_hi, -1\n\t"
    "s_barrier\n\t"
    "ds_read_b64_tr_b16 v[24:25], v11 offset:34816\n\t"
    "ds_read_b64_tr_b16 v[26:27], v11 offset:34880\n\t"
    "ds_read_b64_tr_b16 v[28:29], v11 offset:35328\n\t"
    "ds_read_b64_tr_b16 v[30:31], v11 offset:35392\n\t"
    "ds_read_b64_tr_b16 v[32:33], v11 offset:35840\n\t"
    "ds_read_b64_tr_b16 v[34:35], v11 offset:35904\n\t"
    "ds_read_b64_tr_b16 v[36:37], v11 offset:36352\n\t"
    "ds_read_b64_tr_b16 v[38:39], v11 offset:36416\n\t"
    "s_waitcnt lgkmcnt(6)\n\t"
    "v_mfma_f32_16x16x32_bf16 v[40:43], v[24:27], v[16:19], 0\n\t"
    "s_waitcnt lgkmcnt(4)\n\t"
    "v_mfma_f32_16x16x32_bf16 v[40:43], v[28:31], v[20:23], v[40:43]\n\t"
    "s_waitcnt lgkmcnt(2)\n\t"
    "v_mfma_f32_16x16x32_bf16 v[40:43], v[32:35], v[56:59], v[40:43]\n\t"
    "s_waitcnt lgkmcnt(0)\n\t"
    "v_mfma_f32_16x16x32_bf16 v[40:43], v[36:39], v[60:63], v[40:43]\n\t"
    "5:\n\t"
    "s_waitcnt vmcnt(0)\n\t"
    "v_max3_u32 v11, v6, v7, v8\n\t"
    "v_max_u32 v11, v11, v9\n\t"
    "v_cmp_eq_u32 vcc, -1, v11\n\t"
    "s_cbranch_vccnz 4f\n\t"
    "6:\n\t"
    "s_nop 2\n\t"
    "v_exp_f32 v28, v40\n\t"
    "v_max3_f32 v11, v40, v41, v42\n\t"
    "v_exp_f32 v29, v41\n\t"
    "v_max_f32 v11, v11, v43\n\t"
    "v_exp_f32 v30, v42\n\t"
    "v_cmp_nge_f32 vcc, %[bg], v11\n\t"
    "v_exp_f32 v31, v43\n\t"
    "s_cbranch_vccnz 10f\n\t"
    "11:\n\t"
    "v_cvt_pk_bf16_f32 v24, v28, v29\n\t"
    "v_cvt_pk_bf16_f32 v25, v30, v31\n\t"
    "s_nop 1\n\t"
    "v_mfma_f32_16x16x16_bf16 v[52:55], v[12:13], v[24:25], v[52:55]\n\t"
    "v_mfma_f32_16x16x16_bf16 v[44:47], v[6:7], v[24:25], v[44:47]\n\t"
    "v_mfma_f32_16x16x16_bf16 v[48:51], v[8:9], v[24:25], v[48:51]\n\t"
    "s_nop 5\n\t"
    "v_rcp_f32 v26, v52\n\t"
    "s_nop 0\n\t"
    "v_pk_mul_f32 v[44:45], v[44:45], v[26:27] op_sel_hi:[1,0]\n\t"
    "v_pk_mul_f32 v[46:47], v[46:47], v[26:27] op_sel_hi:[1,0]\n\t"
    "v_pk_mul_f32 v[48:49], v[48:49], v[26:27] op_sel_hi:[1,0]\n\t"
    "v_pk_mul_f32 v[50:51], v[50:51], v[26:27] op_sel_hi:[1,0]\n\t"
    "v_cvt_pk_bf16_f32 v36, v44, v45\n\t"
    "v_cvt_pk_bf16_f32 v37, v46, v47\n\t"
    "v_cvt_pk_bf16_f32 v38, v48, v49\n\t"
    "v_cvt_pk_bf16_f32 v39, v50, v51\n\t"
    "v_min_u32 v36, -2, v36\n\t"
    "v_min_u32 v37, -2, v37\n\t"
    "v_min_u32 v38, -2, v38\n\t"
    "v_min_u32 v39, -2, v39\n\t"
    "global_store_dwordx4 v15, v[36:39], %[od] sc1\n\t"
    "s_barrier\n\t"
    "s_mov_b32 exec_hi, 0\n\t"
    "global_store_dwordx4 v14, v[6:9], %[kv] sc1\n\t"
    "s_mov_b32 exec_hi, -1\n\t"
    "s_nop 0\n\t"
    "s_waitcnt lgkmcnt(0)\n\t"
    "s_branch 9f\n\t"
    "1:\n\t"
    "s_add_u32 %[sp], %[sp], 1\n\t"
    "s_cmp_ge_u32 %[sp], %[cap]\n\t"
    "s_cbranch_scc1 7f\n\t"
    "global_load_dwordx4 v[40:43], v6, %[ag] sc1\n\t"
    "global_load_dwordx4 v[44:47], v6, %[ag] offset:1024 sc1\n\t"
    "s_waitcnt lgkmcnt(0)\n\t"
    "s_memtime %[tc]\n\t"
    "s_nop 7\n\t"
    "s_branch 3b\n\t"
    "7:\n\t"
    "s_mov_b32 %[cap], 0\n\t"
    "s_branch 2b\n\t"
    "4:\n\t"
    "s_add_u32 %[sp], %[sp], 0x10000\n\t"
    "s_lshr_b32 %[st], %[sp], 16\n\t"
    "s_cmp_ge_u32 %[st], %[cap]\n\t"
    "s_cbranch_scc1 8f\n\t"
    "s_mov_b32 exec_hi, 0\n\t"
    "global_load_dwordx4 v[6:9], v14, %[vs] sc1\n\t"
    "s_mov_b32 exec_hi, -1\n\t"
    "s_branch 5b\n\t"
    "8:\n\t"
    "s_mov_b32 %[cap], 0\n\t"
    "s_branch 6b\n\t"
    "10:\n\t"
    "v_max_f32 v11, v11, v10\n\t"
    "v_mov_b32 v26, v11\n\t"
    "s_nop 1\n\t"
    "v_permlane16_swap_b32 v11, v26\n\t"
    "v_max_f32 v11, v11, v26\n\t"
    "v_sub_f32 v28, v40, v11\n\t"
    "v_sub_f32 v29, v41, v11\n\t"
    "v_sub_f32 v30, v42, v11\n\t"
    "v_sub_f32 v31, v43, v11\n\t"
    "v_sub_f32 v11, v10, v11\n\t"
    "v_exp_f32 v11, v11\n\t"
    "v_exp_f32 v28, v28\n\t"
    "v_exp_f32 v29, v29\n\t"
    "v_exp_f32 v30, v30\n\t"
    "v_exp_f32 v31, v31\n\t"
    "v_mul_f32 v44, v44, v11\n\t"
    "v_mul_f32 v45, v45, v11\n\t"
    "v_mul_f32 v46, v46, v11\n\t"
    "v_mul_f32 v47, v47, v11\n\t"
    "v_mul_f32 v48, v48, v11\n\t"
    "v_mul_f32 v49, v49, v11\n\t"
    "v_mul_f32 v50, v50, v11\n\t"
    "v_mul_f32 v51, v51, v11\n\t"
    "v_mul_f32 v52, v52, v11\n\t"
    "v_mul_f32 v53, v53, v11\n\t"
    "v_mul_f32 v54, v54, v11\n\t"
    "v_mul_f32 v55, v55, v11\n\t"
    "s_branch 11b\n\t"
    "12:\n\t"
    "v_max3_f32 v10, v24, v25, v26\n\t"
    "v_max3_f32 v40, v27, v28, v29\n\t"
    "v_max3_f32 v10, v10, v30, v31\n\t"
    "v_max3_f32 v10, v10, v40, %[neg]\n\t"
    "v_mov_b32 v40, v10\n\t"
    "s_nop 1\n\t"
    "v_permlane16_swap_b32 v10, v40\n\t"
    "v_max_f32 v10, v10, v40\n\t"
    "v_mov_b32 v40, v10\n\t"
    "s_nop 1\n\t"
    "v_permlane32_swap_b32 v10, v40\n\t"
    "v_max_f32 v10, v10, v40\n\t"
    "v_sub_f32 v24, v24, v10\n\t"
    "v_sub_f32 v25, v25, v10\n\t"
    "v_sub_f32 v26, v26, v10\n\t"
    "v_sub_f32 v27, v27, v10\n\t"
    "v_sub_f32 v28, v28, v10\n\t"
    "v_sub_f32 v29, v29, v10\n\t"
    "v_sub_f32 v30, v30, v10\n\t"
    "v_sub_f32 v31, v31, v10\n\t"
    "s_mov_b32 %[bg], 0x7fc00000\n\t"
    "s_branch 13b\n\t"
    "9:"
    : [tc] "=&s"(tc), [sp] "=&s"(sp), [cap] "+s"(cap), [st] "=&s"(st), [bg] "=&s"(bg)
    : [ag] "s"(ag), [c44] "s"(0x4444u), [kv] "s"(kv), [vs] "s"(vs), [od] "s"(od), [od2] "s"(od + 262144), [neg] "s"(-1.0e38f), [lsc] "s"(LSC), [one] "s"(0x3F803F80u), [big] "s"(24.0f), "{v[64:95]}"(W.w0), "{v[96:127]}"(W.w1), "{v[128:159]}"(W.w2), "{v[160:191]}"(W.w3), "{v[192:223]}"(W.w4), "{v[224:255]}"(W.w5)
    : "v6", "v7", "v8", "v9", "v10", "v11", "v12", "v13", "v14", "v15", "v16", "v17", "v18", "v19", "v20", "v21", "v22", "v23", "v24", "v25", "v26", "v27", "v28", "v29", "v30", "v31", "v32", "v33", "v34", "v35", "v36", "v37", "v38", "v39", "v40", "v41", "v42", "v43", "v44", "v45", "v46", "v47", "v48", "v49", "v50", "v51", "v52", "v53", "v54", "v55", "v56", "v57", "v58", "v59", "v60", "v61", "v62", "v63", "vcc", "scc", "memory");
}
template <> __device__ __forceinline__ void qk_step<1>(const QKW& W, const char* ag, const char* vs, const char* od, const char* kv, unsigned& cap, unsigned& sp,
                                                        unsigned long long& tc, unsigned long long* ts, unsigned& d0, unsigned& d1) {
  unsigned st, bg;
  asm volatile("v_mbcnt_lo_u32_b32 v60, -1, 0\n\t"
    "v_mbcnt_hi_u32_b32 v60, -1, v60\n\t"
    "v_lshlrev_b32 v6, 4, v60\n\t"
    "s_mov_b32 %[sp], 0\n\t"
    "s_setprio 3\n\t"
    "global_load_dwordx4 v[40:43], v6, %[ag] sc1\n\t"
    "global_load_dwordx4 v[44:47], v6, %[ag] offset:1024 sc1\n\t"
    "s_memtime %[tc]\n\t"
    "v_add_u32 v7, 43008, v6\n\t"
    "v_add_u32 v8, 100352, v6\n\t"
    "ds_read_b128 v[24:27], v7 offset:0\n\t"
    "ds_read_b128 v[28:31], v7 offset:1024\n\t"
    "ds_read_b128 v[32:35], v7 offset:8192\n\t"
    "ds_read_b128 v[36:39], v7 offset:9216\n\t"
    "v_lshrrev_b32 v48, 4, v60\n\t"
    "v_and_b32 v49, 15, v60\n\t"
    "v_and_b32 v61, 4, v60\n\t"
    "v_mul_u32_u24 v61, 0x2aaa, v61\n\t"
    "v_lshrrev_b32 v9, 1, v60\n\t"
    "v_and_b32 v9, 2, v9\n\t"
    "v_or3_b32 v9, v9, v61, %[c44]\n\t"
    "v_add_u32 v14, 512, v6\n\t"
    "v_lshlrev_b32 v61, 2, v48\n\t"
    "v_lshl_or_b32 v10, v49, 4, v61\n\t"
    "v_lshrrev_b32 v50, 2, v49\n\t"
    "v_and_b32 v51, 3, v60\n\t"
    "v_and_b32 v61, 1, v60\n\t"
    "v_lshlrev_b32 v61, 3, v61\n\t"
    "v_lshl_or_b32 v11, v50, 4, v61\n\t"
    "v_lshl_or_b32 v11, v48, 7, v11\n\t"
    "v_xor_b32 v12, v48, v49\n\t"
    "v_lshlrev_b32 v12, 4, v12\n\t"
    "v_lshl_or_b32 v12, v49, 8, v12\n\t"
    "v_lshl_or_b32 v50, v48, 2, v50\n\t"
    "v_or_b32 v51, 4, v51\n\t"
    "v_xor_b32 v51, v51, v50\n\t"
    "v_lshlrev_b32 v13, 4, v51\n\t"
    "v_lshl_or_b32 v13, v50, 8, v13\n\t"
    "v_and_b32 v61, 3, v60\n\t"
    "v_lshlrev_b32 v61, 4, v61\n\t"
    "v_lshl_or_b32 v15, v48, 6, v61\n\t"
    "v_and_b32 v61, 8, v60\n\t"
    "v_lshl_or_b32 v15, v61, 14, v15\n\t"
    "v_and_b32 v61, 4, v60\n\t"
    "v_lshl_or_b32 v15, v61, 8, v15\n\t"
    "v_add_u32 v15, 256, v15\n\t"
    "3:\n\t"
    "v_mov_b32 v16, 0\n\t"
    "v_mov_b32 v17, 0\n\t"
    "v_mov_b32 v18, 0\n\t"
    "v_mov_b32 v19, 0\n\t"
    "v_mov_b32 v20, 0\n\t"
    "v_mov_b32 v21, 0\n\t"
    "v_mov_b32 v22, 0\n\t"
    "v_mov_b32 v23, 0\n\t"
    "s_waitcnt vmcnt(0) lgkmcnt(0)\n\t"
    "14:\n\t"
    "ds_write_b128 v6, v[40:43] offset:2048\n\t"
    "ds_write_b128 v6, v[44:47] offset:3072\n\t"
    "v_smfmac_f32_16x16x64_bf16 v[16:19], v[40:43], v[96:103], v9\n\t"
    "v_max3_u32 v60, v40, v41, v42\n\t"
    "v_max3_u32 v61, v43, v44, v45\n\t"
    "v_smfmac_f32_16x16x64_bf16 v[20:23], v[40:43], v[104:111], v9\n\t"
    "v_max3_u32 v60, v60, v46, v47\n\t"
    "v_max_u32 v60, v60, v61\n\t"
    "v_cmp_eq_u32 vcc, -1, v60\n\t"
    "s_cbranch_vccnz 1f\n\t"
    "2:\n\t"
    "s_waitcnt lgkmcnt(0)\n\t"
    "s_barrier\n\t"
    "ds_read_b128 v[40:43], v6 offset:12288\n\t"
    "ds_read_b128 v[48:51], v6 offset:0\n\t"
    "v_smfmac_f32_16x16x64_bf16 v[16:19], v[44:47], v[112:119], v9\n\t"
    "ds_read_b128 v[52:55], v6 offset:1024\n\t"
    "ds_read_b128 v[56:59], v6 offset:4096\n\t"
    "v_smfmac_f32_16x16x64_bf16 v[20:23], v[44:47], v[120:127], v9\n\t"
    "ds_read_b128 v[60:63], v6 offset:13312\n\t"
    "ds_read_b128 v[44:47], v6 offset:5120\n\t"
    "s_waitcnt lgkmcnt(5)\n\t"
    "v_smfmac_f32_16x16x64_bf16 v[16:19], v[40:43], v[24:31], v9\n\t"
    "ds_read_b128 v[24:27], v7 offset:16384\n\t"
    "ds_read_b128 v[28:31], v7 offset:17408\n\t"
    "v_smfmac_f32_16x16x64_bf16 v[20:23], v[40:43], v[32:39], v9\n\t"
    "ds_read_b128 v[32:35], v7 offset:24576\n\t"
    "ds_read_b128 v[36:39], v7 offset:25600\n\t"
    "ds_read_b128 v[40:43], v6 offset:6144\n\t"
    "s_waitcnt lgkmcnt(9)\n\t"
    "v_smfmac_f32_16x16x64_bf16 v[16:19], v[48:51], v[64:71], v9\n\t"
    "v_smfmac_f32_16x16x64_bf16 v[20:23], v[48:51], v[72:79], v9\n\t"
    "ds_read_b128 v[48:51], v6 offset:7168\n\t"
    "s_waitcnt lgkmcnt(9)\n\t"
    "v_smfmac_f32_16x16x64_bf16 v[16:19], v[52:55], v[80:87], v9\n\t"
    "v_smfmac_f32_16x16x64_bf16 v[20:23], v[52:55], v[88:95], v9\n\t"
    "ds_read_b128 v[52:55], v6 offset:14336\n\t"
    "s_waitcnt lgkmcnt(9)\n\t"
    "v_smfmac_f32_16x16x64_bf16 v[16:19], v[56:59], v[128:135], v9\n\t"
    "v_smfmac_f32_16x16x64_bf16 v[20:23], v[56:59], v[136:143], v9\n\t"
    "ds_read_b128 v[56:59], v6 offset:8192\n\t"
    "s_waitcnt lgkmcnt(6)\n\t"
    "v_smfmac_f32_16x16x64_bf16 v[16:19], v[60:63], v[24:31], v9\n\t"
    "ds_read_b128 v[24:27], v8 offset:0\n\t"
    "ds_read_b128 v[28:31], v8 offset:1024\n\t"
    "s_waitcnt lgkmcnt(6)\n\t"
    "v_smfmac_f32_16x16x64_bf16 v[20:23], v[60:63], v[32:39], v9\n\t"
    "ds_read_b128 v[32:35], v8 offset:8192\n\t"
    "ds_read_b128 v[36:39], v8 offset:9216\n\t"
    "ds_read_b128 v[60:63], v6 offset:9216\n\t"
    "v_smfmac_f32_16x16x64_bf16 v[16:19], v[44:47], v[144:151], v9\n\t"
    "v_smfmac_f32_16x16x64_bf16 v[20:23], v[44:47], v[152:159], v9\n\t"
    "ds_read_b128 v[44:47], v6 offset:10240\n\t"
    "s_waitcnt lgkmcnt(9)\n\t"
    "v_smfmac_f32_16x16x64_bf16 v[16:19], v[40:43], v[160:167], v9\n\t"
    "v_smfmac_f32_16x16x64_bf16 v[20:23], v[40:43], v[168:175], v9\n\t"
    "ds_read_b128 v[40:43], v6 offset:15360\n\t"
    "s_waitcnt lgkmcnt(9)\n\t"
    "v_smfmac_f32_16x16x64_bf16 v[16:19], v[48:51], v[176:183], v9\n\t"
    "v_smfmac_f32_16x16x64_bf16 v[20:23], v[48:51], v[184:191], v9\n\t"
    "ds_read_b128 v[48:51], v6 offset:11264\n\t"
    "s_waitcnt lgkmcnt(6)\n\t"
    "v_smfmac_f32_16x16x64_bf16 v[16:19], v[52:55], v[24:31], v9\n\t"
    "ds_read_b128 v[24:27], v8 offset:16384\n\t"
    "ds_read_b128 v[28:31], v8 offset:17408\n\t"
    "s_waitcnt lgkmcnt(6)\n\t"
    "v_smfmac_f32_16x16x64_bf16 v[20:23], v[52:55], v[32:39], v9\n\t"
    "ds_read_b128 v[32:35], v8 offset:24576\n\t"
    "ds_read_b128 v[36:39], v8 offset:25600\n\t"
    "v_smfmac_f32_16x16x64_bf16 v[16:19], v[56:59], v[192:199], v9\n\t"
    "v_smfmac_f32_16x16x64_bf16 v[20:23], v[56:59], v[200:207], v9\n\t"
    "s_waitcnt lgkmcnt(7)\n\t"
    "v_smfmac_f32_16x16x64_bf16 v[16:19], v[60:63], v[208:215], v9\n\t"
    "v_smfmac_f32_16x16x64_bf16 v[20:23], v[60:63], v[216:223], v9\n\t"
    "s_waitcnt lgkmcnt(6)\n\t"
    "v_smfmac_f32_16x16x64_bf16 v[16:19], v[44:47], v[224:231], v9\n\t"
    "v_smfmac_f32_16x16x64_bf16 v[20:23], v[44:47], v[232:239], v9\n\t"
    "s_waitcnt lgkmcnt(2)\n\t"
    "v_smfmac_f32_16x16x64_bf16 v[16:19], v[40:43], v[24:31], v9\n\t"
    "s_waitcnt lgkmcnt(0)\n\t"
    "v_smfmac_f32_16x16x64_bf16 v[20:23], v[40:43], v[32:39], v9\n\t"
    "v_smfmac_f32_16x16x64_bf16 v[16:19], v[48:51], v[240:247], v9\n\t"
    "v_smfmac_f32_16x16x64_bf16 v[20:23], v[48:51], v[248:255], v9\n\t"
    "v_xor_b32 v7, 64, v12\n\t"
    "v_xor_b32 v8, 0x80, v12\n\t"
    "v_xor_b32 v6, 0xc0, v12\n\t"
    "ds_read_b128 v[24:27], v12 offset:16384\n\t"
    "ds_read_b128 v[28:31], v12 offset:20480\n\t"
    "ds_read_b128 v[32:35], v7 offset:16384\n\t"
    "ds_read_b128 v[36:39], v7 offset:20480\n\t"
    "v_permlane16_swap_b32 v16, v18\n\t"
    "v_permlane16_swap_b32 v17, v19\n\t"
    "v_permlane16_swap_b32 v20, v22\n\t"
    "v_permlane16_swap_b32 v21, v23\n\t"
    "v_add_f32 v16, v16, v18\n\t"
    "v_add_f32 v17, v17, v19\n\t"
    "v_add_f32 v20, v20, v22\n\t"
    "v_mul_f32 v16, %[lsc], v16\n\t"
    "v_mul_f32 v17, %[lsc], v17\n\t"
    "v_cvt_pk_bf16_f32 v16, v16, v17\n\t"
    "v_add_f32 v21, v21, v23\n\t"
    "ds_write_b32 v10, v16 offset:33280\n\t"
    "v_mul_f32 v20, %[lsc], v20\n\t"
    "v_mul_f32 v21, %[lsc], v21\n\t"
    "v_cvt_pk_bf16_f32 v20, v20, v21\n\t"
    "ds_write_b32 v10, v20 offset:33536\n\t"
    "ds_read_b128 v[40:43], v8 offset:16384\n\t"
    "ds_read_b128 v[44:47], v8 offset:20480\n\t"
    "ds_read_b128 v[48:51], v6 offset:16384\n\t"
    "ds_read_b128 v[52:55], v6 offset:20480\n\t"
    "s_waitcnt lgkmcnt(4)\n\t"
    "s_barrier\n\t"
    "ds_read_b64_tr_b16 v[16:17], v11 offset:32768\n\t"
    "ds_read_b64_tr_b16 v[18:19], v11 offset:32832\n\t"
    "ds_read_b64_tr_b16 v[20:21], v11 offset:33280\n\t"
    "ds_read_b64_tr_b16 v[22:23], v11 offset:33344\n\t"
    "ds_read_b64_tr_b16 v[56:57], v11 offset:33792\n\t"
    "ds_read_b64_tr_b16 v[58:59], v11 offset:33856\n\t"
    "ds_read_b64_tr_b16 v[60:61], v11 offset:34304\n\t"
    "ds_read_b64_tr_b16 v[62:63], v11 offset:34368\n\t"
    "v_mov_b32 v6, 0\n\t"
    "v_mov_b32 v7, 0\n\t"
    "v_mov_b32 v8, 0\n\t"
    "v_mov_b32 v9, 0\n\t"
    "s_mov_b32 exec_hi, 0\n\t"
    "global_load_dwordx4 v[6:9], v14, %[vs] sc1\n\t"
    "s_mov_b32 exec_hi, -1\n\t"
    "s_waitcnt lgkmcnt(6)\n\t"
    "v_mfma_f32_16x16x32_bf16 v[24:27], v[24:27], v[16:19], 0\n\t"
    "v_mfma_f32_16x16x32_bf16 v[28:31], v[28:31], v[16:19], 0\n\t"
    "s_waitcnt lgkmcnt(4)\n\t"
    "v_mfma_f32_16x16x32_bf16 v[24:27], v[32:35], v[20:23], v[24:27]\n\t"
    "v_mfma_f32_16x16x32_bf16 v[28:31], v[36:39], v[20:23], v[28:31]\n\t"
    "ds_read_b64_tr_b16 v[32:33], v13 offset:24576\n\t"
    "ds_read_b64_tr_b16 v[34:35], v13 offset:28672\n\t"
    "ds_read_b64_tr_b16 v[36:37], v13 offset:24584\n\t"
    "ds_read_b64_tr_b16 v[38:39], v13 offset:28680\n\t"
    "s_waitcnt lgkmcnt(6)\n\t"
    "v_mfma_f32_16x16x32_bf16 v[24:27], v[40:43], v[56:59], v[24:27]\n\t"
    "v_mfma_f32_16x16x32_bf16 v[28:31], v[44:47], v[56:59], v[28:31]\n\t"
    "s_waitcnt lgkmcnt(4)\n\t"
    "v_mfma_f32_16x16x32_bf16 v[24:27], v[48:51], v[60:63], v[24:27]\n\t"
    "v_mfma_f32_16x16x32_bf16 v[28:31], v[52:55], v[60:63], v[28:31]\n\t"
    "s_setprio 1\n\t"
    "v_mov_b32 v52, %[one]\n\t"
    "v_mov_b32 v53, %[one]\n\t"
    "v_mov_b32 v54, %[one]\n\t"
    "v_mov_b32 v55, %[one]\n\t"
    "s_nop 2\n\t"
    "v_max3_f32 v10, |v24|, |v25|, |v26|\n\t"
    "v_max3_f32 v40, |v27|, |v28|, |v29|\n\t"
    "v_max3_f32 v10, v10, |v30|, |v31|\n\t"
    "v_max_f32 v10, v10, v40\n\t"
    "v_cmp_nge_f32 vcc, %[big], v10\n\t"
    "s_mov_b32 %[bg], %[big]\n\t"
    "v_mov_b32 v10, 0\n\t"
    "s_cbranch_vccnz 12f\n\t"
    "13:\n\t"
    "v_exp_f32 v24, v24\n\t"
    "v_exp_f32 v25, v25\n\t"
    "v_exp_f32 v26, v26\n\t"
    "v_exp_f32 v27, v27\n\t"
    "v_exp_f32 v28, v28\n\t"
    "v_exp_f32 v29, v29\n\t"
    "v_exp_f32 v30, v30\n\t"
    "v_exp_f32 v31, v31\n\t"
    "v_cvt_pk_bf16_f32 v40, v24, v25\n\t"
    "v_cvt_pk_bf16_f32 v41, v26, v27\n\t"
    "v_cvt_pk_bf16_f32 v42, v28, v29\n\t"
    "v_cvt_pk_bf16_f32 v43, v30, v31\n\t"
    "s_setprio 3\n\t"
    "s_waitcnt lgkmcnt(0)\n\t"
    "s_nop 0\n\t"
    "v_mfma_f32_16x16x32_bf16 v[44:47], v[32:35], v[40:43], 0\n\t"
    "v_mfma_f32_16x16x32_bf16 v[48:51], v[36:39], v[40:43], 0\n\t"
    "v_mfma_f32_16x16x32_bf16 v[52:55], v[52:55], v[40:43], 0\n\t"
    "v_mov_b32 v12, 0\n\t"
    "v_mov_b32 v13, 0\n\t"
    "s_mov_b32 exec_hi, 0\n\t"
    "v_mov_b32 v12, %[one]\n\t"
    "v_mov_b32 v13, %[one]\n\t"
    "s_mov_b32 exec_hi, -1\n\t"
    "s_barrier\n\t"
    "ds_read_b64_tr_b16 v[24:25], v11 offset:34816\n\t"
    "ds_read_b64_tr_b16 v[26:27], v11 offset:34880\n\t"
    "ds_read_b64_tr_b16 v[28:29], v11 offset:35328\n\t"
    "ds_read_b64_tr_b16 v[30:31], v11 offset:35392\n\t"
    "ds_read_b64_tr_b16 v[32:33], v11 offset:35840\n\t"
    "ds_read_b64_tr_b16 v[34:35], v11 offset:35904\n\t"
    "ds_read_b64_tr_b16 v[36:37], v11 offset:36352\n\t"
    "ds_read_b64_tr_b16 v[38:39], v11 offset:36416\n\t"
    "s_waitcnt lgkmcnt(6)\n\t"
    "v_mfma_f32_16x16x32_bf16 v[40:43], v[24:27], v[16:19], 0\n\t"
    "s_waitcnt lgkmcnt(4)\n\t"
    "v_mfma_f32_16x16x32_bf16 v[40:43], v[28:31], v[20:23], v[40:43]\n\t"
    "s_waitcnt lgkmcnt(2)\n\t"
    "v_mfma_f32_16x16x32_bf16 v[40:43], v[32:35], v[56:59], v[40:43]\n\t"
    "s_waitcnt lgkmcnt(0)\n\t"
    "v_mfma_f32_16x16x32_bf16 v[40:43], v[36:39], v[60:63], v[40:43]\n\t"
    "5:\n\t"
    "s_waitcnt vmcnt(0)\n\t"
    "v_max3_u32 v11, v6, v7, v8\n\t"
    "v_max_u32 v11, v11, v9\n\t"
    "v_cmp_eq_u32 vcc, -1, v11\n\t"
    "s_cbranch_vccnz 4f\n\t"
    "6:\n\t"
    "s_nop 2\n\t"
    "v_exp_f32 v28, v40\n\t"
    "v_max3_f32 v11, v40, v41, v42\n\t"
    "v_exp_f32 v29, v41\n\t"
    "v_max_f32 v11, v11, v43\n\t"
    "v_exp_f32 v30, v42\n\t"
    "v_cmp_nge_f32 vcc, %[bg], v11\n\t"
    "v_exp_f32 v31, v43\n\t"
    "s_cbranch_vccnz 10f\n\t"
    "11:\n\t"
    "v_cvt_pk_bf16_f32 v24, v28, v29\n\t"
    "v_cvt_pk_bf16_f32 v25, v30, v31\n\t"
    "s_nop 1\n\t"
    "v_mfma_f32_16x16x16_bf16 v[52:55], v[12:13], v[24:25], v[52:55]\n\t"
    "v_mfma_f32_16x16x16_bf16 v[44:47], v[6:7], v[24:25], v[44:47]\n\t"
    "v_mfma_f32_16x16x16_bf16 v[48:51], v[8:9], v[24:25], v[48:51]\n\t"
    "s_nop 5\n\t"
    "v_rcp_f32 v26, v52\n\t"
    "s_nop 0\n\t"
    "v_pk_mul_f32 v[44:45], v[44:45], v[26:27] op_sel_hi:[1,0]\n\t"
    "v_pk_mul_f32 v[46:47], v[46:47], v[26:27] op_sel_hi:[1,0]\n\t"
    "v_pk_mul_f32 v[48:49], v[48:49], v[26:27] op_sel_hi:[1,0]\n\t"
    "v_pk_mul_f32 v[50:51], v[50:51], v[26:27] op_sel_hi:[1,0]\n\t"
    "v_cvt_pk_bf16_f32 v36, v44, v45\n\t"
    "v_cvt_pk_bf16_f32 v37, v46, v47\n\t"
    "v_cvt_pk_bf16_f32 v38, v48, v49\n\t"
    "v_cvt_pk_bf16_f32 v39, v50, v51\n\t"
    "v_min_u32 v36, -2, v36\n\t"
    "v_min_u32 v37, -2, v37\n\t"
    "v_min_u32 v38, -2, v38\n\t"
    "v_min_u32 v39, -2, v39\n\t"
    "global_store_dwordx4 v15, v[36:39], %[od] sc1\n\t"
    "s_barrier\n\t"
    "s_mov_b32 exec_hi, 0\n\t"
    "global_store_dwordx4 v14, v[6:9], %[kv] sc1\n\t"
    "s_mov_b32 exec_hi, -1\n\t"
    "s_nop 0\n\t"
    "s_waitcnt lgkmcnt(0)\n\t"
    "s_branch 9f\n\t"
    "1:\n\t"
    "s_add_u32 %[sp], %[sp], 1\n\t"
    "s_cmp_ge_u32 %[sp], %[cap]\n\t"
    "s_cbranch_scc1 7f\n\t"
    "global_load_dwordx4 v[40:43], v6, %[ag] sc1\n\t"
    "global_load_dwordx4 v[44:47], v6, %[ag] offset:1024 sc1\n\t"
    "s_waitcnt lgkmcnt(0)\n\t"
    "s_memtime %[tc]\n\t"
    "s_nop 7\n\t"
    "s_branch 3b\n\t"
    "7:\n\t"
    "s_mov_b32 %[cap], 0\n\t"
    "s_branch 2b\n\t"
    "4:\n\t"
    "s_add_u32 %[sp], %[sp], 0x10000\n\t"
    "s_lshr_b32 %[st], %[sp], 16\n\t"
    "s_cmp_ge_u32 %[st], %[cap]\n\t"
    "s_cbranch_scc1 8f\n\t"
    "s_mov_b32 exec_hi, 0\n\t"
    "global_load_dwordx4 v[6:9], v14, %[vs] sc1\n\t"
    "s_mov_b32 exec_hi, -1\n\t"
    "s_branch 5b\n\t"
    "8:\n\t"
    "s_mov_b32 %[cap], 0\n\t"
    "s_branch 6b\n\t"
    "10:\n\t"
    "v_max_f32 v11, v11, v10\n\t"
    "v_mov_b32 v26, v11\n\t"
    "s_nop 1\n\t"
    "v_permlane16_swap_b32 v11, v26\n\t"
    "v_max_f32 v11, v11, v26\n\t"
    "v_sub_f32 v28, v40, v11\n\t"
    "v_sub_f32 v29, v41, v11\n\t"
    "v_sub_f32 v30, v42, v11\n\t"
    "v_sub_f32 v31, v43, v11\n\t"
    "v_sub_f32 v11, v10, v11\n\t"
    "v_exp_f32 v11, v11\n\t"
    "v_exp_f32 v28, v28\n\t"
    "v_exp_f32 v29, v29\n\t"
    "v_exp_f32 v30, v30\n\t"
    "v_exp_f32 v31, v31\n\t"
    "v_mul_f32 v44, v44, v11\n\t"
    "v_mul_f32 v45, v45, v11\n\t"
    "v_mul_f32 v46, v46, v11\n\t"
    "v_mul_f32 v47, v47, v11\n\t"
    "v_mul_f32 v48, v48, v11\n\t"
    "v_mul_f32 v49, v49, v11\n\t"
    "v_mul_f32 v50, v50, v11\n\t"
    "v_mul_f32 v51, v51, v11\n\t"
    "v_mul_f32 v52, v52, v11\n\t"
    "v_mul_f32 v53, v53, v11\n\t"
    "v_mul_f32 v54, v54, v11\n\t"
    "v_mul_f32 v55, v55, v11\n\t"
    "s_branch 11b\n\t"
    "12:\n\t"
    "v_max3_f32 v10, v24, v25, v26\n\t"
    "v_max3_f32 v40, v27, v28, v29\n\t"
    "v_max3_f32 v10, v10, v30, v31\n\t"
    "v_max3_f32 v10, v10, v40, %[neg]\n\t"
    "v_mov_b32 v40, v10\n\t"
    "s_nop 1\n\t"
    "v_permlane16_swap_b32 v10, v40\n\t"
    "v_max_f32 v10, v10, v40\n\t"
    "v_mov_b32 v40, v10\n\t"
    "s_nop 1\n\t"
    "v_permlane32_swap_b32 v10, v40\n\t"
    "v_max_f32 v10, v10, v40\n\t"
    "v_sub_f32 v24, v24, v10\n\t"
    "v_sub_f32 v25, v25, v10\n\t"
    "v_sub_f32 v26, v26, v10\n\t"
    "v_sub_f32 v27, v27, v10\n\t"
    "v_sub_f32 v28, v28, v10\n\t"
    "v_sub_f32 v29, v29, v10\n\t"
    "v_sub_f32 v30, v30, v10\n\t"
    "v_sub_f32 v31, v31, v10\n\t"
    "s_mov_b32 %[bg], 0x7fc00000\n\t"
    "s_branch 13b\n\t"
    "9:"
    : [tc] "=&s"(tc), [sp] "=&s"(sp), [cap] "+s"(cap), [st] "=&s"(st), [bg] "=&s"(bg)
    : [ag] "s"(ag), [c44] "s"(0x4444u), [kv] "s"(kv), [vs] "s"(vs), [od] "s"(od), [od2] "s"(od + 262144), [neg] "s"(-1.0e38f), [lsc] "s"(LSC), [one] "s"(0x3F803F80u), [big] "s"(24.0f), "{v[64:95]}"(W.w0), "{v[96:127]}"(W.w1), "{v[128:159]}"(W.w2), "{v[160:191]}"(W.w3), "{v[192:223]}"(W.w4), "{v[224:255]}"(W.w5)
    : "v6", "v7", "v8", "v9", "v10", "v11", "v12", "v13", "v14", "v15", "v16", "v17", "v18", "v19", "v20", "v21", "v22", "v23", "v24", "v25", "v26", "v27", "v28", "v29", "v30", "v31", "v32", "v33", "v34", "v35", "v36", "v37", "v38", "v39", "v40", "v41", "v42", "v43", "v44", "v45", "v46", "v47", "v48", "v49", "v50", "v51", "v52", "v53", "v54", "v55", "v56", "v57", "v58", "v59", "v60", "v61", "v62", "v63", "vcc", "scc", "memory");
}
template <> __device__ __forceinline__ void qk_step<2>(const QKW& W, const char* ag, const char* vs, const char* od, const char* kv, unsigned& cap, unsigned& sp,
                                                        unsigned long long& tc, unsigned long long* ts, unsigned& d0, unsigned& d1) {
  unsigned st, bg;
  asm volatile("v_mbcnt_lo_u32_b32 v60, -1, 0\n\t"
    "v_mbcnt_hi_u32_b32 v60, -1, v60\n\t"
    "v_lshlrev_b32 v6, 4, v60\n\t"
    "s_mov_b32 %[sp], 0\n\t"
    "s_setprio 3\n\t"
    "global_load_dwordx4 v[40:43], v6, %[ag] sc1\n\t"
    "global_load_dwordx4 v[44:47], v6, %[ag] offset:1024 sc1\n\t"
    "s_memtime %[tc]\n\t"
    "v_add_u32 v7, 45056, v6\n\t"
    "v_add_u32 v8, 102400, v6\n\t"
    "ds_read_b128 v[24:27], v7 offset:0\n\t"
    "ds_read_b128 v[28:31], v7 offset:1024\n\t"
    "ds_read_b128 v[32:35], v7 offset:8192\n\t"
    "ds_read_b128 v[36:39], v7 offset:9216\n\t"
    "v_lshrrev_b32 v48, 4, v60\n\t"
    "v_and_b32 v49, 15, v60\n\t"
    "v_and_b32 v61, 4, v60\n\t"
    "v_mul_u32_u24 v61, 0x2aaa, v61\n\t"
    "v_lshrrev_b32 v9, 1, v60\n\t"
    "v_and_b32 v9, 2, v9\n\t"
    "v_or3_b32 v9, v9, v61, %[c44]\n\t"
    "v_add_u32 v14, 1024, v6\n\t"
    "v_lshlrev_b32 v61, 2, v48\n\t"
    "v_lshl_or_b32 v10, v49, 4, v61\n\t"
    "v_lshrrev_b32 v50, 2, v49\n\t"
    "v_and_b32 v51, 3, v60\n\t"
    "v_and_b32 v61, 1, v60\n\t"
    "v_lshlrev_b32 v61, 3, v61\n\t"
    "v_lshl_or_b32 v11, v50, 4, v61\n\t"
    "v_lshl_or_b32 v11, v48, 7, v11\n\t"
    "v_xor_b32 v12, v48, v49\n\t"
    "v_lshlrev_b32 v12, 4, v12\n\t"
    "v_lshl_or_b32 v12, v49, 8, v12\n\t"
    "v_lshl_or_b32 v50, v48, 2, v50\n\t"
    "v_or_b32 v51, 8, v51\n\t"
    "v_xor_b32 v51, v51, v50\n\t"
    "v_lshlrev_b32 v13, 4, v51\n\t"
    "v_lshl_or_b32 v13, v50, 8, v13\n\t"
    "v_and_b32 v61, 3, v60\n\t"
    "v_lshlrev_b32 v61, 4, v61\n\t"
    "v_lshl_or_b32 v15, v48, 6, v61\n\t"
    "v_and_b32 v61, 8, v60\n\t"
    "v_lshl_or_b32 v15, v61, 14, v15\n\t"
    "v_and_b32 v61, 4, v60\n\t"
    "v_lshl_or_b32 v15, v61, 8, v15\n\t"
    "v_add_u32 v15, 512, v15\n\t"
    "3:\n\t"
    "v_mov_b32 v16, 0\n\t"
    "v_mov_b32 v17, 0\n\t"
    "v_mov_b32 v18, 0\n\t"
    "v_mov_b32 v19, 0\n\t"
    "v_mov_b32 v20, 0\n\t"
    "v_mov_b32 v21, 0\n\t"
    "v_mov_b32 v22, 0\n\t"
    "v_mov_b32 v23, 0\n\t"
    "s_waitcnt vmcnt(0) lgkmcnt(0)\n\t"
    "14:\n\t"
    "ds_write_b128 v6, v[40:43] offset:4096\n\t"
    "ds_write_b128 v6, v[44:47] offset:5120\n\t"
    "v_smfmac_f32_16x16x64_bf16 v[16:19], v[40:43], v[128:135], v9\n\t"
    "v_max3_u32 v60, v40, v41, v42\n\t"
    "v_max3_u32 v61, v43, v44, v45\n\t"
    "v_smfmac_f32_16x16x64_bf16 v[20:23], v[40:43], v[136:143], v9\n\t"
    "v_max3_u32 v60, v60, v46, v47\n\t"
    "v_max_u32 v60, v60, v61\n\t"
    "v_cmp_eq_u32 vcc, -1, v60\n\t"
    "s_cbranch_vccnz 1f\n\t"
    "2:\n\t"
    "s_waitcnt lgkmcnt(0)\n\t"
    "s_barrier\n\t"
    "ds_read_b128 v[40:43], v6 offset:12288\n\t"
    "ds_read_b128 v[48:51], v6 offset:0\n\t"
    "v_smfmac_f32_16x16x64_bf16 v[16:19], v[44:47], v[144:151], v9\n\t"
    "ds_read_b128 v[52:55], v6 offset:1024\n\t"
    "ds_read_b128 v[56:59], v6 offset:2048\n\t"
    "v_smfmac_f32_16x16x64_bf16 v[20:23], v[44:47], v[152:159], v9\n\t"
    "ds_read_b128 v[60:63], v6 offset:13312\n\t"
    "ds_read_b128 v[44:47], v6 offset:3072\n\t"
    "s_waitcnt lgkmcnt(5)\n\t"
    "v_smfmac_f32_16x16x64_bf16 v[16:19], v[40:43], v[24:31], v9\n\t"
    "ds_read_b128 v[24:27], v7 offset:16384\n\t"
    "ds_read_b128 v[28:31], v7 offset:17408\n\t"
    "v_smfmac_f32_16x16x64_bf16 v[20:23], v[40:43], v[32:39], v9\n\t"
    "ds_read_b128 v[32:35], v7 offset:24576\n\t"
    "ds_read_b128 v[36:39], v7 offset:25600\n\t"
    "ds_read_b128 v[40:43], v6 offset:6144\n\t"
    "s_waitcnt lgkmcnt(9)\n\t"
    "v_smfmac_f32_16x16x64_bf16 v[16:19], v[48:51], v[64:71], v9\n\t"
    "v_smfmac_f32_16x16x64_bf16 v[20:23], v[48:51], v[72:79], v9\n\t"
    "ds_read_b128 v[48:51], v6 offset:7168\n\t"
    "s_waitcnt lgkmcnt(9)\n\t"
    "v_smfmac_f32_16x16x64_bf16 v[16:19], v[52:55], v[80:87], v9\n\t"
    "v_smfmac_f32_16x16x64_bf16 v[20:23], v[52:55], v[88:95], v9\n\t"
    "ds_read_b128 v[52:55], v6 offset:14336\n\t"
    "s_waitcnt lgkmcnt(9)\n\t"
    "v_smfmac_f32_16x16x64_bf16 v[16:19], v[56:59], v[96:103], v9\n\t"
    "v_smfmac_f32_16x16x64_bf16 v[20:23], v[56:59], v[104:111], v9\n\t"
    "ds_read_b128 v[56:59], v6 offset:8192\n\t"
    "s_waitcnt lgkmcnt(6)\n\t"
    "v_smfmac_f32_16x16x64_bf16 v[16:19], v[60:63], v[24:31], v9\n\t"
    "ds_read_b128 v[24:27], v8 offset:0\n\t"
    "ds_read_b128 v[28:31], v8 offset:1024\n\t"
    "s_waitcnt lgkmcnt(6)\n\t"
    "v_smfmac_f32_16x16x64_bf16 v[20:23], v[60:63], v[32:39], v9\n\t"
    "ds_read_b128 v[32:35], v8 offset:8192\n\t"
    "ds_read_b128 v[36:39], v8 offset:9216\n\t"
    "ds_read_b128 v[60:63], v6 offset:9216\n\t"
    "v_smfmac_f32_16x16x64_bf16 v[16:19], v[44:47], v[112:119], v9\n\t"
    "v_smfmac_f32_16x16x64_bf16 v[20:23], v[44:47], v[120:127], v9\n\t"
    "ds_read_b128 v[44:47], v6 offset:10240\n\t"
    "s_waitcnt lgkmcnt(9)\n\t"
    "v_smfmac_f32_16x16x64_bf16 v[16:19], v[40:43], v[160:167], v9\n\t"
    "v_smfmac_f32_16x16x64_bf16 v[20:23], v[40:43], v[168:175], v9\n\t"
    "ds_read_b128 v[40:43], v6 offset:15360\n\t"
    "s_waitcnt lgkmcnt(9)\n\t"
    "v_smfmac_f32_16x16x64_bf16 v[16:19], v[48:51], v[176:183], v9\n\t"
    "v_smfmac_f32_16x16x64_bf16 v[20:23], v[48:51], v[184:191], v9\n\t"
    "ds_read_b128 v[48:51], v6 offset:11264\n\t"
    "s_waitcnt lgkmcnt(6)\n\t"
    "v_smfmac_f32_16x16x64_bf16 v[16:19], v[52:55], v[24:31], v9\n\t"
    "ds_read_b128 v[24:27], v8 offset:16384\n\t"
    "ds_read_b128 v[28:31], v8 offset:17408\n\t"
    "s_waitcnt lgkmcnt(6)\n\t"
    "v_smfmac_f32_16x16x64_bf16 v[20:23], v[52:55], v[32:39], v9\n\t"
    "ds_read_b128 v[32:35], v8 offset:24576\n\t"
    "ds_read_b128 v[36:39], v8 offset:25600\n\t"
    "v_smfmac_f32_16x16x64_bf16 v[16:19], v[56:59], v[192:199], v9\n\t"
    "v_smfmac_f32_16x16x64_bf16 v[20:23], v[56:59], v[200:207], v9\n\t"
    "s_waitcnt lgkmcnt(7)\n\t"
    "v_smfmac_f32_16x16x64_bf16 v[16:19], v[60:63], v[208:215], v9\n\t"
    "v_smfmac_f32_16x16x64_bf16 v[20:23], v[60:63], v[216:223], v9\n\t"
    "s_waitcnt lgkmcnt(6)\n\t"
    "v_smfmac_f32_16x16x64_bf16 v[16:19], v[44:47], v[224:231], v9\n\t"
    "v_smfmac_f32_16x16x64_bf16 v[20:23], v[44:47], v[232:239], v9\n\t"
    "s_waitcnt lgkmcnt(2)\n\t"
    "v_smfmac_f32_16x16x64_bf16 v[16:19], v[40:43], v[24:31], v9\n\t"
    "s_waitcnt lgkmcnt(0)\n\t"
    "v_smfmac_f32_16x16x64_bf16 v[20:23], v[40:43], v[32:39], v9\n\t"
    "v_smfmac_f32_16x16x64_bf16 v[16:19], v[48:51], v[240:247], v9\n\t"
    "v_smfmac_f32_16x16x64_bf16 v[20:23], v[48:51], v[248:255], v9\n\t"
    "v_xor_b32 v7, 64, v12\n\t"
    "v_xor_b32 v8, 0x80, v12\n\t"
    "v_xor_b32 v6, 0xc0, v12\n\t"
    "ds_read_b128 v[24:27], v12 offset:16384\n\t"
    "ds_read_b128 v[28:31], v12 offset:20480\n\t"
    "ds_read_b128 v[32:35], v7 offset:16384\n\t"
    "ds_read_b128 v[36:39], v7 offset:20480\n\t"
    "v_permlane16_swap_b32 v16, v18\n\t"
    "v_permlane16_swap_b32 v17, v19\n\t"
    "v_permlane16_swap_b32 v20, v22\n\t"
    "v_permlane16_swap_b32 v21, v23\n\t"
    "v_add_f32 v16, v16, v18\n\t"
    "v_add_f32 v17, v17, v19\n\t"
    "v_add_f32 v20, v20, v22\n\t"
    "v_mul_f32 v16, %[lsc], v16\n\t"
    "v_mul_f32 v17, %[lsc], v17\n\t"
    "v_cvt_pk_bf16_f32 v16, v16, v17\n\t"
    "v_add_f32 v21, v21, v23\n\t"
    "ds_write_b32 v10, v16 offset:33792\n\t"
    "v_mul_f32 v20, %[lsc], v20\n\t"
    "v_mul_f32 v21, %[lsc], v21\n\t"
    "v_cvt_pk_bf16_f32 v20, v20, v21\n\t"
    "ds_write_b32 v10, v20 offset:34048\n\t"
    "ds_read_b128 v[40:43], v8 offset:16384\n\t"
    "ds_read_b128 v[44:47], v8 offset:20480\n\t"
    "ds_read_b128 v[48:51], v6 offset:16384\n\t"
    "ds_read_b128 v[52:55], v6 offset:20480\n\t"
    "s_waitcnt lgkmcnt(4)\n\t"
    "s_barrier\n\t"
    "ds_read_b64_tr_b16 v[16:17], v11 offset:32768\n\t"
    "ds_read_b64_tr_b16 v[18:19], v11 offset:32832\n\t"
    "ds_read_b64_tr_b16 v[20:21], v11 offset:33280\n\t"
    "ds_read_b64_tr_b16 v[22:23], v11 offset:33344\n\t"
    "ds_read_b64_tr_b16 v[56:57], v11 offset:33792\n\t"
    "ds_read_b64_tr_b16 v[58:59], v11 offset:33856\n\t"
    "ds_read_b64_tr_b16 v[60:61], v11 offset:34304\n\t"
    "ds_read_b64_tr_b16 v[62:63], v11 offset:34368\n\t"
    "v_mov_b32 v6, 0\n\t"
    "v_mov_b32 v7, 0\n\t"
    "v_mov_b32 v8, 0\n\t"
    "v_mov_b32 v9, 0\n\t"
    "s_mov_b32 exec_hi, 0\n\t"
    "global_load_dwordx4 v[6:9], v14, %[vs] sc1\n\t"
    "s_mov_b32 exec_hi, -1\n\t"
    "s_waitcnt lgkmcnt(6)\n\t"
    "v_mfma_f32_16x16x32_bf16 v[24:27], v[24:27], v[16:19], 0\n\t"
    "v_mfma_f32_16x16x32_bf16 v[28:31], v[28:31], v[16:19], 0\n\t"
    "s_waitcnt lgkmcnt(4)\n\t"
    "v_mfma_f32_16x16x32_bf16 v[24:27], v[32:35], v[20:23], v[24:27]\n\t"
    "v_mfma_f32_16x16x32_bf16 v[28:31], v[36:39], v[20:23], v[28:31]\n\t"
    "ds_read_b64_tr_b16 v[32:33], v13 offset:24576\n\t"
    "ds_read_b64_tr_b16 v[34:35], v13 offset:28672\n\t"
    "ds_read_b64_tr_b16 v[36:37], v13 offset:24584\n\t"
    "ds_read_b64_tr_b16 v[38:39], v13 offset:28680\n\t"
    "s_waitcnt lgkmcnt(6)\n\t"
    "v_mfma_f32_16x16x32_bf16 v[24:27], v[40:43], v[56:59], v[24:27]\n\t"
    "v_mfma_f32_16x16x32_bf16 v[28:31], v[44:47], v[56:59], v[28:31]\n\t"
    "s_waitcnt lgkmcnt(4)\n\t"
    "v_mfma_f32_16x16x32_bf16 v[24:27], v[48:51], v[60:63], v[24:27]\n\t"
    "v_mfma_f32_16x16x32_bf16 v[28:31], v[52:55], v[60:63], v[28:31]\n\t"
    "s_setprio 1\n\t"
    "v_mov_b32 v52, %[one]\n\t"
    "v_mov_b32 v53, %[one]\n\t"
    "v_mov_b32 v54, %[one]\n\t"
    "v_mov_b32 v55, %[one]\n\t"
    "s_nop 2\n\t"
    "v_max3_f32 v10, |v24|, |v25|, |v26|\n\t"
    "v_max3_f32 v40, |v27|, |v28|, |v29|\n\t"
    "v_max3_f32 v10, v10, |v30|, |v31|\n\t"
    "v_max_f32 v10, v10, v40\n\t"
    "v_cmp_nge_f32 vcc, %[big], v10\n\t"
    "s_mov_b32 %[bg], %[big]\n\t"
    "v_mov_b32 v10, 0\n\t"
    "s_cbranch_vccnz 12f\n\t"
    "13:\n\t"
    "v_exp_f32 v24, v24\n\t"
    "v_exp_f32 v25, v25\n\t"
    "v_exp_f32 v26, v26\n\t"
    "v_exp_f32 v27, v27\n\t"
    "v_exp_f32 v28, v28\n\t"
    "v_exp_f32 v29, v29\n\t"
    "v_exp_f32 v30, v30\n\t"
    "v_exp_f32 v31, v31\n\t"
    "v_cvt_pk_bf16_f32 v40, v24, v25\n\t"
    "v_cvt_pk_bf16_f32 v41, v26, v27\n\t"
    "v_cvt_pk_bf16_f32 v42, v28, v29\n\t"
    "v_cvt_pk_bf16_f32 v43, v30, v31\n\t"
    "s_setprio 3\n\t"
    "s_waitcnt lgkmcnt(0)\n\t"
    "s_nop 0\n\t"
    "v_mfma_f32_16x16x32_bf16 v[44:47], v[32:35], v[40:43], 0\n\t"
    "v_mfma_f32_16x16x32_bf16 v[48:51], v[36:39], v[40:43], 0\n\t"
    "v_mfma_f32_16x16x32_bf16 v[52:55], v[52:55], v[40:43], 0\n\t"
    "v_mov_b32 v12, 0\n\t"
    "v_mov_b32 v13, 0\n\t"
    "s_mov_b32 exec_hi, 0\n\t"
    "v_mov_b32 v12, %[one]\n\t"
    "v_mov_b32 v13, %[one]\n\t"
    "s_mov_b32 exec_hi, -1\n\t"
    "s_barrier\n\t"
    "ds_read_b64_tr_b16 v[24:25], v11 offset:34816\n\t"
    "ds_read_b64_tr_b16 v[26:27], v11 offset:34880\n\t"
    "ds_read_b64_tr_b16 v[28:29], v11 offset:35328\n\t"
    "ds_read_b64_tr_b16 v[30:31], v11 offset:35392\n\t"
    "ds_read_b64_tr_b16 v[32:33], v11 offset:35840\n\t"
    "ds_read_b64_tr_b16 v[34:35], v11 offset:35904\n\t"
    "ds_read_b64_tr_b16 v[36:37], v11 offset:36352\n\t"
    "ds_read_b64_tr_b16 v[38:39], v11 offset:36416\n\t"
    "s_waitcnt lgkmcnt(6)\n\t"
    "v_mfma_f32_16x16x32_bf16 v[40:43], v[24:27], v[16:19], 0\n\t"
    "s_waitcnt lgkmcnt(4)\n\t"
    "v_mfma_f32_16x16x32_bf16 v[40:43], v[28:31], v[20:23], v[40:43]\n\t"
    "s_waitcnt lgkmcnt(2)\n\t"
    "v_mfma_f32_16x16x32_bf16 v[40:43], v[32:35], v[56:59], v[40:43]\n\t"
    "s_waitcnt lgkmcnt(0)\n\t"
    "v_mfma_f32_16x16x32_bf16 v[40:43], v[36:39], v[60:63], v[40:43]\n\t"
    "5:\n\t"
    "s_waitcnt vmcnt(0)\n\t"
    "v_max3_u32 v11, v6, v7, v8\n\t"
    "v_max_u32 v11, v11, v9\n\t"
    "v_cmp_eq_u32 vcc, -1, v11\n\t"
    "s_cbranch_vccnz 4f\n\t"
    "6:\n\t"
    "s_nop 2\n\t"
    "v_exp_f32 v28, v40\n\t"
    "v_max3_f32 v11, v40, v41, v42\n\t"
    "v_exp_f32 v29, v41\n\t"
    "v_max_f32 v11, v11, v43\n\t"
    "v_exp_f32 v30, v42\n\t"
    "v_cmp_nge_f32 vcc, %[bg], v11\n\t"
    "v_exp_f32 v31, v43\n\t"
    "s_cbranch_vccnz 10f\n\t"
    "11:\n\t"
    "v_cvt_pk_bf16_f32 v24, v28, v29\n\t"
    "v_cvt_pk_bf16_f32 v25, v30, v31\n\t"
    "s_nop 1\n\t"
    "v_mfma_f32_16x16x16_bf16 v[52:55], v[12:13], v[24:25], v[52:55]\n\t"
    "v_mfma_f32_16x16x16_bf16 v[44:47], v[6:7], v[24:25], v[44:47]\n\t"
    "v_mfma_f32_16x16x16_bf16 v[48:51], v[8:9], v[24:25], v[48:51]\n\t"
    "s_nop 5\n\t"
    "v_rcp_f32 v26, v52\n\t"
    "s_nop 0\n\t"
    "v_pk_mul_f32 v[44:45], v[44:45], v[26:27] op_sel_hi:[1,0]\n\t"
    "v_pk_mul_f32 v[46:47], v[46:47], v[26:27] op_sel_hi:[1,0]\n\t"
    "v_pk_mul_f32 v[48:49], v[48:49], v[26:27] op_sel_hi:[1,0]\n\t"
    "v_pk_mul_f32 v[50:51], v[50:51], v[26:27] op_sel_hi:[1,0]\n\t"
    "v_cvt_pk_bf16_f32 v36, v44, v45\n\t"
    "v_cvt_pk_bf16_f32 v37, v46, v47\n\t"
    "v_cvt_pk_bf16_f32 v38, v48, v49\n\t"
    "v_cvt_pk_bf16_f32 v39, v50, v51\n\t"
    "v_min_u32 v36, -2, v36\n\t"
    "v_min_u32 v37, -2, v37\n\t"
    "v_min_u32 v38, -2, v38\n\t"
    "v_min_u32 v39, -2, v39\n\t"
    "global_store_dwordx4 v15, v[36:39], %[od] sc1\n\t"
    "s_barrier\n\t"
    "s_mov_b32 exec_hi, 0\n\t"
    "global_store_dwordx4 v14, v[6:9], %[kv] sc1\n\t"
    "s_mov_b32 exec_hi, -1\n\t"
    "s_nop 0\n\t"
    "s_waitcnt lgkmcnt(0)\n\t"
    "s_branch 9f\n\t"
    "1:\n\t"
    "s_add_u32 %[sp], %[sp], 1\n\t"
    "s_cmp_ge_u32 %[sp], %[cap]\n\t"
    "s_cbranch_scc1 7f\n\t"
    "global_load_dwordx4 v[40:43], v6, %[ag] sc1\n\t"
    "global_load_dwordx4 v[44:47], v6, %[ag] offset:1024 sc1\n\t"
    "s_waitcnt lgkmcnt(0)\n\t"
    "s_memtime %[tc]\n\t"
    "s_nop 7\n\t"
    "s_branch 3b\n\t"
    "7:\n\t"
    "s_mov_b32 %[cap], 0\n\t"
    "s_branch 2b\n\t"
    "4:\n\t"
    "s_add_u32 %[sp], %[sp], 0x10000\n\t"
    "s_lshr_b32 %[st], %[sp], 16\n\t"
    "s_cmp_ge_u32 %[st], %[cap]\n\t"
    "s_cbranch_scc1 8f\n\t"
    "s_mov_b32 exec_hi, 0\n\t"
    "global_load_dwordx4 v[6:9], v14, %[vs] sc1\n\t"
    "s_mov_b32 exec_hi, -1\n\t"
    "s_branch 5b\n\t"
    "8:\n\t"
    "s_mov_b32 %[cap], 0\n\t"
    "s_branch 6b\n\t"
    "10:\n\t"
    "v_max_f32 v11, v11, v10\n\t"
    "v_mov_b32 v26, v11\n\t"
    "s_nop 1\n\t"
    "v_permlane16_swap_b32 v11, v26\n\t"
    "v_max_f32 v11, v11, v26\n\t"
    "v_sub_f32 v28, v40, v11\n\t"
    "v_sub_f32 v29, v41, v11\n\t"
    "v_sub_f32 v30, v42, v11\n\t"
    "v_sub_f32 v31, v43, v11\n\t"
    "v_sub_f32 v11, v10, v11\n\t"
    "v_exp_f32 v11, v11\n\t"
    "v_exp_f32 v28, v28\n\t"
    "v_exp_f32 v29, v29\n\t"
    "v_exp_f32 v30, v30\n\t"
    "v_exp_f32 v31, v31\n\t"
    "v_mul_f32 v44, v44, v11\n\t"
    "v_mul_f32 v45, v45, v11\n\t"
    "v_mul_f32 v46, v46, v11\n\t"
    "v_mul_f32 v47, v47, v11\n\t"
    "v_mul_f32 v48, v48, v11\n\t"
    "v_mul_f32 v49, v49, v11\n\t"
    "v_mul_f32 v50, v50, v11\n\t"
    "v_mul_f32 v51, v51, v11\n\t"
    "v_mul_f32 v52, v52, v11\n\t"
    "v_mul_f32 v53, v53, v11\n\t"
    "v_mul_f32 v54, v54, v11\n\t"
    "v_mul_f32 v55, v55, v11\n\t"
    "s_branch 11b\n\t"
    "12:\n\t"
    "v_max3_f32 v10, v24, v25, v26\n\t"
    "v_max3_f32 v40, v27, v28, v29\n\t"
    "v_max3_f32 v10, v10, v30, v31\n\t"
    "v_max3_f32 v10, v10, v40, %[neg]\n\t"
    "v_mov_b32 v40, v10\n\t"
    "s_nop 1\n\t"
    "v_permlane16_swap_b32 v10, v40\n\t"
    "v_max_f32 v10, v10, v40\n\t"
    "v_mov_b32 v40, v10\n\t"
    "s_nop 1\n\t"
    "v_permlane32_swap_b32 v10, v40\n\t"
    "v_max_f32 v10, v10, v40\n\t"
    "v_sub_f32 v24, v24, v10\n\t"
    "v_sub_f32 v25, v25, v10\n\t"
    "v_sub_f32 v26, v26, v10\n\t"
    "v_sub_f32 v27, v27, v10\n\t"
    "v_sub_f32 v28, v28, v10\n\t"
    "v_sub_f32 v29, v29, v10\n\t"
    "v_sub_f32 v30, v30, v10\n\t"
    "v_sub_f32 v31, v31, v10\n\t"
    "s_mov_b32 %[bg], 0x7fc00000\n\t"
    "s_branch 13b\n\t"
    "9:"
    : [tc] "=&s"(tc), [sp] "=&s"(sp), [cap] "+s"(cap), [st] "=&s"(st), [bg] "=&s"(bg)
    : [ag] "s"(ag), [c44] "s"(0x4444u), [kv] "s"(kv), [vs] "s"(vs), [od] "s"(od), [od2] "s"(od + 262144), [neg] "s"(-1.0e38f), [lsc] "s"(LSC), [one] "s"(0x3F803F80u), [big] "s"(24.0f), "{v[64:95]}"(W.w0), "{v[96:127]}"(W.w1), "{v[128:159]}"(W.w2), "{v[160:191]}"(W.w3), "{v[192:223]}"(W.w4), "{v[224:255]}"(W.w5)
    : "v6", "v7", "v8", "v9", "v10", "v11", "v12", "v13", "v14", "v15", "v16", "v17", "v18", "v19", "v20", "v21", "v22", "v23", "v24", "v25", "v26", "v27", "v28", "v29", "v30", "v31", "v32", "v33", "v34", "v35", "v36", "v37", "v38", "v39", "v40", "v41", "v42", "v43", "v44", "v45", "v46", "v47", "v48", "v49", "v50", "v51", "v52", "v53", "v54", "v55", "v56", "v57", "v58", "v59", "v60", "v61", "v62", "v63", "vcc", "scc", "memory");
}
template <> __device__ __forceinline__ void qk_step<3>(const QKW& W, const char* ag, const char* vs, const char* od, const char* kv, unsigned& cap, unsigned& sp,
                                                        unsigned long long& tc, unsigned long long* ts, unsigned& d0, unsigned& d1) {
  unsigned st, bg;
  asm volatile("v_mbcnt_lo_u32_b32 v60, -1, 0\n\t"
    "v_mbcnt_hi_u32_b32 v60, -1, v60\n\t"
    "v_lshlrev_b32 v6, 4, v60\n\t"
    "s_mov_b32 %[sp], 0\n\t"
    "s_setprio 3\n\t"
    "global_load_dwordx4 v[40:43], v6, %[ag] sc1\n\t"
    "global_load_dwordx4 v[44:47], v6, %[ag] offset:1024 sc1\n\t"
    "s_memtime %[tc]\n\t"
    "v_add_u32 v7, 47104, v6\n\t"
    "v_add_u32 v8, 104448, v6\n\t"
    "ds_read_b128 v[24:27], v7 offset:0\n\t"
    "ds_read_b128 v[28:31], v7 offset:1024\n\t"
    "ds_read_b128 v[32:35], v7 offset:8192\n\t"
    "ds_read_b128 v[36:39], v7 offset:9216\n\t"
    "v_lshrrev_b32 v48, 4, v60\n\t"
    "v_and_b32 v49, 15, v60\n\t"
    "v_and_b32 v61, 4, v60\n\t"
    "v_mul_u32_u24 v61, 0x2aaa, v61\n\t"
    "v_lshrrev_b32 v9, 1, v60\n\t"
    "v_and_b32 v9, 2, v9\n\t"
    "v_or3_b32 v9, v9, v61, %[c44]\n\t"
    "v_add_u32 v14, 1536, v6\n\t"
    "v_lshlrev_b32 v61, 2, v48\n\t"
    "v_lshl_or_b32 v10, v49, 4, v61\n\t"
    "v_lshrrev_b32 v50, 2, v49\n\t"
    "v_and_b32 v51, 3, v60\n\t"
    "v_and_b32 v61, 1, v60\n\t"
    "v_lshlrev_b32 v61, 3, v61\n\t"
    "v_lshl_or_b32 v11, v50, 4, v61\n\t"
    "v_lshl_or_b32 v11, v48, 7, v11\n\t"
    "v_xor_b32 v12, v48, v49\n\t"
    "v_lshlrev_b32 v12, 4, v12\n\t"
    "v_lshl_or_b32 v12, v49, 8, v12\n\t"
    "v_lshl_or_b32 v50, v48, 2, v50\n\t"
    "v_or_b32 v51, 12, v51\n\t"
    "v_xor_b32 v51, v51, v50\n\t"
    "v_lshlrev_b32 v13, 4, v51\n\t"
    "v_lshl_or_b32 v13, v50, 8, v13\n\t"
    "v_and_b32 v61, 3, v60\n\t"
    "v_lshlrev_b32 v61, 4, v61\n\t"
    "v_lshl_or_b32 v15, v48, 6, v61\n\t"
    "v_and_b32 v61, 8, v60\n\t"
    "v_lshl_or_b32 v15, v61, 14, v15\n\t"
    "v_and_b32 v61, 4, v60\n\t"
    "v_lshl_or_b32 v15, v61, 8, v15\n\t"
    "v_add_u32 v15, 768, v15\n\t"
    "3:\n\t"
    "v_mov_b32 v16, 0\n\t"
    "v_mov_b32 v17, 0\n\t"
    "v_mov_b32 v18, 0\n\t"
    "v_mov_b32 v19, 0\n\t"
    "v_mov_b32 v20, 0\n\t"
    "v_mov_b32 v21, 0\n\t"
    "v_mov_b32 v22, 0\n\t"
    "v_mov_b32 v23, 0\n\t"
    "s_waitcnt vmcnt(0) lgkmcnt(0)\n\t"
    "14:\n\t"
    "ds_write_b128 v6, v[40:43] offset:6144\n\t"
    "ds_write_b128 v6, v[44:47] offset:7168\n\t"
    "v_smfmac_f32_16x16x64_bf16 v[16:19], v[40:43], v[160:167], v9\n\t"
    "v_max3_u32 v60, v40, v41, v42\n\t"
    "v_max3_u32 v61, v43, v44, v45\n\t"
    "v_smfmac_f32_16x16x64_bf16 v[20:23], v[40:43], v[168:175], v9\n\t"
    "v_max3_u32 v60, v60, v46, v47\n\t"
    "v_max_u32 v60, v60, v61\n\t"
    "v_cmp_eq_u32 vcc, -1, v60\n\t"
    "s_cbranch_vccnz 1f\n\t"
    "2:\n\t"
    "s_waitcnt lgkmcnt(0)\n\t"
    "s_barrier\n\t"
    "ds_read_b128 v[40:43], v6 offset:12288\n\t"
    "ds_read_b128 v[48:51], v6 offset:0\n\t"
    "v_smfmac_f32_16x16x64_bf16 v[16:19], v[44:47], v[176:183], v9\n\t"
    "ds_read_b128 v[52:55], v6 offset:1024\n\t"
    "ds_read_b128 v[56:59], v6 offset:2048\n\t"
    "v_smfmac_f32_16x16x64_bf16 v[20:23], v[44:47], v[184:191], v9\n\t"
    "ds_read_b128 v[60:63], v6 offset:13312\n\t"
    "ds_read_b128 v[44:47], v6 offset:3072\n\t"
    "s_waitcnt lgkmcnt(5)\n\t"
    "v_smfmac_f32_16x16x64_bf16 v[16:19], v[40:43], v[24:31], v9\n\t"
    "ds_read_b128 v[24:27], v7 offset:16384\n\t"
    "ds_read_b128 v[28:31], v7 offset:17408\n\t"
    "v_smfmac_f32_16x16x64_bf16 v[20:23], v[40:43], v[32:39], v9\n\t"
    "ds_read_b128 v[32:35], v7 offset:24576\n\t"
    "ds_read_b128 v[36:39], v7 offset:25600\n\t"
    "ds_read_b128 v[40:43], v6 offset:4096\n\t"
    "s_waitcnt lgkmcnt(9)\n\t"
    "v_smfmac_f32_16x16x64_bf16 v[16:19], v[48:51], v[64:71], v9\n\t"
    "v_smfmac_f32_16x16x64_bf16 v[20:23], v[48:51], v[72:79], v9\n\t"
    "ds_read_b128 v[48:51], v6 offset:5120\n\t"
    "s_waitcnt lgkmcnt(9)\n\t"
    "v_smfmac_f32_16x16x64_bf16 v[16:19], v[52:55], v[80:87], v9\n\t"
    "v_smfmac_f32_16x16x64_bf16 v[20:23], v[52:55], v[88:95], v9\n\t"
    "ds_read_b128 v[52:55], v6 offset:14336\n\t"
    "s_waitcnt lgkmcnt(9)\n\t"
    "v_smfmac_f32_16x16x64_bf16 v[16:19], v[56:59], v[96:103], v9\n\t"
    "v_smfmac_f32_16x16x64_bf16 v[20:23], v[56:59], v[104:111], v9\n\t"
    "ds_read_b128 v[56:59], v6 offset:8192\n\t"
    "s_waitcnt lgkmcnt(6)\n\t"
    "v_smfmac_f32_16x16x64_bf16 v[16:19], v[60:63], v[24:31], v9\n\t"
    "ds_read_b128 v[24:27], v8 offset:0\n\t"
    "ds_read_b128 v[28:31], v8 offset:1024\n\t"
    "s_waitcnt lgkmcnt(6)\n\t"
    "v_smfmac_f32_16x16x64_bf16 v[20:23], v[60:63], v[32:39], v9\n\t"
    "ds_read_b128 v[32:35], v8 offset:8192\n\t"
    "ds_read_b128 v[36:39], v8 offset:9216\n\t"
    "ds_read_b128 v[60:63], v6 offset:9216\n\t"
    "v_smfmac_f32_16x16x64_bf16 v[16:19], v[44:47], v[112:119], v9\n\t"
    "v_smfmac_f32_16x16x64_bf16 v[20:23], v[44:47], v[120:127], v9\n\t"
    "ds_read_b128 v[44:47], v6 offset:10240\n\t"
    "s_waitcnt lgkmcnt(9)\n\t"
    "v_smfmac_f32_16x16x64_bf16 v[16:19], v[40:43], v[128:135], v9\n\t"
    "v_smfmac_f32_16x16x64_bf16 v[20:23], v[40:43], v[136:143], v9\n\t"
    "ds_read_b128 v[40:43], v6 offset:15360\n\t"
    "s_waitcnt lgkmcnt(9)\n\t"
    "v_smfmac_f32_16x16x64_bf16 v[16:19], v[48:51], v[144:151], v9\n\t"
    "v_smfmac_f32_16x16x64_bf16 v[20:23], v[48:51], v[152:159], v9\n\t"
    "ds_read_b128 v[48:51], v6 offset:11264\n\t"
    "s_waitcnt lgkmcnt(6)\n\t"
    "v_smfmac_f32_16x16x64_bf16 v[16:19], v[52:55], v[24:31], v9\n\t"
    "ds_read_b128 v[24:27], v8 offset:16384\n\t"
    "ds_read_b128 v[28:31], v8 offset:17408\n\t"
    "s_waitcnt lgkmcnt(6)\n\t"
    "v_smfmac_f32_16x16x64_bf16 v[20:23], v[52:55], v[32:39], v9\n\t"
    "ds_read_b128 v[32:35], v8 offset:24576\n\t"
    "ds_read_b128 v[36:39], v8 offset:25600\n\t"
    "v_smfmac_f32_16x16x64_bf16 v[16:19], v[56:59], v[192:199], v9\n\t"
    "v_smfmac_f32_16x16x64_bf16 v[20:23], v[56:59], v[200:207], v9\n\t"
    "s_waitcnt lgkmcnt(7)\n\t"
    "v_smfmac_f32_16x16x64_bf16 v[16:19], v[60:63], v[208:215], v9\n\t"
    "v_smfmac_f32_16x16x64_bf16 v[20:23], v[60:63], v[216:223], v9\n\t"
    "s_waitcnt lgkmcnt(6)\n\t"
    "v_smfmac_f32_16x16x64_bf16 v[16:19], v[44:47], v[224:231], v9\n\t"
    "v_smfmac_f32_16x16x64_bf16 v[20:23], v[44:47], v[232:239], v9\n\t"
    "s_waitcnt lgkmcnt(2)\n\t"
    "v_smfmac_f32_16x16x64_bf16 v[16:19], v[40:43], v[24:31], v9\n\t"
    "s_waitcnt lgkmcnt(0)\n\t"
    "v_smfmac_f32_16x16x64_bf16 v[20:23], v[40:43], v[32:39], v9\n\t"
    "v_smfmac_f32_16x16x64_bf16 v[16:19], v[48:51], v[240:247], v9\n\t"
    "v_smfmac_f32_16x16x64_bf16 v[20:23], v[48:51], v[248:255], v9\n\t"
    "v_xor_b32 v7, 64, v12\n\t"
    "v_xor_b32 v8, 0x80, v12\n\t"
    "v_xor_b32 v6, 0xc0, v12\n\t"
    "ds_read_b128 v[24:27], v12 offset:16384\n\t"
    "ds_read_b128 v[28:31], v12 offset:20480\n\t"
    "ds_read_b128 v[32:35], v7 offset:16384\n\t"
    "ds_read_b128 v[36:39], v7 offset:20480\n\t"
    "v_permlane16_swap_b32 v16, v18\n\t"
    "v_permlane16_swap_b32 v17, v19\n\t"
    "v_permlane16_swap_b32 v20, v22\n\t"
    "v_permlane16_swap_b32 v21, v23\n\t"
    "v_add_f32 v16, v16, v18\n\t"
    "v_add_f32 v17, v17, v19\n\t"
    "v_add_f32 v20, v20, v22\n\t"
    "v_mul_f32 v16, %[lsc], v16\n\t"
    "v_mul_f32 v17, %[lsc], v17\n\t"
    "v_cvt_pk_bf16_f32 v16, v16, v17\n\t"
    "v_add_f32 v21, v21, v23\n\t"
    "ds_write_b32 v10, v16 offset:34304\n\t"
    "v_mul_f32 v20, %[lsc], v20\n\t"
    "v_mul_f32 v21, %[lsc], v21\n\t"
    "v_cvt_pk_bf16_f32 v20, v20, v21\n\t"
    "ds_write_b32 v10, v20 offset:34560\n\t"
    "ds_read_b128 v[40:43], v8 offset:16384\n\t"
    "ds_read_b128 v[44:47], v8 offset:20480\n\t"
    "ds_read_b128 v[48:51], v6 offset:16384\n\t"
    "ds_read_b128 v[52:55], v6 offset:20480\n\t"
    "s_waitcnt lgkmcnt(4)\n\t"
    "s_barrier\n\t"
    "ds_read_b64_tr_b16 v[16:17], v11 offset:32768\n\t"
    "ds_read_b64_tr_b16 v[18:19], v11 offset:32832\n\t"
    "ds_read_b64_tr_b16 v[20:21], v11 offset:33280\n\t"
    "ds_read_b64_tr_b16 v[22:23], v11 offset:33344\n\t"
    "ds_read_b64_tr_b16 v[56:57], v11 offset:33792\n\t"
    "ds_read_b64_tr_b16 v[58:59], v11 offset:33856\n\t"
    "ds_read_b64_tr_b16 v[60:61], v11 offset:34304\n\t"
    "ds_read_b64_tr_b16 v[62:63], v11 offset:34368\n\t"
    "v_mov_b32 v6, 0\n\t"
    "v_mov_b32 v7, 0\n\t"
    "v_mov_b32 v8, 0\n\t"
    "v_mov_b32 v9, 0\n\t"
    "s_mov_b32 exec_hi, 0\n\t"
    "global_load_dwordx4 v[6:9], v14, %[vs] sc1\n\t"
    "s_mov_b32 exec_hi, -1\n\t"
    "s_waitcnt lgkmcnt(6)\n\t"
    "v_mfma_f32_16x16x32_bf16 v[24:27], v[24:27], v[16:19], 0\n\t"
    "v_mfma_f32_16x16x32_bf16 v[28:31], v[28:31], v[16:19], 0\n\t"
    "s_waitcnt lgkmcnt(4)\n\t"
    "v_mfma_f32_16x16x32_bf16 v[24:27], v[32:35], v[20:23], v[24:27]\n\t"
    "v_mfma_f32_16x16x32_bf16 v[28:31], v[36:39], v[20:23], v[28:31]\n\t"
    "ds_read_b64_tr_b16 v[32:33], v13 offset:24576\n\t"
    "ds_read_b64_tr_b16 v[34:35], v13 offset:28672\n\t"
    "ds_read_b64_tr_b16 v[36:37], v13 offset:24584\n\t"
    "ds_read_b64_tr_b16 v[38:39], v13 offset:28680\n\t"
    "s_waitcnt lgkmcnt(6)\n\t"
    "v_mfma_f32_16x16x32_bf16 v[24:27], v[40:43], v[56:59], v[24:27]\n\t"
    "v_mfma_f32_16x16x32_bf16 v[28:31], v[44:47], v[56:59], v[28:31]\n\t"
    "s_waitcnt lgkmcnt(4)\n\t"
    "v_mfma_f32_16x16x32_bf16 v[24:27], v[48:51], v[60:63], v[24:27]\n\t"
    "v_mfma_f32_16x16x32_bf16 v[28:31], v[52:55], v[60:63], v[28:31]\n\t"
    "s_setprio 1\n\t"
    "v_mov_b32 v52, %[one]\n\t"
    "v_mov_b32 v53, %[one]\n\t"
    "v_mov_b32 v54, %[one]\n\t"
    "v_mov_b32 v55, %[one]\n\t"
    "s_nop 2\n\t"
    "v_max3_f32 v10, |v24|, |v25|, |v26|\n\t"
    "v_max3_f32 v40, |v27|, |v28|, |v29|\n\t"
    "v_max3_f32 v10, v10, |v30|, |v31|\n\t"
    "v_max_f32 v10, v10, v40\n\t"
    "v_cmp_nge_f32 vcc, %[big], v10\n\t"
    "s_mov_b32 %[bg], %[big]\n\t"
    "v_mov_b32 v10, 0\n\t"
    "s_cbranch_vccnz 12f\n\t"
    "13:\n\t"
    "v_exp_f32 v24, v24\n\t"
    "v_exp_f32 v25, v25\n\t"
    "v_exp_f32 v26, v26\n\t"
    "v_exp_f32 v27, v27\n\t"
    "v_exp_f32 v28, v28\n\t"
    "v_exp_f32 v29, v29\n\t"
    "v_exp_f32 v30, v30\n\t"
    "v_exp_f32 v31, v31\n\t"
    "v_cvt_pk_bf16_f32 v40, v24, v25\n\t"
    "v_cvt_pk_bf16_f32 v41, v26, v27\n\t"
    "v_cvt_pk_bf16_f32 v42, v28, v29\n\t"
    "v_cvt_pk_bf16_f32 v43, v30, v31\n\t"
    "s_setprio 3\n\t"
    "s_waitcnt lgkmcnt(0)\n\t"
    "s_nop 0\n\t"
    "v_mfma_f32_16x16x32_bf16 v[44:47], v[32:35], v[40:43], 0\n\t"
    "v_mfma_f32_16x16x32_bf16 v[48:51], v[36:39], v[40:43], 0\n\t"
    "v_mfma_f32_16x16x32_bf16 v[52:55], v[52:55], v[40:43], 0\n\t"
    "v_mov_b32 v12, 0\n\t"
    "v_mov_b32 v13, 0\n\t"
    "s_mov_b32 exec_hi, 0\n\t"
    "v_mov_b32 v12, %[one]\n\t"
    "v_mov_b32 v13, %[one]\n\t"
    "s_mov_b32 exec_hi, -1\n\t"
    "s_barrier\n\t"
    "ds_read_b64_tr_b16 v[24:25], v11 offset:34816\n\t"
    "ds_read_b64_tr_b16 v[26:27], v11 offset:34880\n\t"
    "ds_read_b64_tr_b16 v[28:29], v11 offset:35328\n\t"
    "ds_read_b64_tr_b16 v[30:31], v11 offset:35392\n\t"
    "ds_read_b64_tr_b16 v[32:33], v11 offset:35840\n\t"
    "ds_read_b64_tr_b16 v[34:35], v11 offset:35904\n\t"
    "ds_read_b64_tr_b16 v[36:37], v11 offset:36352\n\t"
    "ds_read_b64_tr_b16 v[38:39], v11 offset:36416\n\t"
    "s_waitcnt lgkmcnt(6)\n\t"
    "v_mfma_f32_16x16x32_bf16 v[40:43], v[24:27], v[16:19], 0\n\t"
    "s_waitcnt lgkmcnt(4)\n\t"
    "v_mfma_f32_16x16x32_bf16 v[40:43], v[28:31], v[20:23], v[40:43]\n\t"
    "s_waitcnt lgkmcnt(2)\n\t"
    "v_mfma_f32_16x16x32_bf16 v[40:43], v[32:35], v[56:59], v[40:43]\n\t"
    "s_waitcnt lgkmcnt(0)\n\t"
    "v_mfma_f32_16x16x32_bf16 v[40:43], v[36:39], v[60:63], v[40:43]\n\t"
    "5:\n\t"
    "s_waitcnt vmcnt(0)\n\t"
    "v_max3_u32 v11, v6, v7, v8\n\t"
    "v_max_u32 v11, v11, v9\n\t"
    "v_cmp_eq_u32 vcc, -1, v11\n\t"
    "s_cbranch_vccnz 4f\n\t"
    "6:\n\t"
    "s_nop 2\n\t"
    "v_exp_f32 v28, v40\n\t"
    "v_max3_f32 v11, v40, v41, v42\n\t"
    "v_exp_f32 v29, v41\n\t"
    "v_max_f32 v11, v11, v43\n\t"
    "v_exp_f32 v30, v42\n\t"
    "v_cmp_nge_f32 vcc, %[bg], v11\n\t"
    "v_exp_f32 v31, v43\n\t"
    "s_cbranch_vccnz 10f\n\t"
    "11:\n\t"
    "v_cvt_pk_bf16_f32 v24, v28, v29\n\t"
    "v_cvt_pk_bf16_f32 v25, v30, v31\n\t"
    "s_nop 1\n\t"
    "v_mfma_f32_16x16x16_bf16 v[52:55], v[12:13], v[24:25], v[52:55]\n\t"
    "v_mfma_f32_16x16x16_bf16 v[44:47], v[6:7], v[24:25], v[44:47]\n\t"
    "v_mfma_f32_16x16x16_bf16 v[48:51], v[8:9], v[24:25], v[48:51]\n\t"
    "s_nop 5\n\t"
    "v_rcp_f32 v26, v52\n\t"
    "s_nop 0\n\t"
    "v_pk_mul_f32 v[44:45], v[44:45], v[26:27] op_sel_hi:[1,0]\n\t"
    "v_pk_mul_f32 v[46:47], v[46:47], v[26:27] op_sel_hi:[1,0]\n\t"
    "v_pk_mul_f32 v[48:49], v[48:49], v[26:27] op_sel_hi:[1,0]\n\t"
    "v_pk_mul_f32 v[50:51], v[50:51], v[26:27] op_sel_hi:[1,0]\n\t"
    "v_cvt_pk_bf16_f32 v36, v44, v45\n\t"
    "v_cvt_pk_bf16_f32 v37, v46, v47\n\t"
    "v_cvt_pk_bf16_f32 v38, v48, v49\n\t"
    "v_cvt_pk_bf16_f32 v39, v50, v51\n\t"
    "v_min_u32 v36, -2, v36\n\t"
    "v_min_u32 v37, -2, v37\n\t"
    "v_min_u32 v38, -2, v38\n\t"
    "v_min_u32 v39, -2, v39\n\t"
    "global_store_dwordx4 v15, v[36:39], %[od] sc1\n\t"
    "s_barrier\n\t"
    "s_mov_b32 exec_hi, 0\n\t"
    "global_store_dwordx4 v14, v[6:9], %[kv] sc1\n\t"
    "s_mov_b32 exec_hi, -1\n\t"
    "s_nop 0\n\t"
    "s_waitcnt lgkmcnt(0)\n\t"
    "s_branch 9f\n\t"
    "1:\n\t"
    "s_add_u32 %[sp], %[sp], 1\n\t"
    "s_cmp_ge_u32 %[sp], %[cap]\n\t"
    "s_cbranch_scc1 7f\n\t"
    "global_load_dwordx4 v[40:43], v6, %[ag] sc1\n\t"
    "global_load_dwordx4 v[44:47], v6, %[ag] offset:1024 sc1\n\t"
    "s_waitcnt lgkmcnt(0)\n\t"
    "s_memtime %[tc]\n\t"
    "s_nop 7\n\t"
    "s_branch 3b\n\t"
    "7:\n\t"
    "s_mov_b32 %[cap], 0\n\t"
    "s_branch 2b\n\t"
    "4:\n\t"
    "s_add_u32 %[sp], %[sp], 0x10000\n\t"
    "s_lshr_b32 %[st], %[sp], 16\n\t"
    "s_cmp_ge_u32 %[st], %[cap]\n\t"
    "s_cbranch_scc1 8f\n\t"
    "s_mov_b32 exec_hi, 0\n\t"
    "global_load_dwordx4 v[6:9], v14, %[vs] sc1\n\t"
    "s_mov_b32 exec_hi, -1\n\t"
    "s_branch 5b\n\t"
    "8:\n\t"
    "s_mov_b32 %[cap], 0\n\t"
    "s_branch 6b\n\t"
    "10:\n\t"
    "v_max_f32 v11, v11, v10\n\t"
    "v_mov_b32 v26, v11\n\t"
    "s_nop 1\n\t"
    "v_permlane16_swap_b32 v11, v26\n\t"
    "v_max_f32 v11, v11, v26\n\t"
    "v_sub_f32 v28, v40, v11\n\t"
    "v_sub_f32 v29, v41, v11\n\t"
    "v_sub_f32 v30, v42, v11\n\t"
    "v_sub_f32 v31, v43, v11\n\t"
    "v_sub_f32 v11, v10, v11\n\t"
    "v_exp_f32 v11, v11\n\t"
    "v_exp_f32 v28, v28\n\t"
    "v_exp_f32 v29, v29\n\t"
    "v_exp_f32 v30, v30\n\t"
    "v_exp_f32 v31, v31\n\t"
    "v_mul_f32 v44, v44, v11\n\t"
    "v_mul_f32 v45, v45, v11\n\t"
    "v_mul_f32 v46, v46, v11\n\t"
    "v_mul_f32 v47, v47, v11\n\t"
    "v_mul_f32 v48, v48, v11\n\t"
    "v_mul_f32 v49, v49, v11\n\t"
    "v_mul_f32 v50, v50, v11\n\t"
    "v_mul_f32 v51, v51, v11\n\t"
    "v_mul_f32 v52, v52, v11\n\t"
    "v_mul_f32 v53, v53, v11\n\t"
    "v_mul_f32 v54, v54, v11\n\t"
    "v_mul_f32 v55, v55, v11\n\t"
    "s_branch 11b\n\t"
    "12:\n\t"
    "v_max3_f32 v10, v24, v25, v26\n\t"
    "v_max3_f32 v40, v27, v28, v29\n\t"
    "v_max3_f32 v10, v10, v30, v31\n\t"
    "v_max3_f32 v10, v10, v40, %[neg]\n\t"
    "v_mov_b32 v40, v10\n\t"
    "s_nop 1\n\t"
    "v_permlane16_swap_b32 v10, v40\n\t"
    "v_max_f32 v10, v10, v40\n\t"
    "v_mov_b32 v40, v10\n\t"
    "s_nop 1\n\t"
    "v_permlane32_swap_b32 v10, v40\n\t"
    "v_max_f32 v10, v10, v40\n\t"
    "v_sub_f32 v24, v24, v10\n\t"
    "v_sub_f32 v25, v25, v10\n\t"
    "v_sub_f32 v26, v26, v10\n\t"
    "v_sub_f32 v27, v27, v10\n\t"
    "v_sub_f32 v28, v28, v10\n\t"
    "v_sub_f32 v29, v29, v10\n\t"
    "v_sub_f32 v30, v30, v10\n\t"
    "v_sub_f32 v31, v31, v10\n\t"
    "s_mov_b32 %[bg], 0x7fc00000\n\t"
    "s_branch 13b\n\t"
    "9:"
    : [tc] "=&s"(tc), [sp] "=&s"(sp), [cap] "+s"(cap), [st] "=&s"(st), [bg] "=&s"(bg)
    : [ag] "s"(ag), [c44] "s"(0x4444u), [kv] "s"(kv), [vs] "s"(vs), [od] "s"(od), [od2] "s"(od + 262144), [neg] "s"(-1.0e38f), [lsc] "s"(LSC), [one] "s"(0x3F803F80u), [big] "s"(24.0f), "{v[64:95]}"(W.w0), "{v[96:127]}"(W.w1), "{v[128:159]}"(W.w2), "{v[160:191]}"(W.w3), "{v[192:223]}"(W.w4), "{v[224:255]}"(W.w5)
    : "v6", "v7", "v8", "v9", "v10", "v11", "v12", "v13", "v14", "v15", "v16", "v17", "v18", "v19", "v20", "v21", "v22", "v23", "v24", "v25", "v26", "v27", "v28", "v29", "v30", "v31", "v32", "v33", "v34", "v35", "v36", "v37", "v38", "v39", "v40", "v41", "v42", "v43", "v44", "v45", "v46", "v47", "v48", "v49", "v50", "v51", "v52", "v53", "v54", "v55", "v56", "v57", "v58", "v59", "v60", "v61", "v62", "v63", "vcc", "scc", "memory");
}
template <> __device__ __forceinline__ void qk_step<4>(const QKW& W, const char* ag, const char* vs, const char* od, const char* kv, unsigned& cap, unsigned& sp,
                                                        unsigned long long& tc, unsigned long long* ts, unsigned& d0, unsigned& d1) {
  unsigned st, bg;
  asm volatile("v_mbcnt_lo_u32_b32 v52, -1, 0\n\t"
    "v_mbcnt_hi_u32_b32 v52, -1, v52\n\t"
    "v_lshlrev_b32 v6, 4, v52\n\t"
    "s_mov_b32 %[sp], 0\n\t"
    "global_load_dwordx4 v[36:39], v6, %[ag] sc1\n\t"
    "global_load_dwordx4 v[40:43], v6, %[ag] offset:1024 sc1\n\t"
    "s_memtime %[tc]\n\t"
    "v_add_u32 v7, 73728, v6\n\t"
    "v_add_u32 v8, 131072, v6\n\t"
    "ds_read_b128 v[20:23], v7 offset:8192\n\t"
    "ds_read_b128 v[24:27], v7 offset:9216\n\t"
    "ds_read_b128 v[28:31], v7 offset:0\n\t"
    "ds_read_b128 v[32:35], v7 offset:1024\n\t"
    "v_lshrrev_b32 v44, 4, v52\n\t"
    "v_and_b32 v45, 15, v52\n\t"
    "v_and_b32 v53, 4, v52\n\t"
    "v_mul_u32_u24 v53, 0x2aaa, v53\n\t"
    "v_lshrrev_b32 v9, 1, v52\n\t"
    "v_and_b32 v9, 2, v9\n\t"
    "v_or3_b32 v9, v9, v53, %[c44]\n\t"
    "v_lshlrev_b32 v53, 2, v44\n\t"
    "v_lshl_or_b32 v10, v45, 4, v53\n\t"
    "3:\n\t"
    "v_mov_b32 v12, 0\n\t"
    "v_mov_b32 v13, 0\n\t"
    "v_mov_b32 v14, 0\n\t"
    "v_mov_b32 v15, 0\n\t"
    "v_mov_b32 v16, 0\n\t"
    "v_mov_b32 v17, 0\n\t"
    "v_mov_b32 v18, 0\n\t"
    "v_mov_b32 v19, 0\n\t"
    "s_waitcnt vmcnt(0) lgkmcnt(0)\n\t"
    "14:\n\t"
    "ds_write_b128 v6, v[36:39] offset:8192\n\t"
    "ds_write_b128 v6, v[40:43] offset:9216\n\t"
    "v_smfmac_f32_16x16x64_bf16 v[12:15], v[36:39], v[128:135], v9\n\t"
    "v_max3_u32 v52, v36, v37, v38\n\t"
    "v_max3_u32 v53, v39, v40, v41\n\t"
    "v_smfmac_f32_16x16x64_bf16 v[16:19], v[36:39], v[136:143], v9\n\t"
    "v_max3_u32 v52, v52, v42, v43\n\t"
    "v_max_u32 v52, v52, v53\n\t"
    "v_cmp_eq_u32 vcc, -1, v52\n\t"
    "s_cbranch_vccnz 1f\n\t"
    "2:\n\t"
    "s_waitcnt lgkmcnt(0)\n\t"
    "s_barrier\n\t"
    "s_memrealtime s[36:37]\n\t"
    "ds_read_b128 v[36:39], v6 offset:0\n\t"
    "ds_read_b128 v[44:47], v6 offset:4096\n\t"
    "v_smfmac_f32_16x16x64_bf16 v[12:15], v[40:43], v[144:151], v9\n\t"
    "ds_read_b128 v[48:51], v6 offset:5120\n\t"
    "ds_read_b128 v[52:55], v6 offset:6144\n\t"
    "v_smfmac_f32_16x16x64_bf16 v[16:19], v[40:43], v[152:159], v9\n\t"
    "ds_read_b128 v[40:43], v6 offset:1024\n\t"
    "s_waitcnt lgkmcnt(0)\n\t"
    "s_mov_b32 s38, s36\n\t"
    "s_min_u32 s37, %[d0], %[d1]\n\t"
    "s_add_u32 s36, s36, s37\n\t"
    "s_and_b32 s36, s36, 0xffffff\n\t"
    "s_or_b32 s36, s36, %[sn]\n\t"
    "v_lshlrev_b32 v11, 10, v6\n\t"
    "v_mov_b32 v5, s36\n\t"
    "s_mov_b64 exec, 15\n\t"
    "global_store_dword v11, v5, %[vs] offset:-4096 sc1\n\t"
    "s_mov_b64 exec, -1\n\t"
    "s_sleep 0\n\t"
    "v_smfmac_f32_16x16x64_bf16 v[12:15], v[36:39], v[56:63], v9\n\t"
    "v_smfmac_f32_16x16x64_bf16 v[16:19], v[36:39], v[28:35], v9\n\t"
    "ds_read_b128 v[28:31], v7 offset:16384\n\t"
    "ds_read_b128 v[32:35], v7 offset:17408\n\t"
    "ds_read_b128 v[36:39], v6 offset:7168\n\t"
    "v_smfmac_f32_16x16x64_bf16 v[12:15], v[44:47], v[64:71], v9\n\t"
    "v_smfmac_f32_16x16x64_bf16 v[16:19], v[44:47], v[72:79], v9\n\t"
    "ds_read_b128 v[44:47], v6 offset:10240\n\t"
    "v_smfmac_f32_16x16x64_bf16 v[12:15], v[48:51], v[80:87], v9\n\t"
    "v_smfmac_f32_16x16x64_bf16 v[16:19], v[48:51], v[88:95], v9\n\t"
    "ds_read_b128 v[48:51], v6 offset:11264\n\t"
    "v_smfmac_f32_16x16x64_bf16 v[12:15], v[52:55], v[96:103], v9\n\t"
    "v_smfmac_f32_16x16x64_bf16 v[16:19], v[52:55], v[104:111], v9\n\t"
    "ds_read_b128 v[52:55], v6 offset:2048\n\t"
    "v_smfmac_f32_16x16x64_bf16 v[12:15], v[40:43], v[20:27], v9\n\t"
    "ds_read_b128 v[20:23], v8 offset:0\n\t"
    "ds_read_b128 v[24:27], v8 offset:1024\n\t"
    "s_waitcnt lgkmcnt(6)\n\t"
    "v_smfmac_f32_16x16x64_bf16 v[16:19], v[40:43], v[28:35], v9\n\t"
    "ds_read_b128 v[28:31], v8 offset:8192\n\t"
    "ds_read_b128 v[32:35], v8 offset:9216\n\t"
    "ds_read_b128 v[40:43], v6 offset:12288\n\t"
    "s_waitcnt lgkmcnt(8)\n\t"
    "v_smfmac_f32_16x16x64_bf16 v[12:15], v[36:39], v[112:119], v9\n\t"
    "v_smfmac_f32_16x16x64_bf16 v[16:19], v[36:39], v[120:127], v9\n\t"
    "ds_read_b128 v[36:39], v6 offset:13312\n\t"
    "s_waitcnt lgkmcnt(8)\n\t"
    "v_smfmac_f32_16x16x64_bf16 v[12:15], v[44:47], v[160:167], v9\n\t"
    "v_smfmac_f32_16x16x64_bf16 v[16:19], v[44:47], v[168:175], v9\n\t"
    "ds_read_b128 v[44:47], v6 offset:14336\n\t"
    "s_barrier\n\t"
    "s_setprio 2\n\t"
    "s_waitcnt lgkmcnt(8)\n\t"
    "v_smfmac_f32_16x16x64_bf16 v[12:15], v[48:51], v[176:183], v9\n\t"
    "v_smfmac_f32_16x16x64_bf16 v[16:19], v[48:51], v[184:191], v9\n\t"
    "ds_read_b128 v[48:51], v6 offset:3072\n\t"
    "s_waitcnt lgkmcnt(6)\n\t"
    "v_smfmac_f32_16x16x64_bf16 v[12:15], v[52:55], v[20:27], v9\n\t"
    "ds_read_b128 v[20:23], v8 offset:16384\n\t"
    "ds_read_b128 v[24:27], v8 offset:17408\n\t"
    "s_waitcnt lgkmcnt(6)\n\t"
    "v_smfmac_f32_16x16x64_bf16 v[16:19], v[52:55], v[28:35], v9\n\t"
    "ds_read_b128 v[28:31], v8 offset:24576\n\t"
    "ds_read_b128 v[32:35], v8 offset:25600\n\t"
    "ds_read_b128 v[52:55], v6 offset:15360\n\t"
    "s_waitcnt lgkmcnt(8)\n\t"
    "v_smfmac_f32_16x16x64_bf16 v[12:15], v[40:43], v[192:199], v9\n\t"
    "v_smfmac_f32_16x16x64_bf16 v[16:19], v[40:43], v[200:207], v9\n\t"
    "s_waitcnt lgkmcnt(7)\n\t"
    "v_smfmac_f32_16x16x64_bf16 v[12:15], v[36:39], v[208:215], v9\n\t"
    "v_smfmac_f32_16x16x64_bf16 v[16:19], v[36:39], v[216:223], v9\n\t"
    "s_waitcnt lgkmcnt(6)\n\t"
    "v_smfmac_f32_16x16x64_bf16 v[12:15], v[44:47], v[224:231], v9\n\t"
    "v_smfmac_f32_16x16x64_bf16 v[16:19], v[44:47], v[232:239], v9\n\t"
    "s_waitcnt lgkmcnt(3)\n\t"
    "v_smfmac_f32_16x16x64_bf16 v[12:15], v[48:51], v[20:27], v9\n\t"
    "s_waitcnt lgkmcnt(1)\n\t"
    "v_smfmac_f32_16x16x64_bf16 v[16:19], v[48:51], v[28:35], v9\n\t"
    "s_waitcnt lgkmcnt(0)\n\t"
    "v_smfmac_f32_16x16x64_bf16 v[12:15], v[52:55], v[240:247], v9\n\t"
    "v_smfmac_f32_16x16x64_bf16 v[16:19], v[52:55], v[248:255], v9\n\t"
    "s_nop 6\n\t"
    "v_permlane16_swap_b32 v12, v14\n\t"
    "v_permlane16_swap_b32 v13, v15\n\t"
    "v_permlane16_swap_b32 v16, v18\n\t"
    "v_permlane16_swap_b32 v17, v19\n\t"
    "v_add_f32 v12, v12, v14\n\t"
    "v_add_f32 v13, v13, v15\n\t"
    "v_add_f32 v16, v16, v18\n\t"
    "v_cvt_pk_bf16_f32 v12, v12, v13\n\t"
    "v_add_f32 v17, v17, v19\n\t"
    "ds_write_b32 v10, v12 offset:34816\n\t"
    "v_cvt_pk_bf16_f32 v16, v16, v17\n\t"
    "ds_write_b32 v10, v16 offset:35072\n\t"
    "s_waitcnt lgkmcnt(0)\n\t"
    "s_setprio 0\n\t"
    "s_barrier\n\t"
    "s_memrealtime s[36:37]\n\t"
    "s_waitcnt lgkmcnt(0)\n\t"
    "s_and_b32 s36, s36, 0xffffff\n\t"
    "s_or_b32 s36, s36, %[sn]\n\t"
    "v_lshlrev_b32 v7, 10, v6\n\t"
    "v_mov_b32 v8, s36\n\t"
    "s_mov_b64 exec, 15\n\t"
    "global_store_dword v7, v8, %[vs] offset:0 sc1\n\t"
    "s_mov_b64 exec, -1\n\t"
    "s_barrier\n\t"
    "s_memrealtime s[36:37]\n\t"
    "s_mov_b32 %[d1], %[d0]\n\t"
    "s_waitcnt lgkmcnt(0)\n\t"
    "s_sub_u32 %[d0], s36, s38\n\t"
    "s_min_u32 %[d0], %[d0], 200\n\t"
    "s_waitcnt lgkmcnt(0)\n\t"
    "s_branch 9f\n\t"
    "1:\n\t"
    "s_add_u32 %[sp], %[sp], 1\n\t"
    "s_cmp_ge_u32 %[sp], %[cap]\n\t"
    "s_cbranch_scc1 7f\n\t"
    "global_load_dwordx4 v[36:39], v6, %[ag] sc1\n\t"
    "global_load_dwordx4 v[40:43], v6, %[ag] offset:1024 sc1\n\t"
    "s_waitcnt lgkmcnt(0)\n\t"
    "s_memtime %[tc]\n\t"
    "s_nop 7\n\t"
    "s_branch 3b\n\t"
    "7:\n\t"
    "s_mov_b32 %[cap], 0\n\t"
    "s_branch 2b\n\t"
    "9:"
    : [tc] "=&s"(tc), [sp] "=&s"(sp), [cap] "+s"(cap), [d0] "+s"(d0), [d1] "+s"(d1)
    : [ag] "s"(ag), [c44] "s"(0x4444u), [vs] "s"(vs), [sn] "s"((unsigned)(size_t)od), "{v[64:95]}"(W.w0), "{v[96:127]}"(W.w1), "{v[128:159]}"(W.w2), "{v[160:191]}"(W.w3), "{v[192:223]}"(W.w4), "{v[224:255]}"(W.w5), "{v[56:63]}"(W.x)
    : "v5", "v6", "v7", "v8", "v9", "v10", "v11", "v12", "v13", "v14", "v15", "v16", "v17", "v18", "v19", "v20", "v21", "v22", "v23", "v24", "v25", "v26", "v27", "v28", "v29", "v30", "v31", "v32", "v33", "v34", "v35", "v36", "v37", "v38", "v39", "v40", "v41", "v42", "v43", "v44", "v45", "v46", "v47", "v48", "v49", "v50", "v51", "v52", "v53", "v54", "v55", "vcc", "scc", "memory", "s36", "s37", "s38");
}
template <> __device__ __forceinline__ void qk_step<5>(const QKW& W, const char* ag, const char* vs, const char* od, const char* kv, unsigned& cap, unsigned& sp,
                                                        unsigned long long& tc, unsigned long long* ts, unsigned& d0, unsigned& d1) {
  unsigned st, bg;
  asm volatile("v_mbcnt_lo_u32_b32 v52, -1, 0\n\t"
    "v_mbcnt_hi_u32_b32 v52, -1, v52\n\t"
    "v_lshlrev_b32 v6, 4, v52\n\t"
    "s_mov_b32 %[sp], 0\n\t"
    "global_load_dwordx4 v[36:39], v6, %[ag] sc1\n\t"
    "global_load_dwordx4 v[40:43], v6, %[ag] offset:1024 sc1\n\t"
    "s_memtime %[tc]\n\t"
    "v_add_u32 v7, 75776, v6\n\t"
    "v_add_u32 v8, 133120, v6\n\t"
    "ds_read_b128 v[20:23], v7 offset:8192\n\t"
    "ds_read_b128 v[24:27], v7 offset:9216\n\t"
    "ds_read_b128 v[28:31], v7 offset:0\n\t"
    "ds_read_b128 v[32:35], v7 offset:1024\n\t"
    "v_lshrrev_b32 v44, 4, v52\n\t"
    "v_and_b32 v45, 15, v52\n\t"
    "v_and_b32 v53, 4, v52\n\t"
    "v_mul_u32_u24 v53, 0x2aaa, v53\n\t"
    "v_lshrrev_b32 v9, 1, v52\n\t"
    "v_and_b32 v9, 2, v9\n\t"
    "v_or3_b32 v9, v9, v53, %[c44]\n\t"
    "v_lshlrev_b32 v53, 2, v44\n\t"
    "v_lshl_or_b32 v10, v45, 4, v53\n\t"
    "v_and_b32 v53, 1, v52\n\t"
    "v_lshlrev_b32 v53, 3, v53\n\t"
    "v_lshl_or_b32 v11, v44, 7, v53\n\t"
    "v_and_b32 v53, 12, v52\n\t"
    "v_lshl_or_b32 v11, v53, 2, v11\n\t"
    "v_and_b32 v53, 2, v52\n\t"
    "v_lshl_or_b32 v11, v53, 8, v11\n\t"
    "3:\n\t"
    "v_mov_b32 v12, 0\n\t"
    "v_mov_b32 v13, 0\n\t"
    "v_mov_b32 v14, 0\n\t"
    "v_mov_b32 v15, 0\n\t"
    "v_mov_b32 v16, 0\n\t"
    "v_mov_b32 v17, 0\n\t"
    "v_mov_b32 v18, 0\n\t"
    "v_mov_b32 v19, 0\n\t"
    "s_waitcnt vmcnt(0) lgkmcnt(0)\n\t"
    "14:\n\t"
    "ds_write_b128 v6, v[36:39] offset:10240\n\t"
    "ds_write_b128 v6, v[40:43] offset:11264\n\t"
    "v_smfmac_f32_16x16x64_bf16 v[12:15], v[36:39], v[160:167], v9\n\t"
    "v_max3_u32 v52, v36, v37, v38\n\t"
    "v_max3_u32 v53, v39, v40, v41\n\t"
    "v_smfmac_f32_16x16x64_bf16 v[16:19], v[36:39], v[168:175], v9\n\t"
    "v_max3_u32 v52, v52, v42, v43\n\t"
    "v_max_u32 v52, v52, v53\n\t"
    "v_cmp_eq_u32 vcc, -1, v52\n\t"
    "s_cbranch_vccnz 1f\n\t"
    "2:\n\t"
    "s_waitcnt lgkmcnt(0)\n\t"
    "s_barrier\n\t"
    "ds_read_b128 v[36:39], v6 offset:0\n\t"
    "ds_read_b128 v[44:47], v6 offset:4096\n\t"
    "v_smfmac_f32_16x16x64_bf16 v[12:15], v[40:43], v[176:183], v9\n\t"
    "ds_read_b128 v[48:51], v6 offset:5120\n\t"
    "ds_read_b128 v[52:55], v6 offset:6144\n\t"
    "v_smfmac_f32_16x16x64_bf16 v[16:19], v[40:43], v[184:191], v9\n\t"
    "ds_read_b128 v[40:43], v6 offset:1024\n\t"
    "s_sleep 0\n\t"
    "s_waitcnt lgkmcnt(4)\n\t"
    "v_smfmac_f32_16x16x64_bf16 v[12:15], v[36:39], v[56:63], v9\n\t"
    "v_smfmac_f32_16x16x64_bf16 v[16:19], v[36:39], v[28:35], v9\n\t"
    "ds_read_b128 v[28:31], v7 offset:16384\n\t"
    "ds_read_b128 v[32:35], v7 offset:17408\n\t"
    "ds_read_b128 v[36:39], v6 offset:7168\n\t"
    "s_waitcnt lgkmcnt(6)\n\t"
    "v_smfmac_f32_16x16x64_bf16 v[12:15], v[44:47], v[64:71], v9\n\t"
    "v_smfmac_f32_16x16x64_bf16 v[16:19], v[44:47], v[72:79], v9\n\t"
    "ds_read_b128 v[44:47], v6 offset:8192\n\t"
    "s_waitcnt lgkmcnt(6)\n\t"
    "v_smfmac_f32_16x16x64_bf16 v[12:15], v[48:51], v[80:87], v9\n\t"
    "v_smfmac_f32_16x16x64_bf16 v[16:19], v[48:51], v[88:95], v9\n\t"
    "ds_read_b128 v[48:51], v6 offset:9216\n\t"
    "s_waitcnt lgkmcnt(6)\n\t"
    "v_smfmac_f32_16x16x64_bf16 v[12:15], v[52:55], v[96:103], v9\n\t"
    "v_smfmac_f32_16x16x64_bf16 v[16:19], v[52:55], v[104:111], v9\n\t"
    "ds_read_b128 v[52:55], v6 offset:2048\n\t"
    "s_waitcnt lgkmcnt(6)\n\t"
    "v_smfmac_f32_16x16x64_bf16 v[12:15], v[40:43], v[20:27], v9\n\t"
    "ds_read_b128 v[20:23], v8 offset:0\n\t"
    "ds_read_b128 v[24:27], v8 offset:1024\n\t"
    "s_waitcnt lgkmcnt(6)\n\t"
    "v_smfmac_f32_16x16x64_bf16 v[16:19], v[40:43], v[28:35], v9\n\t"
    "ds_read_b128 v[28:31], v8 offset:8192\n\t"
    "ds_read_b128 v[32:35], v8 offset:9216\n\t"
    "ds_read_b128 v[40:43], v6 offset:12288\n\t"
    "s_waitcnt lgkmcnt(8)\n\t"
    "v_smfmac_f32_16x16x64_bf16 v[12:15], v[36:39], v[112:119], v9\n\t"
    "v_smfmac_f32_16x16x64_bf16 v[16:19], v[36:39], v[120:127], v9\n\t"
    "ds_read_b128 v[36:39], v6 offset:13312\n\t"
    "s_waitcnt lgkmcnt(8)\n\t"
    "v_smfmac_f32_16x16x64_bf16 v[12:15], v[44:47], v[128:135], v9\n\t"
    "v_smfmac_f32_16x16x64_bf16 v[16:19], v[44:47], v[136:143], v9\n\t"
    "ds_read_b128 v[44:47], v6 offset:14336\n\t"
    "s_barrier\n\t"
    "s_setprio 2\n\t"
    "s_waitcnt lgkmcnt(8)\n\t"
    "v_smfmac_f32_16x16x64_bf16 v[12:15], v[48:51], v[144:151], v9\n\t"
    "v_smfmac_f32_16x16x64_bf16 v[16:19], v[48:51], v[152:159], v9\n\t"
    "ds_read_b128 v[48:51], v6 offset:3072\n\t"
    "s_waitcnt lgkmcnt(6)\n\t"
    "v_smfmac_f32_16x16x64_bf16 v[12:15], v[52:55], v[20:27], v9\n\t"
    "ds_read_b128 v[20:23], v8 offset:16384\n\t"
    "ds_read_b128 v[24:27], v8 offset:17408\n\t"
    "s_waitcnt lgkmcnt(6)\n\t"
    "v_smfmac_f32_16x16x64_bf16 v[16:19], v[52:55], v[28:35], v9\n\t"
    "ds_read_b128 v[28:31], v8 offset:24576\n\t"
    "ds_read_b128 v[32:35], v8 offset:25600\n\t"
    "ds_read_b128 v[52:55], v6 offset:15360\n\t"
    "s_waitcnt lgkmcnt(8)\n\t"
    "v_smfmac_f32_16x16x64_bf16 v[12:15], v[40:43], v[192:199], v9\n\t"
    "v_smfmac_f32_16x16x64_bf16 v[16:19], v[40:43], v[200:207], v9\n\t"
    "s_waitcnt lgkmcnt(7)\n\t"
    "v_smfmac_f32_16x16x64_bf16 v[12:15], v[36:39], v[208:215], v9\n\t"
    "v_smfmac_f32_16x16x64_bf16 v[16:19], v[36:39], v[216:223], v9\n\t"
    "s_waitcnt lgkmcnt(6)\n\t"
    "v_smfmac_f32_16x16x64_bf16 v[12:15], v[44:47], v[224:231], v9\n\t"
    "v_smfmac_f32_16x16x64_bf16 v[16:19], v[44:47], v[232:239], v9\n\t"
    "s_waitcnt lgkmcnt(3)\n\t"
    "v_smfmac_f32_16x16x64_bf16 v[12:15], v[48:51], v[20:27], v9\n\t"
    "s_waitcnt lgkmcnt(1)\n\t"
    "v_smfmac_f32_16x16x64_bf16 v[16:19], v[48:51], v[28:35], v9\n\t"
    "s_waitcnt lgkmcnt(0)\n\t"
    "v_smfmac_f32_16x16x64_bf16 v[12:15], v[52:55], v[240:247], v9\n\t"
    "v_smfmac_f32_16x16x64_bf16 v[16:19], v[52:55], v[248:255], v9\n\t"
    "s_nop 6\n\t"
    "v_permlane16_swap_b32 v12, v14\n\t"
    "v_permlane16_swap_b32 v13, v15\n\t"
    "v_permlane16_swap_b32 v16, v18\n\t"
    "v_permlane16_swap_b32 v17, v19\n\t"
    "v_add_f32 v12, v12, v14\n\t"
    "v_add_f32 v13, v13, v15\n\t"
    "v_add_f32 v16, v16, v18\n\t"
    "v_cvt_pk_bf16_f32 v12, v12, v13\n\t"
    "v_add_f32 v17, v17, v19\n\t"
    "ds_write_b32 v10, v12 offset:35328\n\t"
    "v_cvt_pk_bf16_f32 v16, v16, v17\n\t"
    "ds_write_b32 v10, v16 offset:35584\n\t"
    "s_waitcnt lgkmcnt(0)\n\t"
    "s_setprio 0\n\t"
    "s_barrier\n\t"
    "ds_read_b64_tr_b16 v[36:37], v11 offset:34816\n\t"
    "ds_read_b64_tr_b16 v[38:39], v11 offset:34880\n\t"
    "ds_read_b64_tr_b16 v[40:41], v11 offset:35840\n\t"
    "ds_read_b64_tr_b16 v[42:43], v11 offset:35904\n\t"
    "s_waitcnt lgkmcnt(0)\n\t"
    "v_min_u32 v36, -2, v36\n\t"
    "v_min_u32 v37, -2, v37\n\t"
    "v_min_u32 v38, -2, v38\n\t"
    "v_min_u32 v39, -2, v39\n\t"
    "v_min_u32 v40, -2, v40\n\t"
    "v_min_u32 v41, -2, v41\n\t"
    "v_min_u32 v42, -2, v42\n\t"
    "v_min_u32 v43, -2, v43\n\t"
    "global_store_dwordx4 v6, v[36:39], %[vs] sc1\n\t"
    "global_store_dwordx4 v6, v[40:43], %[vs] offset:1024 sc1\n\t"
    "s_nop 1\n\t"
    "s_barrier\n\t"
    "s_waitcnt lgkmcnt(0)\n\t"
    "s_branch 9f\n\t"
    "1:\n\t"
    "s_add_u32 %[sp], %[sp], 1\n\t"
    "s_cmp_ge_u32 %[sp], %[cap]\n\t"
    "s_cbranch_scc1 7f\n\t"
    "global_load_dwordx4 v[36:39], v6, %[ag] sc1\n\t"
    "global_load_dwordx4 v[40:43], v6, %[ag] offset:1024 sc1\n\t"
    "s_waitcnt lgkmcnt(0)\n\t"
    "s_memtime %[tc]\n\t"
    "s_nop 7\n\t"
    "s_branch 3b\n\t"
    "7:\n\t"
    "s_mov_b32 %[cap], 0\n\t"
    "s_branch 2b\n\t"
    "9:"
    : [tc] "=&s"(tc), [sp] "=&s"(sp), [cap] "+s"(cap)
    : [ag] "s"(ag), [c44] "s"(0x4444u), [vs] "s"(vs), "{v[64:95]}"(W.w0), "{v[96:127]}"(W.w1), "{v[128:159]}"(W.w2), "{v[160:191]}"(W.w3), "{v[192:223]}"(W.w4), "{v[224:255]}"(W.w5), "{v[56:63]}"(W.x)
    : "v6", "v7", "v8", "v9", "v10", "v11", "v12", "v13", "v14", "v15", "v16", "v17", "v18", "v19", "v20", "v21", "v22", "v23", "v24", "v25", "v26", "v27", "v28", "v29", "v30", "v31", "v32", "v33", "v34", "v35", "v36", "v37", "v38", "v39", "v40", "v41", "v42", "v43", "v44", "v45", "v46", "v47", "v48", "v49", "v50", "v51", "v52", "v53", "v54", "v55", "vcc", "scc", "memory");
}
template <> __device__ __forceinline__ void qk_step<6>(const QKW& W, const char* ag, const char* vs, const char* od, const char* kv, unsigned& cap, unsigned& sp,
                                                        unsigned long long& tc, unsigned long long* ts, unsigned& d0, unsigned& d1) {
  unsigned st, bg;
  asm volatile("v_mbcnt_lo_u32_b32 v52, -1, 0\n\t"
    "v_mbcnt_hi_u32_b32 v52, -1, v52\n\t"
    "v_lshlrev_b32 v6, 4, v52\n\t"
    "s_mov_b32 %[sp], 0\n\t"
    "global_load_dwordx4 v[36:39], v6, %[ag] sc1\n\t"
    "global_load_dwordx4 v[40:43], v6, %[ag] offset:1024 sc1\n\t"
    "s_memtime %[tc]\n\t"
    "v_add_u32 v7, 77824, v6\n\t"
    "v_add_u32 v8, 135168, v6\n\t"
    "ds_read_b128 v[20:23], v7 offset:8192\n\t"
    "ds_read_b128 v[24:27], v7 offset:9216\n\t"
    "ds_read_b128 v[28:31], v7 offset:0\n\t"
    "ds_read_b128 v[32:35], v7 offset:1024\n\t"
    "v_lshrrev_b32 v44, 4, v52\n\t"
    "v_and_b32 v45, 15, v52\n\t"
    "v_and_b32 v53, 4, v52\n\t"
    "v_mul_u32_u24 v53, 0x2aaa, v53\n\t"
    "v_lshrrev_b32 v9, 1, v52\n\t"
    "v_and_b32 v9, 2, v9\n\t"
    "v_or3_b32 v9, v9, v53, %[c44]\n\t"
    "v_lshlrev_b32 v53, 2, v44\n\t"
    "v_lshl_or_b32 v10, v45, 4, v53\n\t"
    "3:\n\t"
    "v_mov_b32 v12, 0\n\t"
    "v_mov_b32 v13, 0\n\t"
    "v_mov_b32 v14, 0\n\t"
    "v_mov_b32 v15, 0\n\t"
    "v_mov_b32 v16, 0\n\t"
    "v_mov_b32 v17, 0\n\t"
    "v_mov_b32 v18, 0\n\t"
    "v_mov_b32 v19, 0\n\t"
    "s_waitcnt vmcnt(0) lgkmcnt(0)\n\t"
    "14:\n\t"
    "ds_write_b128 v6, v[36:39] offset:12288\n\t"
    "ds_write_b128 v6, v[40:43] offset:13312\n\t"
    "v_smfmac_f32_16x16x64_bf16 v[12:15], v[36:39], v[192:199], v9\n\t"
    "v_max3_u32 v52, v36, v37, v38\n\t"
    "v_max3_u32 v53, v39, v40, v41\n\t"
    "v_smfmac_f32_16x16x64_bf16 v[16:19], v[36:39], v[200:207], v9\n\t"
    "v_max3_u32 v52, v52, v42, v43\n\t"
    "v_max_u32 v52, v52, v53\n\t"
    "v_cmp_eq_u32 vcc, -1, v52\n\t"
    "s_cbranch_vccnz 1f\n\t"
    "2:\n\t"
    "s_waitcnt lgkmcnt(0)\n\t"
    "s_barrier\n\t"
    "ds_read_b128 v[36:39], v6 offset:0\n\t"
    "ds_read_b128 v[44:47], v6 offset:4096\n\t"
    "v_smfmac_f32_16x16x64_bf16 v[12:15], v[40:43], v[208:215], v9\n\t"
    "ds_read_b128 v[48:51], v6 offset:5120\n\t"
    "ds_read_b128 v[52:55], v6 offset:6144\n\t"
    "v_smfmac_f32_16x16x64_bf16 v[16:19], v[40:43], v[216:223], v9\n\t"
    "ds_read_b128 v[40:43], v6 offset:1024\n\t"
    "s_sleep 0\n\t"
    "s_waitcnt lgkmcnt(4)\n\t"
    "v_smfmac_f32_16x16x64_bf16 v[12:15], v[36:39], v[56:63], v9\n\t"
    "v_smfmac_f32_16x16x64_bf16 v[16:19], v[36:39], v[28:35], v9\n\t"
    "ds_read_b128 v[28:31], v7 offset:16384\n\t"
    "ds_read_b128 v[32:35], v7 offset:17408\n\t"
    "ds_read_b128 v[36:39], v6 offset:7168\n\t"
    "s_waitcnt lgkmcnt(6)\n\t"
    "v_smfmac_f32_16x16x64_bf16 v[12:15], v[44:47], v[64:71], v9\n\t"
    "v_smfmac_f32_16x16x64_bf16 v[16:19], v[44:47], v[72:79], v9\n\t"
    "ds_read_b128 v[44:47], v6 offset:8192\n\t"
    "s_waitcnt lgkmcnt(6)\n\t"
    "v_smfmac_f32_16x16x64_bf16 v[12:15], v[48:51], v[80:87], v9\n\t"
    "v_smfmac_f32_16x16x64_bf16 v[16:19], v[48:51], v[88:95], v9\n\t"
    "ds_read_b128 v[48:51], v6 offset:9216\n\t"
    "s_waitcnt lgkmcnt(6)\n\t"
    "v_smfmac_f32_16x16x64_bf16 v[12:15], v[52:55], v[96:103], v9\n\t"
    "v_smfmac_f32_16x16x64_bf16 v[16:19], v[52:55], v[104:111], v9\n\t"
    "ds_read_b128 v[52:55], v6 offset:2048\n\t"
    "s_waitcnt lgkmcnt(6)\n\t"
    "v_smfmac_f32_16x16x64_bf16 v[12:15], v[40:43], v[20:27], v9\n\t"
    "ds_read_b128 v[20:23], v8 offset:0\n\t"
    "ds_read_b128 v[24:27], v8 offset:1024\n\t"
    "s_waitcnt lgkmcnt(6)\n\t"
    "v_smfmac_f32_16x16x64_bf16 v[16:19], v[40:43], v[28:35], v9\n\t"
    "ds_read_b128 v[28:31], v8 offset:8192\n\t"
    "ds_read_b128 v[32:35], v8 offset:9216\n\t"
    "ds_read_b128 v[40:43], v6 offset:10240\n\t"
    "s_waitcnt lgkmcnt(8)\n\t"
    "v_smfmac_f32_16x16x64_bf16 v[12:15], v[36:39], v[112:119], v9\n\t"
    "v_smfmac_f32_16x16x64_bf16 v[16:19], v[36:39], v[120:127], v9\n\t"
    "ds_read_b128 v[36:39], v6 offset:11264\n\t"
    "s_waitcnt lgkmcnt(8)\n\t"
    "v_smfmac_f32_16x16x64_bf16 v[12:15], v[44:47], v[128:135], v9\n\t"
    "v_smfmac_f32_16x16x64_bf16 v[16:19], v[44:47], v[136:143], v9\n\t"
    "ds_read_b128 v[44:47], v6 offset:14336\n\t"
    "s_barrier\n\t"
    "s_setprio 2\n\t"
    "s_waitcnt lgkmcnt(8)\n\t"
    "v_smfmac_f32_16x16x64_bf16 v[12:15], v[48:51], v[144:151], v9\n\t"
    "v_smfmac_f32_16x16x64_bf16 v[16:19], v[48:51], v[152:159], v9\n\t"
    "ds_read_b128 v[48:51], v6 offset:3072\n\t"
    "s_waitcnt lgkmcnt(6)\n\t"
    "v_smfmac_f32_16x16x64_bf16 v[12:15], v[52:55], v[20:27], v9\n\t"
    "ds_read_b128 v[20:23], v8 offset:16384\n\t"
    "ds_read_b128 v[24:27], v8 offset:17408\n\t"
    "s_waitcnt lgkmcnt(6)\n\t"
    "v_smfmac_f32_16x16x64_bf16 v[16:19], v[52:55], v[28:35], v9\n\t"
    "ds_read_b128 v[28:31], v8 offset:24576\n\t"
    "ds_read_b128 v[32:35], v8 offset:25600\n\t"
    "ds_read_b128 v[52:55], v6 offset:15360\n\t"
    "s_waitcnt lgkmcnt(8)\n\t"
    "v_smfmac_f32_16x16x64_bf16 v[12:15], v[40:43], v[160:167], v9\n\t"
    "v_smfmac_f32_16x16x64_bf16 v[16:19], v[40:43], v[168:175], v9\n\t"
    "s_waitcnt lgkmcnt(7)\n\t"
    "v_smfmac_f32_16x16x64_bf16 v[12:15], v[36:39], v[176:183], v9\n\t"
    "v_smfmac_f32_16x16x64_bf16 v[16:19], v[36:39], v[184:191], v9\n\t"
    "s_waitcnt lgkmcnt(6)\n\t"
    "v_smfmac_f32_16x16x64_bf16 v[12:15], v[44:47], v[224:231], v9\n\t"
    "v_smfmac_f32_16x16x64_bf16 v[16:19], v[44:47], v[232:239], v9\n\t"
    "s_waitcnt lgkmcnt(3)\n\t"
    "v_smfmac_f32_16x16x64_bf16 v[12:15], v[48:51], v[20:27], v9\n\t"
    "s_waitcnt lgkmcnt(1)\n\t"
    "v_smfmac_f32_16x16x64_bf16 v[16:19], v[48:51], v[28:35], v9\n\t"
    "s_waitcnt lgkmcnt(0)\n\t"
    "v_smfmac_f32_16x16x64_bf16 v[12:15], v[52:55], v[240:247], v9\n\t"
    "v_smfmac_f32_16x16x64_bf16 v[16:19], v[52:55], v[248:255], v9\n\t"
    "s_nop 6\n\t"
    "v_permlane16_swap_b32 v12, v14\n\t"
    "v_permlane16_swap_b32 v13, v15\n\t"
    "v_permlane16_swap_b32 v16, v18\n\t"
    "v_permlane16_swap_b32 v17, v19\n\t"
    "v_add_f32 v12, v12, v14\n\t"
    "v_add_f32 v13, v13, v15\n\t"
    "v_add_f32 v16, v16, v18\n\t"
    "v_cvt_pk_bf16_f32 v12, v12, v13\n\t"
    "v_add_f32 v17, v17, v19\n\t"
    "ds_write_b32 v10, v12 offset:35840\n\t"
    "v_cvt_pk_bf16_f32 v16, v16, v17\n\t"
    "ds_write_b32 v10, v16 offset:36096\n\t"
    "s_waitcnt lgkmcnt(0)\n\t"
    "s_setprio 0\n\t"
    "s_barrier\n\t"
    "s_barrier\n\t"
    "s_waitcnt lgkmcnt(0)\n\t"
    "s_branch 9f\n\t"
    "1:\n\t"
    "s_add_u32 %[sp], %[sp], 1\n\t"
    "s_cmp_ge_u32 %[sp], %[cap]\n\t"
    "s_cbranch_scc1 7f\n\t"
    "global_load_dwordx4 v[36:39], v6, %[ag] sc1\n\t"
    "global_load_dwordx4 v[40:43], v6, %[ag] offset:1024 sc1\n\t"
    "s_waitcnt lgkmcnt(0)\n\t"
    "s_memtime %[tc]\n\t"
    "s_nop 7\n\t"
    "s_branch 3b\n\t"
    "7:\n\t"
    "s_mov_b32 %[cap], 0\n\t"
    "s_branch 2b\n\t"
    "9:"
    : [tc] "=&s"(tc), [sp] "=&s"(sp), [cap] "+s"(cap)
    : [ag] "s"(ag), [c44] "s"(0x4444u), "{v[64:95]}"(W.w0), "{v[96:127]}"(W.w1), "{v[128:159]}"(W.w2), "{v[160:191]}"(W.w3), "{v[192:223]}"(W.w4), "{v[224:255]}"(W.w5), "{v[56:63]}"(W.x)
    : "v6", "v7", "v8", "v9", "v10", "v11", "v12", "v13", "v14", "v15", "v16", "v17", "v18", "v19", "v20", "v21", "v22", "v23", "v24", "v25", "v26", "v27", "v28", "v29", "v30", "v31", "v32", "v33", "v34", "v35", "v36", "v37", "v38", "v39", "v40", "v41", "v42", "v43", "v44", "v45", "v46", "v47", "v48", "v49", "v50", "v51", "v52", "v53", "v54", "v55", "vcc", "scc", "memory");
}
template <> __device__ __forceinline__ void qk_step<7>(const QKW& W, const char* ag, const char* vs, const char* od, const char* kv, unsigned& cap, unsigned& sp,
                                                        unsigned long long& tc, unsigned long long* ts, unsigned& d0, unsigned& d1) {
  unsigned st, bg;
  asm volatile("v_mbcnt_lo_u32_b32 v52, -1, 0\n\t"
    "v_mbcnt_hi_u32_b32 v52, -1, v52\n\t"
    "v_lshlrev_b32 v6, 4, v52\n\t"
    "s_mov_b32 %[sp], 0\n\t"
    "global_load_dwordx4 v[36:39], v6, %[ag] sc1\n\t"
    "global_load_dwordx4 v[40:43], v6, %[ag] offset:1024 sc1\n\t"
    "s_memtime %[tc]\n\t"
    "v_add_u32 v7, 79872, v6\n\t"
    "v_add_u32 v8, 137216, v6\n\t"
    "ds_read_b128 v[20:23], v7 offset:8192\n\t"
    "ds_read_b128 v[24:27], v7 offset:9216\n\t"
    "ds_read_b128 v[28:31], v7 offset:0\n\t"
    "ds_read_b128 v[32:35], v7 offset:1024\n\t"
    "v_lshrrev_b32 v44, 4, v52\n\t"
    "v_and_b32 v45, 15, v52\n\t"
    "v_and_b32 v53, 4, v52\n\t"
    "v_mul_u32_u24 v53, 0x2aaa, v53\n\t"
    "v_lshrrev_b32 v9, 1, v52\n\t"
    "v_and_b32 v9, 2, v9\n\t"
    "v_or3_b32 v9, v9, v53, %[c44]\n\t"
    "v_lshlrev_b32 v53, 2, v44\n\t"
    "v_lshl_or_b32 v10, v45, 4, v53\n\t"
    "3:\n\t"
    "v_mov_b32 v12, 0\n\t"
    "v_mov_b32 v13, 0\n\t"
    "v_mov_b32 v14, 0\n\t"
    "v_mov_b32 v15, 0\n\t"
    "v_mov_b32 v16, 0\n\t"
    "v_mov_b32 v17, 0\n\t"
    "v_mov_b32 v18, 0\n\t"
    "v_mov_b32 v19, 0\n\t"
    "s_waitcnt vmcnt(0) lgkmcnt(0)\n\t"
    "14:\n\t"
    "ds_write_b128 v6, v[36:39] offset:14336\n\t"
    "ds_write_b128 v6, v[40:43] offset:15360\n\t"
    "v_smfmac_f32_16x16x64_bf16 v[12:15], v[36:39], v[224:231], v9\n\t"
    "v_max3_u32 v52, v36, v37, v38\n\t"
    "v_max3_u32 v53, v39, v40, v41\n\t"
    "v_smfmac_f32_16x16x64_bf16 v[16:19], v[36:39], v[232:239], v9\n\t"
    "v_max3_u32 v52, v52, v42, v43\n\t"
    "v_max_u32 v52, v52, v53\n\t"
    "v_cmp_eq_u32 vcc, -1, v52\n\t"
    "s_cbranch_vccnz 1f\n\t"
    "2:\n\t"
    "s_waitcnt lgkmcnt(0)\n\t"
    "s_barrier\n\t"
    "ds_read_b128 v[36:39], v6 offset:0\n\t"
    "ds_read_b128 v[44:47], v6 offset:4096\n\t"
    "v_smfmac_f32_16x16x64_bf16 v[12:15], v[40:43], v[240:247], v9\n\t"
    "ds_read_b128 v[48:51], v6 offset:5120\n\t"
    "ds_read_b128 v[52:55], v6 offset:6144\n\t"
    "v_smfmac_f32_16x16x64_bf16 v[16:19], v[40:43], v[248:255], v9\n\t"
    "ds_read_b128 v[40:43], v6 offset:1024\n\t"
    "s_sleep 0\n\t"
    "s_waitcnt lgkmcnt(4)\n\t"
    "v_smfmac_f32_16x16x64_bf16 v[12:15], v[36:39], v[56:63], v9\n\t"
    "v_smfmac_f32_16x16x64_bf16 v[16:19], v[36:39], v[28:35], v9\n\t"
    "ds_read_b128 v[28:31], v7 offset:16384\n\t"
    "ds_read_b128 v[32:35], v7 offset:17408\n\t"
    "ds_read_b128 v[36:39], v6 offset:7168\n\t"
    "s_waitcnt lgkmcnt(6)\n\t"
    "v_smfmac_f32_16x16x64_bf16 v[12:15], v[44:47], v[64:71], v9\n\t"
    "v_smfmac_f32_16x16x64_bf16 v[16:19], v[44:47], v[72:79], v9\n\t"
    "ds_read_b128 v[44:47], v6 offset:8192\n\t"
    "s_waitcnt lgkmcnt(6)\n\t"
    "v_smfmac_f32_16x16x64_bf16 v[12:15], v[48:51], v[80:87], v9\n\t"
    "v_smfmac_f32_16x16x64_bf16 v[16:19], v[48:51], v[88:95], v9\n\t"
    "ds_read_b128 v[48:51], v6 offset:9216\n\t"
    "s_waitcnt lgkmcnt(6)\n\t"
    "v_smfmac_f32_16x16x64_bf16 v[12:15], v[52:55], v[96:103], v9\n\t"
    "v_smfmac_f32_16x16x64_bf16 v[16:19], v[52:55], v[104:111], v9\n\t"
    "ds_read_b128 v[52:55], v6 offset:2048\n\t"
    "s_waitcnt lgkmcnt(6)\n\t"
    "v_smfmac_f32_16x16x64_bf16 v[12:15], v[40:43], v[20:27], v9\n\t"
    "ds_read_b128 v[20:23], v8 offset:0\n\t"
    "ds_read_b128 v[24:27], v8 offset:1024\n\t"
    "s_waitcnt lgkmcnt(6)\n\t"
    "v_smfmac_f32_16x16x64_bf16 v[16:19], v[40:43], v[28:35], v9\n\t"
    "ds_read_b128 v[28:31], v8 offset:8192\n\t"
    "ds_read_b128 v[32:35], v8 offset:9216\n\t"
    "ds_read_b128 v[40:43], v6 offset:10240\n\t"
    "s_waitcnt lgkmcnt(8)\n\t"
    "v_smfmac_f32_16x16x64_bf16 v[12:15], v[36:39], v[112:119], v9\n\t"
    "v_smfmac_f32_16x16x64_bf16 v[16:19], v[36:39], v[120:127], v9\n\t"
    "ds_read_b128 v[36:39], v6 offset:11264\n\t"
    "s_waitcnt lgkmcnt(8)\n\t"
    "v_smfmac_f32_16x16x64_bf16 v[12:15], v[44:47], v[128:135], v9\n\t"
    "v_smfmac_f32_16x16x64_bf16 v[16:19], v[44:47], v[136:143], v9\n\t"
    "ds_read_b128 v[44:47], v6 offset:12288\n\t"
    "s_barrier\n\t"
    "s_setprio 2\n\t"
    "s_waitcnt lgkmcnt(8)\n\t"
    "v_smfmac_f32_16x16x64_bf16 v[12:15], v[48:51], v[144:151], v9\n\t"
    "v_smfmac_f32_16x16x64_bf16 v[16:19], v[48:51], v[152:159], v9\n\t"
    "ds_read_b128 v[48:51], v6 offset:3072\n\t"
    "s_waitcnt lgkmcnt(6)\n\t"
    "v_smfmac_f32_16x16x64_bf16 v[12:15], v[52:55], v[20:27], v9\n\t"
    "ds_read_b128 v[20:23], v8 offset:16384\n\t"
    "ds_read_b128 v[24:27], v8 offset:17408\n\t"
    "s_waitcnt lgkmcnt(6)\n\t"
    "v_smfmac_f32_16x16x64_bf16 v[16:19], v[52:55], v[28:35], v9\n\t"
    "ds_read_b128 v[28:31], v8 offset:24576\n\t"
    "ds_read_b128 v[32:35], v8 offset:25600\n\t"
    "ds_read_b128 v[52:55], v6 offset:13312\n\t"
    "s_waitcnt lgkmcnt(8)\n\t"
    "v_smfmac_f32_16x16x64_bf16 v[12:15], v[40:43], v[160:167], v9\n\t"
    "v_smfmac_f32_16x16x64_bf16 v[16:19], v[40:43], v[168:175], v9\n\t"
    "s_waitcnt lgkmcnt(7)\n\t"
    "v_smfmac_f32_16x16x64_bf16 v[12:15], v[36:39], v[176:183], v9\n\t"
    "v_smfmac_f32_16x16x64_bf16 v[16:19], v[36:39], v[184:191], v9\n\t"
    "s_waitcnt lgkmcnt(6)\n\t"
    "v_smfmac_f32_16x16x64_bf16 v[12:15], v[44:47], v[192:199], v9\n\t"
    "v_smfmac_f32_16x16x64_bf16 v[16:19], v[44:47], v[200:207], v9\n\t"
    "s_waitcnt lgkmcnt(3)\n\t"
    "v_smfmac_f32_16x16x64_bf16 v[12:15], v[48:51], v[20:27], v9\n\t"
    "s_waitcnt lgkmcnt(1)\n\t"
    "v_smfmac_f32_16x16x64_bf16 v[16:19], v[48:51], v[28:35], v9\n\t"
    "s_waitcnt lgkmcnt(0)\n\t"
    "v_smfmac_f32_16x16x64_bf16 v[12:15], v[52:55], v[208:215], v9\n\t"
    "v_smfmac_f32_16x16x64_bf16 v[16:19], v[52:55], v[216:223], v9\n\t"
    "s_nop 6\n\t"
    "v_permlane16_swap_b32 v12, v14\n\t"
    "v_permlane16_swap_b32 v13, v15\n\t"
    "v_permlane16_swap_b32 v16, v18\n\t"
    "v_permlane16_swap_b32 v17, v19\n\t"
    "v_add_f32 v12, v12, v14\n\t"
    "v_add_f32 v13, v13, v15\n\t"
    "v_add_f32 v16, v16, v18\n\t"
    "v_cvt_pk_bf16_f32 v12, v12, v13\n\t"
    "v_add_f32 v17, v17, v19\n\t"
    "ds_write_b32 v10, v12 offset:36352\n\t"
    "v_cvt_pk_bf16_f32 v16, v16, v17\n\t"
    "ds_write_b32 v10, v16 offset:36608\n\t"
    "s_waitcnt lgkmcnt(0)\n\t"
    "s_setprio 0\n\t"
    "s_barrier\n\t"
    "s_barrier\n\t"
    "s_waitcnt lgkmcnt(0)\n\t"
    "s_branch 9f\n\t"
    "1:\n\t"
    "s_add_u32 %[sp], %[sp], 1\n\t"
    "s_cmp_ge_u32 %[sp], %[cap]\n\t"
    "s_cbranch_scc1 7f\n\t"
    "global_load_dwordx4 v[36:39], v6, %[ag] sc1\n\t"
    "global_load_dwordx4 v[40:43], v6, %[ag] offset:1024 sc1\n\t"
    "s_waitcnt lgkmcnt(0)\n\t"
    "s_memtime %[tc]\n\t"
    "s_nop 7\n\t"
    "s_branch 3b\n\t"
    "7:\n\t"
    "s_mov_b32 %[cap], 0\n\t"
    "s_branch 2b\n\t"
    "9:"
    : [tc] "=&s"(tc), [sp] "=&s"(sp), [cap] "+s"(cap)
    : [ag] "s"(ag), [c44] "s"(0x4444u), "{v[64:95]}"(W.w0), "{v[96:127]}"(W.w1), "{v[128:159]}"(W.w2), "{v[160:191]}"(W.w3), "{v[192:223]}"(W.w4), "{v[224:255]}"(W.w5), "{v[56:63]}"(W.x)
    : "v6", "v7", "v8", "v9", "v10", "v11", "v12", "v13", "v14", "v15", "v16", "v17", "v18", "v19", "v20", "v21", "v22", "v23", "v24", "v25", "v26", "v27", "v28", "v29", "v30", "v31", "v32", "v33", "v34", "v35", "v36", "v37", "v38", "v39", "v40", "v41", "v42", "v43", "v44", "v45", "v46", "v47", "v48", "v49", "v50", "v51", "v52", "v53", "v54", "v55", "vcc", "scc", "memory");
}
template <int I> __device__ __forceinline__ void qk_wload(u32x32& w, unsigned a0, unsigned a1);
template <> __device__ __forceinline__ void qk_wload<0>(u32x32& w, unsigned a0, unsigned a1) {
  asm volatile("ds_read_b64_tr_b16 v[64:65], %1 offset:0\n\t"
    "ds_read_b64_tr_b16 v[66:67], %1 offset:512\n\t"
    "ds_read_b64_tr_b16 v[68:69], %1 offset:4096\n\t"
    "ds_read_b64_tr_b16 v[70:71], %1 offset:4608\n\t"
    "ds_read_b64_tr_b16 v[72:73], %2 offset:0\n\t"
    "ds_read_b64_tr_b16 v[74:75], %2 offset:512\n\t"
    "ds_read_b64_tr_b16 v[76:77], %2 offset:4096\n\t"
    "ds_read_b64_tr_b16 v[78:79], %2 offset:4608\n\t"
    "s_waitcnt lgkmcnt(0)\n\t"
    "ds_read_b64_tr_b16 v[80:81], %1 offset:16384\n\t"
    "ds_read_b64_tr_b16 v[82:83], %1 offset:16896\n\t"
    "ds_read_b64_tr_b16 v[84:85], %1 offset:20480\n\t"
    "ds_read_b64_tr_b16 v[86:87], %1 offset:20992\n\t"
    "ds_read_b64_tr_b16 v[88:89], %2 offset:16384\n\t"
    "ds_read_b64_tr_b16 v[90:91], %2 offset:16896\n\t"
    "ds_read_b64_tr_b16 v[92:93], %2 offset:20480\n\t"
    "ds_read_b64_tr_b16 v[94:95], %2 offset:20992\n\t"
    "s_waitcnt lgkmcnt(0)"
    : "=&{v[64:95]}"(w) : "v"(a0), "v"(a1) : "memory");
}
template <> __device__ __forceinline__ void qk_wload<1>(u32x32& w, unsigned a0, unsigned a1) {
  asm volatile("ds_read_b64_tr_b16 v[96:97], %1 offset:0\n\t"
    "ds_read_b64_tr_b16 v[98:99], %1 offset:512\n\t"
    "ds_read_b64_tr_b16 v[100:101], %1 offset:4096\n\t"
    "ds_read_b64_tr_b16 v[102:103], %1 offset:4608\n\t"
    "ds_read_b64_tr_b16 v[104:105], %2 offset:0\n\t"
    "ds_read_b64_tr_b16 v[106:107], %2 offset:512\n\t"
    "ds_read_b64_tr_b16 v[108:109], %2 offset:4096\n\t"
    "ds_read_b64_tr_b16 v[110:111], %2 offset:4608\n\t"
    "s_waitcnt lgkmcnt(0)\n\t"
    "ds_read_b64_tr_b16 v[112:113], %1 offset:16384\n\t"
    "ds_read_b64_tr_b16 v[114:115], %1 offset:16896\n\t"
    "ds_read_b64_tr_b16 v[116:117], %1 offset:20480\n\t"
    "ds_read_b64_tr_b16 v[118:119], %1 offset:20992\n\t"
    "ds_read_b64_tr_b16 v[120:121], %2 offset:16384\n\t"
    "ds_read_b64_tr_b16 v[122:123], %2 offset:16896\n\t"
    "ds_read_b64_tr_b16 v[124:125], %2 offset:20480\n\t"
    "ds_read_b64_tr_b16 v[126:127], %2 offset:20992\n\t"
    "s_waitcnt lgkmcnt(0)"
    : "=&{v[96:127]}"(w) : "v"(a0), "v"(a1) : "memory");
}
template <> __device__ __forceinline__ void qk_wload<2>(u32x32& w, unsigned a0, unsigned a1) {
  asm volatile("ds_read_b64_tr_b16 v[128:129], %1 offset:0\n\t"
    "ds_read_b64_tr_b16 v[130:131], %1 offset:512\n\t"
    "ds_read_b64_tr_b16 v[132:133], %1 offset:4096\n\t"
    "ds_read_b64_tr_b16 v[134:135], %1 offset:4608\n\t"
    "ds_read_b64_tr_b16 v[136:137], %2 offset:0\n\t"
    "ds_read_b64_tr_b16 v[138:139], %2 offset:512\n\t"
    "ds_read_b64_tr_b16 v[140:141], %2 offset:4096\n\t"
    "ds_read_b64_tr_b16 v[142:143], %2 offset:4608\n\t"
    "s_waitcnt lgkmcnt(0)\n\t"
    "ds_read_b64_tr_b16 v[144:145], %1 offset:16384\n\t"
    "ds_read_b64_tr_b16 v[146:147], %1 offset:16896\n\t"
    "ds_read_b64_tr_b16 v[148:149], %1 offset:20480\n\t"
    "ds_read_b64_tr_b16 v[150:151], %1 offset:20992\n\t"
    "ds_read_b64_tr_b16 v[152:153], %2 offset:16384\n\t"
    "ds_read_b64_tr_b16 v[154:155], %2 offset:16896\n\t"
    "ds_read_b64_tr_b16 v[156:157], %2 offset:20480\n\t"
    "ds_read_b64_tr_b16 v[158:159], %2 offset:20992\n\t"
    "s_waitcnt lgkmcnt(0)"
    : "=&{v[128:159]}"(w) : "v"(a0), "v"(a1) : "memory");
}
template <> __device__ __forceinline__ void qk_wload<3>(u32x32& w, unsigned a0, unsigned a1) {
  asm volatile("ds_read_b64_tr_b16 v[160:161], %1 offset:0\n\t"
    "ds_read_b64_tr_b16 v[162:163], %1 offset:512\n\t"
    "ds_read_b64_tr_b16 v[164:165], %1 offset:4096\n\t"
    "ds_read_b64_tr_b16 v[166:167], %1 offset:4608\n\t"
    "ds_read_b64_tr_b16 v[168:169], %2 offset:0\n\t"
    "ds_read_b64_tr_b16 v[170:171], %2 offset:512\n\t"
    "ds_read_b64_tr_b16 v[172:173], %2 offset:4096\n\t"
    "ds_read_b64_tr_b16 v[174:175], %2 offset:4608\n\t"
    "s_waitcnt lgkmcnt(0)\n\t"
    "ds_read_b64_tr_b16 v[176:177], %1 offset:16384\n\t"
    "ds_read_b64_tr_b16 v[178:179], %1 offset:16896\n\t"
    "ds_read_b64_tr_b16 v[180:181], %1 offset:20480\n\t"
    "ds_read_b64_tr_b16 v[182:183], %1 offset:20992\n\t"
    "ds_read_b64_tr_b16 v[184:185], %2 offset:16384\n\t"
    "ds_read_b64_tr_b16 v[186:187], %2 offset:16896\n\t"
    "ds_read_b64_tr_b16 v[188:189], %2 offset:20480\n\t"
    "ds_read_b64_tr_b16 v[190:191], %2 offset:20992\n\t"
    "s_waitcnt lgkmcnt(0)"
    : "=&{v[160:191]}"(w) : "v"(a0), "v"(a1) : "memory");
}
template <> __device__ __forceinline__ void qk_wload<4>(u32x32& w, unsigned a0, unsigned a1) {
  asm volatile("ds_read_b64_tr_b16 v[192:193], %1 offset:0\n\t"
    "ds_read_b64_tr_b16 v[194:195], %1 offset:512\n\t"
    "ds_read_b64_tr_b16 v[196:197], %1 offset:4096\n\t"
    "ds_read_b64_tr_b16 v[198:199], %1 offset:4608\n\t"
    "ds_read_b64_tr_b16 v[200:201], %2 offset:0\n\t"
    "ds_read_b64_tr_b16 v[202:203], %2 offset:512\n\t"
    "ds_read_b64_tr_b16 v[204:205], %2 offset:4096\n\t"
    "ds_read_b64_tr_b16 v[206:207], %2 offset:4608\n\t"
    "s_waitcnt lgkmcnt(0)\n\t"
    "ds_read_b64_tr_b16 v[208:209], %1 offset:16384\n\t"
    "ds_read_b64_tr_b16 v[210:211], %1 offset:16896\n\t"
    "ds_read_b64_tr_b16 v[212:213], %1 offset:20480\n\t"
    "ds_read_b64_tr_b16 v[214:215], %1 offset:20992\n\t"
    "ds_read_b64_tr_b16 v[216:217], %2 offset:16384\n\t"
    "ds_read_b64_tr_b16 v[218:219], %2 offset:16896\n\t"
    "ds_read_b64_tr_b16 v[220:221], %2 offset:20480\n\t"
    "ds_read_b64_tr_b16 v[222:223], %2 offset:20992\n\t"
    "s_waitcnt lgkmcnt(0)"
    : "=&{v[192:223]}"(w) : "v"(a0), "v"(a1) : "memory");
}
template <> __device__ __forceinline__ void qk_wload<5>(u32x32& w, unsigned a0, unsigned a1) {
  asm volatile("ds_read_b64_tr_b16 v[224:225], %1 offset:0\n\t"
    "ds_read_b64_tr_b16 v[226:227], %1 offset:512\n\t"
    "ds_read_b64_tr_b16 v[228:229], %1 offset:4096\n\t"
    "ds_read_b64_tr_b16 v[230:231], %1 offset:4608\n\t"
    "ds_read_b64_tr_b16 v[232:233], %2 offset:0\n\t"
    "ds_read_b64_tr_b16 v[234:235], %2 offset:512\n\t"
    "ds_read_b64_tr_b16 v[236:237], %2 offset:4096\n\t"
    "ds_read_b64_tr_b16 v[238:239], %2 offset:4608\n\t"
    "s_waitcnt lgkmcnt(0)\n\t"
    "ds_read_b64_tr_b16 v[240:241], %1 offset:16384\n\t"
    "ds_read_b64_tr_b16 v[242:243], %1 offset:16896\n\t"
    "ds_read_b64_tr_b16 v[244:245], %1 offset:20480\n\t"
    "ds_read_b64_tr_b16 v[246:247], %1 offset:20992\n\t"
    "ds_read_b64_tr_b16 v[248:249], %2 offset:16384\n\t"
    "ds_read_b64_tr_b16 v[250:251], %2 offset:16896\n\t"
    "ds_read_b64_tr_b16 v[252:253], %2 offset:20480\n\t"
    "ds_read_b64_tr_b16 v[254:255], %2 offset:20992\n\t"
    "s_waitcnt lgkmcnt(0)"
    : "=&{v[224:255]}"(w) : "v"(a0), "v"(a1) : "memory");
}
template <int I> __device__ __forceinline__ void vr_wload(u32x32& w, unsigned a);
template <> __device__ __forceinline__ void vr_wload<0>(u32x32& w, unsigned a) {
  asm volatile("ds_read_b64_tr_b16 v[64:65], %1 offset:0\n\t"
    "ds_read_b64_tr_b16 v[66:67], %1 offset:256\n\t"
    "ds_read_b64_tr_b16 v[68:69], %1 offset:2048\n\t"
    "ds_read_b64_tr_b16 v[70:71], %1 offset:2304\n\t"
    "ds_read_b64_tr_b16 v[72:73], %1 offset:32\n\t"
    "ds_read_b64_tr_b16 v[74:75], %1 offset:288\n\t"
    "ds_read_b64_tr_b16 v[76:77], %1 offset:2080\n\t"
    "ds_read_b64_tr_b16 v[78:79], %1 offset:2336\n\t"
    "s_waitcnt lgkmcnt(0)\n\t"
    "ds_read_b64_tr_b16 v[80:81], %1 offset:64\n\t"
    "ds_read_b64_tr_b16 v[82:83], %1 offset:320\n\t"
    "ds_read_b64_tr_b16 v[84:85], %1 offset:2112\n\t"
    "ds_read_b64_tr_b16 v[86:87], %1 offset:2368\n\t"
    "ds_read_b64_tr_b16 v[88:89], %1 offset:96\n\t"
    "ds_read_b64_tr_b16 v[90:91], %1 offset:352\n\t"
    "ds_read_b64_tr_b16 v[92:93], %1 offset:2144\n\t"
    "ds_read_b64_tr_b16 v[94:95], %1 offset:2400\n\t"
    "s_waitcnt lgkmcnt(0)"
    : "=&{v[64:95]}"(w) : "v"(a) : "memory");
}
template <> __device__ __forceinline__ void vr_wload<1>(u32x32& w, unsigned a) {
  asm volatile("ds_read_b64_tr_b16 v[96:97], %1 offset:8192\n\t"
    "ds_read_b64_tr_b16 v[98:99], %1 offset:8448\n\t"
    "ds_read_b64_tr_b16 v[100:101], %1 offset:10240\n\t"
    "ds_read_b64_tr_b16 v[102:103], %1 offset:10496\n\t"
    "ds_read_b64_tr_b16 v[104:105], %1 offset:8224\n\t"
    "ds_read_b64_tr_b16 v[106:107], %1 offset:8480\n\t"
    "ds_read_b64_tr_b16 v[108:109], %1 offset:10272\n\t"
    "ds_read_b64_tr_b16 v[110:111], %1 offset:10528\n\t"
    "s_waitcnt lgkmcnt(0)\n\t"
    "ds_read_b64_tr_b16 v[112:113], %1 offset:8256\n\t"
    "ds_read_b64_tr_b16 v[114:115], %1 offset:8512\n\t"
    "ds_read_b64_tr_b16 v[116:117], %1 offset:10304\n\t"
    "ds_read_b64_tr_b16 v[118:119], %1 offset:10560\n\t"
    "ds_read_b64_tr_b16 v[120:121], %1 offset:8288\n\t"
    "ds_read_b64_tr_b16 v[122:123], %1 offset:8544\n\t"
    "ds_read_b64_tr_b16 v[124:125], %1 offset:10336\n\t"
    "ds_read_b64_tr_b16 v[126:127], %1 offset:10592\n\t"
    "s_waitcnt lgkmcnt(0)"
    : "=&{v[96:127]}"(w) : "v"(a) : "memory");
}
template <> __device__ __forceinline__ void vr_wload<2>(u32x32& w, unsigned a) {
  asm volatile("ds_read_b64_tr_b16 v[128:129], %1 offset:0\n\t"
    "ds_read_b64_tr_b16 v[130:131], %1 offset:128\n\t"
    "ds_read_b64_tr_b16 v[132:133], %1 offset:1024\n\t"
    "ds_read_b64_tr_b16 v[134:135], %1 offset:1152\n\t"
    "ds_read_b64_tr_b16 v[136:137], %1 offset:32\n\t"
    "ds_read_b64_tr_b16 v[138:139], %1 offset:160\n\t"
    "ds_read_b64_tr_b16 v[140:141], %1 offset:1056\n\t"
    "ds_read_b64_tr_b16 v[142:143], %1 offset:1184\n\t"
    "s_waitcnt lgkmcnt(0)\n\t"
    "ds_read_b64_tr_b16 v[144:145], %1 offset:4096\n\t"
    "ds_read_b64_tr_b16 v[146:147], %1 offset:4224\n\t"
    "ds_read_b64_tr_b16 v[148:149], %1 offset:5120\n\t"
    "ds_read_b64_tr_b16 v[150:151], %1 offset:5248\n\t"
    "ds_read_b64_tr_b16 v[152:153], %1 offset:4128\n\t"
    "ds_read_b64_tr_b16 v[154:155], %1 offset:4256\n\t"
    "ds_read_b64_tr_b16 v[156:157], %1 offset:5152\n\t"
    "ds_read_b64_tr_b16 v[158:159], %1 offset:5280\n\t"
    "s_waitcnt lgkmcnt(0)"
    : "=&{v[128:159]}"(w) : "v"(a) : "memory");
}
template <> __device__ __forceinline__ void vr_wload<3>(u32x32& w, unsigned a) {
  asm volatile("ds_read_b64_tr_b16 v[160:161], %1 offset:8192\n\t"
    "ds_read_b64_tr_b16 v[162:163], %1 offset:8320\n\t"
    "ds_read_b64_tr_b16 v[164:165], %1 offset:9216\n\t"
    "ds_read_b64_tr_b16 v[166:167], %1 offset:9344\n\t"
    "ds_read_b64_tr_b16 v[168:169], %1 offset:8224\n\t"
    "ds_read_b64_tr_b16 v[170:171], %1 offset:8352\n\t"
    "ds_read_b64_tr_b16 v[172:173], %1 offset:9248\n\t"
    "ds_read_b64_tr_b16 v[174:175], %1 offset:9376\n\t"
    "s_waitcnt lgkmcnt(0)\n\t"
    "ds_read_b64_tr_b16 v[176:177], %1 offset:12288\n\t"
    "ds_read_b64_tr_b16 v[178:179], %1 offset:12416\n\t"
    "ds_read_b64_tr_b16 v[180:181], %1 offset:13312\n\t"
    "ds_read_b64_tr_b16 v[182:183], %1 offset:13440\n\t"
    "ds_read_b64_tr_b16 v[184:185], %1 offset:12320\n\t"
    "ds_read_b64_tr_b16 v[186:187], %1 offset:12448\n\t"
    "ds_read_b64_tr_b16 v[188:189], %1 offset:13344\n\t"
    "ds_read_b64_tr_b16 v[190:191], %1 offset:13472\n\t"
    "s_waitcnt lgkmcnt(0)"
    : "=&{v[160:191]}"(w) : "v"(a) : "memory");
}
template <> __device__ __forceinline__ void vr_wload<4>(u32x32& w, unsigned a) {
  asm volatile("ds_read_b64_tr_b16 v[192:193], %1 offset:0\n\t"
    "ds_read_b64_tr_b16 v[194:195], %1 offset:128\n\t"
    "ds_read_b64_tr_b16 v[196:197], %1 offset:1024\n\t"
    "ds_read_b64_tr_b16 v[198:199], %1 offset:1152\n\t"
    "ds_read_b64_tr_b16 v[200:201], %1 offset:32\n\t"
    "ds_read_b64_tr_b16 v[202:203], %1 offset:160\n\t"
    "ds_read_b64_tr_b16 v[204:205], %1 offset:1056\n\t"
    "ds_read_b64_tr_b16 v[206:207], %1 offset:1184\n\t"
    "s_waitcnt lgkmcnt(0)\n\t"
    "ds_read_b64_tr_b16 v[208:209], %1 offset:4096\n\t"
    "ds_read_b64_tr_b16 v[210:211], %1 offset:4224\n\t"
    "ds_read_b64_tr_b16 v[212:213], %1 offset:5120\n\t"
    "ds_read_b64_tr_b16 v[214:215], %1 offset:5248\n\t"
    "ds_read_b64_tr_b16 v[216:217], %1 offset:4128\n\t"
    "ds_read_b64_tr_b16 v[218:219], %1 offset:4256\n\t"
    "ds_read_b64_tr_b16 v[220:221], %1 offset:5152\n\t"
    "ds_read_b64_tr_b16 v[222:223], %1 offset:5280\n\t"
    "s_waitcnt lgkmcnt(0)"
    : "=&{v[192:223]}"(w) : "v"(a) : "memory");
}
template <> __device__ __forceinline__ void vr_wload<5>(u32x32& w, unsigned a) {
  asm volatile("ds_read_b64_tr_b16 v[224:225], %1 offset:8192\n\t"
    "ds_read_b64_tr_b16 v[226:227], %1 offset:8320\n\t"
    "ds_read_b64_tr_b16 v[228:229], %1 offset:9216\n\t"
    "ds_read_b64_tr_b16 v[230:231], %1 offset:9344\n\t"
    "ds_read_b64_tr_b16 v[232:233], %1 offset:8224\n\t"
    "ds_read_b64_tr_b16 v[234:235], %1 offset:8352\n\t"
    "ds_read_b64_tr_b16 v[236:237], %1 offset:9248\n\t"
    "ds_read_b64_tr_b16 v[238:239], %1 offset:9376\n\t"
    "s_waitcnt lgkmcnt(0)\n\t"
    "ds_read_b64_tr_b16 v[240:241], %1 offset:12288\n\t"
    "ds_read_b64_tr_b16 v[242:243], %1 offset:12416\n\t"
    "ds_read_b64_tr_b16 v[244:245], %1 offset:13312\n\t"
    "ds_read_b64_tr_b16 v[246:247], %1 offset:13440\n\t"
    "ds_read_b64_tr_b16 v[248:249], %1 offset:12320\n\t"
    "ds_read_b64_tr_b16 v[250:251], %1 offset:12448\n\t"
    "ds_read_b64_tr_b16 v[252:253], %1 offset:13344\n\t"
    "ds_read_b64_tr_b16 v[254:255], %1 offset:13472\n\t"
    "s_waitcnt lgkmcnt(0)"
    : "=&{v[224:255]}"(w) : "v"(a) : "memory");
}
#define OREP 2
#define AREP 2
struct VRW { u32x32 v0, v1, o0, o1, o2, o3; };
__device__ __forceinline__ void vr_v(const VRW& W, const char* ag, const char* vd, unsigned wa, unsigned ra, unsigned& cap, unsigned& sp, unsigned long long& tc, unsigned long long* ts) {
  asm volatile("v_mbcnt_lo_u32_b32 v16, -1, 0\n\t"
    "v_mbcnt_hi_u32_b32 v16, -1, v16\n\t"
    "v_lshlrev_b32 v17, 4, v16\n\t"
    "s_mov_b32 %[sp], 0\n\t"
    "global_load_dwordx4 v[24:27], v17, %[ag] sc1\n\t"
    "global_load_dwordx4 v[28:31], v17, %[ag] offset:1024 sc1\n\t"
    "s_memtime %[tc]\n\t"
    "v_and_b32 v22, 4, v16\n\t"
    "v_mul_u32_u24 v22, 0x2aaa, v22\n\t"
    "v_lshrrev_b32 v18, 1, v16\n\t"
    "v_and_b32 v18, 2, v18\n\t"
    "v_or3_b32 v18, v18, v22, %[c44]\n\t"
    "v_lshl_add_u32 v19, v16, 3, %[wa]\n\t"
    "v_lshl_add_u32 v20, v16, 3, %[ra]\n\t"
    "v_and_b32 v22, 40, v16\n\t"
    "v_lshlrev_b32 v21, 3, v22\n\t"
    "v_and_b32 v22, 3, v16\n\t"
    "v_lshl_or_b32 v21, v22, 4, v21\n\t"
    "v_and_b32 v22, 4, v16\n\t"
    "v_lshl_or_b32 v21, v22, 1, v21\n\t"
    "v_lshrrev_b32 v22, 2, v16\n\t"
    "v_and_or_b32 v21, v22, 4, v21\n\t"
    "3:\n\t"
    "v_mov_b32 v32, 0\n\t"
    "v_mov_b32 v33, 0\n\t"
    "v_mov_b32 v34, 0\n\t"
    "v_mov_b32 v35, 0\n\t"
    "v_mov_b32 v36, 0\n\t"
    "v_mov_b32 v37, 0\n\t"
    "v_mov_b32 v38, 0\n\t"
    "v_mov_b32 v39, 0\n\t"
    "v_mov_b32 v40, 0\n\t"
    "v_mov_b32 v41, 0\n\t"
    "v_mov_b32 v42, 0\n\t"
    "v_mov_b32 v43, 0\n\t"
    "v_mov_b32 v44, 0\n\t"
    "v_mov_b32 v45, 0\n\t"
    "v_mov_b32 v46, 0\n\t"
    "v_mov_b32 v47, 0\n\t"
    "s_waitcnt vmcnt(0) lgkmcnt(0)\n\t"
    "v_smfmac_f32_16x16x64_bf16 v[32:35], v[24:27], v[64:71], v18\n\t"
    "v_smfmac_f32_16x16x64_bf16 v[32:35], v[28:31], v[96:103], v18\n\t"
    "v_max3_u32 v22, v24, v25, v26\n\t"
    "v_max3_u32 v23, v27, v28, v29\n\t"
    "v_smfmac_f32_16x16x64_bf16 v[36:39], v[24:27], v[72:79], v18\n\t"
    "v_max3_u32 v22, v22, v30, v31\n\t"
    "v_max_u32 v22, v22, v23\n\t"
    "v_smfmac_f32_16x16x64_bf16 v[36:39], v[28:31], v[104:111], v18\n\t"
    "v_cmp_eq_u32 vcc, -1, v22\n\t"
    "s_cbranch_vccnz 1f\n\t"
    "2:\n\t"
    "v_smfmac_f32_16x16x64_bf16 v[40:43], v[24:27], v[80:87], v18\n\t"
    "v_permlane16_swap_b32 v32, v34\n\t"
    "v_permlane16_swap_b32 v33, v35\n\t"
    "v_smfmac_f32_16x16x64_bf16 v[40:43], v[28:31], v[112:119], v18\n\t"
    "v_pk_add_f32 v[32:33], v[32:33], v[34:35]\n\t"
    "ds_write_b64 v19, v[32:33] offset:0\n\t"
    "v_smfmac_f32_16x16x64_bf16 v[44:47], v[24:27], v[88:95], v18\n\t"
    "s_nop 0\n\t"
    "v_permlane16_swap_b32 v36, v38\n\t"
    "v_permlane16_swap_b32 v37, v39\n\t"
    "v_smfmac_f32_16x16x64_bf16 v[44:47], v[28:31], v[120:127], v18\n\t"
    "v_pk_add_f32 v[36:37], v[36:37], v[38:39]\n\t"
    "ds_write_b64 v19, v[36:37] offset:512\n\t"
    "v_permlane16_swap_b32 v40, v42\n\t"
    "v_permlane16_swap_b32 v41, v43\n\t"
    "v_pk_add_f32 v[40:41], v[40:41], v[42:43]\n\t"
    "ds_write_b64 v19, v[40:41] offset:1024\n\t"
    "s_nop 1\n\t"
    "v_permlane16_swap_b32 v44, v46\n\t"
    "v_permlane16_swap_b32 v45, v47\n\t"
    "v_pk_add_f32 v[44:45], v[44:45], v[46:47]\n\t"
    "ds_write_b64 v19, v[44:45] offset:1536\n\t"
    "s_cmp_ge_u32 %[wa], 8192\n\t"
    "s_waitcnt lgkmcnt(0)\n\t"
    "s_barrier\n\t"
    "s_cbranch_scc1 9f\n\t"
    "ds_read_b64 v[48:49], v20 offset:0\n\t"
    "ds_read_b64 v[50:51], v20 offset:2048\n\t"
    "ds_read_b64 v[52:53], v20 offset:4096\n\t"
    "ds_read_b64 v[54:55], v20 offset:6144\n\t"
    "ds_read_b64 v[56:57], v20 offset:8192\n\t"
    "ds_read_b64 v[58:59], v20 offset:10240\n\t"
    "ds_read_b64 v[60:61], v20 offset:12288\n\t"
    "ds_read_b64 v[62:63], v20 offset:14336\n\t"
    "s_waitcnt lgkmcnt(6)\n\t"
    "v_pk_add_f32 v[48:49], v[48:49], v[50:51]\n\t"
    "s_waitcnt lgkmcnt(4)\n\t"
    "v_pk_add_f32 v[52:53], v[52:53], v[54:55]\n\t"
    "v_pk_add_f32 v[48:49], v[48:49], v[52:53]\n\t"
    "s_waitcnt lgkmcnt(2)\n\t"
    "v_pk_add_f32 v[56:57], v[56:57], v[58:59]\n\t"
    "s_waitcnt lgkmcnt(0)\n\t"
    "v_pk_add_f32 v[60:61], v[60:61], v[62:63]\n\t"
    "v_pk_add_f32 v[56:57], v[56:57], v[60:61]\n\t"
    "v_pk_add_f32 v[48:49], v[48:49], v[56:57]\n\t"
    "v_cvt_pk_bf16_f32 v22, v48, v49\n\t"
    "v_min_u32 v22, -2, v22\n\t"
    "global_store_dword v21, v22, %[vd] sc1\n\t"
    "s_waitcnt lgkmcnt(0)\n\t"
    "s_branch 9f\n\t"
    "1:\n\t"
    "s_add_u32 %[sp], %[sp], 1\n\t"
    "s_cmp_ge_u32 %[sp], %[cap]\n\t"
    "s_cbranch_scc1 7f\n\t"
    "global_load_dwordx4 v[24:27], v17, %[ag] sc1\n\t"
    "global_load_dwordx4 v[28:31], v17, %[ag] offset:1024 sc1\n\t"
    "s_waitcnt lgkmcnt(0)\n\t"
    "s_memtime %[tc]\n\t"
    "s_nop 7\n\t"
    "s_branch 3b\n\t"
    "7:\n\t"
    "s_mov_b32 %[cap], 0\n\t"
    "s_branch 2b\n\t"
    "9:"
    : [tc] "=&s"(tc), [sp] "=&s"(sp), [cap] "+s"(cap)
    : [ag] "s"(ag), [vd] "s"(vd), [wa] "s"(wa), [ra] "s"(ra), [c44] "s"(0x4444u), "{v[64:95]}"(W.v0), "{v[96:127]}"(W.v1), "{v[128:159]}"(W.o0), "{v[160:191]}"(W.o1), "{v[192:223]}"(W.o2), "{v[224:255]}"(W.o3)
    : "v16", "v17", "v18", "v19", "v20", "v21", "v22", "v23", "v24", "v25", "v26", "v27", "v28", "v29", "v30", "v31", "v32", "v33", "v34", "v35", "v36", "v37", "v38", "v39", "v40", "v41", "v42", "v43", "v44", "v45", "v46", "v47", "v48", "v49", "v50", "v51", "v52", "v53", "v54", "v55", "v56", "v57", "v58", "v59", "v60", "v61", "v62", "v63", "vcc", "scc", "memory");
}
__device__ __forceinline__ void vr_r(const VRW& W, const char* os, const char* ad, unsigned wa, unsigned& cap, unsigned& sp, unsigned long long* ts) {
  asm volatile("v_mbcnt_lo_u32_b32 v16, -1, 0\n\t"
    "v_mbcnt_hi_u32_b32 v16, -1, v16\n\t"
    "v_lshlrev_b32 v17, 4, v16\n\t"
    "v_add_u32 v18, 0x1000, v17\n\t"
    "global_load_dwordx4 v[24:27], v17, %[os] offset:0 sc1\n\t"
    "global_load_dwordx4 v[28:31], v17, %[os] offset:2048 sc1\n\t"
    "global_load_dwordx4 v[32:35], v18, %[os] offset:0 sc1\n\t"
    "global_load_dwordx4 v[36:39], v18, %[os] offset:2048 sc1\n\t"
    "s_mov_b32 %[sp], 0\n\t"
    "v_and_b32 v23, 4, v16\n\t"
    "v_mul_u32_u24 v23, 0x2aaa, v23\n\t"
    "v_lshrrev_b32 v19, 1, v16\n\t"
    "v_and_b32 v19, 2, v19\n\t"
    "v_or3_b32 v19, v19, v23, %[c44]\n\t"
    "v_and_b32 v23, 15, v16\n\t"
    "v_lshl_add_u32 v20, v23, 2, %[wa]\n\t"
    "v_and_b32 v23, 48, v16\n\t"
    "v_lshl_add_u32 v20, v23, 3, v20\n\t"
    "v_and_b32 v23, 32, v16\n\t"
    "v_and_b32 v21, 28, v16\n\t"
    "v_lshlrev_b32 v21, 4, v21\n\t"
    "v_or_b32 v21, v21, v23\n\t"
    "v_lshlrev_b32 v22, 3, v23\n\t"
    "v_and_b32 v23, 3, v16\n\t"
    "v_lshl_or_b32 v21, v23, 3, v21\n\t"
    "v_and_b32 v23, 31, v16\n\t"
    "v_lshl_or_b32 v22, v23, 2, v22\n\t"
    "3:\n\t"
    "v_mov_b32 v40, 0\n\t"
    "v_mov_b32 v41, 0\n\t"
    "v_mov_b32 v42, 0\n\t"
    "v_mov_b32 v43, 0\n\t"
    "v_mov_b32 v44, 0\n\t"
    "v_mov_b32 v45, 0\n\t"
    "v_mov_b32 v46, 0\n\t"
    "v_mov_b32 v47, 0\n\t"
    "v_mov_b32 v48, 0\n\t"
    "v_mov_b32 v49, 0\n\t"
    "v_mov_b32 v50, 0\n\t"
    "v_mov_b32 v51, 0\n\t"
    "v_mov_b32 v52, 0\n\t"
    "v_mov_b32 v53, 0\n\t"
    "v_mov_b32 v54, 0\n\t"
    "v_mov_b32 v55, 0\n\t"
    "v_mov_b32 v56, 0\n\t"
    "s_waitcnt vmcnt(3)\n\t"
    "v_smfmac_f32_16x16x64_bf16 v[44:47], v[24:27], v[144:151], v19\n\t"
    "v_max3_u32 v56, v56, v24, v25\n\t"
    "v_smfmac_f32_16x16x64_bf16 v[48:51], v[24:27], v[136:143], v19\n\t"
    "v_max3_u32 v56, v56, v26, v27\n\t"
    "v_smfmac_f32_16x16x64_bf16 v[40:43], v[24:27], v[128:135], v19\n\t"
    "v_smfmac_f32_16x16x64_bf16 v[52:55], v[24:27], v[152:159], v19\n\t"
    "s_waitcnt vmcnt(2)\n\t"
    "v_smfmac_f32_16x16x64_bf16 v[44:47], v[28:31], v[176:183], v19\n\t"
    "v_max3_u32 v56, v56, v28, v29\n\t"
    "v_smfmac_f32_16x16x64_bf16 v[48:51], v[28:31], v[168:175], v19\n\t"
    "v_max3_u32 v56, v56, v30, v31\n\t"
    "v_smfmac_f32_16x16x64_bf16 v[40:43], v[28:31], v[160:167], v19\n\t"
    "v_smfmac_f32_16x16x64_bf16 v[52:55], v[28:31], v[184:191], v19\n\t"
    "s_waitcnt vmcnt(1)\n\t"
    "v_smfmac_f32_16x16x64_bf16 v[44:47], v[32:35], v[208:215], v19\n\t"
    "v_max3_u32 v56, v56, v32, v33\n\t"
    "v_smfmac_f32_16x16x64_bf16 v[48:51], v[32:35], v[200:207], v19\n\t"
    "v_max3_u32 v56, v56, v34, v35\n\t"
    "v_smfmac_f32_16x16x64_bf16 v[40:43], v[32:35], v[192:199], v19\n\t"
    "v_smfmac_f32_16x16x64_bf16 v[52:55], v[32:35], v[216:223], v19\n\t"
    "s_waitcnt vmcnt(0)\n\t"
    "v_smfmac_f32_16x16x64_bf16 v[44:47], v[36:39], v[240:247], v19\n\t"
    "v_max3_u32 v56, v56, v36, v37\n\t"
    "v_smfmac_f32_16x16x64_bf16 v[48:51], v[36:39], v[232:239], v19\n\t"
    "v_max3_u32 v56, v56, v38, v39\n\t"
    "v_smfmac_f32_16x16x64_bf16 v[40:43], v[36:39], v[224:231], v19\n\t"
    "v_cmp_eq_u32 vcc, -1, v56\n\t"
    "v_smfmac_f32_16x16x64_bf16 v[52:55], v[36:39], v[248:255], v19\n\t"
    "s_cbranch_vccnz 4f\n\t"
    "5:\n\t"
    "s_nop 2\n\t"
    "v_permlane32_swap_b32 v44, v48\n\t"
    "v_permlane32_swap_b32 v45, v49\n\t"
    "v_permlane32_swap_b32 v46, v50\n\t"
    "v_permlane32_swap_b32 v47, v51\n\t"
    "s_mov_b32 exec_hi, 0\n\t"
    "v_pk_add_f32 v[40:41], v[40:41], v[48:49]\n\t"
    "v_pk_add_f32 v[42:43], v[42:43], v[50:51]\n\t"
    "s_mov_b64 exec, %[hi]\n\t"
    "v_pk_add_f32 v[40:41], v[44:45], v[52:53]\n\t"
    "v_pk_add_f32 v[42:43], v[46:47], v[54:55]\n\t"
    "s_mov_b32 exec_lo, -1\n\t"
    "s_nop 0\n\t"
    "v_permlane16_swap_b32 v40, v42\n\t"
    "v_permlane16_swap_b32 v41, v43\n\t"
    "v_add_f32 v40, v40, v42\n\t"
    "v_add_f32 v41, v41, v43\n\t"
    "ds_write_b32 v20, v40 offset:16384\n\t"
    "ds_write_b32 v20, v41 offset:16448\n\t"
    "s_cmp_ge_u32 %[wa], 1024\n\t"
    "s_waitcnt lgkmcnt(0)\n\t"
    "s_barrier\n\t"
    "s_cbranch_scc1 9f\n\t"
    "ds_read_b64 v[24:25], v21 offset:16384\n\t"
    "ds_read_b64 v[26:27], v21 offset:16896\n\t"
    "ds_read_b64 v[28:29], v21 offset:17408\n\t"
    "ds_read_b64 v[30:31], v21 offset:17920\n\t"
    "ds_read_b64 v[32:33], v21 offset:18432\n\t"
    "ds_read_b64 v[34:35], v21 offset:18944\n\t"
    "ds_read_b64 v[36:37], v21 offset:19456\n\t"
    "ds_read_b64 v[38:39], v21 offset:19968\n\t"
    "s_waitcnt lgkmcnt(6)\n\t"
    "v_pk_add_f32 v[24:25], v[24:25], v[26:27]\n\t"
    "s_waitcnt lgkmcnt(4)\n\t"
    "v_pk_add_f32 v[28:29], v[28:29], v[30:31]\n\t"
    "v_pk_add_f32 v[24:25], v[24:25], v[28:29]\n\t"
    "s_waitcnt lgkmcnt(2)\n\t"
    "v_pk_add_f32 v[32:33], v[32:33], v[34:35]\n\t"
    "s_waitcnt lgkmcnt(0)\n\t"
    "v_pk_add_f32 v[36:37], v[36:37], v[38:39]\n\t"
    "v_pk_add_f32 v[32:33], v[32:33], v[36:37]\n\t"
    "v_pk_add_f32 v[24:25], v[24:25], v[32:33]\n\t"
    "v_cvt_pk_bf16_f32 v23, v24, v25\n\t"
    "v_min_u32 v23, -2, v23\n\t"
    "global_store_dword v22, v23, %[ad] sc1\n\t"
    "s_waitcnt lgkmcnt(0)\n\t"
    "s_branch 9f\n\t"
    "4:\n\t"
    "s_add_u32 %[sp], %[sp], 1\n\t"
    "s_cmp_ge_u32 %[sp], %[cap]\n\t"
    "s_cbranch_scc1 7f\n\t"
    "global_load_dwordx4 v[24:27], v17, %[os] offset:0 sc1\n\t"
    "global_load_dwordx4 v[28:31], v17, %[os] offset:2048 sc1\n\t"
    "global_load_dwordx4 v[32:35], v18, %[os] offset:0 sc1\n\t"
    "global_load_dwordx4 v[36:39], v18, %[os] offset:2048 sc1\n\t"
    "s_nop 7\n\t"
    "s_branch 3b\n\t"
    "7:\n\t"
    "s_mov_b32 %[cap], 0\n\t"
    "s_branch 5b\n\t"
    "9:"
    : [sp] "=&s"(sp), [cap] "+s"(cap)
    : [os] "s"(os), [ad] "s"(ad), [wa] "s"(wa), [c44] "s"(0x4444u), [hi] "s"(0xFFFFFFFF00000000ull), "{v[64:95]}"(W.v0), "{v[96:127]}"(W.v1), "{v[128:159]}"(W.o0), "{v[160:191]}"(W.o1), "{v[192:223]}"(W.o2), "{v[224:255]}"(W.o3)
    : "v16", "v17", "v18", "v19", "v20", "v21", "v22", "v23", "v24", "v25", "v26", "v27", "v28", "v29", "v30", "v31", "v32", "v33", "v34", "v35", "v36", "v37", "v38", "v39", "v40", "v41", "v42", "v43", "v44", "v45", "v46", "v47", "v48", "v49", "v50", "v51", "v52", "v53", "v54", "v55", "v56", "v57", "v58", "v59", "v60", "v61", "v62", "v63", "vcc", "scc", "memory");
}
__device__ __forceinline__ void qk_body(char* smem, int wave_id_, int xg, int h, int wg, const u16* __restrict__ Wq, const u16* __restrict__ Wk,
                                        const char* __restrict__ Kx, const char* __restrict__ V, char* ws, unsigned* tmo, u64 tag) {
  const int wave = WAVE_ID;
  const int ty = wave >> 2, w4 = wave & 3;
  const char* wq = uni((const char*)(Wq + h * DH)); const char* wk = uni((const char*)(Wk + h * DH));
  auto stage = [&](int u) {
    stage_proj((u & 1) ? wk + (size_t)(((u >> 1) + 2) & 7) * 128 * HD * 2 : wq + (size_t)(u >> 1) * 128 * HD * 2, ((u + 4) % 5) * 32768, wave * 64 + lane_id(), wave);
  };
  QKW W;
  if (wave == 0) STS(ws, wg, 7);
  stage(0); stage(1); stage(2); stage(3);
#define WLD(i, w) do { WAIT_V(12); WBAR(); stage(2 * (i) + 4); if (ty) { WAIT_V(12); WBAR(); stage(2 * (i) + 5); } \
    { const int l_ = lane_id(); const unsigned o_ = ty ? ((2 * (i) + 5) % 5) * 32768 : ((2 * (i) + 4) % 5) * 32768; qk_wload<i>(w, proj_base(2 * w4, l_) + o_, proj_base(2 * w4 + 1, l_) + o_); } \
    if (!ty) { WAIT_V(12); WBAR(); stage(2 * (i) + 5); } if (wave == 0) STS(ws, wg, 1 + (i)); } while (0)
  WLD(0, W.w0); WLD(1, W.w1); WLD(2, W.w2); WLD(3, W.w3); WLD(4, W.w4); WLD(5, W.w5);
#undef WLD
  unsigned in = 0;
#pragma unroll
  for (int i = 0; i < 2; ++i) {
    if (i == 0) WAIT_V(8); else WAIT_V(0);
    WBAR();
    if (i == 0 && wave == 1) init_issue(ws, Q_V);
    if (i == 1) in = init_get(ws, tag, smem + Q_V);
    const int lane = lane_id();
    const unsigned o = Q_RING + ty * 32768 + i * 65536, f0 = proj_base(2 * w4, lane) + o, f1 = proj_base(2 * w4 + 1, lane) + o;
    u32x8 x[4] = {proj_frag(smem, f0), proj_frag(smem, f1), proj_frag(smem, f0 + 16384), proj_frag(smem, f1 + 16384)};
    asm volatile("" : "+v"(x[0]), "+v"(x[1]), "+v"(x[2]), "+v"(x[3]));
    BAR();
    char* d = smem + Q_CB + i * 57344 + ty * (i ? 32768 : 24576) + w4 * 2048 + lane * 16;
#pragma unroll
    for (int j = 0; j < 4; ++j)
      if (i || j || !ty) { *(u32x4*)(d + j * 8192) = u32x4{x[j][0], x[j][1], x[j][2], x[j][3]}; *(u32x4*)(d + j * 8192 + 1024) = u32x4{x[j][4], x[j][5], x[j][6], x[j][7]}; }
    if (i == 0 && ty) { *(u32x4*)(smem + Q_MEM + wave * 2048 + lane * 16) = u32x4{x[0][0], x[0][1], x[0][2], x[0][3]}; *(u32x4*)(smem + Q_MEM + wave * 2048 + 1024 + lane * 16) = u32x4{x[0][4], x[0][5], x[0][6], x[0][7]}; }
  }
  if (wave == 4) *(volatile LAS u32x2*)(smem + Q_TM) = u32x2{0u, 0u};
  BAR();
  if (wave == 0) STS(ws, wg, 0);
  unsigned* cnt = cnt_of(ws, in);
  const char* xb = uni(xch_base(ws) + ((size_t)3 << 21) + (size_t)((in & 2u) << 12));
  const char* oh = uni(xch_base(ws) + (size_t)((in & 1u) << 13) + (size_t)(h >> 2) * 16384 + (h & 3) * 2048);
  auto rearm = [&](int ps) {
    unsigned z = SENT; asm volatile("" : "+v"(z));
    const unsigned l16 = lane_id() * 16;
#pragma unroll
    for (int r = 0; r < 2 * OREP; ++r) st16s(oh + (size_t)(ps * 4 + (r >> 1)) * 131072, (r & 1) * 1024u + l16, u32x4{z, z, z, z});
  };
  const char* dbw = xb + (size_t)48 * 16384 + 4096 + h * 128;
  unsigned avail = 0;
  auto stage_kv = [&](int s) {
    if (avail <= (unsigned)s) {
      for (int spins = 0;; ++spins) {
        const unsigned p = ld4_sc1(cnt + (PQKV + h) * 32 + (lane_id() & 15));
        avail = min(qkv_avail(p, 1), qkv_avail(p, 2));
        if (avail > (unsigned)s) break;
        __builtin_amdgcn_s_sleep(8);
        if (give_up(spins, tmo, 5)) break;
      }
    }
    const char* ks = uni(Kx + ((size_t)s * NH + h) * 8192); const char* vs = uni(V + ((size_t)s * NH + h) * 8192);
    const int lane = lane_id();
    head_to_lds(ks, lane, Q_K); head_to_lds(vs, lane, Q_V);
  };
  auto loop = [&](auto w_c) __attribute__((always_inline)) {
    constexpr int WV = decltype(w_c)::value;
    if (WV >= 4) asm volatile("ds_read_b128 v[56:59], %1\n\tds_read_b128 v[60:63], %1 offset:1024\n\ts_waitcnt lgkmcnt(0)" : "=&{v[56:63]}"(W.x) : "v"(Q_MEM + WV * 2048 + lane_id() * 16) : "memory");
    if (WV == 6) stage_kv(0);
    if (WV < 4) __builtin_amdgcn_s_setprio(2);
    unsigned t0 = 0, cap = 60000, d0 = 105, d1 = 200;
    int dq = D_Q;
    int sl = 0;
    for (int s = 0; s < NSEG; ++s) {
      TSDECL;
      const char* ag = xb + (size_t)((sl * 4 + (AREP == 4 ? xg : xg >> 1)) * 2 + (WV >> 2)) * 16384 + (WV & 3) * 2048;
      const char* vs = WV == 4 ? dbw : WV == 5 ? ws + OFF_KVM + ((size_t)s * NH + h) * 4096 : xb + (size_t)(24 + sl * 8 + (h >> 2)) * 16384 + (h & 3) * 2048;
      const char* od = WV == 4 ? (const char*)(size_t)((unsigned)(s + 1) << 24) : oh + (size_t)sl * 524288;
      if (s > 0) wait_rt(t0, dq);
      TS(0);
      unsigned long long tc; unsigned sp;
      qk_step<WV>(W, ag, vs, od, ws + OFF_KVM + ((size_t)s * NH + h) * 4096 + 2048, cap, sp, tc, nullptr, d0, d1);
      dq = (sp & 0xFFFFu) ? min(dq + DQU, D_Q + DQM) : max(dq - DQD, D_Q);
      if (WV < 4 || WV == 5) {
        const unsigned pv = (unsigned)(s + 1) << 24 | (rt_now() & 0xFFFFFFu);
        WAIT_V(0);
        if (lane_id() == 0) st4_sc1(cnt + (PROG + h) * 32 + WV, pv);
      }
      if (WV == 6 && s + 1 < NSEG - 1) stage_kv(s + 1);
      if (WV == 7 && s > 0 && s < NSEG - 1) rearm(sl == 0 ? 2 : sl - 1);
      if (WV == 7 && s >= 8 && s < 8 + NSEG - SDIR) {
        unsigned z = SENT; asm volatile("" : "+v"(z));
        const char* kd = ws + OFF_KVM + ((size_t)(s - 8 + SDIR) * NH + h) * 4096;
#pragma unroll
        for (int r = 0; r < 4; ++r) st16s(kd, r * 1024u + lane_id() * 16, u32x4{z, z, z, z});
      }
      if (s < NSEG - 1) {
        if (WV == 4) {
          delay64<E_B>();
          t0 = bell_time(xb + (size_t)(48 + xg) * 16384 + 4096, (lane_id() & 31) * 128, (unsigned)(s + 1), cap);
          *(volatile LAS u32x2*)(smem + Q_TM) = u32x2{(unsigned)(s + 1), t0};
        } else {
          for (;;) {
            const u32x2 v = *(volatile LAS u32x2*)(smem + Q_TM);
            t0 = __builtin_amdgcn_readfirstlane(v.y);
            if (__builtin_amdgcn_readfirstlane(v.x) == (unsigned)(s + 1)) break;
            __builtin_amdgcn_s_sleep(1);
          }
        }
      }
      sl = sl == 2 ? 0 : sl + 1;
    }
    if (cap == 0 && lane_id() == 0) st4_sc1(tmo, 1u);
  };
  switch (wave) {
    case 0: loop(std::integral_constant<int, 0>{}); break;
    case 1: loop(std::integral_constant<int, 1>{}); break;
    case 2: loop(std::integral_constant<int, 2>{}); break;
    case 3: loop(std::integral_constant<int, 3>{}); break;
    case 4: loop(std::integral_constant<int, 4>{}); break;
    case 5: loop(std::integral_constant<int, 5>{}); break;
    case 6: loop(std::integral_constant<int, 6>{}); break;
    default: loop(std::integral_constant<int, 7>{}); break;
  }
  __builtin_amdgcn_s_setprio(0);
}

__device__ __forceinline__ void vr_body(char* smem, int wave_id_, int xg, int h, int c, int r, int wg, const u16* __restrict__ Wv, const u16* __restrict__ Wo,
                                        char* ws, unsigned* tmo, u64 tag) {
  const int wave = WAVE_ID;
  const unsigned po = (unsigned)(r >> 5) * 16384u + ((r >> 2) & 7) * 1024 + (r & 1) * 512 + ((r >> 1) & 1) * 128;
  const unsigned vo_ = (unsigned)(24 + (h >> 2)) * 16384u + (h & 3) * 2048 + c * 1024;
  const int hg = (wave + ROTW * (r & 3)) & 7;
  VRW W;
  {
    const int lane = lane_id();
    const int g = lane >> 4, q4 = (lane & 15) >> 2, p4 = lane & 3, fr = 4 * g + (q4 & 1) + 32 * (q4 >> 1);
    const unsigned lw = wave * 16384;
    const char* sv_ = uni((const char*)(Wv + (size_t)(128 * wave) * HD + h * DH + 64 * c));
    const char* so = uni((const char*)(Wo + (size_t)(512 * hg) * DM + 64 * (r >> 2) + 16 * (r & 1)));
    const unsigned vo = (unsigned)((32 * (lane >> 5) + ((lane >> 2) & 7)) * DM * 2 + ((lane >> 1) & 1) * 64 + (lane & 1) * 16);
    auto issue = [&](int p) {
#pragma unroll
      for (int i = 0; i < 8; ++i) {
        if (p >= 2 && (NTM & 8)) gldsw(so + (size_t)(128 * (p - 2) + 64 * (i & 1) + 8 * (i >> 1)) * DM * 2, vo, lw + (p & 1) * 8192 + i * 1024);
        else if (p >= 2) glds16s<0>(so + (size_t)(128 * (p - 2) + 64 * (i & 1) + 8 * (i >> 1)) * DM * 2, vo, lw + (p & 1) * 8192 + i * 1024);
        else gldsw(sv_ + (size_t)(8 * p + i) * 8 * HD * 2, (unsigned)((lane >> 3) * HD * 2 + (lane & 7) * 16), lw + (p & 1) * 8192 + i * 1024);
      }
    };
#define VTS(p) do { } while (0)
    if (wave == 0) STS(ws, wg, 7);
    issue(0); issue(1);
    const unsigned av = lw + (unsigned)(fr * 128 + p4 * 8), ao = lw + (unsigned)(fr * 64 + p4 * 8);
    WAIT_V(8); VTS(0); vr_wload<0>(W.v0, av); issue(2);
    WAIT_V(8); VTS(1); vr_wload<1>(W.v1, av); issue(3);
    WAIT_V(8); VTS(2); vr_wload<2>(W.o0, ao); issue(4);
    WAIT_V(8); vr_wload<3>(W.o1, ao); issue(5);
    if (wave == 0) init_issue(ws, 131072);
    WAIT_V(8); vr_wload<4>(W.o2, ao);
    WAIT_V(0); VTS(3); vr_wload<5>(W.o3, ao);
#undef VTS
  }
  if (wave == 0) STS(ws, wg, 5);
  if (wave == 0) STS(ws, wg, 6);
  BAR();
  if (wave == 0) STS(ws, wg, 0);
  const unsigned in = init_get(ws, tag, smem + 131072);
  const char* xb = uni(xch_base(ws) + ((size_t)3 << 21) + (size_t)((in & 2u) << 12));
  const char* ob = uni(xch_base(ws) + (size_t)((in & 1u) << 13));
  auto rearm = [&](int ps, bool ag) {
    const int lane = lane_id();
    unsigned z = SENT; asm volatile("" : "+v"(z));
    const u32x4 sv = u32x4{z, z, z, z};
    if (ag && lane < 16 * AREP) st16s(xb, (unsigned)(ps * 8 + (lane >> 4) * 2) * 16384u + po + ((lane >> 3) & 1) * 256 + (lane & 7) * 16, sv);
    st16s(xb, (unsigned)ps * 131072u + vo_ + lane * 16, sv);
  };
  __builtin_amdgcn_s_setprio(2);
  unsigned cap = 60000;
  int sl = 0;
  for (int s = 0; s < NSEG; ++s) {
    TSDECL;
    const int sn = sl == 2 ? 0 : sl + 1;
    const char* ag = xb + (size_t)((sl * 4 + (AREP == 4 ? xg : xg >> 1)) * 2 + (wave >> 2)) * 16384 + (wave & 3) * 2048;
    const char* vd = xb + (size_t)sl * 131072 + vo_ + (wave & 2) * 256 + (wave & 1) * 128;
    const char* os = ob + (size_t)((sl * 4 + (OREP == 4 ? xg : ORSEL)) * 8 + hg) * 16384 + ((r >> 1) & 1) * 1024;
    const char* ad = xb + (size_t)((sn * 4 + (wave & 3)) * 2) * 16384 + po;
    if (s > 0) delay64<E_AG>();
    TS(0);
    unsigned long long tc; unsigned sp;
    unsigned long long* tsp = nullptr;
    vr_v(W, ag, vd, wave * 2048, (wave & 3) * 512, cap, sp, tc, tsp);
    TN(12, sp + 1);
    if (s == NSEG - 1) break;
    if (wave == 7 && s > 0) rearm(sl == 0 ? 2 : sl - 1, true);
    wait_clk((unsigned)tc, E_O);
    TS(3);
    const unsigned tb = bell_time(xb + (size_t)(48 + xg) * 16384 + hg * 512, (lane_id() & 3) * 128, (unsigned)(s + 1), cap);
    wait_rt(tb, D_O);
    TS(4);
    vr_r(W, os, ad, wave * 512, cap, sp, tsp);
    TN(14, (sp & 0xFFFF) + 1);
    if (wave == 1) TSFLUSH(0x6078);
    sl = sn;
  }
  __builtin_amdgcn_s_setprio(0);
  if (cap == 0 && lane_id() == 0) st4_sc1(tmo, 2u);
}
struct ModIn { u32x4 w[8]; unsigned cv; };
__device__ __forceinline__ void mod_issue(ModIn& I, int wave_id_, int j, const u16* __restrict__ c, const u16* __restrict__ wmod) {
  const int tid = opaque_tid(), k0 = (j >> 5) * 512 + (tid >> 3);
  I.cv = c[k0 + 64 * (tid & 7)];
#pragma unroll
  for (int i = 0; i < 8; ++i) I.w[i] = ldnt<1>(wmod + (size_t)(k0 + 64 * i) * (6 * DM) + (j & 31) * 64 + (tid & 7) * 8);
}
__device__ __forceinline__ void mod_job(char* smem, int wave_id_, int j, u64 tag, const ModIn& I, char* ws) {
  const int tid = opaque_tid(), lane = tid & 63, wave = WAVE_ID, cg = tid & 7;
  const float cv = bflo(I.cv), sl = bfr(cv / (1.0f + __expf(-cv)));
  float acc[8] = {0, 0, 0, 0, 0, 0, 0, 0};
#pragma unroll
  for (int i = 0; i < 8; ++i) {
    const float sv = __shfl(sl, (lane & 56) + i);
#pragma unroll
    for (int e = 0; e < 4; ++e) { acc[2 * e] += sv * bflo(I.w[i][e]); acc[2 * e + 1] += sv * bfhi(I.w[i][e]); }
  }
#pragma unroll
  for (int e = 0; e < 8; ++e) acc[e] = red16_32_add(add_ror8(acc[e]));
  float* red = (float*)(smem + L_GB);
  if (lane < 8) { *(f32x4*)(red + wave * 64 + cg * 8) = f32x4{acc[0], acc[1], acc[2], acc[3]}; *(f32x4*)(red + wave * 64 + cg * 8 + 4) = f32x4{acc[4], acc[5], acc[6], acc[7]}; }
  __syncthreads();
  if (tid < 32) {
    f32x2 s = f32x2{0, 0};
#pragma unroll
    for (int r = 0; r < 8; ++r) s += *(const f32x2*)(red + r * 64 + tid * 2);
    st16_sc1(ws + OFF_MOD + (j * 32 + tid) * 16, u32x4{__float_as_uint(s[0]), (unsigned)tag, __float_as_uint(s[1]), tag2(tag)});
  }
  BAR();
}
#define NIMM 3
constexpr unsigned TK(int m, int hl, int part, int bo, int be) { return (unsigned)(m | hl << 2 | part << 5 | bo << 8 | be << 16); }
#define QX 31
#define C1 42
#define C2 45
#define C3 C2
constexpr unsigned task_tab(int t, int it) {
  return t >= 8 && t < 13 ? (it == 0 ? TK(1, t - 8, 0, 0, 64) : it == 1 ? TK(2, t - 8, 1, C1, 64) : 0)
       : it > 0 ? 0
       : t < 8 ? TK(0, t, 1, QX, 64)
       : t < 16 ? TK(1, t - 8, 0, 0, t == 13 ? C2 : C3)
       : t < 24 ? TK(2, t - 16, 0, 0, t < 22 ? C1 : C2)
       : t == 24 ? TK(2, 5, 1, C1, 64) : t == 25 ? TK(1, 5, 1, C2, 64)
       : t == 26 ? TK(1, 6, 1, C3, (C3 + 64) / 2) : t == 27 ? TK(1, 6, 2, (C3 + 64) / 2, 64)
       : t < 30 ? TK(2, t - 22, 1, C2, 64)
       : t == 30 ? TK(1, 7, 1, C3, (C3 + 64) / 2) : t == 31 ? TK(1, 7, 2, (C3 + 64) / 2, 64)
       : TK(0, t - 32, 0, 0, QX);
}
template <int IT, int I = 0> __device__ __forceinline__ unsigned task_sel(int t) {
  if constexpr (I == 40) return 0u;
  else { unsigned r = task_sel<IT, I + 1>(t); constexpr unsigned c = task_tab(I, IT); if (c) asm volatile("s_cmp_eq_u32 %1, %2\n\ts_cselect_b32 %0, %3, %0" : "+s"(r) : "s"(t), "i"(I), "i"(c) : "scc"); return r; }
}
__global__ void __launch_bounds__(512, 2) k_mega(const u16* __restrict__ Wq, const u16* __restrict__ Wk, const u16* __restrict__ Wv,
                                                 const u16* __restrict__ Wo, const u16* __restrict__ w1, const u16* __restrict__ w3,
                                                 const u16* __restrict__ w2, const u16* __restrict__ x, const u16* __restrict__ cvec,
                                                 const u16* __restrict__ wmod, const u16* __restrict__ mem0, char* ws, u16* out) {
  extern __shared__ __attribute__((aligned(16))) char smem[];
  int wave_id_ = __builtin_amdgcn_readfirstlane((int)threadIdx.x >> 6);
  unsigned bid = blockIdx.x;
  asm volatile("" : "+s"(wave_id_), "+s"(bid));
  unsigned* tmo = (unsigned*)(ws + OFF_TMO);
  const unsigned tst = (unsigned)__builtin_amdgcn_s_memrealtime();
  unsigned long long* mts = nullptr;
  const float* mod = (const float*)(ws + OFF_MOD);
  volatile LAS unsigned* tslot = (volatile LAS unsigned*)(smem + L_SLOT + 768);
#define CPF 131072
  if (CPF && wave_id_ == 1) {
    u64 pc; asm volatile("s_getpc_b64 %0" : "=s"(pc));
    const unsigned o = (((bid >> 3) & 31) * 64 + lane_id()) * 64;
    if (o < CPF) asm volatile("s_mov_b32 m0, %0\n\ts_nop 0\n\tglobal_load_lds_dword %1, %2" :: "s"(155648 + 1024), "v"(o), "s"(pc & ~(u64)127) : "memory", "m0");
  }
  unsigned xcc; asm volatile("s_getreg_b32 %0, hwreg(HW_REG_XCC_ID)" : "=s"(xcc));
  xcc &= 7;
  const unsigned pred = xcc * 32 + ((bid >> 3) & 31);
  ModIn mi;
  auto modj = [](unsigned sl) { return sl >= 128 && ((sl + 8) & 31) < 16 ? (int)(((sl >> 5) & 3) * 16 + (sl & 15)) : -1; };
  if (modj(pred) >= 0) mod_issue(mi, wave_id_, modj(pred), cvec, wmod);
  u64 tag;
  asm volatile("s_load_dwordx2 %0, %1, 0x0\n\ts_waitcnt lgkmcnt(0)" : "=s"(tag) : "s"(ws + OFF_TAG) : "memory");
  tag += 1;
  if (opaque_tid() == 0) {
    unsigned sl = pred;
    const unsigned s0 = sl;
    for (unsigned i = 1; i <= 256; ++i) {
      if (__hip_atomic_exchange((u64*)(ws + OFF_CLAIM + sl * 128), tag, __ATOMIC_RELAXED, __HIP_MEMORY_SCOPE_AGENT) != tag) break;
      sl = i == 256 ? 0xFFFFu : (((s0 + (i & ~31u)) & 0xE0u) | ((s0 + i) & 31u));
    }
    *tslot = sl;
  }
  BAR();
  const int slot = (int)__builtin_amdgcn_readfirstlane(*tslot);
  BAR();
  if (slot >= 256) return;
  if (modj(slot) >= 0) {
    if (slot != (int)pred) mod_issue(mi, wave_id_, modj(slot), cvec, wmod);
    mod_job(smem, wave_id_, modj(slot), tag, mi, ws);
  }
  unsigned* cnt = nullptr;
  const int xg = slot >> 5, t = xg == 3 ? 24 + (slot & 7) : slot & 31;
  if (xg < 3) {
    const int nq = xg == 2 ? 8 : 12;
    if (t < nq) { const int h = xg * 12 + t; qk_body(smem, wave_id_, xg, h, (h >> 3) * 32 + (h & 7), Wq, Wk, ws + OFF_K, ws + OFF_V, ws, tmo, tag); }
    else { const int r = xg * 20 + t - nq; vr_body(smem, wave_id_, xg, r >> 1, r & 1, r, (r >> 4) * 32 + 8 + (r & 15), Wv, Wo, ws, tmo, tag); }
    return;
  }
  const bool cx = xg == 3;
  const int xq = cx ? (slot >> 3) & 3 : xg & 3, wsl = xq * 32 + t;
#define GDEL 1200
#define GDX 1200
  if (cx || t < 8) l1_role(smem, wave_id_, cx, xq * 8 + (t & 7), x, mod, mem0, ws + OFF_H1, cnt, tag, tmo, mts);
  if (t < 8 || t >= 24) {
    while ((unsigned)__builtin_amdgcn_s_memrealtime() - tst < (cx ? GDX : GDEL)) __builtin_amdgcn_s_sleep(8);
    if (!cx && t >= 24) {
      cnt = cnt_of(ws, init_wait(ws, tag));
      mod2_job(smem, wave_id_, xq * 8 + t - 24, mod, cvec, wmod, cnt);
    }
  }
  auto u2_of = [&]() { return cx ? -1 : (t == 22 ? 0 : (t == 23 ? 6 : (t >= 13 && t < 16 ? t - 11 : (t == 24 ? 5 : (t == 25 ? 1 : -1))))); };
  auto f_of = [&]() {
    int fl = t < 8 ? 6 + t : t - 16;
    const int sa = (3 + 2 * xq) & 7, sb = sa + 8 < 14 ? sa + 8 : 13;
    if (fl == 12) fl = sa; else if (fl == sa) fl = 12;
    else if (fl == 13) fl = sb; else if (fl == sb) fl = 13;
    return (int)__builtin_amdgcn_readfirstlane(14 * xq + fl);
  };
  unsigned tk = task_sel<0>(cx ? t + 8 : t), t2 = task_sel<1>(cx ? t + 8 : t), t3 = task_sel<2>(cx ? t + 8 : t);
  for (int it = 0; tk != 0; ++it, tk = t2, t2 = t3, t3 = 0) {
    const int m = tk & 3, hl = (tk >> 2) & 7, part = (tk >> 5) & 3, bo = (tk >> 8) & 255, ns = (int)(tk >> 16) - bo, h = 8 * xq + hl;
    const bool first = m != 0 && bo == 0, late = it > 0;
    const int lane = lane_id();
    const Dep d{nullptr, (unsigned)((lane < 32 ? PL1 : PL1B - 32) + lane) * 128, 8u, (unsigned)((lane < 32 ? 0 : 8) - bo)};
    if (wave_id_ == 0 && late) wait_l1(cnt + (PL1B - PL1) * 32, 7u, tmo);
    BAR();
    gemm7<R_QKV>(smem, wave_id_, (m == 0 ? Wq : (m == 1 ? Wk : Wv)) + h * DH, nullptr, HD, ws + OFF_H1 + (size_t)bo * 65536, 65536, d,
                    ws + (m == 0 ? OFF_Q : (m == 1 ? OFF_K : OFF_V)) + h * 8192 + (size_t)bo * 32 * 8192, 32 * 8192,
                    cnt, (PQKV + h) * 32 + 4 * part + m, tmo, wsl, mts, false, first ? NIMM : 0, ns, late ? ns : 0, bo * 257, tag);
  }
  __syncthreads();
  const int u2 = u2_of();
  if (cx || (u2 >= 0 && (xq < 3 || u2 < 3))) {
    const int ks = cx || xq < 3 ? xq : u2, cn = cx ? (t + 1) & 7 : (xq < 3 ? u2 : 7);
    const int lane = lane_id();
    const Dep d{nullptr, (unsigned)(cx ? (PSA + 4 * xq + ((lane >> 3) & 3)) * 128 + (lane & 7) * 4 : (PF1 + 16 * ks + (lane & 15)) * 128), cx ? 4u : 1u, cx ? (unsigned)((lane >> 1) & 3) : 0u};
    gemm7<R_PART>(smem, wave_id_, (cx ? Wo : w2) + (size_t)ks * 1024 * DM + cn * 128, nullptr, DM, ws + (cx ? OFF_O : OFF_G) + ks * 16 * 4096, (cx ? 64 : 56) * 4096, d,
                  ws + (cx ? OFF_P : OFF_P2) + (ks * DM + cn * 128) * (TLP ? 64 : 2), (size_t)32 * PROW, cnt, ((cx ? PWO : PF2) + ks * 8 + cn) * 32, tmo,
                  128 + xq * 32 + (cx ? cn : 22 + u2), mts, !cx);
  } else if (u2 >= 0) {
    const int c4 = u2 - 3;
    const int lane = lane_id();
    const Dep d{nullptr, (unsigned)(PF1 + 48 + (lane & 7)) * 128, 1u, 0u};
    gemm7<R_PART2>(smem, wave_id_, w2 + (size_t)3072 * DM + c4 * 256, nullptr, DM, ws + OFF_G + 48 * 4096, 56 * 4096, d,
                   ws + OFF_P2 + (3 * DM + c4 * 256) * (TLP ? 64 : 2), (size_t)32 * PROW, cnt, (PF2 + 24 + c4) * 32, tmo, 128 + xq * 32 + 22 + u2, mts, true);
  } else if (t < 8 || (t >= 16 && t < 22)) {
    int f = f_of();
    asm volatile("" : "+s"(f));
    const int lane = lane_id();
    const Dep d{nullptr, (unsigned)((PLN + (lane >> 3)) * 128 + (lane & 7) * 4), 1u, 0u};
    gemm7<R_F1>(smem, wave_id_, w1 + 64 * f, w3 + 64 * f, FF, ws + OFF_H2, 65536, d, ws + OFF_G + f * 4096, 56 * 4096, cnt, (PF1 + f) * 32, tmo, 128 + xq * 18 + 8 + f, mts, false);
  } else if (t >= 9 && t < 13) {
    fin_role(smem, wave_id_, xq * 4 + t - 9, mod, ws + OFF_P2, (const u16*)(ws + OFF_X1), out, cnt, tmo, mts);
    if (xq == 3 && t == 12 && opaque_tid() == 0) {
      const u32x4 v = ld16_sc1(ws + OFF_TAG);
      const u64 tn = (((u64)v.y << 32) | v.x) + 1;
      st8_sc1(ws + OFF_TAG, u32x2{(unsigned)tn, (unsigned)(tn >> 32)});
    }
  } else if (t == 28 || t == 29) {
    row_role(smem, wave_id_, 8 * xq, t - 28, mod, ws + OFF_P, x, (u16*)(ws + OFF_X1), ws + OFF_H2, cnt, tmo, 256 + xq, mts);
  } else if (t >= 26) {
    sa_role(smem, wave_id_, 8 * xq + 2 * (t >= 30 ? t - 30 : t - 24), ws + OFF_Q, ws + OFF_K, ws + OFF_V, ws + OFF_KVM, ws + OFF_O, cnt, tmo, 128 + xq * 32 + 30 + (t & 1), mts);
  }
}

extern "C" void kernel_launch(void* const* d_in, const int* in_sizes, int n_in, void* d_out, int out_size, void* d_ws, size_t ws_size,
                              hipStream_t stream) {
  const u16* x    = (const u16*)d_in[0];
  const u16* mem0 = (const u16*)d_in[1];
  const u16* Wq   = (const u16*)d_in[2];
  const u16* Wk   = (const u16*)d_in[3];
  const u16* Wv   = (const u16*)d_in[4];
  const u16* Wo   = (const u16*)d_in[5];
  const u16* w1   = (const u16*)d_in[8];
  const u16* w3   = (const u16*)d_in[9];
  const u16* w2   = (const u16*)d_in[10];
  const u16* c    = (const u16*)d_in[11];
  const u16* wmod = (const u16*)d_in[12];
#define WSHIFT 0
  char* ws = (char*)d_ws + ((size_t)WSHIFT << 20);
  hipFuncSetAttribute((const void*)k_mega, hipFuncAttributeMaxDynamicSharedMemorySize, MEGA_LDS);
  k_mega<<<256, 512, MEGA_LDS, stream>>>(Wq, Wk, Wv, Wo, w1, w3, w2, x, c, wmod, mem0, ws, (u16*)d_out);
}
```

```cpp
#include <hip/hip_runtime.h>
#include <hip/hip_bf16.h>
#include <stdint.h>
#include <type_traits>
#pragma clang diagnostic ignored "-Winline-asm"
#pragma clang diagnostic ignored "-Wunused-value"

typedef unsigned short u16;
typedef short s16x4 __attribute__((ext_vector_type(4)));
typedef __bf16 bf16x8 __attribute__((ext_vector_type(8)));
typedef float f32x4 __attribute__((ext_vector_type(4)));
typedef float f32x2 __attribute__((ext_vector_type(2)));
typedef unsigned u32x4 __attribute__((ext_vector_type(4)));
typedef unsigned u32x2 __attribute__((ext_vector_type(2)));
#define LAS __attribute__((address_space(3)))

constexpr int SEQ = 2048, DM = 1024, NH = 32, DH = 128, HD = 4096, NMEM = 8, SEG = 32, NSEG = 64, FF = 3584;
constexpr unsigned SENT = 0xFFFFFFFFu;

__device__ __forceinline__ int lane_id() { int l; asm volatile("v_mbcnt_lo_u32_b32 %0, -1, 0\n\tv_mbcnt_hi_u32_b32 %0, -1, %0" : "=v"(l)); return l; }
#define WAVE_ID wave_id_
#define opaque_tid() (WAVE_ID * 64 + lane_id())
__device__ __forceinline__ float bf2f(u16 v) { return __builtin_bit_cast(float, (unsigned)v << 16); }
__device__ __forceinline__ float bflo(unsigned v) { return __builtin_bit_cast(float, v << 16); }
__device__ __forceinline__ float bfhi(unsigned v) { return __builtin_bit_cast(float, v & 0xffff0000u); }
__device__ __forceinline__ float bfr(float x) { return (float)(__bf16)x; }
__device__ __forceinline__ unsigned pack2(float a, float b) {
  typedef __bf16 bf16x2 __attribute__((ext_vector_type(2)));
  bf16x2 v; v[0] = (__bf16)a; v[1] = (__bf16)b; return __builtin_bit_cast(unsigned, v);
}
__device__ __forceinline__ unsigned nosent(unsigned v) { return v == SENT ? 0x7FC07FC0u : v; }
__device__ __forceinline__ bf16x8 asbf8(u32x4 v) { return __builtin_bit_cast(bf16x8, v); }
__device__ __forceinline__ s16x4 ass4(u32x2 v) { return __builtin_bit_cast(s16x4, v); }
__device__ __forceinline__ u32x2 trread(const char* lds_base, unsigned off) {
  s16x4 v = __builtin_amdgcn_ds_read_tr16_b64_v4i16((LAS s16x4*)(lds_base + off));
  return __builtin_bit_cast(u32x2, v);
}
__device__ __forceinline__ void glds16(const void* gsrc, unsigned lds_dst) {
  unsigned keep;
  asm volatile("s_mov_b32 %0, m0\n\ts_mov_b32 m0, %2\n\ts_nop 0\n\tglobal_load_lds_dwordx4 %1, off\n\ts_mov_b32 m0, %0"
               : "=&s"(keep) : "v"(gsrc), "s"(lds_dst) : "memory");
}
#define CNT_ " nt"
#define WNT_ " nt"
__device__ __forceinline__ void glds16_w(const void* gsrc, unsigned lds_dst) {
  unsigned keep;
  asm volatile("s_mov_b32 %0, m0\n\ts_mov_b32 m0, %2\n\ts_nop 0\n\tglobal_load_lds_dwordx4 %1, off" CNT_ "\n\ts_mov_b32 m0, %0"
               : "=&s"(keep) : "v"(gsrc), "s"(lds_dst) : "memory");
}
__device__ __forceinline__ void glds16_sc1(const void* gsrc, unsigned lds_dst) {
  unsigned keep;
  asm volatile("s_mov_b32 %0, m0\n\ts_mov_b32 m0, %2\n\ts_nop 0\n\tglobal_load_lds_dwordx4 %1, off sc1\n\ts_mov_b32 m0, %0"
               : "=&s"(keep) : "v"(gsrc), "s"(lds_dst) : "memory");
}
#define NTM 1
template <int B> __device__ __forceinline__ u32x4 ldnt(const void* p) { if (NTM & B) return __builtin_nontemporal_load((const u32x4*)p); else return *(const u32x4*)p; }
#define MFMA32(a, b, c) __builtin_amdgcn_mfma_f32_16x16x32_bf16((a), (b), (c), 0, 0, 0)

__device__ __forceinline__ void st16_sc1(void* p, u32x4 v) { asm volatile("global_store_dwordx4 %0, %1, off sc1\n\ts_nop 1" :: "v"(p), "v"(v) : "memory"); }
__device__ __forceinline__ void st8_sc1(void* p, u32x2 v) { asm volatile("global_store_dwordx2 %0, %1, off sc1\n\ts_nop 1" :: "v"(p), "v"(v) : "memory"); }
template <bool WT> __device__ __forceinline__ void st8(void* p, u32x2 v) { if (WT) st8_sc1(p, v); else *(u32x2*)p = v; }
template <bool WT> __device__ __forceinline__ void st16(void* p, u32x4 v) { if (WT) st16_sc1(p, v); else *(u32x4*)p = v; }
__device__ __forceinline__ u32x4 ld16_sc1(const void* p) {
  u32x4 r; asm volatile("global_load_dwordx4 %0, %1, off sc1\n\ts_waitcnt vmcnt(0)" : "=&v"(r) : "v"(p) : "memory"); return r;
}
__device__ __forceinline__ void ld16x4_sc1(const void* p, u32x4& a, u32x4& b, u32x4& c, u32x4& d) {
  asm volatile("global_load_dwordx4 %0, %4, off sc1\n\tglobal_load_dwordx4 %1, %4, off offset:1024 sc1\n\t"
               "global_load_dwordx4 %2, %4, off offset:2048 sc1\n\tglobal_load_dwordx4 %3, %4, off offset:3072 sc1\n\ts_waitcnt vmcnt(0)"
               : "=&v"(a), "=&v"(b), "=&v"(c), "=&v"(d) : "v"(p) : "memory");
}
typedef __amdgpu_buffer_rsrc_t rsrc_t;
__device__ __forceinline__ rsrc_t mkrs(const void* p) { return __builtin_amdgcn_make_buffer_rsrc((void*)p, 0, 0x7FFFFFFF, 0x00020000); }
__device__ __forceinline__ u32x4 bld16(rsrc_t r, unsigned off) { return __builtin_amdgcn_raw_buffer_load_b128(r, off, 0, 16); }
__device__ __forceinline__ u32x2 bld8(rsrc_t r, unsigned off) { return __builtin_amdgcn_raw_buffer_load_b64(r, off, 0, 16); }
__device__ __forceinline__ bool ok4(u32x4 v) { return v.x != SENT && v.y != SENT && v.z != SENT && v.w != SENT; }
constexpr int SPIN_CAP = 2000000;
__device__ __forceinline__ bool give_up(int spins, unsigned* tmo, unsigned code) {
  if ((spins & 4095) != 4095) return false;
  if (spins > SPIN_CAP) { __hip_atomic_store(tmo, code, __ATOMIC_RELAXED, __HIP_MEMORY_SCOPE_AGENT); return true; }
  return __hip_atomic_load(tmo, __ATOMIC_RELAXED, __HIP_MEMORY_SCOPE_AGENT) != 0u;
}
template <bool ACQ = true>
__device__ __forceinline__ void wait_cnt(unsigned* c, unsigned n, unsigned* tmo) {
  for (int spins = 0;; ++spins) {
    const unsigned v = __hip_atomic_load(c, __ATOMIC_RELAXED, __HIP_MEMORY_SCOPE_AGENT);
    if (v >= n) break;
    __builtin_amdgcn_s_sleep(8);
    if (give_up(spins, tmo, 5)) break;
  }
  if (ACQ) {
    __builtin_amdgcn_fence(__ATOMIC_ACQUIRE, "agent");
    asm volatile("s_waitcnt vmcnt(0)" ::: "memory");
  }
}

__device__ __forceinline__ const char* uni(const char* p) {
  const unsigned long long v = (unsigned long long)p;
  const unsigned lo = __builtin_amdgcn_readfirstlane((unsigned)v), hi = __builtin_amdgcn_readfirstlane((unsigned)(v >> 32));
  return (const char*)(((unsigned long long)hi << 32) | lo);
}
#define TSCLK "s_memrealtime"
#define TC(k) do { } while (0)
#define TS(k) do { } while (0)
#define TSDECL do { } while (0)
#define TSFLUSH(mask) do { } while (0)

constexpr size_t OFF_MOD  = 0;
constexpr size_t OFF_MOD2 = 65536;
constexpr size_t OFF_TMO  = 98304;
constexpr size_t OFF_TAG  = 131072;
constexpr size_t OFF_CLAIM = 135168;
constexpr size_t OFF_INIT = 196608;
constexpr size_t OFF_H1   = 262144;
constexpr size_t OFF_Q    = OFF_H1 + (size_t)SEQ * DM * 2;
constexpr size_t OFF_K    = OFF_Q + (size_t)SEQ * HD * 2;
constexpr size_t OFF_V    = OFF_K + (size_t)SEQ * HD * 2;
constexpr size_t OFF_O    = OFF_V + (size_t)SEQ * HD * 2;
constexpr size_t OFF_X1   = OFF_O + (size_t)SEQ * HD * 2;
constexpr size_t OFF_H2   = OFF_X1 + (size_t)SEQ * DM * 2;
constexpr size_t OFF_G    = OFF_H2 + (size_t)SEQ * DM * 2;
constexpr size_t OFF_KVM  = (size_t)92 << 20;
constexpr size_t OFF_XCH  = (size_t)100 << 20;
constexpr size_t OFF_P    = (size_t)112 << 20;
constexpr size_t OFF_P2   = (size_t)128 << 20;
constexpr size_t OFF_DUMP = (size_t)144 << 20;
constexpr int PROW = 4 * DM * 2;
#define TLQ 1
#define TLP 1
static_assert(OFF_G + (size_t)SEQ * FF * 2 <= OFF_KVM, "layout");
__device__ __forceinline__ char* xch_base(char* ws) { return (char*)(((size_t)(ws + OFF_XCH) + ((2u << 20) - 1)) & ~(size_t)((2u << 20) - 1)); }
#define XPAR 0
constexpr int PROG = 16;
constexpr int PQKV = 64;
constexpr int PL1 = 96;
constexpr int PL1B = 350;
constexpr int PSA = 128;
constexpr int PWO = 192;
constexpr int PLN = 224;
constexpr int PF2 = 320;
constexpr int PF1 = 448;
#define SDIR 40
#define SB 56
#define MARG 20
#define MARL 15
#define MARF 8
#define SAW 0
constexpr int L_RED = 131072;
constexpr int L_SLOT = L_RED + 16384;
constexpr int L_GB = L_SLOT + 1024;
constexpr int MEGA_LDS = 163840;
static_assert(L_GB + 12288 + 2048 <= MEGA_LDS, "lds");
#define XPARC 0

__device__ __forceinline__ int bswz(int row) { return (((row >> 1) & 1) | (((row >> 3) & 1) << 1)) << 1; }
template <int OFF, bool SC1 = false>
__device__ __forceinline__ void glds16s(const char* sbase, unsigned voff, unsigned lds_base) {
  unsigned keep;
  if (SC1)
    asm volatile("s_mov_b32 %0, m0\n\ts_add_u32 m0, %3, %4\n\ts_nop 2\n\tglobal_load_lds_dwordx4 %1, %2 sc1\n\ts_mov_b32 m0, %0"
                 : "=&s"(keep) : "v"(voff), "s"(sbase), "s"(lds_base), "i"(OFF) : "memory", "scc");
  else
    asm volatile("s_mov_b32 %0, m0\n\ts_add_u32 m0, %3, %4\n\ts_nop 2\n\tglobal_load_lds_dwordx4 %1, %2\n\ts_mov_b32 m0, %0"
                 : "=&s"(keep) : "v"(voff), "s"(sbase), "s"(lds_base), "i"(OFF) : "memory", "scc");
}
template <int OFF>
__device__ __forceinline__ void glds16x2s(const char* sbase, unsigned voff, unsigned lds_base) {
  unsigned keep;
  asm volatile("s_mov_b32 %0, m0\n\ts_add_u32 m0, %3, %4\n\ts_nop 2\n\tglobal_load_lds_dwordx4 %1, %2 sc1\n\tglobal_load_lds_dwordx4 %1, %2 offset:1024 sc1\n\ts_mov_b32 m0, %0"
               : "=&s"(keep) : "v"(voff), "s"(sbase), "s"(lds_base), "i"(OFF) : "memory", "scc");
}
__device__ __forceinline__ void st16s(const char* sb, unsigned vo, u32x4 v) { asm volatile("global_store_dwordx4 %0, %1, %2 sc1\n\ts_nop 1" :: "v"(vo), "v"(v), "s"(sb) : "memory"); }
__device__ __forceinline__ void head_to_lds(const char* src, int lane, unsigned lds) {
  if (TLQ) {
    const int c = (lane & 15) ^ (lane >> 4);
    const unsigned v0 = (unsigned)((c >> 2) * 2048 + (lane >> 4) * 64 + (c & 3) * 16);
#pragma unroll
    for (int i = 0; i < 8; ++i) glds16s<0, true>(src + i * 256, v0 ^ ((i & 3) * 2048), lds + i * 1024);
  } else {
#pragma unroll
    for (int i = 0; i < 4; ++i) glds16x2s<0>(src + i * 2048, (unsigned)lane * 16, lds + i * 2048);
  }
}
#define TCHP ""
__device__ __forceinline__ void touch(const void* sb, unsigned vo, unsigned lds) {
  asm volatile("s_mov_b32 m0, %0\n\ts_nop 0\n\tglobal_load_lds_dword %1, %2" TCHP :: "s"(lds), "v"(vo), "s"(sb) : "memory", "m0");
}
#define WAIT_V(n) asm volatile("s_waitcnt vmcnt(" #n ")" ::: "memory")
#define BAR() do { asm volatile("s_waitcnt lgkmcnt(0)" ::: "memory"); __builtin_amdgcn_s_barrier(); asm volatile("" ::: "memory"); } while (0)
__device__ __forceinline__ void st4_sc1(void* p, unsigned v) { asm volatile("global_store_dword %0, %1, off sc1" :: "v"(p), "v"(v) : "memory"); }
__device__ __forceinline__ unsigned ld4_sc1(const void* p) {
  unsigned r; asm volatile("global_load_dword %0, %1, off sc1\n\ts_waitcnt vmcnt(0)" : "=&v"(r) : "v"(p) : "memory"); return r;
}
template <int CTRL> __device__ __forceinline__ unsigned dppu(unsigned x) { return (unsigned)__builtin_amdgcn_update_dpp(0, (int)x, CTRL, 0xf, 0xf, false); }
__device__ __forceinline__ unsigned rowmin16(unsigned v) {
  v = min(v, dppu<0x128>(v)); v = min(v, dppu<0x124>(v)); v = min(v, dppu<0x122>(v)); v = min(v, dppu<0x121>(v));
  return v;
}
__device__ __forceinline__ unsigned rdl(unsigned v, int l) { return (unsigned)__builtin_amdgcn_readlane((int)v, l); }
__device__ __forceinline__ unsigned qkv_avail(unsigned v, int m) {
  unsigned a = rdl(v, m);
#pragma unroll
  for (int j = 1; j < 4; ++j) { const unsigned p = rdl(v, 4 * j + m); if ((p >> 8) <= a) a = max(a, p & 255u); }
  return a;
}
constexpr float SCALE = 0.08838834764831845f;
constexpr float LOG2E = 1.4426950408889634f;
template <int N> __device__ __forceinline__ void delay64() {
  if (N >= 16) { __builtin_amdgcn_s_sleep(15); delay64<(N >= 16 ? N - 15 : 0)>(); } else if (N > 0) __builtin_amdgcn_s_sleep(N);
}
__device__ __forceinline__ void sw16(float x, float& a, float& b) {
  a = x; b = x; asm volatile("s_nop 1\n\tv_permlane16_swap_b32 %0, %1" : "+v"(a), "+v"(b));
}
__device__ __forceinline__ void sw32(float x, float& a, float& b) {
  a = x; b = x; asm volatile("s_nop 1\n\tv_permlane32_swap_b32 %0, %1" : "+v"(a), "+v"(b));
}
__device__ __forceinline__ float red16_32_max(float x) {
  float a, b; sw16(x, a, b); x = fmaxf(a, b); sw32(x, a, b); return fmaxf(a, b);
}
__device__ __forceinline__ float red16_32_add(float x) {
  float a, b; sw16(x, a, b); x = a + b; sw32(x, a, b); return a + b;
}
__device__ __forceinline__ unsigned cvtpk(float a, float b) { unsigned r; asm("v_cvt_pk_bf16_f32 %0, %1, %2" : "=v"(r) : "v"(a), "v"(b)); return r; }
__device__ __forceinline__ float max3(float a, float b, float c) { float r; asm("v_max3_f32 %0, %1, %2, %3" : "=v"(r) : "v"(a), "v"(b), "v"(c)); return r; }
__device__ __forceinline__ float max2(float a, float b) { float r; asm("v_max_f32 %0, %1, %2" : "=v"(r) : "v"(a), "v"(b)); return r; }
__device__ __forceinline__ unsigned ror8u(unsigned x) { return (unsigned)__builtin_amdgcn_update_dpp(0, (int)x, 0x128, 0xf, 0xf, false); }
__device__ __forceinline__ float ror8(float x) { return __builtin_bit_cast(float, ror8u(__builtin_bit_cast(unsigned, x))); }
__device__ __forceinline__ float add_ror8(float x) {
  return x + __builtin_bit_cast(float, __builtin_amdgcn_update_dpp(0, __builtin_bit_cast(int, x), 0x128, 0xf, 0xf, false));
}

__device__ __forceinline__ unsigned rt_now() { return (unsigned)__builtin_amdgcn_s_memrealtime(); }
__device__ __forceinline__ unsigned latest(unsigned v) {
  const unsigned now = rt_now() + 400u, age = rowmin16((now - v) & 0xFFFFFFu);
  return now - min(min(min(rdl(age, 0), rdl(age, 16)), min(rdl(age, 32), rdl(age, 48))), 900u);
}
__device__ __forceinline__ void wait_rt(unsigned t0, int d) {
  while ((int)(rt_now() - t0) < d) { }
}
__device__ __forceinline__ bool bells(bool ok, unsigned u, int b, int marg) {
  if (b < SB || !__all(ok || (int)(u >> 24) > b)) return false;
  wait_rt(latest(!ok && (int)(u >> 24) == b + 1 ? u : rt_now() - 400u), marg);
  return true;
}
__device__ __forceinline__ void ld_fb(unsigned& v, unsigned& u, const void* sb, unsigned vo) {
  asm volatile("global_load_dword %0, %2, %3 sc1\n\tglobal_load_dword %1, %2, %3 offset:64 sc1\n\ts_waitcnt vmcnt(0)" : "=&v"(v), "=&v"(u) : "v"(vo), "s"(sb) : "memory");
}
__device__ __forceinline__ unsigned bell(int b, unsigned t) { return (unsigned)(b + 1) << 24 | (t & 0xFFFFFFu); }
typedef unsigned long long u64;
__device__ __forceinline__ unsigned tag2(u64 tag) { return (unsigned)(tag >> 32) ^ ((unsigned)tag * 0x9E3779B1u); }
__device__ __forceinline__ unsigned init_eval(u32x4 v, u64 tag) {
  if (!__all((v.x & ~63u) == (unsigned)tag << 6 && v.y == (unsigned)(tag >> 26))) return 0u;
  unsigned r = 8u;
#pragma unroll
  for (int c = 0; c < 3; ++c) r |= __popcll(__ballot(v.x & (1u << c))) > __popcll(__ballot(v.x & (8u << c))) ? 1u << c : 0u;
  return r ^ (XPAR * 3u + XPARC * 4u);
}
__device__ __forceinline__ unsigned init_wait(const char* ws, u64 tag) {
  for (int spins = 0; spins < SPIN_CAP; ++spins) {
    const unsigned r = init_eval(ld16_sc1(ws + OFF_INIT + (lane_id() & 31) * 128), tag);
    if (r) return r;
    __builtin_amdgcn_s_sleep(4);
  }
  return 8u;
}
__device__ __forceinline__ void init_issue(const char* ws, unsigned lds) { glds16s<0, true>(uni(ws + OFF_INIT), (unsigned)(lane_id() & 31) * 128u, lds); }
__device__ __forceinline__ unsigned init_get(const char* ws, u64 tag, const char* copy) {
  const unsigned r = init_eval(*(volatile LAS u32x4*)(copy + lane_id() * 16), tag);
  return r ? r : init_wait(ws, tag);
}
__device__ __forceinline__ unsigned* cnt_of(char* ws, unsigned in) { return (unsigned*)uni(xch_base(ws) + ((size_t)4 << 21) + (size_t)((in & 4u) << 11)); }
__device__ __forceinline__ void wait_l1(const unsigned* cnt, unsigned n, unsigned* tmo) {
  const int lane = lane_id();
  for (int spins = 0;; ++spins) {
    const unsigned v = ld4_sc1(cnt + (PL1 + (lane & 31)) * 32);
    if (__all(v >= n)) break;
    __builtin_amdgcn_s_sleep(8);
    if (give_up(spins, tmo, 12)) break;
  }
}
#define GKV 1
__device__ __forceinline__ void wait_k0(const unsigned* cnt, int h, unsigned* tmo) {
  for (int spins = 0;; ++spins) {
    const int l = lane_id();
    const unsigned v = ld4_sc1(cnt + (PQKV + h) * 32 + 1 + (l & 1));
    if (__all(v >= (unsigned)GKV)) break;
    __builtin_amdgcn_s_sleep(32);
    if (give_up(spins, tmo, 13)) break;
  }
}
#define WCLK(slot, b) do { } while (0)
#define WTM(slot, b, k) do { } while (0)
#define WTS(slot, b, k) do { } while (0)
#define WFB() do { } while (0)
#define STS(ws_, slot_, k_) do { } while (0)
typedef __bf16 bf16x2 __attribute__((ext_vector_type(2)));
__device__ __forceinline__ f32x2 pkfma(f32x2 a, f32x2 b, f32x2 c) { return __builtin_elementwise_fma(a, b, c); }
__device__ __forceinline__ float wavesum(float v) {
  v += __builtin_bit_cast(float, dppu<0x128>(__builtin_bit_cast(unsigned, v))); v += __builtin_bit_cast(float, dppu<0x124>(__builtin_bit_cast(unsigned, v)));
  v += __builtin_bit_cast(float, dppu<0x122>(__builtin_bit_cast(unsigned, v))); v += __builtin_bit_cast(float, dppu<0x121>(__builtin_bit_cast(unsigned, v)));
  return red16_32_add(v);
}
__device__ __forceinline__ float adds(unsigned v, unsigned sel, float c) { return __builtin_amdgcn_fdot2_f32_bf16(__builtin_bit_cast(bf16x2, v), __builtin_bit_cast(bf16x2, sel), c, false); }
__device__ __forceinline__ void sum4(const u32x4 (&p)[4], f32x2 (&a)[4]) {
  unsigned slo = 0x00003F80u, shi = 0x3F800000u;
  asm("" : "+s"(slo), "+s"(shi));
#pragma unroll
  for (int e = 0; e < 4; ++e) {
    float lo = bflo(p[0][e]), hi = bfhi(p[0][e]);
#pragma unroll
    for (int q = 1; q < 4; ++q) { lo = adds(p[q][e], slo, lo); hi = adds(p[q][e], shi, hi); }
    a[e] = f32x2{lo, hi};
  }
}
__device__ __forceinline__ u32x4 norm8(const f32x2 (&v)[4], const char* mt, float mb, float rden) {
  const f32x4 h0 = *(const f32x4*)(mt + 4096), h1 = *(const f32x4*)(mt + 4096 + 1024), c0 = *(const f32x4*)(mt + 8192), c1 = *(const f32x4*)(mt + 8192 + 1024);
  const f32x2 m2 = f32x2{mb, mb}, d2 = f32x2{rden, rden};
  const f32x2 r0 = pkfma(v[0] - m2, f32x2{c0[0], c0[1]} * d2, f32x2{h0[0], h0[1]}), r1 = pkfma(v[1] - m2, f32x2{c0[2], c0[3]} * d2, f32x2{h0[2], h0[3]});
  const f32x2 r2 = pkfma(v[2] - m2, f32x2{c1[0], c1[1]} * d2, f32x2{h1[0], h1[1]}), r3 = pkfma(v[3] - m2, f32x2{c1[2], c1[3]} * d2, f32x2{h1[2], h1[3]});
  return u32x4{pack2(r0[0], r0[1]), pack2(r1[0], r1[1]), pack2(r2[0], r2[1]), pack2(r3[0], r3[1])};
}
__device__ __forceinline__ void half_load(const char* P, const u16* res, int row, int hf, int lane, f32x2 (&a)[4], u32x4& rv) {
  const rsrc_t rp = mkrs(P), rr = mkrs(res);
  const unsigned po = TLP ? (unsigned)((row >> 5) * 32 * PROW + (hf * 16 + (lane >> 2)) * 2048 + (row & 31) * 64 + (lane & 3) * 16) : (unsigned)(row * PROW + hf * 1024 + lane * 16), ro = (unsigned)(row * DM * 2 + hf * 1024 + lane * 16);
  u32x4 p[4];
#pragma unroll
  for (int q = 0; q < 4; ++q) p[q] = bld16(rp, po + q * (TLP ? 65536 : 2048));
  if (res) rv = bld16(rr, ro);
  sum4(p, a);
}
__device__ __forceinline__ bool fin_half(bool fin, const char* P, const u16* res, const char* gbt, u16* __restrict__ o1, int row, int hf, int lane) {
  f32x2 a[4]; u32x4 rv;
  half_load(P, res, row, hf, lane, a, rv);
  const u32x4 gv = *(const u32x4*)(gbt + hf * 1024 + lane * 16);
  u32x4 o; f32x2 t = f32x2{0, 0};
#pragma unroll
  for (int e = 0; e < 4; ++e) { const f32x2 s = pkfma(f32x2{bflo(gv[e]), bfhi(gv[e])}, a[e], f32x2{bflo(rv[e]), bfhi(rv[e])}); o[e] = pack2(s[0], s[1]); t += s; }
  if (!fin && !__all(t[0] == t[0] && t[1] == t[1])) return false;
  st16_sc1(o1 + (size_t)row * DM + hf * 512 + lane * 8, o);
  return true;
}
#define DLN 10
#define LNE 1
template <int CTRL> __device__ __forceinline__ float dppf(float x) { return __builtin_bit_cast(float, dppu<CTRL>(__builtin_bit_cast(unsigned, x))); }
__device__ __forceinline__ bool ln_row(bool fin, int bb, unsigned* bl, const f32x2 (&a)[2][4], const f32x2 (&xf)[8], const f32x2 (&tg)[8], const f32x2 (&th)[8], const f32x2 (&tc)[8],
                                       const char* x1, const char* h2, unsigned xo, unsigned ho) {
  f32x2 v[8]; u32x4 o[2];
#pragma unroll
  for (int e = 0; e < 8; ++e) { const f32x2 t = pkfma(tg[e], a[e >> 2][e & 3], xf[e]); const unsigned u = pack2(t[0], t[1]); o[e >> 2][e & 3] = u; v[e] = f32x2{bflo(u), bfhi(u)}; }
  const float pv = __builtin_bit_cast(float, __builtin_amdgcn_readfirstlane(__float_as_int(v[0][0])));
  f32x2 d[8];
#pragma unroll
  for (int e = 0; e < 8; ++e) d[e] = v[e] - f32x2{pv, pv};
  const f32x2 s2 = ((d[0] + d[1]) + (d[2] + d[3])) + ((d[4] + d[5]) + (d[6] + d[7]));
  const f32x2 q2 = pkfma(d[3], d[3], pkfma(d[2], d[2], pkfma(d[1], d[1], d[0] * d[0]))) + pkfma(d[7], d[7], pkfma(d[6], d[6], pkfma(d[5], d[5], d[4] * d[4])));
  float s = s2[0] + s2[1], q = q2[0] + q2[1];
  if (!fin && !__all(s == s)) return false;
  if (LNE && bb >= SB && lane_id() == 0) st4_sc1(bl, bell(bb, rt_now() + DLN + 30));
  s += dppf<0x128>(s); q += dppf<0x128>(q); s += dppf<0x124>(s); q += dppf<0x124>(q);
  s += dppf<0x122>(s); q += dppf<0x122>(q); s += dppf<0x121>(s); q += dppf<0x121>(q);
  asm volatile("s_nop 1\n\tv_permlane16_swap_b32 %0, %1" : "+v"(s), "+v"(q));
  s += q;
  { float x, y; sw32(s, x, y); s = x + y; }
  const float sh = __builtin_bit_cast(float, rdl(__builtin_bit_cast(unsigned, s), 0)) * (1.0f / DM), sq = __builtin_bit_cast(float, rdl(__builtin_bit_cast(unsigned, s), 16));
  const float mb = bfr(pv + sh), vb = bfr(fmaxf(sq * (1.0f / DM) - sh * sh, 0.0f));
  const float rden = __builtin_amdgcn_rcpf(bfr(__builtin_amdgcn_sqrtf(bfr(vb + 1e-5f))));
#pragma unroll
  for (int i = 0; i < 2; ++i) {
    u32x4 r;
#pragma unroll
    for (int e = 0; e < 4; ++e) { const f32x2 t = pkfma(v[4 * i + e] - f32x2{mb, mb}, tc[4 * i + e] * f32x2{rden, rden}, th[4 * i + e]); r[e] = pack2(t[0], t[1]); }
    st16s(h2, ho + i * 32768, r);
  }
  if (!LNE && bb >= SB && lane_id() == 0) st4_sc1(bl, bell(bb, rt_now() + DLN));
  st16s(x1, xo, o[0]); st16s(x1, xo + 1024, o[1]);
  return true;
}
__device__ __forceinline__ void row_table(char* tab, const float* mod, const unsigned* cnt, unsigned* tmo, int tid) {
  wait_l1(cnt + 1, 1u, tmo);
  const rsrc_t rm = mkrs((const char*)mod + OFF_MOD2);
#pragma unroll
  for (int i = 0; i < 2; ++i) {
    const int u = tid + 512 * i, a = u >> 8, c = u & 255;
    f32x4 v = __builtin_bit_cast(f32x4, bld16(rm, (unsigned)u * 16));
    v = f32x4{bfr(v[0]), bfr(v[1]), bfr(v[2]), bfr(v[3])};
    if (a == 2) v = f32x4{bfr(1.0f + v[0]), bfr(1.0f + v[1]), bfr(1.0f + v[2]), bfr(1.0f + v[3])};
    if (a < 3) *(f32x4*)(tab + a * 4096 + ((c >> 7) * 2 + (c & 1)) * 1024 + ((c >> 1) & 63) * 16) = v;
    else *(u32x2*)(tab + 12288 + c * 8) = u32x2{pack2(v[0], v[1]), pack2(v[2], v[3])};
  }
  __syncthreads();
}
__device__ __forceinline__ void row_role(char* smem, int wave_id_, int rbase, int par, const float* mod, const char* P, const u16* x, u16* x1, char* h2c,
                                         unsigned* cnt, unsigned* tmo, int slot, unsigned long long* mts) {
  const int tid = opaque_tid(), lane = tid & 63, wave = WAVE_ID;
  if (tid < 80) *(volatile LAS unsigned*)(smem + 16384 + tid * 4) = 0u;
  {
    static_assert(TLP && SB % 2 == 0, "");
    unsigned z = SENT; asm volatile("" : "+v"(z));
    const int r4 = rbase + 4 * par + (wave & 3);
    for (int bb = SB + (wave >> 2); bb < NSEG; bb += 2)
#pragma unroll
      for (int i = 0; i < 2; ++i) {
        st16s(uni((const char*)x1 + (size_t)bb * 65536), (unsigned)(r4 * DM * 2 + i * 1024 + lane * 16), u32x4{z, z, z, z});
        st16s(uni(h2c + (size_t)bb * 65536), (unsigned)(r4 * 128 + (lane & 7) * 16 + (lane >> 3) * 4096 + i * 32768), u32x4{z, z, z, z});
      }
    WAIT_V(0);
  }
  row_table(smem, mod, cnt, tmo, tid);
  const int rb = rbase + wave + 32 * par;
  const rsrc_t rp = mkrs(P), rr = mkrs(x);
  const unsigned l16 = lane * 16;
  const unsigned pwo = (unsigned)(PWO + (lane & 31)) * 128;
  unsigned* myp = cnt + (PLN + (rb >> 3)) * 32 + wave;
  f32x2 a[2][4];
  const int gp = wave >> 2, r4 = rbase + 4 * par + (wave & 3);
  volatile LAS unsigned* lwb = (volatile LAS unsigned*)(smem + 16384 + gp * 8);
  if (gp && lane == 0) st4_sc1(myp, 1u);
  f32x2 tg[8], th[8], tc[8];
#pragma unroll
  for (int i = 0; i < 4; ++i) {
    const char* mt = smem + lane * 16 + i * 1024;
    const f32x4 g_ = *(const f32x4*)mt, h_ = *(const f32x4*)(mt + 4096), c_ = *(const f32x4*)(mt + 8192);
    tg[2 * i] = f32x2{g_[0], g_[1]}; tg[2 * i + 1] = f32x2{g_[2], g_[3]}; th[2 * i] = f32x2{h_[0], h_[1]}; th[2 * i + 1] = f32x2{h_[2], h_[3]};
    tc[2 * i] = f32x2{c_[0], c_[1]}; tc[2 * i + 1] = f32x2{c_[2], c_[3]};
  }
  const unsigned xo = (unsigned)(r4 * DM * 2 + lane * 16), ho = (unsigned)(r4 * 128 + (((lane & 7) ^ (r4 & 7)) << 4) + (lane >> 3) * 4096);
  const unsigned po = (l16 >> 6) * 2048 + (l16 & 48);
  u32x4 rv[2];
#pragma unroll
  for (int i = 0; i < 2; ++i) rv[i] = __builtin_amdgcn_raw_buffer_load_b128(rr, xo + i * 1024, (unsigned)gp * 65536u, 0);
  unsigned wb = 0;
  for (int bb = gp; bb < NSEG; bb += 2) {
    f32x2 xf[8];
#pragma unroll
    for (int e = 0; e < 8; ++e) xf[e] = f32x2{bflo(rv[e >> 2][e & 3]), bfhi(rv[e >> 2][e & 3])};
    asm volatile("" : "+v"(xf[0]), "+v"(xf[1]), "+v"(xf[2]), "+v"(xf[3]), "+v"(xf[4]), "+v"(xf[5]), "+v"(xf[6]), "+v"(xf[7]));
    if (wb <= (unsigned)bb) {
      if ((wave & 3) == 0) {
        wb = (unsigned)(bb + 1);
        for (int spins = 0;; ++spins) {
          unsigned v, u;
          ld_fb(v, u, cnt, pwo);
          if (__all(v > (unsigned)bb + 2u)) { v = rowmin16(v); wb = min(rdl(v, 0), rdl(v, 16)); break; }
          if (__all(v > (unsigned)bb) || bells(v > (unsigned)bb, u, bb, MARL)) break;
          __builtin_amdgcn_s_sleep(2);
          if (give_up(spins, tmo, 11)) break;
        }
        lwb[0] = wb;
      } else {
        while ((wb = __builtin_amdgcn_readfirstlane(lwb[0])) <= (unsigned)bb) __builtin_amdgcn_s_sleep(1);
      }
    }
    WTM(slot, bb, 0);
    bool fin = bb < SB;
    for (int spins = 0;; ++spins) {
      u32x4 pp[2][4];
#pragma unroll
      for (int i = 0; i < 2; ++i)
#pragma unroll
        for (int q = 0; q < 4; ++q) pp[i][q] = __builtin_amdgcn_raw_buffer_load_b128(rp, po + i * 32768, (unsigned)(bb * 32 * PROW + q * 65536 + r4 * 64), 16);
      sum4(pp[0], a[0]); sum4(pp[1], a[1]);
      if (ln_row(fin, bb, myp + 16, a, xf, tg, th, tc, uni((const char*)x1 + (size_t)bb * 65536), uni(h2c + (size_t)bb * 65536), xo, ho)) break;
      fin = __all(ld4_sc1((const char*)cnt + pwo) > (unsigned)bb) || give_up(spins, tmo, 11);
    }
    WTM(slot, bb, 3);
#pragma unroll
    for (int i = 0; i < 2; ++i) rv[i] = __builtin_amdgcn_raw_buffer_load_b128(rr, xo + i * 1024, (unsigned)min(bb + 2, NSEG - 1) * 65536u, 0);
    WAIT_V(2);
    unsigned o = 0;
    if (lane == 0) o = __hip_atomic_fetch_add((LAS unsigned*)(smem + 16448 + bb * 4), 1u, __ATOMIC_RELAXED, __HIP_MEMORY_SCOPE_WORKGROUP);
    if (__builtin_amdgcn_readfirstlane(o) == 3u) {
      if (lane < 4) st4_sc1(myp - (wave & 3) + lane, (unsigned)(bb + 2));
      WTM(slot, bb, 2);
    }
  }
}
#define NCS 8
__device__ __forceinline__ void l1_role(char* smem, int wave_id_, bool fst, int c, const u16* __restrict__ x, const float* __restrict__ mod,
                                        const u16* __restrict__ mem0, char* __restrict__ h1c, unsigned*& cnt, u64 tag, unsigned* tmo, unsigned long long* mts) {
  const int tid = opaque_tid(), lane = tid & 63, wave = WAVE_ID;
  char* ws = (char*)mod - OFF_MOD;
  u32x4 xr[7][2];
  const int row0 = (fst ? 0 : 256) + 8 * c + wave;
  const u16* src = x + (size_t)row0 * DM + lane * 8;
  xr[0][0] = ldnt<2>(src); xr[0][1] = ldnt<2>(src + 512);
  volatile LAS unsigned* sg = (volatile LAS unsigned*)(smem + L_SLOT + 784);
  if (!fst) {
#pragma unroll
    for (int pp = 1; pp < 7; ++pp) { xr[pp][0] = ldnt<2>(src + (size_t)pp * 256 * DM); xr[pp][1] = ldnt<2>(src + (size_t)pp * 256 * DM + 512); }
  } else if (wave >= 5) {
    char* cal = xch_base(ws) + ((size_t)(wave == 5 ? 0 : wave - 3) << 21) + (126 + (c >> 4)) * 16384 + (c & 15) * 512;
    st16_sc1(cal + (lane >> 5) * 8192 + (lane & 31) * 16, u32x4{SENT, SENT, SENT, SENT});
    WAIT_V(0);
    unsigned best[2] = {1u << 30, 1u << 30};
    const char* sb = uni(cal);
#pragma unroll
    for (int r = 1; r < NCS; ++r) {
      unsigned long long t0, t1; unsigned v;
      asm volatile("s_memtime %0\n\ts_waitcnt lgkmcnt(0)\n\ts_load_dword %2, %3, %4 glc\n\ts_waitcnt lgkmcnt(0)\n\ts_memtime %1\n\ts_waitcnt lgkmcnt(0)"
                   : "=&s"(t0), "=&s"(t1), "=&s"(v) : "s"(sb), "i"((r & 1) * 8192 + (r >> 1) * 128) : "memory");
      if (r > 1) best[r & 1] = min(best[r & 1], (unsigned)(t1 - t0));
    }
    unsigned xcc; asm volatile("s_getreg_b32 %0, hwreg(HW_REG_XCC_ID)" : "=s"(xcc));
    const unsigned df = max(best[0], best[1]) - min(best[0], best[1]);
    sg[wave - 5] = df < 110u || df > 400u ? 0u : (((best[1] < best[0]) != ((xcc & 4) != 0)) ? 1u : 8u) << (wave - 5);
  } else if (wave < 3) {
    const int b = c + 32 * wave;
    if (b < 89) {
      const char* p = uni(xch_base(ws) + (b < 48 ? (size_t)((b >> 4) * 32 + (b & 15)) * 16384
                                        : b < 56 ? ((size_t)3 << 21) + (size_t)(8 + ((b - 48) >> 2) * 8 + (b & 3)) * 16384
                                        : b < 84 ? ((size_t)3 << 21) + (size_t)(b - 56 + 24) * 16384 : ((size_t)4 << 21) + (size_t)(b - 84) * 16384));
      const unsigned v = b < 80 ? SENT : 0u;
#pragma unroll
      for (int i = 0; i < 16; ++i) st16s(p, (unsigned)(i * 1024 + lane * 16), u32x4{v, v, v, v});
    } else if (b == 89 && lane == 0) st4_sc1(tmo, 0u);
    WAIT_V(0);
  } else {
    const int pc = (c & 7) * 2 + (wave & 1), g = lane >> 4, hf = (lane >> 2) & 1, row = ((lane >> 3) & 1) * 4 + (lane & 3);
    u32x4 v = *(const u32x4*)(mem0 + row * DM + 32 * (2 * pc + hf) + 8 * g);
    v.x = nosent(v.x); v.y = nosent(v.y); v.z = nosent(v.z); v.w = nosent(v.w);
    st16_sc1(xch_base(ws) + ((size_t)3 << 21) + (size_t)(((c >> 3) & 1) * 2 + (pc >> 3)) * 16384 + (c >> 4) * 8192 + (pc & 7) * 1024 + lane * 16, v);
    WAIT_V(0);
  }
  BAR();
  if (fst && tid == 0) st8_sc1(ws + OFF_INIT + c * 128, u32x2{(unsigned)tag << 6 | sg[0] | sg[1] | sg[2], (unsigned)(tag >> 26)});
  if (fst && wave == 0) STS(ws, 128 + (c >> 3) * 32 + 24 + (c & 7), 2);
  {
    f32x4 v;
    for (int spins = 0; spins < SPIN_CAP; ++spins) {
      u32x4 m[4];
      asm volatile("s_nop 4\n\tglobal_load_dwordx4 %0, %4, %6 sc1\n\tglobal_load_dwordx4 %1, %4, %6 offset:16 sc1\n\t"
                   "global_load_dwordx4 %2, %5, %6 sc1\n\tglobal_load_dwordx4 %3, %5, %6 offset:16 sc1\n\ts_waitcnt vmcnt(0)"
                   : "=&v"(m[0]), "=&v"(m[1]), "=&v"(m[2]), "=&v"(m[3]) : "v"((unsigned)tid * 32), "v"((unsigned)tid * 32 + 16384), "s"(mod) : "memory");
      bool ok = true;
#pragma unroll
      for (int i = 0; i < 4; ++i) ok = ok && m[i].y == (unsigned)tag && m[i].w == tag2(tag);
      v = f32x4{__uint_as_float(m[0].x) + __uint_as_float(m[2].x), __uint_as_float(m[0].z) + __uint_as_float(m[2].z),
                __uint_as_float(m[1].x) + __uint_as_float(m[3].x), __uint_as_float(m[1].z) + __uint_as_float(m[3].z)};
      if (__all(ok)) break;
      __builtin_amdgcn_s_sleep(4);
    }
    v = f32x4{bfr(v[0]), bfr(v[1]), bfr(v[2]), bfr(v[3])};
    const int a = tid >> 8, cc = tid & 255;
    if (a) v = f32x4{bfr(1.0f + v[0]), bfr(1.0f + v[1]), bfr(1.0f + v[2]), bfr(1.0f + v[3])};
    *(f32x4*)(smem + 4096 + a * 4096 + ((cc >> 7) * 2 + (cc & 1)) * 1024 + ((cc >> 1) & 63) * 16) = v;
  }
  BAR();
  if (fst && wave == 0) STS(ws, 128 + (c >> 3) * 32 + 24 + (c & 7), 3);
#pragma unroll
  for (int p = 0; p < 7; ++p) {
    if (p > 0 && fst) break;
    f32x2 v[2][4];
#pragma unroll
    for (int i = 0; i < 2; ++i)
#pragma unroll
      for (int e = 0; e < 4; ++e) v[i][e] = f32x2{bflo(xr[p][i][e]), bfhi(xr[p][i][e])};
    const f32x2 s2 = ((v[0][0] + v[0][1]) + (v[0][2] + v[0][3])) + ((v[1][0] + v[1][1]) + (v[1][2] + v[1][3]));
    const float mean = wavesum(s2[0] + s2[1]) * (1.0f / DM);
    f32x2 q2 = f32x2{0, 0};
#pragma unroll
    for (int i = 0; i < 8; ++i) { const f32x2 d = v[i >> 2][i & 3] - f32x2{mean, mean}; q2 = pkfma(d, d, q2); }
    const float q = wavesum(q2[0] + q2[1]);
    if (p == 0 && fst && wave == 0) STS((char*)tmo - OFF_TMO, 128 + (c >> 3) * 32 + 24 + (c & 7), 5);
    const float mb = bfr(mean), vb = bfr(q * (1.0f / DM));
    const float rden = 1.0f / bfr(sqrtf(bfr(vb + 1e-5f)));
    const int row = 256 * p + row0, rb = row & 31;
    char* dst = h1c + (size_t)(row >> 5) * 65536 + (lane >> 3) * 4096 + rb * 128 + (((lane & 7) ^ (rb & 7)) << 4);
    const char* ml = smem + lane * 16;
#pragma unroll
    for (int i = 0; i < 2; ++i) st16_sc1(dst + i * 32768, norm8(v[i], ml + i * 2048, mb, rden));
  }
  cnt = cnt_of(ws, init_wait(ws, tag));
  WAIT_V(0);
  __builtin_amdgcn_s_barrier();
  if (tid == 0) st4_sc1(cnt + ((fst ? PL1 : PL1B) + c) * 32, fst ? 8u : 7u);
  if (wave == 0) STS(ws, 128 + (c >> 3) * 32 + 24 + (c & 7), fst ? 6 : 7);
}
__device__ __forceinline__ void mod2_job(char* smem, int wave_id_, int c, const float* __restrict__ mod, const u16* __restrict__ cvec, const u16* __restrict__ wmod, unsigned* cnt) {
  const int tid = opaque_tid();
  {
    const float sva = bf2f(cvec[tid]), svb = bf2f(cvec[tid + 512]);
    float* sv = (float*)(smem + 12288);
    sv[tid] = bfr(sva / (1.0f + __expf(-sva))); sv[tid + 512] = bfr(svb / (1.0f + __expf(-svb)));
  }
  __syncthreads();
  {
    const int cg = tid & 15, r0 = tid >> 4;
    const u16* wp = wmod + (size_t)r0 * (6 * DM) + 2 * DM + 128 * c + cg * 8;
    const float* sv = (const float*)(smem + 12288) + r0;
    f32x2 acc[4] = {f32x2{0, 0}, f32x2{0, 0}, f32x2{0, 0}, f32x2{0, 0}};
#pragma unroll 1
    for (int o = 0; o < 4; ++o) {
      u32x4 w[8];
#pragma unroll
      for (int i = 0; i < 8; ++i) w[i] = ldnt<1>(wp + (size_t)(o * 8 + i) * 32 * (6 * DM));
#pragma unroll
      for (int i = 0; i < 8; ++i) {
        const float s = sv[(o * 8 + i) * 32];
#pragma unroll
        for (int e = 0; e < 4; ++e) acc[e] = pkfma(f32x2{s, s}, f32x2{bflo(w[i][e]), bfhi(w[i][e])}, acc[e]);
      }
    }
    float* red = (float*)(smem + 16384);
    *(f32x4*)(red + r0 * 128 + cg * 8) = f32x4{acc[0][0], acc[0][1], acc[1][0], acc[1][1]};
    *(f32x4*)(red + r0 * 128 + cg * 8 + 4) = f32x4{acc[2][0], acc[2][1], acc[3][0], acc[3][1]};
    __syncthreads();
    if (tid < 128) {
      float s = 0;
#pragma unroll
      for (int r = 0; r < 32; ++r) s += red[r * 128 + tid];
      st4_sc1((char*)mod + OFF_MOD2 + (128 * c + tid) * 4, __builtin_bit_cast(unsigned, s));
    }
    WAIT_V(0);
    __builtin_amdgcn_s_barrier();
    if (tid == 0) st4_sc1(cnt + (PL1 + c) * 32 + 1, 1u);
  }
}
enum { R_QKV = 0, R_PART = 1, R_F1 = 2, R_PART2 = 3 };
struct Dep {
  const char* base; unsigned voff; unsigned mul, add;
};
__device__ __forceinline__ unsigned dep_eval(unsigned v, const Dep& d) {
  v = rowmin16(v * d.mul + (d.add + 64u));
  return min(min(rdl(v, 0), rdl(v, 16)), min(rdl(v, 32), rdl(v, 48))) - 64u;
}
typedef unsigned u32x32 __attribute__((ext_vector_type(32)));
typedef unsigned u32x16 __attribute__((ext_vector_type(16)));
__device__ __forceinline__ void fast_ld0(const u32x32 (&W)[4], u32x16& pd, unsigned ea, unsigned da, int& b, int& lf, int cp, unsigned up, unsigned sa, unsigned mu, unsigned ad,
    unsigned rw, unsigned ab, const char* p1, const char* p0, unsigned l1, unsigned l0, const unsigned (&av)[8], int la) {
  asm volatile("s_setprio 0\n\t"
    "s_cmp_eq_u32 %[la], 3\n\t"
    "s_waitcnt vmcnt(12)\n\t"
    "s_cbranch_scc1 20f\n\t"
    "s_waitcnt vmcnt(4)\n\t"
    "20:\n\t"
    "s_barrier\n\t"
    "ds_read_b128 v[96:99], %[ea] offset:0\n\t"
    "ds_read_b128 v[100:103], %[ea] offset:2048\n\t"
    "ds_read_b128 v[104:107], %[da] offset:0\n\t"
    "ds_read_b128 v[108:111], %[da] offset:2048\n\t"
    "ds_read_b128 v[112:115], %[ea] offset:4096\n\t"
    "ds_read_b128 v[116:119], %[ea] offset:6144\n\t"
    "s_branch 2f\n\t"
    "1:\n\t"
    "s_cmp_eq_u32 %[la], 3\n\t"
    "s_waitcnt vmcnt(12)\n\t"
    "s_cbranch_scc1 21f\n\t"
    "s_waitcnt vmcnt(4)\n\t"
    "21:\n\t"
    "s_barrier\n\t"
    "2:\n\t"
    "s_waitcnt lgkmcnt(4)\n\t"
    "v_mfma_f32_16x16x32_bf16 v[64:67], v[128:131], v[96:99], 0\n\t"
    "ds_read_b128 v[120:123], %[da] offset:4096\n\t"
    "s_add_u32 m0, %[l1], 0\n\t"
    "ds_write_b128 %[rw], v[80:83] offset:0\n\t"
    "v_mfma_f32_16x16x32_bf16 v[68:71], v[132:135], v[96:99], 0\n\t"
    "ds_read_b128 v[124:127], %[da] offset:6144\n\t"
    "global_load_lds_dwordx4 %[av0], s[36:37] sc1\n\t"
    "ds_write_b128 %[rw], v[84:87] offset:1024\n\t"
    "v_mfma_f32_16x16x32_bf16 v[72:75], v[128:131], v[100:103], 0\n\t"
    "s_add_u32 m0, %[l1], 32768\n\t"
    "ds_write_b128 %[rw], v[88:91] offset:2048\n\t"
    "v_mfma_f32_16x16x32_bf16 v[76:79], v[132:135], v[100:103], 0\n\t"
    "global_load_lds_dwordx4 %[av1], s[36:37] sc1\n\t"
    "ds_write_b128 %[rw], v[92:95] offset:3072\n\t"
    "s_waitcnt lgkmcnt(8)\n\t"
    "v_mfma_f32_16x16x32_bf16 v[64:67], v[136:139], v[104:107], v[64:67]\n\t"
    "ds_read_b128 v[96:99], %[ea] offset:8192\n\t"
    "s_add_u32 m0, %[l1], 4096\n\t"
    "v_mfma_f32_16x16x32_bf16 v[68:71], v[140:143], v[104:107], v[68:71]\n\t"
    "ds_read_b128 v[100:103], %[ea] offset:10240\n\t"
    "global_load_lds_dwordx4 %[av2], s[36:37] sc1\n\t"
    "v_mfma_f32_16x16x32_bf16 v[72:75], v[136:139], v[108:111], v[72:75]\n\t"
    "s_add_u32 m0, %[l1], 36864\n\t"
    "v_mfma_f32_16x16x32_bf16 v[76:79], v[140:143], v[108:111], v[76:79]\n\t"
    "global_load_lds_dwordx4 %[av3], s[36:37] sc1\n\t"
    "s_waitcnt lgkmcnt(8)\n\t"
    "v_mfma_f32_16x16x32_bf16 v[64:67], v[144:147], v[112:115], v[64:67]\n\t"
    "ds_read_b128 v[104:107], %[da] offset:8192\n\t"
    "s_add_u32 m0, %[l1], 8192\n\t"
    "v_mfma_f32_16x16x32_bf16 v[68:71], v[148:151], v[112:115], v[68:71]\n\t"
    "ds_read_b128 v[108:111], %[da] offset:10240\n\t"
    "global_load_lds_dwordx4 %[av4], s[36:37] sc1\n\t"
    "v_mfma_f32_16x16x32_bf16 v[72:75], v[144:147], v[116:119], v[72:75]\n\t"
    "s_add_u32 m0, %[l1], 40960\n\t"
    "v_mfma_f32_16x16x32_bf16 v[76:79], v[148:151], v[116:119], v[76:79]\n\t"
    "global_load_lds_dwordx4 %[av5], s[36:37] sc1\n\t"
    "s_waitcnt lgkmcnt(7)\n\t"
    "v_mfma_f32_16x16x32_bf16 v[64:67], v[152:155], v[120:123], v[64:67]\n\t"
    "ds_read_b128 v[112:115], %[ea] offset:12288\n\t"
    "s_add_u32 m0, %[l1], 12288\n\t"
    "v_mfma_f32_16x16x32_bf16 v[68:71], v[156:159], v[120:123], v[68:71]\n\t"
    "ds_read_b128 v[116:119], %[ea] offset:14336\n\t"
    "global_load_lds_dwordx4 %[av6], s[36:37] sc1\n\t"
    "v_mfma_f32_16x16x32_bf16 v[72:75], v[152:155], v[124:127], v[72:75]\n\t"
    "s_add_u32 m0, %[l1], 45056\n\t"
    "v_mfma_f32_16x16x32_bf16 v[76:79], v[156:159], v[124:127], v[76:79]\n\t"
    "global_load_lds_dwordx4 %[av7], s[36:37] sc1\n\t"
    "s_waitcnt lgkmcnt(4)\n\t"
    "v_mfma_f32_16x16x32_bf16 v[64:67], v[160:163], v[96:99], v[64:67]\n\t"
    "ds_read_b128 v[120:123], %[da] offset:12288\n\t"
    "v_mfma_f32_16x16x32_bf16 v[68:71], v[164:167], v[96:99], v[68:71]\n\t"
    "ds_read_b128 v[124:127], %[da] offset:14336\n\t"
    "v_mfma_f32_16x16x32_bf16 v[72:75], v[160:163], v[100:103], v[72:75]\n\t"
    "v_mfma_f32_16x16x32_bf16 v[76:79], v[164:167], v[100:103], v[76:79]\n\t"
    "s_waitcnt lgkmcnt(4)\n\t"
    "v_mfma_f32_16x16x32_bf16 v[64:67], v[168:171], v[104:107], v[64:67]\n\t"
    "ds_read_b128 v[96:99], %[ea] offset:16384\n\t"
    "v_mfma_f32_16x16x32_bf16 v[68:71], v[172:175], v[104:107], v[68:71]\n\t"
    "ds_read_b128 v[100:103], %[ea] offset:18432\n\t"
    "v_mfma_f32_16x16x32_bf16 v[72:75], v[168:171], v[108:111], v[72:75]\n\t"
    "v_mfma_f32_16x16x32_bf16 v[76:79], v[172:175], v[108:111], v[76:79]\n\t"
    "s_waitcnt lgkmcnt(4)\n\t"
    "v_mfma_f32_16x16x32_bf16 v[64:67], v[176:179], v[112:115], v[64:67]\n\t"
    "ds_read_b128 v[104:107], %[da] offset:16384\n\t"
    "v_mfma_f32_16x16x32_bf16 v[68:71], v[180:183], v[112:115], v[68:71]\n\t"
    "ds_read_b128 v[108:111], %[da] offset:18432\n\t"
    "v_mfma_f32_16x16x32_bf16 v[72:75], v[176:179], v[116:119], v[72:75]\n\t"
    "v_mfma_f32_16x16x32_bf16 v[76:79], v[180:183], v[116:119], v[76:79]\n\t"
    "s_waitcnt lgkmcnt(4)\n\t"
    "v_mfma_f32_16x16x32_bf16 v[64:67], v[184:187], v[120:123], v[64:67]\n\t"
    "ds_read_b128 v[112:115], %[ea] offset:20480\n\t"
    "v_mfma_f32_16x16x32_bf16 v[68:71], v[188:191], v[120:123], v[68:71]\n\t"
    "ds_read_b128 v[116:119], %[ea] offset:22528\n\t"
    "v_mfma_f32_16x16x32_bf16 v[72:75], v[184:187], v[124:127], v[72:75]\n\t"
    "v_mfma_f32_16x16x32_bf16 v[76:79], v[188:191], v[124:127], v[76:79]\n\t"
    "s_cmp_eq_u32 %[la], 3\n\t"
    "s_waitcnt vmcnt(12)\n\t"
    "s_cbranch_scc1 22f\n\t"
    "s_waitcnt vmcnt(4)\n\t"
    "22:\n\t"
    "s_barrier\n\t"
    "s_waitcnt lgkmcnt(4)\n\t"
    "v_mfma_f32_16x16x32_bf16 v[64:67], v[192:195], v[96:99], v[64:67]\n\t"
    "ds_read_b128 v[120:123], %[da] offset:20480\n\t"
    "s_add_u32 m0, %[l0], 0\n\t"
    "v_mfma_f32_16x16x32_bf16 v[68:71], v[196:199], v[96:99], v[68:71]\n\t"
    "ds_read_b128 v[124:127], %[da] offset:22528\n\t"
    "global_load_lds_dwordx4 %[av0], s[38:39] sc1\n\t"
    "v_mfma_f32_16x16x32_bf16 v[72:75], v[192:195], v[100:103], v[72:75]\n\t"
    "s_add_u32 m0, %[l0], 32768\n\t"
    "v_mfma_f32_16x16x32_bf16 v[76:79], v[196:199], v[100:103], v[76:79]\n\t"
    "global_load_lds_dwordx4 %[av1], s[38:39] sc1\n\t"
    "s_waitcnt lgkmcnt(4)\n\t"
    "v_mfma_f32_16x16x32_bf16 v[64:67], v[200:203], v[104:107], v[64:67]\n\t"
    "ds_read_b128 v[96:99], %[ea] offset:24576\n\t"
    "s_add_u32 m0, %[l0], 4096\n\t"
    "v_mfma_f32_16x16x32_bf16 v[68:71], v[204:207], v[104:107], v[68:71]\n\t"
    "ds_read_b128 v[100:103], %[ea] offset:26624\n\t"
    "global_load_lds_dwordx4 %[av2], s[38:39] sc1\n\t"
    "v_mfma_f32_16x16x32_bf16 v[72:75], v[200:203], v[108:111], v[72:75]\n\t"
    "s_add_u32 m0, %[l0], 36864\n\t"
    "v_mfma_f32_16x16x32_bf16 v[76:79], v[204:207], v[108:111], v[76:79]\n\t"
    "global_load_lds_dwordx4 %[av3], s[38:39] sc1\n\t"
    "s_add_u32 s36, s36, %[ab]\n\t"
    "s_addc_u32 s37, s37, 0\n\t"
    "s_waitcnt lgkmcnt(4)\n\t"
    "v_mfma_f32_16x16x32_bf16 v[64:67], v[208:211], v[112:115], v[64:67]\n\t"
    "ds_read_b128 v[104:107], %[da] offset:24576\n\t"
    "s_add_u32 m0, %[l0], 8192\n\t"
    "v_mfma_f32_16x16x32_bf16 v[68:71], v[212:215], v[112:115], v[68:71]\n\t"
    "ds_read_b128 v[108:111], %[da] offset:26624\n\t"
    "global_load_lds_dwordx4 %[av4], s[38:39] sc1\n\t"
    "v_mfma_f32_16x16x32_bf16 v[72:75], v[208:211], v[116:119], v[72:75]\n\t"
    "ds_read_b32 v39, %[sa]\n\t"
    "s_add_u32 m0, %[l0], 40960\n\t"
    "v_mfma_f32_16x16x32_bf16 v[76:79], v[212:215], v[116:119], v[76:79]\n\t"
    "global_load_lds_dwordx4 %[av5], s[38:39] sc1\n\t"
    "s_xor_b32 %[l1], %[l1], 0x10000\n\t"
    "s_waitcnt lgkmcnt(5)\n\t"
    "v_mfma_f32_16x16x32_bf16 v[64:67], v[216:219], v[120:123], v[64:67]\n\t"
    "ds_read_b128 v[112:115], %[ea] offset:28672\n\t"
    "s_add_u32 m0, %[l0], 12288\n\t"
    "v_mfma_f32_16x16x32_bf16 v[68:71], v[220:223], v[120:123], v[68:71]\n\t"
    "ds_read_b128 v[116:119], %[ea] offset:30720\n\t"
    "global_load_lds_dwordx4 %[av6], s[38:39] sc1\n\t"
    "v_mfma_f32_16x16x32_bf16 v[72:75], v[216:219], v[124:127], v[72:75]\n\t"
    "v_xor_b32 %[ea], 0x10000, %[ea]\n\t"
    "s_add_u32 m0, %[l0], 45056\n\t"
    "v_mfma_f32_16x16x32_bf16 v[76:79], v[220:223], v[124:127], v[76:79]\n\t"
    "global_load_lds_dwordx4 %[av7], s[38:39] sc1\n\t"
    "s_waitcnt lgkmcnt(5)\n\t"
    "v_mfma_f32_16x16x32_bf16 v[64:67], v[224:227], v[96:99], v[64:67]\n\t"
    "ds_read_b128 v[120:123], %[da] offset:28672\n\t"
    "v_mfma_f32_16x16x32_bf16 v[68:71], v[228:231], v[96:99], v[68:71]\n\t"
    "ds_read_b128 v[124:127], %[da] offset:30720\n\t"
    "v_mfma_f32_16x16x32_bf16 v[72:75], v[224:227], v[100:103], v[72:75]\n\t"
    "v_xor_b32 %[da], 0x10000, %[da]\n\t"
    "v_mfma_f32_16x16x32_bf16 v[76:79], v[228:231], v[100:103], v[76:79]\n\t"
    "s_waitcnt lgkmcnt(5)\n\t"
    "v_mfma_f32_16x16x32_bf16 v[64:67], v[232:235], v[104:107], v[64:67]\n\t"
    "ds_read_b128 v[96:99], %[ea] offset:0\n\t"
    "v_mfma_f32_16x16x32_bf16 v[68:71], v[236:239], v[104:107], v[68:71]\n\t"
    "ds_read_b128 v[100:103], %[ea] offset:2048\n\t"
    "v_mfma_f32_16x16x32_bf16 v[72:75], v[232:235], v[108:111], v[72:75]\n\t"
    "v_mfma_f32_16x16x32_bf16 v[76:79], v[236:239], v[108:111], v[76:79]\n\t"
    "s_waitcnt lgkmcnt(4)\n\t"
    "v_mfma_f32_16x16x32_bf16 v[64:67], v[240:243], v[112:115], v[64:67]\n\t"
    "ds_read_b128 v[104:107], %[da] offset:0\n\t"
    "v_mad_u32_u24 v39, v39, %[mu], %[ad]\n\t"
    "v_mfma_f32_16x16x32_bf16 v[68:71], v[244:247], v[112:115], v[68:71]\n\t"
    "ds_read_b128 v[108:111], %[da] offset:2048\n\t"
    "v_cmp_ge_i32 vcc, %[lf], v39\n\t"
    "s_cmp_eq_u64 vcc, 0\n\t"
    "s_addc_u32 %[lf], %[lf], 0\n\t"
    "v_mfma_f32_16x16x32_bf16 v[72:75], v[240:243], v[116:119], v[72:75]\n\t"
    "v_mfma_f32_16x16x32_bf16 v[76:79], v[244:247], v[116:119], v[76:79]\n\t"
    "v_cmp_ge_i32 vcc, %[lf], v39\n\t"
    "s_cmp_eq_u64 vcc, 0\n\t"
    "s_addc_u32 %[lf], %[lf], 0\n\t"
    "s_waitcnt lgkmcnt(4)\n\t"
    "v_mfma_f32_16x16x32_bf16 v[80:83], v[248:251], v[120:123], v[64:67]\n\t"
    "ds_read_b128 v[112:115], %[ea] offset:4096\n\t"
    "s_min_i32 %[lf], %[lf], %[cp]\n\t"
    "s_add_u32 %[b], %[b], 1\n\t"
    "v_mfma_f32_16x16x32_bf16 v[84:87], v[252:255], v[120:123], v[68:71]\n\t"
    "ds_read_b128 v[116:119], %[ea] offset:6144\n\t"
    "v_mfma_f32_16x16x32_bf16 v[88:91], v[248:251], v[124:127], v[72:75]\n\t"
    "v_mfma_f32_16x16x32_bf16 v[92:95], v[252:255], v[124:127], v[76:79]\n\t"
    "s_add_u32 s38, s38, %[ab]\n\t"
    "s_addc_u32 s39, s39, 0\n\t"
    "s_xor_b32 %[l0], %[l0], 0x10000\n\t"
    "s_sub_u32 m0, %[lf], %[b]\n\t"
    "s_sub_u32 m0, m0, 1\n\t"
    "s_cmp_lt_u32 m0, %[up]\n\t"
    "s_cbranch_scc1 1b\n\t"
    "s_waitcnt lgkmcnt(0)\n\t"
    "s_nop 4"
    : "=&{v[80:95]}"(pd), [ea] "+&v"(ea), [da] "+&v"(da), [b] "+&s"(b), [lf] "+&s"(lf), "+{s[36:37]}"(p1), "+{s[38:39]}"(p0), [sa] "+&v"(sa), [l1] "+&s"(l1), [l0] "+&s"(l0)
    : [rw] "v"(rw), [ab] "s"(ab), [cp] "s"(cp), [mu] "s"(mu), [ad] "v"(ad), [up] "s"(up), "{v[128:159]}"(W[0]), "{v[160:191]}"(W[1]), "{v[192:223]}"(W[2]), "{v[224:255]}"(W[3]), [av0] "v"(av[0]), [av1] "v"(av[1]), [av2] "v"(av[2]), [av3] "v"(av[3]), [av4] "v"(av[4]), [av5] "v"(av[5]), [av6] "v"(av[6]), [av7] "v"(av[7]), [la] "s"(la)
    : "v39", "v64", "v65", "v66", "v67", "v68", "v69", "v70", "v71", "v72", "v73", "v74", "v75", "v76", "v77", "v78", "v79", "v96", "v97", "v98", "v99", "v100", "v101", "v102", "v103", "v104", "v105", "v106", "v107", "v108", "v109", "v110", "v111", "v112", "v113", "v114", "v115", "v116", "v117", "v118", "v119", "v120", "v121", "v122", "v123", "v124", "v125", "v126", "v127", "vcc", "scc", "m0", "memory");
}
__device__ __forceinline__ void fast_ld2(const u32x32 (&W)[4], u32x16& pd, unsigned ea, unsigned da, int& b, int& lf, int cp, unsigned up, unsigned sa, unsigned mu, unsigned ad,
    unsigned rw, unsigned ab, const char* p1, const char* p0, unsigned l1, unsigned l0, const unsigned (&av)[8], int la) {
  asm volatile("s_setprio 0\n\t"
    "s_cmp_eq_u32 %[la], 3\n\t"
    "s_waitcnt vmcnt(6)\n\t"
    "s_cbranch_scc1 20f\n\t"
    "s_waitcnt vmcnt(2)\n\t"
    "20:\n\t"
    "s_barrier\n\t"
    "ds_read_b128 v[96:99], %[ea] offset:0\n\t"
    "ds_read_b128 v[100:103], %[ea] offset:2048\n\t"
    "ds_read_b128 v[104:107], %[da] offset:0\n\t"
    "ds_read_b128 v[108:111], %[da] offset:2048\n\t"
    "ds_read_b128 v[112:115], %[ea] offset:4096\n\t"
    "ds_read_b128 v[116:119], %[ea] offset:6144\n\t"
    "s_branch 2f\n\t"
    "1:\n\t"
    "s_cmp_eq_u32 %[la], 3\n\t"
    "s_waitcnt vmcnt(6)\n\t"
    "s_cbranch_scc1 21f\n\t"
    "s_waitcnt vmcnt(2)\n\t"
    "21:\n\t"
    "s_barrier\n\t"
    "2:\n\t"
    "s_waitcnt lgkmcnt(4)\n\t"
    "v_mfma_f32_16x16x32_bf16 v[64:67], v[128:131], v[96:99], 0\n\t"
    "ds_read_b128 v[120:123], %[da] offset:4096\n\t"
    "s_add_u32 m0, %[l1], 0\n\t"
    "ds_write_b128 %[rw], v[80:83] offset:0\n\t"
    "v_mfma_f32_16x16x32_bf16 v[68:71], v[132:135], v[96:99], 0\n\t"
    "ds_read_b128 v[124:127], %[da] offset:6144\n\t"
    "global_load_lds_dwordx4 %[av0], s[36:37] sc1\n\t"
    "ds_write_b128 %[rw], v[84:87] offset:1024\n\t"
    "v_mfma_f32_16x16x32_bf16 v[72:75], v[128:131], v[100:103], 0\n\t"
    "ds_write_b128 %[rw], v[88:91] offset:2048\n\t"
    "v_mfma_f32_16x16x32_bf16 v[76:79], v[132:135], v[100:103], 0\n\t"
    "ds_write_b128 %[rw], v[92:95] offset:3072\n\t"
    "s_waitcnt lgkmcnt(8)\n\t"
    "v_mfma_f32_16x16x32_bf16 v[64:67], v[136:139], v[104:107], v[64:67]\n\t"
    "ds_read_b128 v[96:99], %[ea] offset:8192\n\t"
    "s_add_u32 m0, %[l1], 4096\n\t"
    "v_mfma_f32_16x16x32_bf16 v[68:71], v[140:143], v[104:107], v[68:71]\n\t"
    "ds_read_b128 v[100:103], %[ea] offset:10240\n\t"
    "global_load_lds_dwordx4 %[av2], s[36:37] sc1\n\t"
    "v_mfma_f32_16x16x32_bf16 v[72:75], v[136:139], v[108:111], v[72:75]\n\t"
    "v_mfma_f32_16x16x32_bf16 v[76:79], v[140:143], v[108:111], v[76:79]\n\t"
    "s_waitcnt lgkmcnt(8)\n\t"
    "v_mfma_f32_16x16x32_bf16 v[64:67], v[144:147], v[112:115], v[64:67]\n\t"
    "ds_read_b128 v[104:107], %[da] offset:8192\n\t"
    "s_add_u32 m0, %[l1], 8192\n\t"
    "v_mfma_f32_16x16x32_bf16 v[68:71], v[148:151], v[112:115], v[68:71]\n\t"
    "ds_read_b128 v[108:111], %[da] offset:10240\n\t"
    "global_load_lds_dwordx4 %[av4], s[36:37] sc1\n\t"
    "v_mfma_f32_16x16x32_bf16 v[72:75], v[144:147], v[116:119], v[72:75]\n\t"
    "v_mfma_f32_16x16x32_bf16 v[76:79], v[148:151], v[116:119], v[76:79]\n\t"
    "s_waitcnt lgkmcnt(7)\n\t"
    "v_mfma_f32_16x16x32_bf16 v[64:67], v[152:155], v[120:123], v[64:67]\n\t"
    "ds_read_b128 v[112:115], %[ea] offset:12288\n\t"
    "s_add_u32 m0, %[l1], 12288\n\t"
    "v_mfma_f32_16x16x32_bf16 v[68:71], v[156:159], v[120:123], v[68:71]\n\t"
    "ds_read_b128 v[116:119], %[ea] offset:14336\n\t"
    "global_load_lds_dwordx4 %[av6], s[36:37] sc1\n\t"
    "v_mfma_f32_16x16x32_bf16 v[72:75], v[152:155], v[124:127], v[72:75]\n\t"
    "v_mfma_f32_16x16x32_bf16 v[76:79], v[156:159], v[124:127], v[76:79]\n\t"
    "s_waitcnt lgkmcnt(4)\n\t"
    "v_mfma_f32_16x16x32_bf16 v[64:67], v[160:163], v[96:99], v[64:67]\n\t"
    "ds_read_b128 v[120:123], %[da] offset:12288\n\t"
    "v_mfma_f32_16x16x32_bf16 v[68:71], v[164:167], v[96:99], v[68:71]\n\t"
    "ds_read_b128 v[124:127], %[da] offset:14336\n\t"
    "v_mfma_f32_16x16x32_bf16 v[72:75], v[160:163], v[100:103], v[72:75]\n\t"
    "v_mfma_f32_16x16x32_bf16 v[76:79], v[164:167], v[100:103], v[76:79]\n\t"
    "s_waitcnt lgkmcnt(4)\n\t"
    "v_mfma_f32_16x16x32_bf16 v[64:67], v[168:171], v[104:107], v[64:67]\n\t"
    "ds_read_b128 v[96:99], %[ea] offset:16384\n\t"
    "v_mfma_f32_16x16x32_bf16 v[68:71], v[172:175], v[104:107], v[68:71]\n\t"
    "ds_read_b128 v[100:103], %[ea] offset:18432\n\t"
    "v_mfma_f32_16x16x32_bf16 v[72:75], v[168:171], v[108:111], v[72:75]\n\t"
    "v_mfma_f32_16x16x32_bf16 v[76:79], v[172:175], v[108:111], v[76:79]\n\t"
    "s_waitcnt lgkmcnt(4)\n\t"
    "v_mfma_f32_16x16x32_bf16 v[64:67], v[176:179], v[112:115], v[64:67]\n\t"
    "ds_read_b128 v[104:107], %[da] offset:16384\n\t"
    "v_mfma_f32_16x16x32_bf16 v[68:71], v[180:183], v[112:115], v[68:71]\n\t"
    "ds_read_b128 v[108:111], %[da] offset:18432\n\t"
    "v_mfma_f32_16x16x32_bf16 v[72:75], v[176:179], v[116:119], v[72:75]\n\t"
    "v_mfma_f32_16x16x32_bf16 v[76:79], v[180:183], v[116:119], v[76:79]\n\t"
    "s_waitcnt lgkmcnt(4)\n\t"
    "v_mfma_f32_16x16x32_bf16 v[64:67], v[184:187], v[120:123], v[64:67]\n\t"
    "ds_read_b128 v[112:115], %[ea] offset:20480\n\t"
    "v_mfma_f32_16x16x32_bf16 v[68:71], v[188:191], v[120:123], v[68:71]\n\t"
    "ds_read_b128 v[116:119], %[ea] offset:22528\n\t"
    "v_mfma_f32_16x16x32_bf16 v[72:75], v[184:187], v[124:127], v[72:75]\n\t"
    "v_mfma_f32_16x16x32_bf16 v[76:79], v[188:191], v[124:127], v[76:79]\n\t"
    "s_cmp_eq_u32 %[la], 3\n\t"
    "s_waitcnt vmcnt(6)\n\t"
    "s_cbranch_scc1 22f\n\t"
    "s_waitcnt vmcnt(2)\n\t"
    "22:\n\t"
    "s_barrier\n\t"
    "s_waitcnt lgkmcnt(4)\n\t"
    "v_mfma_f32_16x16x32_bf16 v[64:67], v[192:195], v[96:99], v[64:67]\n\t"
    "ds_read_b128 v[120:123], %[da] offset:20480\n\t"
    "s_add_u32 m0, %[l0], 0\n\t"
    "v_mfma_f32_16x16x32_bf16 v[68:71], v[196:199], v[96:99], v[68:71]\n\t"
    "ds_read_b128 v[124:127], %[da] offset:22528\n\t"
    "global_load_lds_dwordx4 %[av0], s[38:39] sc1\n\t"
    "v_mfma_f32_16x16x32_bf16 v[72:75], v[192:195], v[100:103], v[72:75]\n\t"
    "v_mfma_f32_16x16x32_bf16 v[76:79], v[196:199], v[100:103], v[76:79]\n\t"
    "s_waitcnt lgkmcnt(4)\n\t"
    "v_mfma_f32_16x16x32_bf16 v[64:67], v[200:203], v[104:107], v[64:67]\n\t"
    "ds_read_b128 v[96:99], %[ea] offset:24576\n\t"
    "s_add_u32 m0, %[l0], 4096\n\t"
    "v_mfma_f32_16x16x32_bf16 v[68:71], v[204:207], v[104:107], v[68:71]\n\t"
    "ds_read_b128 v[100:103], %[ea] offset:26624\n\t"
    "global_load_lds_dwordx4 %[av2], s[38:39] sc1\n\t"
    "v_mfma_f32_16x16x32_bf16 v[72:75], v[200:203], v[108:111], v[72:75]\n\t"
    "v_mfma_f32_16x16x32_bf16 v[76:79], v[204:207], v[108:111], v[76:79]\n\t"
    "s_add_u32 s36, s36, %[ab]\n\t"
    "s_addc_u32 s37, s37, 0\n\t"
    "s_waitcnt lgkmcnt(4)\n\t"
    "v_mfma_f32_16x16x32_bf16 v[64:67], v[208:211], v[112:115], v[64:67]\n\t"
    "ds_read_b128 v[104:107], %[da] offset:24576\n\t"
    "s_add_u32 m0, %[l0], 8192\n\t"
    "v_mfma_f32_16x16x32_bf16 v[68:71], v[212:215], v[112:115], v[68:71]\n\t"
    "ds_read_b128 v[108:111], %[da] offset:26624\n\t"
    "global_load_lds_dwordx4 %[av4], s[38:39] sc1\n\t"
    "v_mfma_f32_16x16x32_bf16 v[72:75], v[208:211], v[116:119], v[72:75]\n\t"
    "ds_read_b32 v39, %[sa]\n\t"
    "v_mfma_f32_16x16x32_bf16 v[76:79], v[212:215], v[116:119], v[76:79]\n\t"
    "s_xor_b32 %[l1], %[l1], 0x10000\n\t"
    "s_waitcnt lgkmcnt(5)\n\t"
    "v_mfma_f32_16x16x32_bf16 v[64:67], v[216:219], v[120:123], v[64:67]\n\t"
    "ds_read_b128 v[112:115], %[ea] offset:28672\n\t"
    "s_add_u32 m0, %[l0], 12288\n\t"
    "v_mfma_f32_16x16x32_bf16 v[68:71], v[220:223], v[120:123], v[68:71]\n\t"
    "ds_read_b128 v[116:119], %[ea] offset:30720\n\t"
    "global_load_lds_dwordx4 %[av6], s[38:39] sc1\n\t"
    "v_mfma_f32_16x16x32_bf16 v[72:75], v[216:219], v[124:127], v[72:75]\n\t"
    "v_xor_b32 %[ea], 0x10000, %[ea]\n\t"
    "v_mfma_f32_16x16x32_bf16 v[76:79], v[220:223], v[124:127], v[76:79]\n\t"
    "s_waitcnt lgkmcnt(5)\n\t"
    "v_mfma_f32_16x16x32_bf16 v[64:67], v[224:227], v[96:99], v[64:67]\n\t"
    "ds_read_b128 v[120:123], %[da] offset:28672\n\t"
    "v_mfma_f32_16x16x32_bf16 v[68:71], v[228:231], v[96:99], v[68:71]\n\t"
    "ds_read_b128 v[124:127], %[da] offset:30720\n\t"
    "v_mfma_f32_16x16x32_bf16 v[72:75], v[224:227], v[100:103], v[72:75]\n\t"
    "v_xor_b32 %[da], 0x10000, %[da]\n\t"
    "v_mfma_f32_16x16x32_bf16 v[76:79], v[228:231], v[100:103], v[76:79]\n\t"
    "s_waitcnt lgkmcnt(5)\n\t"
    "v_mfma_f32_16x16x32_bf16 v[64:67], v[232:235], v[104:107], v[64:67]\n\t"
    "ds_read_b128 v[96:99], %[ea] offset:0\n\t"
    "v_mfma_f32_16x16x32_bf16 v[68:71], v[236:239], v[104:107], v[68:71]\n\t"
    "ds_read_b128 v[100:103], %[ea] offset:2048\n\t"
    "v_mfma_f32_16x16x32_bf16 v[72:75], v[232:235], v[108:111], v[72:75]\n\t"
    "v_mfma_f32_16x16x32_bf16 v[76:79], v[236:239], v[108:111], v[76:79]\n\t"
    "s_waitcnt lgkmcnt(4)\n\t"
    "v_mfma_f32_16x16x32_bf16 v[64:67], v[240:243], v[112:115], v[64:67]\n\t"
    "ds_read_b128 v[104:107], %[da] offset:0\n\t"
    "v_mad_u32_u24 v39, v39, %[mu], %[ad]\n\t"
    "v_mfma_f32_16x16x32_bf16 v[68:71], v[244:247], v[112:115], v[68:71]\n\t"
    "ds_read_b128 v[108:111], %[da] offset:2048\n\t"
    "v_cmp_ge_i32 vcc, %[lf], v39\n\t"
    "s_cmp_eq_u64 vcc, 0\n\t"
    "s_addc_u32 %[lf], %[lf], 0\n\t"
    "v_mfma_f32_16x16x32_bf16 v[72:75], v[240:243], v[116:119], v[72:75]\n\t"
    "v_mfma_f32_16x16x32_bf16 v[76:79], v[244:247], v[116:119], v[76:79]\n\t"
    "v_cmp_ge_i32 vcc, %[lf], v39\n\t"
    "s_cmp_eq_u64 vcc, 0\n\t"
    "s_addc_u32 %[lf], %[lf], 0\n\t"
    "s_waitcnt lgkmcnt(4)\n\t"
    "v_mfma_f32_16x16x32_bf16 v[80:83], v[248:251], v[120:123], v[64:67]\n\t"
    "ds_read_b128 v[112:115], %[ea] offset:4096\n\t"
    "s_min_i32 %[lf], %[lf], %[cp]\n\t"
    "s_add_u32 %[b], %[b], 1\n\t"
    "v_mfma_f32_16x16x32_bf16 v[84:87], v[252:255], v[120:123], v[68:71]\n\t"
    "ds_read_b128 v[116:119], %[ea] offset:6144\n\t"
    "v_mfma_f32_16x16x32_bf16 v[88:91], v[248:251], v[124:127], v[72:75]\n\t"
    "v_mfma_f32_16x16x32_bf16 v[92:95], v[252:255], v[124:127], v[76:79]\n\t"
    "s_add_u32 s38, s38, %[ab]\n\t"
    "s_addc_u32 s39, s39, 0\n\t"
    "s_xor_b32 %[l0], %[l0], 0x10000\n\t"
    "s_sub_u32 m0, %[lf], %[b]\n\t"
    "s_sub_u32 m0, m0, 1\n\t"
    "s_cmp_lt_u32 m0, %[up]\n\t"
    "s_cbranch_scc1 1b\n\t"
    "s_waitcnt lgkmcnt(0)\n\t"
    "s_nop 4"
    : "=&{v[80:95]}"(pd), [ea] "+&v"(ea), [da] "+&v"(da), [b] "+&s"(b), [lf] "+&s"(lf), "+{s[36:37]}"(p1), "+{s[38:39]}"(p0), [sa] "+&v"(sa), [l1] "+&s"(l1), [l0] "+&s"(l0)
    : [rw] "v"(rw), [ab] "s"(ab), [cp] "s"(cp), [mu] "s"(mu), [ad] "v"(ad), [up] "s"(up), "{v[128:159]}"(W[0]), "{v[160:191]}"(W[1]), "{v[192:223]}"(W[2]), "{v[224:255]}"(W[3]), [av0] "v"(av[0]), [av1] "v"(av[1]), [av2] "v"(av[2]), [av3] "v"(av[3]), [av4] "v"(av[4]), [av5] "v"(av[5]), [av6] "v"(av[6]), [av7] "v"(av[7]), [la] "s"(la)
    : "v39", "v64", "v65", "v66", "v67", "v68", "v69", "v70", "v71", "v72", "v73", "v74", "v75", "v76", "v77", "v78", "v79", "v96", "v97", "v98", "v99", "v100", "v101", "v102", "v103", "v104", "v105", "v106", "v107", "v108", "v109", "v110", "v111", "v112", "v113", "v114", "v115", "v116", "v117", "v118", "v119", "v120", "v121", "v122", "v123", "v124", "v125", "v126", "v127", "vcc", "scc", "m0", "memory");
}
__device__ __forceinline__ void fast_st0(const u32x32 (&W)[4], u32x16& pd, unsigned ea, unsigned da, int& b, int& lf, int cp, unsigned up, unsigned sa, unsigned mu, unsigned ad,
    unsigned rw, unsigned ab, const char* p1, const char* p0, unsigned co0, unsigned co1, int po, int pf, unsigned w0, unsigned sl, unsigned dv, const char* db, const char* pg) {
  unsigned st;
  asm volatile("s_barrier\n\t"
    "ds_read_b128 v[96:99], %[ea] offset:0\n\t"
    "ds_read_b128 v[100:103], %[ea] offset:2048\n\t"
    "ds_read_b128 v[104:107], %[da] offset:0\n\t"
    "ds_read_b128 v[108:111], %[da] offset:2048\n\t"
    "ds_read_b128 v[112:115], %[ea] offset:4096\n\t"
    "ds_read_b128 v[116:119], %[ea] offset:6144\n\t"
    "s_branch 2f\n\t"
    "1:\n\t"
    "s_barrier\n\t"
    "2:\n\t"
    "s_waitcnt lgkmcnt(4)\n\t"
    "v_mfma_f32_16x16x32_bf16 v[64:67], v[128:131], v[96:99], 0\n\t"
    "ds_read_b128 v[120:123], %[da] offset:4096\n\t"
    "v_mfma_f32_16x16x32_bf16 v[68:71], v[132:135], v[96:99], 0\n\t"
    "ds_read_b128 v[124:127], %[da] offset:6144\n\t"
    "v_mfma_f32_16x16x32_bf16 v[72:75], v[128:131], v[100:103], 0\n\t"
    "v_mfma_f32_16x16x32_bf16 v[76:79], v[132:135], v[100:103], 0\n\t"
    "s_waitcnt lgkmcnt(4)\n\t"
    "v_mfma_f32_16x16x32_bf16 v[64:67], v[136:139], v[104:107], v[64:67]\n\t"
    "ds_read_b128 v[96:99], %[ea] offset:8192\n\t"
    "v_mfma_f32_16x16x32_bf16 v[68:71], v[140:143], v[104:107], v[68:71]\n\t"
    "ds_read_b128 v[100:103], %[ea] offset:10240\n\t"
    "v_mfma_f32_16x16x32_bf16 v[72:75], v[136:139], v[108:111], v[72:75]\n\t"
    "v_mfma_f32_16x16x32_bf16 v[76:79], v[140:143], v[108:111], v[76:79]\n\t"
    "s_cmp_eq_u32 %[w0], 1\n\t"
    "s_cbranch_scc1 7f\n\t"
    "8:\n\t"
    "s_waitcnt lgkmcnt(4)\n\t"
    "v_mfma_f32_16x16x32_bf16 v[64:67], v[144:147], v[112:115], v[64:67]\n\t"
    "ds_read_b128 v[104:107], %[da] offset:8192\n\t"
    "v_mfma_f32_16x16x32_bf16 v[68:71], v[148:151], v[112:115], v[68:71]\n\t"
    "ds_read_b128 v[108:111], %[da] offset:10240\n\t"
    "v_mfma_f32_16x16x32_bf16 v[72:75], v[144:147], v[116:119], v[72:75]\n\t"
    "v_mfma_f32_16x16x32_bf16 v[76:79], v[148:151], v[116:119], v[76:79]\n\t"
    "s_waitcnt lgkmcnt(4)\n\t"
    "v_mfma_f32_16x16x32_bf16 v[64:67], v[152:155], v[120:123], v[64:67]\n\t"
    "ds_read_b128 v[112:115], %[ea] offset:12288\n\t"
    "v_mfma_f32_16x16x32_bf16 v[68:71], v[156:159], v[120:123], v[68:71]\n\t"
    "ds_read_b128 v[116:119], %[ea] offset:14336\n\t"
    "v_mfma_f32_16x16x32_bf16 v[72:75], v[152:155], v[124:127], v[72:75]\n\t"
    "v_mfma_f32_16x16x32_bf16 v[76:79], v[156:159], v[124:127], v[76:79]\n\t"
    "s_waitcnt lgkmcnt(4)\n\t"
    "v_mfma_f32_16x16x32_bf16 v[64:67], v[160:163], v[96:99], v[64:67]\n\t"
    "ds_read_b128 v[120:123], %[da] offset:12288\n\t"
    "v_mfma_f32_16x16x32_bf16 v[68:71], v[164:167], v[96:99], v[68:71]\n\t"
    "ds_read_b128 v[124:127], %[da] offset:14336\n\t"
    "v_mfma_f32_16x16x32_bf16 v[72:75], v[160:163], v[100:103], v[72:75]\n\t"
    "v_mfma_f32_16x16x32_bf16 v[76:79], v[164:167], v[100:103], v[76:79]\n\t"
    "s_waitcnt lgkmcnt(4)\n\t"
    "v_mfma_f32_16x16x32_bf16 v[64:67], v[168:171], v[104:107], v[64:67]\n\t"
    "ds_read_b128 v[96:99], %[ea] offset:16384\n\t"
    "v_mfma_f32_16x16x32_bf16 v[68:71], v[172:175], v[104:107], v[68:71]\n\t"
    "ds_read_b128 v[100:103], %[ea] offset:18432\n\t"
    "v_mfma_f32_16x16x32_bf16 v[72:75], v[168:171], v[108:111], v[72:75]\n\t"
    "v_mfma_f32_16x16x32_bf16 v[76:79], v[172:175], v[108:111], v[76:79]\n\t"
    "s_waitcnt lgkmcnt(4)\n\t"
    "v_mfma_f32_16x16x32_bf16 v[64:67], v[176:179], v[112:115], v[64:67]\n\t"
    "ds_read_b128 v[104:107], %[da] offset:16384\n\t"
    "v_mfma_f32_16x16x32_bf16 v[68:71], v[180:183], v[112:115], v[68:71]\n\t"
    "ds_read_b128 v[108:111], %[da] offset:18432\n\t"
    "v_mfma_f32_16x16x32_bf16 v[72:75], v[176:179], v[116:119], v[72:75]\n\t"
    "v_mfma_f32_16x16x32_bf16 v[76:79], v[180:183], v[116:119], v[76:79]\n\t"
    "s_waitcnt lgkmcnt(4)\n\t"
    "v_mfma_f32_16x16x32_bf16 v[64:67], v[184:187], v[120:123], v[64:67]\n\t"
    "ds_read_b128 v[112:115], %[ea] offset:20480\n\t"
    "v_mfma_f32_16x16x32_bf16 v[68:71], v[188:191], v[120:123], v[68:71]\n\t"
    "ds_read_b128 v[116:119], %[ea] offset:22528\n\t"
    "v_mfma_f32_16x16x32_bf16 v[72:75], v[184:187], v[124:127], v[72:75]\n\t"
    "v_mfma_f32_16x16x32_bf16 v[76:79], v[188:191], v[124:127], v[76:79]\n\t"
    "s_waitcnt vmcnt(0)\n\t"
    "s_barrier\n\t"
    "s_waitcnt lgkmcnt(4)\n\t"
    "v_mfma_f32_16x16x32_bf16 v[64:67], v[192:195], v[96:99], v[64:67]\n\t"
    "ds_read_b128 v[120:123], %[da] offset:20480\n\t"
    "v_mfma_f32_16x16x32_bf16 v[68:71], v[196:199], v[96:99], v[68:71]\n\t"
    "ds_read_b128 v[124:127], %[da] offset:22528\n\t"
    "v_mfma_f32_16x16x32_bf16 v[72:75], v[192:195], v[100:103], v[72:75]\n\t"
    "ds_read_b128 v[48:51], %[rw] offset:0\n\t"
    "v_mfma_f32_16x16x32_bf16 v[76:79], v[196:199], v[100:103], v[76:79]\n\t"
    "ds_read_b128 v[52:55], %[rw] offset:1024\n\t"
    "s_cmp_eq_u32 %[w0], 0\n\t"
    "s_cbranch_scc1 5f\n\t"
    "6:\n\t"
    "s_waitcnt lgkmcnt(6)\n\t"
    "v_mfma_f32_16x16x32_bf16 v[64:67], v[200:203], v[104:107], v[64:67]\n\t"
    "ds_read_b128 v[96:99], %[ea] offset:24576\n\t"
    "v_mfma_f32_16x16x32_bf16 v[68:71], v[204:207], v[104:107], v[68:71]\n\t"
    "ds_read_b128 v[100:103], %[ea] offset:26624\n\t"
    "v_mfma_f32_16x16x32_bf16 v[72:75], v[200:203], v[108:111], v[72:75]\n\t"
    "ds_read_b128 v[56:59], %[rw] offset:2048\n\t"
    "v_mfma_f32_16x16x32_bf16 v[76:79], v[204:207], v[108:111], v[76:79]\n\t"
    "ds_read_b128 v[60:63], %[rw] offset:3072\n\t"
    "s_waitcnt lgkmcnt(0)\n\t"
    "v_mfma_f32_16x16x32_bf16 v[64:67], v[208:211], v[112:115], v[64:67]\n\t"
    "ds_read_b128 v[104:107], %[da] offset:24576\n\t"
    "v_add_f32 v48, v48, v80\n\t"
    "v_add_f32 v49, v49, v81\n\t"
    "v_mfma_f32_16x16x32_bf16 v[68:71], v[212:215], v[112:115], v[68:71]\n\t"
    "ds_read_b128 v[108:111], %[da] offset:26624\n\t"
    "v_add_f32 v50, v50, v82\n\t"
    "v_mfma_f32_16x16x32_bf16 v[72:75], v[208:211], v[116:119], v[72:75]\n\t"
    "ds_read_b32 v39, %[sa]\n\t"
    "v_add_f32 v51, v51, v83\n\t"
    "v_mfma_f32_16x16x32_bf16 v[76:79], v[212:215], v[116:119], v[76:79]\n\t"
    "v_add_f32 v52, v52, v84\n\t"
    "v_add_f32 v53, v53, v85\n\t"
    "v_mfma_f32_16x16x32_bf16 v[64:67], v[216:219], v[120:123], v[64:67]\n\t"
    "ds_read_b128 v[112:115], %[ea] offset:28672\n\t"
    "v_add_f32 v54, v54, v86\n\t"
    "v_mfma_f32_16x16x32_bf16 v[68:71], v[220:223], v[120:123], v[68:71]\n\t"
    "ds_read_b128 v[116:119], %[ea] offset:30720\n\t"
    "v_add_f32 v55, v55, v87\n\t"
    "v_mfma_f32_16x16x32_bf16 v[72:75], v[216:219], v[124:127], v[72:75]\n\t"
    "v_xor_b32 %[ea], 0x10000, %[ea]\n\t"
    "v_add_f32 v56, v56, v88\n\t"
    "v_add_f32 v57, v57, v89\n\t"
    "v_mfma_f32_16x16x32_bf16 v[76:79], v[220:223], v[124:127], v[76:79]\n\t"
    "v_add_f32 v58, v58, v90\n\t"
    "v_mfma_f32_16x16x32_bf16 v[64:67], v[224:227], v[96:99], v[64:67]\n\t"
    "ds_read_b128 v[120:123], %[da] offset:28672\n\t"
    "v_add_f32 v59, v59, v91\n\t"
    "v_mfma_f32_16x16x32_bf16 v[68:71], v[228:231], v[96:99], v[68:71]\n\t"
    "ds_read_b128 v[124:127], %[da] offset:30720\n\t"
    "v_add_f32 v60, v60, v92\n\t"
    "v_mfma_f32_16x16x32_bf16 v[72:75], v[224:227], v[100:103], v[72:75]\n\t"
    "v_xor_b32 %[da], 0x10000, %[da]\n\t"
    "v_add_f32 v61, v61, v93\n\t"
    "v_add_f32 v62, v62, v94\n\t"
    "v_mfma_f32_16x16x32_bf16 v[76:79], v[228:231], v[100:103], v[76:79]\n\t"
    "v_add_f32 v63, v63, v95\n\t"
    "s_waitcnt lgkmcnt(5)\n\t"
    "v_mfma_f32_16x16x32_bf16 v[64:67], v[232:235], v[104:107], v[64:67]\n\t"
    "ds_read_b128 v[96:99], %[ea] offset:0\n\t"
    "v_cvt_pk_bf16_f32 v40, v48, v49\n\t"
    "v_mfma_f32_16x16x32_bf16 v[68:71], v[236:239], v[104:107], v[68:71]\n\t"
    "ds_read_b128 v[100:103], %[ea] offset:2048\n\t"
    "v_cvt_pk_bf16_f32 v41, v50, v51\n\t"
    "v_cvt_pk_bf16_f32 v42, v52, v53\n\t"
    "v_mfma_f32_16x16x32_bf16 v[72:75], v[232:235], v[108:111], v[72:75]\n\t"
    "v_cvt_pk_bf16_f32 v43, v54, v55\n\t"
    "v_mfma_f32_16x16x32_bf16 v[76:79], v[236:239], v[108:111], v[76:79]\n\t"
    "global_store_dwordx4 %[co0], v[40:43], s[36:37] sc1\n\t"
    "s_waitcnt lgkmcnt(4)\n\t"
    "v_mfma_f32_16x16x32_bf16 v[64:67], v[240:243], v[112:115], v[64:67]\n\t"
    "ds_read_b128 v[104:107], %[da] offset:0\n\t"
    "v_mad_u32_u24 v39, v39, %[mu], %[ad]\n\t"
    "v_cvt_pk_bf16_f32 v44, v56, v57\n\t"
    "v_cvt_pk_bf16_f32 v45, v58, v59\n\t"
    "v_mfma_f32_16x16x32_bf16 v[68:71], v[244:247], v[112:115], v[68:71]\n\t"
    "ds_read_b128 v[108:111], %[da] offset:2048\n\t"
    "v_cmp_ge_i32 vcc, %[lf], v39\n\t"
    "s_cmp_eq_u64 vcc, 0\n\t"
    "s_addc_u32 %[lf], %[lf], 0\n\t"
    "v_cvt_pk_bf16_f32 v46, v60, v61\n\t"
    "v_mfma_f32_16x16x32_bf16 v[72:75], v[240:243], v[116:119], v[72:75]\n\t"
    "v_cvt_pk_bf16_f32 v47, v62, v63\n\t"
    "v_mfma_f32_16x16x32_bf16 v[76:79], v[244:247], v[116:119], v[76:79]\n\t"
    "v_cmp_ge_i32 vcc, %[lf], v39\n\t"
    "s_cmp_eq_u64 vcc, 0\n\t"
    "s_addc_u32 %[lf], %[lf], 0\n\t"
    "global_store_dwordx4 %[co1], v[44:47], s[36:37] sc1\n\t"
    "s_waitcnt lgkmcnt(4)\n\t"
    "v_mfma_f32_16x16x32_bf16 v[80:83], v[248:251], v[120:123], v[64:67]\n\t"
    "ds_read_b128 v[112:115], %[ea] offset:4096\n\t"
    "s_min_i32 %[lf], %[lf], %[cp]\n\t"
    "s_add_u32 %[b], %[b], 1\n\t"
    "v_mfma_f32_16x16x32_bf16 v[84:87], v[252:255], v[120:123], v[68:71]\n\t"
    "ds_read_b128 v[116:119], %[ea] offset:6144\n\t"
    "v_mfma_f32_16x16x32_bf16 v[88:91], v[248:251], v[124:127], v[72:75]\n\t"
    "v_mfma_f32_16x16x32_bf16 v[92:95], v[252:255], v[124:127], v[76:79]\n\t"
    "s_mov_b64 s[36:37], s[38:39]\n\t"
    "s_add_u32 s38, s38, %[ab]\n\t"
    "s_addc_u32 s39, s39, 0\n\t"
    "s_sub_u32 m0, %[lf], %[b]\n\t"
    "s_sub_u32 m0, m0, 1\n\t"
    "s_cmp_lt_u32 m0, %[up]\n\t"
    "s_cbranch_scc1 1b\n\t"
    "s_waitcnt lgkmcnt(0)\n\t"
    "s_nop 4\n\t"
    "s_branch 9f\n\t"
    "5:\n\t"
    "s_add_u32 %[st], %[b], %[po]\n\t"
    "s_max_i32 %[st], %[st], %[p0]\n\t"
    "v_mov_b32 v39, %[st]\n\t"
    "v_mov_b32 v38, 0\n\t"
    "s_mov_b64 exec, 1\n\t"
    "global_store_dword v38, v39, %[pg] sc1\n\t"
    "s_mov_b64 exec, -1\n\t"
    "s_mov_b32 %[p0], %[p1]\n\t"
    "s_branch 6b\n\t"
    "7:\n\t"
    "s_cmp_ge_i32 %[lf], %[cp]\n\t"
    "s_cbranch_scc1 8b\n\t"
    "s_mov_b32 m0, %[sl]\n\t"
    "s_nop 0\n\t"
    "global_load_lds_dword %[dv], %[db] sc1\n\t"
    "s_branch 8b\n\t"
    "9:"
    : "=&{v[80:95]}"(pd), [ea] "+&v"(ea), [da] "+&v"(da), [b] "+&s"(b), [lf] "+&s"(lf), "+{s[36:37]}"(p1), "+{s[38:39]}"(p0), [sa] "+&v"(sa), [st] "=&s"(st), [sl] "+&s"(sl), [p0] "+&s"(pf)
    : [rw] "v"(rw), [ab] "s"(ab), [cp] "s"(cp), [mu] "s"(mu), [ad] "v"(ad), [up] "s"(up), "{v[128:159]}"(W[0]), "{v[160:191]}"(W[1]), "{v[192:223]}"(W[2]), "{v[224:255]}"(W[3]), [co0] "v"(co0), [w0] "s"(w0), [dv] "v"(dv), [db] "s"(db), [pg] "s"(pg), [po] "s"(po - 1), [p1] "s"(b + po), [co1] "v"(co1)
    : "v38", "v39", "v40", "v41", "v42", "v43", "v44", "v45", "v46", "v47", "v48", "v49", "v50", "v51", "v52", "v53", "v54", "v55", "v56", "v57", "v58", "v59", "v60", "v61", "v62", "v63", "v64", "v65", "v66", "v67", "v68", "v69", "v70", "v71", "v72", "v73", "v74", "v75", "v76", "v77", "v78", "v79", "v96", "v97", "v98", "v99", "v100", "v101", "v102", "v103", "v104", "v105", "v106", "v107", "v108", "v109", "v110", "v111", "v112", "v113", "v114", "v115", "v116", "v117", "v118", "v119", "v120", "v121", "v122", "v123", "v124", "v125", "v126", "v127", "vcc", "scc", "m0", "memory");
}
__device__ __forceinline__ void fast_st1(const u32x32 (&W)[4], u32x16& pd, unsigned ea, unsigned da, int& b, int& lf, int cp, unsigned up, unsigned sa, unsigned mu, unsigned ad,
    unsigned rw, unsigned ab, const char* p1, const char* p0, unsigned co0, unsigned co1, int po, int pf, unsigned w0, unsigned sl, unsigned dv, const char* db, const char* pg) {
  unsigned st;
  asm volatile("s_barrier\n\t"
    "ds_read_b128 v[96:99], %[ea] offset:0\n\t"
    "ds_read_b128 v[100:103], %[ea] offset:2048\n\t"
    "ds_read_b128 v[104:107], %[da] offset:0\n\t"
    "ds_read_b128 v[108:111], %[da] offset:2048\n\t"
    "ds_read_b128 v[112:115], %[ea] offset:4096\n\t"
    "ds_read_b128 v[116:119], %[ea] offset:6144\n\t"
    "s_branch 2f\n\t"
    "1:\n\t"
    "s_barrier\n\t"
    "2:\n\t"
    "s_waitcnt lgkmcnt(4)\n\t"
    "v_mfma_f32_16x16x32_bf16 v[64:67], v[128:131], v[96:99], 0\n\t"
    "ds_read_b128 v[120:123], %[da] offset:4096\n\t"
    "v_mfma_f32_16x16x32_bf16 v[68:71], v[132:135], v[96:99], 0\n\t"
    "ds_read_b128 v[124:127], %[da] offset:6144\n\t"
    "v_mfma_f32_16x16x32_bf16 v[72:75], v[128:131], v[100:103], 0\n\t"
    "v_mfma_f32_16x16x32_bf16 v[76:79], v[132:135], v[100:103], 0\n\t"
    "s_waitcnt lgkmcnt(4)\n\t"
    "v_mfma_f32_16x16x32_bf16 v[64:67], v[136:139], v[104:107], v[64:67]\n\t"
    "ds_read_b128 v[96:99], %[ea] offset:8192\n\t"
    "v_mfma_f32_16x16x32_bf16 v[68:71], v[140:143], v[104:107], v[68:71]\n\t"
    "ds_read_b128 v[100:103], %[ea] offset:10240\n\t"
    "v_mfma_f32_16x16x32_bf16 v[72:75], v[136:139], v[108:111], v[72:75]\n\t"
    "v_mfma_f32_16x16x32_bf16 v[76:79], v[140:143], v[108:111], v[76:79]\n\t"
    "s_cmp_eq_u32 %[w0], 1\n\t"
    "s_cbranch_scc1 7f\n\t"
    "8:\n\t"
    "s_waitcnt lgkmcnt(4)\n\t"
    "v_mfma_f32_16x16x32_bf16 v[64:67], v[144:147], v[112:115], v[64:67]\n\t"
    "ds_read_b128 v[104:107], %[da] offset:8192\n\t"
    "v_mfma_f32_16x16x32_bf16 v[68:71], v[148:151], v[112:115], v[68:71]\n\t"
    "ds_read_b128 v[108:111], %[da] offset:10240\n\t"
    "v_mfma_f32_16x16x32_bf16 v[72:75], v[144:147], v[116:119], v[72:75]\n\t"
    "v_mfma_f32_16x16x32_bf16 v[76:79], v[148:151], v[116:119], v[76:79]\n\t"
    "s_waitcnt lgkmcnt(4)\n\t"
    "v_mfma_f32_16x16x32_bf16 v[64:67], v[152:155], v[120:123], v[64:67]\n\t"
    "ds_read_b128 v[112:115], %[ea] offset:12288\n\t"
    "v_mfma_f32_16x16x32_bf16 v[68:71], v[156:159], v[120:123], v[68:71]\n\t"
    "ds_read_b128 v[116:119], %[ea] offset:14336\n\t"
    "v_mfma_f32_16x16x32_bf16 v[72:75], v[152:155], v[124:127], v[72:75]\n\t"
    "v_mfma_f32_16x16x32_bf16 v[76:79], v[156:159], v[124:127], v[76:79]\n\t"
    "s_waitcnt lgkmcnt(4)\n\t"
    "v_mfma_f32_16x16x32_bf16 v[64:67], v[160:163], v[96:99], v[64:67]\n\t"
    "ds_read_b128 v[120:123], %[da] offset:12288\n\t"
    "v_mfma_f32_16x16x32_bf16 v[68:71], v[164:167], v[96:99], v[68:71]\n\t"
    "ds_read_b128 v[124:127], %[da] offset:14336\n\t"
    "v_mfma_f32_16x16x32_bf16 v[72:75], v[160:163], v[100:103], v[72:75]\n\t"
    "v_mfma_f32_16x16x32_bf16 v[76:79], v[164:167], v[100:103], v[76:79]\n\t"
    "s_waitcnt lgkmcnt(4)\n\t"
    "v_mfma_f32_16x16x32_bf16 v[64:67], v[168:171], v[104:107], v[64:67]\n\t"
    "ds_read_b128 v[96:99], %[ea] offset:16384\n\t"
    "v_mfma_f32_16x16x32_bf16 v[68:71], v[172:175], v[104:107], v[68:71]\n\t"
    "ds_read_b128 v[100:103], %[ea] offset:18432\n\t"
    "v_mfma_f32_16x16x32_bf16 v[72:75], v[168:171], v[108:111], v[72:75]\n\t"
    "v_mfma_f32_16x16x32_bf16 v[76:79], v[172:175], v[108:111], v[76:79]\n\t"
    "s_waitcnt lgkmcnt(4)\n\t"
    "v_mfma_f32_16x16x32_bf16 v[64:67], v[176:179], v[112:115], v[64:67]\n\t"
    "ds_read_b128 v[104:107], %[da] offset:16384\n\t"
    "v_mfma_f32_16x16x32_bf16 v[68:71], v[180:183], v[112:115], v[68:71]\n\t"
    "ds_read_b128 v[108:111], %[da] offset:18432\n\t"
    "v_mfma_f32_16x16x32_bf16 v[72:75], v[176:179], v[116:119], v[72:75]\n\t"
    "v_mfma_f32_16x16x32_bf16 v[76:79], v[180:183], v[116:119], v[76:79]\n\t"
    "s_waitcnt lgkmcnt(4)\n\t"
    "v_mfma_f32_16x16x32_bf16 v[64:67], v[184:187], v[120:123], v[64:67]\n\t"
    "ds_read_b128 v[112:115], %[ea] offset:20480\n\t"
    "v_mfma_f32_16x16x32_bf16 v[68:71], v[188:191], v[120:123], v[68:71]\n\t"
    "ds_read_b128 v[116:119], %[ea] offset:22528\n\t"
    "v_mfma_f32_16x16x32_bf16 v[72:75], v[184:187], v[124:127], v[72:75]\n\t"
    "v_mfma_f32_16x16x32_bf16 v[76:79], v[188:191], v[124:127], v[76:79]\n\t"
    "s_waitcnt vmcnt(0)\n\t"
    "s_barrier\n\t"
    "s_waitcnt lgkmcnt(4)\n\t"
    "v_mfma_f32_16x16x32_bf16 v[64:67], v[192:195], v[96:99], v[64:67]\n\t"
    "ds_read_b128 v[120:123], %[da] offset:20480\n\t"
    "v_mfma_f32_16x16x32_bf16 v[68:71], v[196:199], v[96:99], v[68:71]\n\t"
    "ds_read_b128 v[124:127], %[da] offset:22528\n\t"
    "v_mfma_f32_16x16x32_bf16 v[72:75], v[192:195], v[100:103], v[72:75]\n\t"
    "ds_read_b128 v[48:51], %[rw] offset:0\n\t"
    "v_mfma_f32_16x16x32_bf16 v[76:79], v[196:199], v[100:103], v[76:79]\n\t"
    "ds_read_b128 v[52:55], %[rw] offset:1024\n\t"
    "s_cmp_eq_u32 %[w0], 0\n\t"
    "s_cbranch_scc1 5f\n\t"
    "6:\n\t"
    "s_waitcnt lgkmcnt(6)\n\t"
    "v_mfma_f32_16x16x32_bf16 v[64:67], v[200:203], v[104:107], v[64:67]\n\t"
    "ds_read_b128 v[96:99], %[ea] offset:24576\n\t"
    "v_mfma_f32_16x16x32_bf16 v[68:71], v[204:207], v[104:107], v[68:71]\n\t"
    "ds_read_b128 v[100:103], %[ea] offset:26624\n\t"
    "v_mfma_f32_16x16x32_bf16 v[72:75], v[200:203], v[108:111], v[72:75]\n\t"
    "ds_read_b128 v[56:59], %[rw] offset:2048\n\t"
    "v_mfma_f32_16x16x32_bf16 v[76:79], v[204:207], v[108:111], v[76:79]\n\t"
    "ds_read_b128 v[60:63], %[rw] offset:3072\n\t"
    "s_waitcnt lgkmcnt(0)\n\t"
    "v_mfma_f32_16x16x32_bf16 v[64:67], v[208:211], v[112:115], v[64:67]\n\t"
    "ds_read_b128 v[104:107], %[da] offset:24576\n\t"
    "v_add_f32 v48, v48, v80\n\t"
    "v_add_f32 v49, v49, v81\n\t"
    "v_add_f32 v50, v50, v82\n\t"
    "v_mfma_f32_16x16x32_bf16 v[68:71], v[212:215], v[112:115], v[68:71]\n\t"
    "ds_read_b128 v[108:111], %[da] offset:26624\n\t"
    "v_add_f32 v51, v51, v83\n\t"
    "v_add_f32 v52, v52, v84\n\t"
    "v_add_f32 v53, v53, v85\n\t"
    "v_mfma_f32_16x16x32_bf16 v[72:75], v[208:211], v[116:119], v[72:75]\n\t"
    "ds_read_b32 v39, %[sa]\n\t"
    "v_add_f32 v54, v54, v86\n\t"
    "v_add_f32 v55, v55, v87\n\t"
    "v_add_f32 v56, v56, v88\n\t"
    "v_mfma_f32_16x16x32_bf16 v[76:79], v[212:215], v[116:119], v[76:79]\n\t"
    "v_add_f32 v57, v57, v89\n\t"
    "v_add_f32 v58, v58, v90\n\t"
    "v_add_f32 v59, v59, v91\n\t"
    "v_mfma_f32_16x16x32_bf16 v[64:67], v[216:219], v[120:123], v[64:67]\n\t"
    "ds_read_b128 v[112:115], %[ea] offset:28672\n\t"
    "v_add_f32 v60, v60, v92\n\t"
    "v_add_f32 v61, v61, v93\n\t"
    "v_add_f32 v62, v62, v94\n\t"
    "v_mfma_f32_16x16x32_bf16 v[68:71], v[220:223], v[120:123], v[68:71]\n\t"
    "ds_read_b128 v[116:119], %[ea] offset:30720\n\t"
    "v_add_f32 v63, v63, v95\n\t"
    "v_mul_f32 v40, %[nl], v48\n\t"
    "v_mul_f32 v41, %[nl], v49\n\t"
    "v_mfma_f32_16x16x32_bf16 v[72:75], v[216:219], v[124:127], v[72:75]\n\t"
    "v_xor_b32 %[ea], 0x10000, %[ea]\n\t"
    "v_mul_f32 v42, %[nl], v50\n\t"
    "v_mul_f32 v43, %[nl], v51\n\t"
    "v_mul_f32 v44, %[nl], v56\n\t"
    "v_mfma_f32_16x16x32_bf16 v[76:79], v[220:223], v[124:127], v[76:79]\n\t"
    "v_mul_f32 v45, %[nl], v57\n\t"
    "v_mul_f32 v46, %[nl], v58\n\t"
    "v_mul_f32 v47, %[nl], v59\n\t"
    "v_mfma_f32_16x16x32_bf16 v[64:67], v[224:227], v[96:99], v[64:67]\n\t"
    "ds_read_b128 v[120:123], %[da] offset:28672\n\t"
    "v_exp_f32 v40, v40\n\t"
    "v_exp_f32 v41, v41\n\t"
    "v_exp_f32 v42, v42\n\t"
    "v_mfma_f32_16x16x32_bf16 v[68:71], v[228:231], v[96:99], v[68:71]\n\t"
    "ds_read_b128 v[124:127], %[da] offset:30720\n\t"
    "v_exp_f32 v43, v43\n\t"
    "v_exp_f32 v44, v44\n\t"
    "v_exp_f32 v45, v45\n\t"
    "v_mfma_f32_16x16x32_bf16 v[72:75], v[224:227], v[100:103], v[72:75]\n\t"
    "v_xor_b32 %[da], 0x10000, %[da]\n\t"
    "v_exp_f32 v46, v46\n\t"
    "v_exp_f32 v47, v47\n\t"
    "v_add_f32 v40, 1.0, v40\n\t"
    "v_mfma_f32_16x16x32_bf16 v[76:79], v[228:231], v[100:103], v[76:79]\n\t"
    "v_add_f32 v41, 1.0, v41\n\t"
    "v_add_f32 v42, 1.0, v42\n\t"
    "v_add_f32 v43, 1.0, v43\n\t"
    "s_waitcnt lgkmcnt(5)\n\t"
    "v_mfma_f32_16x16x32_bf16 v[64:67], v[232:235], v[104:107], v[64:67]\n\t"
    "ds_read_b128 v[96:99], %[ea] offset:0\n\t"
    "v_add_f32 v44, 1.0, v44\n\t"
    "v_add_f32 v45, 1.0, v45\n\t"
    "v_add_f32 v46, 1.0, v46\n\t"
    "v_mfma_f32_16x16x32_bf16 v[68:71], v[236:239], v[104:107], v[68:71]\n\t"
    "ds_read_b128 v[100:103], %[ea] offset:2048\n\t"
    "v_add_f32 v47, 1.0, v47\n\t"
    "v_rcp_f32 v40, v40\n\t"
    "v_rcp_f32 v41, v41\n\t"
    "v_mfma_f32_16x16x32_bf16 v[72:75], v[232:235], v[108:111], v[72:75]\n\t"
    "v_rcp_f32 v42, v42\n\t"
    "v_rcp_f32 v43, v43\n\t"
    "v_rcp_f32 v44, v44\n\t"
    "v_mfma_f32_16x16x32_bf16 v[76:79], v[236:239], v[108:111], v[76:79]\n\t"
    "v_rcp_f32 v45, v45\n\t"
    "v_rcp_f32 v46, v46\n\t"
    "v_rcp_f32 v47, v47\n\t"
    "s_waitcnt lgkmcnt(4)\n\t"
    "v_mfma_f32_16x16x32_bf16 v[64:67], v[240:243], v[112:115], v[64:67]\n\t"
    "ds_read_b128 v[104:107], %[da] offset:0\n\t"
    "v_mad_u32_u24 v39, v39, %[mu], %[ad]\n\t"
    "v_mul_f32 v40, v48, v40\n\t"
    "v_mul_f32 v41, v49, v41\n\t"
    "v_mul_f32 v42, v50, v42\n\t"
    "v_mfma_f32_16x16x32_bf16 v[68:71], v[244:247], v[112:115], v[68:71]\n\t"
    "ds_read_b128 v[108:111], %[da] offset:2048\n\t"
    "v_cmp_ge_i32 vcc, %[lf], v39\n\t"
    "s_cmp_eq_u64 vcc, 0\n\t"
    "s_addc_u32 %[lf], %[lf], 0\n\t"
    "v_mul_f32 v43, v51, v43\n\t"
    "v_mul_f32 v44, v56, v44\n\t"
    "v_mul_f32 v45, v57, v45\n\t"
    "v_mfma_f32_16x16x32_bf16 v[72:75], v[240:243], v[116:119], v[72:75]\n\t"
    "v_mul_f32 v46, v58, v46\n\t"
    "v_mul_f32 v47, v59, v47\n\t"
    "v_mul_f32 v40, v40, v52\n\t"
    "v_mfma_f32_16x16x32_bf16 v[76:79], v[244:247], v[116:119], v[76:79]\n\t"
    "v_cmp_ge_i32 vcc, %[lf], v39\n\t"
    "s_cmp_eq_u64 vcc, 0\n\t"
    "s_addc_u32 %[lf], %[lf], 0\n\t"
    "v_mul_f32 v41, v41, v53\n\t"
    "v_mul_f32 v42, v42, v54\n\t"
    "v_mul_f32 v43, v43, v55\n\t"
    "s_waitcnt lgkmcnt(4)\n\t"
    "v_mfma_f32_16x16x32_bf16 v[80:83], v[248:251], v[120:123], v[64:67]\n\t"
    "ds_read_b128 v[112:115], %[ea] offset:4096\n\t"
    "s_min_i32 %[lf], %[lf], %[cp]\n\t"
    "s_add_u32 %[b], %[b], 1\n\t"
    "v_mul_f32 v44, v44, v60\n\t"
    "v_mul_f32 v45, v45, v61\n\t"
    "v_mul_f32 v46, v46, v62\n\t"
    "v_mfma_f32_16x16x32_bf16 v[84:87], v[252:255], v[120:123], v[68:71]\n\t"
    "ds_read_b128 v[116:119], %[ea] offset:6144\n\t"
    "v_mul_f32 v47, v47, v63\n\t"
    "v_cvt_pk_bf16_f32 v48, v40, v41\n\t"
    "v_cvt_pk_bf16_f32 v49, v42, v43\n\t"
    "v_mfma_f32_16x16x32_bf16 v[88:91], v[248:251], v[124:127], v[72:75]\n\t"
    "v_cvt_pk_bf16_f32 v50, v44, v45\n\t"
    "v_cvt_pk_bf16_f32 v51, v46, v47\n\t"
    "s_nop 1\n\t"
    "v_mfma_f32_16x16x32_bf16 v[92:95], v[252:255], v[124:127], v[76:79]\n\t"
    "v_permlane16_swap_b32 v48, v50\n\t"
    "v_permlane16_swap_b32 v49, v51\n\t"
    "global_store_dwordx4 %[co0], v[48:51], s[36:37] sc1\n\t"
    "s_mov_b64 s[36:37], s[38:39]\n\t"
    "s_add_u32 s38, s38, %[ab]\n\t"
    "s_addc_u32 s39, s39, 0\n\t"
    "s_sub_u32 m0, %[lf], %[b]\n\t"
    "s_sub_u32 m0, m0, 1\n\t"
    "s_cmp_lt_u32 m0, %[up]\n\t"
    "s_cbranch_scc1 1b\n\t"
    "s_waitcnt lgkmcnt(0)\n\t"
    "s_nop 4\n\t"
    "s_branch 9f\n\t"
    "5:\n\t"
    "s_add_u32 %[st], %[b], %[po]\n\t"
    "s_max_i32 %[st], %[st], %[p0]\n\t"
    "v_mov_b32 v39, %[st]\n\t"
    "v_mov_b32 v38, 0\n\t"
    "s_mov_b64 exec, 1\n\t"
    "global_store_dword v38, v39, %[pg] sc1\n\t"
    "s_mov_b64 exec, -1\n\t"
    "s_mov_b32 %[p0], %[p1]\n\t"
    "s_branch 6b\n\t"
    "7:\n\t"
    "s_cmp_ge_i32 %[lf], %[cp]\n\t"
    "s_cbranch_scc1 8b\n\t"
    "s_mov_b32 m0, %[sl]\n\t"
    "s_nop 0\n\t"
    "global_load_lds_dword %[dv], %[db] sc1\n\t"
    "s_branch 8b\n\t"
    "9:"
    : "=&{v[80:95]}"(pd), [ea] "+&v"(ea), [da] "+&v"(da), [b] "+&s"(b), [lf] "+&s"(lf), "+{s[36:37]}"(p1), "+{s[38:39]}"(p0), [sa] "+&v"(sa), [st] "=&s"(st), [sl] "+&s"(sl), [p0] "+&s"(pf)
    : [rw] "v"(rw), [ab] "s"(ab), [cp] "s"(cp), [mu] "s"(mu), [ad] "v"(ad), [up] "s"(up), "{v[128:159]}"(W[0]), "{v[160:191]}"(W[1]), "{v[192:223]}"(W[2]), "{v[224:255]}"(W[3]), [co0] "v"(co0), [w0] "s"(w0), [dv] "v"(dv), [db] "s"(db), [pg] "s"(pg), [po] "s"(po - 1), [p1] "s"(b + po), [nl] "s"(-LOG2E)
    : "v38", "v39", "v40", "v41", "v42", "v43", "v44", "v45", "v46", "v47", "v48", "v49", "v50", "v51", "v52", "v53", "v54", "v55", "v56", "v57", "v58", "v59", "v60", "v61", "v62", "v63", "v64", "v65", "v66", "v67", "v68", "v69", "v70", "v71", "v72", "v73", "v74", "v75", "v76", "v77", "v78", "v79", "v96", "v97", "v98", "v99", "v100", "v101", "v102", "v103", "v104", "v105", "v106", "v107", "v108", "v109", "v110", "v111", "v112", "v113", "v114", "v115", "v116", "v117", "v118", "v119", "v120", "v121", "v122", "v123", "v124", "v125", "v126", "v127", "vcc", "scc", "m0", "memory");
}
__device__ __forceinline__ void fast_st2(const u32x32 (&W)[4], u32x16& pd, unsigned ea, unsigned da, int& b, int& lf, int cp, unsigned up, unsigned sa, unsigned mu, unsigned ad,
    unsigned rw, unsigned ab, const char* p1, const char* p0, unsigned co0, unsigned co1, int po, int pf, unsigned w0, unsigned sl, unsigned dv, const char* db, const char* pg) {
  unsigned st;
  asm volatile("s_barrier\n\t"
    "ds_read_b128 v[96:99], %[ea] offset:0\n\t"
    "ds_read_b128 v[100:103], %[ea] offset:2048\n\t"
    "ds_read_b128 v[104:107], %[da] offset:0\n\t"
    "ds_read_b128 v[108:111], %[da] offset:2048\n\t"
    "ds_read_b128 v[112:115], %[ea] offset:4096\n\t"
    "ds_read_b128 v[116:119], %[ea] offset:6144\n\t"
    "s_branch 2f\n\t"
    "1:\n\t"
    "s_barrier\n\t"
    "2:\n\t"
    "s_waitcnt lgkmcnt(4)\n\t"
    "v_mfma_f32_16x16x32_bf16 v[64:67], v[128:131], v[96:99], 0\n\t"
    "ds_read_b128 v[120:123], %[da] offset:4096\n\t"
    "v_mfma_f32_16x16x32_bf16 v[68:71], v[132:135], v[96:99], 0\n\t"
    "ds_read_b128 v[124:127], %[da] offset:6144\n\t"
    "v_mfma_f32_16x16x32_bf16 v[72:75], v[128:131], v[100:103], 0\n\t"
    "v_mfma_f32_16x16x32_bf16 v[76:79], v[132:135], v[100:103], 0\n\t"
    "s_waitcnt lgkmcnt(4)\n\t"
    "v_mfma_f32_16x16x32_bf16 v[64:67], v[136:139], v[104:107], v[64:67]\n\t"
    "ds_read_b128 v[96:99], %[ea] offset:8192\n\t"
    "v_mfma_f32_16x16x32_bf16 v[68:71], v[140:143], v[104:107], v[68:71]\n\t"
    "ds_read_b128 v[100:103], %[ea] offset:10240\n\t"
    "v_mfma_f32_16x16x32_bf16 v[72:75], v[136:139], v[108:111], v[72:75]\n\t"
    "v_mfma_f32_16x16x32_bf16 v[76:79], v[140:143], v[108:111], v[76:79]\n\t"
    "s_cmp_eq_u32 %[w0], 1\n\t"
    "s_cbranch_scc1 7f\n\t"
    "8:\n\t"
    "s_waitcnt lgkmcnt(4)\n\t"
    "v_mfma_f32_16x16x32_bf16 v[64:67], v[144:147], v[112:115], v[64:67]\n\t"
    "ds_read_b128 v[104:107], %[da] offset:8192\n\t"
    "v_mfma_f32_16x16x32_bf16 v[68:71], v[148:151], v[112:115], v[68:71]\n\t"
    "ds_read_b128 v[108:111], %[da] offset:10240\n\t"
    "v_mfma_f32_16x16x32_bf16 v[72:75], v[144:147], v[116:119], v[72:75]\n\t"
    "v_mfma_f32_16x16x32_bf16 v[76:79], v[148:151], v[116:119], v[76:79]\n\t"
    "s_waitcnt lgkmcnt(4)\n\t"
    "v_mfma_f32_16x16x32_bf16 v[64:67], v[152:155], v[120:123], v[64:67]\n\t"
    "ds_read_b128 v[112:115], %[ea] offset:12288\n\t"
    "v_mfma_f32_16x16x32_bf16 v[68:71], v[156:159], v[120:123], v[68:71]\n\t"
    "ds_read_b128 v[116:119], %[ea] offset:14336\n\t"
    "v_mfma_f32_16x16x32_bf16 v[72:75], v[152:155], v[124:127], v[72:75]\n\t"
    "v_mfma_f32_16x16x32_bf16 v[76:79], v[156:159], v[124:127], v[76:79]\n\t"
    "s_waitcnt lgkmcnt(4)\n\t"
    "v_mfma_f32_16x16x32_bf16 v[64:67], v[160:163], v[96:99], v[64:67]\n\t"
    "ds_read_b128 v[120:123], %[da] offset:12288\n\t"
    "v_mfma_f32_16x16x32_bf16 v[68:71], v[164:167], v[96:99], v[68:71]\n\t"
    "ds_read_b128 v[124:127], %[da] offset:14336\n\t"
    "v_mfma_f32_16x16x32_bf16 v[72:75], v[160:163], v[100:103], v[72:75]\n\t"
    "v_mfma_f32_16x16x32_bf16 v[76:79], v[164:167], v[100:103], v[76:79]\n\t"
    "s_waitcnt lgkmcnt(4)\n\t"
    "v_mfma_f32_16x16x32_bf16 v[64:67], v[168:171], v[104:107], v[64:67]\n\t"
    "ds_read_b128 v[96:99], %[ea] offset:16384\n\t"
    "v_mfma_f32_16x16x32_bf16 v[68:71], v[172:175], v[104:107], v[68:71]\n\t"
    "ds_read_b128 v[100:103], %[ea] offset:18432\n\t"
    "v_mfma_f32_16x16x32_bf16 v[72:75], v[168:171], v[108:111], v[72:75]\n\t"
    "v_mfma_f32_16x16x32_bf16 v[76:79], v[172:175], v[108:111], v[76:79]\n\t"
    "s_waitcnt lgkmcnt(4)\n\t"
    "v_mfma_f32_16x16x32_bf16 v[64:67], v[176:179], v[112:115], v[64:67]\n\t"
    "ds_read_b128 v[104:107], %[da] offset:16384\n\t"
    "v_mfma_f32_16x16x32_bf16 v[68:71], v[180:183], v[112:115], v[68:71]\n\t"
    "ds_read_b128 v[108:111], %[da] offset:18432\n\t"
    "v_mfma_f32_16x16x32_bf16 v[72:75], v[176:179], v[116:119], v[72:75]\n\t"
    "v_mfma_f32_16x16x32_bf16 v[76:79], v[180:183], v[116:119], v[76:79]\n\t"
    "s_waitcnt lgkmcnt(4)\n\t"
    "v_mfma_f32_16x16x32_bf16 v[64:67], v[184:187], v[120:123], v[64:67]\n\t"
    "ds_read_b128 v[112:115], %[ea] offset:20480\n\t"
    "v_mfma_f32_16x16x32_bf16 v[68:71], v[188:191], v[120:123], v[68:71]\n\t"
    "ds_read_b128 v[116:119], %[ea] offset:22528\n\t"
    "v_mfma_f32_16x16x32_bf16 v[72:75], v[184:187], v[124:127], v[72:75]\n\t"
    "v_mfma_f32_16x16x32_bf16 v[76:79], v[188:191], v[124:127], v[76:79]\n\t"
    "s_waitcnt vmcnt(0)\n\t"
    "s_barrier\n\t"
    "s_waitcnt lgkmcnt(4)\n\t"
    "v_mfma_f32_16x16x32_bf16 v[64:67], v[192:195], v[96:99], v[64:67]\n\t"
    "ds_read_b128 v[120:123], %[da] offset:20480\n\t"
    "v_mfma_f32_16x16x32_bf16 v[68:71], v[196:199], v[96:99], v[68:71]\n\t"
    "ds_read_b128 v[124:127], %[da] offset:22528\n\t"
    "v_mfma_f32_16x16x32_bf16 v[72:75], v[192:195], v[100:103], v[72:75]\n\t"
    "ds_read_b128 v[48:51], %[rw] offset:0\n\t"
    "v_mfma_f32_16x16x32_bf16 v[76:79], v[196:199], v[100:103], v[76:79]\n\t"
    "ds_read_b128 v[52:55], %[rw] offset:1024\n\t"
    "s_cmp_eq_u32 %[w0], 0\n\t"
    "s_cbranch_scc1 5f\n\t"
    "6:\n\t"
    "s_waitcnt lgkmcnt(6)\n\t"
    "v_mfma_f32_16x16x32_bf16 v[64:67], v[200:203], v[104:107], v[64:67]\n\t"
    "ds_read_b128 v[96:99], %[ea] offset:24576\n\t"
    "v_mfma_f32_16x16x32_bf16 v[68:71], v[204:207], v[104:107], v[68:71]\n\t"
    "ds_read_b128 v[100:103], %[ea] offset:26624\n\t"
    "v_mfma_f32_16x16x32_bf16 v[72:75], v[200:203], v[108:111], v[72:75]\n\t"
    "ds_read_b128 v[56:59], %[rw] offset:2048\n\t"
    "v_mfma_f32_16x16x32_bf16 v[76:79], v[204:207], v[108:111], v[76:79]\n\t"
    "ds_read_b128 v[60:63], %[rw] offset:3072\n\t"
    "s_waitcnt lgkmcnt(0)\n\t"
    "v_mfma_f32_16x16x32_bf16 v[64:67], v[208:211], v[112:115], v[64:67]\n\t"
    "ds_read_b128 v[104:107], %[da] offset:24576\n\t"
    "v_cvt_pk_bf16_f32 v40, v80, v81\n\t"
    "v_mfma_f32_16x16x32_bf16 v[68:71], v[212:215], v[112:115], v[68:71]\n\t"
    "ds_read_b128 v[108:111], %[da] offset:26624\n\t"
    "v_cvt_pk_bf16_f32 v41, v82, v83\n\t"
    "v_mfma_f32_16x16x32_bf16 v[72:75], v[208:211], v[116:119], v[72:75]\n\t"
    "ds_read_b32 v39, %[sa]\n\t"
    "v_cvt_pk_bf16_f32 v42, v84, v85\n\t"
    "v_mfma_f32_16x16x32_bf16 v[76:79], v[212:215], v[116:119], v[76:79]\n\t"
    "v_cvt_pk_bf16_f32 v43, v86, v87\n\t"
    "v_mfma_f32_16x16x32_bf16 v[64:67], v[216:219], v[120:123], v[64:67]\n\t"
    "ds_read_b128 v[112:115], %[ea] offset:28672\n\t"
    "global_store_dwordx4 %[co0], v[40:43], s[36:37] sc1\n\t"
    "v_mfma_f32_16x16x32_bf16 v[68:71], v[220:223], v[120:123], v[68:71]\n\t"
    "ds_read_b128 v[116:119], %[ea] offset:30720\n\t"
    "v_cvt_pk_bf16_f32 v44, v88, v89\n\t"
    "v_mfma_f32_16x16x32_bf16 v[72:75], v[216:219], v[124:127], v[72:75]\n\t"
    "v_xor_b32 %[ea], 0x10000, %[ea]\n\t"
    "v_cvt_pk_bf16_f32 v45, v90, v91\n\t"
    "v_mfma_f32_16x16x32_bf16 v[76:79], v[220:223], v[124:127], v[76:79]\n\t"
    "v_cvt_pk_bf16_f32 v46, v92, v93\n\t"
    "v_mfma_f32_16x16x32_bf16 v[64:67], v[224:227], v[96:99], v[64:67]\n\t"
    "ds_read_b128 v[120:123], %[da] offset:28672\n\t"
    "v_cvt_pk_bf16_f32 v47, v94, v95\n\t"
    "v_mfma_f32_16x16x32_bf16 v[68:71], v[228:231], v[96:99], v[68:71]\n\t"
    "ds_read_b128 v[124:127], %[da] offset:30720\n\t"
    "global_store_dwordx4 %[co1], v[44:47], s[36:37] sc1\n\t"
    "v_mfma_f32_16x16x32_bf16 v[72:75], v[224:227], v[100:103], v[72:75]\n\t"
    "v_xor_b32 %[da], 0x10000, %[da]\n\t"
    "v_cvt_pk_bf16_f32 v48, v48, v49\n\t"
    "v_mfma_f32_16x16x32_bf16 v[76:79], v[228:231], v[100:103], v[76:79]\n\t"
    "v_cvt_pk_bf16_f32 v49, v50, v51\n\t"
    "s_waitcnt lgkmcnt(5)\n\t"
    "v_mfma_f32_16x16x32_bf16 v[64:67], v[232:235], v[104:107], v[64:67]\n\t"
    "ds_read_b128 v[96:99], %[ea] offset:0\n\t"
    "v_cvt_pk_bf16_f32 v50, v52, v53\n\t"
    "v_mfma_f32_16x16x32_bf16 v[68:71], v[236:239], v[104:107], v[68:71]\n\t"
    "ds_read_b128 v[100:103], %[ea] offset:2048\n\t"
    "v_cvt_pk_bf16_f32 v51, v54, v55\n\t"
    "v_mfma_f32_16x16x32_bf16 v[72:75], v[232:235], v[108:111], v[72:75]\n\t"
    "global_store_dwordx4 %[co0], v[48:51], s[36:37] offset:2048 sc1\n\t"
    "v_mfma_f32_16x16x32_bf16 v[76:79], v[236:239], v[108:111], v[76:79]\n\t"
    "v_cvt_pk_bf16_f32 v56, v56, v57\n\t"
    "s_waitcnt lgkmcnt(4)\n\t"
    "v_mfma_f32_16x16x32_bf16 v[64:67], v[240:243], v[112:115], v[64:67]\n\t"
    "ds_read_b128 v[104:107], %[da] offset:0\n\t"
    "v_mad_u32_u24 v39, v39, %[mu], %[ad]\n\t"
    "v_cvt_pk_bf16_f32 v57, v58, v59\n\t"
    "v_mfma_f32_16x16x32_bf16 v[68:71], v[244:247], v[112:115], v[68:71]\n\t"
    "ds_read_b128 v[108:111], %[da] offset:2048\n\t"
    "v_cmp_ge_i32 vcc, %[lf], v39\n\t"
    "s_cmp_eq_u64 vcc, 0\n\t"
    "s_addc_u32 %[lf], %[lf], 0\n\t"
    "v_cvt_pk_bf16_f32 v58, v60, v61\n\t"
    "v_mfma_f32_16x16x32_bf16 v[72:75], v[240:243], v[116:119], v[72:75]\n\t"
    "v_cvt_pk_bf16_f32 v59, v62, v63\n\t"
    "v_mfma_f32_16x16x32_bf16 v[76:79], v[244:247], v[116:119], v[76:79]\n\t"
    "v_cmp_ge_i32 vcc, %[lf], v39\n\t"
    "s_cmp_eq_u64 vcc, 0\n\t"
    "s_addc_u32 %[lf], %[lf], 0\n\t"
    "global_store_dwordx4 %[co1], v[56:59], s[36:37] offset:2048 sc1\n\t"
    "s_waitcnt lgkmcnt(4)\n\t"
    "v_mfma_f32_16x16x32_bf16 v[80:83], v[248:251], v[120:123], v[64:67]\n\t"
    "ds_read_b128 v[112:115], %[ea] offset:4096\n\t"
    "s_min_i32 %[lf], %[lf], %[cp]\n\t"
    "s_add_u32 %[b], %[b], 1\n\t"
    "v_mfma_f32_16x16x32_bf16 v[84:87], v[252:255], v[120:123], v[68:71]\n\t"
    "ds_read_b128 v[116:119], %[ea] offset:6144\n\t"
    "v_mfma_f32_16x16x32_bf16 v[88:91], v[248:251], v[124:127], v[72:75]\n\t"
    "v_mfma_f32_16x16x32_bf16 v[92:95], v[252:255], v[124:127], v[76:79]\n\t"
    "s_mov_b64 s[36:37], s[38:39]\n\t"
    "s_add_u32 s38, s38, %[ab]\n\t"
    "s_addc_u32 s39, s39, 0\n\t"
    "s_sub_u32 m0, %[lf], %[b]\n\t"
    "s_sub_u32 m0, m0, 1\n\t"
    "s_cmp_lt_u32 m0, %[up]\n\t"
    "s_cbranch_scc1 1b\n\t"
    "s_waitcnt lgkmcnt(0)\n\t"
    "s_nop 4\n\t"
    "s_branch 9f\n\t"
    "5:\n\t"
    "s_add_u32 %[st], %[b], %[po]\n\t"
    "s_max_i32 %[st], %[st], %[p0]\n\t"
    "v_mov_b32 v39, %[st]\n\t"
    "v_mov_b32 v38, 0\n\t"
    "s_mov_b64 exec, 1\n\t"
    "global_store_dword v38, v39, %[pg] sc1\n\t"
    "s_mov_b64 exec, -1\n\t"
    "s_mov_b32 %[p0], %[p1]\n\t"
    "s_branch 6b\n\t"
    "7:\n\t"
    "s_cmp_ge_i32 %[lf], %[cp]\n\t"
    "s_cbranch_scc1 8b\n\t"
    "s_mov_b32 m0, %[sl]\n\t"
    "s_nop 0\n\t"
    "global_load_lds_dword %[dv], %[db] sc1\n\t"
    "s_branch 8b\n\t"
    "9:"
    : "=&{v[80:95]}"(pd), [ea] "+&v"(ea), [da] "+&v"(da), [b] "+&s"(b), [lf] "+&s"(lf), "+{s[36:37]}"(p1), "+{s[38:39]}"(p0), [sa] "+&v"(sa), [st] "=&s"(st), [sl] "+&s"(sl), [p0] "+&s"(pf)
    : [rw] "v"(rw), [ab] "s"(ab), [cp] "s"(cp), [mu] "s"(mu), [ad] "v"(ad), [up] "s"(up), "{v[128:159]}"(W[0]), "{v[160:191]}"(W[1]), "{v[192:223]}"(W[2]), "{v[224:255]}"(W[3]), [co0] "v"(co0), [w0] "s"(w0), [dv] "v"(dv), [db] "s"(db), [pg] "s"(pg), [po] "s"(po - 1), [p1] "s"(b + po), [co1] "v"(co1)
    : "v38", "v39", "v40", "v41", "v42", "v43", "v44", "v45", "v46", "v47", "v48", "v49", "v50", "v51", "v52", "v53", "v54", "v55", "v56", "v57", "v58", "v59", "v60", "v61", "v62", "v63", "v64", "v65", "v66", "v67", "v68", "v69", "v70", "v71", "v72", "v73", "v74", "v75", "v76", "v77", "v78", "v79", "v96", "v97", "v98", "v99", "v100", "v101", "v102", "v103", "v104", "v105", "v106", "v107", "v108", "v109", "v110", "v111", "v112", "v113", "v114", "v115", "v116", "v117", "v118", "v119", "v120", "v121", "v122", "v123", "v124", "v125", "v126", "v127", "vcc", "scc", "m0", "memory");
}
__device__ __forceinline__ void lean_ld0(const u32x32 (&W)[4], unsigned ea, unsigned da, unsigned x, unsigned lb, const char* s0, const char* s1, const unsigned (&av)[8], unsigned ps, unsigned dv, const char* db, unsigned rw, unsigned fa, unsigned fv,
    unsigned ga, unsigned mu, unsigned ad, int th, int tb, unsigned& sv, unsigned& su, unsigned& got) {
  asm volatile("s_setprio 2\n\t"
    "v_mov_b32 v76, %[ga]\n\t"
    "ds_read_b32 v76, v76 offset:24\n\t"
    "ds_read_b128 v[96:99], %[ea] offset:0\n\t"
    "ds_read_b128 v[100:103], %[ea] offset:2048\n\t"
    "ds_read_b128 v[104:107], %[da] offset:0\n\t"
    "ds_read_b128 v[108:111], %[da] offset:2048\n\t"
    "ds_read_b128 v[112:115], %[ea] offset:4096\n\t"
    "ds_read_b128 v[116:119], %[ea] offset:6144\n\t"
    "s_waitcnt lgkmcnt(6)\n\t"
    "v_cmp_ne_u32 vcc, 0, v76\n\t"
    "s_mov_b32 %[got], -1\n\t"
    "s_cbranch_vccnz 12f\n\t"
    "s_waitcnt lgkmcnt(4)\n\t"
    "v_mfma_f32_16x16x32_bf16 v[80:83], v[128:131], v[96:99], 0\n\t"
    "ds_read_b128 v[120:123], %[da] offset:4096\n\t"
    "v_mfma_f32_16x16x32_bf16 v[84:87], v[132:135], v[96:99], 0\n\t"
    "ds_read_b128 v[124:127], %[da] offset:6144\n\t"
    "v_mfma_f32_16x16x32_bf16 v[88:91], v[128:131], v[100:103], 0\n\t"
    "v_mfma_f32_16x16x32_bf16 v[92:95], v[132:135], v[100:103], 0\n\t"
    "s_waitcnt vmcnt(24)\n\t"
    "s_cmp_lt_u32 %[x], 2\n\t"
    "s_cbranch_scc0 41f\n\t"
    "s_waitcnt vmcnt(8)\n\t"
    "41:\n\t"
    "s_barrier\n\t"
    "s_waitcnt lgkmcnt(4)\n\t"
    "v_mfma_f32_16x16x32_bf16 v[80:83], v[136:139], v[104:107], v[80:83]\n\t"
    "ds_read_b128 v[96:99], %[ea] offset:8192\n\t"
    "v_mfma_f32_16x16x32_bf16 v[84:87], v[140:143], v[104:107], v[84:87]\n\t"
    "ds_read_b128 v[100:103], %[ea] offset:10240\n\t"
    "v_mfma_f32_16x16x32_bf16 v[88:91], v[136:139], v[108:111], v[88:91]\n\t"
    "v_mfma_f32_16x16x32_bf16 v[92:95], v[140:143], v[108:111], v[92:95]\n\t"
    "s_waitcnt lgkmcnt(4)\n\t"
    "v_mfma_f32_16x16x32_bf16 v[80:83], v[144:147], v[112:115], v[80:83]\n\t"
    "ds_read_b128 v[104:107], %[da] offset:8192\n\t"
    "v_mfma_f32_16x16x32_bf16 v[84:87], v[148:151], v[112:115], v[84:87]\n\t"
    "ds_read_b128 v[108:111], %[da] offset:10240\n\t"
    "v_mfma_f32_16x16x32_bf16 v[88:91], v[144:147], v[116:119], v[88:91]\n\t"
    "v_mfma_f32_16x16x32_bf16 v[92:95], v[148:151], v[116:119], v[92:95]\n\t"
    "s_waitcnt lgkmcnt(4)\n\t"
    "v_mfma_f32_16x16x32_bf16 v[80:83], v[152:155], v[120:123], v[80:83]\n\t"
    "ds_read_b128 v[112:115], %[ea] offset:12288\n\t"
    "v_mfma_f32_16x16x32_bf16 v[84:87], v[156:159], v[120:123], v[84:87]\n\t"
    "ds_read_b128 v[116:119], %[ea] offset:14336\n\t"
    "v_mfma_f32_16x16x32_bf16 v[88:91], v[152:155], v[124:127], v[88:91]\n\t"
    "v_mfma_f32_16x16x32_bf16 v[92:95], v[156:159], v[124:127], v[92:95]\n\t"
    "s_waitcnt lgkmcnt(4)\n\t"
    "v_mfma_f32_16x16x32_bf16 v[80:83], v[160:163], v[96:99], v[80:83]\n\t"
    "ds_read_b128 v[120:123], %[da] offset:12288\n\t"
    "v_mfma_f32_16x16x32_bf16 v[84:87], v[164:167], v[96:99], v[84:87]\n\t"
    "ds_read_b128 v[124:127], %[da] offset:14336\n\t"
    "v_mfma_f32_16x16x32_bf16 v[88:91], v[160:163], v[100:103], v[88:91]\n\t"
    "v_mfma_f32_16x16x32_bf16 v[92:95], v[164:167], v[100:103], v[92:95]\n\t"
    "s_waitcnt vmcnt(20)\n\t"
    "s_cmp_lt_u32 %[x], 2\n\t"
    "s_cbranch_scc0 45f\n\t"
    "s_waitcnt vmcnt(4)\n\t"
    "45:\n\t"
    "s_barrier\n\t"
    "s_waitcnt lgkmcnt(4)\n\t"
    "v_mfma_f32_16x16x32_bf16 v[80:83], v[168:171], v[104:107], v[80:83]\n\t"
    "ds_read_b128 v[96:99], %[ea] offset:16384\n\t"
    "v_mfma_f32_16x16x32_bf16 v[84:87], v[172:175], v[104:107], v[84:87]\n\t"
    "ds_read_b128 v[100:103], %[ea] offset:18432\n\t"
    "v_mfma_f32_16x16x32_bf16 v[88:91], v[168:171], v[108:111], v[88:91]\n\t"
    "v_mfma_f32_16x16x32_bf16 v[92:95], v[172:175], v[108:111], v[92:95]\n\t"
    "s_waitcnt lgkmcnt(4)\n\t"
    "v_mfma_f32_16x16x32_bf16 v[80:83], v[176:179], v[112:115], v[80:83]\n\t"
    "ds_read_b128 v[104:107], %[da] offset:16384\n\t"
    "v_mfma_f32_16x16x32_bf16 v[84:87], v[180:183], v[112:115], v[84:87]\n\t"
    "ds_read_b128 v[108:111], %[da] offset:18432\n\t"
    "v_mfma_f32_16x16x32_bf16 v[88:91], v[176:179], v[116:119], v[88:91]\n\t"
    "v_mfma_f32_16x16x32_bf16 v[92:95], v[180:183], v[116:119], v[92:95]\n\t"
    "s_waitcnt lgkmcnt(4)\n\t"
    "v_mfma_f32_16x16x32_bf16 v[80:83], v[184:187], v[120:123], v[80:83]\n\t"
    "ds_read_b128 v[112:115], %[ea] offset:20480\n\t"
    "v_mfma_f32_16x16x32_bf16 v[84:87], v[188:191], v[120:123], v[84:87]\n\t"
    "ds_read_b128 v[116:119], %[ea] offset:22528\n\t"
    "v_mfma_f32_16x16x32_bf16 v[88:91], v[184:187], v[124:127], v[88:91]\n\t"
    "v_mfma_f32_16x16x32_bf16 v[92:95], v[188:191], v[124:127], v[92:95]\n\t"
    "s_waitcnt lgkmcnt(4)\n\t"
    "v_mfma_f32_16x16x32_bf16 v[80:83], v[192:195], v[96:99], v[80:83]\n\t"
    "ds_read_b128 v[120:123], %[da] offset:20480\n\t"
    "v_mfma_f32_16x16x32_bf16 v[84:87], v[196:199], v[96:99], v[84:87]\n\t"
    "ds_read_b128 v[124:127], %[da] offset:22528\n\t"
    "v_mfma_f32_16x16x32_bf16 v[88:91], v[192:195], v[100:103], v[88:91]\n\t"
    "v_mfma_f32_16x16x32_bf16 v[92:95], v[196:199], v[100:103], v[92:95]\n\t"
    "s_waitcnt vmcnt(16)\n\t"
    "s_cmp_lt_u32 %[x], 2\n\t"
    "s_cbranch_scc0 49f\n\t"
    "s_waitcnt vmcnt(0)\n\t"
    "49:\n\t"
    "s_barrier\n\t"
    "s_waitcnt lgkmcnt(4)\n\t"
    "v_mfma_f32_16x16x32_bf16 v[80:83], v[200:203], v[104:107], v[80:83]\n\t"
    "ds_read_b128 v[96:99], %[ea] offset:24576\n\t"
    "v_mfma_f32_16x16x32_bf16 v[84:87], v[204:207], v[104:107], v[84:87]\n\t"
    "ds_read_b128 v[100:103], %[ea] offset:26624\n\t"
    "v_mfma_f32_16x16x32_bf16 v[88:91], v[200:203], v[108:111], v[88:91]\n\t"
    "v_mfma_f32_16x16x32_bf16 v[92:95], v[204:207], v[108:111], v[92:95]\n\t"
    "s_waitcnt lgkmcnt(4)\n\t"
    "v_mfma_f32_16x16x32_bf16 v[80:83], v[208:211], v[112:115], v[80:83]\n\t"
    "ds_read_b128 v[104:107], %[da] offset:24576\n\t"
    "v_mfma_f32_16x16x32_bf16 v[84:87], v[212:215], v[112:115], v[84:87]\n\t"
    "ds_read_b128 v[108:111], %[da] offset:26624\n\t"
    "v_mfma_f32_16x16x32_bf16 v[88:91], v[208:211], v[116:119], v[88:91]\n\t"
    "v_mfma_f32_16x16x32_bf16 v[92:95], v[212:215], v[116:119], v[92:95]\n\t"
    "s_cmp_eq_u32 %[ps], 0\n\t"
    "s_cbranch_scc1 10f\n\t"
    "global_load_dword %[sv], %[dv], %[db] sc1\n\t"
    "global_load_dword %[su], %[dv], %[db] offset:64 sc1\n\t"
    "10:\n\t"
    "s_waitcnt lgkmcnt(4)\n\t"
    "v_mfma_f32_16x16x32_bf16 v[80:83], v[216:219], v[120:123], v[80:83]\n\t"
    "ds_read_b128 v[112:115], %[ea] offset:28672\n\t"
    "v_mfma_f32_16x16x32_bf16 v[84:87], v[220:223], v[120:123], v[84:87]\n\t"
    "ds_read_b128 v[116:119], %[ea] offset:30720\n\t"
    "v_mfma_f32_16x16x32_bf16 v[88:91], v[216:219], v[124:127], v[88:91]\n\t"
    "v_mfma_f32_16x16x32_bf16 v[92:95], v[220:223], v[124:127], v[92:95]\n\t"
    "s_waitcnt lgkmcnt(4)\n\t"
    "v_mfma_f32_16x16x32_bf16 v[80:83], v[224:227], v[96:99], v[80:83]\n\t"
    "ds_read_b128 v[120:123], %[da] offset:28672\n\t"
    "v_mfma_f32_16x16x32_bf16 v[84:87], v[228:231], v[96:99], v[84:87]\n\t"
    "ds_read_b128 v[124:127], %[da] offset:30720\n\t"
    "v_mfma_f32_16x16x32_bf16 v[88:91], v[224:227], v[100:103], v[88:91]\n\t"
    "v_mfma_f32_16x16x32_bf16 v[92:95], v[228:231], v[100:103], v[92:95]\n\t"
    "s_waitcnt lgkmcnt(4)\n\t"
    "v_mfma_f32_16x16x32_bf16 v[80:83], v[232:235], v[104:107], v[80:83]\n\t"
    "v_mfma_f32_16x16x32_bf16 v[84:87], v[236:239], v[104:107], v[84:87]\n\t"
    "v_mfma_f32_16x16x32_bf16 v[88:91], v[232:235], v[108:111], v[88:91]\n\t"
    "v_mfma_f32_16x16x32_bf16 v[92:95], v[236:239], v[108:111], v[92:95]\n\t"
    "s_waitcnt lgkmcnt(2)\n\t"
    "v_mfma_f32_16x16x32_bf16 v[80:83], v[240:243], v[112:115], v[80:83]\n\t"
    "v_mfma_f32_16x16x32_bf16 v[84:87], v[244:247], v[112:115], v[84:87]\n\t"
    "v_mfma_f32_16x16x32_bf16 v[88:91], v[240:243], v[116:119], v[88:91]\n\t"
    "v_mfma_f32_16x16x32_bf16 v[92:95], v[244:247], v[116:119], v[92:95]\n\t"
    "s_waitcnt lgkmcnt(0)\n\t"
    "v_mfma_f32_16x16x32_bf16 v[80:83], v[248:251], v[120:123], v[80:83]\n\t"
    "s_memrealtime s[36:37]\n\t"
    "v_mfma_f32_16x16x32_bf16 v[84:87], v[252:255], v[120:123], v[84:87]\n\t"
    "v_mfma_f32_16x16x32_bf16 v[88:91], v[248:251], v[124:127], v[88:91]\n\t"
    "v_mfma_f32_16x16x32_bf16 v[92:95], v[252:255], v[124:127], v[92:95]\n\t"
    "s_mov_b32 %[got], 0\n\t"
    "v_mov_b32 v76, %[fa]\n\t"
    "v_mov_b32 v77, %[fv]\n\t"
    "v_add_u32 v78, 1, v77\n\t"
    "v_mov_b32 v79, %[ga]\n\t"
    "s_waitcnt lgkmcnt(0)\n\t"
    "ds_write_b128 %[rw], v[80:83] offset:0\n\t"
    "ds_write_b128 %[rw], v[84:87] offset:1024\n\t"
    "ds_write_b128 %[rw], v[88:91] offset:2048\n\t"
    "ds_write_b128 %[rw], v[92:95] offset:3072\n\t"
    "ds_write_b32 v76, v77\n\t"
    "s_cmp_eq_u32 %[ps], 0\n\t"
    "s_cbranch_scc1 9f\n\t"
    "s_waitcnt vmcnt(0)\n\t"
    "v_mad_u32_u24 v76, %[sv], %[mu], %[ad]\n\t"
    "v_cmp_ge_i32 vcc, %[th], v76\n\t"
    "s_cbranch_vccz 11f\n\t"
    "s_sub_u32 s36, s36, 20\n\t"
    "v_sub_u32 v76, s36, %[su]\n\t"
    "v_lshrrev_b32 v77, 24, %[su]\n\t"
    "v_lshlrev_b32 v76, 8, v76\n\t"
    "v_ashrrev_i32 v76, 31, v76\n\t"
    "v_add_u32 v77, v77, v76\n\t"
    "v_cmp_lt_i32 s[36:37], %[tb], v77\n\t"
    "s_andn2_b64 vcc, vcc, s[36:37]\n\t"
    "s_cbranch_scc1 9f\n\t"
    "11:\n\t"
    "ds_write2_b32 v79, v78, v78 offset1:1\n\t"
    "s_add_u32 m0, %[lb], 0\n\t"
    "s_nop 0\n\t"
    "global_load_lds_dwordx4 %[av0], %[s0] sc1\n\t"
    "s_add_u32 m0, %[lb], 32768\n\t"
    "s_nop 0\n\t"
    "global_load_lds_dwordx4 %[av1], %[s0] sc1\n\t"
    "s_add_u32 m0, %[lb], 4096\n\t"
    "s_nop 0\n\t"
    "global_load_lds_dwordx4 %[av2], %[s0] sc1\n\t"
    "s_add_u32 m0, %[lb], 36864\n\t"
    "s_nop 0\n\t"
    "global_load_lds_dwordx4 %[av3], %[s0] sc1\n\t"
    "s_add_u32 m0, %[lb], 8192\n\t"
    "s_nop 0\n\t"
    "global_load_lds_dwordx4 %[av4], %[s0] sc1\n\t"
    "s_add_u32 m0, %[lb], 40960\n\t"
    "s_nop 0\n\t"
    "global_load_lds_dwordx4 %[av5], %[s0] sc1\n\t"
    "s_add_u32 m0, %[lb], 12288\n\t"
    "s_nop 0\n\t"
    "global_load_lds_dwordx4 %[av6], %[s0] sc1\n\t"
    "s_add_u32 m0, %[lb], 45056\n\t"
    "s_nop 0\n\t"
    "global_load_lds_dwordx4 %[av7], %[s0] sc1\n\t"
    "s_add_u32 m0, %[lb], 16384\n\t"
    "s_nop 0\n\t"
    "global_load_lds_dwordx4 %[av0], %[s1] sc1\n\t"
    "s_add_u32 m0, %[lb], 49152\n\t"
    "s_nop 0\n\t"
    "global_load_lds_dwordx4 %[av1], %[s1] sc1\n\t"
    "s_add_u32 m0, %[lb], 20480\n\t"
    "s_nop 0\n\t"
    "global_load_lds_dwordx4 %[av2], %[s1] sc1\n\t"
    "s_add_u32 m0, %[lb], 53248\n\t"
    "s_nop 0\n\t"
    "global_load_lds_dwordx4 %[av3], %[s1] sc1\n\t"
    "s_add_u32 m0, %[lb], 24576\n\t"
    "s_nop 0\n\t"
    "global_load_lds_dwordx4 %[av4], %[s1] sc1\n\t"
    "s_add_u32 m0, %[lb], 57344\n\t"
    "s_nop 0\n\t"
    "global_load_lds_dwordx4 %[av5], %[s1] sc1\n\t"
    "s_add_u32 m0, %[lb], 28672\n\t"
    "s_nop 0\n\t"
    "global_load_lds_dwordx4 %[av6], %[s1] sc1\n\t"
    "s_add_u32 m0, %[lb], 61440\n\t"
    "s_nop 0\n\t"
    "global_load_lds_dwordx4 %[av7], %[s1] sc1\n\t"
    "s_mov_b32 %[got], 1\n\t"
    "s_branch 9f\n\t"
    "12:\n\t"
    "s_waitcnt lgkmcnt(0)\n\t"
    "9:"
    : [sv] "+&v"(sv), [su] "+&v"(su), [got] "=&s"(got)
    : [ea] "v"(ea), [da] "v"(da), [x] "s"(x), "{v[128:159]}"(W[0]), "{v[160:191]}"(W[1]), "{v[192:223]}"(W[2]), "{v[224:255]}"(W[3]), [lb] "s"(lb), [s0] "s"(s0), [s1] "s"(s1), [av0] "v"(av[0]), [av1] "v"(av[1]), [av2] "v"(av[2]), [av3] "v"(av[3]), [av4] "v"(av[4]), [av5] "v"(av[5]), [av6] "v"(av[6]), [av7] "v"(av[7]), [ps] "s"(ps), [dv] "v"(dv), [db] "s"(db), [rw] "v"(rw), [fa] "s"(fa), [fv] "s"(fv), [ga] "s"(ga), [mu] "s"(mu), [ad] "v"(ad), [th] "s"(th), [tb] "s"(tb)
    : "v96", "v97", "v98", "v99", "v100", "v101", "v102", "v103", "v104", "v105", "v106", "v107", "v108", "v109", "v110", "v111", "v112", "v113", "v114", "v115", "v116", "v117", "v118", "v119", "v120", "v121", "v122", "v123", "v124", "v125", "v126", "v127", "v76", "v77", "v78", "v79", "v80", "v81", "v82", "v83", "v84", "v85", "v86", "v87", "v88", "v89", "v90", "v91", "v92", "v93", "v94", "v95", "vcc", "scc", "m0", "memory", "s36", "s37", "s38", "s39");
}
__device__ __forceinline__ void lean_dm0(const u32x32 (&W)[4], unsigned ea, unsigned da, unsigned x, unsigned lb, const char* s0, const char* s1, const unsigned (&av)[8], unsigned ps, unsigned dv, const char* db, unsigned rw, unsigned fa, unsigned fv,
    unsigned ga, unsigned mu, unsigned ad, int th, int tb, unsigned& sv, unsigned& su, unsigned& got) {
  asm volatile("s_setprio 2\n\t"
    "ds_read_b128 v[96:99], %[ea] offset:0\n\t"
    "ds_read_b128 v[100:103], %[ea] offset:2048\n\t"
    "ds_read_b128 v[104:107], %[da] offset:0\n\t"
    "ds_read_b128 v[108:111], %[da] offset:2048\n\t"
    "ds_read_b128 v[112:115], %[ea] offset:4096\n\t"
    "ds_read_b128 v[116:119], %[ea] offset:6144\n\t"
    "s_waitcnt lgkmcnt(4)\n\t"
    "v_mfma_f32_16x16x32_bf16 v[80:83], v[128:131], v[96:99], 0\n\t"
    "ds_read_b128 v[120:123], %[da] offset:4096\n\t"
    "s_add_u32 m0, %[lb], 0\n\t"
    "v_mfma_f32_16x16x32_bf16 v[84:87], v[132:135], v[96:99], 0\n\t"
    "ds_read_b128 v[124:127], %[da] offset:6144\n\t"
    "global_load_lds_dwordx4 %[av0], %[s0] sc1\n\t"
    "v_mfma_f32_16x16x32_bf16 v[88:91], v[128:131], v[100:103], 0\n\t"
    "s_add_u32 m0, %[lb], 32768\n\t"
    "v_mfma_f32_16x16x32_bf16 v[92:95], v[132:135], v[100:103], 0\n\t"
    "global_load_lds_dwordx4 %[av1], %[s0] sc1\n\t"
    "s_waitcnt vmcnt(10)\n\t"
    "s_barrier\n\t"
    "s_waitcnt lgkmcnt(4)\n\t"
    "v_mfma_f32_16x16x32_bf16 v[80:83], v[136:139], v[104:107], v[80:83]\n\t"
    "ds_read_b128 v[96:99], %[ea] offset:8192\n\t"
    "s_add_u32 m0, %[lb], 4096\n\t"
    "v_mfma_f32_16x16x32_bf16 v[84:87], v[140:143], v[104:107], v[84:87]\n\t"
    "ds_read_b128 v[100:103], %[ea] offset:10240\n\t"
    "global_load_lds_dwordx4 %[av2], %[s0] sc1\n\t"
    "v_mfma_f32_16x16x32_bf16 v[88:91], v[136:139], v[108:111], v[88:91]\n\t"
    "s_add_u32 m0, %[lb], 36864\n\t"
    "v_mfma_f32_16x16x32_bf16 v[92:95], v[140:143], v[108:111], v[92:95]\n\t"
    "global_load_lds_dwordx4 %[av3], %[s0] sc1\n\t"
    "s_waitcnt lgkmcnt(4)\n\t"
    "v_mfma_f32_16x16x32_bf16 v[80:83], v[144:147], v[112:115], v[80:83]\n\t"
    "ds_read_b128 v[104:107], %[da] offset:8192\n\t"
    "s_add_u32 m0, %[lb], 8192\n\t"
    "v_mfma_f32_16x16x32_bf16 v[84:87], v[148:151], v[112:115], v[84:87]\n\t"
    "ds_read_b128 v[108:111], %[da] offset:10240\n\t"
    "global_load_lds_dwordx4 %[av4], %[s0] sc1\n\t"
    "v_mfma_f32_16x16x32_bf16 v[88:91], v[144:147], v[116:119], v[88:91]\n\t"
    "s_add_u32 m0, %[lb], 40960\n\t"
    "v_mfma_f32_16x16x32_bf16 v[92:95], v[148:151], v[116:119], v[92:95]\n\t"
    "global_load_lds_dwordx4 %[av5], %[s0] sc1\n\t"
    "s_waitcnt lgkmcnt(4)\n\t"
    "v_mfma_f32_16x16x32_bf16 v[80:83], v[152:155], v[120:123], v[80:83]\n\t"
    "ds_read_b128 v[112:115], %[ea] offset:12288\n\t"
    "s_add_u32 m0, %[lb], 12288\n\t"
    "v_mfma_f32_16x16x32_bf16 v[84:87], v[156:159], v[120:123], v[84:87]\n\t"
    "ds_read_b128 v[116:119], %[ea] offset:14336\n\t"
    "global_load_lds_dwordx4 %[av6], %[s0] sc1\n\t"
    "v_mfma_f32_16x16x32_bf16 v[88:91], v[152:155], v[124:127], v[88:91]\n\t"
    "s_add_u32 m0, %[lb], 45056\n\t"
    "v_mfma_f32_16x16x32_bf16 v[92:95], v[156:159], v[124:127], v[92:95]\n\t"
    "global_load_lds_dwordx4 %[av7], %[s0] sc1\n\t"
    "s_waitcnt lgkmcnt(4)\n\t"
    "v_mfma_f32_16x16x32_bf16 v[80:83], v[160:163], v[96:99], v[80:83]\n\t"
    "ds_read_b128 v[120:123], %[da] offset:12288\n\t"
    "s_add_u32 m0, %[lb], 16384\n\t"
    "v_mfma_f32_16x16x32_bf16 v[84:87], v[164:167], v[96:99], v[84:87]\n\t"
    "ds_read_b128 v[124:127], %[da] offset:14336\n\t"
    "global_load_lds_dwordx4 %[av0], %[s1] sc1\n\t"
    "v_mfma_f32_16x16x32_bf16 v[88:91], v[160:163], v[100:103], v[88:91]\n\t"
    "s_add_u32 m0, %[lb], 49152\n\t"
    "v_mfma_f32_16x16x32_bf16 v[92:95], v[164:167], v[100:103], v[92:95]\n\t"
    "global_load_lds_dwordx4 %[av1], %[s1] sc1\n\t"
    "s_waitcnt vmcnt(14)\n\t"
    "s_barrier\n\t"
    "s_waitcnt lgkmcnt(4)\n\t"
    "v_mfma_f32_16x16x32_bf16 v[80:83], v[168:171], v[104:107], v[80:83]\n\t"
    "ds_read_b128 v[96:99], %[ea] offset:16384\n\t"
    "s_add_u32 m0, %[lb], 20480\n\t"
    "v_mfma_f32_16x16x32_bf16 v[84:87], v[172:175], v[104:107], v[84:87]\n\t"
    "ds_read_b128 v[100:103], %[ea] offset:18432\n\t"
    "global_load_lds_dwordx4 %[av2], %[s1] sc1\n\t"
    "v_mfma_f32_16x16x32_bf16 v[88:91], v[168:171], v[108:111], v[88:91]\n\t"
    "s_add_u32 m0, %[lb], 53248\n\t"
    "v_mfma_f32_16x16x32_bf16 v[92:95], v[172:175], v[108:111], v[92:95]\n\t"
    "global_load_lds_dwordx4 %[av3], %[s1] sc1\n\t"
    "s_waitcnt lgkmcnt(4)\n\t"
    "v_mfma_f32_16x16x32_bf16 v[80:83], v[176:179], v[112:115], v[80:83]\n\t"
    "ds_read_b128 v[104:107], %[da] offset:16384\n\t"
    "s_add_u32 m0, %[lb], 24576\n\t"
    "v_mfma_f32_16x16x32_bf16 v[84:87], v[180:183], v[112:115], v[84:87]\n\t"
    "ds_read_b128 v[108:111], %[da] offset:18432\n\t"
    "global_load_lds_dwordx4 %[av4], %[s1] sc1\n\t"
    "v_mfma_f32_16x16x32_bf16 v[88:91], v[176:179], v[116:119], v[88:91]\n\t"
    "s_add_u32 m0, %[lb], 57344\n\t"
    "v_mfma_f32_16x16x32_bf16 v[92:95], v[180:183], v[116:119], v[92:95]\n\t"
    "global_load_lds_dwordx4 %[av5], %[s1] sc1\n\t"
    "s_waitcnt lgkmcnt(4)\n\t"
    "v_mfma_f32_16x16x32_bf16 v[80:83], v[184:187], v[120:123], v[80:83]\n\t"
    "ds_read_b128 v[112:115], %[ea] offset:20480\n\t"
    "s_add_u32 m0, %[lb], 28672\n\t"
    "v_mfma_f32_16x16x32_bf16 v[84:87], v[188:191], v[120:123], v[84:87]\n\t"
    "ds_read_b128 v[116:119], %[ea] offset:22528\n\t"
    "global_load_lds_dwordx4 %[av6], %[s1] sc1\n\t"
    "v_mfma_f32_16x16x32_bf16 v[88:91], v[184:187], v[124:127], v[88:91]\n\t"
    "s_add_u32 m0, %[lb], 61440\n\t"
    "v_mfma_f32_16x16x32_bf16 v[92:95], v[188:191], v[124:127], v[92:95]\n\t"
    "global_load_lds_dwordx4 %[av7], %[s1] sc1\n\t"
    "s_waitcnt lgkmcnt(4)\n\t"
    "v_mfma_f32_16x16x32_bf16 v[80:83], v[192:195], v[96:99], v[80:83]\n\t"
    "ds_read_b128 v[120:123], %[da] offset:20480\n\t"
    "v_mfma_f32_16x16x32_bf16 v[84:87], v[196:199], v[96:99], v[84:87]\n\t"
    "ds_read_b128 v[124:127], %[da] offset:22528\n\t"
    "v_mfma_f32_16x16x32_bf16 v[88:91], v[192:195], v[100:103], v[88:91]\n\t"
    "v_mfma_f32_16x16x32_bf16 v[92:95], v[196:199], v[100:103], v[92:95]\n\t"
    "s_waitcnt vmcnt(16)\n\t"
    "s_barrier\n\t"
    "s_waitcnt lgkmcnt(4)\n\t"
    "v_mfma_f32_16x16x32_bf16 v[80:83], v[200:203], v[104:107], v[80:83]\n\t"
    "ds_read_b128 v[96:99], %[ea] offset:24576\n\t"
    "v_mfma_f32_16x16x32_bf16 v[84:87], v[204:207], v[104:107], v[84:87]\n\t"
    "ds_read_b128 v[100:103], %[ea] offset:26624\n\t"
    "v_mfma_f32_16x16x32_bf16 v[88:91], v[200:203], v[108:111], v[88:91]\n\t"
    "v_mfma_f32_16x16x32_bf16 v[92:95], v[204:207], v[108:111], v[92:95]\n\t"
    "s_waitcnt lgkmcnt(4)\n\t"
    "v_mfma_f32_16x16x32_bf16 v[80:83], v[208:211], v[112:115], v[80:83]\n\t"
    "ds_read_b128 v[104:107], %[da] offset:24576\n\t"
    "v_mfma_f32_16x16x32_bf16 v[84:87], v[212:215], v[112:115], v[84:87]\n\t"
    "ds_read_b128 v[108:111], %[da] offset:26624\n\t"
    "v_mfma_f32_16x16x32_bf16 v[88:91], v[208:211], v[116:119], v[88:91]\n\t"
    "v_mfma_f32_16x16x32_bf16 v[92:95], v[212:215], v[116:119], v[92:95]\n\t"
    "s_cmp_eq_u32 %[ps], 0\n\t"
    "s_cbranch_scc1 10f\n\t"
    "global_load_dword %[sv], %[dv], %[db] sc1\n\t"
    "global_load_dword %[su], %[dv], %[db] offset:64 sc1\n\t"
    "10:\n\t"
    "s_waitcnt lgkmcnt(4)\n\t"
    "v_mfma_f32_16x16x32_bf16 v[80:83], v[216:219], v[120:123], v[80:83]\n\t"
    "ds_read_b128 v[112:115], %[ea] offset:28672\n\t"
    "v_mfma_f32_16x16x32_bf16 v[84:87], v[220:223], v[120:123], v[84:87]\n\t"
    "ds_read_b128 v[116:119], %[ea] offset:30720\n\t"
    "v_mfma_f32_16x16x32_bf16 v[88:91], v[216:219], v[124:127], v[88:91]\n\t"
    "v_mfma_f32_16x16x32_bf16 v[92:95], v[220:223], v[124:127], v[92:95]\n\t"
    "s_waitcnt lgkmcnt(4)\n\t"
    "v_mfma_f32_16x16x32_bf16 v[80:83], v[224:227], v[96:99], v[80:83]\n\t"
    "ds_read_b128 v[120:123], %[da] offset:28672\n\t"
    "v_mfma_f32_16x16x32_bf16 v[84:87], v[228:231], v[96:99], v[84:87]\n\t"
    "ds_read_b128 v[124:127], %[da] offset:30720\n\t"
    "v_mfma_f32_16x16x32_bf16 v[88:91], v[224:227], v[100:103], v[88:91]\n\t"
    "v_mfma_f32_16x16x32_bf16 v[92:95], v[228:231], v[100:103], v[92:95]\n\t"
    "s_waitcnt lgkmcnt(4)\n\t"
    "v_mfma_f32_16x16x32_bf16 v[80:83], v[232:235], v[104:107], v[80:83]\n\t"
    "v_mfma_f32_16x16x32_bf16 v[84:87], v[236:239], v[104:107], v[84:87]\n\t"
    "v_mfma_f32_16x16x32_bf16 v[88:91], v[232:235], v[108:111], v[88:91]\n\t"
    "v_mfma_f32_16x16x32_bf16 v[92:95], v[236:239], v[108:111], v[92:95]\n\t"
    "s_waitcnt lgkmcnt(2)\n\t"
    "v_mfma_f32_16x16x32_bf16 v[80:83], v[240:243], v[112:115], v[80:83]\n\t"
    "v_mfma_f32_16x16x32_bf16 v[84:87], v[244:247], v[112:115], v[84:87]\n\t"
    "v_mfma_f32_16x16x32_bf16 v[88:91], v[240:243], v[116:119], v[88:91]\n\t"
    "v_mfma_f32_16x16x32_bf16 v[92:95], v[244:247], v[116:119], v[92:95]\n\t"
    "s_waitcnt lgkmcnt(0)\n\t"
    "v_mfma_f32_16x16x32_bf16 v[80:83], v[248:251], v[120:123], v[80:83]\n\t"
    "v_mfma_f32_16x16x32_bf16 v[84:87], v[252:255], v[120:123], v[84:87]\n\t"
    "v_mfma_f32_16x16x32_bf16 v[88:91], v[248:251], v[124:127], v[88:91]\n\t"
    "v_mfma_f32_16x16x32_bf16 v[92:95], v[252:255], v[124:127], v[92:95]\n\t"
    "s_mov_b32 %[got], 0\n\t"
    "v_mov_b32 v76, %[fa]\n\t"
    "v_mov_b32 v77, %[fv]\n\t"
    "v_add_u32 v78, 1, v77\n\t"
    "v_mov_b32 v79, %[ga]\n\t"
    "ds_write_b128 %[rw], v[80:83] offset:0\n\t"
    "ds_write_b128 %[rw], v[84:87] offset:1024\n\t"
    "ds_write_b128 %[rw], v[88:91] offset:2048\n\t"
    "ds_write_b128 %[rw], v[92:95] offset:3072\n\t"
    "ds_write_b32 v76, v77\n\t"
    "s_cmp_eq_u32 %[ps], 0\n\t"
    "s_cbranch_scc1 9f\n\t"
    "s_waitcnt vmcnt(0)\n\t"
    "9:"
    : [sv] "+&v"(sv), [su] "+&v"(su), [got] "=&s"(got)
    : [ea] "v"(ea), [da] "v"(da), [x] "s"(x), "{v[128:159]}"(W[0]), "{v[160:191]}"(W[1]), "{v[192:223]}"(W[2]), "{v[224:255]}"(W[3]), [lb] "s"(lb), [s0] "s"(s0), [s1] "s"(s1), [av0] "v"(av[0]), [av1] "v"(av[1]), [av2] "v"(av[2]), [av3] "v"(av[3]), [av4] "v"(av[4]), [av5] "v"(av[5]), [av6] "v"(av[6]), [av7] "v"(av[7]), [ps] "s"(ps), [dv] "v"(dv), [db] "s"(db), [rw] "v"(rw), [fa] "s"(fa), [fv] "s"(fv), [ga] "s"(ga), [mu] "s"(mu), [ad] "v"(ad), [th] "s"(th), [tb] "s"(tb)
    : "v96", "v97", "v98", "v99", "v100", "v101", "v102", "v103", "v104", "v105", "v106", "v107", "v108", "v109", "v110", "v111", "v112", "v113", "v114", "v115", "v116", "v117", "v118", "v119", "v120", "v121", "v122", "v123", "v124", "v125", "v126", "v127", "v76", "v77", "v78", "v79", "v80", "v81", "v82", "v83", "v84", "v85", "v86", "v87", "v88", "v89", "v90", "v91", "v92", "v93", "v94", "v95", "vcc", "scc", "m0", "memory");
}
__device__ __forceinline__ void lean_ld1(const u32x32 (&W)[4], unsigned ea, unsigned da, unsigned x, unsigned lb, const char* s0, const char* s1, const unsigned (&av)[8], unsigned ps, unsigned dv, const char* db, unsigned rw, unsigned fa, unsigned fv,
    unsigned ga, unsigned mu, unsigned ad, int th, int tb, unsigned& sv, unsigned& su, unsigned& got) {
  asm volatile("s_setprio 2\n\t"
    "v_mov_b32 v76, %[ga]\n\t"
    "ds_read_b32 v76, v76 offset:24\n\t"
    "ds_read_b128 v[96:99], %[ea] offset:0\n\t"
    "ds_read_b128 v[100:103], %[ea] offset:2048\n\t"
    "ds_read_b128 v[104:107], %[da] offset:0\n\t"
    "ds_read_b128 v[108:111], %[da] offset:2048\n\t"
    "ds_read_b128 v[112:115], %[ea] offset:4096\n\t"
    "ds_read_b128 v[116:119], %[ea] offset:6144\n\t"
    "s_waitcnt lgkmcnt(6)\n\t"
    "v_cmp_ne_u32 vcc, 0, v76\n\t"
    "s_mov_b32 %[got], -1\n\t"
    "s_cbranch_vccnz 12f\n\t"
    "s_waitcnt lgkmcnt(4)\n\t"
    "v_mfma_f32_16x16x32_bf16 v[80:83], v[128:131], v[96:99], 0\n\t"
    "ds_read_b128 v[120:123], %[da] offset:4096\n\t"
    "v_mfma_f32_16x16x32_bf16 v[84:87], v[132:135], v[96:99], 0\n\t"
    "ds_read_b128 v[124:127], %[da] offset:6144\n\t"
    "v_mfma_f32_16x16x32_bf16 v[88:91], v[128:131], v[100:103], 0\n\t"
    "v_mfma_f32_16x16x32_bf16 v[92:95], v[132:135], v[100:103], 0\n\t"
    "s_waitcnt vmcnt(12)\n\t"
    "s_cmp_lt_u32 %[x], 2\n\t"
    "s_cbranch_scc0 41f\n\t"
    "s_waitcnt vmcnt(4)\n\t"
    "41:\n\t"
    "s_barrier\n\t"
    "s_waitcnt lgkmcnt(4)\n\t"
    "v_mfma_f32_16x16x32_bf16 v[80:83], v[136:139], v[104:107], v[80:83]\n\t"
    "ds_read_b128 v[96:99], %[ea] offset:8192\n\t"
    "v_mfma_f32_16x16x32_bf16 v[84:87], v[140:143], v[104:107], v[84:87]\n\t"
    "ds_read_b128 v[100:103], %[ea] offset:10240\n\t"
    "v_mfma_f32_16x16x32_bf16 v[88:91], v[136:139], v[108:111], v[88:91]\n\t"
    "v_mfma_f32_16x16x32_bf16 v[92:95], v[140:143], v[108:111], v[92:95]\n\t"
    "s_waitcnt lgkmcnt(4)\n\t"
    "v_mfma_f32_16x16x32_bf16 v[80:83], v[144:147], v[112:115], v[80:83]\n\t"
    "ds_read_b128 v[104:107], %[da] offset:8192\n\t"
    "v_mfma_f32_16x16x32_bf16 v[84:87], v[148:151], v[112:115], v[84:87]\n\t"
    "ds_read_b128 v[108:111], %[da] offset:10240\n\t"
    "v_mfma_f32_16x16x32_bf16 v[88:91], v[144:147], v[116:119], v[88:91]\n\t"
    "v_mfma_f32_16x16x32_bf16 v[92:95], v[148:151], v[116:119], v[92:95]\n\t"
    "s_waitcnt lgkmcnt(4)\n\t"
    "v_mfma_f32_16x16x32_bf16 v[80:83], v[152:155], v[120:123], v[80:83]\n\t"
    "ds_read_b128 v[112:115], %[ea] offset:12288\n\t"
    "v_mfma_f32_16x16x32_bf16 v[84:87], v[156:159], v[120:123], v[84:87]\n\t"
    "ds_read_b128 v[116:119], %[ea] offset:14336\n\t"
    "v_mfma_f32_16x16x32_bf16 v[88:91], v[152:155], v[124:127], v[88:91]\n\t"
    "v_mfma_f32_16x16x32_bf16 v[92:95], v[156:159], v[124:127], v[92:95]\n\t"
    "s_waitcnt lgkmcnt(4)\n\t"
    "v_mfma_f32_16x16x32_bf16 v[80:83], v[160:163], v[96:99], v[80:83]\n\t"
    "ds_read_b128 v[120:123], %[da] offset:12288\n\t"
    "v_mfma_f32_16x16x32_bf16 v[84:87], v[164:167], v[96:99], v[84:87]\n\t"
    "ds_read_b128 v[124:127], %[da] offset:14336\n\t"
    "v_mfma_f32_16x16x32_bf16 v[88:91], v[160:163], v[100:103], v[88:91]\n\t"
    "v_mfma_f32_16x16x32_bf16 v[92:95], v[164:167], v[100:103], v[92:95]\n\t"
    "s_waitcnt vmcnt(10)\n\t"
    "s_cmp_lt_u32 %[x], 2\n\t"
    "s_cbranch_scc0 45f\n\t"
    "s_waitcnt vmcnt(2)\n\t"
    "45:\n\t"
    "s_barrier\n\t"
    "s_waitcnt lgkmcnt(4)\n\t"
    "v_mfma_f32_16x16x32_bf16 v[80:83], v[168:171], v[104:107], v[80:83]\n\t"
    "ds_read_b128 v[96:99], %[ea] offset:16384\n\t"
    "v_mfma_f32_16x16x32_bf16 v[84:87], v[172:175], v[104:107], v[84:87]\n\t"
    "ds_read_b128 v[100:103], %[ea] offset:18432\n\t"
    "v_mfma_f32_16x16x32_bf16 v[88:91], v[168:171], v[108:111], v[88:91]\n\t"
    "v_mfma_f32_16x16x32_bf16 v[92:95], v[172:175], v[108:111], v[92:95]\n\t"
    "s_waitcnt lgkmcnt(4)\n\t"
    "v_mfma_f32_16x16x32_bf16 v[80:83], v[176:179], v[112:115], v[80:83]\n\t"
    "ds_read_b128 v[104:107], %[da] offset:16384\n\t"
    "v_mfma_f32_16x16x32_bf16 v[84:87], v[180:183], v[112:115], v[84:87]\n\t"
    "ds_read_b128 v[108:111], %[da] offset:18432\n\t"
    "v_mfma_f32_16x16x32_bf16 v[88:91], v[176:179], v[116:119], v[88:91]\n\t"
    "v_mfma_f32_16x16x32_bf16 v[92:95], v[180:183], v[116:119], v[92:95]\n\t"
    "s_waitcnt lgkmcnt(4)\n\t"
    "v_mfma_f32_16x16x32_bf16 v[80:83], v[184:187], v[120:123], v[80:83]\n\t"
    "ds_read_b128 v[112:115], %[ea] offset:20480\n\t"
    "v_mfma_f32_16x16x32_bf16 v[84:87], v[188:191], v[120:123], v[84:87]\n\t"
    "ds_read_b128 v[116:119], %[ea] offset:22528\n\t"
    "v_mfma_f32_16x16x32_bf16 v[88:91], v[184:187], v[124:127], v[88:91]\n\t"
    "v_mfma_f32_16x16x32_bf16 v[92:95], v[188:191], v[124:127], v[92:95]\n\t"
    "s_waitcnt lgkmcnt(4)\n\t"
    "v_mfma_f32_16x16x32_bf16 v[80:83], v[192:195], v[96:99], v[80:83]\n\t"
    "ds_read_b128 v[120:123], %[da] offset:20480\n\t"
    "v_mfma_f32_16x16x32_bf16 v[84:87], v[196:199], v[96:99], v[84:87]\n\t"
    "ds_read_b128 v[124:127], %[da] offset:22528\n\t"
    "v_mfma_f32_16x16x32_bf16 v[88:91], v[192:195], v[100:103], v[88:91]\n\t"
    "v_mfma_f32_16x16x32_bf16 v[92:95], v[196:199], v[100:103], v[92:95]\n\t"
    "s_waitcnt vmcnt(8)\n\t"
    "s_cmp_lt_u32 %[x], 2\n\t"
    "s_cbranch_scc0 49f\n\t"
    "s_waitcnt vmcnt(0)\n\t"
    "49:\n\t"
    "s_barrier\n\t"
    "s_waitcnt lgkmcnt(4)\n\t"
    "v_mfma_f32_16x16x32_bf16 v[80:83], v[200:203], v[104:107], v[80:83]\n\t"
    "ds_read_b128 v[96:99], %[ea] offset:24576\n\t"
    "v_mfma_f32_16x16x32_bf16 v[84:87], v[204:207], v[104:107], v[84:87]\n\t"
    "ds_read_b128 v[100:103], %[ea] offset:26624\n\t"
    "v_mfma_f32_16x16x32_bf16 v[88:91], v[200:203], v[108:111], v[88:91]\n\t"
    "v_mfma_f32_16x16x32_bf16 v[92:95], v[204:207], v[108:111], v[92:95]\n\t"
    "s_waitcnt lgkmcnt(4)\n\t"
    "v_mfma_f32_16x16x32_bf16 v[80:83], v[208:211], v[112:115], v[80:83]\n\t"
    "ds_read_b128 v[104:107], %[da] offset:24576\n\t"
    "v_mfma_f32_16x16x32_bf16 v[84:87], v[212:215], v[112:115], v[84:87]\n\t"
    "ds_read_b128 v[108:111], %[da] offset:26624\n\t"
    "v_mfma_f32_16x16x32_bf16 v[88:91], v[208:211], v[116:119], v[88:91]\n\t"
    "v_mfma_f32_16x16x32_bf16 v[92:95], v[212:215], v[116:119], v[92:95]\n\t"
    "s_cmp_eq_u32 %[ps], 0\n\t"
    "s_cbranch_scc1 10f\n\t"
    "global_load_dword %[sv], %[dv], %[db] sc1\n\t"
    "global_load_dword %[su], %[dv], %[db] offset:64 sc1\n\t"
    "10:\n\t"
    "s_waitcnt lgkmcnt(4)\n\t"
    "v_mfma_f32_16x16x32_bf16 v[80:83], v[216:219], v[120:123], v[80:83]\n\t"
    "ds_read_b128 v[112:115], %[ea] offset:28672\n\t"
    "v_mfma_f32_16x16x32_bf16 v[84:87], v[220:223], v[120:123], v[84:87]\n\t"
    "ds_read_b128 v[116:119], %[ea] offset:30720\n\t"
    "v_mfma_f32_16x16x32_bf16 v[88:91], v[216:219], v[124:127], v[88:91]\n\t"
    "v_mfma_f32_16x16x32_bf16 v[92:95], v[220:223], v[124:127], v[92:95]\n\t"
    "s_waitcnt lgkmcnt(4)\n\t"
    "v_mfma_f32_16x16x32_bf16 v[80:83], v[224:227], v[96:99], v[80:83]\n\t"
    "ds_read_b128 v[120:123], %[da] offset:28672\n\t"
    "v_mfma_f32_16x16x32_bf16 v[84:87], v[228:231], v[96:99], v[84:87]\n\t"
    "ds_read_b128 v[124:127], %[da] offset:30720\n\t"
    "v_mfma_f32_16x16x32_bf16 v[88:91], v[224:227], v[100:103], v[88:91]\n\t"
    "v_mfma_f32_16x16x32_bf16 v[92:95], v[228:231], v[100:103], v[92:95]\n\t"
    "s_waitcnt lgkmcnt(4)\n\t"
    "v_mfma_f32_16x16x32_bf16 v[80:83], v[232:235], v[104:107], v[80:83]\n\t"
    "v_mfma_f32_16x16x32_bf16 v[84:87], v[236:239], v[104:107], v[84:87]\n\t"
    "v_mfma_f32_16x16x32_bf16 v[88:91], v[232:235], v[108:111], v[88:91]\n\t"
    "v_mfma_f32_16x16x32_bf16 v[92:95], v[236:239], v[108:111], v[92:95]\n\t"
    "s_waitcnt lgkmcnt(2)\n\t"
    "v_mfma_f32_16x16x32_bf16 v[80:83], v[240:243], v[112:115], v[80:83]\n\t"
    "v_mfma_f32_16x16x32_bf16 v[84:87], v[244:247], v[112:115], v[84:87]\n\t"
    "v_mfma_f32_16x16x32_bf16 v[88:91], v[240:243], v[116:119], v[88:91]\n\t"
    "v_mfma_f32_16x16x32_bf16 v[92:95], v[244:247], v[116:119], v[92:95]\n\t"
    "s_waitcnt lgkmcnt(0)\n\t"
    "v_mfma_f32_16x16x32_bf16 v[80:83], v[248:251], v[120:123], v[80:83]\n\t"
    "s_memrealtime s[36:37]\n\t"
    "v_mfma_f32_16x16x32_bf16 v[84:87], v[252:255], v[120:123], v[84:87]\n\t"
    "v_mfma_f32_16x16x32_bf16 v[88:91], v[248:251], v[124:127], v[88:91]\n\t"
    "v_mfma_f32_16x16x32_bf16 v[92:95], v[252:255], v[124:127], v[92:95]\n\t"
    "s_mov_b32 %[got], 0\n\t"
    "v_mov_b32 v76, %[fa]\n\t"
    "v_mov_b32 v77, %[fv]\n\t"
    "v_add_u32 v78, 1, v77\n\t"
    "v_mov_b32 v79, %[ga]\n\t"
    "s_waitcnt lgkmcnt(0)\n\t"
    "ds_write_b128 %[rw], v[80:83] offset:0\n\t"
    "ds_write_b128 %[rw], v[84:87] offset:1024\n\t"
    "ds_write_b128 %[rw], v[88:91] offset:2048\n\t"
    "ds_write_b128 %[rw], v[92:95] offset:3072\n\t"
    "ds_write_b32 v76, v77\n\t"
    "s_cmp_eq_u32 %[ps], 0\n\t"
    "s_cbranch_scc1 9f\n\t"
    "s_waitcnt vmcnt(0)\n\t"
    "v_mad_u32_u24 v76, %[sv], %[mu], %[ad]\n\t"
    "v_cmp_ge_i32 vcc, %[th], v76\n\t"
    "s_cbranch_vccz 11f\n\t"
    "s_sub_u32 s36, s36, 20\n\t"
    "v_sub_u32 v76, s36, %[su]\n\t"
    "v_lshrrev_b32 v77, 24, %[su]\n\t"
    "v_lshlrev_b32 v76, 8, v76\n\t"
    "v_ashrrev_i32 v76, 31, v76\n\t"
    "v_add_u32 v77, v77, v76\n\t"
    "v_cmp_lt_i32 s[36:37], %[tb], v77\n\t"
    "s_andn2_b64 vcc, vcc, s[36:37]\n\t"
    "s_cbranch_scc1 9f\n\t"
    "11:\n\t"
    "ds_write2_b32 v79, v78, v78 offset1:1\n\t"
    "s_add_u32 m0, %[lb], 0\n\t"
    "s_nop 0\n\t"
    "global_load_lds_dwordx4 %[av0], %[s0] sc1\n\t"
    "s_add_u32 m0, %[lb], 4096\n\t"
    "s_nop 0\n\t"
    "global_load_lds_dwordx4 %[av2], %[s0] sc1\n\t"
    "s_add_u32 m0, %[lb], 8192\n\t"
    "s_nop 0\n\t"
    "global_load_lds_dwordx4 %[av4], %[s0] sc1\n\t"
    "s_add_u32 m0, %[lb], 12288\n\t"
    "s_nop 0\n\t"
    "global_load_lds_dwordx4 %[av6], %[s0] sc1\n\t"
    "s_add_u32 m0, %[lb], 16384\n\t"
    "s_nop 0\n\t"
    "global_load_lds_dwordx4 %[av0], %[s1] sc1\n\t"
    "s_add_u32 m0, %[lb], 20480\n\t"
    "s_nop 0\n\t"
    "global_load_lds_dwordx4 %[av2], %[s1] sc1\n\t"
    "s_add_u32 m0, %[lb], 24576\n\t"
    "s_nop 0\n\t"
    "global_load_lds_dwordx4 %[av4], %[s1] sc1\n\t"
    "s_add_u32 m0, %[lb], 28672\n\t"
    "s_nop 0\n\t"
    "global_load_lds_dwordx4 %[av6], %[s1] sc1\n\t"
    "s_mov_b32 %[got], 1\n\t"
    "s_branch 9f\n\t"
    "12:\n\t"
    "s_waitcnt lgkmcnt(0)\n\t"
    "9:"
    : [sv] "+&v"(sv), [su] "+&v"(su), [got] "=&s"(got)
    : [ea] "v"(ea), [da] "v"(da), [x] "s"(x), "{v[128:159]}"(W[0]), "{v[160:191]}"(W[1]), "{v[192:223]}"(W[2]), "{v[224:255]}"(W[3]), [lb] "s"(lb), [s0] "s"(s0), [s1] "s"(s1), [av0] "v"(av[0]), [av1] "v"(av[1]), [av2] "v"(av[2]), [av3] "v"(av[3]), [av4] "v"(av[4]), [av5] "v"(av[5]), [av6] "v"(av[6]), [av7] "v"(av[7]), [ps] "s"(ps), [dv] "v"(dv), [db] "s"(db), [rw] "v"(rw), [fa] "s"(fa), [fv] "s"(fv), [ga] "s"(ga), [mu] "s"(mu), [ad] "v"(ad), [th] "s"(th), [tb] "s"(tb)
    : "v96", "v97", "v98", "v99", "v100", "v101", "v102", "v103", "v104", "v105", "v106", "v107", "v108", "v109", "v110", "v111", "v112", "v113", "v114", "v115", "v116", "v117", "v118", "v119", "v120", "v121", "v122", "v123", "v124", "v125", "v126", "v127", "v76", "v77", "v78", "v79", "v80", "v81", "v82", "v83", "v84", "v85", "v86", "v87", "v88", "v89", "v90", "v91", "v92", "v93", "v94", "v95", "vcc", "scc", "m0", "memory", "s36", "s37", "s38", "s39");
}
__device__ __forceinline__ void lean_dm1(const u32x32 (&W)[4], unsigned ea, unsigned da, unsigned x, unsigned lb, const char* s0, const char* s1, const unsigned (&av)[8], unsigned ps, unsigned dv, const char* db, unsigned rw, unsigned fa, unsigned fv,
    unsigned ga, unsigned mu, unsigned ad, int th, int tb, unsigned& sv, unsigned& su, unsigned& got) {
  asm volatile("s_setprio 2\n\t"
    "ds_read_b128 v[96:99], %[ea] offset:0\n\t"
    "ds_read_b128 v[100:103], %[ea] offset:2048\n\t"
    "ds_read_b128 v[104:107], %[da] offset:0\n\t"
    "ds_read_b128 v[108:111], %[da] offset:2048\n\t"
    "ds_read_b128 v[112:115], %[ea] offset:4096\n\t"
    "ds_read_b128 v[116:119], %[ea] offset:6144\n\t"
    "s_waitcnt lgkmcnt(4)\n\t"
    "v_mfma_f32_16x16x32_bf16 v[80:83], v[128:131], v[96:99], 0\n\t"
    "ds_read_b128 v[120:123], %[da] offset:4096\n\t"
    "s_add_u32 m0, %[lb], 0\n\t"
    "v_mfma_f32_16x16x32_bf16 v[84:87], v[132:135], v[96:99], 0\n\t"
    "ds_read_b128 v[124:127], %[da] offset:6144\n\t"
    "global_load_lds_dwordx4 %[av0], %[s0] sc1\n\t"
    "v_mfma_f32_16x16x32_bf16 v[88:91], v[128:131], v[100:103], 0\n\t"
    "v_mfma_f32_16x16x32_bf16 v[92:95], v[132:135], v[100:103], 0\n\t"
    "s_waitcnt vmcnt(5)\n\t"
    "s_barrier\n\t"
    "s_waitcnt lgkmcnt(4)\n\t"
    "v_mfma_f32_16x16x32_bf16 v[80:83], v[136:139], v[104:107], v[80:83]\n\t"
    "ds_read_b128 v[96:99], %[ea] offset:8192\n\t"
    "s_add_u32 m0, %[lb], 4096\n\t"
    "v_mfma_f32_16x16x32_bf16 v[84:87], v[140:143], v[104:107], v[84:87]\n\t"
    "ds_read_b128 v[100:103], %[ea] offset:10240\n\t"
    "global_load_lds_dwordx4 %[av2], %[s0] sc1\n\t"
    "v_mfma_f32_16x16x32_bf16 v[88:91], v[136:139], v[108:111], v[88:91]\n\t"
    "v_mfma_f32_16x16x32_bf16 v[92:95], v[140:143], v[108:111], v[92:95]\n\t"
    "s_waitcnt lgkmcnt(4)\n\t"
    "v_mfma_f32_16x16x32_bf16 v[80:83], v[144:147], v[112:115], v[80:83]\n\t"
    "ds_read_b128 v[104:107], %[da] offset:8192\n\t"
    "s_add_u32 m0, %[lb], 8192\n\t"
    "v_mfma_f32_16x16x32_bf16 v[84:87], v[148:151], v[112:115], v[84:87]\n\t"
    "ds_read_b128 v[108:111], %[da] offset:10240\n\t"
    "global_load_lds_dwordx4 %[av4], %[s0] sc1\n\t"
    "v_mfma_f32_16x16x32_bf16 v[88:91], v[144:147], v[116:119], v[88:91]\n\t"
    "v_mfma_f32_16x16x32_bf16 v[92:95], v[148:151], v[116:119], v[92:95]\n\t"
    "s_waitcnt lgkmcnt(4)\n\t"
    "v_mfma_f32_16x16x32_bf16 v[80:83], v[152:155], v[120:123], v[80:83]\n\t"
    "ds_read_b128 v[112:115], %[ea] offset:12288\n\t"
    "s_add_u32 m0, %[lb], 12288\n\t"
    "v_mfma_f32_16x16x32_bf16 v[84:87], v[156:159], v[120:123], v[84:87]\n\t"
    "ds_read_b128 v[116:119], %[ea] offset:14336\n\t"
    "global_load_lds_dwordx4 %[av6], %[s0] sc1\n\t"
    "v_mfma_f32_16x16x32_bf16 v[88:91], v[152:155], v[124:127], v[88:91]\n\t"
    "v_mfma_f32_16x16x32_bf16 v[92:95], v[156:159], v[124:127], v[92:95]\n\t"
    "s_waitcnt lgkmcnt(4)\n\t"
    "v_mfma_f32_16x16x32_bf16 v[80:83], v[160:163], v[96:99], v[80:83]\n\t"
    "ds_read_b128 v[120:123], %[da] offset:12288\n\t"
    "s_add_u32 m0, %[lb], 16384\n\t"
    "v_mfma_f32_16x16x32_bf16 v[84:87], v[164:167], v[96:99], v[84:87]\n\t"
    "ds_read_b128 v[124:127], %[da] offset:14336\n\t"
    "global_load_lds_dwordx4 %[av0], %[s1] sc1\n\t"
    "v_mfma_f32_16x16x32_bf16 v[88:91], v[160:163], v[100:103], v[88:91]\n\t"
    "v_mfma_f32_16x16x32_bf16 v[92:95], v[164:167], v[100:103], v[92:95]\n\t"
    "s_waitcnt vmcnt(7)\n\t"
    "s_barrier\n\t"
    "s_waitcnt lgkmcnt(4)\n\t"
    "v_mfma_f32_16x16x32_bf16 v[80:83], v[168:171], v[104:107], v[80:83]\n\t"
    "ds_read_b128 v[96:99], %[ea] offset:16384\n\t"
    "s_add_u32 m0, %[lb], 20480\n\t"
    "v_mfma_f32_16x16x32_bf16 v[84:87], v[172:175], v[104:107], v[84:87]\n\t"
    "ds_read_b128 v[100:103], %[ea] offset:18432\n\t"
    "global_load_lds_dwordx4 %[av2], %[s1] sc1\n\t"
    "v_mfma_f32_16x16x32_bf16 v[88:91], v[168:171], v[108:111], v[88:91]\n\t"
    "v_mfma_f32_16x16x32_bf16 v[92:95], v[172:175], v[108:111], v[92:95]\n\t"
    "s_waitcnt lgkmcnt(4)\n\t"
    "v_mfma_f32_16x16x32_bf16 v[80:83], v[176:179], v[112:115], v[80:83]\n\t"
    "ds_read_b128 v[104:107], %[da] offset:16384\n\t"
    "s_add_u32 m0, %[lb], 24576\n\t"
    "v_mfma_f32_16x16x32_bf16 v[84:87], v[180:183], v[112:115], v[84:87]\n\t"
    "ds_read_b128 v[108:111], %[da] offset:18432\n\t"
    "global_load_lds_dwordx4 %[av4], %[s1] sc1\n\t"
    "v_mfma_f32_16x16x32_bf16 v[88:91], v[176:179], v[116:119], v[88:91]\n\t"
    "v_mfma_f32_16x16x32_bf16 v[92:95], v[180:183], v[116:119], v[92:95]\n\t"
    "s_waitcnt lgkmcnt(4)\n\t"
    "v_mfma_f32_16x16x32_bf16 v[80:83], v[184:187], v[120:123], v[80:83]\n\t"
    "ds_read_b128 v[112:115], %[ea] offset:20480\n\t"
    "s_add_u32 m0, %[lb], 28672\n\t"
    "v_mfma_f32_16x16x32_bf16 v[84:87], v[188:191], v[120:123], v[84:87]\n\t"
    "ds_read_b128 v[116:119], %[ea] offset:22528\n\t"
    "global_load_lds_dwordx4 %[av6], %[s1] sc1\n\t"
    "v_mfma_f32_16x16x32_bf16 v[88:91], v[184:187], v[124:127], v[88:91]\n\t"
    "v_mfma_f32_16x16x32_bf16 v[92:95], v[188:191], v[124:127], v[92:95]\n\t"
    "s_waitcnt lgkmcnt(4)\n\t"
    "v_mfma_f32_16x16x32_bf16 v[80:83], v[192:195], v[96:99], v[80:83]\n\t"
    "ds_read_b128 v[120:123], %[da] offset:20480\n\t"
    "v_mfma_f32_16x16x32_bf16 v[84:87], v[196:199], v[96:99], v[84:87]\n\t"
    "ds_read_b128 v[124:127], %[da] offset:22528\n\t"
    "v_mfma_f32_16x16x32_bf16 v[88:91], v[192:195], v[100:103], v[88:91]\n\t"
    "v_mfma_f32_16x16x32_bf16 v[92:95], v[196:199], v[100:103], v[92:95]\n\t"
    "s_waitcnt vmcnt(8)\n\t"
    "s_barrier\n\t"
    "s_waitcnt lgkmcnt(4)\n\t"
    "v_mfma_f32_16x16x32_bf16 v[80:83], v[200:203], v[104:107], v[80:83]\n\t"
    "ds_read_b128 v[96:99], %[ea] offset:24576\n\t"
    "v_mfma_f32_16x16x32_bf16 v[84:87], v[204:207], v[104:107], v[84:87]\n\t"
    "ds_read_b128 v[100:103], %[ea] offset:26624\n\t"
    "v_mfma_f32_16x16x32_bf16 v[88:91], v[200:203], v[108:111], v[88:91]\n\t"
    "v_mfma_f32_16x16x32_bf16 v[92:95], v[204:207], v[108:111], v[92:95]\n\t"
    "s_waitcnt lgkmcnt(4)\n\t"
    "v_mfma_f32_16x16x32_bf16 v[80:83], v[208:211], v[112:115], v[80:83]\n\t"
    "ds_read_b128 v[104:107], %[da] offset:24576\n\t"
    "v_mfma_f32_16x16x32_bf16 v[84:87], v[212:215], v[112:115], v[84:87]\n\t"
    "ds_read_b128 v[108:111], %[da] offset:26624\n\t"
    "v_mfma_f32_16x16x32_bf16 v[88:91], v[208:211], v[116:119], v[88:91]\n\t"
    "v_mfma_f32_16x16x32_bf16 v[92:95], v[212:215], v[116:119], v[92:95]\n\t"
    "s_cmp_eq_u32 %[ps], 0\n\t"
    "s_cbranch_scc1 10f\n\t"
    "global_load_dword %[sv], %[dv], %[db] sc1\n\t"
    "global_load_dword %[su], %[dv], %[db] offset:64 sc1\n\t"
    "10:\n\t"
    "s_waitcnt lgkmcnt(4)\n\t"
    "v_mfma_f32_16x16x32_bf16 v[80:83], v[216:219], v[120:123], v[80:83]\n\t"
    "ds_read_b128 v[112:115], %[ea] offset:28672\n\t"
    "v_mfma_f32_16x16x32_bf16 v[84:87], v[220:223], v[120:123], v[84:87]\n\t"
    "ds_read_b128 v[116:119], %[ea] offset:30720\n\t"
    "v_mfma_f32_16x16x32_bf16 v[88:91], v[216:219], v[124:127], v[88:91]\n\t"
    "v_mfma_f32_16x16x32_bf16 v[92:95], v[220:223], v[124:127], v[92:95]\n\t"
    "s_waitcnt lgkmcnt(4)\n\t"
    "v_mfma_f32_16x16x32_bf16 v[80:83], v[224:227], v[96:99], v[80:83]\n\t"
    "ds_read_b128 v[120:123], %[da] offset:28672\n\t"
    "v_mfma_f32_16x16x32_bf16 v[84:87], v[228:231], v[96:99], v[84:87]\n\t"
    "ds_read_b128 v[124:127], %[da] offset:30720\n\t"
    "v_mfma_f32_16x16x32_bf16 v[88:91], v[224:227], v[100:103], v[88:91]\n\t"
    "v_mfma_f32_16x16x32_bf16 v[92:95], v[228:231], v[100:103], v[92:95]\n\t"
    "s_waitcnt lgkmcnt(4)\n\t"
    "v_mfma_f32_16x16x32_bf16 v[80:83], v[232:235], v[104:107], v[80:83]\n\t"
    "v_mfma_f32_16x16x32_bf16 v[84:87], v[236:239], v[104:107], v[84:87]\n\t"
    "v_mfma_f32_16x16x32_bf16 v[88:91], v[232:235], v[108:111], v[88:91]\n\t"
    "v_mfma_f32_16x16x32_bf16 v[92:95], v[236:239], v[108:111], v[92:95]\n\t"
    "s_waitcnt lgkmcnt(2)\n\t"
    "v_mfma_f32_16x16x32_bf16 v[80:83], v[240:243], v[112:115], v[80:83]\n\t"
    "v_mfma_f32_16x16x32_bf16 v[84:87], v[244:247], v[112:115], v[84:87]\n\t"
    "v_mfma_f32_16x16x32_bf16 v[88:91], v[240:243], v[116:119], v[88:91]\n\t"
    "v_mfma_f32_16x16x32_bf16 v[92:95], v[244:247], v[116:119], v[92:95]\n\t"
    "s_waitcnt lgkmcnt(0)\n\t"
    "v_mfma_f32_16x16x32_bf16 v[80:83], v[248:251], v[120:123], v[80:83]\n\t"
    "v_mfma_f32_16x16x32_bf16 v[84:87], v[252:255], v[120:123], v[84:87]\n\t"
    "v_mfma_f32_16x16x32_bf16 v[88:91], v[248:251], v[124:127], v[88:91]\n\t"
    "v_mfma_f32_16x16x32_bf16 v[92:95], v[252:255], v[124:127], v[92:95]\n\t"
    "s_mov_b32 %[got], 0\n\t"
    "v_mov_b32 v76, %[fa]\n\t"
    "v_mov_b32 v77, %[fv]\n\t"
    "v_add_u32 v78, 1, v77\n\t"
    "v_mov_b32 v79, %[ga]\n\t"
    "ds_write_b128 %[rw], v[80:83] offset:0\n\t"
    "ds_write_b128 %[rw], v[84:87] offset:1024\n\t"
    "ds_write_b128 %[rw], v[88:91] offset:2048\n\t"
    "ds_write_b128 %[rw], v[92:95] offset:3072\n\t"
    "ds_write_b32 v76, v77\n\t"
    "s_cmp_eq_u32 %[ps], 0\n\t"
    "s_cbranch_scc1 9f\n\t"
    "s_waitcnt vmcnt(0)\n\t"
    "9:"
    : [sv] "+&v"(sv), [su] "+&v"(su), [got] "=&s"(got)
    : [ea] "v"(ea), [da] "v"(da), [x] "s"(x), "{v[128:159]}"(W[0]), "{v[160:191]}"(W[1]), "{v[192:223]}"(W[2]), "{v[224:255]}"(W[3]), [lb] "s"(lb), [s0] "s"(s0), [s1] "s"(s1), [av0] "v"(av[0]), [av1] "v"(av[1]), [av2] "v"(av[2]), [av3] "v"(av[3]), [av4] "v"(av[4]), [av5] "v"(av[5]), [av6] "v"(av[6]), [av7] "v"(av[7]), [ps] "s"(ps), [dv] "v"(dv), [db] "s"(db), [rw] "v"(rw), [fa] "s"(fa), [fv] "s"(fv), [ga] "s"(ga), [mu] "s"(mu), [ad] "v"(ad), [th] "s"(th), [tb] "s"(tb)
    : "v96", "v97", "v98", "v99", "v100", "v101", "v102", "v103", "v104", "v105", "v106", "v107", "v108", "v109", "v110", "v111", "v112", "v113", "v114", "v115", "v116", "v117", "v118", "v119", "v120", "v121", "v122", "v123", "v124", "v125", "v126", "v127", "v76", "v77", "v78", "v79", "v80", "v81", "v82", "v83", "v84", "v85", "v86", "v87", "v88", "v89", "v90", "v91", "v92", "v93", "v94", "v95", "vcc", "scc", "m0", "memory");
}
__device__ __forceinline__ void lean_st0(const u32x32 (&W)[4], unsigned ea, unsigned da, unsigned x, u32x16& pd, unsigned pv, unsigned cn, const char* pg, unsigned bl, unsigned& tk, unsigned& st) {
  asm volatile("v_mov_b32 v76, %[cn]\n\t"
    "ds_read_b32 v76, v76 offset:28\n\t"
    "ds_read_b128 v[96:99], %[ea] offset:0\n\t"
    "ds_read_b128 v[100:103], %[ea] offset:2048\n\t"
    "ds_read_b128 v[104:107], %[da] offset:0\n\t"
    "ds_read_b128 v[108:111], %[da] offset:2048\n\t"
    "ds_read_b128 v[112:115], %[ea] offset:4096\n\t"
    "ds_read_b128 v[116:119], %[ea] offset:6144\n\t"
    "s_waitcnt lgkmcnt(6)\n\t"
    "v_cmp_ne_u32 vcc, 0, v76\n\t"
    "s_mov_b32 %[st], -1\n\t"
    "s_cbranch_vccnz 12f\n\t"
    "s_waitcnt lgkmcnt(4)\n\t"
    "v_mfma_f32_16x16x32_bf16 v[80:83], v[128:131], v[96:99], 0\n\t"
    "ds_read_b128 v[120:123], %[da] offset:4096\n\t"
    "v_mfma_f32_16x16x32_bf16 v[84:87], v[132:135], v[96:99], 0\n\t"
    "ds_read_b128 v[124:127], %[da] offset:6144\n\t"
    "v_mfma_f32_16x16x32_bf16 v[88:91], v[128:131], v[100:103], 0\n\t"
    "v_mfma_f32_16x16x32_bf16 v[92:95], v[132:135], v[100:103], 0\n\t"
    "s_barrier\n\t"
    "s_bitcmp1_b32 %[x], 2\n\t"
    "s_cbranch_scc1 7f\n\t"
    "8:\n\t"
    "s_bitcmp1_b32 %[x], 3\n\t"
    "s_cbranch_scc1 13f\n\t"
    "14:\n\t"
    "s_waitcnt lgkmcnt(4)\n\t"
    "v_mfma_f32_16x16x32_bf16 v[80:83], v[136:139], v[104:107], v[80:83]\n\t"
    "ds_read_b128 v[96:99], %[ea] offset:8192\n\t"
    "v_mfma_f32_16x16x32_bf16 v[84:87], v[140:143], v[104:107], v[84:87]\n\t"
    "ds_read_b128 v[100:103], %[ea] offset:10240\n\t"
    "v_mfma_f32_16x16x32_bf16 v[88:91], v[136:139], v[108:111], v[88:91]\n\t"
    "v_mfma_f32_16x16x32_bf16 v[92:95], v[140:143], v[108:111], v[92:95]\n\t"
    "s_waitcnt lgkmcnt(4)\n\t"
    "v_mfma_f32_16x16x32_bf16 v[80:83], v[144:147], v[112:115], v[80:83]\n\t"
    "ds_read_b128 v[104:107], %[da] offset:8192\n\t"
    "v_mfma_f32_16x16x32_bf16 v[84:87], v[148:151], v[112:115], v[84:87]\n\t"
    "ds_read_b128 v[108:111], %[da] offset:10240\n\t"
    "v_mfma_f32_16x16x32_bf16 v[88:91], v[144:147], v[116:119], v[88:91]\n\t"
    "v_mfma_f32_16x16x32_bf16 v[92:95], v[148:151], v[116:119], v[92:95]\n\t"
    "s_waitcnt lgkmcnt(4)\n\t"
    "v_mfma_f32_16x16x32_bf16 v[80:83], v[152:155], v[120:123], v[80:83]\n\t"
    "ds_read_b128 v[112:115], %[ea] offset:12288\n\t"
    "v_mfma_f32_16x16x32_bf16 v[84:87], v[156:159], v[120:123], v[84:87]\n\t"
    "ds_read_b128 v[116:119], %[ea] offset:14336\n\t"
    "v_mfma_f32_16x16x32_bf16 v[88:91], v[152:155], v[124:127], v[88:91]\n\t"
    "v_mfma_f32_16x16x32_bf16 v[92:95], v[156:159], v[124:127], v[92:95]\n\t"
    "s_waitcnt lgkmcnt(4)\n\t"
    "v_mfma_f32_16x16x32_bf16 v[80:83], v[160:163], v[96:99], v[80:83]\n\t"
    "ds_read_b128 v[120:123], %[da] offset:12288\n\t"
    "v_mfma_f32_16x16x32_bf16 v[84:87], v[164:167], v[96:99], v[84:87]\n\t"
    "ds_read_b128 v[124:127], %[da] offset:14336\n\t"
    "v_mfma_f32_16x16x32_bf16 v[88:91], v[160:163], v[100:103], v[88:91]\n\t"
    "v_mfma_f32_16x16x32_bf16 v[92:95], v[164:167], v[100:103], v[92:95]\n\t"
    "s_barrier\n\t"
    "s_waitcnt lgkmcnt(4)\n\t"
    "v_mfma_f32_16x16x32_bf16 v[80:83], v[168:171], v[104:107], v[80:83]\n\t"
    "ds_read_b128 v[96:99], %[ea] offset:16384\n\t"
    "v_mfma_f32_16x16x32_bf16 v[84:87], v[172:175], v[104:107], v[84:87]\n\t"
    "ds_read_b128 v[100:103], %[ea] offset:18432\n\t"
    "v_mfma_f32_16x16x32_bf16 v[88:91], v[168:171], v[108:111], v[88:91]\n\t"
    "v_mfma_f32_16x16x32_bf16 v[92:95], v[172:175], v[108:111], v[92:95]\n\t"
    "s_waitcnt lgkmcnt(4)\n\t"
    "v_mfma_f32_16x16x32_bf16 v[80:83], v[176:179], v[112:115], v[80:83]\n\t"
    "ds_read_b128 v[104:107], %[da] offset:16384\n\t"
    "v_mfma_f32_16x16x32_bf16 v[84:87], v[180:183], v[112:115], v[84:87]\n\t"
    "ds_read_b128 v[108:111], %[da] offset:18432\n\t"
    "v_mfma_f32_16x16x32_bf16 v[88:91], v[176:179], v[116:119], v[88:91]\n\t"
    "v_mfma_f32_16x16x32_bf16 v[92:95], v[180:183], v[116:119], v[92:95]\n\t"
    "s_waitcnt lgkmcnt(4)\n\t"
    "v_mfma_f32_16x16x32_bf16 v[80:83], v[184:187], v[120:123], v[80:83]\n\t"
    "ds_read_b128 v[112:115], %[ea] offset:20480\n\t"
    "v_mfma_f32_16x16x32_bf16 v[84:87], v[188:191], v[120:123], v[84:87]\n\t"
    "ds_read_b128 v[116:119], %[ea] offset:22528\n\t"
    "v_mfma_f32_16x16x32_bf16 v[88:91], v[184:187], v[124:127], v[88:91]\n\t"
    "v_mfma_f32_16x16x32_bf16 v[92:95], v[188:191], v[124:127], v[92:95]\n\t"
    "s_waitcnt lgkmcnt(4)\n\t"
    "v_mfma_f32_16x16x32_bf16 v[80:83], v[192:195], v[96:99], v[80:83]\n\t"
    "ds_read_b128 v[120:123], %[da] offset:20480\n\t"
    "v_mfma_f32_16x16x32_bf16 v[84:87], v[196:199], v[96:99], v[84:87]\n\t"
    "ds_read_b128 v[124:127], %[da] offset:22528\n\t"
    "v_mfma_f32_16x16x32_bf16 v[88:91], v[192:195], v[100:103], v[88:91]\n\t"
    "v_mfma_f32_16x16x32_bf16 v[92:95], v[196:199], v[100:103], v[92:95]\n\t"
    "s_barrier\n\t"
    "s_and_b32 %[st], %[x], 3\n\t"
    "s_cbranch_scc1 3f\n\t"
    "4:\n\t"
    "s_waitcnt lgkmcnt(4)\n\t"
    "v_mfma_f32_16x16x32_bf16 v[80:83], v[200:203], v[104:107], v[80:83]\n\t"
    "ds_read_b128 v[96:99], %[ea] offset:24576\n\t"
    "v_mfma_f32_16x16x32_bf16 v[84:87], v[204:207], v[104:107], v[84:87]\n\t"
    "ds_read_b128 v[100:103], %[ea] offset:26624\n\t"
    "v_mfma_f32_16x16x32_bf16 v[88:91], v[200:203], v[108:111], v[88:91]\n\t"
    "v_mfma_f32_16x16x32_bf16 v[92:95], v[204:207], v[108:111], v[92:95]\n\t"
    "s_waitcnt lgkmcnt(4)\n\t"
    "v_mfma_f32_16x16x32_bf16 v[80:83], v[208:211], v[112:115], v[80:83]\n\t"
    "ds_read_b128 v[104:107], %[da] offset:24576\n\t"
    "v_mfma_f32_16x16x32_bf16 v[84:87], v[212:215], v[112:115], v[84:87]\n\t"
    "ds_read_b128 v[108:111], %[da] offset:26624\n\t"
    "v_mfma_f32_16x16x32_bf16 v[88:91], v[208:211], v[116:119], v[88:91]\n\t"
    "v_mfma_f32_16x16x32_bf16 v[92:95], v[212:215], v[116:119], v[92:95]\n\t"
    "s_waitcnt lgkmcnt(4)\n\t"
    "v_mfma_f32_16x16x32_bf16 v[80:83], v[216:219], v[120:123], v[80:83]\n\t"
    "ds_read_b128 v[112:115], %[ea] offset:28672\n\t"
    "v_mfma_f32_16x16x32_bf16 v[84:87], v[220:223], v[120:123], v[84:87]\n\t"
    "ds_read_b128 v[116:119], %[ea] offset:30720\n\t"
    "v_mfma_f32_16x16x32_bf16 v[88:91], v[216:219], v[124:127], v[88:91]\n\t"
    "v_mfma_f32_16x16x32_bf16 v[92:95], v[220:223], v[124:127], v[92:95]\n\t"
    "s_waitcnt lgkmcnt(4)\n\t"
    "v_mfma_f32_16x16x32_bf16 v[80:83], v[224:227], v[96:99], v[80:83]\n\t"
    "ds_read_b128 v[120:123], %[da] offset:28672\n\t"
    "v_mfma_f32_16x16x32_bf16 v[84:87], v[228:231], v[96:99], v[84:87]\n\t"
    "ds_read_b128 v[124:127], %[da] offset:30720\n\t"
    "v_mfma_f32_16x16x32_bf16 v[88:91], v[224:227], v[100:103], v[88:91]\n\t"
    "v_mfma_f32_16x16x32_bf16 v[92:95], v[228:231], v[100:103], v[92:95]\n\t"
    "s_waitcnt lgkmcnt(4)\n\t"
    "v_mfma_f32_16x16x32_bf16 v[80:83], v[232:235], v[104:107], v[80:83]\n\t"
    "v_mfma_f32_16x16x32_bf16 v[84:87], v[236:239], v[104:107], v[84:87]\n\t"
    "v_mfma_f32_16x16x32_bf16 v[88:91], v[232:235], v[108:111], v[88:91]\n\t"
    "v_mfma_f32_16x16x32_bf16 v[92:95], v[236:239], v[108:111], v[92:95]\n\t"
    "s_waitcnt lgkmcnt(2)\n\t"
    "v_mfma_f32_16x16x32_bf16 v[80:83], v[240:243], v[112:115], v[80:83]\n\t"
    "v_mfma_f32_16x16x32_bf16 v[84:87], v[244:247], v[112:115], v[84:87]\n\t"
    "v_mfma_f32_16x16x32_bf16 v[88:91], v[240:243], v[116:119], v[88:91]\n\t"
    "v_mfma_f32_16x16x32_bf16 v[92:95], v[244:247], v[116:119], v[92:95]\n\t"
    "s_waitcnt lgkmcnt(0)\n\t"
    "v_mfma_f32_16x16x32_bf16 v[80:83], v[248:251], v[120:123], v[80:83]\n\t"
    "v_mfma_f32_16x16x32_bf16 v[84:87], v[252:255], v[120:123], v[84:87]\n\t"
    "v_mfma_f32_16x16x32_bf16 v[88:91], v[248:251], v[124:127], v[88:91]\n\t"
    "v_mfma_f32_16x16x32_bf16 v[92:95], v[252:255], v[124:127], v[92:95]\n\t"
    "s_mov_b32 %[st], 0\n\t"
    "s_nop 5\n\t"
    "s_branch 9f\n\t"
    "13:\n\t"
    "s_memrealtime s[36:37]\n\t"
    "v_mov_b32 v79, 0\n\t"
    "s_waitcnt lgkmcnt(0)\n\t"
    "s_mov_b32 %[tk], s36\n\t"
    "s_add_u32 s36, s36, %[bl]\n\t"
    "s_and_b32 s36, s36, 0xffffff\n\t"
    "s_and_b32 s37, %[bl], 0xff000000\n\t"
    "s_or_b32 s36, s36, s37\n\t"
    "v_mov_b32 v78, s36\n\t"
    "s_mov_b64 exec, 1\n\t"
    "global_store_dword v79, v78, %[pg] offset:64 sc1\n\t"
    "s_mov_b64 exec, -1\n\t"
    "s_branch 14b\n\t"
    "3:\n\t"
    "7:\n\t"
    "s_and_b32 %[st], %[pv], 1\n\t"
    "s_mul_i32 %[st], %[st], 36\n\t"
    "s_add_u32 %[st], %[st], %[cn]\n\t"
    "v_mov_b32 v76, %[st]\n\t"
    "v_mov_b32 v77, 1\n\t"
    "v_mov_b32 v78, %[pv]\n\t"
    "v_mov_b32 v79, 0\n\t"
    "s_waitcnt vmcnt(0)\n\t"
    "s_mov_b64 exec, 1\n\t"
    "ds_add_rtn_u32 v77, v76, v77\n\t"
    "s_waitcnt lgkmcnt(0)\n\t"
    "v_readfirstlane_b32 %[st], v77\n\t"
    "s_and_b32 %[st], %[st], 3\n\t"
    "s_cmp_lg_u32 %[st], 3\n\t"
    "s_cbranch_scc1 6f\n\t"
    "global_store_dword v79, v78, %[pg] sc1\n\t"
    "6:\n\t"
    "s_mov_b64 exec, -1\n\t"
    "s_bitcmp1_b32 %[x], 2\n\t"
    "s_cbranch_scc1 8b\n\t"
    "s_branch 4b\n\t"
    "12:\n\t"
    "s_waitcnt lgkmcnt(0)\n\t"
    "9:"
    : "=&{v[80:95]}"(pd), [st] "=&s"(st), [tk] "+&s"(tk)
    : [ea] "v"(ea), [da] "v"(da), [x] "s"(x), "{v[128:159]}"(W[0]), "{v[160:191]}"(W[1]), "{v[192:223]}"(W[2]), "{v[224:255]}"(W[3]), [pv] "s"(pv), [cn] "s"(cn), [pg] "s"(pg), [bl] "s"(bl)
    : "v96", "v97", "v98", "v99", "v100", "v101", "v102", "v103", "v104", "v105", "v106", "v107", "v108", "v109", "v110", "v111", "v112", "v113", "v114", "v115", "v116", "v117", "v118", "v119", "v120", "v121", "v122", "v123", "v124", "v125", "v126", "v127", "v76", "v77", "v78", "v79", "vcc", "scc", "m0", "memory", "s36", "s37", "s38", "s39");
}
#define LEAN_QB 1
#define PREK 10
#define NEARLY 16
__device__ __forceinline__ void fin_ld(const u32x16& pd, unsigned rw, unsigned fa, unsigned fv) {
  asm volatile("ds_write_b128 %[rw], v[80:83] offset:0\n\t"
    "ds_write_b128 %[rw], v[84:87] offset:1024\n\t"
    "ds_write_b128 %[rw], v[88:91] offset:2048\n\t"
    "ds_write_b128 %[rw], v[92:95] offset:3072\n\t"
    "ds_write_b32 %[fa], %[fv]"
    :
    : [rw] "v"(rw), "{v[80:95]}"(pd), [fa] "v"(fa), [fv] "v"(fv)
    : "memory");
}
__device__ __forceinline__ void fin_st0(const u32x16& pd, unsigned rw, unsigned fa, unsigned fv, const char* cb, unsigned co0, unsigned co1, unsigned pa, unsigned& bad, unsigned& pk) {
  asm volatile("1:\n\t"
    "ds_read_b32 v39, %[fa]\n\t"
    "ds_read_b128 v[48:51], %[rw] offset:0\n\t"
    "ds_read_b128 v[52:55], %[rw] offset:1024\n\t"
    "ds_read_b128 v[56:59], %[rw] offset:2048\n\t"
    "ds_read_b128 v[60:63], %[rw] offset:3072\n\t"
    "s_waitcnt lgkmcnt(2)\n\t"
    "v_cmp_ne_u32 vcc, v39, %[fv]\n\t"
    "s_cbranch_vccz 2f\n\t"
    "s_waitcnt lgkmcnt(0)\n\t"
    "s_branch 1b\n\t"
    "2:\n\t"
    "v_add_f32 v48, v48, v80\n\t"
    "v_add_f32 v49, v49, v81\n\t"
    "v_add_f32 v50, v50, v82\n\t"
    "v_add_f32 v51, v51, v83\n\t"
    "v_add_f32 v52, v52, v84\n\t"
    "v_add_f32 v53, v53, v85\n\t"
    "v_add_f32 v54, v54, v86\n\t"
    "v_add_f32 v55, v55, v87\n\t"
    "v_cvt_pk_bf16_f32 v40, v48, v49\n\t"
    "v_cvt_pk_bf16_f32 v41, v50, v51\n\t"
    "v_cvt_pk_bf16_f32 v42, v52, v53\n\t"
    "v_cvt_pk_bf16_f32 v43, v54, v55\n\t"
    "global_store_dwordx4 %[co0], v[40:43], %[cb] sc1\n\t"
    "s_waitcnt lgkmcnt(0)\n\t"
    "v_mov_b32 v39, %[pa]\n\t"
    "ds_read_b32 v39, v39\n\t"
    "v_add_f32 v56, v56, v88\n\t"
    "v_add_f32 v57, v57, v89\n\t"
    "v_add_f32 v58, v58, v90\n\t"
    "v_add_f32 v59, v59, v91\n\t"
    "v_add_f32 v60, v60, v92\n\t"
    "v_add_f32 v61, v61, v93\n\t"
    "v_add_f32 v62, v62, v94\n\t"
    "v_add_f32 v63, v63, v95\n\t"
    "v_cvt_pk_bf16_f32 v44, v56, v57\n\t"
    "v_cvt_pk_bf16_f32 v45, v58, v59\n\t"
    "v_cvt_pk_bf16_f32 v46, v60, v61\n\t"
    "v_cvt_pk_bf16_f32 v47, v62, v63\n\t"
    "global_store_dwordx4 %[co1], v[44:47], %[cb] sc1\n\t"
    "v_cmp_u_f32 vcc, v48, v56\n\t"
    "s_waitcnt lgkmcnt(0)\n\t"
    "s_or_b32 %[bad], vcc_lo, vcc_hi\n\t"
    "v_readfirstlane_b32 %[pk], v39"
    : [bad] "=&s"(bad), [pk] "=&s"(pk)
    : [rw] "v"(rw), "{v[80:95]}"(pd), [fa] "v"(fa), [fv] "v"(fv), [cb] "s"(cb), [co0] "v"(co0), [co1] "v"(co1), [pa] "s"(pa)
    : "v39", "v40", "v41", "v42", "v43", "v44", "v45", "v46", "v47", "v48", "v49", "v50", "v51", "v52", "v53", "v54", "v55", "v56", "v57", "v58", "v59", "v60", "v61", "v62", "v63", "vcc", "scc", "memory");
}
__device__ __forceinline__ void fin_st1(const u32x16& pd, unsigned rw, unsigned fa, unsigned fv, const char* cb, unsigned co0, unsigned co1, unsigned pa, unsigned& bad, unsigned& pk) {
  asm volatile("1:\n\t"
    "ds_read_b32 v39, %[fa]\n\t"
    "ds_read_b128 v[48:51], %[rw] offset:0\n\t"
    "ds_read_b128 v[52:55], %[rw] offset:1024\n\t"
    "ds_read_b128 v[56:59], %[rw] offset:2048\n\t"
    "ds_read_b128 v[60:63], %[rw] offset:3072\n\t"
    "s_waitcnt lgkmcnt(2)\n\t"
    "v_cmp_ne_u32 vcc, v39, %[fv]\n\t"
    "s_cbranch_vccz 2f\n\t"
    "s_waitcnt lgkmcnt(0)\n\t"
    "s_branch 1b\n\t"
    "2:\n\t"
    "v_add_f32 v48, v48, v80\n\t"
    "v_add_f32 v49, v49, v81\n\t"
    "v_add_f32 v50, v50, v82\n\t"
    "v_add_f32 v51, v51, v83\n\t"
    "v_add_f32 v52, v52, v84\n\t"
    "v_add_f32 v53, v53, v85\n\t"
    "v_add_f32 v54, v54, v86\n\t"
    "v_add_f32 v55, v55, v87\n\t"
    "s_waitcnt lgkmcnt(0)\n\t"
    "v_mov_b32 v39, %[pa]\n\t"
    "ds_read_b32 v39, v39\n\t"
    "v_add_f32 v56, v56, v88\n\t"
    "v_add_f32 v57, v57, v89\n\t"
    "v_add_f32 v58, v58, v90\n\t"
    "v_add_f32 v59, v59, v91\n\t"
    "v_add_f32 v60, v60, v92\n\t"
    "v_add_f32 v61, v61, v93\n\t"
    "v_add_f32 v62, v62, v94\n\t"
    "v_add_f32 v63, v63, v95\n\t"
    "v_mul_f32 v40, %[nl], v48\n\t"
    "v_mul_f32 v41, %[nl], v49\n\t"
    "v_mul_f32 v42, %[nl], v50\n\t"
    "v_mul_f32 v43, %[nl], v51\n\t"
    "v_mul_f32 v44, %[nl], v56\n\t"
    "v_mul_f32 v45, %[nl], v57\n\t"
    "v_mul_f32 v46, %[nl], v58\n\t"
    "v_mul_f32 v47, %[nl], v59\n\t"
    "v_exp_f32 v40, v40\n\t"
    "v_exp_f32 v41, v41\n\t"
    "v_exp_f32 v42, v42\n\t"
    "v_exp_f32 v43, v43\n\t"
    "v_exp_f32 v44, v44\n\t"
    "v_exp_f32 v45, v45\n\t"
    "v_exp_f32 v46, v46\n\t"
    "v_exp_f32 v47, v47\n\t"
    "v_add_f32 v40, 1.0, v40\n\t"
    "v_add_f32 v41, 1.0, v41\n\t"
    "v_add_f32 v42, 1.0, v42\n\t"
    "v_add_f32 v43, 1.0, v43\n\t"
    "v_add_f32 v44, 1.0, v44\n\t"
    "v_add_f32 v45, 1.0, v45\n\t"
    "v_add_f32 v46, 1.0, v46\n\t"
    "v_add_f32 v47, 1.0, v47\n\t"
    "v_rcp_f32 v40, v40\n\t"
    "v_rcp_f32 v41, v41\n\t"
    "v_rcp_f32 v42, v42\n\t"
    "v_rcp_f32 v43, v43\n\t"
    "v_rcp_f32 v44, v44\n\t"
    "v_rcp_f32 v45, v45\n\t"
    "v_rcp_f32 v46, v46\n\t"
    "v_rcp_f32 v47, v47\n\t"
    "v_mul_f32 v40, v48, v40\n\t"
    "v_mul_f32 v41, v49, v41\n\t"
    "v_mul_f32 v42, v50, v42\n\t"
    "v_mul_f32 v43, v51, v43\n\t"
    "v_mul_f32 v44, v56, v44\n\t"
    "v_mul_f32 v45, v57, v45\n\t"
    "v_mul_f32 v46, v58, v46\n\t"
    "v_mul_f32 v47, v59, v47\n\t"
    "v_mul_f32 v40, v40, v52\n\t"
    "v_mul_f32 v41, v41, v53\n\t"
    "v_mul_f32 v42, v42, v54\n\t"
    "v_mul_f32 v43, v43, v55\n\t"
    "v_mul_f32 v44, v44, v60\n\t"
    "v_mul_f32 v45, v45, v61\n\t"
    "v_mul_f32 v46, v46, v62\n\t"
    "v_mul_f32 v47, v47, v63\n\t"
    "v_cvt_pk_bf16_f32 v48, v40, v41\n\t"
    "v_cvt_pk_bf16_f32 v49, v42, v43\n\t"
    "v_cvt_pk_bf16_f32 v50, v44, v45\n\t"
    "v_cvt_pk_bf16_f32 v51, v46, v47\n\t"
    "s_nop 1\n\t"
    "v_permlane16_swap_b32 v48, v50\n\t"
    "v_permlane16_swap_b32 v49, v51\n\t"
    "global_store_dwordx4 %[co0], v[48:51], %[cb] sc1\n\t"
    "v_cmp_u_f32 vcc, v52, v60\n\t"
    "s_waitcnt lgkmcnt(0)\n\t"
    "s_or_b32 %[bad], vcc_lo, vcc_hi\n\t"
    "v_readfirstlane_b32 %[pk], v39"
    : [bad] "=&s"(bad), [pk] "=&s"(pk)
    : [rw] "v"(rw), "{v[80:95]}"(pd), [fa] "v"(fa), [fv] "v"(fv), [cb] "s"(cb), [co0] "v"(co0), [nl] "s"(-LOG2E), [pa] "s"(pa)
    : "v39", "v40", "v41", "v42", "v43", "v44", "v45", "v46", "v47", "v48", "v49", "v50", "v51", "v52", "v53", "v54", "v55", "v56", "v57", "v58", "v59", "v60", "v61", "v62", "v63", "vcc", "scc", "memory");
}
__device__ __forceinline__ void fin_st2(const u32x16& pd, unsigned rw, unsigned fa, unsigned fv, const char* cb, unsigned co0, unsigned co1, unsigned pa, unsigned& bad, unsigned& pk) {
  asm volatile("s_nop 1\n\t"
    "v_cvt_pk_bf16_f32 v40, v80, v81\n\t"
    "v_cvt_pk_bf16_f32 v41, v82, v83\n\t"
    "v_cvt_pk_bf16_f32 v42, v84, v85\n\t"
    "v_cvt_pk_bf16_f32 v43, v86, v87\n\t"
    "global_store_dwordx4 %[co0], v[40:43], %[cb] sc1\n\t"
    "v_cvt_pk_bf16_f32 v44, v88, v89\n\t"
    "v_cvt_pk_bf16_f32 v45, v90, v91\n\t"
    "v_cvt_pk_bf16_f32 v46, v92, v93\n\t"
    "v_cvt_pk_bf16_f32 v47, v94, v95\n\t"
    "global_store_dwordx4 %[co1], v[44:47], %[cb] sc1\n\t"
    "1:\n\t"
    "ds_read_b32 v39, %[fa]\n\t"
    "ds_read_b128 v[48:51], %[rw] offset:0\n\t"
    "ds_read_b128 v[52:55], %[rw] offset:1024\n\t"
    "ds_read_b128 v[56:59], %[rw] offset:2048\n\t"
    "ds_read_b128 v[60:63], %[rw] offset:3072\n\t"
    "s_waitcnt lgkmcnt(2)\n\t"
    "v_cmp_ne_u32 vcc, v39, %[fv]\n\t"
    "s_cbranch_vccz 2f\n\t"
    "s_waitcnt lgkmcnt(0)\n\t"
    "s_branch 1b\n\t"
    "2:\n\t"
    "v_cvt_pk_bf16_f32 v48, v48, v49\n\t"
    "v_cvt_pk_bf16_f32 v49, v50, v51\n\t"
    "v_cvt_pk_bf16_f32 v50, v52, v53\n\t"
    "v_cvt_pk_bf16_f32 v51, v54, v55\n\t"
    "global_store_dwordx4 %[co0], v[48:51], %[cb] offset:2048 sc1\n\t"
    "s_waitcnt lgkmcnt(0)\n\t"
    "v_mov_b32 v39, %[pa]\n\t"
    "ds_read_b32 v39, v39\n\t"
    "v_cvt_pk_bf16_f32 v56, v56, v57\n\t"
    "v_cvt_pk_bf16_f32 v57, v58, v59\n\t"
    "v_cvt_pk_bf16_f32 v58, v60, v61\n\t"
    "v_cvt_pk_bf16_f32 v59, v62, v63\n\t"
    "global_store_dwordx4 %[co1], v[56:59], %[cb] offset:2048 sc1\n\t"
    "v_cmp_u_f32 vcc, v80, v88\n\t"
    "s_waitcnt lgkmcnt(0)\n\t"
    "s_or_b32 %[bad], vcc_lo, vcc_hi\n\t"
    "v_readfirstlane_b32 %[pk], v39"
    : [bad] "=&s"(bad), [pk] "=&s"(pk)
    : [rw] "v"(rw), "{v[80:95]}"(pd), [fa] "v"(fa), [fv] "v"(fv), [cb] "s"(cb), [co0] "v"(co0), [co1] "v"(co1), [pa] "s"(pa)
    : "v39", "v40", "v41", "v42", "v43", "v44", "v45", "v46", "v47", "v48", "v49", "v50", "v51", "v52", "v53", "v54", "v55", "v56", "v57", "v58", "v59", "v60", "v61", "v62", "v63", "vcc", "scc", "memory");
}
template <int R> __device__ __forceinline__ void gw_load(u32x32& w, unsigned a0, unsigned a1);
template <> __device__ __forceinline__ void gw_load<0>(u32x32& w, unsigned a0, unsigned a1) {
  asm volatile("ds_read_b64_tr_b16 v[128:129], %1 offset:0\n\t"
    "ds_read_b64_tr_b16 v[130:131], %1 offset:512\n\t"
    "ds_read_b64_tr_b16 v[132:133], %2 offset:0\n\t"
    "ds_read_b64_tr_b16 v[134:135], %2 offset:512\n\t"
    "ds_read_b64_tr_b16 v[136:137], %1 offset:4096\n\t"
    "ds_read_b64_tr_b16 v[138:139], %1 offset:4608\n\t"
    "ds_read_b64_tr_b16 v[140:141], %2 offset:4096\n\t"
    "ds_read_b64_tr_b16 v[142:143], %2 offset:4608\n\t"
    "s_waitcnt lgkmcnt(0)\n\t"
    "ds_read_b64_tr_b16 v[144:145], %1 offset:16384\n\t"
    "ds_read_b64_tr_b16 v[146:147], %1 offset:16896\n\t"
    "ds_read_b64_tr_b16 v[148:149], %2 offset:16384\n\t"
    "ds_read_b64_tr_b16 v[150:151], %2 offset:16896\n\t"
    "ds_read_b64_tr_b16 v[152:153], %1 offset:20480\n\t"
    "ds_read_b64_tr_b16 v[154:155], %1 offset:20992\n\t"
    "ds_read_b64_tr_b16 v[156:157], %2 offset:20480\n\t"
    "ds_read_b64_tr_b16 v[158:159], %2 offset:20992\n\t"
    "s_waitcnt lgkmcnt(0)"
    : "=&{v[128:159]}"(w) : "v"(a0), "v"(a1) : "memory");
}
template <> __device__ __forceinline__ void gw_load<1>(u32x32& w, unsigned a0, unsigned a1) {
  asm volatile("ds_read_b64_tr_b16 v[160:161], %1 offset:0\n\t"
    "ds_read_b64_tr_b16 v[162:163], %1 offset:512\n\t"
    "ds_read_b64_tr_b16 v[164:165], %2 offset:0\n\t"
    "ds_read_b64_tr_b16 v[166:167], %2 offset:512\n\t"
    "ds_read_b64_tr_b16 v[168:169], %1 offset:4096\n\t"
    "ds_read_b64_tr_b16 v[170:171], %1 offset:4608\n\t"
    "ds_read_b64_tr_b16 v[172:173], %2 offset:4096\n\t"
    "ds_read_b64_tr_b16 v[174:175], %2 offset:4608\n\t"
    "s_waitcnt lgkmcnt(0)\n\t"
    "ds_read_b64_tr_b16 v[176:177], %1 offset:16384\n\t"
    "ds_read_b64_tr_b16 v[178:179], %1 offset:16896\n\t"
    "ds_read_b64_tr_b16 v[180:181], %2 offset:16384\n\t"
    "ds_read_b64_tr_b16 v[182:183], %2 offset:16896\n\t"
    "ds_read_b64_tr_b16 v[184:185], %1 offset:20480\n\t"
    "ds_read_b64_tr_b16 v[186:187], %1 offset:20992\n\t"
    "ds_read_b64_tr_b16 v[188:189], %2 offset:20480\n\t"
    "ds_read_b64_tr_b16 v[190:191], %2 offset:20992\n\t"
    "s_waitcnt lgkmcnt(0)"
    : "=&{v[160:191]}"(w) : "v"(a0), "v"(a1) : "memory");
}
template <> __device__ __forceinline__ void gw_load<2>(u32x32& w, unsigned a0, unsigned a1) {
  asm volatile("ds_read_b64_tr_b16 v[192:193], %1 offset:0\n\t"
    "ds_read_b64_tr_b16 v[194:195], %1 offset:512\n\t"
    "ds_read_b64_tr_b16 v[196:197], %2 offset:0\n\t"
    "ds_read_b64_tr_b16 v[198:199], %2 offset:512\n\t"
    "ds_read_b64_tr_b16 v[200:201], %1 offset:4096\n\t"
    "ds_read_b64_tr_b16 v[202:203], %1 offset:4608\n\t"
    "ds_read_b64_tr_b16 v[204:205], %2 offset:4096\n\t"
    "ds_read_b64_tr_b16 v[206:207], %2 offset:4608\n\t"
    "s_waitcnt lgkmcnt(0)\n\t"
    "ds_read_b64_tr_b16 v[208:209], %1 offset:16384\n\t"
    "ds_read_b64_tr_b16 v[210:211], %1 offset:16896\n\t"
    "ds_read_b64_tr_b16 v[212:213], %2 offset:16384\n\t"
    "ds_read_b64_tr_b16 v[214:215], %2 offset:16896\n\t"
    "ds_read_b64_tr_b16 v[216:217], %1 offset:20480\n\t"
    "ds_read_b64_tr_b16 v[218:219], %1 offset:20992\n\t"
    "ds_read_b64_tr_b16 v[220:221], %2 offset:20480\n\t"
    "ds_read_b64_tr_b16 v[222:223], %2 offset:20992\n\t"
    "s_waitcnt lgkmcnt(0)"
    : "=&{v[192:223]}"(w) : "v"(a0), "v"(a1) : "memory");
}
template <> __device__ __forceinline__ void gw_load<3>(u32x32& w, unsigned a0, unsigned a1) {
  asm volatile("ds_read_b64_tr_b16 v[224:225], %1 offset:0\n\t"
    "ds_read_b64_tr_b16 v[226:227], %1 offset:512\n\t"
    "ds_read_b64_tr_b16 v[228:229], %2 offset:0\n\t"
    "ds_read_b64_tr_b16 v[230:231], %2 offset:512\n\t"
    "ds_read_b64_tr_b16 v[232:233], %1 offset:4096\n\t"
    "ds_read_b64_tr_b16 v[234:235], %1 offset:4608\n\t"
    "ds_read_b64_tr_b16 v[236:237], %2 offset:4096\n\t"
    "ds_read_b64_tr_b16 v[238:239], %2 offset:4608\n\t"
    "s_waitcnt lgkmcnt(0)\n\t"
    "ds_read_b64_tr_b16 v[240:241], %1 offset:16384\n\t"
    "ds_read_b64_tr_b16 v[242:243], %1 offset:16896\n\t"
    "ds_read_b64_tr_b16 v[244:245], %2 offset:16384\n\t"
    "ds_read_b64_tr_b16 v[246:247], %2 offset:16896\n\t"
    "ds_read_b64_tr_b16 v[248:249], %1 offset:20480\n\t"
    "ds_read_b64_tr_b16 v[250:251], %1 offset:20992\n\t"
    "ds_read_b64_tr_b16 v[252:253], %2 offset:20480\n\t"
    "ds_read_b64_tr_b16 v[254:255], %2 offset:20992\n\t"
    "s_waitcnt lgkmcnt(0)"
    : "=&{v[224:255]}"(w) : "v"(a0), "v"(a1) : "memory");
}
template <int I, int E, class F> __device__ __forceinline__ void sfor(F&& f) { if constexpr (I < E) { f(std::integral_constant<int, I>{}); sfor<I + 1, E>(f); } }
template <int ROLE, int KQ, class F>
__device__ __forceinline__ void wload(char* smem, int wave_id_, const u16* W0, const u16* W1, int ldb, u32x32 (&bw)[4], int rot, F&& hook) {
  constexpr int ABUF = 65536;
  const int tid = opaque_tid(), lane = tid & 63, wave = WAVE_ID;
  const int kq = KQ == 2 ? wave >> 2 : 0, nq = KQ == 2 ? wave & 3 : wave;
  const int g = lane >> 4, r16 = lane & 15, q4 = r16 >> 2, p4 = lane & 3;
  const unsigned ldsb = __builtin_amdgcn_readfirstlane((unsigned)(size_t)(LAS char*)smem);
  {
    const int srow = 8 * wave + (lane >> 3), ch = (lane & 7) ^ bswz(srow);
    const unsigned voff = (unsigned)(srow * ldb + ch * 8) * 2;
    const char* w0 = uni((const char*)W0); const char* w1 = uni((const char*)(ROLE == R_F1 ? W1 : W0 + 64));
    const size_t kb = (size_t)64 * ldb * 2;
    auto wstage = [&](int r) {
      const unsigned ld = ldsb + (r & 1) * ABUF + wave * 1024;
#pragma unroll
      for (int i = 0; i < 8; ++i) {
        const int kbi = KQ == 2 ? (ROLE == R_PART2 ? 0 : (i >> 2) * 8) + ((2 * r + ((i >> 1) & 1)) ^ rot) : 2 * r + (i >> 2);
        const char* src = KQ == 2 ? ((i & 1) ? w1 : w0) + kbi * kb + (ROLE == R_PART2 ? (i >> 2) * 256 : 0) : w0 + kbi * kb + (i & 3) * 128;
        unsigned keep;
        asm volatile("s_mov_b32 %0, m0\n\ts_mov_b32 m0, %3\n\ts_nop 2\n\tglobal_load_lds_dwordx4 %1, %2" WNT_ "\n\ts_mov_b32 m0, %0"
                     : "=&s"(keep) : "v"(voff), "s"(src), "s"(ld + i * 8192) : "memory");
      }
    };
    const int brow = 8 * g + q4, bs = bswz(brow);
    unsigned la[2];
#pragma unroll
    for (int j = 0; j < 2; ++j) {
      int blk, c16, sub;
      if (ROLE == R_F1) { blk = j; c16 = nq * 2 + (p4 >> 1); sub = (p4 & 1) * 8; }
      else { blk = nq >> 1; c16 = (nq & 1) * 4 + p4; sub = j * 8; }
      int set = kq;
      if (ROLE == R_PART2 && TLP) { set = nq >> 1; blk = nq & 1; c16 = kq * 4 + p4; }
      la[j] = (unsigned)((KQ == 2 ? set * 4 + blk : blk) * 8192 + brow * 128 + ((c16 ^ bs) << 4) + sub);
    }
    hook(4);
    wstage(0); wstage(1);
    sfor<0, 4>([&](auto r_c) __attribute__((always_inline)) {
      constexpr int r = decltype(r_c)::value;
      if (r < 3) WAIT_V(8); else WAIT_V(0);
      __builtin_amdgcn_s_barrier(); asm volatile("" ::: "memory");
      gw_load<r>(bw[r], ldsb + (r & 1) * ABUF + la[0], ldsb + (r & 1) * ABUF + la[1]);
      BAR();
      if (r + 2 < 4) wstage(r + 2);
      hook(r);
    });
  }
}
#define DSR2(d0, d1, a0_, a1_, off) asm volatile("ds_read_b128 %0, %2 offset:%4\n\tds_read_b128 %1, %3 offset:%4" : "=&v"(d0), "=&v"(d1) : "v"(a0_), "v"(a1_), "i"(off))
template <int N> __device__ __forceinline__ void waitl(u32x4& a, u32x4& b) { asm volatile("s_waitcnt lgkmcnt(%2)" : "+v"(a), "+v"(b) : "i"(N)); }
__device__ __forceinline__ void glds1(const char* sbase, unsigned voff, unsigned lds) {
  unsigned keep;
  asm volatile("s_mov_b32 %0, m0\n\ts_mov_b32 m0, %3\n\ts_nop 2\n\tglobal_load_lds_dwordx4 %1, %2 sc1\n\ts_mov_b32 m0, %0"
               : "=&s"(keep) : "v"(voff), "s"(sbase), "s"(lds) : "memory");
}
#define XS(i) do { } while (0)
#define XFL(b_) do { } while (0)
#define XA_ 0
#define XF_ 0
#define XE_ 0
#define XP_ 0
#define PUBK 9
#define LATE 0
#define NOWP 0
#define TB 48
#define TPUB 1
#define DUR0 160
#define PUP 4
#define PDQ 5
#define PDM 2
#define PDEEP 5
template <int ROLE>
__device__ __forceinline__ void gemm7(char* smem, int wave_id_, const u16* W0, const u16* W1, int ldb, const char* A_, size_t a_blk, Dep dep,
                                      char* C_, size_t c_blk, unsigned*& cnt, int myw, unsigned* tmo, int slot, unsigned long long* mts,
                                      bool gl, int nimm = 0, int nseg = NSEG, int wm0 = 0, int pofs = 0, u64 tag = 0) {
  dep.base = (const char*)cnt;
  unsigned* myprog = cnt + myw;
  volatile LAS unsigned* wsl = (volatile LAS unsigned*)(smem + L_SLOT + 512);
  if (opaque_tid() < 144) *(volatile LAS unsigned*)(smem + L_SLOT + opaque_tid() * 4) = 0u;
  const int tid = opaque_tid(), lane = tid & 63, wave = WAVE_ID;
  const int kq = wave >> 2, nq = wave & 3;
  const unsigned ldsb = __builtin_amdgcn_readfirstlane((unsigned)(size_t)(LAS char*)smem);
  constexpr int rot = 0;
  const char* A = uni(A_) + nq * 1024;
  const char* Cu = uni(C_);
  constexpr int PD = PDM;
  constexpr bool P2C = ROLE == R_PART2;
  int pw = 0;
  int wm = wm0, nh = 0;
  constexpr bool SPEC = ROLE != R_QKV;
  auto lgo = [&](int b, unsigned& v, unsigned& u, bool pre, bool force) {
    if (wave == 4) {
      for (int spins = 0; __builtin_amdgcn_readfirstlane(wsl[2]) != (unsigned)(b + 1); ++spins) {
        if (!pre) ld_fb(v, u, dep.base, dep.voff);
        const bool ok = (int)(v * dep.mul + dep.add) > b;
        if (__all(ok)) break;
        if (SPEC && !force && bells(ok, u, b, MARG)) break;
        if (!pre) __builtin_amdgcn_s_sleep(2);
        pre = false;
        if (give_up(spins, tmo, 9)) { v = 255u; break; }
      }
      wsl[2] = (unsigned)(b + 1);
    } else {
      while (__builtin_amdgcn_readfirstlane(wsl[2]) != (unsigned)(b + 1)) __builtin_amdgcn_s_sleep(1);
    }
  };
  auto lwm = [&](int b, unsigned v) {
    unsigned w;
    if (wave == 4) {
      const int w2 = (int)dep_eval(v, dep);
      w = (unsigned)min(max(max(w2, wm), b + 1), 255);
      wsl[0] = ((unsigned)(b + 1) << 8) | w;
    } else {
      for (;;) {
        w = __builtin_amdgcn_readfirstlane(wsl[0]);
        if ((w >> 8) == (unsigned)(b + 1)) break;
        __builtin_amdgcn_s_sleep(1);
      }
      w &= 255u;
    }
    return (int)w;
  };
  auto pubw = [&](int n) {
    WAIT_V(0);
    if (lane == 0) {
      const unsigned o = __hip_atomic_fetch_add((LAS unsigned*)(smem + L_SLOT + 516 + ((n + pofs) & 1) * 36), 1u, __ATOMIC_RELAXED, __HIP_MEMORY_SCOPE_WORKGROUP);
      if ((o & 3u) == 3u) {
        st4_sc1(myprog, (unsigned)(n + pofs));
      }
    }
    asm volatile("s_waitcnt lgkmcnt(0)" ::: "memory");
  };
  auto hsrc = [&](int hh) { return A + (size_t)(hh >> 1) * a_blk + ((hh ^ (rot >> 2)) & 1) * 16384; };
  auto hlds = [&](int hh) { return ldsb + (unsigned)(((hh >> 1) & 1) * 65536 + (hh & 1) * 16384 + nq * 1024); };
#define POFF(i_) (((i_) & 1) * 32768 + ((i_) >> 1) * 4096)
#define PIECE(s_, l_, i_) asm volatile("s_add_u32 m0, %2, %3\n\ts_nop 0\n\tglobal_load_lds_dwordx4 %0, %1 sc1" :: "v"(av[i_]), "s"(s_), "s"(l_), "i"(POFF(i_)) : "memory", "scc", "m0")
  int na = SB;
  auto arm = [&](int bb) {
    unsigned z = SENT; asm volatile("" : "+v"(z));
    constexpr int N = ROLE == R_F1 ? 1 : (P2C ? 4 : 2);
    const unsigned o = nq * N * 1024 + lane_id() * 16;
    const char* p = Cu + (size_t)bb * c_blk;
#pragma unroll
    for (int i = 0; i < N; ++i) st16s(p, o + i * 1024, u32x4{z, z, z, z});
  };
  u32x32 BW[4];
  wload<ROLE, 2>(smem, wave_id_, W0, W1, ldb, BW, rot, [&](int r) {
    if (ROLE == R_QKV && nimm > 0 && wave == 0) STS((char*)tmo - OFF_TMO, 128 + slot, 3 + r);
    if (SPEC && r == 2 && (ROLE != R_PART || gl)) { for (na += kq; na < NSEG; na += 2) arm(na); na = NSEG; }
    if (ROLE == R_QKV && nimm > 0 && r == 0 && wave == 4) init_issue((char*)tmo - OFF_TMO, ldsb + L_RED);
    if (ROLE == R_QKV && nimm > 0 && r == 2) {
      cnt = cnt_of((char*)tmo - OFF_TMO, init_get((char*)tmo - OFF_TMO, tag, smem + L_RED));
      dep.base = (const char*)cnt; myprog = cnt + myw;
      if (kq) {
        unsigned v0 = 0, u0;
        lgo(0, v0, u0, false, true);
        wm = lwm(0, v0);
        const unsigned l16 = lane_id() * 16;
#pragma unroll
        for (int i = 0; i < 16; ++i) glds1(hsrc(i >> 3) + (i & 1) * 32768 + ((((i & 7) >> 1) ^ rot) & 3) * 4096, l16, hlds(i >> 3) + POFF(i & 7));
      }
      nh = 2;
    }
  });
  if (ROLE == R_QKV && nimm > 0 && !kq) wm = (int)(__builtin_amdgcn_readfirstlane(wsl[0]) & 255u);
  if (ROLE == R_QKV && nimm > 0 && opaque_tid() == 0) STS((char*)tmo - OFF_TMO, 128 + slot, 0);
  if (wave == 0) WFB();
  auto body = [&](auto ldr_c) __attribute__((always_inline)) {
    constexpr bool LDR = decltype(ldr_c)::value;
    if (XP_ == 0 ? !LDR : (XP_ == 1 ? LDR : false)) __builtin_amdgcn_s_setprio(1);
    unsigned av[8];
#pragma unroll
    for (int i = 0; i < 8; ++i) av[i] = lane * 16 + (P2C ? 0 : (i & 1) * 32768) + (((i >> 1) ^ rot) & 3) * 4096;
    auto issue_all = [&](int hh) {
      if (LDR) {
        const char* s_ = hsrc(hh); const unsigned l_ = hlds(hh);
#pragma unroll
        for (int i = 0; i < 8; ++i) if (!(P2C && (i & 1))) PIECE(s_, l_, i);
      }
    };
    int b = 0;
    bool pre = false;
    unsigned smu = 0, tk = 0;
    unsigned smp = 0, got = 0;
    while (b < nseg) {
      const bool fast = (wm < nseg ? wm : nseg) >= b + PD && b >= nimm && nh <= 2 * b + 2;
      if (SPEC && b >= TB && !fast) break;
      const bool pl = wm <= b || pre, cold = nh < 2 * b + 2, nh3 = nh == 2 * b + 3;
      if (!fast) {
        XS(0);
        if (LDR && wm <= b && !got) lgo(b, smp, smu, false, true);
        XS(1);
        if (got) nh += NEARLY / 8;
        { const int upto = nh3 ? 2 * b + 4 : 2 * b + 2; for (; nh < upto; ++nh) issue_all(nh); }
        if (LDR && pl) wm = lwm(b, smp);
        XS(2);
      }
      const int ln = lane_id(), g = ln >> 4, r16 = ln & 15;
      const unsigned e0 = ldsb + (unsigned)((P2C ? 0 : kq * 32768) + (b & 1) * 65536 + (gl ? (g >> 1) * 512 + r16 * 32 + (g & 1) * 16 : r16 * 128 + ((g ^ (r16 & 7)) << 4)));
      const unsigned d0 = e0 ^ (gl ? 1024u : 64u);
      const unsigned rw = ldsb + L_RED + nq * 4096 + ln * 16;
      const unsigned co0 = ROLE == R_F1 ? (unsigned)((g & 1) * 2048 + nq * 512 + r16 * 32 + (g >> 1) * 16)
                         : ((ROLE == R_QKV ? TLQ : TLP) ? (unsigned)(nq * (P2C ? 4096 : 2048) + r16 * 64 + g * 16)
                         : (ROLE == R_QKV ? (unsigned)(r16 * 256 + (((4 * nq + g) ^ r16) << 4)) : (unsigned)(r16 * PROW + (32 * nq + 8 * g) * 2)));
      const unsigned co1 = co0 + ((ROLE == R_QKV ? TLQ : TLP) ? 1024u : (ROLE == R_QKV ? 4096u : 16u * PROW));
      u32x16 pv_;
      bool pr2 = false;

      if (fast) {
        __builtin_amdgcn_s_barrier(); asm volatile("" ::: "memory");
        const int LA = (wm < nseg ? wm : nseg) >= b + (ROLE == R_QKV ? PDQ : PDEEP) ? 3 : 2;
        for (; nh < 2 * b + LA; ++nh) issue_all(nh);
        int lf = (wm < nseg ? wm : nseg) - (LA - 1), cp = nseg - (LA - 1);
        const unsigned sa = ldsb + L_SLOT + ln * 4, ad = dep.add - (unsigned)(LA - 1);
        const unsigned up = LA == 3 ? 1000u : (unsigned)PUP;
#define FARGS BW, pv_, e0, d0, b, lf, cp, up, sa, dep.mul, ad, rw, (unsigned)a_blk, hsrc(2 * b + LA), hsrc(2 * b + LA + 1), hlds(2 * b + LA), hlds(2 * b + LA + 1), av
        if constexpr (LDR) (P2C ? fast_ld2 : fast_ld0)(FARGS, LA);
#undef FARGS
        else {
          const char* c0_ = uni((const char*)tmo - OFF_TMO + OFF_DUMP); const char* c1_ = Cu + (size_t)b * c_blk;
          const int sf = b + pofs;
          const char* pg = uni((const char*)myprog);
          (ROLE == R_F1 ? fast_st1 : (P2C ? fast_st2 : fast_st0))(BW, pv_, e0, d0, b, lf, cp, up, sa, dep.mul, ad, rw, (unsigned)c_blk, c0_, c1_, co0, co1, pofs, sf, (unsigned)wave,
                                                                   ldsb + L_SLOT, dep.voff, dep.base, pg);
        }
        wm = lf + LA - 1 > wm ? lf + LA - 1 : wm;
        nh = 2 * b + 2;
        --b;
        __builtin_amdgcn_s_barrier(); asm volatile("" ::: "memory");
      } else {
        if (LDR) { if (nh >= 2 * b + 4) { if (P2C) WAIT_V(14); else WAIT_V(28); } else { if (P2C) WAIT_V(6); else WAIT_V(12); } }
        BAR();
        XS(3);
        if (ROLE == R_QKV && nimm > 0 && b < 2 && opaque_tid() == 0) STS((char*)tmo - OFF_TMO, 128 + slot, 1 + b);
        if constexpr (!LDR) {
          const unsigned wv = wsl[0];
          unsigned st;
          lean_st0(BW, e0, d0, __builtin_amdgcn_readfirstlane(pw > 0 ? (cold ? 4u : 1u) : 0u), pv_, (unsigned)(pw + pofs), ldsb + L_SLOT + 516, uni((const char*)myprog), 0u, tk, st);
          if (pl) wm = (int)(__builtin_amdgcn_readfirstlane(wv) & 255u);
        }
        const bool dm = nh == 2 * b + 2 && wm > b + 1 && b + 1 < nseg;
        pr2 = b + 1 < nseg && wm < nseg;
        if constexpr (LDR) {
          const bool er = nh == 2 * b + 2 && !dm;
          const unsigned ps = pr2 && (wave == 4 || er) ? (unsigned)(b + 1) : 0u;
#define LARGS BW, e0, d0, (unsigned)(nh - 2 * b - 2), hlds(nh), hsrc(nh), hsrc(nh + 1), av, ps, dep.voff, dep.base, \
              rw, ldsb + L_SLOT + 528 + nq * 4, (unsigned)(b + 1), ldsb + L_SLOT + 520, dep.mul, dep.add, er ? b + 1 : 0x7FFFFFFF, 0x7FFFFFFF, smp, smu, got
          if (dm) (P2C ? lean_dm1 : lean_dm0)(LARGS); else (P2C ? lean_ld1 : lean_ld0)(LARGS);
#undef LARGS
          got = __builtin_amdgcn_readfirstlane(got);
        }
        if (dm) nh += 2;
        XS(5);
      }
      unsigned bad = 0, pk = 0;
      if constexpr (LDR) { if (fast) fin_ld(pv_, rw, ldsb + L_SLOT + 528 + nq * 4, (unsigned)(b + 1)); }
      else (ROLE == R_F1 ? fin_st1 : (P2C ? fin_st2 : fin_st0))(pv_, rw, ldsb + L_SLOT + 528 + nq * 4, (unsigned)(b + 1), Cu + (size_t)b * c_blk, co0, co1, ldsb + L_SLOT + 524, bad, pk);
      XS(6);
      pre = pr2;
      bool now = (nh < 2 * b + 4 && wm < b + 1 + PD) || b + 1 >= nseg || b < nimm;
      if (!LDR && now) {
        if (NOWP == 0 && b >= nimm && pk == (unsigned)(b + 2)) now = false;
        else pubw(b + 1);
      }
      XS(7);
      XFL(b);
      pw = now ? 0 : b + 1;
      if (SPEC && !LDR && !fast && b >= 16 && na < NSEG && na > b + 1) { arm(na); arm(na + 1); na += 2; }
      ++b;
    }
    if (SPEC && b < nseg) {
      static_assert(!SPEC || (NEARLY == 16 && TLP), "");
      const int ln = lane_id(), g = ln >> 4, r16 = ln & 15;
      const unsigned e0 = ldsb + (unsigned)((P2C ? 0 : kq * 32768) + (gl ? (g >> 1) * 512 + r16 * 32 + (g & 1) * 16 : r16 * 128 + ((g ^ (r16 & 7)) << 4)));
      const unsigned rw = ldsb + L_RED + nq * 4096 + ln * 16;
      const unsigned co0 = ROLE == R_F1 ? (unsigned)((g & 1) * 2048 + nq * 512 + r16 * 32 + (g >> 1) * 16) : (unsigned)(nq * (P2C ? 4096 : 2048) + r16 * 64 + g * 16);
      const char* pg = uni((const char*)myprog);
      bool force = false;
      unsigned dur = DUR0, dp = 0, x8 = __builtin_amdgcn_readfirstlane(wave == 0 ? 8u : 0u);
      if (nh >= 2 * b + 2) got = 1;
      pre = false;
      for (;;) {
        unsigned rd;
        XS(0);
        if (LDR && b < nseg) {
          if (!got) { lgo(b, smp, smu, pre, force); issue_all(2 * b); issue_all(2 * b + 1); }
          XS(2);
          if (P2C) WAIT_V(6); else WAIT_V(12);
        }
        if (!LDR && b >= SB && na <= NSEG) { for (na = max(na, b); na < NSEG; ++na) arm(na); WAIT_V(0); ++na; }
        const unsigned ea = e0 + (b & 1) * 65536;
        BAR();
        XS(3);
        if (b >= nseg) rd = __builtin_amdgcn_readfirstlane(wsl[8]);
        else if constexpr (LDR) {
          const int th = b + 1 < nseg ? b + 1 : 0;
          (P2C ? lean_ld1 : lean_ld0)(BW, ea, ea ^ (gl ? 1024u : 64u), 0u, hlds(2 * b + 2), hsrc(2 * b + 2), hsrc(2 * b + 3), av, (unsigned)th, dep.voff, dep.base,
                                      rw, ldsb + L_SLOT + 528 + nq * 4, (unsigned)(b + 1), ldsb + L_SLOT + 520, dep.mul, dep.add, th, th >= SB ? th : 0x7FFFFFFF, smp, smu, got);
          got = __builtin_amdgcn_readfirstlane(got);
          rd = got >> 1;
          pre = true;
        } else {
          u32x16 pv_;
          lean_st0(BW, ea, ea ^ (gl ? 1024u : 64u), b >= SB - 2 ? (unsigned)min(pw, 1) | x8 : (unsigned)min(pw, 1) * TPUB, pv_, (unsigned)pw, ldsb + L_SLOT + 516, pg, (unsigned)(b + 1) << 24 | dur, tk, rd);
          rd = __builtin_amdgcn_readfirstlane(rd);
          XS(5);
          if (!rd) {
            unsigned bad, pk;
            (ROLE == R_F1 ? fin_st1 : (P2C ? fin_st2 : fin_st0))(pv_, rw, ldsb + L_SLOT + 528 + nq * 4, (unsigned)(b + 1), Cu + (size_t)b * c_blk, co0, co0 + 1024u,
                                                                 ldsb + L_SLOT + 524, bad, pk);
            XS(6);
            pw = b + 1;
            if (x8 && b >= SB - 2) { const unsigned d = min(rt_now() - __builtin_amdgcn_readfirstlane(tk), 300u); dur = max(d, dp); dp = d; }
            if (bad && !force && b >= SB) wsl[8] = 1u;
            else if (b == SB - 1 || (pk != (unsigned)(b + 2) && __builtin_amdgcn_readfirstlane(wsl[2]) != (unsigned)(b + 2))) {
              pubw(b + 1);
              pw = 0;
            }
            if (na < NSEG && na > b + 1) { arm(na); arm(na + 1); na += 2; }
          }
        }
        XS(7);
        if (rd) {
          WAIT_V(0);
          BAR();
          if (wave == 4 && ln < 11) wsl[ln] = 0u;
          BAR();
          --b; pw = 0; pre = false; got = 0; force = true;
          continue;
        }
        if (b >= nseg) break;
        force = false;
        ++b;
      }
    }
    __builtin_amdgcn_s_setprio(0);
  };
  if (kq) body(std::true_type{}); else body(std::false_type{});
#undef PIECE
#undef POFF
  WAIT_V(0);
  BAR();
}
__device__ __forceinline__ void fin_role(char* smem, int wave_id_, int fidx, const float* mod, const char* P2, const u16* x1, u16* out, unsigned* cnt, unsigned* tmo,
                                         unsigned long long* mts) {
  const int tid = opaque_tid(), lane = tid & 63, wave = WAVE_ID;
  row_table(smem + L_GB, mod, cnt, tmo, tid);
  volatile LAS unsigned* wsl = (volatile LAS unsigned*)(smem + L_SLOT + 512);
  int wf = 0, par = 0;
  const unsigned pf2 = (unsigned)(PF2 + ((lane & 31) < 28 ? (lane & 31) : 0)) * 128, pln = (unsigned)((PLN + (lane >> 3)) * 128 + (lane & 7) * 4);
  for (int nf = fidx >> 3; nf < NSEG; nf += 2) {
    if (wf <= nf) {
      if (wave == 0) {
        unsigned w;
        for (int spins = 0;; ++spins) {
          unsigned v, u;
          ld_fb(v, u, cnt, pf2);
          if (!__all(v > (unsigned)nf) && bells(v > (unsigned)nf, u, nf, MARF)) { w = 0; break; }
          v = rowmin16(v);
          w = min(rdl(v, 0), rdl(v, 16));
          if ((int)w > nf) break;
          __builtin_amdgcn_s_sleep(2);
          if (give_up(spins, tmo, 9)) { w = NSEG; break; }
        }
        if (lane == 0) wsl[par] = w;
      }
      BAR();
      wf = (int)__builtin_amdgcn_readfirstlane(wsl[par]);
      par ^= 1;
    }
    int ln = lane; asm volatile("" : "+v"(ln));
    if (wave == 0) WTS(256 + 8 + fidx, nf / 2, 0);
    bool fin = nf < SB;
    for (int spins = 0; !fin_half(fin, P2, x1, smem + L_GB + 12288, out, nf * 32 + 4 * (fidx & 7) + (wave >> 1), wave & 1, ln); ++spins)
      fin = __all(ld4_sc1((const char*)cnt + pf2) > (unsigned)nf && ld4_sc1((const char*)cnt + pln) > (unsigned)nf) || give_up(spins, tmo, 9);
    if (wave == 0) WTS(256 + 8 + fidx, nf / 2, 2);
  }
}
#define LD8(r, vo, sb) asm volatile("global_load_dwordx4 %0, %8, %12 sc1\n\tglobal_load_dwordx4 %1, %9, %12 sc1\n\t" \
    "global_load_dwordx4 %2, %10, %12 sc1\n\tglobal_load_dwordx4 %3, %11, %12 sc1\n\t" \
    "global_load_dwordx4 %4, %8, %13 sc1\n\tglobal_load_dwordx4 %5, %9, %13 sc1\n\t" \
    "global_load_dwordx4 %6, %10, %13 sc1\n\tglobal_load_dwordx4 %7, %11, %13 sc1" \
    : "=&v"(r[0][0]), "=&v"(r[0][1]), "=&v"(r[0][2]), "=&v"(r[0][3]), "=&v"(r[1][0]), "=&v"(r[1][1]), "=&v"(r[1][2]), "=&v"(r[1][3]) \
    : "v"(vo), "v"(vo ^ (TLQ ? 2048u : 64u)), "v"(vo ^ (TLQ ? 4096u : 128u)), "v"(vo ^ (TLQ ? 6144u : 192u)), "s"(sb), "s"((sb) + (TLQ ? 1024 : 4096)) : "memory")
#define TIE8(r) "+v"(r[0][0]), "+v"(r[0][1]), "+v"(r[0][2]), "+v"(r[0][3]), "+v"(r[1][0]), "+v"(r[1][1]), "+v"(r[1][2]), "+v"(r[1][3])
#define SADL 255
#define DSA 20
#define SASL 8
__device__ __forceinline__ void sa_role(char* smem, int wave_id_, int hbase, const char* __restrict__ Q, const char* __restrict__ Kx, const char* __restrict__ V,
                                        const char* kvm, char* __restrict__ O, unsigned* cnt, unsigned* tmo, int slot, unsigned long long* mts) {
  constexpr float LS = LOG2E * SCALE;
  const int tid = opaque_tid(), wave = WAVE_ID;
  const int h = hbase + (wave & 1), s0 = wave >> 1;
  char* vl = smem + wave * 17408;
  const unsigned ldsw = __builtin_amdgcn_readfirstlane((unsigned)(size_t)(LAS char*)vl);
  unsigned avail = 0;
  {
    static_assert(SB % 4 == 0, "");
    unsigned z = SENT; asm volatile("" : "+v"(z));
    for (int s = SB + s0; s < NSEG; s += 4)
#pragma unroll
      for (int i = 0; i < 8; ++i) st16s(uni(O + ((size_t)s * 64 + 2 * h) * 4096), (unsigned)(i * 1024 + (tid & 63) * 16), u32x4{z, z, z, z});
  }
  for (int s = s0, it = 1; s < NSEG; s += 4, ++it) {
    u32x4 qv[2][4], kv[2][4];
    int lane = tid & 63; asm volatile("" : "+v"(lane));
    const int g = lane >> 4, r16 = lane & 15, q4 = r16 >> 2, p4 = lane & 3;
    auto request = [&](int sr) {
      const size_t o = ((size_t)sr * NH + h) * 8192;
      const char* qs = uni(Q + o); const char* ks = uni(Kx + o); const char* vs = uni(V + o);
      const unsigned fo = TLQ ? (unsigned)(r16 * 64 + g * 16) : (unsigned)(r16 * 256 + ((g ^ r16) << 4));
      LD8(qv, fo, qs); LD8(kv, fo, ks);
      head_to_lds(vs, lane, ldsw);
    };
    {
      for (int spins = 0; avail <= (unsigned)s; ++spins) {
        const unsigned p = ld4_sc1(cnt + (PQKV + h) * 32 + (lane & 15));
        avail = min(min(qkv_avail(p, 0), qkv_avail(p, 1)), qkv_avail(p, 2));
        if (avail > (unsigned)s) break;
        __builtin_amdgcn_s_sleep(32);
        if (give_up(spins, tmo, 10)) break;
      }
      request(s);
      asm volatile("s_waitcnt vmcnt(0)" : TIE8(qv), TIE8(kv) :: "memory");
    }
    WTM(slot, s, 0);
    f32x4 s00 = f32x4{0, 0, 0, 0}, s01 = s00, s11 = s00;
#pragma unroll
    for (int ks = 0; ks < 4; ++ks) {
      s00 = MFMA32(asbf8(kv[0][ks]), asbf8(qv[0][ks]), s00);
      s01 = MFMA32(asbf8(kv[0][ks]), asbf8(qv[1][ks]), s01);
      s11 = MFMA32(asbf8(kv[1][ks]), asbf8(qv[1][ks]), s11);
    }
    float mx[2], sum[2];
    u32x4 pseg[2];
    {
#pragma unroll
      for (int r = 0; r < 4; ++r) { const bool ok = 4 * g + r <= r16; s00[r] = ok ? s00[r] : -3.0e38f; s11[r] = ok ? s11[r] : -3.0e38f; }
      mx[0] = red16_32_max(max3(max3(s00[0], s00[1], s00[2]), s00[3], -1.0e38f));
      mx[1] = red16_32_max(max3(max3(fmaxf(fmaxf(s01[0], s01[1]), fmaxf(s01[2], s01[3])), s11[0], -1.0e38f), max3(s11[1], s11[2], s11[3]), -1.0e38f));
      const float n0 = -mx[0] * LS, n1 = -mx[1] * LS;
#pragma unroll
      for (int r = 0; r < 4; ++r) {
        s00[r] = __builtin_amdgcn_exp2f(s00[r] * LS + n0); s01[r] = __builtin_amdgcn_exp2f(s01[r] * LS + n1); s11[r] = __builtin_amdgcn_exp2f(s11[r] * LS + n1);
      }
      sum[0] = (s00[0] + s00[1]) + (s00[2] + s00[3]);
      sum[1] = ((s01[0] + s01[1]) + (s01[2] + s01[3])) + ((s11[0] + s11[1]) + (s11[2] + s11[3]));
      pseg[0] = u32x4{pack2(s00[0], s00[1]), pack2(s00[2], s00[3]), 0u, 0u};
      pseg[1] = u32x4{pack2(s01[0], s01[1]), pack2(s01[2], s01[3]), pack2(s11[0], s11[1]), pack2(s11[2], s11[3])};
    }
    f32x4 o[8][2];
#pragma unroll
    for (int dt = 0; dt < 8; ++dt) {
      const int row0 = 4 * g + q4;
      const unsigned xo = (unsigned)(row0 * 256 + (((4 * (dt >> 1) + p4) ^ row0) << 4) + (dt & 1) * 8);
      const u32x2 lo = trread(vl, xo), hi = trread(vl, xo + 4096);
      o[dt][0] = MFMA32(asbf8(u32x4{lo.x, lo.y, 0u, 0u}), asbf8(pseg[0]), (f32x4{0, 0, 0, 0}));
      o[dt][1] = MFMA32(asbf8(u32x4{lo.x, lo.y, hi.x, hi.y}), asbf8(pseg[1]), (f32x4{0, 0, 0, 0}));
    }
    float n0[2]; f32x4 sm[2];
#pragma unroll
    for (int b = 0; b < 2; ++b) { const float t = red16_32_add(sum[b]); sm[b] = f32x4{t, t, t, t}; n0[b] = -mx[b] * LS; }
    unsigned oa = (unsigned)(r16 * 128 + ((g ^ (r16 & 7)) << 4)), ka = (unsigned)(g * 256 + (r16 & 7) * 16), va = (unsigned)lane * 16;
    unsigned one = g < 2 ? 0x3F803F80u : 0u;
    u32x4 kmv[4], vmv[4];
#pragma unroll
    for (int i = 0; i < 4; ++i) vmv[i] = u32x4{0u, 0u, 0u, 0u};
#pragma unroll
    for (int dt = 0; dt < 8; ++dt) asm volatile("" : "+v"(o[dt][0]), "+v"(o[dt][1]), "+v"(vmv[dt >> 1]));
    asm volatile("s_waitcnt lgkmcnt(0)" : "+v"(sm[0]), "+v"(sm[1]), "+v"(n0[0]), "+v"(n0[1]), "+v"(oa), "+v"(ka), "+v"(va), "+v"(one) :: "memory");
    unsigned pg;
    for (int spins = 0;; ++spins) {
      pg = ld4_sc1(cnt + (PROG + h) * 32 + ((lane & 4) ? 5 : (lane & 3)));
      if (__all((pg >> 24) + (s >= SDIR ? 1u : 0u) > (unsigned)s)) break;
      __builtin_amdgcn_s_sleep(SASL);
      if (give_up(spins, tmo, 6)) break;
    }
    if (__all((pg >> 24) == (unsigned)s)) wait_rt(latest(pg), SADL);
    const char* mk = uni(kvm + ((size_t)s * NH + h) * 4096);
    for (int spins = 0;; ++spins) {
      asm volatile("global_load_dwordx4 %0, %8, %10 sc1\n\tglobal_load_dwordx4 %1, %8, %10 offset:128 sc1\n\t"
                   "global_load_dwordx4 %2, %8, %10 offset:1024 sc1\n\tglobal_load_dwordx4 %3, %8, %10 offset:1152 sc1\n\ts_mov_b32 exec_hi, 0\n\t"
                   "global_load_dwordx4 %4, %9, %10 offset:2048 sc1\n\tglobal_load_dwordx4 %5, %9, %10 offset:2560 sc1\n\t"
                   "global_load_dwordx4 %6, %9, %10 offset:3072 sc1\n\tglobal_load_dwordx4 %7, %9, %10 offset:3584 sc1\n\ts_mov_b32 exec_hi, -1\n\ts_waitcnt vmcnt(0)"
                   : "=&v"(kmv[0]), "=&v"(kmv[1]), "=&v"(kmv[2]), "=&v"(kmv[3]), "+v"(vmv[0]), "+v"(vmv[1]), "+v"(vmv[2]), "+v"(vmv[3])
                   : "v"(ka), "v"(va), "s"(mk) : "memory");
      unsigned mx_ = 0;
#pragma unroll
      for (int i = 0; i < 4; ++i) mx_ = max(max(max(max(mx_, kmv[i].x), kmv[i].y), max(kmv[i].z, kmv[i].w)), max(max(vmv[i].x, vmv[i].y), max(vmv[i].z, vmv[i].w)));
      if (__all(mx_ != SENT)) break;
      __builtin_amdgcn_s_sleep(2);
      if (give_up(spins, tmo, 6)) break;
    }
    WTM(slot, s, 1);
    f32x4 tt[2] = {f32x4{0, 0, 0, 0}, f32x4{0, 0, 0, 0}};
#pragma unroll
    for (int ks = 0; ks < 4; ++ks) { tt[0] = MFMA32(asbf8(kmv[ks]), asbf8(qv[0][ks]), tt[0]); tt[1] = MFMA32(asbf8(kmv[ks]), asbf8(qv[1][ks]), tt[1]); }
#pragma unroll
    for (int b = 0; b < 2; ++b)
#pragma unroll
      for (int r = 0; r < 4; ++r) tt[b][r] = tt[b][r] * LS + n0[b];
    if (!__all(max3(max3(tt[0][0], tt[0][1], tt[0][2]), max3(tt[0][3], tt[1][0], tt[1][1]), max2(tt[1][2], tt[1][3])) <= 40.0f)) {
#pragma unroll
      for (int b = 0; b < 2; ++b) {
        float m = max3(max3(tt[b][0], tt[b][1], tt[b][2]), tt[b][3], 0.0f);
        { float x, y; sw16(m, x, y); m = max2(x, y); }
        const float f = __builtin_amdgcn_exp2f(-m);
        tt[b] -= m; sm[b] *= f;
#pragma unroll
        for (int dt = 0; dt < 8; ++dt) o[dt][b] *= f;
      }
    }
    u32x2 pm[2]; float inv[2];
#pragma unroll
    for (int b = 0; b < 2; ++b) {
      pm[b] = u32x2{pack2(__builtin_amdgcn_exp2f(tt[b][0]), __builtin_amdgcn_exp2f(tt[b][1])), pack2(__builtin_amdgcn_exp2f(tt[b][2]), __builtin_amdgcn_exp2f(tt[b][3]))};
      sm[b] = __builtin_amdgcn_mfma_f32_16x16x16bf16_1k(ass4(u32x2{one, one}), ass4(pm[b]), sm[b], 0, 0, 0);
    }
#pragma unroll
    for (int dt = 0; dt < 8; ++dt)
#pragma unroll
      for (int b = 0; b < 2; ++b) o[dt][b] = __builtin_amdgcn_mfma_f32_16x16x16bf16_1k(ass4((dt & 1) ? u32x2{vmv[dt >> 1].z, vmv[dt >> 1].w} : u32x2{vmv[dt >> 1].x, vmv[dt >> 1].y}), ass4(pm[b]), o[dt][b], 0, 0, 0);
    inv[0] = __builtin_amdgcn_rcpf(sm[0][0]); inv[1] = __builtin_amdgcn_rcpf(sm[1][0]);
    const char* od = uni(O + ((size_t)s * 64 + 2 * h) * 4096);
#pragma unroll
    for (int j = 0; j < 4; ++j) {
#pragma unroll
      for (int b = 0; b < 2; ++b) {
        const f32x4 r0 = o[2 * j][b] * inv[b], r1 = o[2 * j + 1][b] * inv[b];
        st16s(od + (j >> 1) * 4096 + b * 2048, oa ^ ((j & 1) * 64u), u32x4{pack2(r0[0], r0[1]), pack2(r0[2], r0[3]), pack2(r1[0], r1[1]), pack2(r1[2], r1[3])});
      }
    }
    if (s >= SB && lane == 0) st4_sc1(cnt + (PSA + (hbase >> 1)) * 32 + 16 + wave, bell(s, rt_now() + DSA));
    WTM(slot, s, 3);
    WAIT_V(0);
    if (lane == 0) st4_sc1(cnt + (PSA + (hbase >> 1)) * 32 + wave, (unsigned)it);
    WTM(slot, s, 2);
  }
}
#undef LD8
#undef TIE8

constexpr int Q_MEM = 0;
constexpr int Q_K   = 16384;
constexpr int Q_V   = 24576;
constexpr int Q_QK  = 32768;
constexpr int Q_TM  = 36864;
constexpr int Q_CB  = 40960;
constexpr int Q_RING = 32768;
constexpr int V_P1  = 0;
constexpr int V_P2  = 16384;
constexpr int NCHAIN = 96;
#define VHN 2
#define ROTH 0
#define NQQ 6
#define NQK 4
#define E_AG 9
#define RDYI -2
#define ROTW 2
#define DQU 6
#define DQM 18
#define DQD 1
#define ORSEL (xg >> 1)
#define D_Q 105
#define D_O 4
#define E_B 6
#define E_O 1900
#define KDEL 5
#define VPOS 0

typedef __bf16 bf16x16 __attribute__((ext_vector_type(16)));
typedef unsigned u32x8 __attribute__((ext_vector_type(8)));
__device__ __forceinline__ f32x4 SMF(u32x4 a, u32x8 b, f32x4 c, int idx) {
  return __builtin_amdgcn_smfmac_f32_16x16x64_bf16(__builtin_bit_cast(bf16x8, a), __builtin_bit_cast(bf16x16, b), c, idx, 0, 0);
}
__device__ __forceinline__ float fold32(float a, float b) {
  const auto p = __builtin_amdgcn_permlane32_swap(__float_as_uint(a), __float_as_uint(b), false, false);
  return __uint_as_float(p[0]) + __uint_as_float(p[1]);
}
__device__ __forceinline__ f32x2 rowsum(f32x4 x) { return f32x2{fold32(x[0], x[2]), fold32(x[1], x[3])}; }
__device__ __forceinline__ unsigned nosent1(unsigned v) { return v < 0xFFFFFFFEu ? v : 0xFFFFFFFEu; }
__device__ __forceinline__ void st4s(const char* sb, unsigned vo, unsigned v) { asm volatile("global_store_dword %0, %1, %2 sc1\n\ts_nop 0" :: "v"(vo), "v"(v), "s"(sb) : "memory"); }
__device__ __forceinline__ void st8s(const char* sb, unsigned vo, u32x2 v) { asm volatile("global_store_dwordx2 %0, %1, %2 sc1\n\ts_nop 1" :: "v"(vo), "v"(v), "s"(sb) : "memory"); }
__device__ __forceinline__ void gldsw(const char* sbase, unsigned voff, unsigned lds) {
  unsigned keep;
  asm volatile("s_mov_b32 %0, m0\n\ts_mov_b32 m0, %3\n\ts_nop 2\n\tglobal_load_lds_dwordx4 %1, %2" CNT_ "\n\ts_mov_b32 m0, %0"
               : "=&s"(keep) : "v"(voff), "s"(sbase), "s"(lds) : "memory");
}
__device__ __forceinline__ void wait_clk(unsigned t0, unsigned d) {
  while ((unsigned)__builtin_amdgcn_s_memtime() - t0 < d) __builtin_amdgcn_s_sleep(1);
}
__device__ __forceinline__ unsigned bell_time(const char* a, unsigned vo, unsigned tag, unsigned cap) {
  unsigned v;
  for (unsigned spins = 0;; ++spins) {
    asm volatile("global_load_dword %0, %1, %2 sc1\n\ts_waitcnt vmcnt(0)" : "=&v"(v) : "v"(vo), "s"(a) : "memory");
    if (__all((v >> 24) >= tag) || spins >= cap) break;
  }
  const unsigned now = (unsigned)__builtin_amdgcn_s_memrealtime() + 250u;
  const unsigned age = rowmin16((now - v) & 0xFFFFFFu);
  return now - min(min(rdl(age, 0), rdl(age, 16)), 500u);
}
#define WBAR() do { __builtin_amdgcn_s_barrier(); asm volatile("" ::: "memory"); } while (0)
#define TN(k, v) do { } while (0)
__device__ __forceinline__ void stage_proj(const char* src, unsigned lds, int tid, int wave) {
  const int row = tid >> 4, f = 2 * ((row & 1) | (((row >> 2) & 1) << 1));
#pragma unroll
  for (int j = 0; j < 4; ++j)
    gldsw(src + (size_t)j * 32 * HD * 2, (unsigned)(row * HD * 2 + (((tid & 15) ^ f ^ ((j & 1) << 3)) << 4)), lds + (wave * 64 + 512 * j) * 16);
}
__device__ __forceinline__ unsigned proj_base(int T, int lane) {
  const int g = lane >> 4, q4 = (lane & 15) >> 2, p4 = lane & 3, fs = 2 * ((q4 & 1) | ((g & 1) << 1) | ((q4 >> 1) << 2));
  return (unsigned)((4 * g + (q4 & 1) + 32 * (q4 >> 1)) * 256 + (((2 * T) ^ fs) << 4) + p4 * 8);
}
__device__ __forceinline__ u32x8 proj_frag(const char* smem, unsigned a) {
  const u32x2 t0 = trread(smem, a), t1 = trread(smem, a + 512), t2 = trread(smem, a + 4096), t3 = trread(smem, a + 4608);
  return u32x8{t0.x, t0.y, t1.x, t1.y, t2.x, t2.y, t3.x, t3.y};
}
__device__ __forceinline__ void flag_frags(unsigned fa, unsigned a, unsigned want, bf16x8 (&f)[4]) {
  u32x2 t[8];
  for (;;) {
    u32x4 fl;
    asm volatile("ds_read_b128 %0, %9\n\t"
                 "ds_read_b64_tr_b16 %1, %10\n\tds_read_b64_tr_b16 %2, %10 offset:64\n\t"
                 "ds_read_b64_tr_b16 %3, %10 offset:512\n\tds_read_b64_tr_b16 %4, %10 offset:576\n\t"
                 "ds_read_b64_tr_b16 %5, %10 offset:1024\n\tds_read_b64_tr_b16 %6, %10 offset:1088\n\t"
                 "ds_read_b64_tr_b16 %7, %10 offset:1536\n\tds_read_b64_tr_b16 %8, %10 offset:1600\n\ts_waitcnt lgkmcnt(0)"
                 : "=&v"(fl), "=&v"(t[0]), "=&v"(t[1]), "=&v"(t[2]), "=&v"(t[3]), "=&v"(t[4]), "=&v"(t[5]), "=&v"(t[6]), "=&v"(t[7])
                 : "v"(fa), "v"(a) : "memory");
    if (__builtin_amdgcn_readfirstlane(min(min(fl.x, fl.y), min(fl.z, fl.w))) >= want) break;
  }
#pragma unroll
  for (int ks = 0; ks < 4; ++ks) f[ks] = asbf8(u32x4{t[2 * ks].x, t[2 * ks].y, t[2 * ks + 1].x, t[2 * ks + 1].y});
}
__device__ __forceinline__ void wait_flags(const char* p, unsigned want) {
  for (;;) {
    const u32x4 fl = *(volatile LAS u32x4*)p;
    if (__builtin_amdgcn_readfirstlane(min(min(fl.x, fl.y), min(fl.z, fl.w))) >= want) break;
  }
}
__device__ __forceinline__ f32x4 zero4() { float z = 0.0f; asm volatile("" : "+v"(z)); return f32x4{z, z, z, z}; }
__device__ __forceinline__ unsigned lds_word(const char* p) { return __builtin_amdgcn_readfirstlane(*(volatile LAS unsigned*)p); }

#define MFMA16(a, b, c) __builtin_amdgcn_mfma_f32_16x16x16bf16_1k((a), (b), (c), 0, 0, 0)
#define UF(x) __uint_as_float(x)
__device__ __forceinline__ unsigned umax3(unsigned a, unsigned b, unsigned c) { return max(max(a, b), c); }
__device__ __forceinline__ f32x2 logit2(float a, float b) {
  return f32x2{a, b};
}
constexpr float LSC = LOG2E * SCALE;

struct QKW { u32x32 w0, w1, w2, w3, w4, w5; u32x8 x; };
template <int W_> __device__ __forceinline__ void qk_step(const QKW& W, const char* ag, const char* vs, const char* od, const char* kv, unsigned& cap, unsigned& sp,
                                                          unsigned long long& tc, unsigned long long* ts, unsigned& d0, unsigned& d1);
template <> __device__ __forceinline__ void qk_step<0>(const QKW& W, const char* ag, const char* vs, const char* od, const char* kv, unsigned& cap, unsigned& sp,
                                                        unsigned long long& tc, unsigned long long* ts, unsigned& d0, unsigned& d1) {
  unsigned st, bg;
  asm volatile("v_mbcnt_lo_u32_b32 v60, -1, 0\n\t"
    "v_mbcnt_hi_u32_b32 v60, -1, v60\n\t"
    "v_lshlrev_b32 v6, 4, v60\n\t"
    "s_mov_b32 %[sp], 0\n\t"
    "s_setprio 3\n\t"
    "global_load_dwordx4 v[40:43], v6, %[ag] sc1\n\t"
    "global_load_dwordx4 v[44:47], v6, %[ag] offset:1024 sc1\n\t"
    "s_memtime %[tc]\n\t"
    "v_add_u32 v7, 40960, v6\n\t"
    "v_add_u32 v8, 98304, v6\n\t"
    "ds_read_b128 v[24:27], v7 offset:0\n\t"
    "ds_read_b128 v[28:31], v7 offset:1024\n\t"
    "ds_read_b128 v[32:35], v7 offset:8192\n\t"
    "ds_read_b128 v[36:39], v7 offset:9216\n\t"
    "v_lshrrev_b32 v48, 4, v60\n\t"
    "v_and_b32 v49, 15, v60\n\t"
    "v_and_b32 v61, 4, v60\n\t"
    "v_mul_u32_u24 v61, 0x2aaa, v61\n\t"
    "v_lshrrev_b32 v9, 1, v60\n\t"
    "v_and_b32 v9, 2, v9\n\t"
    "v_or3_b32 v9, v9, v61, %[c44]\n\t"
    "v_add_u32 v14, 0, v6\n\t"
    "v_lshlrev_b32 v61, 2, v48\n\t"
    "v_lshl_or_b32 v10, v49, 4, v61\n\t"
    "v_lshrrev_b32 v50, 2, v49\n\t"
    "v_and_b32 v51, 3, v60\n\t"
    "v_and_b32 v61, 1, v60\n\t"
    "v_lshlrev_b32 v61, 3, v61\n\t"
    "v_lshl_or_b32 v11, v50, 4, v61\n\t"
    "v_lshl_or_b32 v11, v48, 7, v11\n\t"
    "v_xor_b32 v12, v48, v49\n\t"
    "v_lshlrev_b32 v12, 4, v12\n\t"
    "v_lshl_or_b32 v12, v49, 8, v12\n\t"
    "v_lshl_or_b32 v50, v48, 2, v50\n\t"
    "v_or_b32 v51, 0, v51\n\t"
    "v_xor_b32 v51, v51, v50\n\t"
    "v_lshlrev_b32 v13, 4, v51\n\t"
    "v_lshl_or_b32 v13, v50, 8, v13\n\t"
    "v_and_b32 v61, 3, v60\n\t"
    "v_lshlrev_b32 v61, 4, v61\n\t"
    "v_lshl_or_b32 v15, v48, 6, v61\n\t"
    "v_and_b32 v61, 8, v60\n\t"
    "v_lshl_or_b32 v15, v61, 14, v15\n\t"
    "v_and_b32 v61, 4, v60\n\t"
    "v_lshl_or_b32 v15, v61, 8, v15\n\t"
    "v_add_u32 v15, 0, v15\n\t"
    "3:\n\t"
    "v_mov_b32 v16, 0\n\t"
    "v_mov_b32 v17, 0\n\t"
    "v_mov_b32 v18, 0\n\t"
    "v_mov_b32 v19, 0\n\t"
    "v_mov_b32 v20, 0\n\t"
    "v_mov_b32 v21, 0\n\t"
    "v_mov_b32 v22, 0\n\t"
    "v_mov_b32 v23, 0\n\t"
    "s_waitcnt vmcnt(0) lgkmcnt(0)\n\t"
    "14:\n\t"
    "ds_write_b128 v6, v[40:43] offset:0\n\t"
    "ds_write_b128 v6, v[44:47] offset:1024\n\t"
    "v_smfmac_f32_16x16x64_bf16 v[16:19], v[40:43], v[64:71], v9\n\t"
    "v_max3_u32 v60, v40, v41, v42\n\t"
    "v_max3_u32 v61, v43, v44, v45\n\t"
    "v_smfmac_f32_16x16x64_bf16 v[20:23], v[40:43], v[72:79], v9\n\t"
    "v_max3_u32 v60, v60, v46, v47\n\t"
    "v_max_u32 v60, v60, v61\n\t"
    "v_cmp_eq_u32 vcc, -1, v60\n\t"
    "s_cbranch_vccnz 1f\n\t"
    "2:\n\t"
    "s_waitcnt lgkmcnt(0)\n\t"
    "s_barrier\n\t"
    "ds_read_b128 v[40:43], v6 offset:12288\n\t"
    "ds_read_b128 v[48:51], v6 offset:2048\n\t"
    "v_smfmac_f32_16x16x64_bf16 v[16:19], v[44:47], v[80:87], v9\n\t"
    "ds_read_b128 v[52:55], v6 offset:3072\n\t"
    "ds_read_b128 v[56:59], v6 offset:4096\n\t"
    "v_smfmac_f32_16x16x64_bf16 v[20:23], v[44:47], v[88:95], v9\n\t"
    "ds_read_b128 v[60:63], v6 offset:13312\n\t"
    "ds_read_b128 v[44:47], v6 offset:5120\n\t"
    "s_waitcnt lgkmcnt(5)\n\t"
    "v_smfmac_f32_16x16x64_bf16 v[16:19], v[40:43], v[24:31], v9\n\t"
    "ds_read_b128 v[24:27], v7 offset:16384\n\t"
    "ds_read_b128 v[28:31], v7 offset:17408\n\t"
    "v_smfmac_f32_16x16x64_bf16 v[20:23], v[40:43], v[32:39], v9\n\t"
    "ds_read_b128 v[32:35], v7 offset:24576\n\t"
    "ds_read_b128 v[36:39], v7 offset:25600\n\t"
    "ds_read_b128 v[40:43], v6 offset:6144\n\t"
    "s_waitcnt lgkmcnt(9)\n\t"
    "v_smfmac_f32_16x16x64_bf16 v[16:19], v[48:51], v[96:103], v9\n\t"
    "v_smfmac_f32_16x16x64_bf16 v[20:23], v[48:51], v[104:111], v9\n\t"
    "ds_read_b128 v[48:51], v6 offset:7168\n\t"
    "s_waitcnt lgkmcnt(9)\n\t"
    "v_smfmac_f32_16x16x64_bf16 v[16:19], v[52:55], v[112:119], v9\n\t"
    "v_smfmac_f32_16x16x64_bf16 v[20:23], v[52:55], v[120:127], v9\n\t"
    "ds_read_b128 v[52:55], v6 offset:14336\n\t"
    "s_waitcnt lgkmcnt(9)\n\t"
    "v_smfmac_f32_16x16x64_bf16 v[16:19], v[56:59], v[128:135], v9\n\t"
    "v_smfmac_f32_16x16x64_bf16 v[20:23], v[56:59], v[136:143], v9\n\t"
    "ds_read_b128 v[56:59], v6 offset:8192\n\t"
    "s_waitcnt lgkmcnt(6)\n\t"
    "v_smfmac_f32_16x16x64_bf16 v[16:19], v[60:63], v[24:31], v9\n\t"
    "ds_read_b128 v[24:27], v8 offset:0\n\t"
    "ds_read_b128 v[28:31], v8 offset:1024\n\t"
    "s_waitcnt lgkmcnt(6)\n\t"
    "v_smfmac_f32_16x16x64_bf16 v[20:23], v[60:63], v[32:39], v9\n\t"
    "ds_read_b128 v[32:35], v8 offset:8192\n\t"
    "ds_read_b128 v[36:39], v8 offset:9216\n\t"
    "ds_read_b128 v[60:63], v6 offset:9216\n\t"
    "v_smfmac_f32_16x16x64_bf16 v[16:19], v[44:47], v[144:151], v9\n\t"
    "v_smfmac_f32_16x16x64_bf16 v[20:23], v[44:47], v[152:159], v9\n\t"
    "ds_read_b128 v[44:47], v6 offset:10240\n\t"
    "s_waitcnt lgkmcnt(9)\n\t"
    "v_smfmac_f32_16x16x64_bf16 v[16:19], v[40:43], v[160:167], v9\n\t"
    "v_smfmac_f32_16x16x64_bf16 v[20:23], v[40:43], v[168:175], v9\n\t"
    "ds_read_b128 v[40:43], v6 offset:15360\n\t"
    "s_waitcnt lgkmcnt(9)\n\t"
    "v_smfmac_f32_16x16x64_bf16 v[16:19], v[48:51], v[176:183], v9\n\t"
    "v_smfmac_f32_16x16x64_bf16 v[20:23], v[48:51], v[184:191], v9\n\t"
    "ds_read_b128 v[48:51], v6 offset:11264\n\t"
    "s_waitcnt lgkmcnt(6)\n\t"
    "v_smfmac_f32_16x16x64_bf16 v[16:19], v[52:55], v[24:31], v9\n\t"
    "ds_read_b128 v[24:27], v8 offset:16384\n\t"
    "ds_read_b128 v[28:31], v8 offset:17408\n\t"
    "s_waitcnt lgkmcnt(6)\n\t"
    "v_smfmac_f32_16x16x64_bf16 v[20:23], v[52:55], v[32:39], v9\n\t"
    "ds_read_b128 v[32:35], v8 offset:24576\n\t"
    "ds_read_b128 v[36:39], v8 offset:25600\n\t"
    "v_smfmac_f32_16x16x64_bf16 v[16:19], v[56:59], v[192:199], v9\n\t"
    "v_smfmac_f32_16x16x64_bf16 v[20:23], v[56:59], v[200:207], v9\n\t"
    "s_waitcnt lgkmcnt(7)\n\t"
    "v_smfmac_f32_16x16x64_bf16 v[16:19], v[60:63], v[208:215], v9\n\t"
    "v_smfmac_f32_16x16x64_bf16 v[20:23], v[60:63], v[216:223], v9\n\t"
    "s_waitcnt lgkmcnt(6)\n\t"
    "v_smfmac_f32_16x16x64_bf16 v[16:19], v[44:47], v[224:231], v9\n\t"
    "v_smfmac_f32_16x16x64_bf16 v[20:23], v[44:47], v[232:239], v9\n\t"
    "s_waitcnt lgkmcnt(2)\n\t"
    "v_smfmac_f32_16x16x64_bf16 v[16:19], v[40:43], v[24:31], v9\n\t"
    "s_waitcnt lgkmcnt(0)\n\t"
    "v_smfmac_f32_16x16x64_bf16 v[20:23], v[40:43], v[32:39], v9\n\t"
    "v_smfmac_f32_16x16x64_bf16 v[16:19], v[48:51], v[240:247], v9\n\t"
    "v_smfmac_f32_16x16x64_bf16 v[20:23], v[48:51], v[248:255], v9\n\t"
    "v_xor_b32 v7, 64, v12\n\t"
    "v_xor_b32 v8, 0x80, v12\n\t"
    "v_xor_b32 v6, 0xc0, v12\n\t"
    "ds_read_b128 v[24:27], v12 offset:16384\n\t"
    "ds_read_b128 v[28:31], v12 offset:20480\n\t"
    "ds_read_b128 v[32:35], v7 offset:16384\n\t"
    "ds_read_b128 v[36:39], v7 offset:20480\n\t"
    "v_permlane16_swap_b32 v16, v18\n\t"
    "v_permlane16_swap_b32 v17, v19\n\t"
    "v_permlane16_swap_b32 v20, v22\n\t"
    "v_permlane16_swap_b32 v21, v23\n\t"
    "v_add_f32 v16, v16, v18\n\t"
    "v_add_f32 v17, v17, v19\n\t"
    "v_add_f32 v20, v20, v22\n\t"
    "v_mul_f32 v16, %[lsc], v16\n\t"
    "v_mul_f32 v17, %[lsc], v17\n\t"
    "v_cvt_pk_bf16_f32 v16, v16, v17\n\t"
    "v_add_f32 v21, v21, v23\n\t"
    "ds_write_b32 v10, v16 offset:32768\n\t"
    "v_mul_f32 v20, %[lsc], v20\n\t"
    "v_mul_f32 v21, %[lsc], v21\n\t"
    "v_cvt_pk_bf16_f32 v20, v20, v21\n\t"
    "ds_write_b32 v10, v20 offset:33024\n\t"
    "ds_read_b128 v[40:43], v8 offset:16384\n\t"
    "ds_read_b128 v[44:47], v8 offset:20480\n\t"
    "ds_read_b128 v[48:51], v6 offset:16384\n\t"
    "ds_read_b128 v[52:55], v6 offset:20480\n\t"
    "s_waitcnt lgkmcnt(4)\n\t"
    "s_barrier\n\t"
    "ds_read_b64_tr_b16 v[16:17], v11 offset:32768\n\t"
    "ds_read_b64_tr_b16 v[18:19], v11 offset:32832\n\t"
    "ds_read_b64_tr_b16 v[20:21], v11 offset:33280\n\t"
    "ds_read_b64_tr_b16 v[22:23], v11 offset:33344\n\t"
    "ds_read_b64_tr_b16 v[56:57], v11 offset:33792\n\t"
    "ds_read_b64_tr_b16 v[58:59], v11 offset:33856\n\t"
    "ds_read_b64_tr_b16 v[60:61], v11 offset:34304\n\t"
    "ds_read_b64_tr_b16 v[62:63], v11 offset:34368\n\t"
    "v_mov_b32 v6, 0\n\t"
    "v_mov_b32 v7, 0\n\t"
    "v_mov_b32 v8, 0\n\t"
    "v_mov_b32 v9, 0\n\t"
    "s_mov_b32 exec_hi, 0\n\t"
    "global_load_dwordx4 v[6:9], v14, %[vs] sc1\n\t"
    "s_mov_b32 exec_hi, -1\n\t"
    "s_waitcnt lgkmcnt(6)\n\t"
    "v_mfma_f32_16x16x32_bf16 v[24:27], v[24:27], v[16:19], 0\n\t"
    "v_mfma_f32_16x16x32_bf16 v[28:31], v[28:31], v[16:19], 0\n\t"
    "s_waitcnt lgkmcnt(4)\n\t"
    "v_mfma_f32_16x16x32_bf16 v[24:27], v[32:35], v[20:23], v[24:27]\n\t"
    "v_mfma_f32_16x16x32_bf16 v[28:31], v[36:39], v[20:23], v[28:31]\n\t"
    "ds_read_b64_tr_b16 v[32:33], v13 offset:24576\n\t"
    "ds_read_b64_tr_b16 v[34:35], v13 offset:28672\n\t"
    "ds_read_b64_tr_b16 v[36:37], v13 offset:24584\n\t"
    "ds_read_b64_tr_b16 v[38:39], v13 offset:28680\n\t"
    "s_waitcnt lgkmcnt(6)\n\t"
    "v_mfma_f32_16x16x32_bf16 v[24:27], v[40:43], v[56:59], v[24:27]\n\t"
    "v_mfma_f32_16x16x32_bf16 v[28:31], v[44:47], v[56:59], v[28:31]\n\t"
    "s_waitcnt lgkmcnt(4)\n\t"
    "v_mfma_f32_16x16x32_bf16 v[24:27], v[48:51], v[60:63], v[24:27]\n\t"
    "v_mfma_f32_16x16x32_bf16 v[28:31], v[52:55], v[60:63], v[28:31]\n\t"
    "s_setprio 1\n\t"
    "v_mov_b32 v52, %[one]\n\t"
    "v_mov_b32 v53, %[one]\n\t"
    "v_mov_b32 v54, %[one]\n\t"
    "v_mov_b32 v55, %[one]\n\t"
    "s_nop 2\n\t"
    "v_max3_f32 v10, |v24|, |v25|, |v26|\n\t"
    "v_max3_f32 v40, |v27|, |v28|, |v29|\n\t"
    "v_max3_f32 v10, v10, |v30|, |v31|\n\t"
    "v_max_f32 v10, v10, v40\n\t"
    "v_cmp_nge_f32 vcc, %[big], v10\n\t"
    "s_mov_b32 %[bg], %[big]\n\t"
    "v_mov_b32 v10, 0\n\t"
    "s_cbranch_vccnz 12f\n\t"
    "13:\n\t"
    "v_exp_f32 v24, v24\n\t"
    "v_exp_f32 v25, v25\n\t"
    "v_exp_f32 v26, v26\n\t"
    "v_exp_f32 v27, v27\n\t"
    "v_exp_f32 v28, v28\n\t"
    "v_exp_f32 v29, v29\n\t"
    "v_exp_f32 v30, v30\n\t"
    "v_exp_f32 v31, v31\n\t"
    "v_cvt_pk_bf16_f32 v40, v24, v25\n\t"
    "v_cvt_pk_bf16_f32 v41, v26, v27\n\t"
    "v_cvt_pk_bf16_f32 v42, v28, v29\n\t"
    "v_cvt_pk_bf16_f32 v43, v30, v31\n\t"
    "s_setprio 3\n\t"
    "s_waitcnt lgkmcnt(0)\n\t"
    "s_nop 0\n\t"
    "v_mfma_f32_16x16x32_bf16 v[44:47], v[32:35], v[40:43], 0\n\t"
    "v_mfma_f32_16x16x32_bf16 v[48:51], v[36:39], v[40:43], 0\n\t"
    "v_mfma_f32_16x16x32_bf16 v[52:55], v[52:55], v[40:43], 0\n\t"
    "v_mov_b32 v12, 0\n\t"
    "v_mov_b32 v13, 0\n\t"
    "s_mov_b32 exec_hi, 0\n\t"
    "v_mov_b32 v12, %[one]\n\t"
    "v_mov_b32 v13, %[one]\n\t"
    "s_mov_b32 exec_hi, -1\n\t"
    "s_barrier\n\t"
    "ds_read_b64_tr_b16 v[24:25], v11 offset:34816\n\t"
    "ds_read_b64_tr_b16 v[26:27], v11 offset:34880\n\t"
    "ds_read_b64_tr_b16 v[28:29], v11 offset:35328\n\t"
    "ds_read_b64_tr_b16 v[30:31], v11 offset:35392\n\t"
    "ds_read_b64_tr_b16 v[32:33], v11 offset:35840\n\t"
    "ds_read_b64_tr_b16 v[34:35], v11 offset:35904\n\t"
    "ds_read_b64_tr_b16 v[36:37], v11 offset:36352\n\t"
    "ds_read_b64_tr_b16 v[38:39], v11 offset:36416\n\t"
    "s_waitcnt lgkmcnt(6)\n\t"
    "v_mfma_f32_16x16x32_bf16 v[40:43], v[24:27], v[16:19], 0\n\t"
    "s_waitcnt lgkmcnt(4)\n\t"
    "v_mfma_f32_16x16x32_bf16 v[40:43], v[28:31], v[20:23], v[40:43]\n\t"
    "s_waitcnt lgkmcnt(2)\n\t"
    "v_mfma_f32_16x16x32_bf16 v[40:43], v[32:35], v[56:59], v[40:43]\n\t"
    "s_waitcnt lgkmcnt(0)\n\t"
    "v_mfma_f32_16x16x32_bf16 v[40:43], v[36:39], v[60:63], v[40:43]\n\t"
    "5:\n\t"
    "s_waitcnt vmcnt(0)\n\t"
    "v_max3_u32 v11, v6, v7, v8\n\t"
    "v_max_u32 v11, v11, v9\n\t"
    "v_cmp_eq_u32 vcc, -1, v11\n\t"
    "s_cbranch_vccnz 4f\n\t"
    "6:\n\t"
    "s_nop 2\n\t"
    "v_exp_f32 v28, v40\n\t"
    "v_max3_f32 v11, v40, v41, v42\n\t"
    "v_exp_f32 v29, v41\n\t"
    "v_max_f32 v11, v11, v43\n\t"
    "v_exp_f32 v30, v42\n\t"
    "v_cmp_nge_f32 vcc, %[bg], v11\n\t"
    "v_exp_f32 v31, v43\n\t"
    "s_cbranch_vccnz 10f\n\t"
    "11:\n\t"
    "v_cvt_pk_bf16_f32 v24, v28, v29\n\t"
    "v_cvt_pk_bf16_f32 v25, v30, v31\n\t"
    "s_nop 1\n\t"
    "v_mfma_f32_16x16x16_bf16 v[52:55], v[12:13], v[24:25], v[52:55]\n\t"
    "v_mfma_f32_16x16x16_bf16 v[44:47], v[6:7], v[24:25], v[44:47]\n\t"
    "v_mfma_f32_16x16x16_bf16 v[48:51], v[8:9], v[24:25], v[48:51]\n\t"
    "s_nop 5\n\t"
    "v_rcp_f32 v26, v52\n\t"
    "s_nop 0\n\t"
    "v_pk_mul_f32 v[44:45], v[44:45], v[26:27] op_sel_hi:[1,0]\n\t"
    "v_pk_mul_f32 v[46:47], v[46:47], v[26:27] op_sel_hi:[1,0]\n\t"
    "v_pk_mul_f32 v[48:49], v[48:49], v[26:27] op_sel_hi:[1,0]\n\t"
    "v_pk_mul_f32 v[50:51], v[50:51], v[26:27] op_sel_hi:[1,0]\n\t"
    "v_cvt_pk_bf16_f32 v36, v44, v45\n\t"
    "v_cvt_pk_bf16_f32 v37, v46, v47\n\t"
    "v_cvt_pk_bf16_f32 v38, v48, v49\n\t"
    "v_cvt_pk_bf16_f32 v39, v50, v51\n\t"
    "v_min_u32 v36, -2, v36\n\t"
    "v_min_u32 v37, -2, v37\n\t"
    "v_min_u32 v38, -2, v38\n\t"
    "v_min_u32 v39, -2, v39\n\t"
    "global_store_dwordx4 v15, v[36:39], %[od] sc1\n\t"
    "s_barrier\n\t"
    "s_mov_b32 exec_hi, 0\n\t"
    "global_store_dwordx4 v14, v[6:9], %[kv] sc1\n\t"
    "s_mov_b32 exec_hi, -1\n\t"
    "s_nop 0\n\t"
    "s_waitcnt lgkmcnt(0)\n\t"
    "s_branch 9f\n\t"
    "1:\n\t"
    "s_add_u32 %[sp], %[sp], 1\n\t"
    "s_cmp_ge_u32 %[sp], %[cap]\n\t"
    "s_cbranch_scc1 7f\n\t"
    "global_load_dwordx4 v[40:43], v6, %[ag] sc1\n\t"
    "global_load_dwordx4 v[44:47], v6, %[ag] offset:1024 sc1\n\t"
    "s_waitcnt lgkmcnt(0)\n\t"
    "s_memtime %[tc]\n\t"
    "s_nop 7\n\t"
    "s_branch 3b\n\t"
    "7:\n\t"
    "s_mov_b32 %[cap], 0\n\t"
    "s_branch 2b\n\t"
    "4:\n\t"
    "s_add_u32 %[sp], %[sp], 0x10000\n\t"
    "s_lshr_b32 %[st], %[sp], 16\n\t"
    "s_cmp_ge_u32 %[st], %[cap]\n\t"
    "s_cbranch_scc1 8f\n\t"
    "s_mov_b32 exec_hi, 0\n\t"
    "global_load_dwordx4 v[6:9], v14, %[vs] sc1\n\t"
    "s_mov_b32 exec_hi, -1\n\t"
    "s_branch 5b\n\t"
    "8:\n\t"
    "s_mov_b32 %[cap], 0\n\t"
    "s_branch 6b\n\t"
    "10:\n\t"
    "v_max_f32 v11, v11, v10\n\t"
    "v_mov_b32 v26, v11\n\t"
    "s_nop 1\n\t"
    "v_permlane16_swap_b32 v11, v26\n\t"
    "v_max_f32 v11, v11, v26\n\t"
    "v_sub_f32 v28, v40, v11\n\t"
    "v_sub_f32 v29, v41, v11\n\t"
    "v_sub_f32 v30, v42, v11\n\t"
    "v_sub_f32 v31, v43, v11\n\t"
    "v_sub_f32 v11, v10, v11\n\t"
    "v_exp_f32 v11, v11\n\t"
    "v_exp_f32 v28, v28\n\t"
    "v_exp_f32 v29, v29\n\t"
    "v_exp_f32 v30, v30\n\t"
    "v_exp_f32 v31, v31\n\t"
    "v_mul_f32 v44, v44, v11\n\t"
    "v_mul_f32 v45, v45, v11\n\t"
    "v_mul_f32 v46, v46, v11\n\t"
    "v_mul_f32 v47, v47, v11\n\t"
    "v_mul_f32 v48, v48, v11\n\t"
    "v_mul_f32 v49, v49, v11\n\t"
    "v_mul_f32 v50, v50, v11\n\t"
    "v_mul_f32 v51, v51, v11\n\t"
    "v_mul_f32 v52, v52, v11\n\t"
    "v_mul_f32 v53, v53, v11\n\t"
    "v_mul_f32 v54, v54, v11\n\t"
    "v_mul_f32 v55, v55, v11\n\t"
    "s_branch 11b\n\t"
    "12:\n\t"
    "v_max3_f32 v10, v24, v25, v26\n\t"
    "v_max3_f32 v40, v27, v28, v29\n\t"
    "v_max3_f32 v10, v10, v30, v31\n\t"
    "v_max3_f32 v10, v10, v40, %[neg]\n\t"
    "v_mov_b32 v40, v10\n\t"
    "s_nop 1\n\t"
    "v_permlane16_swap_b32 v10, v40\n\t"
    "v_max_f32 v10, v10, v40\n\t"
    "v_mov_b32 v40, v10\n\t"
    "s_nop 1\n\t"
    "v_permlane32_swap_b32 v10, v40\n\t"
    "v_max_f32 v10, v10, v40\n\t"
    "v_sub_f32 v24, v24, v10\n\t"
    "v_sub_f32 v25, v25, v10\n\t"
    "v_sub_f32 v26, v26, v10\n\t"
    "v_sub_f32 v27, v27, v10\n\t"
    "v_sub_f32 v28, v28, v10\n\t"
    "v_sub_f32 v29, v29, v10\n\t"
    "v_sub_f32 v30, v30, v10\n\t"
    "v_sub_f32 v31, v31, v10\n\t"
    "s_mov_b32 %[bg], 0x7fc00000\n\t"
    "s_branch 13b\n\t"
    "9:"
    : [tc] "=&s"(tc), [sp] "=&s"(sp), [cap] "+s"(cap), [st] "=&s"(st), [bg] "=&s"(bg)
    : [ag] "s"(ag), [c44] "s"(0x4444u), [kv] "s"(kv), [vs] "s"(vs), [od] "s"(od), [od2] "s"(od + 262144), [neg] "s"(-1.0e38f), [lsc] "s"(LSC), [one] "s"(0x3F803F80u), [big] "s"(24.0f), "{v[64:95]}"(W.w0), "{v[96:127]}"(W.w1), "{v[128:159]}"(W.w2), "{v[160:191]}"(W.w3), "{v[192:223]}"(W.w4), "{v[224:255]}"(W.w5)
    : "v6", "v7", "v8", "v9", "v10", "v11", "v12", "v13", "v14", "v15", "v16", "v17", "v18", "v19", "v20", "v21", "v22", "v23", "v24", "v25", "v26", "v27", "v28", "v29", "v30", "v31", "v32", "v33", "v34", "v35", "v36", "v37", "v38", "v39", "v40", "v41", "v42", "v43", "v44", "v45", "v46", "v47", "v48", "v49", "v50", "v51", "v52", "v53", "v54", "v55", "v56", "v57", "v58", "v59", "v60", "v61", "v62", "v63", "vcc", "scc", "memory");
}
template <> __device__ __forceinline__ void qk_step<1>(const QKW& W, const char* ag, const char* vs, const char* od, const char* kv, unsigned& cap, unsigned& sp,
                                                        unsigned long long& tc, unsigned long long* ts, unsigned& d0, unsigned& d1) {
  unsigned st, bg;
  asm volatile("v_mbcnt_lo_u32_b32 v60, -1, 0\n\t"
    "v_mbcnt_hi_u32_b32 v60, -1, v60\n\t"
    "v_lshlrev_b32 v6, 4, v60\n\t"
    "s_mov_b32 %[sp], 0\n\t"
    "s_setprio 3\n\t"
    "global_load_dwordx4 v[40:43], v6, %[ag] sc1\n\t"
    "global_load_dwordx4 v[44:47], v6, %[ag] offset:1024 sc1\n\t"
    "s_memtime %[tc]\n\t"
    "v_add_u32 v7, 43008, v6\n\t"
    "v_add_u32 v8, 100352, v6\n\t"
    "ds_read_b128 v[24:27], v7 offset:0\n\t"
    "ds_read_b128 v[28:31], v7 offset:1024\n\t"
    "ds_read_b128 v[32:35], v7 offset:8192\n\t"
    "ds_read_b128 v[36:39], v7 offset:9216\n\t"
    "v_lshrrev_b32 v48, 4, v60\n\t"
    "v_and_b32 v49, 15, v60\n\t"
    "v_and_b32 v61, 4, v60\n\t"
    "v_mul_u32_u24 v61, 0x2aaa, v61\n\t"
    "v_lshrrev_b32 v9, 1, v60\n\t"
    "v_and_b32 v9, 2, v9\n\t"
    "v_or3_b32 v9, v9, v61, %[c44]\n\t"
    "v_add_u32 v14, 512, v6\n\t"
    "v_lshlrev_b32 v61, 2, v48\n\t"
    "v_lshl_or_b32 v10, v49, 4, v61\n\t"
    "v_lshrrev_b32 v50, 2, v49\n\t"
    "v_and_b32 v51, 3, v60\n\t"
    "v_and_b32 v61, 1, v60\n\t"
    "v_lshlrev_b32 v61, 3, v61\n\t"
    "v_lshl_or_b32 v11, v50, 4, v61\n\t"
    "v_lshl_or_b32 v11, v48, 7, v11\n\t"
    "v_xor_b32 v12, v48, v49\n\t"
    "v_lshlrev_b32 v12, 4, v12\n\t"
    "v_lshl_or_b32 v12, v49, 8, v12\n\t"
    "v_lshl_or_b32 v50, v48, 2, v50\n\t"
    "v_or_b32 v51, 4, v51\n\t"
    "v_xor_b32 v51, v51, v50\n\t"
    "v_lshlrev_b32 v13, 4, v51\n\t"
    "v_lshl_or_b32 v13, v50, 8, v13\n\t"
    "v_and_b32 v61, 3, v60\n\t"
    "v_lshlrev_b32 v61, 4, v61\n\t"
    "v_lshl_or_b32 v15, v48, 6, v61\n\t"
    "v_and_b32 v61, 8, v60\n\t"
    "v_lshl_or_b32 v15, v61, 14, v15\n\t"
    "v_and_b32 v61, 4, v60\n\t"
    "v_lshl_or_b32 v15, v61, 8, v15\n\t"
    "v_add_u32 v15, 256, v15\n\t"
    "3:\n\t"
    "v_mov_b32 v16, 0\n\t"
    "v_mov_b32 v17, 0\n\t"
    "v_mov_b32 v18, 0\n\t"
    "v_mov_b32 v19, 0\n\t"
    "v_mov_b32 v20, 0\n\t"
    "v_mov_b32 v21, 0\n\t"
    "v_mov_b32 v22, 0\n\t"
    "v_mov_b32 v23, 0\n\t"
    "s_waitcnt vmcnt(0) lgkmcnt(0)\n\t"
    "14:\n\t"
    "ds_write_b128 v6, v[40:43] offset:2048\n\t"
    "ds_write_b128 v6, v[44:47] offset:3072\n\t"
    "v_smfmac_f32_16x16x64_bf16 v[16:19], v[40:43], v[96:103], v9\n\t"
    "v_max3_u32 v60, v40, v41, v42\n\t"
    "v_max3_u32 v61, v43, v44, v45\n\t"
    "v_smfmac_f32_16x16x64_bf16 v[20:23], v[40:43], v[104:111], v9\n\t"
    "v_max3_u32 v60, v60, v46, v47\n\t"
    "v_max_u32 v60, v60, v61\n\t"
    "v_cmp_eq_u32 vcc, -1, v60\n\t"
    "s_cbranch_vccnz 1f\n\t"
    "2:\n\t"
    "s_waitcnt lgkmcnt(0)\n\t"
    "s_barrier\n\t"
    "ds_read_b128 v[40:43], v6 offset:12288\n\t"
    "ds_read_b128 v[48:51], v6 offset:0\n\t"
    "v_smfmac_f32_16x16x64_bf16 v[16:19], v[44:47], v[112:119], v9\n\t"
    "ds_read_b128 v[52:55], v6 offset:1024\n\t"
    "ds_read_b128 v[56:59], v6 offset:4096\n\t"
    "v_smfmac_f32_16x16x64_bf16 v[20:23], v[44:47], v[120:127], v9\n\t"
    "ds_read_b128 v[60:63], v6 offset:13312\n\t"
    "ds_read_b128 v[44:47], v6 offset:5120\n\t"
    "s_waitcnt lgkmcnt(5)\n\t"
    "v_smfmac_f32_16x16x64_bf16 v[16:19], v[40:43], v[24:31], v9\n\t"
    "ds_read_b128 v[24:27], v7 offset:16384\n\t"
    "ds_read_b128 v[28:31], v7 offset:17408\n\t"
    "v_smfmac_f32_16x16x64_bf16 v[20:23], v[40:43], v[32:39], v9\n\t"
    "ds_read_b128 v[32:35], v7 offset:24576\n\t"
    "ds_read_b128 v[36:39], v7 offset:25600\n\t"
    "ds_read_b128 v[40:43], v6 offset:6144\n\t"
    "s_waitcnt lgkmcnt(9)\n\t"
    "v_smfmac_f32_16x16x64_bf16 v[16:19], v[48:51], v[64:71], v9\n\t"
    "v_smfmac_f32_16x16x64_bf16 v[20:23], v[48:51], v[72:79], v9\n\t"
    "ds_read_b128 v[48:51], v6 offset:7168\n\t"
    "s_waitcnt lgkmcnt(9)\n\t"
    "v_smfmac_f32_16x16x64_bf16 v[16:19], v[52:55], v[80:87], v9\n\t"
    "v_smfmac_f32_16x16x64_bf16 v[20:23], v[52:55], v[88:95], v9\n\t"
    "ds_read_b128 v[52:55], v6 offset:14336\n\t"
    "s_waitcnt lgkmcnt(9)\n\t"
    "v_smfmac_f32_16x16x64_bf16 v[16:19], v[56:59], v[128:135], v9\n\t"
    "v_smfmac_f32_16x16x64_bf16 v[20:23], v[56:59], v[136:143], v9\n\t"
    "ds_read_b128 v[56:59], v6 offset:8192\n\t"
    "s_waitcnt lgkmcnt(6)\n\t"
    "v_smfmac_f32_16x16x64_bf16 v[16:19], v[60:63], v[24:31], v9\n\t"
    "ds_read_b128 v[24:27], v8 offset:0\n\t"
    "ds_read_b128 v[28:31], v8 offset:1024\n\t"
    "s_waitcnt lgkmcnt(6)\n\t"
    "v_smfmac_f32_16x16x64_bf16 v[20:23], v[60:63], v[32:39], v9\n\t"
    "ds_read_b128 v[32:35], v8 offset:8192\n\t"
    "ds_read_b128 v[36:39], v8 offset:9216\n\t"
    "ds_read_b128 v[60:63], v6 offset:9216\n\t"
    "v_smfmac_f32_16x16x64_bf16 v[16:19], v[44:47], v[144:151], v9\n\t"
    "v_smfmac_f32_16x16x64_bf16 v[20:23], v[44:47], v[152:159], v9\n\t"
    "ds_read_b128 v[44:47], v6 offset:10240\n\t"
    "s_waitcnt lgkmcnt(9)\n\t"
    "v_smfmac_f32_16x16x64_bf16 v[16:19], v[40:43], v[160:167], v9\n\t"
    "v_smfmac_f32_16x16x64_bf16 v[20:23], v[40:43], v[168:175], v9\n\t"
    "ds_read_b128 v[40:43], v6 offset:15360\n\t"
    "s_waitcnt lgkmcnt(9)\n\t"
    "v_smfmac_f32_16x16x64_bf16 v[16:19], v[48:51], v[176:183], v9\n\t"
    "v_smfmac_f32_16x16x64_bf16 v[20:23], v[48:51], v[184:191], v9\n\t"
    "ds_read_b128 v[48:51], v6 offset:11264\n\t"
    "s_waitcnt lgkmcnt(6)\n\t"
    "v_smfmac_f32_16x16x64_bf16 v[16:19], v[52:55], v[24:31], v9\n\t"
    "ds_read_b128 v[24:27], v8 offset:16384\n\t"
    "ds_read_b128 v[28:31], v8 offset:17408\n\t"
    "s_waitcnt lgkmcnt(6)\n\t"
    "v_smfmac_f32_16x16x64_bf16 v[20:23], v[52:55], v[32:39], v9\n\t"
    "ds_read_b128 v[32:35], v8 offset:24576\n\t"
    "ds_read_b128 v[36:39], v8 offset:25600\n\t"
    "v_smfmac_f32_16x16x64_bf16 v[16:19], v[56:59], v[192:199], v9\n\t"
    "v_smfmac_f32_16x16x64_bf16 v[20:23], v[56:59], v[200:207], v9\n\t"
    "s_waitcnt lgkmcnt(7)\n\t"
    "v_smfmac_f32_16x16x64_bf16 v[16:19], v[60:63], v[208:215], v9\n\t"
    "v_smfmac_f32_16x16x64_bf16 v[20:23], v[60:63], v[216:223], v9\n\t"
    "s_waitcnt lgkmcnt(6)\n\t"
    "v_smfmac_f32_16x16x64_bf16 v[16:19], v[44:47], v[224:231], v9\n\t"
    "v_smfmac_f32_16x16x64_bf16 v[20:23], v[44:47], v[232:239], v9\n\t"
    "s_waitcnt lgkmcnt(2)\n\t"
    "v_smfmac_f32_16x16x64_bf16 v[16:19], v[40:43], v[24:31], v9\n\t"
    "s_waitcnt lgkmcnt(0)\n\t"
    "v_smfmac_f32_16x16x64_bf16 v[20:23], v[40:43], v[32:39], v9\n\t"
    "v_smfmac_f32_16x16x64_bf16 v[16:19], v[48:51], v[240:247], v9\n\t"
    "v_smfmac_f32_16x16x64_bf16 v[20:23], v[48:51], v[248:255], v9\n\t"
    "v_xor_b32 v7, 64, v12\n\t"
    "v_xor_b32 v8, 0x80, v12\n\t"
    "v_xor_b32 v6, 0xc0, v12\n\t"
    "ds_read_b128 v[24:27], v12 offset:16384\n\t"
    "ds_read_b128 v[28:31], v12 offset:20480\n\t"
    "ds_read_b128 v[32:35], v7 offset:16384\n\t"
    "ds_read_b128 v[36:39], v7 offset:20480\n\t"
    "v_permlane16_swap_b32 v16, v18\n\t"
    "v_permlane16_swap_b32 v17, v19\n\t"
    "v_permlane16_swap_b32 v20, v22\n\t"
    "v_permlane16_swap_b32 v21, v23\n\t"
    "v_add_f32 v16, v16, v18\n\t"
    "v_add_f32 v17, v17, v19\n\t"
    "v_add_f32 v20, v20, v22\n\t"
    "v_mul_f32 v16, %[lsc], v16\n\t"
    "v_mul_f32 v17, %[lsc], v17\n\t"
    "v_cvt_pk_bf16_f32 v16, v16, v17\n\t"
    "v_add_f32 v21, v21, v23\n\t"
    "ds_write_b32 v10, v16 offset:33280\n\t"
    "v_mul_f32 v20, %[lsc], v20\n\t"
    "v_mul_f32 v21, %[lsc], v21\n\t"
    "v_cvt_pk_bf16_f32 v20, v20, v21\n\t"
    "ds_write_b32 v10, v20 offset:33536\n\t"
    "ds_read_b128 v[40:43], v8 offset:16384\n\t"
    "ds_read_b128 v[44:47], v8 offset:20480\n\t"
    "ds_read_b128 v[48:51], v6 offset:16384\n\t"
    "ds_read_b128 v[52:55], v6 offset:20480\n\t"
    "s_waitcnt lgkmcnt(4)\n\t"
    "s_barrier\n\t"
    "ds_read_b64_tr_b16 v[16:17], v11 offset:32768\n\t"
    "ds_read_b64_tr_b16 v[18:19], v11 offset:32832\n\t"
    "ds_read_b64_tr_b16 v[20:21], v11 offset:33280\n\t"
    "ds_read_b64_tr_b16 v[22:23], v11 offset:33344\n\t"
    "ds_read_b64_tr_b16 v[56:57], v11 offset:33792\n\t"
    "ds_read_b64_tr_b16 v[58:59], v11 offset:33856\n\t"
    "ds_read_b64_tr_b16 v[60:61], v11 offset:34304\n\t"
    "ds_read_b64_tr_b16 v[62:63], v11 offset:34368\n\t"
    "v_mov_b32 v6, 0\n\t"
    "v_mov_b32 v7, 0\n\t"
    "v_mov_b32 v8, 0\n\t"
    "v_mov_b32 v9, 0\n\t"
    "s_mov_b32 exec_hi, 0\n\t"
    "global_load_dwordx4 v[6:9], v14, %[vs] sc1\n\t"
    "s_mov_b32 exec_hi, -1\n\t"
    "s_waitcnt lgkmcnt(6)\n\t"
    "v_mfma_f32_16x16x32_bf16 v[24:27], v[24:27], v[16:19], 0\n\t"
    "v_mfma_f32_16x16x32_bf16 v[28:31], v[28:31], v[16:19], 0\n\t"
    "s_waitcnt lgkmcnt(4)\n\t"
    "v_mfma_f32_16x16x32_bf16 v[24:27], v[32:35], v[20:23], v[24:27]\n\t"
    "v_mfma_f32_16x16x32_bf16 v[28:31], v[36:39], v[20:23], v[28:31]\n\t"
    "ds_read_b64_tr_b16 v[32:33], v13 offset:24576\n\t"
    "ds_read_b64_tr_b16 v[34:35], v13 offset:28672\n\t"
    "ds_read_b64_tr_b16 v[36:37], v13 offset:24584\n\t"
    "ds_read_b64_tr_b16 v[38:39], v13 offset:28680\n\t"
    "s_waitcnt lgkmcnt(6)\n\t"
    "v_mfma_f32_16x16x32_bf16 v[24:27], v[40:43], v[56:59], v[24:27]\n\t"
    "v_mfma_f32_16x16x32_bf16 v[28:31], v[44:47], v[56:59], v[28:31]\n\t"
    "s_waitcnt lgkmcnt(4)\n\t"
    "v_mfma_f32_16x16x32_bf16 v[24:27], v[48:51], v[60:63], v[24:27]\n\t"
    "v_mfma_f32_16x16x32_bf16 v[28:31], v[52:55], v[60:63], v[28:31]\n\t"
    "s_setprio 1\n\t"
    "v_mov_b32 v52, %[one]\n\t"
    "v_mov_b32 v53, %[one]\n\t"
    "v_mov_b32 v54, %[one]\n\t"
    "v_mov_b32 v55, %[one]\n\t"
    "s_nop 2\n\t"
    "v_max3_f32 v10, |v24|, |v25|, |v26|\n\t"
    "v_max3_f32 v40, |v27|, |v28|, |v29|\n\t"
    "v_max3_f32 v10, v10, |v30|, |v31|\n\t"
    "v_max_f32 v10, v10, v40\n\t"
    "v_cmp_nge_f32 vcc, %[big], v10\n\t"
    "s_mov_b32 %[bg], %[big]\n\t"
    "v_mov_b32 v10, 0\n\t"
    "s_cbranch_vccnz 12f\n\t"
    "13:\n\t"
    "v_exp_f32 v24, v24\n\t"
    "v_exp_f32 v25, v25\n\t"
    "v_exp_f32 v26, v26\n\t"
    "v_exp_f32 v27, v27\n\t"
    "v_exp_f32 v28, v28\n\t"
    "v_exp_f32 v29, v29\n\t"
    "v_exp_f32 v30, v30\n\t"
    "v_exp_f32 v31, v31\n\t"
    "v_cvt_pk_bf16_f32 v40, v24, v25\n\t"
    "v_cvt_pk_bf16_f32 v41, v26, v27\n\t"
    "v_cvt_pk_bf16_f32 v42, v28, v29\n\t"
    "v_cvt_pk_bf16_f32 v43, v30, v31\n\t"
    "s_setprio 3\n\t"
    "s_waitcnt lgkmcnt(0)\n\t"
    "s_nop 0\n\t"
    "v_mfma_f32_16x16x32_bf16 v[44:47], v[32:35], v[40:43], 0\n\t"
    "v_mfma_f32_16x16x32_bf16 v[48:51], v[36:39], v[40:43], 0\n\t"
    "v_mfma_f32_16x16x32_bf16 v[52:55], v[52:55], v[40:43], 0\n\t"
    "v_mov_b32 v12, 0\n\t"
    "v_mov_b32 v13, 0\n\t"
    "s_mov_b32 exec_hi, 0\n\t"
    "v_mov_b32 v12, %[one]\n\t"
    "v_mov_b32 v13, %[one]\n\t"
    "s_mov_b32 exec_hi, -1\n\t"
    "s_barrier\n\t"
    "ds_read_b64_tr_b16 v[24:25], v11 offset:34816\n\t"
    "ds_read_b64_tr_b16 v[26:27], v11 offset:34880\n\t"
    "ds_read_b64_tr_b16 v[28:29], v11 offset:35328\n\t"
    "ds_read_b64_tr_b16 v[30:31], v11 offset:35392\n\t"
    "ds_read_b64_tr_b16 v[32:33], v11 offset:35840\n\t"
    "ds_read_b64_tr_b16 v[34:35], v11 offset:35904\n\t"
    "ds_read_b64_tr_b16 v[36:37], v11 offset:36352\n\t"
    "ds_read_b64_tr_b16 v[38:39], v11 offset:36416\n\t"
    "s_waitcnt lgkmcnt(6)\n\t"
    "v_mfma_f32_16x16x32_bf16 v[40:43], v[24:27], v[16:19], 0\n\t"
    "s_waitcnt lgkmcnt(4)\n\t"
    "v_mfma_f32_16x16x32_bf16 v[40:43], v[28:31], v[20:23], v[40:43]\n\t"
    "s_waitcnt lgkmcnt(2)\n\t"
    "v_mfma_f32_16x16x32_bf16 v[40:43], v[32:35], v[56:59], v[40:43]\n\t"
    "s_waitcnt lgkmcnt(0)\n\t"
    "v_mfma_f32_16x16x32_bf16 v[40:43], v[36:39], v[60:63], v[40:43]\n\t"
    "5:\n\t"
    "s_waitcnt vmcnt(0)\n\t"
    "v_max3_u32 v11, v6, v7, v8\n\t"
    "v_max_u32 v11, v11, v9\n\t"
    "v_cmp_eq_u32 vcc, -1, v11\n\t"
    "s_cbranch_vccnz 4f\n\t"
    "6:\n\t"
    "s_nop 2\n\t"
    "v_exp_f32 v28, v40\n\t"
    "v_max3_f32 v11, v40, v41, v42\n\t"
    "v_exp_f32 v29, v41\n\t"
    "v_max_f32 v11, v11, v43\n\t"
    "v_exp_f32 v30, v42\n\t"
    "v_cmp_nge_f32 vcc, %[bg], v11\n\t"
    "v_exp_f32 v31, v43\n\t"
    "s_cbranch_vccnz 10f\n\t"
    "11:\n\t"
    "v_cvt_pk_bf16_f32 v24, v28, v29\n\t"
    "v_cvt_pk_bf16_f32 v25, v30, v31\n\t"
    "s_nop 1\n\t"
    "v_mfma_f32_16x16x16_bf16 v[52:55], v[12:13], v[24:25], v[52:55]\n\t"
    "v_mfma_f32_16x16x16_bf16 v[44:47], v[6:7], v[24:25], v[44:47]\n\t"
    "v_mfma_f32_16x16x16_bf16 v[48:51], v[8:9], v[24:25], v[48:51]\n\t"
    "s_nop 5\n\t"
    "v_rcp_f32 v26, v52\n\t"
    "s_nop 0\n\t"
    "v_pk_mul_f32 v[44:45], v[44:45], v[26:27] op_sel_hi:[1,0]\n\t"
    "v_pk_mul_f32 v[46:47], v[46:47], v[26:27] op_sel_hi:[1,0]\n\t"
    "v_pk_mul_f32 v[48:49], v[48:49], v[26:27] op_sel_hi:[1,0]\n\t"
    "v_pk_mul_f32 v[50:51], v[50:51], v[26:27] op_sel_hi:[1,0]\n\t"
    "v_cvt_pk_bf16_f32 v36, v44, v45\n\t"
    "v_cvt_pk_bf16_f32 v37, v46, v47\n\t"
    "v_cvt_pk_bf16_f32 v38, v48, v49\n\t"
    "v_cvt_pk_bf16_f32 v39, v50, v51\n\t"
    "v_min_u32 v36, -2, v36\n\t"
    "v_min_u32 v37, -2, v37\n\t"
    "v_min_u32 v38, -2, v38\n\t"
    "v_min_u32 v39, -2, v39\n\t"
    "global_store_dwordx4 v15, v[36:39], %[od] sc1\n\t"
    "s_barrier\n\t"
    "s_mov_b32 exec_hi, 0\n\t"
    "global_store_dwordx4 v14, v[6:9], %[kv] sc1\n\t"
    "s_mov_b32 exec_hi, -1\n\t"
    "s_nop 0\n\t"
    "s_waitcnt lgkmcnt(0)\n\t"
    "s_branch 9f\n\t"
    "1:\n\t"
    "s_add_u32 %[sp], %[sp], 1\n\t"
    "s_cmp_ge_u32 %[sp], %[cap]\n\t"
    "s_cbranch_scc1 7f\n\t"
    "global_load_dwordx4 v[40:43], v6, %[ag] sc1\n\t"
    "global_load_dwordx4 v[44:47], v6, %[ag] offset:1024 sc1\n\t"
    "s_waitcnt lgkmcnt(0)\n\t"
    "s_memtime %[tc]\n\t"
    "s_nop 7\n\t"
    "s_branch 3b\n\t"
    "7:\n\t"
    "s_mov_b32 %[cap], 0\n\t"
    "s_branch 2b\n\t"
    "4:\n\t"
    "s_add_u32 %[sp], %[sp], 0x10000\n\t"
    "s_lshr_b32 %[st], %[sp], 16\n\t"
    "s_cmp_ge_u32 %[st], %[cap]\n\t"
    "s_cbranch_scc1 8f\n\t"
    "s_mov_b32 exec_hi, 0\n\t"
    "global_load_dwordx4 v[6:9], v14, %[vs] sc1\n\t"
    "s_mov_b32 exec_hi, -1\n\t"
    "s_branch 5b\n\t"
    "8:\n\t"
    "s_mov_b32 %[cap], 0\n\t"
    "s_branch 6b\n\t"
    "10:\n\t"
    "v_max_f32 v11, v11, v10\n\t"
    "v_mov_b32 v26, v11\n\t"
    "s_nop 1\n\t"
    "v_permlane16_swap_b32 v11, v26\n\t"
    "v_max_f32 v11, v11, v26\n\t"
    "v_sub_f32 v28, v40, v11\n\t"
    "v_sub_f32 v29, v41, v11\n\t"
    "v_sub_f32 v30, v42, v11\n\t"
    "v_sub_f32 v31, v43, v11\n\t"
    "v_sub_f32 v11, v10, v11\n\t"
    "v_exp_f32 v11, v11\n\t"
    "v_exp_f32 v28, v28\n\t"
    "v_exp_f32 v29, v29\n\t"
    "v_exp_f32 v30, v30\n\t"
    "v_exp_f32 v31, v31\n\t"
    "v_mul_f32 v44, v44, v11\n\t"
    "v_mul_f32 v45, v45, v11\n\t"
    "v_mul_f32 v46, v46, v11\n\t"
    "v_mul_f32 v47, v47, v11\n\t"
    "v_mul_f32 v48, v48, v11\n\t"
    "v_mul_f32 v49, v49, v11\n\t"
    "v_mul_f32 v50, v50, v11\n\t"
    "v_mul_f32 v51, v51, v11\n\t"
    "v_mul_f32 v52, v52, v11\n\t"
    "v_mul_f32 v53, v53, v11\n\t"
    "v_mul_f32 v54, v54, v11\n\t"
    "v_mul_f32 v55, v55, v11\n\t"
    "s_branch 11b\n\t"
    "12:\n\t"
    "v_max3_f32 v10, v24, v25, v26\n\t"
    "v_max3_f32 v40, v27, v28, v29\n\t"
    "v_max3_f32 v10, v10, v30, v31\n\t"
    "v_max3_f32 v10, v10, v40, %[neg]\n\t"
    "v_mov_b32 v40, v10\n\t"
    "s_nop 1\n\t"
    "v_permlane16_swap_b32 v10, v40\n\t"
    "v_max_f32 v10, v10, v40\n\t"
    "v_mov_b32 v40, v10\n\t"
    "s_nop 1\n\t"
    "v_permlane32_swap_b32 v10, v40\n\t"
    "v_max_f32 v10, v10, v40\n\t"
    "v_sub_f32 v24, v24, v10\n\t"
    "v_sub_f32 v25, v25, v10\n\t"
    "v_sub_f32 v26, v26, v10\n\t"
    "v_sub_f32 v27, v27, v10\n\t"
    "v_sub_f32 v28, v28, v10\n\t"
    "v_sub_f32 v29, v29, v10\n\t"
    "v_sub_f32 v30, v30, v10\n\t"
    "v_sub_f32 v31, v31, v10\n\t"
    "s_mov_b32 %[bg], 0x7fc00000\n\t"
    "s_branch 13b\n\t"
    "9:"
    : [tc] "=&s"(tc), [sp] "=&s"(sp), [cap] "+s"(cap), [st] "=&s"(st), [bg] "=&s"(bg)
    : [ag] "s"(ag), [c44] "s"(0x4444u), [kv] "s"(kv), [vs] "s"(vs), [od] "s"(od), [od2] "s"(od + 262144), [neg] "s"(-1.0e38f), [lsc] "s"(LSC), [one] "s"(0x3F803F80u), [big] "s"(24.0f), "{v[64:95]}"(W.w0), "{v[96:127]}"(W.w1), "{v[128:159]}"(W.w2), "{v[160:191]}"(W.w3), "{v[192:223]}"(W.w4), "{v[224:255]}"(W.w5)
    : "v6", "v7", "v8", "v9", "v10", "v11", "v12", "v13", "v14", "v15", "v16", "v17", "v18", "v19", "v20", "v21", "v22", "v23", "v24", "v25", "v26", "v27", "v28", "v29", "v30", "v31", "v32", "v33", "v34", "v35", "v36", "v37", "v38", "v39", "v40", "v41", "v42", "v43", "v44", "v45", "v46", "v47", "v48", "v49", "v50", "v51", "v52", "v53", "v54", "v55", "v56", "v57", "v58", "v59", "v60", "v61", "v62", "v63", "vcc", "scc", "memory");
}
template <> __device__ __forceinline__ void qk_step<2>(const QKW& W, const char* ag, const char* vs, const char* od, const char* kv, unsigned& cap, unsigned& sp,
                                                        unsigned long long& tc, unsigned long long* ts, unsigned& d0, unsigned& d1) {
  unsigned st, bg;
  asm volatile("v_mbcnt_lo_u32_b32 v60, -1, 0\n\t"
    "v_mbcnt_hi_u32_b32 v60, -1, v60\n\t"
    "v_lshlrev_b32 v6, 4, v60\n\t"
    "s_mov_b32 %[sp], 0\n\t"
    "s_setprio 3\n\t"
    "global_load_dwordx4 v[40:43], v6, %[ag] sc1\n\t"
    "global_load_dwordx4 v[44:47], v6, %[ag] offset:1024 sc1\n\t"
    "s_memtime %[tc]\n\t"
    "v_add_u32 v7, 45056, v6\n\t"
    "v_add_u32 v8, 102400, v6\n\t"
    "ds_read_b128 v[24:27], v7 offset:0\n\t"
    "ds_read_b128 v[28:31], v7 offset:1024\n\t"
    "ds_read_b128 v[32:35], v7 offset:8192\n\t"
    "ds_read_b128 v[36:39], v7 offset:9216\n\t"
    "v_lshrrev_b32 v48, 4, v60\n\t"
    "v_and_b32 v49, 15, v60\n\t"
    "v_and_b32 v61, 4, v60\n\t"
    "v_mul_u32_u24 v61, 0x2aaa, v61\n\t"
    "v_lshrrev_b32 v9, 1, v60\n\t"
    "v_and_b32 v9, 2, v9\n\t"
    "v_or3_b32 v9, v9, v61, %[c44]\n\t"
    "v_add_u32 v14, 1024, v6\n\t"
    "v_lshlrev_b32 v61, 2, v48\n\t"
    "v_lshl_or_b32 v10, v49, 4, v61\n\t"
    "v_lshrrev_b32 v50, 2, v49\n\t"
    "v_and_b32 v51, 3, v60\n\t"
    "v_and_b32 v61, 1, v60\n\t"
    "v_lshlrev_b32 v61, 3, v61\n\t"
    "v_lshl_or_b32 v11, v50, 4, v61\n\t"
    "v_lshl_or_b32 v11, v48, 7, v11\n\t"
    "v_xor_b32 v12, v48, v49\n\t"
    "v_lshlrev_b32 v12, 4, v12\n\t"
    "v_lshl_or_b32 v12, v49, 8, v12\n\t"
    "v_lshl_or_b32 v50, v48, 2, v50\n\t"
    "v_or_b32 v51, 8, v51\n\t"
    "v_xor_b32 v51, v51, v50\n\t"
    "v_lshlrev_b32 v13, 4, v51\n\t"
    "v_lshl_or_b32 v13, v50, 8, v13\n\t"
    "v_and_b32 v61, 3, v60\n\t"
    "v_lshlrev_b32 v61, 4, v61\n\t"
    "v_lshl_or_b32 v15, v48, 6, v61\n\t"
    "v_and_b32 v61, 8, v60\n\t"
    "v_lshl_or_b32 v15, v61, 14, v15\n\t"
    "v_and_b32 v61, 4, v60\n\t"
    "v_lshl_or_b32 v15, v61, 8, v15\n\t"
    "v_add_u32 v15, 512, v15\n\t"
    "3:\n\t"
    "v_mov_b32 v16, 0\n\t"
    "v_mov_b32 v17, 0\n\t"
    "v_mov_b32 v18, 0\n\t"
    "v_mov_b32 v19, 0\n\t"
    "v_mov_b32 v20, 0\n\t"
    "v_mov_b32 v21, 0\n\t"
    "v_mov_b32 v22, 0\n\t"
    "v_mov_b32 v23, 0\n\t"
    "s_waitcnt vmcnt(0) lgkmcnt(0)\n\t"
    "14:\n\t"
    "ds_write_b128 v6, v[40:43] offset:4096\n\t"
    "ds_write_b128 v6, v[44:47] offset:5120\n\t"
    "v_smfmac_f32_16x16x64_bf16 v[16:19], v[40:43], v[128:135], v9\n\t"
    "v_max3_u32 v60, v40, v41, v42\n\t"
    "v_max3_u32 v61, v43, v44, v45\n\t"
    "v_smfmac_f32_16x16x64_bf16 v[20:23], v[40:43], v[136:143], v9\n\t"
    "v_max3_u32 v60, v60, v46, v47\n\t"
    "v_max_u32 v60, v60, v61\n\t"
    "v_cmp_eq_u32 vcc, -1, v60\n\t"
    "s_cbranch_vccnz 1f\n\t"
    "2:\n\t"
    "s_waitcnt lgkmcnt(0)\n\t"
    "s_barrier\n\t"
    "ds_read_b128 v[40:43], v6 offset:12288\n\t"
    "ds_read_b128 v[48:51], v6 offset:0\n\t"
    "v_smfmac_f32_16x16x64_bf16 v[16:19], v[44:47], v[144:151], v9\n\t"
    "ds_read_b128 v[52:55], v6 offset:1024\n\t"
    "ds_read_b128 v[56:59], v6 offset:2048\n\t"
    "v_smfmac_f32_16x16x64_bf16 v[20:23], v[44:47], v[152:159], v9\n\t"
    "ds_read_b128 v[60:63], v6 offset:13312\n\t"
    "ds_read_b128 v[44:47], v6 offset:3072\n\t"
    "s_waitcnt lgkmcnt(5)\n\t"
    "v_smfmac_f32_16x16x64_bf16 v[16:19], v[40:43], v[24:31], v9\n\t"
    "ds_read_b128 v[24:27], v7 offset:16384\n\t"
    "ds_read_b128 v[28:31], v7 offset:17408\n\t"
    "v_smfmac_f32_16x16x64_bf16 v[20:23], v[40:43], v[32:39], v9\n\t"
    "ds_read_b128 v[32:35], v7 offset:24576\n\t"
    "ds_read_b128 v[36:39], v7 offset:25600\n\t"
    "ds_read_b128 v[40:43], v6 offset:6144\n\t"
    "s_waitcnt lgkmcnt(9)\n\t"
    "v_smfmac_f32_16x16x64_bf16 v[16:19], v[48:51], v[64:71], v9\n\t"
    "v_smfmac_f32_16x16x64_bf16 v[20:23], v[48:51], v[72:79], v9\n\t"
    "ds_read_b128 v[48:51], v6 offset:7168\n\t"
    "s_waitcnt lgkmcnt(9)\n\t"
    "v_smfmac_f32_16x16x64_bf16 v[16:19], v[52:55], v[80:87], v9\n\t"
    "v_smfmac_f32_16x16x64_bf16 v[20:23], v[52:55], v[88:95], v9\n\t"
    "ds_read_b128 v[52:55], v6 offset:14336\n\t"
    "s_waitcnt lgkmcnt(9)\n\t"
    "v_smfmac_f32_16x16x64_bf16 v[16:19], v[56:59], v[96:103], v9\n\t"
    "v_smfmac_f32_16x16x64_bf16 v[20:23], v[56:59], v[104:111], v9\n\t"
    "ds_read_b128 v[56:59], v6 offset:8192\n\t"
    "s_waitcnt lgkmcnt(6)\n\t"
    "v_smfmac_f32_16x16x64_bf16 v[16:19], v[60:63], v[24:31], v9\n\t"
    "ds_read_b128 v[24:27], v8 offset:0\n\t"
    "ds_read_b128 v[28:31], v8 offset:1024\n\t"
    "s_waitcnt lgkmcnt(6)\n\t"
    "v_smfmac_f32_16x16x64_bf16 v[20:23], v[60:63], v[32:39], v9\n\t"
    "ds_read_b128 v[32:35], v8 offset:8192\n\t"
    "ds_read_b128 v[36:39], v8 offset:9216\n\t"
    "ds_read_b128 v[60:63], v6 offset:9216\n\t"
    "v_smfmac_f32_16x16x64_bf16 v[16:19], v[44:47], v[112:119], v9\n\t"
    "v_smfmac_f32_16x16x64_bf16 v[20:23], v[44:47], v[120:127], v9\n\t"
    "ds_read_b128 v[44:47], v6 offset:10240\n\t"
    "s_waitcnt lgkmcnt(9)\n\t"
    "v_smfmac_f32_16x16x64_bf16 v[16:19], v[40:43], v[160:167], v9\n\t"
    "v_smfmac_f32_16x16x64_bf16 v[20:23], v[40:43], v[168:175], v9\n\t"
    "ds_read_b128 v[40:43], v6 offset:15360\n\t"
    "s_waitcnt lgkmcnt(9)\n\t"
    "v_smfmac_f32_16x16x64_bf16 v[16:19], v[48:51], v[176:183], v9\n\t"
    "v_smfmac_f32_16x16x64_bf16 v[20:23], v[48:51], v[184:191], v9\n\t"
    "ds_read_b128 v[48:51], v6 offset:11264\n\t"
    "s_waitcnt lgkmcnt(6)\n\t"
    "v_smfmac_f32_16x16x64_bf16 v[16:19], v[52:55], v[24:31], v9\n\t"
    "ds_read_b128 v[24:27], v8 offset:16384\n\t"
    "ds_read_b128 v[28:31], v8 offset:17408\n\t"
    "s_waitcnt lgkmcnt(6)\n\t"
    "v_smfmac_f32_16x16x64_bf16 v[20:23], v[52:55], v[32:39], v9\n\t"
    "ds_read_b128 v[32:35], v8 offset:24576\n\t"
    "ds_read_b128 v[36:39], v8 offset:25600\n\t"
    "v_smfmac_f32_16x16x64_bf16 v[16:19], v[56:59], v[192:199], v9\n\t"
    "v_smfmac_f32_16x16x64_bf16 v[20:23], v[56:59], v[200:207], v9\n\t"
    "s_waitcnt lgkmcnt(7)\n\t"
    "v_smfmac_f32_16x16x64_bf16 v[16:19], v[60:63], v[208:215], v9\n\t"
    "v_smfmac_f32_16x16x64_bf16 v[20:23], v[60:63], v[216:223], v9\n\t"
    "s_waitcnt lgkmcnt(6)\n\t"
    "v_smfmac_f32_16x16x64_bf16 v[16:19], v[44:47], v[224:231], v9\n\t"
    "v_smfmac_f32_16x16x64_bf16 v[20:23], v[44:47], v[232:239], v9\n\t"
    "s_waitcnt lgkmcnt(2)\n\t"
    "v_smfmac_f32_16x16x64_bf16 v[16:19], v[40:43], v[24:31], v9\n\t"
    "s_waitcnt lgkmcnt(0)\n\t"
    "v_smfmac_f32_16x16x64_bf16 v[20:23], v[40:43], v[32:39], v9\n\t"
    "v_smfmac_f32_16x16x64_bf16 v[16:19], v[48:51], v[240:247], v9\n\t"
    "v_smfmac_f32_16x16x64_bf16 v[20:23], v[48:51], v[248:255], v9\n\t"
    "v_xor_b32 v7, 64, v12\n\t"
    "v_xor_b32 v8, 0x80, v12\n\t"
    "v_xor_b32 v6, 0xc0, v12\n\t"
    "ds_read_b128 v[24:27], v12 offset:16384\n\t"
    "ds_read_b128 v[28:31], v12 offset:20480\n\t"
    "ds_read_b128 v[32:35], v7 offset:16384\n\t"
    "ds_read_b128 v[36:39], v7 offset:20480\n\t"
    "v_permlane16_swap_b32 v16, v18\n\t"
    "v_permlane16_swap_b32 v17, v19\n\t"
    "v_permlane16_swap_b32 v20, v22\n\t"
    "v_permlane16_swap_b32 v21, v23\n\t"
    "v_add_f32 v16, v16, v18\n\t"
    "v_add_f32 v17, v17, v19\n\t"
    "v_add_f32 v20, v20, v22\n\t"
    "v_mul_f32 v16, %[lsc], v16\n\t"
    "v_mul_f32 v17, %[lsc], v17\n\t"
    "v_cvt_pk_bf16_f32 v16, v16, v17\n\t"
    "v_add_f32 v21, v21, v23\n\t"
    "ds_write_b32 v10, v16 offset:33792\n\t"
    "v_mul_f32 v20, %[lsc], v20\n\t"
    "v_mul_f32 v21, %[lsc], v21\n\t"
    "v_cvt_pk_bf16_f32 v20, v20, v21\n\t"
    "ds_write_b32 v10, v20 offset:34048\n\t"
    "ds_read_b128 v[40:43], v8 offset:16384\n\t"
    "ds_read_b128 v[44:47], v8 offset:20480\n\t"
    "ds_read_b128 v[48:51], v6 offset:16384\n\t"
    "ds_read_b128 v[52:55], v6 offset:20480\n\t"
    "s_waitcnt lgkmcnt(4)\n\t"
    "s_barrier\n\t"
    "ds_read_b64_tr_b16 v[16:17], v11 offset:32768\n\t"
    "ds_read_b64_tr_b16 v[18:19], v11 offset:32832\n\t"
    "ds_read_b64_tr_b16 v[20:21], v11 offset:33280\n\t"
    "ds_read_b64_tr_b16 v[22:23], v11 offset:33344\n\t"
    "ds_read_b64_tr_b16 v[56:57], v11 offset:33792\n\t"
    "ds_read_b64_tr_b16 v[58:59], v11 offset:33856\n\t"
    "ds_read_b64_tr_b16 v[60:61], v11 offset:34304\n\t"
    "ds_read_b64_tr_b16 v[62:63], v11 offset:34368\n\t"
    "v_mov_b32 v6, 0\n\t"
    "v_mov_b32 v7, 0\n\t"
    "v_mov_b32 v8, 0\n\t"
    "v_mov_b32 v9, 0\n\t"
    "s_mov_b32 exec_hi, 0\n\t"
    "global_load_dwordx4 v[6:9], v14, %[vs] sc1\n\t"
    "s_mov_b32 exec_hi, -1\n\t"
    "s_waitcnt lgkmcnt(6)\n\t"
    "v_mfma_f32_16x16x32_bf16 v[24:27], v[24:27], v[16:19], 0\n\t"
    "v_mfma_f32_16x16x32_bf16 v[28:31], v[28:31], v[16:19], 0\n\t"
    "s_waitcnt lgkmcnt(4)\n\t"
    "v_mfma_f32_16x16x32_bf16 v[24:27], v[32:35], v[20:23], v[24:27]\n\t"
    "v_mfma_f32_16x16x32_bf16 v[28:31], v[36:39], v[20:23], v[28:31]\n\t"
    "ds_read_b64_tr_b16 v[32:33], v13 offset:24576\n\t"
    "ds_read_b64_tr_b16 v[34:35], v13 offset:28672\n\t"
    "ds_read_b64_tr_b16 v[36:37], v13 offset:24584\n\t"
    "ds_read_b64_tr_b16 v[38:39], v13 offset:28680\n\t"
    "s_waitcnt lgkmcnt(6)\n\t"
    "v_mfma_f32_16x16x32_bf16 v[24:27], v[40:43], v[56:59], v[24:27]\n\t"
    "v_mfma_f32_16x16x32_bf16 v[28:31], v[44:47], v[56:59], v[28:31]\n\t"
    "s_waitcnt lgkmcnt(4)\n\t"
    "v_mfma_f32_16x16x32_bf16 v[24:27], v[48:51], v[60:63], v[24:27]\n\t"
    "v_mfma_f32_16x16x32_bf16 v[28:31], v[52:55], v[60:63], v[28:31]\n\t"
    "s_setprio 1\n\t"
    "v_mov_b32 v52, %[one]\n\t"
    "v_mov_b32 v53, %[one]\n\t"
    "v_mov_b32 v54, %[one]\n\t"
    "v_mov_b32 v55, %[one]\n\t"
    "s_nop 2\n\t"
    "v_max3_f32 v10, |v24|, |v25|, |v26|\n\t"
    "v_max3_f32 v40, |v27|, |v28|, |v29|\n\t"
    "v_max3_f32 v10, v10, |v30|, |v31|\n\t"
    "v_max_f32 v10, v10, v40\n\t"
    "v_cmp_nge_f32 vcc, %[big], v10\n\t"
    "s_mov_b32 %[bg], %[big]\n\t"
    "v_mov_b32 v10, 0\n\t"
    "s_cbranch_vccnz 12f\n\t"
    "13:\n\t"
    "v_exp_f32 v24, v24\n\t"
    "v_exp_f32 v25, v25\n\t"
    "v_exp_f32 v26, v26\n\t"
    "v_exp_f32 v27, v27\n\t"
    "v_exp_f32 v28, v28\n\t"
    "v_exp_f32 v29, v29\n\t"
    "v_exp_f32 v30, v30\n\t"
    "v_exp_f32 v31, v31\n\t"
    "v_cvt_pk_bf16_f32 v40, v24, v25\n\t"
    "v_cvt_pk_bf16_f32 v41, v26, v27\n\t"
    "v_cvt_pk_bf16_f32 v42, v28, v29\n\t"
    "v_cvt_pk_bf16_f32 v43, v30, v31\n\t"
    "s_setprio 3\n\t"
    "s_waitcnt lgkmcnt(0)\n\t"
    "s_nop 0\n\t"
    "v_mfma_f32_16x16x32_bf16 v[44:47], v[32:35], v[40:43], 0\n\t"
    "v_mfma_f32_16x16x32_bf16 v[48:51], v[36:39], v[40:43], 0\n\t"
    "v_mfma_f32_16x16x32_bf16 v[52:55], v[52:55], v[40:43], 0\n\t"
    "v_mov_b32 v12, 0\n\t"
    "v_mov_b32 v13, 0\n\t"
    "s_mov_b32 exec_hi, 0\n\t"
    "v_mov_b32 v12, %[one]\n\t"
    "v_mov_b32 v13, %[one]\n\t"
    "s_mov_b32 exec_hi, -1\n\t"
    "s_barrier\n\t"
    "ds_read_b64_tr_b16 v[24:25], v11 offset:34816\n\t"
    "ds_read_b64_tr_b16 v[26:27], v11 offset:34880\n\t"
    "ds_read_b64_tr_b16 v[28:29], v11 offset:35328\n\t"
    "ds_read_b64_tr_b16 v[30:31], v11 offset:35392\n\t"
    "ds_read_b64_tr_b16 v[32:33], v11 offset:35840\n\t"
    "ds_read_b64_tr_b16 v[34:35], v11 offset:35904\n\t"
    "ds_read_b64_tr_b16 v[36:37], v11 offset:36352\n\t"
    "ds_read_b64_tr_b16 v[38:39], v11 offset:36416\n\t"
    "s_waitcnt lgkmcnt(6)\n\t"
    "v_mfma_f32_16x16x32_bf16 v[40:43], v[24:27], v[16:19], 0\n\t"
    "s_waitcnt lgkmcnt(4)\n\t"
    "v_mfma_f32_16x16x32_bf16 v[40:43], v[28:31], v[20:23], v[40:43]\n\t"
    "s_waitcnt lgkmcnt(2)\n\t"
    "v_mfma_f32_16x16x32_bf16 v[40:43], v[32:35], v[56:59], v[40:43]\n\t"
    "s_waitcnt lgkmcnt(0)\n\t"
    "v_mfma_f32_16x16x32_bf16 v[40:43], v[36:39], v[60:63], v[40:43]\n\t"
    "5:\n\t"
    "s_waitcnt vmcnt(0)\n\t"
    "v_max3_u32 v11, v6, v7, v8\n\t"
    "v_max_u32 v11, v11, v9\n\t"
    "v_cmp_eq_u32 vcc, -1, v11\n\t"
    "s_cbranch_vccnz 4f\n\t"
    "6:\n\t"
    "s_nop 2\n\t"
    "v_exp_f32 v28, v40\n\t"
    "v_max3_f32 v11, v40, v41, v42\n\t"
    "v_exp_f32 v29, v41\n\t"
    "v_max_f32 v11, v11, v43\n\t"
    "v_exp_f32 v30, v42\n\t"
    "v_cmp_nge_f32 vcc, %[bg], v11\n\t"
    "v_exp_f32 v31, v43\n\t"
    "s_cbranch_vccnz 10f\n\t"
    "11:\n\t"
    "v_cvt_pk_bf16_f32 v24, v28, v29\n\t"
    "v_cvt_pk_bf16_f32 v25, v30, v31\n\t"
    "s_nop 1\n\t"
    "v_mfma_f32_16x16x16_bf16 v[52:55], v[12:13], v[24:25], v[52:55]\n\t"
    "v_mfma_f32_16x16x16_bf16 v[44:47], v[6:7], v[24:25], v[44:47]\n\t"
    "v_mfma_f32_16x16x16_bf16 v[48:51], v[8:9], v[24:25], v[48:51]\n\t"
    "s_nop 5\n\t"
    "v_rcp_f32 v26, v52\n\t"
    "s_nop 0\n\t"
    "v_pk_mul_f32 v[44:45], v[44:45], v[26:27] op_sel_hi:[1,0]\n\t"
    "v_pk_mul_f32 v[46:47], v[46:47], v[26:27] op_sel_hi:[1,0]\n\t"
    "v_pk_mul_f32 v[48:49], v[48:49], v[26:27] op_sel_hi:[1,0]\n\t"
    "v_pk_mul_f32 v[50:51], v[50:51], v[26:27] op_sel_hi:[1,0]\n\t"
    "v_cvt_pk_bf16_f32 v36, v44, v45\n\t"
    "v_cvt_pk_bf16_f32 v37, v46, v47\n\t"
    "v_cvt_pk_bf16_f32 v38, v48, v49\n\t"
    "v_cvt_pk_bf16_f32 v39, v50, v51\n\t"
    "v_min_u32 v36, -2, v36\n\t"
    "v_min_u32 v37, -2, v37\n\t"
    "v_min_u32 v38, -2, v38\n\t"
    "v_min_u32 v39, -2, v39\n\t"
    "global_store_dwordx4 v15, v[36:39], %[od] sc1\n\t"
    "s_barrier\n\t"
    "s_mov_b32 exec_hi, 0\n\t"
    "global_store_dwordx4 v14, v[6:9], %[kv] sc1\n\t"
    "s_mov_b32 exec_hi, -1\n\t"
    "s_nop 0\n\t"
    "s_waitcnt lgkmcnt(0)\n\t"
    "s_branch 9f\n\t"
    "1:\n\t"
    "s_add_u32 %[sp], %[sp], 1\n\t"
    "s_cmp_ge_u32 %[sp], %[cap]\n\t"
    "s_cbranch_scc1 7f\n\t"
    "global_load_dwordx4 v[40:43], v6, %[ag] sc1\n\t"
    "global_load_dwordx4 v[44:47], v6, %[ag] offset:1024 sc1\n\t"
    "s_waitcnt lgkmcnt(0)\n\t"
    "s_memtime %[tc]\n\t"
    "s_nop 7\n\t"
    "s_branch 3b\n\t"
    "7:\n\t"
    "s_mov_b32 %[cap], 0\n\t"
    "s_branch 2b\n\t"
    "4:\n\t"
    "s_add_u32 %[sp], %[sp], 0x10000\n\t"
    "s_lshr_b32 %[st], %[sp], 16\n\t"
    "s_cmp_ge_u32 %[st], %[cap]\n\t"
    "s_cbranch_scc1 8f\n\t"
    "s_mov_b32 exec_hi, 0\n\t"
    "global_load_dwordx4 v[6:9], v14, %[vs] sc1\n\t"
    "s_mov_b32 exec_hi, -1\n\t"
    "s_branch 5b\n\t"
    "8:\n\t"
    "s_mov_b32 %[cap], 0\n\t"
    "s_branch 6b\n\t"
    "10:\n\t"
    "v_max_f32 v11, v11, v10\n\t"
    "v_mov_b32 v26, v11\n\t"
    "s_nop 1\n\t"
    "v_permlane16_swap_b32 v11, v26\n\t"
    "v_max_f32 v11, v11, v26\n\t"
    "v_sub_f32 v28, v40, v11\n\t"
    "v_sub_f32 v29, v41, v11\n\t"
    "v_sub_f32 v30, v42, v11\n\t"
    "v_sub_f32 v31, v43, v11\n\t"
    "v_sub_f32 v11, v10, v11\n\t"
    "v_exp_f32 v11, v11\n\t"
    "v_exp_f32 v28, v28\n\t"
    "v_exp_f32 v29, v29\n\t"
    "v_exp_f32 v30, v30\n\t"
    "v_exp_f32 v31, v31\n\t"
    "v_mul_f32 v44, v44, v11\n\t"
    "v_mul_f32 v45, v45, v11\n\t"
    "v_mul_f32 v46, v46, v11\n\t"
    "v_mul_f32 v47, v47, v11\n\t"
    "v_mul_f32 v48, v48, v11\n\t"
    "v_mul_f32 v49, v49, v11\n\t"
    "v_mul_f32 v50, v50, v11\n\t"
    "v_mul_f32 v51, v51, v11\n\t"
    "v_mul_f32 v52, v52, v11\n\t"
    "v_mul_f32 v53, v53, v11\n\t"
    "v_mul_f32 v54, v54, v11\n\t"
    "v_mul_f32 v55, v55, v11\n\t"
    "s_branch 11b\n\t"
    "12:\n\t"
    "v_max3_f32 v10, v24, v25, v26\n\t"
    "v_max3_f32 v40, v27, v28, v29\n\t"
    "v_max3_f32 v10, v10, v30, v31\n\t"
    "v_max3_f32 v10, v10, v40, %[neg]\n\t"
    "v_mov_b32 v40, v10\n\t"
    "s_nop 1\n\t"
    "v_permlane16_swap_b32 v10, v40\n\t"
    "v_max_f32 v10, v10, v40\n\t"
    "v_mov_b32 v40, v10\n\t"
    "s_nop 1\n\t"
    "v_permlane32_swap_b32 v10, v40\n\t"
    "v_max_f32 v10, v10, v40\n\t"
    "v_sub_f32 v24, v24, v10\n\t"
    "v_sub_f32 v25, v25, v10\n\t"
    "v_sub_f32 v26, v26, v10\n\t"
    "v_sub_f32 v27, v27, v10\n\t"
    "v_sub_f32 v28, v28, v10\n\t"
    "v_sub_f32 v29, v29, v10\n\t"
    "v_sub_f32 v30, v30, v10\n\t"
    "v_sub_f32 v31, v31, v10\n\t"
    "s_mov_b32 %[bg], 0x7fc00000\n\t"
    "s_branch 13b\n\t"
    "9:"
    : [tc] "=&s"(tc), [sp] "=&s"(sp), [cap] "+s"(cap), [st] "=&s"(st), [bg] "=&s"(bg)
    : [ag] "s"(ag), [c44] "s"(0x4444u), [kv] "s"(kv), [vs] "s"(vs), [od] "s"(od), [od2] "s"(od + 262144), [neg] "s"(-1.0e38f), [lsc] "s"(LSC), [one] "s"(0x3F803F80u), [big] "s"(24.0f), "{v[64:95]}"(W.w0), "{v[96:127]}"(W.w1), "{v[128:159]}"(W.w2), "{v[160:191]}"(W.w3), "{v[192:223]}"(W.w4), "{v[224:255]}"(W.w5)
    : "v6", "v7", "v8", "v9", "v10", "v11", "v12", "v13", "v14", "v15", "v16", "v17", "v18", "v19", "v20", "v21", "v22", "v23", "v24", "v25", "v26", "v27", "v28", "v29", "v30", "v31", "v32", "v33", "v34", "v35", "v36", "v37", "v38", "v39", "v40", "v41", "v42", "v43", "v44", "v45", "v46", "v47", "v48", "v49", "v50", "v51", "v52", "v53", "v54", "v55", "v56", "v57", "v58", "v59", "v60", "v61", "v62", "v63", "vcc", "scc", "memory");
}
template <> __device__ __forceinline__ void qk_step<3>(const QKW& W, const char* ag, const char* vs, const char* od, const char* kv, unsigned& cap, unsigned& sp,
                                                        unsigned long long& tc, unsigned long long* ts, unsigned& d0, unsigned& d1) {
  unsigned st, bg;
  asm volatile("v_mbcnt_lo_u32_b32 v60, -1, 0\n\t"
    "v_mbcnt_hi_u32_b32 v60, -1, v60\n\t"
    "v_lshlrev_b32 v6, 4, v60\n\t"
    "s_mov_b32 %[sp], 0\n\t"
    "s_setprio 3\n\t"
    "global_load_dwordx4 v[40:43], v6, %[ag] sc1\n\t"
    "global_load_dwordx4 v[44:47], v6, %[ag] offset:1024 sc1\n\t"
    "s_memtime %[tc]\n\t"
    "v_add_u32 v7, 47104, v6\n\t"
    "v_add_u32 v8, 104448, v6\n\t"
    "ds_read_b128 v[24:27], v7 offset:0\n\t"
    "ds_read_b128 v[28:31], v7 offset:1024\n\t"
    "ds_read_b128 v[32:35], v7 offset:8192\n\t"
    "ds_read_b128 v[36:39], v7 offset:9216\n\t"
    "v_lshrrev_b32 v48, 4, v60\n\t"
    "v_and_b32 v49, 15, v60\n\t"
    "v_and_b32 v61, 4, v60\n\t"
    "v_mul_u32_u24 v61, 0x2aaa, v61\n\t"
    "v_lshrrev_b32 v9, 1, v60\n\t"
    "v_and_b32 v9, 2, v9\n\t"
    "v_or3_b32 v9, v9, v61, %[c44]\n\t"
    "v_add_u32 v14, 1536, v6\n\t"
    "v_lshlrev_b32 v61, 2, v48\n\t"
    "v_lshl_or_b32 v10, v49, 4, v61\n\t"
    "v_lshrrev_b32 v50, 2, v49\n\t"
    "v_and_b32 v51, 3, v60\n\t"
    "v_and_b32 v61, 1, v60\n\t"
    "v_lshlrev_b32 v61, 3, v61\n\t"
    "v_lshl_or_b32 v11, v50, 4, v61\n\t"
    "v_lshl_or_b32 v11, v48, 7, v11\n\t"
    "v_xor_b32 v12, v48, v49\n\t"
    "v_lshlrev_b32 v12, 4, v12\n\t"
    "v_lshl_or_b32 v12, v49, 8, v12\n\t"
    "v_lshl_or_b32 v50, v48, 2, v50\n\t"
    "v_or_b32 v51, 12, v51\n\t"
    "v_xor_b32 v51, v51, v50\n\t"
    "v_lshlrev_b32 v13, 4, v51\n\t"
    "v_lshl_or_b32 v13, v50, 8, v13\n\t"
    "v_and_b32 v61, 3, v60\n\t"
    "v_lshlrev_b32 v61, 4, v61\n\t"
    "v_lshl_or_b32 v15, v48, 6, v61\n\t"
    "v_and_b32 v61, 8, v60\n\t"
    "v_lshl_or_b32 v15, v61, 14, v15\n\t"
    "v_and_b32 v61, 4, v60\n\t"
    "v_lshl_or_b32 v15, v61, 8, v15\n\t"
    "v_add_u32 v15, 768, v15\n\t"
    "3:\n\t"
    "v_mov_b32 v16, 0\n\t"
    "v_mov_b32 v17, 0\n\t"
    "v_mov_b32 v18, 0\n\t"
    "v_mov_b32 v19, 0\n\t"
    "v_mov_b32 v20, 0\n\t"
    "v_mov_b32 v21, 0\n\t"
    "v_mov_b32 v22, 0\n\t"
    "v_mov_b32 v23, 0\n\t"
    "s_waitcnt vmcnt(0) lgkmcnt(0)\n\t"
    "14:\n\t"
    "ds_write_b128 v6, v[40:43] offset:6144\n\t"
    "ds_write_b128 v6, v[44:47] offset:7168\n\t"
    "v_smfmac_f32_16x16x64_bf16 v[16:19], v[40:43], v[160:167], v9\n\t"
    "v_max3_u32 v60, v40, v41, v42\n\t"
    "v_max3_u32 v61, v43, v44, v45\n\t"
    "v_smfmac_f32_16x16x64_bf16 v[20:23], v[40:43], v[168:175], v9\n\t"
    "v_max3_u32 v60, v60, v46, v47\n\t"
    "v_max_u32 v60, v60, v61\n\t"
    "v_cmp_eq_u32 vcc, -1, v60\n\t"
    "s_cbranch_vccnz 1f\n\t"
    "2:\n\t"
    "s_waitcnt lgkmcnt(0)\n\t"
    "s_barrier\n\t"
    "ds_read_b128 v[40:43], v6 offset:12288\n\t"
    "ds_read_b128 v[48:51], v6 offset:0\n\t"
    "v_smfmac_f32_16x16x64_bf16 v[16:19], v[44:47], v[176:183], v9\n\t"
    "ds_read_b128 v[52:55], v6 offset:1024\n\t"
    "ds_read_b128 v[56:59], v6 offset:2048\n\t"
    "v_smfmac_f32_16x16x64_bf16 v[20:23], v[44:47], v[184:191], v9\n\t"
    "ds_read_b128 v[60:63], v6 offset:13312\n\t"
    "ds_read_b128 v[44:47], v6 offset:3072\n\t"
    "s_waitcnt lgkmcnt(5)\n\t"
    "v_smfmac_f32_16x16x64_bf16 v[16:19], v[40:43], v[24:31], v9\n\t"
    "ds_read_b128 v[24:27], v7 offset:16384\n\t"
    "ds_read_b128 v[28:31], v7 offset:17408\n\t"
    "v_smfmac_f32_16x16x64_bf16 v[20:23], v[40:43], v[32:39], v9\n\t"
    "ds_read_b128 v[32:35], v7 offset:24576\n\t"
    "ds_read_b128 v[36:39], v7 offset:25600\n\t"
    "ds_read_b128 v[40:43], v6 offset:4096\n\t"
    "s_waitcnt lgkmcnt(9)\n\t"
    "v_smfmac_f32_16x16x64_bf16 v[16:19], v[48:51], v[64:71], v9\n\t"
    "v_smfmac_f32_16x16x64_bf16 v[20:23], v[48:51], v[72:79], v9\n\t"
    "ds_read_b128 v[48:51], v6 offset:5120\n\t"
    "s_waitcnt lgkmcnt(9)\n\t"
    "v_smfmac_f32_16x16x64_bf16 v[16:19], v[52:55], v[80:87], v9\n\t"
    "v_smfmac_f32_16x16x64_bf16 v[20:23], v[52:55], v[88:95], v9\n\t"
    "ds_read_b128 v[52:55], v6 offset:14336\n\t"
    "s_waitcnt lgkmcnt(9)\n\t"
    "v_smfmac_f32_16x16x64_bf16 v[16:19], v[56:59], v[96:103], v9\n\t"
    "v_smfmac_f32_16x16x64_bf16 v[20:23], v[56:59], v[104:111], v9\n\t"
    "ds_read_b128 v[56:59], v6 offset:8192\n\t"
    "s_waitcnt lgkmcnt(6)\n\t"
    "v_smfmac_f32_16x16x64_bf16 v[16:19], v[60:63], v[24:31], v9\n\t"
    "ds_read_b128 v[24:27], v8 offset:0\n\t"
    "ds_read_b128 v[28:31], v8 offset:1024\n\t"
    "s_waitcnt lgkmcnt(6)\n\t"
    "v_smfmac_f32_16x16x64_bf16 v[20:23], v[60:63], v[32:39], v9\n\t"
    "ds_read_b128 v[32:35], v8 offset:8192\n\t"
    "ds_read_b128 v[36:39], v8 offset:9216\n\t"
    "ds_read_b128 v[60:63], v6 offset:9216\n\t"
    "v_smfmac_f32_16x16x64_bf16 v[16:19], v[44:47], v[112:119], v9\n\t"
    "v_smfmac_f32_16x16x64_bf16 v[20:23], v[44:47], v[120:127], v9\n\t"
    "ds_read_b128 v[44:47], v6 offset:10240\n\t"
    "s_waitcnt lgkmcnt(9)\n\t"
    "v_smfmac_f32_16x16x64_bf16 v[16:19], v[40:43], v[128:135], v9\n\t"
    "v_smfmac_f32_16x16x64_bf16 v[20:23], v[40:43], v[136:143], v9\n\t"
    "ds_read_b128 v[40:43], v6 offset:15360\n\t"
    "s_waitcnt lgkmcnt(9)\n\t"
    "v_smfmac_f32_16x16x64_bf16 v[16:19], v[48:51], v[144:151], v9\n\t"
    "v_smfmac_f32_16x16x64_bf16 v[20:23], v[48:51], v[152:159], v9\n\t"
    "ds_read_b128 v[48:51], v6 offset:11264\n\t"
    "s_waitcnt lgkmcnt(6)\n\t"
    "v_smfmac_f32_16x16x64_bf16 v[16:19], v[52:55], v[24:31], v9\n\t"
    "ds_read_b128 v[24:27], v8 offset:16384\n\t"
    "ds_read_b128 v[28:31], v8 offset:17408\n\t"
    "s_waitcnt lgkmcnt(6)\n\t"
    "v_smfmac_f32_16x16x64_bf16 v[20:23], v[52:55], v[32:39], v9\n\t"
    "ds_read_b128 v[32:35], v8 offset:24576\n\t"
    "ds_read_b128 v[36:39], v8 offset:25600\n\t"
    "v_smfmac_f32_16x16x64_bf16 v[16:19], v[56:59], v[192:199], v9\n\t"
    "v_smfmac_f32_16x16x64_bf16 v[20:23], v[56:59], v[200:207], v9\n\t"
    "s_waitcnt lgkmcnt(7)\n\t"
    "v_smfmac_f32_16x16x64_bf16 v[16:19], v[60:63], v[208:215], v9\n\t"
    "v_smfmac_f32_16x16x64_bf16 v[20:23], v[60:63], v[216:223], v9\n\t"
    "s_waitcnt lgkmcnt(6)\n\t"
    "v_smfmac_f32_16x16x64_bf16 v[16:19], v[44:47], v[224:231], v9\n\t"
    "v_smfmac_f32_16x16x64_bf16 v[20:23], v[44:47], v[232:239], v9\n\t"
    "s_waitcnt lgkmcnt(2)\n\t"
    "v_smfmac_f32_16x16x64_bf16 v[16:19], v[40:43], v[24:31], v9\n\t"
    "s_waitcnt lgkmcnt(0)\n\t"
    "v_smfmac_f32_16x16x64_bf16 v[20:23], v[40:43], v[32:39], v9\n\t"
    "v_smfmac_f32_16x16x64_bf16 v[16:19], v[48:51], v[240:247], v9\n\t"
    "v_smfmac_f32_16x16x64_bf16 v[20:23], v[48:51], v[248:255], v9\n\t"
    "v_xor_b32 v7, 64, v12\n\t"
    "v_xor_b32 v8, 0x80, v12\n\t"
    "v_xor_b32 v6, 0xc0, v12\n\t"
    "ds_read_b128 v[24:27], v12 offset:16384\n\t"
    "ds_read_b128 v[28:31], v12 offset:20480\n\t"
    "ds_read_b128 v[32:35], v7 offset:16384\n\t"
    "ds_read_b128 v[36:39], v7 offset:20480\n\t"
    "v_permlane16_swap_b32 v16, v18\n\t"
    "v_permlane16_swap_b32 v17, v19\n\t"
    "v_permlane16_swap_b32 v20, v22\n\t"
    "v_permlane16_swap_b32 v21, v23\n\t"
    "v_add_f32 v16, v16, v18\n\t"
    "v_add_f32 v17, v17, v19\n\t"
    "v_add_f32 v20, v20, v22\n\t"
    "v_mul_f32 v16, %[lsc], v16\n\t"
    "v_mul_f32 v17, %[lsc], v17\n\t"
    "v_cvt_pk_bf16_f32 v16, v16, v17\n\t"
    "v_add_f32 v21, v21, v23\n\t"
    "ds_write_b32 v10, v16 offset:34304\n\t"
    "v_mul_f32 v20, %[lsc], v20\n\t"
    "v_mul_f32 v21, %[lsc], v21\n\t"
    "v_cvt_pk_bf16_f32 v20, v20, v21\n\t"
    "ds_write_b32 v10, v20 offset:34560\n\t"
    "ds_read_b128 v[40:43], v8 offset:16384\n\t"
    "ds_read_b128 v[44:47], v8 offset:20480\n\t"
    "ds_read_b128 v[48:51], v6 offset:16384\n\t"
    "ds_read_b128 v[52:55], v6 offset:20480\n\t"
    "s_waitcnt lgkmcnt(4)\n\t"
    "s_barrier\n\t"
    "ds_read_b64_tr_b16 v[16:17], v11 offset:32768\n\t"
    "ds_read_b64_tr_b16 v[18:19], v11 offset:32832\n\t"
    "ds_read_b64_tr_b16 v[20:21], v11 offset:33280\n\t"
    "ds_read_b64_tr_b16 v[22:23], v11 offset:33344\n\t"
    "ds_read_b64_tr_b16 v[56:57], v11 offset:33792\n\t"
    "ds_read_b64_tr_b16 v[58:59], v11 offset:33856\n\t"
    "ds_read_b64_tr_b16 v[60:61], v11 offset:34304\n\t"
    "ds_read_b64_tr_b16 v[62:63], v11 offset:34368\n\t"
    "v_mov_b32 v6, 0\n\t"
    "v_mov_b32 v7, 0\n\t"
    "v_mov_b32 v8, 0\n\t"
    "v_mov_b32 v9, 0\n\t"
    "s_mov_b32 exec_hi, 0\n\t"
    "global_load_dwordx4 v[6:9], v14, %[vs] sc1\n\t"
    "s_mov_b32 exec_hi, -1\n\t"
    "s_waitcnt lgkmcnt(6)\n\t"
    "v_mfma_f32_16x16x32_bf16 v[24:27], v[24:27], v[16:19], 0\n\t"
    "v_mfma_f32_16x16x32_bf16 v[28:31], v[28:31], v[16:19], 0\n\t"
    "s_waitcnt lgkmcnt(4)\n\t"
    "v_mfma_f32_16x16x32_bf16 v[24:27], v[32:35], v[20:23], v[24:27]\n\t"
    "v_mfma_f32_16x16x32_bf16 v[28:31], v[36:39], v[20:23], v[28:31]\n\t"
    "ds_read_b64_tr_b16 v[32:33], v13 offset:24576\n\t"
    "ds_read_b64_tr_b16 v[34:35], v13 offset:28672\n\t"
    "ds_read_b64_tr_b16 v[36:37], v13 offset:24584\n\t"
    "ds_read_b64_tr_b16 v[38:39], v13 offset:28680\n\t"
    "s_waitcnt lgkmcnt(6)\n\t"
    "v_mfma_f32_16x16x32_bf16 v[24:27], v[40:43], v[56:59], v[24:27]\n\t"
    "v_mfma_f32_16x16x32_bf16 v[28:31], v[44:47], v[56:59], v[28:31]\n\t"
    "s_waitcnt lgkmcnt(4)\n\t"
    "v_mfma_f32_16x16x32_bf16 v[24:27], v[48:51], v[60:63], v[24:27]\n\t"
    "v_mfma_f32_16x16x32_bf16 v[28:31], v[52:55], v[60:63], v[28:31]\n\t"
    "s_setprio 1\n\t"
    "v_mov_b32 v52, %[one]\n\t"
    "v_mov_b32 v53, %[one]\n\t"
    "v_mov_b32 v54, %[one]\n\t"
    "v_mov_b32 v55, %[one]\n\t"
    "s_nop 2\n\t"
    "v_max3_f32 v10, |v24|, |v25|, |v26|\n\t"
    "v_max3_f32 v40, |v27|, |v28|, |v29|\n\t"
    "v_max3_f32 v10, v10, |v30|, |v31|\n\t"
    "v_max_f32 v10, v10, v40\n\t"
    "v_cmp_nge_f32 vcc, %[big], v10\n\t"
    "s_mov_b32 %[bg], %[big]\n\t"
    "v_mov_b32 v10, 0\n\t"
    "s_cbranch_vccnz 12f\n\t"
    "13:\n\t"
    "v_exp_f32 v24, v24\n\t"
    "v_exp_f32 v25, v25\n\t"
    "v_exp_f32 v26, v26\n\t"
    "v_exp_f32 v27, v27\n\t"
    "v_exp_f32 v28, v28\n\t"
    "v_exp_f32 v29, v29\n\t"
    "v_exp_f32 v30, v30\n\t"
    "v_exp_f32 v31, v31\n\t"
    "v_cvt_pk_bf16_f32 v40, v24, v25\n\t"
    "v_cvt_pk_bf16_f32 v41, v26, v27\n\t"
    "v_cvt_pk_bf16_f32 v42, v28, v29\n\t"
    "v_cvt_pk_bf16_f32 v43, v30, v31\n\t"
    "s_setprio 3\n\t"
    "s_waitcnt lgkmcnt(0)\n\t"
    "s_nop 0\n\t"
    "v_mfma_f32_16x16x32_bf16 v[44:47], v[32:35], v[40:43], 0\n\t"
    "v_mfma_f32_16x16x32_bf16 v[48:51], v[36:39], v[40:43], 0\n\t"
    "v_mfma_f32_16x16x32_bf16 v[52:55], v[52:55], v[40:43], 0\n\t"
    "v_mov_b32 v12, 0\n\t"
    "v_mov_b32 v13, 0\n\t"
    "s_mov_b32 exec_hi, 0\n\t"
    "v_mov_b32 v12, %[one]\n\t"
    "v_mov_b32 v13, %[one]\n\t"
    "s_mov_b32 exec_hi, -1\n\t"
    "s_barrier\n\t"
    "ds_read_b64_tr_b16 v[24:25], v11 offset:34816\n\t"
    "ds_read_b64_tr_b16 v[26:27], v11 offset:34880\n\t"
    "ds_read_b64_tr_b16 v[28:29], v11 offset:35328\n\t"
    "ds_read_b64_tr_b16 v[30:31], v11 offset:35392\n\t"
    "ds_read_b64_tr_b16 v[32:33], v11 offset:35840\n\t"
    "ds_read_b64_tr_b16 v[34:35], v11 offset:35904\n\t"
    "ds_read_b64_tr_b16 v[36:37], v11 offset:36352\n\t"
    "ds_read_b64_tr_b16 v[38:39], v11 offset:36416\n\t"
    "s_waitcnt lgkmcnt(6)\n\t"
    "v_mfma_f32_16x16x32_bf16 v[40:43], v[24:27], v[16:19], 0\n\t"
    "s_waitcnt lgkmcnt(4)\n\t"
    "v_mfma_f32_16x16x32_bf16 v[40:43], v[28:31], v[20:23], v[40:43]\n\t"
    "s_waitcnt lgkmcnt(2)\n\t"
    "v_mfma_f32_16x16x32_bf16 v[40:43], v[32:35], v[56:59], v[40:43]\n\t"
    "s_waitcnt lgkmcnt(0)\n\t"
    "v_mfma_f32_16x16x32_bf16 v[40:43], v[36:39], v[60:63], v[40:43]\n\t"
    "5:\n\t"
    "s_waitcnt vmcnt(0)\n\t"
    "v_max3_u32 v11, v6, v7, v8\n\t"
    "v_max_u32 v11, v11, v9\n\t"
    "v_cmp_eq_u32 vcc, -1, v11\n\t"
    "s_cbranch_vccnz 4f\n\t"
    "6:\n\t"
    "s_nop 2\n\t"
    "v_exp_f32 v28, v40\n\t"
    "v_max3_f32 v11, v40, v41, v42\n\t"
    "v_exp_f32 v29, v41\n\t"
    "v_max_f32 v11, v11, v43\n\t"
    "v_exp_f32 v30, v42\n\t"
    "v_cmp_nge_f32 vcc, %[bg], v11\n\t"
    "v_exp_f32 v31, v43\n\t"
    "s_cbranch_vccnz 10f\n\t"
    "11:\n\t"
    "v_cvt_pk_bf16_f32 v24, v28, v29\n\t"
    "v_cvt_pk_bf16_f32 v25, v30, v31\n\t"
    "s_nop 1\n\t"
    "v_mfma_f32_16x16x16_bf16 v[52:55], v[12:13], v[24:25], v[52:55]\n\t"
    "v_mfma_f32_16x16x16_bf16 v[44:47], v[6:7], v[24:25], v[44:47]\n\t"
    "v_mfma_f32_16x16x16_bf16 v[48:51], v[8:9], v[24:25], v[48:51]\n\t"
    "s_nop 5\n\t"
    "v_rcp_f32 v26, v52\n\t"
    "s_nop 0\n\t"
    "v_pk_mul_f32 v[44:45], v[44:45], v[26:27] op_sel_hi:[1,0]\n\t"
    "v_pk_mul_f32 v[46:47], v[46:47], v[26:27] op_sel_hi:[1,0]\n\t"
    "v_pk_mul_f32 v[48:49], v[48:49], v[26:27] op_sel_hi:[1,0]\n\t"
    "v_pk_mul_f32 v[50:51], v[50:51], v[26:27] op_sel_hi:[1,0]\n\t"
    "v_cvt_pk_bf16_f32 v36, v44, v45\n\t"
    "v_cvt_pk_bf16_f32 v37, v46, v47\n\t"
    "v_cvt_pk_bf16_f32 v38, v48, v49\n\t"
    "v_cvt_pk_bf16_f32 v39, v50, v51\n\t"
    "v_min_u32 v36, -2, v36\n\t"
    "v_min_u32 v37, -2, v37\n\t"
    "v_min_u32 v38, -2, v38\n\t"
    "v_min_u32 v39, -2, v39\n\t"
    "global_store_dwordx4 v15, v[36:39], %[od] sc1\n\t"
    "s_barrier\n\t"
    "s_mov_b32 exec_hi, 0\n\t"
    "global_store_dwordx4 v14, v[6:9], %[kv] sc1\n\t"
    "s_mov_b32 exec_hi, -1\n\t"
    "s_nop 0\n\t"
    "s_waitcnt lgkmcnt(0)\n\t"
    "s_branch 9f\n\t"
    "1:\n\t"
    "s_add_u32 %[sp], %[sp], 1\n\t"
    "s_cmp_ge_u32 %[sp], %[cap]\n\t"
    "s_cbranch_scc1 7f\n\t"
    "global_load_dwordx4 v[40:43], v6, %[ag] sc1\n\t"
    "global_load_dwordx4 v[44:47], v6, %[ag] offset:1024 sc1\n\t"
    "s_waitcnt lgkmcnt(0)\n\t"
    "s_memtime %[tc]\n\t"
    "s_nop 7\n\t"
    "s_branch 3b\n\t"
    "7:\n\t"
    "s_mov_b32 %[cap], 0\n\t"
    "s_branch 2b\n\t"
    "4:\n\t"
    "s_add_u32 %[sp], %[sp], 0x10000\n\t"
    "s_lshr_b32 %[st], %[sp], 16\n\t"
    "s_cmp_ge_u32 %[st], %[cap]\n\t"
    "s_cbranch_scc1 8f\n\t"
    "s_mov_b32 exec_hi, 0\n\t"
    "global_load_dwordx4 v[6:9], v14, %[vs] sc1\n\t"
    "s_mov_b32 exec_hi, -1\n\t"
    "s_branch 5b\n\t"
    "8:\n\t"
    "s_mov_b32 %[cap], 0\n\t"
    "s_branch 6b\n\t"
    "10:\n\t"
    "v_max_f32 v11, v11, v10\n\t"
    "v_mov_b32 v26, v11\n\t"
    "s_nop 1\n\t"
    "v_permlane16_swap_b32 v11, v26\n\t"
    "v_max_f32 v11, v11, v26\n\t"
    "v_sub_f32 v28, v40, v11\n\t"
    "v_sub_f32 v29, v41, v11\n\t"
    "v_sub_f32 v30, v42, v11\n\t"
    "v_sub_f32 v31, v43, v11\n\t"
    "v_sub_f32 v11, v10, v11\n\t"
    "v_exp_f32 v11, v11\n\t"
    "v_exp_f32 v28, v28\n\t"
    "v_exp_f32 v29, v29\n\t"
    "v_exp_f32 v30, v30\n\t"
    "v_exp_f32 v31, v31\n\t"
    "v_mul_f32 v44, v44, v11\n\t"
    "v_mul_f32 v45, v45, v11\n\t"
    "v_mul_f32 v46, v46, v11\n\t"
    "v_mul_f32 v47, v47, v11\n\t"
    "v_mul_f32 v48, v48, v11\n\t"
    "v_mul_f32 v49, v49, v11\n\t"
    "v_mul_f32 v50, v50, v11\n\t"
    "v_mul_f32 v51, v51, v11\n\t"
    "v_mul_f32 v52, v52, v11\n\t"
    "v_mul_f32 v53, v53, v11\n\t"
    "v_mul_f32 v54, v54, v11\n\t"
    "v_mul_f32 v55, v55, v11\n\t"
    "s_branch 11b\n\t"
    "12:\n\t"
    "v_max3_f32 v10, v24, v25, v26\n\t"
    "v_max3_f32 v40, v27, v28, v29\n\t"
    "v_max3_f32 v10, v10, v30, v31\n\t"
    "v_max3_f32 v10, v10, v40, %[neg]\n\t"
    "v_mov_b32 v40, v10\n\t"
    "s_nop 1\n\t"
    "v_permlane16_swap_b32 v10, v40\n\t"
    "v_max_f32 v10, v10, v40\n\t"
    "v_mov_b32 v40, v10\n\t"
    "s_nop 1\n\t"
    "v_permlane32_swap_b32 v10, v40\n\t"
    "v_max_f32 v10, v10, v40\n\t"
    "v_sub_f32 v24, v24, v10\n\t"
    "v_sub_f32 v25, v25, v10\n\t"
    "v_sub_f32 v26, v26, v10\n\t"
    "v_sub_f32 v27, v27, v10\n\t"
    "v_sub_f32 v28, v28, v10\n\t"
    "v_sub_f32 v29, v29, v10\n\t"
    "v_sub_f32 v30, v30, v10\n\t"
    "v_sub_f32 v31, v31, v10\n\t"
    "s_mov_b32 %[bg], 0x7fc00000\n\t"
    "s_branch 13b\n\t"
    "9:"
    : [tc] "=&s"(tc), [sp] "=&s"(sp), [cap] "+s"(cap), [st] "=&s"(st), [bg] "=&s"(bg)
    : [ag] "s"(ag), [c44] "s"(0x4444u), [kv] "s"(kv), [vs] "s"(vs), [od] "s"(od), [od2] "s"(od + 262144), [neg] "s"(-1.0e38f), [lsc] "s"(LSC), [one] "s"(0x3F803F80u), [big] "s"(24.0f), "{v[64:95]}"(W.w0), "{v[96:127]}"(W.w1), "{v[128:159]}"(W.w2), "{v[160:191]}"(W.w3), "{v[192:223]}"(W.w4), "{v[224:255]}"(W.w5)
    : "v6", "v7", "v8", "v9", "v10", "v11", "v12", "v13", "v14", "v15", "v16", "v17", "v18", "v19", "v20", "v21", "v22", "v23", "v24", "v25", "v26", "v27", "v28", "v29", "v30", "v31", "v32", "v33", "v34", "v35", "v36", "v37", "v38", "v39", "v40", "v41", "v42", "v43", "v44", "v45", "v46", "v47", "v48", "v49", "v50", "v51", "v52", "v53", "v54", "v55", "v56", "v57", "v58", "v59", "v60", "v61", "v62", "v63", "vcc", "scc", "memory");
}
template <> __device__ __forceinline__ void qk_step<4>(const QKW& W, const char* ag, const char* vs, const char* od, const char* kv, unsigned& cap, unsigned& sp,
                                                        unsigned long long& tc, unsigned long long* ts, unsigned& d0, unsigned& d1) {
  unsigned st, bg;
  asm volatile("v_mbcnt_lo_u32_b32 v52, -1, 0\n\t"
    "v_mbcnt_hi_u32_b32 v52, -1, v52\n\t"
    "v_lshlrev_b32 v6, 4, v52\n\t"
    "s_mov_b32 %[sp], 0\n\t"
    "global_load_dwordx4 v[36:39], v6, %[ag] sc1\n\t"
    "global_load_dwordx4 v[40:43], v6, %[ag] offset:1024 sc1\n\t"
    "s_memtime %[tc]\n\t"
    "v_add_u32 v7, 73728, v6\n\t"
    "v_add_u32 v8, 131072, v6\n\t"
    "ds_read_b128 v[20:23], v7 offset:8192\n\t"
    "ds_read_b128 v[24:27], v7 offset:9216\n\t"
    "ds_read_b128 v[28:31], v7 offset:0\n\t"
    "ds_read_b128 v[32:35], v7 offset:1024\n\t"
    "v_lshrrev_b32 v44, 4, v52\n\t"
    "v_and_b32 v45, 15, v52\n\t"
    "v_and_b32 v53, 4, v52\n\t"
    "v_mul_u32_u24 v53, 0x2aaa, v53\n\t"
    "v_lshrrev_b32 v9, 1, v52\n\t"
    "v_and_b32 v9, 2, v9\n\t"
    "v_or3_b32 v9, v9, v53, %[c44]\n\t"
    "v_lshlrev_b32 v53, 2, v44\n\t"
    "v_lshl_or_b32 v10, v45, 4, v53\n\t"
    "3:\n\t"
    "v_mov_b32 v12, 0\n\t"
    "v_mov_b32 v13, 0\n\t"
    "v_mov_b32 v14, 0\n\t"
    "v_mov_b32 v15, 0\n\t"
    "v_mov_b32 v16, 0\n\t"
    "v_mov_b32 v17, 0\n\t"
    "v_mov_b32 v18, 0\n\t"
    "v_mov_b32 v19, 0\n\t"
    "s_waitcnt vmcnt(0) lgkmcnt(0)\n\t"
    "14:\n\t"
    "ds_write_b128 v6, v[36:39] offset:8192\n\t"
    "ds_write_b128 v6, v[40:43] offset:9216\n\t"
    "v_smfmac_f32_16x16x64_bf16 v[12:15], v[36:39], v[128:135], v9\n\t"
    "v_max3_u32 v52, v36, v37, v38\n\t"
    "v_max3_u32 v53, v39, v40, v41\n\t"
    "v_smfmac_f32_16x16x64_bf16 v[16:19], v[36:39], v[136:143], v9\n\t"
    "v_max3_u32 v52, v52, v42, v43\n\t"
    "v_max_u32 v52, v52, v53\n\t"
    "v_cmp_eq_u32 vcc, -1, v52\n\t"
    "s_cbranch_vccnz 1f\n\t"
    "2:\n\t"
    "s_waitcnt lgkmcnt(0)\n\t"
    "s_barrier\n\t"
    "s_memrealtime s[36:37]\n\t"
    "ds_read_b128 v[36:39], v6 offset:0\n\t"
    "ds_read_b128 v[44:47], v6 offset:4096\n\t"
    "v_smfmac_f32_16x16x64_bf16 v[12:15], v[40:43], v[144:151], v9\n\t"
    "ds_read_b128 v[48:51], v6 offset:5120\n\t"
    "ds_read_b128 v[52:55], v6 offset:6144\n\t"
    "v_smfmac_f32_16x16x64_bf16 v[16:19], v[40:43], v[152:159], v9\n\t"
    "ds_read_b128 v[40:43], v6 offset:1024\n\t"
    "s_waitcnt lgkmcnt(0)\n\t"
    "s_mov_b32 s38, s36\n\t"
    "s_min_u32 s37, %[d0], %[d1]\n\t"
    "s_add_u32 s36, s36, s37\n\t"
    "s_and_b32 s36, s36, 0xffffff\n\t"
    "s_or_b32 s36, s36, %[sn]\n\t"
    "v_lshlrev_b32 v11, 10, v6\n\t"
    "v_mov_b32 v5, s36\n\t"
    "s_mov_b64 exec, 15\n\t"
    "global_store_dword v11, v5, %[vs] offset:-4096 sc1\n\t"
    "s_mov_b64 exec, -1\n\t"
    "s_sleep 0\n\t"
    "v_smfmac_f32_16x16x64_bf16 v[12:15], v[36:39], v[56:63], v9\n\t"
    "v_smfmac_f32_16x16x64_bf16 v[16:19], v[36:39], v[28:35], v9\n\t"
    "ds_read_b128 v[28:31], v7 offset:16384\n\t"
    "ds_read_b128 v[32:35], v7 offset:17408\n\t"
    "ds_read_b128 v[36:39], v6 offset:7168\n\t"
    "v_smfmac_f32_16x16x64_bf16 v[12:15], v[44:47], v[64:71], v9\n\t"
    "v_smfmac_f32_16x16x64_bf16 v[16:19], v[44:47], v[72:79], v9\n\t"
    "ds_read_b128 v[44:47], v6 offset:10240\n\t"
    "v_smfmac_f32_16x16x64_bf16 v[12:15], v[48:51], v[80:87], v9\n\t"
    "v_smfmac_f32_16x16x64_bf16 v[16:19], v[48:51], v[88:95], v9\n\t"
    "ds_read_b128 v[48:51], v6 offset:11264\n\t"
    "v_smfmac_f32_16x16x64_bf16 v[12:15], v[52:55], v[96:103], v9\n\t"
    "v_smfmac_f32_16x16x64_bf16 v[16:19], v[52:55], v[104:111], v9\n\t"
    "ds_read_b128 v[52:55], v6 offset:2048\n\t"
    "v_smfmac_f32_16x16x64_bf16 v[12:15], v[40:43], v[20:27], v9\n\t"
    "ds_read_b128 v[20:23], v8 offset:0\n\t"
    "ds_read_b128 v[24:27], v8 offset:1024\n\t"
    "s_waitcnt lgkmcnt(6)\n\t"
    "v_smfmac_f32_16x16x64_bf16 v[16:19], v[40:43], v[28:35], v9\n\t"
    "ds_read_b128 v[28:31], v8 offset:8192\n\t"
    "ds_read_b128 v[32:35], v8 offset:9216\n\t"
    "ds_read_b128 v[40:43], v6 offset:12288\n\t"
    "s_waitcnt lgkmcnt(8)\n\t"
    "v_smfmac_f32_16x16x64_bf16 v[12:15], v[36:39], v[112:119], v9\n\t"
    "v_smfmac_f32_16x16x64_bf16 v[16:19], v[36:39], v[120:127], v9\n\t"
    "ds_read_b128 v[36:39], v6 offset:13312\n\t"
    "s_waitcnt lgkmcnt(8)\n\t"
    "v_smfmac_f32_16x16x64_bf16 v[12:15], v[44:47], v[160:167], v9\n\t"
    "v_smfmac_f32_16x16x64_bf16 v[16:19], v[44:47], v[168:175], v9\n\t"
    "ds_read_b128 v[44:47], v6 offset:14336\n\t"
    "s_barrier\n\t"
    "s_setprio 2\n\t"
    "s_waitcnt lgkmcnt(8)\n\t"
    "v_smfmac_f32_16x16x64_bf16 v[12:15], v[48:51], v[176:183], v9\n\t"
    "v_smfmac_f32_16x16x64_bf16 v[16:19], v[48:51], v[184:191], v9\n\t"
    "ds_read_b128 v[48:51], v6 offset:3072\n\t"
    "s_waitcnt lgkmcnt(6)\n\t"
    "v_smfmac_f32_16x16x64_bf16 v[12:15], v[52:55], v[20:27], v9\n\t"
    "ds_read_b128 v[20:23], v8 offset:16384\n\t"
    "ds_read_b128 v[24:27], v8 offset:17408\n\t"
    "s_waitcnt lgkmcnt(6)\n\t"
    "v_smfmac_f32_16x16x64_bf16 v[16:19], v[52:55], v[28:35], v9\n\t"
    "ds_read_b128 v[28:31], v8 offset:24576\n\t"
    "ds_read_b128 v[32:35], v8 offset:25600\n\t"
    "ds_read_b128 v[52:55], v6 offset:15360\n\t"
    "s_waitcnt lgkmcnt(8)\n\t"
    "v_smfmac_f32_16x16x64_bf16 v[12:15], v[40:43], v[192:199], v9\n\t"
    "v_smfmac_f32_16x16x64_bf16 v[16:19], v[40:43], v[200:207], v9\n\t"
    "s_waitcnt lgkmcnt(7)\n\t"
    "v_smfmac_f32_16x16x64_bf16 v[12:15], v[36:39], v[208:215], v9\n\t"
    "v_smfmac_f32_16x16x64_bf16 v[16:19], v[36:39], v[216:223], v9\n\t"
    "s_waitcnt lgkmcnt(6)\n\t"
    "v_smfmac_f32_16x16x64_bf16 v[12:15], v[44:47], v[224:231], v9\n\t"
    "v_smfmac_f32_16x16x64_bf16 v[16:19], v[44:47], v[232:239], v9\n\t"
    "s_waitcnt lgkmcnt(3)\n\t"
    "v_smfmac_f32_16x16x64_bf16 v[12:15], v[48:51], v[20:27], v9\n\t"
    "s_waitcnt lgkmcnt(1)\n\t"
    "v_smfmac_f32_16x16x64_bf16 v[16:19], v[48:51], v[28:35], v9\n\t"
    "s_waitcnt lgkmcnt(0)\n\t"
    "v_smfmac_f32_16x16x64_bf16 v[12:15], v[52:55], v[240:247], v9\n\t"
    "v_smfmac_f32_16x16x64_bf16 v[16:19], v[52:55], v[248:255], v9\n\t"
    "s_nop 6\n\t"
    "v_permlane16_swap_b32 v12, v14\n\t"
    "v_permlane16_swap_b32 v13, v15\n\t"
    "v_permlane16_swap_b32 v16, v18\n\t"
    "v_permlane16_swap_b32 v17, v19\n\t"
    "v_add_f32 v12, v12, v14\n\t"
    "v_add_f32 v13, v13, v15\n\t"
    "v_add_f32 v16, v16, v18\n\t"
    "v_cvt_pk_bf16_f32 v12, v12, v13\n\t"
    "v_add_f32 v17, v17, v19\n\t"
    "ds_write_b32 v10, v12 offset:34816\n\t"
    "v_cvt_pk_bf16_f32 v16, v16, v17\n\t"
    "ds_write_b32 v10, v16 offset:35072\n\t"
    "s_waitcnt lgkmcnt(0)\n\t"
    "s_setprio 0\n\t"
    "s_barrier\n\t"
    "s_memrealtime s[36:37]\n\t"
    "s_waitcnt lgkmcnt(0)\n\t"
    "s_and_b32 s36, s36, 0xffffff\n\t"
    "s_or_b32 s36, s36, %[sn]\n\t"
    "v_lshlrev_b32 v7, 10, v6\n\t"
    "v_mov_b32 v8, s36\n\t"
    "s_mov_b64 exec, 15\n\t"
    "global_store_dword v7, v8, %[vs] offset:0 sc1\n\t"
    "s_mov_b64 exec, -1\n\t"
    "s_barrier\n\t"
    "s_memrealtime s[36:37]\n\t"
    "s_mov_b32 %[d1], %[d0]\n\t"
    "s_waitcnt lgkmcnt(0)\n\t"
    "s_sub_u32 %[d0], s36, s38\n\t"
    "s_min_u32 %[d0], %[d0], 200\n\t"
    "s_waitcnt lgkmcnt(0)\n\t"
    "s_branch 9f\n\t"
    "1:\n\t"
    "s_add_u32 %[sp], %[sp], 1\n\t"
    "s_cmp_ge_u32 %[sp], %[cap]\n\t"
    "s_cbranch_scc1 7f\n\t"
    "global_load_dwordx4 v[36:39], v6, %[ag] sc1\n\t"
    "global_load_dwordx4 v[40:43], v6, %[ag] offset:1024 sc1\n\t"
    "s_waitcnt lgkmcnt(0)\n\t"
    "s_memtime %[tc]\n\t"
    "s_nop 7\n\t"
    "s_branch 3b\n\t"
    "7:\n\t"
    "s_mov_b32 %[cap], 0\n\t"
    "s_branch 2b\n\t"
    "9:"
    : [tc] "=&s"(tc), [sp] "=&s"(sp), [cap] "+s"(cap), [d0] "+s"(d0), [d1] "+s"(d1)
    : [ag] "s"(ag), [c44] "s"(0x4444u), [vs] "s"(vs), [sn] "s"((unsigned)(size_t)od), "{v[64:95]}"(W.w0), "{v[96:127]}"(W.w1), "{v[128:159]}"(W.w2), "{v[160:191]}"(W.w3), "{v[192:223]}"(W.w4), "{v[224:255]}"(W.w5), "{v[56:63]}"(W.x)
    : "v5", "v6", "v7", "v8", "v9", "v10", "v11", "v12", "v13", "v14", "v15", "v16", "v17", "v18", "v19", "v20", "v21", "v22", "v23", "v24", "v25", "v26", "v27", "v28", "v29", "v30", "v31", "v32", "v33", "v34", "v35", "v36", "v37", "v38", "v39", "v40", "v41", "v42", "v43", "v44", "v45", "v46", "v47", "v48", "v49", "v50", "v51", "v52", "v53", "v54", "v55", "vcc", "scc", "memory", "s36", "s37", "s38");
}
template <> __device__ __forceinline__ void qk_step<5>(const QKW& W, const char* ag, const char* vs, const char* od, const char* kv, unsigned& cap, unsigned& sp,
                                                        unsigned long long& tc, unsigned long long* ts, unsigned& d0, unsigned& d1) {
  unsigned st, bg;
  asm volatile("v_mbcnt_lo_u32_b32 v52, -1, 0\n\t"
    "v_mbcnt_hi_u32_b32 v52, -1, v52\n\t"
    "v_lshlrev_b32 v6, 4, v52\n\t"
    "s_mov_b32 %[sp], 0\n\t"
    "global_load_dwordx4 v[36:39], v6, %[ag] sc1\n\t"
    "global_load_dwordx4 v[40:43], v6, %[ag] offset:1024 sc1\n\t"
    "s_memtime %[tc]\n\t"
    "v_add_u32 v7, 75776, v6\n\t"
    "v_add_u32 v8, 133120, v6\n\t"
    "ds_read_b128 v[20:23], v7 offset:8192\n\t"
    "ds_read_b128 v[24:27], v7 offset:9216\n\t"
    "ds_read_b128 v[28:31], v7 offset:0\n\t"
    "ds_read_b128 v[32:35], v7 offset:1024\n\t"
    "v_lshrrev_b32 v44, 4, v52\n\t"
    "v_and_b32 v45, 15, v52\n\t"
    "v_and_b32 v53, 4, v52\n\t"
    "v_mul_u32_u24 v53, 0x2aaa, v53\n\t"
    "v_lshrrev_b32 v9, 1, v52\n\t"
    "v_and_b32 v9, 2, v9\n\t"
    "v_or3_b32 v9, v9, v53, %[c44]\n\t"
    "v_lshlrev_b32 v53, 2, v44\n\t"
    "v_lshl_or_b32 v10, v45, 4, v53\n\t"
    "v_and_b32 v53, 1, v52\n\t"
    "v_lshlrev_b32 v53, 3, v53\n\t"
    "v_lshl_or_b32 v11, v44, 7, v53\n\t"
    "v_and_b32 v53, 12, v52\n\t"
    "v_lshl_or_b32 v11, v53, 2, v11\n\t"
    "v_and_b32 v53, 2, v52\n\t"
    "v_lshl_or_b32 v11, v53, 8, v11\n\t"
    "3:\n\t"
    "v_mov_b32 v12, 0\n\t"
    "v_mov_b32 v13, 0\n\t"
    "v_mov_b32 v14, 0\n\t"
    "v_mov_b32 v15, 0\n\t"
    "v_mov_b32 v16, 0\n\t"
    "v_mov_b32 v17, 0\n\t"
    "v_mov_b32 v18, 0\n\t"
    "v_mov_b32 v19, 0\n\t"
    "s_waitcnt vmcnt(0) lgkmcnt(0)\n\t"
    "14:\n\t"
    "ds_write_b128 v6, v[36:39] offset:10240\n\t"
    "ds_write_b128 v6, v[40:43] offset:11264\n\t"
    "v_smfmac_f32_16x16x64_bf16 v[12:15], v[36:39], v[160:167], v9\n\t"
    "v_max3_u32 v52, v36, v37, v38\n\t"
    "v_max3_u32 v53, v39, v40, v41\n\t"
    "v_smfmac_f32_16x16x64_bf16 v[16:19], v[36:39], v[168:175], v9\n\t"
    "v_max3_u32 v52, v52, v42, v43\n\t"
    "v_max_u32 v52, v52, v53\n\t"
    "v_cmp_eq_u32 vcc, -1, v52\n\t"
    "s_cbranch_vccnz 1f\n\t"
    "2:\n\t"
    "s_waitcnt lgkmcnt(0)\n\t"
    "s_barrier\n\t"
    "ds_read_b128 v[36:39], v6 offset:0\n\t"
    "ds_read_b128 v[44:47], v6 offset:4096\n\t"
    "v_smfmac_f32_16x16x64_bf16 v[12:15], v[40:43], v[176:183], v9\n\t"
    "ds_read_b128 v[48:51], v6 offset:5120\n\t"
    "ds_read_b128 v[52:55], v6 offset:6144\n\t"
    "v_smfmac_f32_16x16x64_bf16 v[16:19], v[40:43], v[184:191], v9\n\t"
    "ds_read_b128 v[40:43], v6 offset:1024\n\t"
    "s_sleep 0\n\t"
    "s_waitcnt lgkmcnt(4)\n\t"
    "v_smfmac_f32_16x16x64_bf16 v[12:15], v[36:39], v[56:63], v9\n\t"
    "v_smfmac_f32_16x16x64_bf16 v[16:19], v[36:39], v[28:35], v9\n\t"
    "ds_read_b128 v[28:31], v7 offset:16384\n\t"
    "ds_read_b128 v[32:35], v7 offset:17408\n\t"
    "ds_read_b128 v[36:39], v6 offset:7168\n\t"
    "s_waitcnt lgkmcnt(6)\n\t"
    "v_smfmac_f32_16x16x64_bf16 v[12:15], v[44:47], v[64:71], v9\n\t"
    "v_smfmac_f32_16x16x64_bf16 v[16:19], v[44:47], v[72:79], v9\n\t"
    "ds_read_b128 v[44:47], v6 offset:8192\n\t"
    "s_waitcnt lgkmcnt(6)\n\t"
    "v_smfmac_f32_16x16x64_bf16 v[12:15], v[48:51], v[80:87], v9\n\t"
    "v_smfmac_f32_16x16x64_bf16 v[16:19], v[48:51], v[88:95], v9\n\t"
    "ds_read_b128 v[48:51], v6 offset:9216\n\t"
    "s_waitcnt lgkmcnt(6)\n\t"
    "v_smfmac_f32_16x16x64_bf16 v[12:15], v[52:55], v[96:103], v9\n\t"
    "v_smfmac_f32_16x16x64_bf16 v[16:19], v[52:55], v[104:111], v9\n\t"
    "ds_read_b128 v[52:55], v6 offset:2048\n\t"
    "s_waitcnt lgkmcnt(6)\n\t"
    "v_smfmac_f32_16x16x64_bf16 v[12:15], v[40:43], v[20:27], v9\n\t"
    "ds_read_b128 v[20:23], v8 offset:0\n\t"
    "ds_read_b128 v[24:27], v8 offset:1024\n\t"
    "s_waitcnt lgkmcnt(6)\n\t"
    "v_smfmac_f32_16x16x64_bf16 v[16:19], v[40:43], v[28:35], v9\n\t"
    "ds_read_b128 v[28:31], v8 offset:8192\n\t"
    "ds_read_b128 v[32:35], v8 offset:9216\n\t"
    "ds_read_b128 v[40:43], v6 offset:12288\n\t"
    "s_waitcnt lgkmcnt(8)\n\t"
    "v_smfmac_f32_16x16x64_bf16 v[12:15], v[36:39], v[112:119], v9\n\t"
    "v_smfmac_f32_16x16x64_bf16 v[16:19], v[36:39], v[120:127], v9\n\t"
    "ds_read_b128 v[36:39], v6 offset:13312\n\t"
    "s_waitcnt lgkmcnt(8)\n\t"
    "v_smfmac_f32_16x16x64_bf16 v[12:15], v[44:47], v[128:135], v9\n\t"
    "v_smfmac_f32_16x16x64_bf16 v[16:19], v[44:47], v[136:143], v9\n\t"
    "ds_read_b128 v[44:47], v6 offset:14336\n\t"
    "s_barrier\n\t"
    "s_setprio 2\n\t"
    "s_waitcnt lgkmcnt(8)\n\t"
    "v_smfmac_f32_16x16x64_bf16 v[12:15], v[48:51], v[144:151], v9\n\t"
    "v_smfmac_f32_16x16x64_bf16 v[16:19], v[48:51], v[152:159], v9\n\t"
    "ds_read_b128 v[48:51], v6 offset:3072\n\t"
    "s_waitcnt lgkmcnt(6)\n\t"
    "v_smfmac_f32_16x16x64_bf16 v[12:15], v[52:55], v[20:27], v9\n\t"
    "ds_read_b128 v[20:23], v8 offset:16384\n\t"
    "ds_read_b128 v[24:27], v8 offset:17408\n\t"
    "s_waitcnt lgkmcnt(6)\n\t"
    "v_smfmac_f32_16x16x64_bf16 v[16:19], v[52:55], v[28:35], v9\n\t"
    "ds_read_b128 v[28:31], v8 offset:24576\n\t"
    "ds_read_b128 v[32:35], v8 offset:25600\n\t"
    "ds_read_b128 v[52:55], v6 offset:15360\n\t"
    "s_waitcnt lgkmcnt(8)\n\t"
    "v_smfmac_f32_16x16x64_bf16 v[12:15], v[40:43], v[192:199], v9\n\t"
    "v_smfmac_f32_16x16x64_bf16 v[16:19], v[40:43], v[200:207], v9\n\t"
    "s_waitcnt lgkmcnt(7)\n\t"
    "v_smfmac_f32_16x16x64_bf16 v[12:15], v[36:39], v[208:215], v9\n\t"
    "v_smfmac_f32_16x16x64_bf16 v[16:19], v[36:39], v[216:223], v9\n\t"
    "s_waitcnt lgkmcnt(6)\n\t"
    "v_smfmac_f32_16x16x64_bf16 v[12:15], v[44:47], v[224:231], v9\n\t"
    "v_smfmac_f32_16x16x64_bf16 v[16:19], v[44:47], v[232:239], v9\n\t"
    "s_waitcnt lgkmcnt(3)\n\t"
    "v_smfmac_f32_16x16x64_bf16 v[12:15], v[48:51], v[20:27], v9\n\t"
    "s_waitcnt lgkmcnt(1)\n\t"
    "v_smfmac_f32_16x16x64_bf16 v[16:19], v[48:51], v[28:35], v9\n\t"
    "s_waitcnt lgkmcnt(0)\n\t"
    "v_smfmac_f32_16x16x64_bf16 v[12:15], v[52:55], v[240:247], v9\n\t"
    "v_smfmac_f32_16x16x64_bf16 v[16:19], v[52:55], v[248:255], v9\n\t"
    "s_nop 6\n\t"
    "v_permlane16_swap_b32 v12, v14\n\t"
    "v_permlane16_swap_b32 v13, v15\n\t"
    "v_permlane16_swap_b32 v16, v18\n\t"
    "v_permlane16_swap_b32 v17, v19\n\t"
    "v_add_f32 v12, v12, v14\n\t"
    "v_add_f32 v13, v13, v15\n\t"
    "v_add_f32 v16, v16, v18\n\t"
    "v_cvt_pk_bf16_f32 v12, v12, v13\n\t"
    "v_add_f32 v17, v17, v19\n\t"
    "ds_write_b32 v10, v12 offset:35328\n\t"
    "v_cvt_pk_bf16_f32 v16, v16, v17\n\t"
    "ds_write_b32 v10, v16 offset:35584\n\t"
    "s_waitcnt lgkmcnt(0)\n\t"
    "s_setprio 0\n\t"
    "s_barrier\n\t"
    "ds_read_b64_tr_b16 v[36:37], v11 offset:34816\n\t"
    "ds_read_b64_tr_b16 v[38:39], v11 offset:34880\n\t"
    "ds_read_b64_tr_b16 v[40:41], v11 offset:35840\n\t"
    "ds_read_b64_tr_b16 v[42:43], v11 offset:35904\n\t"
    "s_waitcnt lgkmcnt(0)\n\t"
    "v_min_u32 v36, -2, v36\n\t"
    "v_min_u32 v37, -2, v37\n\t"
    "v_min_u32 v38, -2, v38\n\t"
    "v_min_u32 v39, -2, v39\n\t"
    "v_min_u32 v40, -2, v40\n\t"
    "v_min_u32 v41, -2, v41\n\t"
    "v_min_u32 v42, -2, v42\n\t"
    "v_min_u32 v43, -2, v43\n\t"
    "global_store_dwordx4 v6, v[36:39], %[vs] sc1\n\t"
    "global_store_dwordx4 v6, v[40:43], %[vs] offset:1024 sc1\n\t"
    "s_nop 1\n\t"
    "s_barrier\n\t"
    "s_waitcnt lgkmcnt(0)\n\t"
    "s_branch 9f\n\t"
    "1:\n\t"
    "s_add_u32 %[sp], %[sp], 1\n\t"
    "s_cmp_ge_u32 %[sp], %[cap]\n\t"
    "s_cbranch_scc1 7f\n\t"
    "global_load_dwordx4 v[36:39], v6, %[ag] sc1\n\t"
    "global_load_dwordx4 v[40:43], v6, %[ag] offset:1024 sc1\n\t"
    "s_waitcnt lgkmcnt(0)\n\t"
    "s_memtime %[tc]\n\t"
    "s_nop 7\n\t"
    "s_branch 3b\n\t"
    "7:\n\t"
    "s_mov_b32 %[cap], 0\n\t"
    "s_branch 2b\n\t"
    "9:"
    : [tc] "=&s"(tc), [sp] "=&s"(sp), [cap] "+s"(cap)
    : [ag] "s"(ag), [c44] "s"(0x4444u), [vs] "s"(vs), "{v[64:95]}"(W.w0), "{v[96:127]}"(W.w1), "{v[128:159]}"(W.w2), "{v[160:191]}"(W.w3), "{v[192:223]}"(W.w4), "{v[224:255]}"(W.w5), "{v[56:63]}"(W.x)
    : "v6", "v7", "v8", "v9", "v10", "v11", "v12", "v13", "v14", "v15", "v16", "v17", "v18", "v19", "v20", "v21", "v22", "v23", "v24", "v25", "v26", "v27", "v28", "v29", "v30", "v31", "v32", "v33", "v34", "v35", "v36", "v37", "v38", "v39", "v40", "v41", "v42", "v43", "v44", "v45", "v46", "v47", "v48", "v49", "v50", "v51", "v52", "v53", "v54", "v55", "vcc", "scc", "memory");
}
template <> __device__ __forceinline__ void qk_step<6>(const QKW& W, const char* ag, const char* vs, const char* od, const char* kv, unsigned& cap, unsigned& sp,
                                                        unsigned long long& tc, unsigned long long* ts, unsigned& d0, unsigned& d1) {
  unsigned st, bg;
  asm volatile("v_mbcnt_lo_u32_b32 v52, -1, 0\n\t"
    "v_mbcnt_hi_u32_b32 v52, -1, v52\n\t"
    "v_lshlrev_b32 v6, 4, v52\n\t"
    "s_mov_b32 %[sp], 0\n\t"
    "global_load_dwordx4 v[36:39], v6, %[ag] sc1\n\t"
    "global_load_dwordx4 v[40:43], v6, %[ag] offset:1024 sc1\n\t"
    "s_memtime %[tc]\n\t"
    "v_add_u32 v7, 77824, v6\n\t"
    "v_add_u32 v8, 135168, v6\n\t"
    "ds_read_b128 v[20:23], v7 offset:8192\n\t"
    "ds_read_b128 v[24:27], v7 offset:9216\n\t"
    "ds_read_b128 v[28:31], v7 offset:0\n\t"
    "ds_read_b128 v[32:35], v7 offset:1024\n\t"
    "v_lshrrev_b32 v44, 4, v52\n\t"
    "v_and_b32 v45, 15, v52\n\t"
    "v_and_b32 v53, 4, v52\n\t"
    "v_mul_u32_u24 v53, 0x2aaa, v53\n\t"
    "v_lshrrev_b32 v9, 1, v52\n\t"
    "v_and_b32 v9, 2, v9\n\t"
    "v_or3_b32 v9, v9, v53, %[c44]\n\t"
    "v_lshlrev_b32 v53, 2, v44\n\t"
    "v_lshl_or_b32 v10, v45, 4, v53\n\t"
    "3:\n\t"
    "v_mov_b32 v12, 0\n\t"
    "v_mov_b32 v13, 0\n\t"
    "v_mov_b32 v14, 0\n\t"
    "v_mov_b32 v15, 0\n\t"
    "v_mov_b32 v16, 0\n\t"
    "v_mov_b32 v17, 0\n\t"
    "v_mov_b32 v18, 0\n\t"
    "v_mov_b32 v19, 0\n\t"
    "s_waitcnt vmcnt(0) lgkmcnt(0)\n\t"
    "14:\n\t"
    "ds_write_b128 v6, v[36:39] offset:12288\n\t"
    "ds_write_b128 v6, v[40:43] offset:13312\n\t"
    "v_smfmac_f32_16x16x64_bf16 v[12:15], v[36:39], v[192:199], v9\n\t"
    "v_max3_u32 v52, v36, v37, v38\n\t"
    "v_max3_u32 v53, v39, v40, v41\n\t"
    "v_smfmac_f32_16x16x64_bf16 v[16:19], v[36:39], v[200:207], v9\n\t"
    "v_max3_u32 v52, v52, v42, v43\n\t"
    "v_max_u32 v52, v52, v53\n\t"
    "v_cmp_eq_u32 vcc, -1, v52\n\t"
    "s_cbranch_vccnz 1f\n\t"
    "2:\n\t"
    "s_waitcnt lgkmcnt(0)\n\t"
    "s_barrier\n\t"
    "ds_read_b128 v[36:39], v6 offset:0\n\t"
    "ds_read_b128 v[44:47], v6 offset:4096\n\t"
    "v_smfmac_f32_16x16x64_bf16 v[12:15], v[40:43], v[208:215], v9\n\t"
    "ds_read_b128 v[48:51], v6 offset:5120\n\t"
    "ds_read_b128 v[52:55], v6 offset:6144\n\t"
    "v_smfmac_f32_16x16x64_bf16 v[16:19], v[40:43], v[216:223], v9\n\t"
    "ds_read_b128 v[40:43], v6 offset:1024\n\t"
    "s_sleep 0\n\t"
    "s_waitcnt lgkmcnt(4)\n\t"
    "v_smfmac_f32_16x16x64_bf16 v[12:15], v[36:39], v[56:63], v9\n\t"
    "v_smfmac_f32_16x16x64_bf16 v[16:19], v[36:39], v[28:35], v9\n\t"
    "ds_read_b128 v[28:31], v7 offset:16384\n\t"
    "ds_read_b128 v[32:35], v7 offset:17408\n\t"
    "ds_read_b128 v[36:39], v6 offset:7168\n\t"
    "s_waitcnt lgkmcnt(6)\n\t"
    "v_smfmac_f32_16x16x64_bf16 v[12:15], v[44:47], v[64:71], v9\n\t"
    "v_smfmac_f32_16x16x64_bf16 v[16:19], v[44:47], v[72:79], v9\n\t"
    "ds_read_b128 v[44:47], v6 offset:8192\n\t"
    "s_waitcnt lgkmcnt(6)\n\t"
    "v_smfmac_f32_16x16x64_bf16 v[12:15], v[48:51], v[80:87], v9\n\t"
    "v_smfmac_f32_16x16x64_bf16 v[16:19], v[48:51], v[88:95], v9\n\t"
    "ds_read_b128 v[48:51], v6 offset:9216\n\t"
    "s_waitcnt lgkmcnt(6)\n\t"
    "v_smfmac_f32_16x16x64_bf16 v[12:15], v[52:55], v[96:103], v9\n\t"
    "v_smfmac_f32_16x16x64_bf16 v[16:19], v[52:55], v[104:111], v9\n\t"
    "ds_read_b128 v[52:55], v6 offset:2048\n\t"
    "s_waitcnt lgkmcnt(6)\n\t"
    "v_smfmac_f32_16x16x64_bf16 v[12:15], v[40:43], v[20:27], v9\n\t"
    "ds_read_b128 v[20:23], v8 offset:0\n\t"
    "ds_read_b128 v[24:27], v8 offset:1024\n\t"
    "s_waitcnt lgkmcnt(6)\n\t"
    "v_smfmac_f32_16x16x64_bf16 v[16:19], v[40:43], v[28:35], v9\n\t"
    "ds_read_b128 v[28:31], v8 offset:8192\n\t"
    "ds_read_b128 v[32:35], v8 offset:9216\n\t"
    "ds_read_b128 v[40:43], v6 offset:10240\n\t"
    "s_waitcnt lgkmcnt(8)\n\t"
    "v_smfmac_f32_16x16x64_bf16 v[12:15], v[36:39], v[112:119], v9\n\t"
    "v_smfmac_f32_16x16x64_bf16 v[16:19], v[36:39], v[120:127], v9\n\t"
    "ds_read_b128 v[36:39], v6 offset:11264\n\t"
    "s_waitcnt lgkmcnt(8)\n\t"
    "v_smfmac_f32_16x16x64_bf16 v[12:15], v[44:47], v[128:135], v9\n\t"
    "v_smfmac_f32_16x16x64_bf16 v[16:19], v[44:47], v[136:143], v9\n\t"
    "ds_read_b128 v[44:47], v6 offset:14336\n\t"
    "s_barrier\n\t"
    "s_setprio 2\n\t"
    "s_waitcnt lgkmcnt(8)\n\t"
    "v_smfmac_f32_16x16x64_bf16 v[12:15], v[48:51], v[144:151], v9\n\t"
    "v_smfmac_f32_16x16x64_bf16 v[16:19], v[48:51], v[152:159], v9\n\t"
    "ds_read_b128 v[48:51], v6 offset:3072\n\t"
    "s_waitcnt lgkmcnt(6)\n\t"
    "v_smfmac_f32_16x16x64_bf16 v[12:15], v[52:55], v[20:27], v9\n\t"
    "ds_read_b128 v[20:23], v8 offset:16384\n\t"
    "ds_read_b128 v[24:27], v8 offset:17408\n\t"
    "s_waitcnt lgkmcnt(6)\n\t"
    "v_smfmac_f32_16x16x64_bf16 v[16:19], v[52:55], v[28:35], v9\n\t"
    "ds_read_b128 v[28:31], v8 offset:24576\n\t"
    "ds_read_b128 v[32:35], v8 offset:25600\n\t"
    "ds_read_b128 v[52:55], v6 offset:15360\n\t"
    "s_waitcnt lgkmcnt(8)\n\t"
    "v_smfmac_f32_16x16x64_bf16 v[12:15], v[40:43], v[160:167], v9\n\t"
    "v_smfmac_f32_16x16x64_bf16 v[16:19], v[40:43], v[168:175], v9\n\t"
    "s_waitcnt lgkmcnt(7)\n\t"
    "v_smfmac_f32_16x16x64_bf16 v[12:15], v[36:39], v[176:183], v9\n\t"
    "v_smfmac_f32_16x16x64_bf16 v[16:19], v[36:39], v[184:191], v9\n\t"
    "s_waitcnt lgkmcnt(6)\n\t"
    "v_smfmac_f32_16x16x64_bf16 v[12:15], v[44:47], v[224:231], v9\n\t"
    "v_smfmac_f32_16x16x64_bf16 v[16:19], v[44:47], v[232:239], v9\n\t"
    "s_waitcnt lgkmcnt(3)\n\t"
    "v_smfmac_f32_16x16x64_bf16 v[12:15], v[48:51], v[20:27], v9\n\t"
    "s_waitcnt lgkmcnt(1)\n\t"
    "v_smfmac_f32_16x16x64_bf16 v[16:19], v[48:51], v[28:35], v9\n\t"
    "s_waitcnt lgkmcnt(0)\n\t"
    "v_smfmac_f32_16x16x64_bf16 v[12:15], v[52:55], v[240:247], v9\n\t"
    "v_smfmac_f32_16x16x64_bf16 v[16:19], v[52:55], v[248:255], v9\n\t"
    "s_nop 6\n\t"
    "v_permlane16_swap_b32 v12, v14\n\t"
    "v_permlane16_swap_b32 v13, v15\n\t"
    "v_permlane16_swap_b32 v16, v18\n\t"
    "v_permlane16_swap_b32 v17, v19\n\t"
    "v_add_f32 v12, v12, v14\n\t"
    "v_add_f32 v13, v13, v15\n\t"
    "v_add_f32 v16, v16, v18\n\t"
    "v_cvt_pk_bf16_f32 v12, v12, v13\n\t"
    "v_add_f32 v17, v17, v19\n\t"
    "ds_write_b32 v10, v12 offset:35840\n\t"
    "v_cvt_pk_bf16_f32 v16, v16, v17\n\t"
    "ds_write_b32 v10, v16 offset:36096\n\t"
    "s_waitcnt lgkmcnt(0)\n\t"
    "s_setprio 0\n\t"
    "s_barrier\n\t"
    "s_barrier\n\t"
    "s_waitcnt lgkmcnt(0)\n\t"
    "s_branch 9f\n\t"
    "1:\n\t"
    "s_add_u32 %[sp], %[sp], 1\n\t"
    "s_cmp_ge_u32 %[sp], %[cap]\n\t"
    "s_cbranch_scc1 7f\n\t"
    "global_load_dwordx4 v[36:39], v6, %[ag] sc1\n\t"
    "global_load_dwordx4 v[40:43], v6, %[ag] offset:1024 sc1\n\t"
    "s_waitcnt lgkmcnt(0)\n\t"
    "s_memtime %[tc]\n\t"
    "s_nop 7\n\t"
    "s_branch 3b\n\t"
    "7:\n\t"
    "s_mov_b32 %[cap], 0\n\t"
    "s_branch 2b\n\t"
    "9:"
    : [tc] "=&s"(tc), [sp] "=&s"(sp), [cap] "+s"(cap)
    : [ag] "s"(ag), [c44] "s"(0x4444u), "{v[64:95]}"(W.w0), "{v[96:127]}"(W.w1), "{v[128:159]}"(W.w2), "{v[160:191]}"(W.w3), "{v[192:223]}"(W.w4), "{v[224:255]}"(W.w5), "{v[56:63]}"(W.x)
    : "v6", "v7", "v8", "v9", "v10", "v11", "v12", "v13", "v14", "v15", "v16", "v17", "v18", "v19", "v20", "v21", "v22", "v23", "v24", "v25", "v26", "v27", "v28", "v29", "v30", "v31", "v32", "v33", "v34", "v35", "v36", "v37", "v38", "v39", "v40", "v41", "v42", "v43", "v44", "v45", "v46", "v47", "v48", "v49", "v50", "v51", "v52", "v53", "v54", "v55", "vcc", "scc", "memory");
}
template <> __device__ __forceinline__ void qk_step<7>(const QKW& W, const char* ag, const char* vs, const char* od, const char* kv, unsigned& cap, unsigned& sp,
                                                        unsigned long long& tc, unsigned long long* ts, unsigned& d0, unsigned& d1) {
  unsigned st, bg;
  asm volatile("v_mbcnt_lo_u32_b32 v52, -1, 0\n\t"
    "v_mbcnt_hi_u32_b32 v52, -1, v52\n\t"
    "v_lshlrev_b32 v6, 4, v52\n\t"
    "s_mov_b32 %[sp], 0\n\t"
    "global_load_dwordx4 v[36:39], v6, %[ag] sc1\n\t"
    "global_load_dwordx4 v[40:43], v6, %[ag] offset:1024 sc1\n\t"
    "s_memtime %[tc]\n\t"
    "v_add_u32 v7, 79872, v6\n\t"
    "v_add_u32 v8, 137216, v6\n\t"
    "ds_read_b128 v[20:23], v7 offset:8192\n\t"
    "ds_read_b128 v[24:27], v7 offset:9216\n\t"
    "ds_read_b128 v[28:31], v7 offset:0\n\t"
    "ds_read_b128 v[32:35], v7 offset:1024\n\t"
    "v_lshrrev_b32 v44, 4, v52\n\t"
    "v_and_b32 v45, 15, v52\n\t"
    "v_and_b32 v53, 4, v52\n\t"
    "v_mul_u32_u24 v53, 0x2aaa, v53\n\t"
    "v_lshrrev_b32 v9, 1, v52\n\t"
    "v_and_b32 v9, 2, v9\n\t"
    "v_or3_b32 v9, v9, v53, %[c44]\n\t"
    "v_lshlrev_b32 v53, 2, v44\n\t"
    "v_lshl_or_b32 v10, v45, 4, v53\n\t"
    "3:\n\t"
    "v_mov_b32 v12, 0\n\t"
    "v_mov_b32 v13, 0\n\t"
    "v_mov_b32 v14, 0\n\t"
    "v_mov_b32 v15, 0\n\t"
    "v_mov_b32 v16, 0\n\t"
    "v_mov_b32 v17, 0\n\t"
    "v_mov_b32 v18, 0\n\t"
    "v_mov_b32 v19, 0\n\t"
    "s_waitcnt vmcnt(0) lgkmcnt(0)\n\t"
    "14:\n\t"
    "ds_write_b128 v6, v[36:39] offset:14336\n\t"
    "ds_write_b128 v6, v[40:43] offset:15360\n\t"
    "v_smfmac_f32_16x16x64_bf16 v[12:15], v[36:39], v[224:231], v9\n\t"
    "v_max3_u32 v52, v36, v37, v38\n\t"
    "v_max3_u32 v53, v39, v40, v41\n\t"
    "v_smfmac_f32_16x16x64_bf16 v[16:19], v[36:39], v[232:239], v9\n\t"
    "v_max3_u32 v52, v52, v42, v43\n\t"
    "v_max_u32 v52, v52, v53\n\t"
    "v_cmp_eq_u32 vcc, -1, v52\n\t"
    "s_cbranch_vccnz 1f\n\t"
    "2:\n\t"
    "s_waitcnt lgkmcnt(0)\n\t"
    "s_barrier\n\t"
    "ds_read_b128 v[36:39], v6 offset:0\n\t"
    "ds_read_b128 v[44:47], v6 offset:4096\n\t"
    "v_smfmac_f32_16x16x64_bf16 v[12:15], v[40:43], v[240:247], v9\n\t"
    "ds_read_b128 v[48:51], v6 offset:5120\n\t"
    "ds_read_b128 v[52:55], v6 offset:6144\n\t"
    "v_smfmac_f32_16x16x64_bf16 v[16:19], v[40:43], v[248:255], v9\n\t"
    "ds_read_b128 v[40:43], v6 offset:1024\n\t"
    "s_sleep 0\n\t"
    "s_waitcnt lgkmcnt(4)\n\t"
    "v_smfmac_f32_16x16x64_bf16 v[12:15], v[36:39], v[56:63], v9\n\t"
    "v_smfmac_f32_16x16x64_bf16 v[16:19], v[36:39], v[28:35], v9\n\t"
    "ds_read_b128 v[28:31], v7 offset:16384\n\t"
    "ds_read_b128 v[32:35], v7 offset:17408\n\t"
    "ds_read_b128 v[36:39], v6 offset:7168\n\t"
    "s_waitcnt lgkmcnt(6)\n\t"
    "v_smfmac_f32_16x16x64_bf16 v[12:15], v[44:47], v[64:71], v9\n\t"
    "v_smfmac_f32_16x16x64_bf16 v[16:19], v[44:47], v[72:79], v9\n\t"
    "ds_read_b128 v[44:47], v6 offset:8192\n\t"
    "s_waitcnt lgkmcnt(6)\n\t"
    "v_smfmac_f32_16x16x64_bf16 v[12:15], v[48:51], v[80:87], v9\n\t"
    "v_smfmac_f32_16x16x64_bf16 v[16:19], v[48:51], v[88:95], v9\n\t"
    "ds_read_b128 v[48:51], v6 offset:9216\n\t"
    "s_waitcnt lgkmcnt(6)\n\t"
    "v_smfmac_f32_16x16x64_bf16 v[12:15], v[52:55], v[96:103], v9\n\t"
    "v_smfmac_f32_16x16x64_bf16 v[16:19], v[52:55], v[104:111], v9\n\t"
    "ds_read_b128 v[52:55], v6 offset:2048\n\t"
    "s_waitcnt lgkmcnt(6)\n\t"
    "v_smfmac_f32_16x16x64_bf16 v[12:15], v[40:43], v[20:27], v9\n\t"
    "ds_read_b128 v[20:23], v8 offset:0\n\t"
    "ds_read_b128 v[24:27], v8 offset:1024\n\t"
    "s_waitcnt lgkmcnt(6)\n\t"
    "v_smfmac_f32_16x16x64_bf16 v[16:19], v[40:43], v[28:35], v9\n\t"
    "ds_read_b128 v[28:31], v8 offset:8192\n\t"
    "ds_read_b128 v[32:35], v8 offset:9216\n\t"
    "ds_read_b128 v[40:43], v6 offset:10240\n\t"
    "s_waitcnt lgkmcnt(8)\n\t"
    "v_smfmac_f32_16x16x64_bf16 v[12:15], v[36:39], v[112:119], v9\n\t"
    "v_smfmac_f32_16x16x64_bf16 v[16:19], v[36:39], v[120:127], v9\n\t"
    "ds_read_b128 v[36:39], v6 offset:11264\n\t"
    "s_waitcnt lgkmcnt(8)\n\t"
    "v_smfmac_f32_16x16x64_bf16 v[12:15], v[44:47], v[128:135], v9\n\t"
    "v_smfmac_f32_16x16x64_bf16 v[16:19], v[44:47], v[136:143], v9\n\t"
    "ds_read_b128 v[44:47], v6 offset:12288\n\t"
    "s_barrier\n\t"
    "s_setprio 2\n\t"
    "s_waitcnt lgkmcnt(8)\n\t"
    "v_smfmac_f32_16x16x64_bf16 v[12:15], v[48:51], v[144:151], v9\n\t"
    "v_smfmac_f32_16x16x64_bf16 v[16:19], v[48:51], v[152:159], v9\n\t"
    "ds_read_b128 v[48:51], v6 offset:3072\n\t"
    "s_waitcnt lgkmcnt(6)\n\t"
    "v_smfmac_f32_16x16x64_bf16 v[12:15], v[52:55], v[20:27], v9\n\t"
    "ds_read_b128 v[20:23], v8 offset:16384\n\t"
    "ds_read_b128 v[24:27], v8 offset:17408\n\t"
    "s_waitcnt lgkmcnt(6)\n\t"
    "v_smfmac_f32_16x16x64_bf16 v[16:19], v[52:55], v[28:35], v9\n\t"
    "ds_read_b128 v[28:31], v8 offset:24576\n\t"
    "ds_read_b128 v[32:35], v8 offset:25600\n\t"
    "ds_read_b128 v[52:55], v6 offset:13312\n\t"
    "s_waitcnt lgkmcnt(8)\n\t"
    "v_smfmac_f32_16x16x64_bf16 v[12:15], v[40:43], v[160:167], v9\n\t"
    "v_smfmac_f32_16x16x64_bf16 v[16:19], v[40:43], v[168:175], v9\n\t"
    "s_waitcnt lgkmcnt(7)\n\t"
    "v_smfmac_f32_16x16x64_bf16 v[12:15], v[36:39], v[176:183], v9\n\t"
    "v_smfmac_f32_16x16x64_bf16 v[16:19], v[36:39], v[184:191], v9\n\t"
    "s_waitcnt lgkmcnt(6)\n\t"
    "v_smfmac_f32_16x16x64_bf16 v[12:15], v[44:47], v[192:199], v9\n\t"
    "v_smfmac_f32_16x16x64_bf16 v[16:19], v[44:47], v[200:207], v9\n\t"
    "s_waitcnt lgkmcnt(3)\n\t"
    "v_smfmac_f32_16x16x64_bf16 v[12:15], v[48:51], v[20:27], v9\n\t"
    "s_waitcnt lgkmcnt(1)\n\t"
    "v_smfmac_f32_16x16x64_bf16 v[16:19], v[48:51], v[28:35], v9\n\t"
    "s_waitcnt lgkmcnt(0)\n\t"
    "v_smfmac_f32_16x16x64_bf16 v[12:15], v[52:55], v[208:215], v9\n\t"
    "v_smfmac_f32_16x16x64_bf16 v[16:19], v[52:55], v[216:223], v9\n\t"
    "s_nop 6\n\t"
    "v_permlane16_swap_b32 v12, v14\n\t"
    "v_permlane16_swap_b32 v13, v15\n\t"
    "v_permlane16_swap_b32 v16, v18\n\t"
    "v_permlane16_swap_b32 v17, v19\n\t"
    "v_add_f32 v12, v12, v14\n\t"
    "v_add_f32 v13, v13, v15\n\t"
    "v_add_f32 v16, v16, v18\n\t"
    "v_cvt_pk_bf16_f32 v12, v12, v13\n\t"
    "v_add_f32 v17, v17, v19\n\t"
    "ds_write_b32 v10, v12 offset:36352\n\t"
    "v_cvt_pk_bf16_f32 v16, v16, v17\n\t"
    "ds_write_b32 v10, v16 offset:36608\n\t"
    "s_waitcnt lgkmcnt(0)\n\t"
    "s_setprio 0\n\t"
    "s_barrier\n\t"
    "s_barrier\n\t"
    "s_waitcnt lgkmcnt(0)\n\t"
    "s_branch 9f\n\t"
    "1:\n\t"
    "s_add_u32 %[sp], %[sp], 1\n\t"
    "s_cmp_ge_u32 %[sp], %[cap]\n\t"
    "s_cbranch_scc1 7f\n\t"
    "global_load_dwordx4 v[36:39], v6, %[ag] sc1\n\t"
    "global_load_dwordx4 v[40:43], v6, %[ag] offset:1024 sc1\n\t"
    "s_waitcnt lgkmcnt(0)\n\t"
    "s_memtime %[tc]\n\t"
    "s_nop 7\n\t"
    "s_branch 3b\n\t"
    "7:\n\t"
    "s_mov_b32 %[cap], 0\n\t"
    "s_branch 2b\n\t"
    "9:"
    : [tc] "=&s"(tc), [sp] "=&s"(sp), [cap] "+s"(cap)
    : [ag] "s"(ag), [c44] "s"(0x4444u), "{v[64:95]}"(W.w0), "{v[96:127]}"(W.w1), "{v[128:159]}"(W.w2), "{v[160:191]}"(W.w3), "{v[192:223]}"(W.w4), "{v[224:255]}"(W.w5), "{v[56:63]}"(W.x)
    : "v6", "v7", "v8", "v9", "v10", "v11", "v12", "v13", "v14", "v15", "v16", "v17", "v18", "v19", "v20", "v21", "v22", "v23", "v24", "v25", "v26", "v27", "v28", "v29", "v30", "v31", "v32", "v33", "v34", "v35", "v36", "v37", "v38", "v39", "v40", "v41", "v42", "v43", "v44", "v45", "v46", "v47", "v48", "v49", "v50", "v51", "v52", "v53", "v54", "v55", "vcc", "scc", "memory");
}
template <int I> __device__ __forceinline__ void qk_wload(u32x32& w, unsigned a0, unsigned a1);
template <> __device__ __forceinline__ void qk_wload<0>(u32x32& w, unsigned a0, unsigned a1) {
  asm volatile("ds_read_b64_tr_b16 v[64:65], %1 offset:0\n\t"
    "ds_read_b64_tr_b16 v[66:67], %1 offset:512\n\t"
    "ds_read_b64_tr_b16 v[68:69], %1 offset:4096\n\t"
    "ds_read_b64_tr_b16 v[70:71], %1 offset:4608\n\t"
    "ds_read_b64_tr_b16 v[72:73], %2 offset:0\n\t"
    "ds_read_b64_tr_b16 v[74:75], %2 offset:512\n\t"
    "ds_read_b64_tr_b16 v[76:77], %2 offset:4096\n\t"
    "ds_read_b64_tr_b16 v[78:79], %2 offset:4608\n\t"
    "s_waitcnt lgkmcnt(0)\n\t"
    "ds_read_b64_tr_b16 v[80:81], %1 offset:16384\n\t"
    "ds_read_b64_tr_b16 v[82:83], %1 offset:16896\n\t"
    "ds_read_b64_tr_b16 v[84:85], %1 offset:20480\n\t"
    "ds_read_b64_tr_b16 v[86:87], %1 offset:20992\n\t"
    "ds_read_b64_tr_b16 v[88:89], %2 offset:16384\n\t"
    "ds_read_b64_tr_b16 v[90:91], %2 offset:16896\n\t"
    "ds_read_b64_tr_b16 v[92:93], %2 offset:20480\n\t"
    "ds_read_b64_tr_b16 v[94:95], %2 offset:20992\n\t"
    "s_waitcnt lgkmcnt(0)"
    : "=&{v[64:95]}"(w) : "v"(a0), "v"(a1) : "memory");
}
template <> __device__ __forceinline__ void qk_wload<1>(u32x32& w, unsigned a0, unsigned a1) {
  asm volatile("ds_read_b64_tr_b16 v[96:97], %1 offset:0\n\t"
    "ds_read_b64_tr_b16 v[98:99], %1 offset:512\n\t"
    "ds_read_b64_tr_b16 v[100:101], %1 offset:4096\n\t"
    "ds_read_b64_tr_b16 v[102:103], %1 offset:4608\n\t"
    "ds_read_b64_tr_b16 v[104:105], %2 offset:0\n\t"
    "ds_read_b64_tr_b16 v[106:107], %2 offset:512\n\t"
    "ds_read_b64_tr_b16 v[108:109], %2 offset:4096\n\t"
    "ds_read_b64_tr_b16 v[110:111], %2 offset:4608\n\t"
    "s_waitcnt lgkmcnt(0)\n\t"
    "ds_read_b64_tr_b16 v[112:113], %1 offset:16384\n\t"
    "ds_read_b64_tr_b16 v[114:115], %1 offset:16896\n\t"
    "ds_read_b64_tr_b16 v[116:117], %1 offset:20480\n\t"
    "ds_read_b64_tr_b16 v[118:119], %1 offset:20992\n\t"
    "ds_read_b64_tr_b16 v[120:121], %2 offset:16384\n\t"
    "ds_read_b64_tr_b16 v[122:123], %2 offset:16896\n\t"
    "ds_read_b64_tr_b16 v[124:125], %2 offset:20480\n\t"
    "ds_read_b64_tr_b16 v[126:127], %2 offset:20992\n\t"
    "s_waitcnt lgkmcnt(0)"
    : "=&{v[96:127]}"(w) : "v"(a0), "v"(a1) : "memory");
}
template <> __device__ __forceinline__ void qk_wload<2>(u32x32& w, unsigned a0, unsigned a1) {
  asm volatile("ds_read_b64_tr_b16 v[128:129], %1 offset:0\n\t"
    "ds_read_b64_tr_b16 v[130:131], %1 offset:512\n\t"
    "ds_read_b64_tr_b16 v[132:133], %1 offset:4096\n\t"
    "ds_read_b64_tr_b16 v[134:135], %1 offset:4608\n\t"
    "ds_read_b64_tr_b16 v[136:137], %2 offset:0\n\t"
    "ds_read_b64_tr_b16 v[138:139], %2 offset:512\n\t"
    "ds_read_b64_tr_b16 v[140:141], %2 offset:4096\n\t"
    "ds_read_b64_tr_b16 v[142:143], %2 offset:4608\n\t"
    "s_waitcnt lgkmcnt(0)\n\t"
    "ds_read_b64_tr_b16 v[144:145], %1 offset:16384\n\t"
    "ds_read_b64_tr_b16 v[146:147], %1 offset:16896\n\t"
    "ds_read_b64_tr_b16 v[148:149], %1 offset:20480\n\t"
    "ds_read_b64_tr_b16 v[150:151], %1 offset:20992\n\t"
    "ds_read_b64_tr_b16 v[152:153], %2 offset:16384\n\t"
    "ds_read_b64_tr_b16 v[154:155], %2 offset:16896\n\t"
    "ds_read_b64_tr_b16 v[156:157], %2 offset:20480\n\t"
    "ds_read_b64_tr_b16 v[158:159], %2 offset:20992\n\t"
    "s_waitcnt lgkmcnt(0)"
    : "=&{v[128:159]}"(w) : "v"(a0), "v"(a1) : "memory");
}
template <> __device__ __forceinline__ void qk_wload<3>(u32x32& w, unsigned a0, unsigned a1) {
  asm volatile("ds_read_b64_tr_b16 v[160:161], %1 offset:0\n\t"
    "ds_read_b64_tr_b16 v[162:163], %1 offset:512\n\t"
    "ds_read_b64_tr_b16 v[164:165], %1 offset:4096\n\t"
    "ds_read_b64_tr_b16 v[166:167], %1 offset:4608\n\t"
    "ds_read_b64_tr_b16 v[168:169], %2 offset:0\n\t"
    "ds_read_b64_tr_b16 v[170:171], %2 offset:512\n\t"
    "ds_read_b64_tr_b16 v[172:173], %2 offset:4096\n\t"
    "ds_read_b64_tr_b16 v[174:175], %2 offset:4608\n\t"
    "s_waitcnt lgkmcnt(0)\n\t"
    "ds_read_b64_tr_b16 v[176:177], %1 offset:16384\n\t"
    "ds_read_b64_tr_b16 v[178:179], %1 offset:16896\n\t"
    "ds_read_b64_tr_b16 v[180:181], %1 offset:20480\n\t"
    "ds_read_b64_tr_b16 v[182:183], %1 offset:20992\n\t"
    "ds_read_b64_tr_b16 v[184:185], %2 offset:16384\n\t"
    "ds_read_b64_tr_b16 v[186:187], %2 offset:16896\n\t"
    "ds_read_b64_tr_b16 v[188:189], %2 offset:20480\n\t"
    "ds_read_b64_tr_b16 v[190:191], %2 offset:20992\n\t"
    "s_waitcnt lgkmcnt(0)"
    : "=&{v[160:191]}"(w) : "v"(a0), "v"(a1) : "memory");
}
template <> __device__ __forceinline__ void qk_wload<4>(u32x32& w, unsigned a0, unsigned a1) {
  asm volatile("ds_read_b64_tr_b16 v[192:193], %1 offset:0\n\t"
    "ds_read_b64_tr_b16 v[194:195], %1 offset:512\n\t"
    "ds_read_b64_tr_b16 v[196:197], %1 offset:4096\n\t"
    "ds_read_b64_tr_b16 v[198:199], %1 offset:4608\n\t"
    "ds_read_b64_tr_b16 v[200:201], %2 offset:0\n\t"
    "ds_read_b64_tr_b16 v[202:203], %2 offset:512\n\t"
    "ds_read_b64_tr_b16 v[204:205], %2 offset:4096\n\t"
    "ds_read_b64_tr_b16 v[206:207], %2 offset:4608\n\t"
    "s_waitcnt lgkmcnt(0)\n\t"
    "ds_read_b64_tr_b16 v[208:209], %1 offset:16384\n\t"
    "ds_read_b64_tr_b16 v[210:211], %1 offset:16896\n\t"
    "ds_read_b64_tr_b16 v[212:213], %1 offset:20480\n\t"
    "ds_read_b64_tr_b16 v[214:215], %1 offset:20992\n\t"
    "ds_read_b64_tr_b16 v[216:217], %2 offset:16384\n\t"
    "ds_read_b64_tr_b16 v[218:219], %2 offset:16896\n\t"
    "ds_read_b64_tr_b16 v[220:221], %2 offset:20480\n\t"
    "ds_read_b64_tr_b16 v[222:223], %2 offset:20992\n\t"
    "s_waitcnt lgkmcnt(0)"
    : "=&{v[192:223]}"(w) : "v"(a0), "v"(a1) : "memory");
}
template <> __device__ __forceinline__ void qk_wload<5>(u32x32& w, unsigned a0, unsigned a1) {
  asm volatile("ds_read_b64_tr_b16 v[224:225], %1 offset:0\n\t"
    "ds_read_b64_tr_b16 v[226:227], %1 offset:512\n\t"
    "ds_read_b64_tr_b16 v[228:229], %1 offset:4096\n\t"
    "ds_read_b64_tr_b16 v[230:231], %1 offset:4608\n\t"
    "ds_read_b64_tr_b16 v[232:233], %2 offset:0\n\t"
    "ds_read_b64_tr_b16 v[234:235], %2 offset:512\n\t"
    "ds_read_b64_tr_b16 v[236:237], %2 offset:4096\n\t"
    "ds_read_b64_tr_b16 v[238:239], %2 offset:4608\n\t"
    "s_waitcnt lgkmcnt(0)\n\t"
    "ds_read_b64_tr_b16 v[240:241], %1 offset:16384\n\t"
    "ds_read_b64_tr_b16 v[242:243], %1 offset:16896\n\t"
    "ds_read_b64_tr_b16 v[244:245], %1 offset:20480\n\t"
    "ds_read_b64_tr_b16 v[246:247], %1 offset:20992\n\t"
    "ds_read_b64_tr_b16 v[248:249], %2 offset:16384\n\t"
    "ds_read_b64_tr_b16 v[250:251], %2 offset:16896\n\t"
    "ds_read_b64_tr_b16 v[252:253], %2 offset:20480\n\t"
    "ds_read_b64_tr_b16 v[254:255], %2 offset:20992\n\t"
    "s_waitcnt lgkmcnt(0)"
    : "=&{v[224:255]}"(w) : "v"(a0), "v"(a1) : "memory");
}
template <int I> __device__ __forceinline__ void vr_wload(u32x32& w, unsigned a);
template <> __device__ __forceinline__ void vr_wload<0>(u32x32& w, unsigned a) {
  asm volatile("ds_read_b64_tr_b16 v[64:65], %1 offset:0\n\t"
    "ds_read_b64_tr_b16 v[66:67], %1 offset:256\n\t"
    "ds_read_b64_tr_b16 v[68:69], %1 offset:2048\n\t"
    "ds_read_b64_tr_b16 v[70:71], %1 offset:2304\n\t"
    "ds_read_b64_tr_b16 v[72:73], %1 offset:32\n\t"
    "ds_read_b64_tr_b16 v[74:75], %1 offset:288\n\t"
    "ds_read_b64_tr_b16 v[76:77], %1 offset:2080\n\t"
    "ds_read_b64_tr_b16 v[78:79], %1 offset:2336\n\t"
    "s_waitcnt lgkmcnt(0)\n\t"
    "ds_read_b64_tr_b16 v[80:81], %1 offset:64\n\t"
    "ds_read_b64_tr_b16 v[82:83], %1 offset:320\n\t"
    "ds_read_b64_tr_b16 v[84:85], %1 offset:2112\n\t"
    "ds_read_b64_tr_b16 v[86:87], %1 offset:2368\n\t"
    "ds_read_b64_tr_b16 v[88:89], %1 offset:96\n\t"
    "ds_read_b64_tr_b16 v[90:91], %1 offset:352\n\t"
    "ds_read_b64_tr_b16 v[92:93], %1 offset:2144\n\t"
    "ds_read_b64_tr_b16 v[94:95], %1 offset:2400\n\t"
    "s_waitcnt lgkmcnt(0)"
    : "=&{v[64:95]}"(w) : "v"(a) : "memory");
}
template <> __device__ __forceinline__ void vr_wload<1>(u32x32& w, unsigned a) {
  asm volatile("ds_read_b64_tr_b16 v[96:97], %1 offset:8192\n\t"
    "ds_read_b64_tr_b16 v[98:99], %1 offset:8448\n\t"
    "ds_read_b64_tr_b16 v[100:101], %1 offset:10240\n\t"
    "ds_read_b64_tr_b16 v[102:103], %1 offset:10496\n\t"
    "ds_read_b64_tr_b16 v[104:105], %1 offset:8224\n\t"
    "ds_read_b64_tr_b16 v[106:107], %1 offset:8480\n\t"
    "ds_read_b64_tr_b16 v[108:109], %1 offset:10272\n\t"
    "ds_read_b64_tr_b16 v[110:111], %1 offset:10528\n\t"
    "s_waitcnt lgkmcnt(0)\n\t"
    "ds_read_b64_tr_b16 v[112:113], %1 offset:8256\n\t"
    "ds_read_b64_tr_b16 v[114:115], %1 offset:8512\n\t"
    "ds_read_b64_tr_b16 v[116:117], %1 offset:10304\n\t"
    "ds_read_b64_tr_b16 v[118:119], %1 offset:10560\n\t"
    "ds_read_b64_tr_b16 v[120:121], %1 offset:8288\n\t"
    "ds_read_b64_tr_b16 v[122:123], %1 offset:8544\n\t"
    "ds_read_b64_tr_b16 v[124:125], %1 offset:10336\n\t"
    "ds_read_b64_tr_b16 v[126:127], %1 offset:10592\n\t"
    "s_waitcnt lgkmcnt(0)"
    : "=&{v[96:127]}"(w) : "v"(a) : "memory");
}
template <> __device__ __forceinline__ void vr_wload<2>(u32x32& w, unsigned a) {
  asm volatile("ds_read_b64_tr_b16 v[128:129], %1 offset:0\n\t"
    "ds_read_b64_tr_b16 v[130:131], %1 offset:128\n\t"
    "ds_read_b64_tr_b16 v[132:133], %1 offset:1024\n\t"
    "ds_read_b64_tr_b16 v[134:135], %1 offset:1152\n\t"
    "ds_read_b64_tr_b16 v[136:137], %1 offset:32\n\t"
    "ds_read_b64_tr_b16 v[138:139], %1 offset:160\n\t"
    "ds_read_b64_tr_b16 v[140:141], %1 offset:1056\n\t"
    "ds_read_b64_tr_b16 v[142:143], %1 offset:1184\n\t"
    "s_waitcnt lgkmcnt(0)\n\t"
    "ds_read_b64_tr_b16 v[144:145], %1 offset:4096\n\t"
    "ds_read_b64_tr_b16 v[146:147], %1 offset:4224\n\t"
    "ds_read_b64_tr_b16 v[148:149], %1 offset:5120\n\t"
    "ds_read_b64_tr_b16 v[150:151], %1 offset:5248\n\t"
    "ds_read_b64_tr_b16 v[152:153], %1 offset:4128\n\t"
    "ds_read_b64_tr_b16 v[154:155], %1 offset:4256\n\t"
    "ds_read_b64_tr_b16 v[156:157], %1 offset:5152\n\t"
    "ds_read_b64_tr_b16 v[158:159], %1 offset:5280\n\t"
    "s_waitcnt lgkmcnt(0)"
    : "=&{v[128:159]}"(w) : "v"(a) : "memory");
}
template <> __device__ __forceinline__ void vr_wload<3>(u32x32& w, unsigned a) {
  asm volatile("ds_read_b64_tr_b16 v[160:161], %1 offset:8192\n\t"
    "ds_read_b64_tr_b16 v[162:163], %1 offset:8320\n\t"
    "ds_read_b64_tr_b16 v[164:165], %1 offset:9216\n\t"
    "ds_read_b64_tr_b16 v[166:167], %1 offset:9344\n\t"
    "ds_read_b64_tr_b16 v[168:169], %1 offset:8224\n\t"
    "ds_read_b64_tr_b16 v[170:171], %1 offset:8352\n\t"
    "ds_read_b64_tr_b16 v[172:173], %1 offset:9248\n\t"
    "ds_read_b64_tr_b16 v[174:175], %1 offset:9376\n\t"
    "s_waitcnt lgkmcnt(0)\n\t"
    "ds_read_b64_tr_b16 v[176:177], %1 offset:12288\n\t"
    "ds_read_b64_tr_b16 v[178:179], %1 offset:12416\n\t"
    "ds_read_b64_tr_b16 v[180:181], %1 offset:13312\n\t"
    "ds_read_b64_tr_b16 v[182:183], %1 offset:13440\n\t"
    "ds_read_b64_tr_b16 v[184:185], %1 offset:12320\n\t"
    "ds_read_b64_tr_b16 v[186:187], %1 offset:12448\n\t"
    "ds_read_b64_tr_b16 v[188:189], %1 offset:13344\n\t"
    "ds_read_b64_tr_b16 v[190:191], %1 offset:13472\n\t"
    "s_waitcnt lgkmcnt(0)"
    : "=&{v[160:191]}"(w) : "v"(a) : "memory");
}
template <> __device__ __forceinline__ void vr_wload<4>(u32x32& w, unsigned a) {
  asm volatile("ds_read_b64_tr_b16 v[192:193], %1 offset:0\n\t"
    "ds_read_b64_tr_b16 v[194:195], %1 offset:128\n\t"
    "ds_read_b64_tr_b16 v[196:197], %1 offset:1024\n\t"
    "ds_read_b64_tr_b16 v[198:199], %1 offset:1152\n\t"
    "ds_read_b64_tr_b16 v[200:201], %1 offset:32\n\t"
    "ds_read_b64_tr_b16 v[202:203], %1 offset:160\n\t"
    "ds_read_b64_tr_b16 v[204:205], %1 offset:1056\n\t"
    "ds_read_b64_tr_b16 v[206:207], %1 offset:1184\n\t"
    "s_waitcnt lgkmcnt(0)\n\t"
    "ds_read_b64_tr_b16 v[208:209], %1 offset:4096\n\t"
    "ds_read_b64_tr_b16 v[210:211], %1 offset:4224\n\t"
    "ds_read_b64_tr_b16 v[212:213], %1 offset:5120\n\t"
    "ds_read_b64_tr_b16 v[214:215], %1 offset:5248\n\t"
    "ds_read_b64_tr_b16 v[216:217], %1 offset:4128\n\t"
    "ds_read_b64_tr_b16 v[218:219], %1 offset:4256\n\t"
    "ds_read_b64_tr_b16 v[220:221], %1 offset:5152\n\t"
    "ds_read_b64_tr_b16 v[222:223], %1 offset:5280\n\t"
    "s_waitcnt lgkmcnt(0)"
    : "=&{v[192:223]}"(w) : "v"(a) : "memory");
}
template <> __device__ __forceinline__ void vr_wload<5>(u32x32& w, unsigned a) {
  asm volatile("ds_read_b64_tr_b16 v[224:225], %1 offset:8192\n\t"
    "ds_read_b64_tr_b16 v[226:227], %1 offset:8320\n\t"
    "ds_read_b64_tr_b16 v[228:229], %1 offset:9216\n\t"
    "ds_read_b64_tr_b16 v[230:231], %1 offset:9344\n\t"
    "ds_read_b64_tr_b16 v[232:233], %1 offset:8224\n\t"
    "ds_read_b64_tr_b16 v[234:235], %1 offset:8352\n\t"
    "ds_read_b64_tr_b16 v[236:237], %1 offset:9248\n\t"
    "ds_read_b64_tr_b16 v[238:239], %1 offset:9376\n\t"
    "s_waitcnt lgkmcnt(0)\n\t"
    "ds_read_b64_tr_b16 v[240:241], %1 offset:12288\n\t"
    "ds_read_b64_tr_b16 v[242:243], %1 offset:12416\n\t"
    "ds_read_b64_tr_b16 v[244:245], %1 offset:13312\n\t"
    "ds_read_b64_tr_b16 v[246:247], %1 offset:13440\n\t"
    "ds_read_b64_tr_b16 v[248:249], %1 offset:12320\n\t"
    "ds_read_b64_tr_b16 v[250:251], %1 offset:12448\n\t"
    "ds_read_b64_tr_b16 v[252:253], %1 offset:13344\n\t"
    "ds_read_b64_tr_b16 v[254:255], %1 offset:13472\n\t"
    "s_waitcnt lgkmcnt(0)"
    : "=&{v[224:255]}"(w) : "v"(a) : "memory");
}
#define OREP 2
#define AREP 2
struct VRW { u32x32 v0, v1, o0, o1, o2, o3; };
__device__ __forceinline__ void vr_v(const VRW& W, const char* ag, const char* vd, unsigned wa, unsigned ra, unsigned& cap, unsigned& sp, unsigned long long& tc, unsigned long long* ts) {
  asm volatile("v_mbcnt_lo_u32_b32 v16, -1, 0\n\t"
    "v_mbcnt_hi_u32_b32 v16, -1, v16\n\t"
    "v_lshlrev_b32 v17, 4, v16\n\t"
    "s_mov_b32 %[sp], 0\n\t"
    "global_load_dwordx4 v[24:27], v17, %[ag] sc1\n\t"
    "global_load_dwordx4 v[28:31], v17, %[ag] offset:1024 sc1\n\t"
    "s_memtime %[tc]\n\t"
    "v_and_b32 v22, 4, v16\n\t"
    "v_mul_u32_u24 v22, 0x2aaa, v22\n\t"
    "v_lshrrev_b32 v18, 1, v16\n\t"
    "v_and_b32 v18, 2, v18\n\t"
    "v_or3_b32 v18, v18, v22, %[c44]\n\t"
    "v_lshl_add_u32 v19, v16, 3, %[wa]\n\t"
    "v_lshl_add_u32 v20, v16, 3, %[ra]\n\t"
    "v_and_b32 v22, 40, v16\n\t"
    "v_lshlrev_b32 v21, 3, v22\n\t"
    "v_and_b32 v22, 3, v16\n\t"
    "v_lshl_or_b32 v21, v22, 4, v21\n\t"
    "v_and_b32 v22, 4, v16\n\t"
    "v_lshl_or_b32 v21, v22, 1, v21\n\t"
    "v_lshrrev_b32 v22, 2, v16\n\t"
    "v_and_or_b32 v21, v22, 4, v21\n\t"
    "3:\n\t"
    "v_mov_b32 v32, 0\n\t"
    "v_mov_b32 v33, 0\n\t"
    "v_mov_b32 v34, 0\n\t"
    "v_mov_b32 v35, 0\n\t"
    "v_mov_b32 v36, 0\n\t"
    "v_mov_b32 v37, 0\n\t"
    "v_mov_b32 v38, 0\n\t"
    "v_mov_b32 v39, 0\n\t"
    "v_mov_b32 v40, 0\n\t"
    "v_mov_b32 v41, 0\n\t"
    "v_mov_b32 v42, 0\n\t"
    "v_mov_b32 v43, 0\n\t"
    "v_mov_b32 v44, 0\n\t"
    "v_mov_b32 v45, 0\n\t"
    "v_mov_b32 v46, 0\n\t"
    "v_mov_b32 v47, 0\n\t"
    "s_waitcnt vmcnt(0) lgkmcnt(0)\n\t"
    "v_smfmac_f32_16x16x64_bf16 v[32:35], v[24:27], v[64:71], v18\n\t"
    "v_smfmac_f32_16x16x64_bf16 v[32:35], v[28:31], v[96:103], v18\n\t"
    "v_max3_u32 v22, v24, v25, v26\n\t"
    "v_max3_u32 v23, v27, v28, v29\n\t"
    "v_smfmac_f32_16x16x64_bf16 v[36:39], v[24:27], v[72:79], v18\n\t"
    "v_max3_u32 v22, v22, v30, v31\n\t"
    "v_max_u32 v22, v22, v23\n\t"
    "v_smfmac_f32_16x16x64_bf16 v[36:39], v[28:31], v[104:111], v18\n\t"
    "v_cmp_eq_u32 vcc, -1, v22\n\t"
    "s_cbranch_vccnz 1f\n\t"
    "2:\n\t"
    "v_smfmac_f32_16x16x64_bf16 v[40:43], v[24:27], v[80:87], v18\n\t"
    "v_permlane16_swap_b32 v32, v34\n\t"
    "v_permlane16_swap_b32 v33, v35\n\t"
    "v_smfmac_f32_16x16x64_bf16 v[40:43], v[28:31], v[112:119], v18\n\t"
    "v_pk_add_f32 v[32:33], v[32:33], v[34:35]\n\t"
    "ds_write_b64 v19, v[32:33] offset:0\n\t"
    "v_smfmac_f32_16x16x64_bf16 v[44:47], v[24:27], v[88:95], v18\n\t"
    "s_nop 0\n\t"
    "v_permlane16_swap_b32 v36, v38\n\t"
    "v_permlane16_swap_b32 v37, v39\n\t"
    "v_smfmac_f32_16x16x64_bf16 v[44:47], v[28:31], v[120:127], v18\n\t"
    "v_pk_add_f32 v[36:37], v[36:37], v[38:39]\n\t"
    "ds_write_b64 v19, v[36:37] offset:512\n\t"
    "v_permlane16_swap_b32 v40, v42\n\t"
    "v_permlane16_swap_b32 v41, v43\n\t"
    "v_pk_add_f32 v[40:41], v[40:41], v[42:43]\n\t"
    "ds_write_b64 v19, v[40:41] offset:1024\n\t"
    "s_nop 1\n\t"
    "v_permlane16_swap_b32 v44, v46\n\t"
    "v_permlane16_swap_b32 v45, v47\n\t"
    "v_pk_add_f32 v[44:45], v[44:45], v[46:47]\n\t"
    "ds_write_b64 v19, v[44:45] offset:1536\n\t"
    "s_cmp_ge_u32 %[wa], 8192\n\t"
    "s_waitcnt lgkmcnt(0)\n\t"
    "s_barrier\n\t"
    "s_cbranch_scc1 9f\n\t"
    "ds_read_b64 v[48:49], v20 offset:0\n\t"
    "ds_read_b64 v[50:51], v20 offset:2048\n\t"
    "ds_read_b64 v[52:53], v20 offset:4096\n\t"
    "ds_read_b64 v[54:55], v20 offset:6144\n\t"
    "ds_read_b64 v[56:57], v20 offset:8192\n\t"
    "ds_read_b64 v[58:59], v20 offset:10240\n\t"
    "ds_read_b64 v[60:61], v20 offset:12288\n\t"
    "ds_read_b64 v[62:63], v20 offset:14336\n\t"
    "s_waitcnt lgkmcnt(6)\n\t"
    "v_pk_add_f32 v[48:49], v[48:49], v[50:51]\n\t"
    "s_waitcnt lgkmcnt(4)\n\t"
    "v_pk_add_f32 v[52:53], v[52:53], v[54:55]\n\t"
    "v_pk_add_f32 v[48:49], v[48:49], v[52:53]\n\t"
    "s_waitcnt lgkmcnt(2)\n\t"
    "v_pk_add_f32 v[56:57], v[56:57], v[58:59]\n\t"
    "s_waitcnt lgkmcnt(0)\n\t"
    "v_pk_add_f32 v[60:61], v[60:61], v[62:63]\n\t"
    "v_pk_add_f32 v[56:57], v[56:57], v[60:61]\n\t"
    "v_pk_add_f32 v[48:49], v[48:49], v[56:57]\n\t"
    "v_cvt_pk_bf16_f32 v22, v48, v49\n\t"
    "v_min_u32 v22, -2, v22\n\t"
    "global_store_dword v21, v22, %[vd] sc1\n\t"
    "s_waitcnt lgkmcnt(0)\n\t"
    "s_branch 9f\n\t"
    "1:\n\t"
    "s_add_u32 %[sp], %[sp], 1\n\t"
    "s_cmp_ge_u32 %[sp], %[cap]\n\t"
    "s_cbranch_scc1 7f\n\t"
    "global_load_dwordx4 v[24:27], v17, %[ag] sc1\n\t"
    "global_load_dwordx4 v[28:31], v17, %[ag] offset:1024 sc1\n\t"
    "s_waitcnt lgkmcnt(0)\n\t"
    "s_memtime %[tc]\n\t"
    "s_nop 7\n\t"
    "s_branch 3b\n\t"
    "7:\n\t"
    "s_mov_b32 %[cap], 0\n\t"
    "s_branch 2b\n\t"
    "9:"
    : [tc] "=&s"(tc), [sp] "=&s"(sp), [cap] "+s"(cap)
    : [ag] "s"(ag), [vd] "s"(vd), [wa] "s"(wa), [ra] "s"(ra), [c44] "s"(0x4444u), "{v[64:95]}"(W.v0), "{v[96:127]}"(W.v1), "{v[128:159]}"(W.o0), "{v[160:191]}"(W.o1), "{v[192:223]}"(W.o2), "{v[224:255]}"(W.o3)
    : "v16", "v17", "v18", "v19", "v20", "v21", "v22", "v23", "v24", "v25", "v26", "v27", "v28", "v29", "v30", "v31", "v32", "v33", "v34", "v35", "v36", "v37", "v38", "v39", "v40", "v41", "v42", "v43", "v44", "v45", "v46", "v47", "v48", "v49", "v50", "v51", "v52", "v53", "v54", "v55", "v56", "v57", "v58", "v59", "v60", "v61", "v62", "v63", "vcc", "scc", "memory");
}
__device__ __forceinline__ void vr_r(const VRW& W, const char* os, const char* ad, unsigned wa, unsigned jx, unsigned& cap, unsigned& sp, unsigned long long* ts) {
  asm volatile("v_mbcnt_lo_u32_b32 v16, -1, 0\n\t"
    "v_mbcnt_hi_u32_b32 v16, -1, v16\n\t"
    "v_lshl_or_b32 v17, v16, 4, %[jx]\n\t"
    "global_load_dwordx4 v[24:27], v17, %[os] sc1\n\t"
    "v_xor_b32 v18, 2048, v17\n\t"
    "global_load_dwordx4 v[28:31], v18, %[os] sc1\n\t"
    "v_xor_b32 v57, 4096, v17\n\t"
    "global_load_dwordx4 v[32:35], v57, %[os] sc1\n\t"
    "v_xor_b32 v58, 6144, v17\n\t"
    "global_load_dwordx4 v[36:39], v58, %[os] sc1\n\t"
    "s_mov_b32 %[sp], 0\n\t"
    "v_and_b32 v23, 4, v16\n\t"
    "v_mul_u32_u24 v23, 0x2aaa, v23\n\t"
    "v_lshrrev_b32 v19, 1, v16\n\t"
    "v_and_b32 v19, 2, v19\n\t"
    "v_or3_b32 v19, v19, v23, %[c44]\n\t"
    "v_and_b32 v23, 15, v16\n\t"
    "v_lshl_add_u32 v20, v23, 2, %[wa]\n\t"
    "v_and_b32 v23, 48, v16\n\t"
    "v_lshl_add_u32 v20, v23, 3, v20\n\t"
    "v_and_b32 v23, 32, v16\n\t"
    "v_and_b32 v21, 28, v16\n\t"
    "v_lshlrev_b32 v21, 4, v21\n\t"
    "v_or_b32 v21, v21, v23\n\t"
    "v_lshlrev_b32 v22, 3, v23\n\t"
    "v_and_b32 v23, 3, v16\n\t"
    "v_lshl_or_b32 v21, v23, 3, v21\n\t"
    "v_and_b32 v23, 31, v16\n\t"
    "v_lshl_or_b32 v22, v23, 2, v22\n\t"
    "3:\n\t"
    "v_mov_b32 v40, 0\n\t"
    "v_mov_b32 v41, 0\n\t"
    "v_mov_b32 v42, 0\n\t"
    "v_mov_b32 v43, 0\n\t"
    "v_mov_b32 v44, 0\n\t"
    "v_mov_b32 v45, 0\n\t"
    "v_mov_b32 v46, 0\n\t"
    "v_mov_b32 v47, 0\n\t"
    "v_mov_b32 v48, 0\n\t"
    "v_mov_b32 v49, 0\n\t"
    "v_mov_b32 v50, 0\n\t"
    "v_mov_b32 v51, 0\n\t"
    "v_mov_b32 v52, 0\n\t"
    "v_mov_b32 v53, 0\n\t"
    "v_mov_b32 v54, 0\n\t"
    "v_mov_b32 v55, 0\n\t"
    "v_mov_b32 v56, 0\n\t"
    "s_waitcnt vmcnt(3)\n\t"
    "v_smfmac_f32_16x16x64_bf16 v[44:47], v[24:27], v[144:151], v19\n\t"
    "v_max3_u32 v56, v56, v24, v25\n\t"
    "v_smfmac_f32_16x16x64_bf16 v[48:51], v[24:27], v[136:143], v19\n\t"
    "v_max3_u32 v56, v56, v26, v27\n\t"
    "v_smfmac_f32_16x16x64_bf16 v[40:43], v[24:27], v[128:135], v19\n\t"
    "v_smfmac_f32_16x16x64_bf16 v[52:55], v[24:27], v[152:159], v19\n\t"
    "s_waitcnt vmcnt(2)\n\t"
    "v_smfmac_f32_16x16x64_bf16 v[44:47], v[28:31], v[176:183], v19\n\t"
    "v_max3_u32 v56, v56, v28, v29\n\t"
    "v_smfmac_f32_16x16x64_bf16 v[48:51], v[28:31], v[168:175], v19\n\t"
    "v_max3_u32 v56, v56, v30, v31\n\t"
    "v_smfmac_f32_16x16x64_bf16 v[40:43], v[28:31], v[160:167], v19\n\t"
    "v_smfmac_f32_16x16x64_bf16 v[52:55], v[28:31], v[184:191], v19\n\t"
    "s_waitcnt vmcnt(1)\n\t"
    "v_smfmac_f32_16x16x64_bf16 v[44:47], v[32:35], v[208:215], v19\n\t"
    "v_max3_u32 v56, v56, v32, v33\n\t"
    "v_smfmac_f32_16x16x64_bf16 v[48:51], v[32:35], v[200:207], v19\n\t"
    "v_max3_u32 v56, v56, v34, v35\n\t"
    "v_smfmac_f32_16x16x64_bf16 v[40:43], v[32:35], v[192:199], v19\n\t"
    "v_smfmac_f32_16x16x64_bf16 v[52:55], v[32:35], v[216:223], v19\n\t"
    "s_waitcnt vmcnt(0)\n\t"
    "v_smfmac_f32_16x16x64_bf16 v[44:47], v[36:39], v[240:247], v19\n\t"
    "v_max3_u32 v56, v56, v36, v37\n\t"
    "v_smfmac_f32_16x16x64_bf16 v[48:51], v[36:39], v[232:239], v19\n\t"
    "v_max3_u32 v56, v56, v38, v39\n\t"
    "v_smfmac_f32_16x16x64_bf16 v[40:43], v[36:39], v[224:231], v19\n\t"
    "v_cmp_eq_u32 vcc, -1, v56\n\t"
    "v_smfmac_f32_16x16x64_bf16 v[52:55], v[36:39], v[248:255], v19\n\t"
    "s_cbranch_vccnz 4f\n\t"
    "5:\n\t"
    "s_nop 2\n\t"
    "v_permlane32_swap_b32 v44, v48\n\t"
    "v_permlane32_swap_b32 v45, v49\n\t"
    "v_permlane32_swap_b32 v46, v50\n\t"
    "v_permlane32_swap_b32 v47, v51\n\t"
    "s_mov_b32 exec_hi, 0\n\t"
    "v_pk_add_f32 v[40:41], v[40:41], v[48:49]\n\t"
    "v_pk_add_f32 v[42:43], v[42:43], v[50:51]\n\t"
    "s_mov_b64 exec, %[hi]\n\t"
    "v_pk_add_f32 v[40:41], v[44:45], v[52:53]\n\t"
    "v_pk_add_f32 v[42:43], v[46:47], v[54:55]\n\t"
    "s_mov_b32 exec_lo, -1\n\t"
    "s_nop 0\n\t"
    "v_permlane16_swap_b32 v40, v42\n\t"
    "v_permlane16_swap_b32 v41, v43\n\t"
    "v_add_f32 v40, v40, v42\n\t"
    "v_add_f32 v41, v41, v43\n\t"
    "ds_write_b32 v20, v40 offset:16384\n\t"
    "ds_write_b32 v20, v41 offset:16448\n\t"
    "s_cmp_ge_u32 %[wa], 1024\n\t"
    "s_waitcnt lgkmcnt(0)\n\t"
    "s_barrier\n\t"
    "s_cbranch_scc1 9f\n\t"
    "ds_read_b64 v[24:25], v21 offset:16384\n\t"
    "ds_read_b64 v[26:27], v21 offset:16896\n\t"
    "ds_read_b64 v[28:29], v21 offset:17408\n\t"
    "ds_read_b64 v[30:31], v21 offset:17920\n\t"
    "ds_read_b64 v[32:33], v21 offset:18432\n\t"
    "ds_read_b64 v[34:35], v21 offset:18944\n\t"
    "ds_read_b64 v[36:37], v21 offset:19456\n\t"
    "ds_read_b64 v[38:39], v21 offset:19968\n\t"
    "s_waitcnt lgkmcnt(6)\n\t"
    "v_pk_add_f32 v[24:25], v[24:25], v[26:27]\n\t"
    "s_waitcnt lgkmcnt(4)\n\t"
    "v_pk_add_f32 v[28:29], v[28:29], v[30:31]\n\t"
    "v_pk_add_f32 v[24:25], v[24:25], v[28:29]\n\t"
    "s_waitcnt lgkmcnt(2)\n\t"
    "v_pk_add_f32 v[32:33], v[32:33], v[34:35]\n\t"
    "s_waitcnt lgkmcnt(0)\n\t"
    "v_pk_add_f32 v[36:37], v[36:37], v[38:39]\n\t"
    "v_pk_add_f32 v[32:33], v[32:33], v[36:37]\n\t"
    "v_pk_add_f32 v[24:25], v[24:25], v[32:33]\n\t"
    "v_cvt_pk_bf16_f32 v23, v24, v25\n\t"
    "v_min_u32 v23, -2, v23\n\t"
    "global_store_dword v22, v23, %[ad] sc1\n\t"
    "s_waitcnt lgkmcnt(0)\n\t"
    "s_branch 9f\n\t"
    "4:\n\t"
    "s_add_u32 %[sp], %[sp], 1\n\t"
    "s_cmp_ge_u32 %[sp], %[cap]\n\t"
    "s_cbranch_scc1 7f\n\t"
    "global_load_dwordx4 v[24:27], v17, %[os] sc1\n\t"
    "global_load_dwordx4 v[28:31], v18, %[os] sc1\n\t"
    "global_load_dwordx4 v[32:35], v57, %[os] sc1\n\t"
    "global_load_dwordx4 v[36:39], v58, %[os] sc1\n\t"
    "s_nop 7\n\t"
    "s_branch 3b\n\t"
    "7:\n\t"
    "s_mov_b32 %[cap], 0\n\t"
    "s_branch 5b\n\t"
    "9:"
    : [sp] "=&s"(sp), [cap] "+s"(cap)
    : [os] "s"(os), [ad] "s"(ad), [wa] "s"(wa), [jx] "s"(jx), [c44] "s"(0x4444u), [hi] "s"(0xFFFFFFFF00000000ull), "{v[64:95]}"(W.v0), "{v[96:127]}"(W.v1), "{v[128:159]}"(W.o0), "{v[160:191]}"(W.o1), "{v[192:223]}"(W.o2), "{v[224:255]}"(W.o3)
    : "v16", "v17", "v18", "v19", "v20", "v21", "v22", "v23", "v24", "v25", "v26", "v27", "v28", "v29", "v30", "v31", "v32", "v33", "v34", "v35", "v36", "v37", "v38", "v39", "v40", "v41", "v42", "v43", "v44", "v45", "v46", "v47", "v48", "v49", "v50", "v51", "v52", "v53", "v54", "v55", "v56", "v57", "v58", "v59", "v60", "v61", "v62", "v63", "vcc", "scc", "memory");
}
__device__ __forceinline__ void qk_body(char* smem, int wave_id_, int xg, int h, int wg, const u16* __restrict__ Wq, const u16* __restrict__ Wk,
                                        const char* __restrict__ Kx, const char* __restrict__ V, char* ws, unsigned* tmo, u64 tag) {
  const int wave = WAVE_ID;
  const int ty = wave >> 2, w4 = wave & 3;
  const char* wq = uni((const char*)(Wq + h * DH)); const char* wk = uni((const char*)(Wk + h * DH));
  auto stage = [&](int u) {
    stage_proj((u & 1) ? wk + (size_t)(((u >> 1) + 2) & 7) * 128 * HD * 2 : wq + (size_t)(u >> 1) * 128 * HD * 2, ((u + 4) % 5) * 32768, wave * 64 + lane_id(), wave);
  };
  QKW W;
  if (wave == 0) STS(ws, wg, 7);
  stage(0); stage(1); stage(2); stage(3);
#define WLD(i, w) do { WAIT_V(12); WBAR(); stage(2 * (i) + 4); if (ty) { WAIT_V(12); WBAR(); stage(2 * (i) + 5); } \
    { const int l_ = lane_id(); const unsigned o_ = ty ? ((2 * (i) + 5) % 5) * 32768 : ((2 * (i) + 4) % 5) * 32768; qk_wload<i>(w, proj_base(2 * w4, l_) + o_, proj_base(2 * w4 + 1, l_) + o_); } \
    if (!ty) { WAIT_V(12); WBAR(); stage(2 * (i) + 5); } if (wave == 0) STS(ws, wg, 1 + (i)); } while (0)
  WLD(0, W.w0); WLD(1, W.w1); WLD(2, W.w2); WLD(3, W.w3); WLD(4, W.w4); WLD(5, W.w5);
#undef WLD
  unsigned in = 0;
#pragma unroll
  for (int i = 0; i < 2; ++i) {
    if (i == 0) WAIT_V(8); else WAIT_V(0);
    WBAR();
    if (i == 0 && wave == 1) init_issue(ws, Q_V);
    if (i == 1) in = init_get(ws, tag, smem + Q_V);
    const int lane = lane_id();
    const unsigned o = Q_RING + ty * 32768 + i * 65536, f0 = proj_base(2 * w4, lane) + o, f1 = proj_base(2 * w4 + 1, lane) + o;
    u32x8 x[4] = {proj_frag(smem, f0), proj_frag(smem, f1), proj_frag(smem, f0 + 16384), proj_frag(smem, f1 + 16384)};
    asm volatile("" : "+v"(x[0]), "+v"(x[1]), "+v"(x[2]), "+v"(x[3]));
    BAR();
    char* d = smem + Q_CB + i * 57344 + ty * (i ? 32768 : 24576) + w4 * 2048 + lane * 16;
#pragma unroll
    for (int j = 0; j < 4; ++j)
      if (i || j || !ty) { *(u32x4*)(d + j * 8192) = u32x4{x[j][0], x[j][1], x[j][2], x[j][3]}; *(u32x4*)(d + j * 8192 + 1024) = u32x4{x[j][4], x[j][5], x[j][6], x[j][7]}; }
    if (i == 0 && ty) { *(u32x4*)(smem + Q_MEM + wave * 2048 + lane * 16) = u32x4{x[0][0], x[0][1], x[0][2], x[0][3]}; *(u32x4*)(smem + Q_MEM + wave * 2048 + 1024 + lane * 16) = u32x4{x[0][4], x[0][5], x[0][6], x[0][7]}; }
  }
  if (wave == 4) *(volatile LAS u32x2*)(smem + Q_TM) = u32x2{0u, 0u};
  BAR();
  if (wave == 0) STS(ws, wg, 0);
  unsigned* cnt = cnt_of(ws, in);
  const char* xb = uni(xch_base(ws) + ((size_t)3 << 21) + (size_t)((in & 2u) << 12));
  const char* oh = uni(xch_base(ws) + (size_t)((in & 1u) << 13) + (size_t)(h >> 2) * 16384 + (h & 3) * 2048);
  auto rearm = [&](int ps) {
    unsigned z = SENT; asm volatile("" : "+v"(z));
    const unsigned l16 = lane_id() * 16;
#pragma unroll
    for (int r = 0; r < 2 * OREP; ++r) st16s(oh + (size_t)(ps * 4 + (r >> 1)) * 131072, (r & 1) * 1024u + l16, u32x4{z, z, z, z});
  };
  const char* dbw = xb + (size_t)48 * 16384 + 4096 + h * 128;
  unsigned avail = 0;
  auto stage_kv = [&](int s) {
    if (avail <= (unsigned)s) {
      for (int spins = 0;; ++spins) {
        const unsigned p = ld4_sc1(cnt + (PQKV + h) * 32 + (lane_id() & 15));
        avail = min(qkv_avail(p, 1), qkv_avail(p, 2));
        if (avail > (unsigned)s) break;
        __builtin_amdgcn_s_sleep(8);
        if (give_up(spins, tmo, 5)) break;
      }
    }
    const char* ks = uni(Kx + ((size_t)s * NH + h) * 8192); const char* vs = uni(V + ((size_t)s * NH + h) * 8192);
    const int lane = lane_id();
    head_to_lds(ks, lane, Q_K); head_to_lds(vs, lane, Q_V);
  };
  auto loop = [&](auto w_c) __attribute__((always_inline)) {
    constexpr int WV = decltype(w_c)::value;
    if (WV >= 4) asm volatile("ds_read_b128 v[56:59], %1\n\tds_read_b128 v[60:63], %1 offset:1024\n\ts_waitcnt lgkmcnt(0)" : "=&{v[56:63]}"(W.x) : "v"(Q_MEM + WV * 2048 + lane_id() * 16) : "memory");
    if (WV == 6) stage_kv(0);
    if (WV < 4) __builtin_amdgcn_s_setprio(2);
    unsigned t0 = 0, cap = 60000, d0 = 105, d1 = 200;
    int dq = D_Q;
    int sl = 0;
    for (int s = 0; s < NSEG; ++s) {
      TSDECL;
      const char* ag = xb + (size_t)((sl * 4 + (AREP == 4 ? xg : xg >> 1)) * 2 + (WV >> 2)) * 16384 + (WV & 3) * 2048;
      const char* vs = WV == 4 ? dbw : WV == 5 ? ws + OFF_KVM + ((size_t)s * NH + h) * 4096 : xb + (size_t)(24 + sl * 8 + (h >> 2)) * 16384 + (h & 3) * 2048;
      const char* od = WV == 4 ? (const char*)(size_t)((unsigned)(s + 1) << 24) : oh + (size_t)sl * 524288;
      if (s > 0) wait_rt(t0, dq);
      TS(0);
      unsigned long long tc; unsigned sp;
      qk_step<WV>(W, ag, vs, od, ws + OFF_KVM + ((size_t)s * NH + h) * 4096 + 2048, cap, sp, tc, nullptr, d0, d1);
      dq = (sp & 0xFFFFu) ? min(dq + DQU, D_Q + DQM) : max(dq - DQD, D_Q);
      if (WV < 4 || WV == 5) {
        const unsigned pv = (unsigned)(s + 1) << 24 | (rt_now() & 0xFFFFFFu);
        WAIT_V(0);
        if (lane_id() == 0) st4_sc1(cnt + (PROG + h) * 32 + WV, pv);
      }
      if (WV == 6 && s + 1 < NSEG - 1) stage_kv(s + 1);
      if (WV == 7 && s > 0 && s < NSEG - 1) rearm(sl == 0 ? 2 : sl - 1);
      if (WV == 7 && s >= 8 && s < 8 + NSEG - SDIR) {
        unsigned z = SENT; asm volatile("" : "+v"(z));
        const char* kd = ws + OFF_KVM + ((size_t)(s - 8 + SDIR) * NH + h) * 4096;
#pragma unroll
        for (int r = 0; r < 4; ++r) st16s(kd, r * 1024u + lane_id() * 16, u32x4{z, z, z, z});
      }
      if (s < NSEG - 1) {
        if (WV == 4) {
          delay64<E_B>();
          t0 = bell_time(xb + (size_t)(48 + xg) * 16384 + 4096, (lane_id() & 31) * 128, (unsigned)(s + 1), cap);
          *(volatile LAS u32x2*)(smem + Q_TM) = u32x2{(unsigned)(s + 1), t0};
        } else {
          for (;;) {
            const u32x2 v = *(volatile LAS u32x2*)(smem + Q_TM);
            t0 = __builtin_amdgcn_readfirstlane(v.y);
            if (__builtin_amdgcn_readfirstlane(v.x) == (unsigned)(s + 1)) break;
            __builtin_amdgcn_s_sleep(1);
          }
        }
      }
      sl = sl == 2 ? 0 : sl + 1;
    }
    if (cap == 0 && lane_id() == 0) st4_sc1(tmo, 1u);
  };
  switch (wave) {
    case 0: loop(std::integral_constant<int, 0>{}); break;
    case 1: loop(std::integral_constant<int, 1>{}); break;
    case 2: loop(std::integral_constant<int, 2>{}); break;
    case 3: loop(std::integral_constant<int, 3>{}); break;
    case 4: loop(std::integral_constant<int, 4>{}); break;
    case 5: loop(std::integral_constant<int, 5>{}); break;
    case 6: loop(std::integral_constant<int, 6>{}); break;
    default: loop(std::integral_constant<int, 7>{}); break;
  }
  __builtin_amdgcn_s_setprio(0);
}

__device__ __forceinline__ void vr_body(char* smem, int wave_id_, int xg, int h, int c, int r, int wg, const u16* __restrict__ Wv, const u16* __restrict__ Wo,
                                        const u16* __restrict__ Wq, const u16* __restrict__ Wk, char* ws, unsigned* tmo, u64 tag) {
  const int wave = WAVE_ID;
  const unsigned po = (unsigned)(r >> 5) * 16384u + ((r >> 2) & 7) * 1024 + (r & 1) * 512 + ((r >> 1) & 1) * 128;
  const unsigned vo_ = (unsigned)(24 + (h >> 2)) * 16384u + (h & 3) * 2048 + c * 1024;
  const int hg = (wave + ROTW * (r & 3)) & 7;
  const int jr = ROTH ? r & 3 : 0;
  VRW W;
  {
    const int lane = lane_id();
    const int g = lane >> 4, q4 = (lane & 15) >> 2, p4 = lane & 3, fr = 4 * g + (q4 & 1) + 32 * (q4 >> 1);
    const unsigned lw = wave * 16384;
    const char* sv_ = uni((const char*)(Wv + (size_t)(128 * wave) * HD + h * DH + 64 * c));
    const char* so = uni((const char*)(Wo + (size_t)(512 * hg) * DM + 64 * (r >> 2) + 16 * (r & 1)));
    const unsigned vo = (unsigned)((32 * (lane >> 5) + ((lane >> 2) & 7)) * DM * 2 + ((lane >> 1) & 1) * 64 + (lane & 1) * 16);
    auto issue = [&](int p) {
#pragma unroll
      for (int i = 0; i < 8; ++i) {
        if (p >= 2 && (NTM & 8)) gldsw(so + (size_t)(128 * ((p - 2) ^ jr) + 64 * (i & 1) + 8 * (i >> 1)) * DM * 2, vo, lw + (p & 1) * 8192 + i * 1024);
        else if (p >= 2) glds16s<0>(so + (size_t)(128 * ((p - 2) ^ jr) + 64 * (i & 1) + 8 * (i >> 1)) * DM * 2, vo, lw + (p & 1) * 8192 + i * 1024);
        else gldsw(sv_ + (size_t)(8 * p + i) * 8 * HD * 2, (unsigned)((lane >> 3) * HD * 2 + (lane & 7) * 16), lw + (p & 1) * 8192 + i * 1024);
      }
    };
#define VTS(p) do { } while (0)
    if (wave == 0) STS(ws, wg, 7);
    issue(0); issue(1);
    const unsigned av = lw + (unsigned)(fr * 128 + p4 * 8), ao = lw + (unsigned)(fr * 64 + p4 * 8);
    WAIT_V(8); VTS(0); vr_wload<0>(W.v0, av); issue(2);
    WAIT_V(8); VTS(1); vr_wload<1>(W.v1, av); issue(3);
    WAIT_V(8); VTS(2); vr_wload<2>(W.o0, ao); issue(4);
    WAIT_V(8); vr_wload<3>(W.o1, ao); issue(5);
    if (wave == 0) init_issue(ws, 131072);
    WAIT_V(8); vr_wload<4>(W.o2, ao);
    WAIT_V(0); VTS(3);
    if (VHN) {
      const int rl = r - 20 * xg, hn = ((xg == 1 ? rl : rl - (rl >= (xg ? 8 : 16) ? 4 : 0)) * 8 + wave) * (6 * VHN) + (lane >> 1);
      const int pm = hn / 384, hr = hn - pm * 384, p = 8 - VHN + (pm >> 1), hh = 12 * xg + 1 + 4 * (hr >> 7);
      const u16* hp = ((pm & 1) ? Wk : Wq) + (size_t)(((pm & 1) ? (p + 2) & 7 : p) * 128 + (hr & 127)) * HD + min(hh, 31) * DH + (lane & 1) * 64;
      if (((r >> 2) & 7) != 3 && lane < 12 * VHN && pm < 2 * VHN) asm volatile("s_mov_b32 m0, %0\n\ts_nop 0\n\tglobal_load_lds_dword %1, off" :: "s"(155648 + wave * 256), "v"(hp) : "memory", "m0");
    }
    vr_wload<5>(W.o3, ao);
#undef VTS
  }
  if (wave == 0) STS(ws, wg, 5);
  if (wave == 0) STS(ws, wg, 6);
  BAR();
  if (wave == 0) STS(ws, wg, 0);
  const unsigned in = init_get(ws, tag, smem + 131072);
  const char* xb = uni(xch_base(ws) + ((size_t)3 << 21) + (size_t)((in & 2u) << 12));
  const char* ob = uni(xch_base(ws) + (size_t)((in & 1u) << 13));
  auto rearm = [&](int ps, bool ag) {
    const int lane = lane_id();
    unsigned z = SENT; asm volatile("" : "+v"(z));
    const u32x4 sv = u32x4{z, z, z, z};
    if (ag && lane < 16 * AREP) st16s(xb, (unsigned)(ps * 8 + (lane >> 4) * 2) * 16384u + po + ((lane >> 3) & 1) * 256 + (lane & 7) * 16, sv);
    st16s(xb, (unsigned)ps * 131072u + vo_ + lane * 16, sv);
  };
  __builtin_amdgcn_s_setprio(2);
  unsigned cap = 60000;
  int sl = 0;
  for (int s = 0; s < NSEG; ++s) {
    TSDECL;
    const int sn = sl == 2 ? 0 : sl + 1;
    const char* ag = xb + (size_t)((sl * 4 + (AREP == 4 ? xg : xg >> 1)) * 2 + (wave >> 2)) * 16384 + (wave & 3) * 2048;
    const char* vd = xb + (size_t)sl * 131072 + vo_ + (wave & 2) * 256 + (wave & 1) * 128;
    const char* os = ob + (size_t)((sl * 4 + (OREP == 4 ? xg : ORSEL)) * 8 + hg) * 16384 + ((r >> 1) & 1) * 1024;
    const char* ad = xb + (size_t)((sn * 4 + (wave & 3)) * 2) * 16384 + po;
    if (s > 0) delay64<E_AG>();
    TS(0);
    unsigned long long tc; unsigned sp;
    unsigned long long* tsp = nullptr;
    vr_v(W, ag, vd, wave * 2048, (wave & 3) * 512, cap, sp, tc, tsp);
    TN(12, sp + 1);
    if (s == NSEG - 1) break;
    if (wave == 7 && s > 0) rearm(sl == 0 ? 2 : sl - 1, true);
    wait_clk((unsigned)tc, E_O);
    TS(3);
    const unsigned tb = bell_time(xb + (size_t)(48 + xg) * 16384 + hg * 512, (lane_id() & 3) * 128, (unsigned)(s + 1), cap);
    wait_rt(tb, D_O);
    TS(4);
    vr_r(W, os, ad, wave * 512, jr * 2048, cap, sp, tsp);
    TN(14, (sp & 0xFFFF) + 1);
    if (wave == 1) TSFLUSH(0x6078);
    sl = sn;
  }
  __builtin_amdgcn_s_setprio(0);
  if (cap == 0 && lane_id() == 0) st4_sc1(tmo, 2u);
}
struct ModIn { u32x4 w[8]; unsigned cv; };
__device__ __forceinline__ void mod_issue(ModIn& I, int wave_id_, int j, const u16* __restrict__ c, const u16* __restrict__ wmod) {
  const int tid = opaque_tid(), k0 = (j >> 5) * 512 + (tid >> 3);
  I.cv = c[k0 + 64 * (tid & 7)];
#pragma unroll
  for (int i = 0; i < 8; ++i) I.w[i] = ldnt<1>(wmod + (size_t)(k0 + 64 * i) * (6 * DM) + (j & 31) * 64 + (tid & 7) * 8);
}
__device__ __forceinline__ void mod_job(char* smem, int wave_id_, int j, u64 tag, const ModIn& I, char* ws) {
  const int tid = opaque_tid(), lane = tid & 63, wave = WAVE_ID, cg = tid & 7;
  const float cv = bflo(I.cv), sl = bfr(cv / (1.0f + __expf(-cv)));
  float acc[8] = {0, 0, 0, 0, 0, 0, 0, 0};
#pragma unroll
  for (int i = 0; i < 8; ++i) {
    const float sv = __shfl(sl, (lane & 56) + i);
#pragma unroll
    for (int e = 0; e < 4; ++e) { acc[2 * e] += sv * bflo(I.w[i][e]); acc[2 * e + 1] += sv * bfhi(I.w[i][e]); }
  }
#pragma unroll
  for (int e = 0; e < 8; ++e) acc[e] = red16_32_add(add_ror8(acc[e]));
  float* red = (float*)(smem + L_GB);
  if (lane < 8) { *(f32x4*)(red + wave * 64 + cg * 8) = f32x4{acc[0], acc[1], acc[2], acc[3]}; *(f32x4*)(red + wave * 64 + cg * 8 + 4) = f32x4{acc[4], acc[5], acc[6], acc[7]}; }
  __syncthreads();
  if (tid < 32) {
    f32x2 s = f32x2{0, 0};
#pragma unroll
    for (int r = 0; r < 8; ++r) s += *(const f32x2*)(red + r * 64 + tid * 2);
    st16_sc1(ws + OFF_MOD + (j * 32 + tid) * 16, u32x4{__float_as_uint(s[0]), (unsigned)tag, __float_as_uint(s[1]), tag2(tag)});
  }
  BAR();
}
#define NIMM 3
constexpr unsigned TK(int m, int hl, int part, int bo, int be) { return (unsigned)(m | hl << 2 | part << 5 | bo << 8 | be << 16); }
#define QX 31
#define CA 36
#define CB 40
#define C2 44
constexpr unsigned task_tab(int t, int it) {
  return t >= 8 && t < 13 ? (it == 0 ? TK(1, t - 8, 0, 0, 64) : it == 1 ? (t < 12 ? TK(2, t - 4, 1, C2, 64) : TK(1, 5, 1, C2, 64)) : 0)
       : it > 0 ? 0
       : t < 8 ? TK(0, t, 1, QX, 64)
       : t < 16 ? TK(1, t - 8, 0, 0, C2)
       : t < 24 ? TK(2, t - 16, 0, 0, t < 18 ? CA : (t < 20 ? CB : C2))
       : t < 26 ? TK(2, t - 24, 1, CA, 64)
       : t == 26 ? TK(1, 6, 1, C2, (C2 + 64) / 2) : t == 27 ? TK(1, 6, 2, (C2 + 64) / 2, 64)
       : t < 30 ? TK(2, t - 26, 1, CB, 64)
       : t == 30 ? TK(1, 7, 1, C2, (C2 + 64) / 2) : t == 31 ? TK(1, 7, 2, (C2 + 64) / 2, 64)
       : TK(0, t - 32, 0, 0, QX);
}
template <int IT, int I = 0> __device__ __forceinline__ unsigned task_sel(int t) {
  if constexpr (I == 40) return 0u;
  else { unsigned r = task_sel<IT, I + 1>(t); constexpr unsigned c = task_tab(I, IT); if (c) asm volatile("s_cmp_eq_u32 %1, %2\n\ts_cselect_b32 %0, %3, %0" : "+s"(r) : "s"(t), "i"(I), "i"(c) : "scc"); return r; }
}
__global__ void __launch_bounds__(512, 2) k_mega(const u16* __restrict__ Wq, const u16* __restrict__ Wk, const u16* __restrict__ Wv,
                                                 const u16* __restrict__ Wo, const u16* __restrict__ w1, const u16* __restrict__ w3,
                                                 const u16* __restrict__ w2, const u16* __restrict__ x, const u16* __restrict__ cvec,
                                                 const u16* __restrict__ wmod, const u16* __restrict__ mem0, char* ws, u16* out) {
  extern __shared__ __attribute__((aligned(16))) char smem[];
  int wave_id_ = __builtin_amdgcn_readfirstlane((int)threadIdx.x >> 6);
  unsigned bid = blockIdx.x;
  asm volatile("" : "+s"(wave_id_), "+s"(bid));
  unsigned* tmo = (unsigned*)(ws + OFF_TMO);
  const unsigned tst = (unsigned)__builtin_amdgcn_s_memrealtime();
  unsigned long long* mts = nullptr;
  const float* mod = (const float*)(ws + OFF_MOD);
  volatile LAS unsigned* tslot = (volatile LAS unsigned*)(smem + L_SLOT + 768);
#define CPF 131072
  if (CPF && wave_id_ == 1) {
    u64 pc; asm volatile("s_getpc_b64 %0" : "=s"(pc));
    const unsigned o = (((bid >> 3) & 31) * 64 + lane_id()) * 64;
    if (o < CPF) asm volatile("s_mov_b32 m0, %0\n\ts_nop 0\n\tglobal_load_lds_dword %1, %2" :: "s"(155648 + 1024), "v"(o), "s"(pc & ~(u64)127) : "memory", "m0");
  }
  unsigned xcc; asm volatile("s_getreg_b32 %0, hwreg(HW_REG_XCC_ID)" : "=s"(xcc));
  xcc &= 7;
  const unsigned pred = xcc * 32 + ((bid >> 3) & 31);
  ModIn mi;
  auto modj = [](unsigned sl) { return sl >= 128 && ((sl + 8) & 31) < 16 ? (int)(((sl >> 5) & 3) * 16 + (sl & 15)) : -1; };
  if (modj(pred) >= 0) mod_issue(mi, wave_id_, modj(pred), cvec, wmod);
  u64 tag;
  asm volatile("s_load_dwordx2 %0, %1, 0x0\n\ts_waitcnt lgkmcnt(0)" : "=s"(tag) : "s"(ws + OFF_TAG) : "memory");
  tag += 1;
  if (opaque_tid() == 0) {
    unsigned sl = pred;
    const unsigned s0 = sl;
    for (unsigned i = 1; i <= 256; ++i) {
      if (__hip_atomic_exchange((u64*)(ws + OFF_CLAIM + sl * 128), tag, __ATOMIC_RELAXED, __HIP_MEMORY_SCOPE_AGENT) != tag) break;
      sl = i == 256 ? 0xFFFFu : (((s0 + (i & ~31u)) & 0xE0u) | ((s0 + i) & 31u));
    }
    *tslot = sl;
  }
  BAR();
  const int slot = (int)__builtin_amdgcn_readfirstlane(*tslot);
  BAR();
  if (slot >= 256) return;
  if (modj(slot) >= 0) {
    if (slot != (int)pred) mod_issue(mi, wave_id_, modj(slot), cvec, wmod);
    mod_job(smem, wave_id_, modj(slot), tag, mi, ws);
  }
  unsigned* cnt = nullptr;
  const int xg = slot >> 5, t = xg == 3 ? 24 + (slot & 7) : slot & 31;
  if (xg < 3) {
    const int nq = xg == 2 ? 8 : 12;
    if (t < nq) { const int h = xg * 12 + t; qk_body(smem, wave_id_, xg, h, (h >> 3) * 32 + (h & 7), Wq, Wk, ws + OFF_K, ws + OFF_V, ws, tmo, tag); }
    else { const int r = xg * 20 + t - nq; vr_body(smem, wave_id_, xg, r >> 1, r & 1, r, (r >> 4) * 32 + 8 + (r & 15), Wv, Wo, Wq, Wk, ws, tmo, tag); }
    return;
  }
  const bool cx = xg == 3;
  const int xq = cx ? (slot >> 3) & 3 : xg & 3, wsl = xq * 32 + t;
#define GDEL 1200
#define GDX 1200
  if (cx || t < 8) l1_role(smem, wave_id_, cx, xq * 8 + (t & 7), x, mod, mem0, ws + OFF_H1, cnt, tag, tmo, mts);
#define KH 0
  if (KH && !cx && t >= 24) {
    const int it = (t & 7) * 8 + wave_id_, lane = lane_id();
    touch(((it & 16) ? Wk : Wv) + (size_t)(256 + (it & 8) * 64 + (it & 7) * 32) * HD + (8 * xq + 1 + 4 * (it >> 5)) * DH,
          (unsigned)((lane >> 1) * HD * 2 + (lane & 1) * 128), 155648 + wave_id_ * 256);
  }
  if (t < 8 || t >= 24) {
    while ((unsigned)__builtin_amdgcn_s_memrealtime() - tst < (cx ? GDX : GDEL)) __builtin_amdgcn_s_sleep(8);
    if (!cx && t >= 24) {
      cnt = cnt_of(ws, init_wait(ws, tag));
      mod2_job(smem, wave_id_, xq * 8 + t - 24, mod, cvec, wmod, cnt);
    }
  }
  auto u2_of = [&]() { return cx ? -1 : (t >= 13 && t < 16 ? t - 13 : (t >= 20 && t < 24 ? t - 17 : -1)); };
  auto f_of = [&]() {
    int fl = t < 8 ? t : (t < 18 ? t - 4 : (t < 20 ? t - 10 : t - 14));
    const int sa = (3 + 2 * xq) & 7, sb = sa + 8 < 14 ? sa + 8 : 13;
    if (fl == 12) fl = sa; else if (fl == sa) fl = 12;
    else if (fl == 13) fl = sb; else if (fl == sb) fl = 13;
    return (int)__builtin_amdgcn_readfirstlane(14 * xq + fl);
  };
  unsigned tk = task_sel<0>(cx ? t + 8 : t), t2 = task_sel<1>(cx ? t + 8 : t), t3 = task_sel<2>(cx ? t + 8 : t);
  for (int it = 0; tk != 0; ++it, tk = t2, t2 = t3, t3 = 0) {
    const int m = tk & 3, hl = (tk >> 2) & 7, part = (tk >> 5) & 3, bo = (tk >> 8) & 255, ns = (int)(tk >> 16) - bo, h = 8 * xq + hl;
    const bool first = m != 0 && bo == 0, late = it > 0;
    const int lane = lane_id();
    const Dep d{nullptr, (unsigned)((lane < 32 ? PL1 : PL1B - 32) + lane) * 128, 8u, (unsigned)((lane < 32 ? 0 : 8) - bo)};
    if (wave_id_ == 0 && late) wait_l1(cnt + (PL1B - PL1) * 32, 7u, tmo);
    BAR();
    gemm7<R_QKV>(smem, wave_id_, (m == 0 ? Wq : (m == 1 ? Wk : Wv)) + h * DH, nullptr, HD, ws + OFF_H1 + (size_t)bo * 65536, 65536, d,
                    ws + (m == 0 ? OFF_Q : (m == 1 ? OFF_K : OFF_V)) + h * 8192 + (size_t)bo * 32 * 8192, 32 * 8192,
                    cnt, (PQKV + h) * 32 + 4 * part + m, tmo, wsl, mts, false, first ? NIMM : 0, ns, late ? ns : 0, bo * 257, tag);
  }
  __syncthreads();
  const int u2 = u2_of();
  if (cx || (u2 >= 0 && (xq < 3 || u2 < 3))) {
    const int ks = cx || xq < 3 ? xq : u2, cn = cx ? (t + 1) & 7 : (xq < 3 ? u2 : 7);
    const int lane = lane_id();
    const Dep d{nullptr, (unsigned)(cx ? (PSA + 4 * xq + ((lane >> 3) & 3)) * 128 + (lane & 7) * 4 : (PF1 + 16 * ks + (lane & 15)) * 128), cx ? 4u : 1u, cx ? (unsigned)((lane >> 1) & 3) : 0u};
    gemm7<R_PART>(smem, wave_id_, (cx ? Wo : w2) + (size_t)ks * 1024 * DM + cn * 128, nullptr, DM, ws + (cx ? OFF_O : OFF_G) + ks * 16 * 4096, (cx ? 64 : 56) * 4096, d,
                  ws + (cx ? OFF_P : OFF_P2) + (ks * DM + cn * 128) * (TLP ? 64 : 2), (size_t)32 * PROW, cnt, ((cx ? PWO : PF2) + ks * 8 + cn) * 32, tmo,
                  128 + xq * 32 + (cx ? cn : 22 + u2), mts, !cx);
  } else if (u2 >= 0) {
    const int c4 = u2 - 3;
    const int lane = lane_id();
    const Dep d{nullptr, (unsigned)(PF1 + 48 + (lane & 7)) * 128, 1u, 0u};
    gemm7<R_PART2>(smem, wave_id_, w2 + (size_t)3072 * DM + c4 * 256, nullptr, DM, ws + OFF_G + 48 * 4096, 56 * 4096, d,
                   ws + OFF_P2 + (3 * DM + c4 * 256) * (TLP ? 64 : 2), (size_t)32 * PROW, cnt, (PF2 + 24 + c4) * 32, tmo, 128 + xq * 32 + 22 + u2, mts, true);
  } else if (t < 8 || (t >= 16 && t < 20) || t == 24 || t == 25) {
    int f = f_of();
    asm volatile("" : "+s"(f));
    const int lane = lane_id();
    const Dep d{nullptr, (unsigned)((PLN + (lane >> 3)) * 128 + (lane & 7) * 4), 1u, 0u};
    gemm7<R_F1>(smem, wave_id_, w1 + 64 * f, w3 + 64 * f, FF, ws + OFF_H2, 65536, d, ws + OFF_G + f * 4096, 56 * 4096, cnt, (PF1 + f) * 32, tmo, 128 + xq * 18 + 8 + f, mts, false);
  } else if (t >= 9 && t < 13) {
    fin_role(smem, wave_id_, xq * 4 + t - 9, mod, ws + OFF_P2, (const u16*)(ws + OFF_X1), out, cnt, tmo, mts);
    if (xq == 3 && t == 12 && opaque_tid() == 0) {
      const u32x4 v = ld16_sc1(ws + OFF_TAG);
      const u64 tn = (((u64)v.y << 32) | v.x) + 1;
      st8_sc1(ws + OFF_TAG, u32x2{(unsigned)tn, (unsigned)(tn >> 32)});
    }
  } else if (t == 28 || t == 29) {
    row_role(smem, wave_id_, 8 * xq, t - 28, mod, ws + OFF_P, x, (u16*)(ws + OFF_X1), ws + OFF_H2, cnt, tmo, 256 + xq, mts);
  } else if (t >= 26) {
    sa_role(smem, wave_id_, 8 * xq + 2 * (t >= 30 ? t - 30 : t - 24), ws + OFF_Q, ws + OFF_K, ws + OFF_V, ws + OFF_KVM, ws + OFF_O, cnt, tmo, 128 + xq * 32 + 30 + (t & 1), mts);
  }
}

extern "C" void kernel_launch(void* const* d_in, const int* in_sizes, int n_in, void* d_out, int out_size, void* d_ws, size_t ws_size,
                              hipStream_t stream) {
  const u16* x    = (const u16*)d_in[0];
  const u16* mem0 = (const u16*)d_in[1];
  const u16* Wq   = (const u16*)d_in[2];
  const u16* Wk   = (const u16*)d_in[3];
  const u16* Wv   = (const u16*)d_in[4];
  const u16* Wo   = (const u16*)d_in[5];
  const u16* w1   = (const u16*)d_in[8];
  const u16* w3   = (const u16*)d_in[9];
  const u16* w2   = (const u16*)d_in[10];
  const u16* c    = (const u16*)d_in[11];
  const u16* wmod = (const u16*)d_in[12];
#define WSHIFT 0
  char* ws = (char*)d_ws + ((size_t)WSHIFT << 20);
  hipFuncSetAttribute((const void*)k_mega, hipFuncAttributeMaxDynamicSharedMemorySize, MEGA_LDS);
  k_mega<<<256, 512, MEGA_LDS, stream>>>(Wq, Wk, Wv, Wo, w1, w3, w2, x, c, wmod, mem0, ws, (u16*)d_out);
}
```

```cpp
#include <hip/hip_runtime.h>
#include <hip/hip_bf16.h>
#include <stdint.h>
#include <type_traits>
#pragma clang diagnostic ignored "-Winline-asm"
#pragma clang diagnostic ignored "-Wunused-value"

typedef unsigned short u16;
typedef short s16x4 __attribute__((ext_vector_type(4)));
typedef __bf16 bf16x8 __attribute__((ext_vector_type(8)));
typedef float f32x4 __attribute__((ext_vector_type(4)));
typedef float f32x2 __attribute__((ext_vector_type(2)));
typedef unsigned u32x4 __attribute__((ext_vector_type(4)));
typedef unsigned u32x2 __attribute__((ext_vector_type(2)));
#define LAS __attribute__((address_space(3)))

constexpr int SEQ = 2048, DM = 1024, NH = 32, DH = 128, HD = 4096, NMEM = 8, SEG = 32, NSEG = 64, FF = 3584;
constexpr unsigned SENT = 0xFFFFFFFFu;

__device__ __forceinline__ int lane_id() { int l; asm volatile("v_mbcnt_lo_u32_b32 %0, -1, 0\n\tv_mbcnt_hi_u32_b32 %0, -1, %0" : "=v"(l)); return l; }
#define WAVE_ID wave_id_
#define opaque_tid() (WAVE_ID * 64 + lane_id())
__device__ __forceinline__ float bf2f(u16 v) { return __builtin_bit_cast(float, (unsigned)v << 16); }
__device__ __forceinline__ float bflo(unsigned v) { return __builtin_bit_cast(float, v << 16); }
__device__ __forceinline__ float bfhi(unsigned v) { return __builtin_bit_cast(float, v & 0xffff0000u); }
__device__ __forceinline__ float bfr(float x) { return (float)(__bf16)x; }
__device__ __forceinline__ unsigned pack2(float a, float b) {
  typedef __bf16 bf16x2 __attribute__((ext_vector_type(2)));
  bf16x2 v; v[0] = (__bf16)a; v[1] = (__bf16)b; return __builtin_bit_cast(unsigned, v);
}
__device__ __forceinline__ unsigned nosent(unsigned v) { return v == SENT ? 0x7FC07FC0u : v; }
__device__ __forceinline__ bf16x8 asbf8(u32x4 v) { return __builtin_bit_cast(bf16x8, v); }
__device__ __forceinline__ s16x4 ass4(u32x2 v) { return __builtin_bit_cast(s16x4, v); }
__device__ __forceinline__ u32x2 trread(const char* lds_base, unsigned off) {
  s16x4 v = __builtin_amdgcn_ds_read_tr16_b64_v4i16((LAS s16x4*)(lds_base + off));
  return __builtin_bit_cast(u32x2, v);
}
__device__ __forceinline__ void glds16(const void* gsrc, unsigned lds_dst) {
  unsigned keep;
  asm volatile("s_mov_b32 %0, m0\n\ts_mov_b32 m0, %2\n\ts_nop 0\n\tglobal_load_lds_dwordx4 %1, off\n\ts_mov_b32 m0, %0"
               : "=&s"(keep) : "v"(gsrc), "s"(lds_dst) : "memory");
}
#define CNT_ " nt"
#define WNT_ " nt"
__device__ __forceinline__ void glds16_w(const void* gsrc, unsigned lds_dst) {
  unsigned keep;
  asm volatile("s_mov_b32 %0, m0\n\ts_mov_b32 m0, %2\n\ts_nop 0\n\tglobal_load_lds_dwordx4 %1, off" CNT_ "\n\ts_mov_b32 m0, %0"
               : "=&s"(keep) : "v"(gsrc), "s"(lds_dst) : "memory");
}
__device__ __forceinline__ void glds16_sc1(const void* gsrc, unsigned lds_dst) {
  unsigned keep;
  asm volatile("s_mov_b32 %0, m0\n\ts_mov_b32 m0, %2\n\ts_nop 0\n\tglobal_load_lds_dwordx4 %1, off sc1\n\ts_mov_b32 m0, %0"
               : "=&s"(keep) : "v"(gsrc), "s"(lds_dst) : "memory");
}
#define NTM 1
template <int B> __device__ __forceinline__ u32x4 ldnt(const void* p) { if (NTM & B) return __builtin_nontemporal_load((const u32x4*)p); else return *(const u32x4*)p; }
#define MFMA32(a, b, c) __builtin_amdgcn_mfma_f32_16x16x32_bf16((a), (b), (c), 0, 0, 0)

__device__ __forceinline__ void st16_sc1(void* p, u32x4 v) { asm volatile("global_store_dwordx4 %0, %1, off sc1\n\ts_nop 1" :: "v"(p), "v"(v) : "memory"); }
__device__ __forceinline__ void st8_sc1(void* p, u32x2 v) { asm volatile("global_store_dwordx2 %0, %1, off sc1\n\ts_nop 1" :: "v"(p), "v"(v) : "memory"); }
template <bool WT> __device__ __forceinline__ void st8(void* p, u32x2 v) { if (WT) st8_sc1(p, v); else *(u32x2*)p = v; }
template <bool WT> __device__ __forceinline__ void st16(void* p, u32x4 v) { if (WT) st16_sc1(p, v); else *(u32x4*)p = v; }
__device__ __forceinline__ u32x4 ld16_sc1(const void* p) {
  u32x4 r; asm volatile("global_load_dwordx4 %0, %1, off sc1\n\ts_waitcnt vmcnt(0)" : "=&v"(r) : "v"(p) : "memory"); return r;
}
__device__ __forceinline__ void ld16x4_sc1(const void* p, u32x4& a, u32x4& b, u32x4& c, u32x4& d) {
  asm volatile("global_load_dwordx4 %0, %4, off sc1\n\tglobal_load_dwordx4 %1, %4, off offset:1024 sc1\n\t"
               "global_load_dwordx4 %2, %4, off offset:2048 sc1\n\tglobal_load_dwordx4 %3, %4, off offset:3072 sc1\n\ts_waitcnt vmcnt(0)"
               : "=&v"(a), "=&v"(b), "=&v"(c), "=&v"(d) : "v"(p) : "memory");
}
typedef __amdgpu_buffer_rsrc_t rsrc_t;
__device__ __forceinline__ rsrc_t mkrs(const void* p) { return __builtin_amdgcn_make_buffer_rsrc((void*)p, 0, 0x7FFFFFFF, 0x00020000); }
__device__ __forceinline__ u32x4 bld16(rsrc_t r, unsigned off) { return __builtin_amdgcn_raw_buffer_load_b128(r, off, 0, 16); }
__device__ __forceinline__ u32x2 bld8(rsrc_t r, unsigned off) { return __builtin_amdgcn_raw_buffer_load_b64(r, off, 0, 16); }
__device__ __forceinline__ bool ok4(u32x4 v) { return v.x != SENT && v.y != SENT && v.z != SENT && v.w != SENT; }
constexpr int SPIN_CAP = 2000000;
__device__ __forceinline__ bool give_up(int spins, unsigned* tmo, unsigned code) {
  if ((spins & 4095) != 4095) return false;
  if (spins > SPIN_CAP) { __hip_atomic_store(tmo, code, __ATOMIC_RELAXED, __HIP_MEMORY_SCOPE_AGENT); return true; }
  return __hip_atomic_load(tmo, __ATOMIC_RELAXED, __HIP_MEMORY_SCOPE_AGENT) != 0u;
}
template <bool ACQ = true>
__device__ __forceinline__ void wait_cnt(unsigned* c, unsigned n, unsigned* tmo) {
  for (int spins = 0;; ++spins) {
    const unsigned v = __hip_atomic_load(c, __ATOMIC_RELAXED, __HIP_MEMORY_SCOPE_AGENT);
    if (v >= n) break;
    __builtin_amdgcn_s_sleep(8);
    if (give_up(spins, tmo, 5)) break;
  }
  if (ACQ) {
    __builtin_amdgcn_fence(__ATOMIC_ACQUIRE, "agent");
    asm volatile("s_waitcnt vmcnt(0)" ::: "memory");
  }
}

__device__ __forceinline__ const char* uni(const char* p) {
  const unsigned long long v = (unsigned long long)p;
  const unsigned lo = __builtin_amdgcn_readfirstlane((unsigned)v), hi = __builtin_amdgcn_readfirstlane((unsigned)(v >> 32));
  return (const char*)(((unsigned long long)hi << 32) | lo);
}
#define TSCLK "s_memrealtime"
#define TC(k) do { } while (0)
#define TS(k) do { } while (0)
#define TSDECL do { } while (0)
#define TSFLUSH(mask) do { } while (0)

constexpr size_t OFF_MOD  = 0;
constexpr size_t OFF_MOD2 = 65536;
constexpr size_t OFF_TMO  = 98304;
constexpr size_t OFF_TAG  = 131072;
constexpr size_t OFF_CLAIM = 135168;
constexpr size_t OFF_INIT = 196608;
constexpr size_t OFF_H1   = 262144;
constexpr size_t OFF_Q    = OFF_H1 + (size_t)SEQ * DM * 2;
constexpr size_t OFF_K    = OFF_Q + (size_t)SEQ * HD * 2;
constexpr size_t OFF_V    = OFF_K + (size_t)SEQ * HD * 2;
constexpr size_t OFF_O    = OFF_V + (size_t)SEQ * HD * 2;
constexpr size_t OFF_X1   = OFF_O + (size_t)SEQ * HD * 2;
constexpr size_t OFF_H2   = OFF_X1 + (size_t)SEQ * DM * 2;
constexpr size_t OFF_G    = OFF_H2 + (size_t)SEQ * DM * 2;
constexpr size_t OFF_KVM  = (size_t)92 << 20;
constexpr size_t OFF_XCH  = (size_t)100 << 20;
constexpr size_t OFF_P    = (size_t)112 << 20;
constexpr size_t OFF_P2   = (size_t)128 << 20;
constexpr size_t OFF_DUMP = (size_t)144 << 20;
constexpr int PROW = 4 * DM * 2;
#define TLQ 1
#define TLP 1
static_assert(OFF_G + (size_t)SEQ * FF * 2 <= OFF_KVM, "layout");
__device__ __forceinline__ char* xch_base(char* ws) { return (char*)(((size_t)(ws + OFF_XCH) + ((2u << 20) - 1)) & ~(size_t)((2u << 20) - 1)); }
#define XPAR 0
constexpr int PROG = 16;
constexpr int PQKV = 64;
constexpr int PL1 = 96;
constexpr int PL1B = 350;
constexpr int PSA = 128;
constexpr int PWO = 192;
constexpr int PLN = 224;
constexpr int PF2 = 320;
constexpr int PF1 = 448;
#define SDIR 40
#define SB 48
#define MARG 20
#define MARL 15
#define MARF 8
#define SAW 0
constexpr int L_RED = 131072;
constexpr int L_SLOT = L_RED + 16384;
constexpr int L_GB = L_SLOT + 1024;
constexpr int MEGA_LDS = 163840;
static_assert(L_GB + 12288 + 2048 <= MEGA_LDS, "lds");
#define XPARC 0

__device__ __forceinline__ int bswz(int row) { return (((row >> 1) & 1) | (((row >> 3) & 1) << 1)) << 1; }
template <int OFF, bool SC1 = false>
__device__ __forceinline__ void glds16s(const char* sbase, unsigned voff, unsigned lds_base) {
  unsigned keep;
  if (SC1)
    asm volatile("s_mov_b32 %0, m0\n\ts_add_u32 m0, %3, %4\n\ts_nop 2\n\tglobal_load_lds_dwordx4 %1, %2 sc1\n\ts_mov_b32 m0, %0"
                 : "=&s"(keep) : "v"(voff), "s"(sbase), "s"(lds_base), "i"(OFF) : "memory", "scc");
  else
    asm volatile("s_mov_b32 %0, m0\n\ts_add_u32 m0, %3, %4\n\ts_nop 2\n\tglobal_load_lds_dwordx4 %1, %2\n\ts_mov_b32 m0, %0"
                 : "=&s"(keep) : "v"(voff), "s"(sbase), "s"(lds_base), "i"(OFF) : "memory", "scc");
}
template <int OFF>
__device__ __forceinline__ void glds16x2s(const char* sbase, unsigned voff, unsigned lds_base) {
  unsigned keep;
  asm volatile("s_mov_b32 %0, m0\n\ts_add_u32 m0, %3, %4\n\ts_nop 2\n\tglobal_load_lds_dwordx4 %1, %2 sc1\n\tglobal_load_lds_dwordx4 %1, %2 offset:1024 sc1\n\ts_mov_b32 m0, %0"
               : "=&s"(keep) : "v"(voff), "s"(sbase), "s"(lds_base), "i"(OFF) : "memory", "scc");
}
__device__ __forceinline__ void st16s(const char* sb, unsigned vo, u32x4 v) { asm volatile("global_store_dwordx4 %0, %1, %2 sc1\n\ts_nop 1" :: "v"(vo), "v"(v), "s"(sb) : "memory"); }
__device__ __forceinline__ void head_to_lds(const char* src, int lane, unsigned lds) {
  if (TLQ) {
    const int c = (lane & 15) ^ (lane >> 4);
    const unsigned v0 = (unsigned)((c >> 2) * 2048 + (lane >> 4) * 64 + (c & 3) * 16);
#pragma unroll
    for (int i = 0; i < 8; ++i) glds16s<0, true>(src + i * 256, v0 ^ ((i & 3) * 2048), lds + i * 1024);
  } else {
#pragma unroll
    for (int i = 0; i < 4; ++i) glds16x2s<0>(src + i * 2048, (unsigned)lane * 16, lds + i * 2048);
  }
}
#define TCHP ""
__device__ __forceinline__ void touch(const void* sb, unsigned vo, unsigned lds) {
  asm volatile("s_mov_b32 m0, %0\n\ts_nop 0\n\tglobal_load_lds_dword %1, %2" TCHP :: "s"(lds), "v"(vo), "s"(sb) : "memory", "m0");
}
#define WAIT_V(n) asm volatile("s_waitcnt vmcnt(" #n ")" ::: "memory")
#define BAR() do { asm volatile("s_waitcnt lgkmcnt(0)" ::: "memory"); __builtin_amdgcn_s_barrier(); asm volatile("" ::: "memory"); } while (0)
__device__ __forceinline__ void st4_sc1(void* p, unsigned v) { asm volatile("global_store_dword %0, %1, off sc1" :: "v"(p), "v"(v) : "memory"); }
__device__ __forceinline__ unsigned ld4_sc1(const void* p) {
  unsigned r; asm volatile("global_load_dword %0, %1, off sc1\n\ts_waitcnt vmcnt(0)" : "=&v"(r) : "v"(p) : "memory"); return r;
}
template <int CTRL> __device__ __forceinline__ unsigned dppu(unsigned x) { return (unsigned)__builtin_amdgcn_update_dpp(0, (int)x, CTRL, 0xf, 0xf, false); }
__device__ __forceinline__ unsigned rowmin16(unsigned v) {
  v = min(v, dppu<0x128>(v)); v = min(v, dppu<0x124>(v)); v = min(v, dppu<0x122>(v)); v = min(v, dppu<0x121>(v));
  return v;
}
__device__ __forceinline__ unsigned rdl(unsigned v, int l) { return (unsigned)__builtin_amdgcn_readlane((int)v, l); }
__device__ __forceinline__ unsigned qkv_avail(unsigned v, int m) {
  unsigned a = rdl(v, m);
#pragma unroll
  for (int j = 1; j < 4; ++j) { const unsigned p = rdl(v, 4 * j + m); if ((p >> 8) <= a) a = max(a, p & 255u); }
  return a;
}
constexpr float SCALE = 0.08838834764831845f;
constexpr float LOG2E = 1.4426950408889634f;
template <int N> __device__ __forceinline__ void delay64() {
  if (N >= 16) { __builtin_amdgcn_s_sleep(15); delay64<(N >= 16 ? N - 15 : 0)>(); } else if (N > 0) __builtin_amdgcn_s_sleep(N);
}
__device__ __forceinline__ void sw16(float x, float& a, float& b) {
  a = x; b = x; asm volatile("s_nop 1\n\tv_permlane16_swap_b32 %0, %1" : "+v"(a), "+v"(b));
}
__device__ __forceinline__ void sw32(float x, float& a, float& b) {
  a = x; b = x; asm volatile("s_nop 1\n\tv_permlane32_swap_b32 %0, %1" : "+v"(a), "+v"(b));
}
__device__ __forceinline__ float red16_32_max(float x) {
  float a, b; sw16(x, a, b); x = fmaxf(a, b); sw32(x, a, b); return fmaxf(a, b);
}
__device__ __forceinline__ float red16_32_add(float x) {
  float a, b; sw16(x, a, b); x = a + b; sw32(x, a, b); return a + b;
}
__device__ __forceinline__ unsigned cvtpk(float a, float b) { unsigned r; asm("v_cvt_pk_bf16_f32 %0, %1, %2" : "=v"(r) : "v"(a), "v"(b)); return r; }
__device__ __forceinline__ float max3(float a, float b, float c) { float r; asm("v_max3_f32 %0, %1, %2, %3" : "=v"(r) : "v"(a), "v"(b), "v"(c)); return r; }
__device__ __forceinline__ float max2(float a, float b) { float r; asm("v_max_f32 %0, %1, %2" : "=v"(r) : "v"(a), "v"(b)); return r; }
__device__ __forceinline__ unsigned ror8u(unsigned x) { return (unsigned)__builtin_amdgcn_update_dpp(0, (int)x, 0x128, 0xf, 0xf, false); }
__device__ __forceinline__ float ror8(float x) { return __builtin_bit_cast(float, ror8u(__builtin_bit_cast(unsigned, x))); }
__device__ __forceinline__ float add_ror8(float x) {
  return x + __builtin_bit_cast(float, __builtin_amdgcn_update_dpp(0, __builtin_bit_cast(int, x), 0x128, 0xf, 0xf, false));
}

__device__ __forceinline__ unsigned rt_now() { return (unsigned)__builtin_amdgcn_s_memrealtime(); }
__device__ __forceinline__ unsigned latest(unsigned v) {
  const unsigned now = rt_now() + 400u, age = rowmin16((now - v) & 0xFFFFFFu);
  return now - min(min(min(rdl(age, 0), rdl(age, 16)), min(rdl(age, 32), rdl(age, 48))), 900u);
}
__device__ __forceinline__ void wait_rt(unsigned t0, int d) {
  while ((int)(rt_now() - t0) < d) { }
}
__device__ __forceinline__ bool bells(bool ok, unsigned u, int b, int marg) {
  if (b < SB || !__all(ok || (int)(u >> 24) > b)) return false;
  wait_rt(latest(!ok && (int)(u >> 24) == b + 1 ? u : rt_now() - 400u), marg);
  return true;
}
__device__ __forceinline__ void ld_fb(unsigned& v, unsigned& u, const void* sb, unsigned vo) {
  asm volatile("global_load_dword %0, %2, %3 sc1\n\tglobal_load_dword %1, %2, %3 offset:64 sc1\n\ts_waitcnt vmcnt(0)" : "=&v"(v), "=&v"(u) : "v"(vo), "s"(sb) : "memory");
}
__device__ __forceinline__ unsigned bell(int b, unsigned t) { return (unsigned)(b + 1) << 24 | (t & 0xFFFFFFu); }
typedef unsigned long long u64;
__device__ __forceinline__ unsigned tag2(u64 tag) { return (unsigned)(tag >> 32) ^ ((unsigned)tag * 0x9E3779B1u); }
__device__ __forceinline__ unsigned init_eval(u32x4 v, u64 tag) {
  if (!__all((v.x & ~63u) == (unsigned)tag << 6 && v.y == (unsigned)(tag >> 26))) return 0u;
  unsigned r = 8u;
#pragma unroll
  for (int c = 0; c < 3; ++c) r |= __popcll(__ballot(v.x & (1u << c))) > __popcll(__ballot(v.x & (8u << c))) ? 1u << c : 0u;
  return r ^ (XPAR * 3u + XPARC * 4u);
}
__device__ __forceinline__ unsigned init_wait(const char* ws, u64 tag) {
  for (int spins = 0; spins < SPIN_CAP; ++spins) {
    const unsigned r = init_eval(ld16_sc1(ws + OFF_INIT + (lane_id() & 31) * 128), tag);
    if (r) return r;
    __builtin_amdgcn_s_sleep(4);
  }
  return 8u;
}
__device__ __forceinline__ void init_issue(const char* ws, unsigned lds) { glds16s<0, true>(uni(ws + OFF_INIT), (unsigned)(lane_id() & 31) * 128u, lds); }
__device__ __forceinline__ unsigned init_get(const char* ws, u64 tag, const char* copy) {
  const unsigned r = init_eval(*(volatile LAS u32x4*)(copy + lane_id() * 16), tag);
  return r ? r : init_wait(ws, tag);
}
__device__ __forceinline__ unsigned* cnt_of(char* ws, unsigned in) { return (unsigned*)uni(xch_base(ws) + ((size_t)4 << 21) + (size_t)((in & 4u) << 11)); }
__device__ __forceinline__ void wait_l1(const unsigned* cnt, unsigned n, unsigned* tmo) {
  const int lane = lane_id();
  for (int spins = 0;; ++spins) {
    const unsigned v = ld4_sc1(cnt + (PL1 + (lane & 31)) * 32);
    if (__all(v >= n)) break;
    __builtin_amdgcn_s_sleep(8);
    if (give_up(spins, tmo, 12)) break;
  }
}
#define GKV 1
__device__ __forceinline__ void wait_k0(const unsigned* cnt, int h, unsigned* tmo) {
  for (int spins = 0;; ++spins) {
    const int l = lane_id();
    const unsigned v = ld4_sc1(cnt + (PQKV + h) * 32 + 1 + (l & 1));
    if (__all(v >= (unsigned)GKV)) break;
    __builtin_amdgcn_s_sleep(32);
    if (give_up(spins, tmo, 13)) break;
  }
}
#define WCLK(slot, b) do { } while (0)
#define WTM(slot, b, k) do { } while (0)
#define WTS(slot, b, k) do { } while (0)
#define WFB() do { } while (0)
#define STS(ws_, slot_, k_) do { } while (0)
typedef __bf16 bf16x2 __attribute__((ext_vector_type(2)));
__device__ __forceinline__ f32x2 pkfma(f32x2 a, f32x2 b, f32x2 c) { return __builtin_elementwise_fma(a, b, c); }
__device__ __forceinline__ float wavesum(float v) {
  v += __builtin_bit_cast(float, dppu<0x128>(__builtin_bit_cast(unsigned, v))); v += __builtin_bit_cast(float, dppu<0x124>(__builtin_bit_cast(unsigned, v)));
  v += __builtin_bit_cast(float, dppu<0x122>(__builtin_bit_cast(unsigned, v))); v += __builtin_bit_cast(float, dppu<0x121>(__builtin_bit_cast(unsigned, v)));
  return red16_32_add(v);
}
__device__ __forceinline__ float adds(unsigned v, unsigned sel, float c) { return __builtin_amdgcn_fdot2_f32_bf16(__builtin_bit_cast(bf16x2, v), __builtin_bit_cast(bf16x2, sel), c, false); }
__device__ __forceinline__ void sum4(const u32x4 (&p)[4], f32x2 (&a)[4]) {
  unsigned slo = 0x00003F80u, shi = 0x3F800000u;
  asm("" : "+s"(slo), "+s"(shi));
#pragma unroll
  for (int e = 0; e < 4; ++e) {
    float lo = bflo(p[0][e]), hi = bfhi(p[0][e]);
#pragma unroll
    for (int q = 1; q < 4; ++q) { lo = adds(p[q][e], slo, lo); hi = adds(p[q][e], shi, hi); }
    a[e] = f32x2{lo, hi};
  }
}
__device__ __forceinline__ u32x4 norm8(const f32x2 (&v)[4], const char* mt, float mb, float rden) {
  const f32x4 h0 = *(const f32x4*)(mt + 4096), h1 = *(const f32x4*)(mt + 4096 + 1024), c0 = *(const f32x4*)(mt + 8192), c1 = *(const f32x4*)(mt + 8192 + 1024);
  const f32x2 m2 = f32x2{mb, mb}, d2 = f32x2{rden, rden};
  const f32x2 r0 = pkfma(v[0] - m2, f32x2{c0[0], c0[1]} * d2, f32x2{h0[0], h0[1]}), r1 = pkfma(v[1] - m2, f32x2{c0[2], c0[3]} * d2, f32x2{h0[2], h0[3]});
  const f32x2 r2 = pkfma(v[2] - m2, f32x2{c1[0], c1[1]} * d2, f32x2{h1[0], h1[1]}), r3 = pkfma(v[3] - m2, f32x2{c1[2], c1[3]} * d2, f32x2{h1[2], h1[3]});
  return u32x4{pack2(r0[0], r0[1]), pack2(r1[0], r1[1]), pack2(r2[0], r2[1]), pack2(r3[0], r3[1])};
}
__device__ __forceinline__ void half_load(const char* P, const u16* res, int row, int hf, int lane, f32x2 (&a)[4], u32x4& rv) {
  const rsrc_t rp = mkrs(P), rr = mkrs(res);
  const unsigned po = TLP ? (unsigned)((row >> 5) * 32 * PROW + (hf * 16 + (lane >> 2)) * 2048 + (row & 31) * 64 + (lane & 3) * 16) : (unsigned)(row * PROW + hf * 1024 + lane * 16), ro = (unsigned)(row * DM * 2 + hf * 1024 + lane * 16);
  u32x4 p[4];
#pragma unroll
  for (int q = 0; q < 4; ++q) p[q] = bld16(rp, po + q * (TLP ? 65536 : 2048));
  if (res) rv = bld16(rr, ro);
  sum4(p, a);
}
__device__ __forceinline__ bool fin_half(bool fin, const char* P, const u16* res, const char* gbt, u16* __restrict__ o1, int row, int hf, int lane) {
  f32x2 a[4]; u32x4 rv;
  half_load(P, res, row, hf, lane, a, rv);
  const u32x4 gv = *(const u32x4*)(gbt + hf * 1024 + lane * 16);
  u32x4 o; f32x2 t = f32x2{0, 0};
#pragma unroll
  for (int e = 0; e < 4; ++e) { const f32x2 s = pkfma(f32x2{bflo(gv[e]), bfhi(gv[e])}, a[e], f32x2{bflo(rv[e]), bfhi(rv[e])}); o[e] = pack2(s[0], s[1]); t += s; }
  if (!fin && !__all(t[0] == t[0] && t[1] == t[1])) return false;
  st16_sc1(o1 + (size_t)row * DM + hf * 512 + lane * 8, o);
  return true;
}
#define DLN 10
#define LNE 1
template <int CTRL> __device__ __forceinline__ float dppf(float x) { return __builtin_bit_cast(float, dppu<CTRL>(__builtin_bit_cast(unsigned, x))); }
__device__ __forceinline__ bool ln_row(bool fin, int bb, unsigned* bl, const f32x2 (&a)[2][4], const f32x2 (&xf)[8], const f32x2 (&tg)[8], const f32x2 (&th)[8], const f32x2 (&tc)[8],
                                       const char* x1, const char* h2, unsigned xo, unsigned ho) {
  f32x2 v[8]; u32x4 o[2];
#pragma unroll
  for (int e = 0; e < 8; ++e) { const f32x2 t = pkfma(tg[e], a[e >> 2][e & 3], xf[e]); const unsigned u = pack2(t[0], t[1]); o[e >> 2][e & 3] = u; v[e] = f32x2{bflo(u), bfhi(u)}; }
  const float pv = __builtin_bit_cast(float, __builtin_amdgcn_readfirstlane(__float_as_int(v[0][0])));
  f32x2 d[8];
#pragma unroll
  for (int e = 0; e < 8; ++e) d[e] = v[e] - f32x2{pv, pv};
  const f32x2 s2 = ((d[0] + d[1]) + (d[2] + d[3])) + ((d[4] + d[5]) + (d[6] + d[7]));
  const f32x2 q2 = pkfma(d[3], d[3], pkfma(d[2], d[2], pkfma(d[1], d[1], d[0] * d[0]))) + pkfma(d[7], d[7], pkfma(d[6], d[6], pkfma(d[5], d[5], d[4] * d[4])));
  float s = s2[0] + s2[1], q = q2[0] + q2[1];
  if (!fin && !__all(s == s)) return false;
  if (LNE && bb >= SB && lane_id() == 0) st4_sc1(bl, bell(bb, rt_now() + DLN + 30));
  s += dppf<0x128>(s); q += dppf<0x128>(q); s += dppf<0x124>(s); q += dppf<0x124>(q);
  s += dppf<0x122>(s); q += dppf<0x122>(q); s += dppf<0x121>(s); q += dppf<0x121>(q);
  asm volatile("s_nop 1\n\tv_permlane16_swap_b32 %0, %1" : "+v"(s), "+v"(q));
  s += q;
  { float x, y; sw32(s, x, y); s = x + y; }
  const float sh = __builtin_bit_cast(float, rdl(__builtin_bit_cast(unsigned, s), 0)) * (1.0f / DM), sq = __builtin_bit_cast(float, rdl(__builtin_bit_cast(unsigned, s), 16));
  const float mb = bfr(pv + sh), vb = bfr(fmaxf(sq * (1.0f / DM) - sh * sh, 0.0f));
  const float rden = __builtin_amdgcn_rcpf(bfr(__builtin_amdgcn_sqrtf(bfr(vb + 1e-5f))));
#pragma unroll
  for (int i = 0; i < 2; ++i) {
    u32x4 r;
#pragma unroll
    for (int e = 0; e < 4; ++e) { const f32x2 t = pkfma(v[4 * i + e] - f32x2{mb, mb}, tc[4 * i + e] * f32x2{rden, rden}, th[4 * i + e]); r[e] = pack2(t[0], t[1]); }
    st16s(h2, ho + i * 32768, r);
  }
  if (!LNE && bb >= SB && lane_id() == 0) st4_sc1(bl, bell(bb, rt_now() + DLN));
  st16s(x1, xo, o[0]); st16s(x1, xo + 1024, o[1]);
  return true;
}
__device__ __forceinline__ void row_table(char* tab, const float* mod, const unsigned* cnt, unsigned* tmo, int tid) {
  wait_l1(cnt + 1, 1u, tmo);
  const rsrc_t rm = mkrs((const char*)mod + OFF_MOD2);
#pragma unroll
  for (int i = 0; i < 2; ++i) {
    const int u = tid + 512 * i, a = u >> 8, c = u & 255;
    f32x4 v = __builtin_bit_cast(f32x4, bld16(rm, (unsigned)u * 16));
    v = f32x4{bfr(v[0]), bfr(v[1]), bfr(v[2]), bfr(v[3])};
    if (a == 2) v = f32x4{bfr(1.0f + v[0]), bfr(1.0f + v[1]), bfr(1.0f + v[2]), bfr(1.0f + v[3])};
    if (a < 3) *(f32x4*)(tab + a * 4096 + ((c >> 7) * 2 + (c & 1)) * 1024 + ((c >> 1) & 63) * 16) = v;
    else *(u32x2*)(tab + 12288 + c * 8) = u32x2{pack2(v[0], v[1]), pack2(v[2], v[3])};
  }
  __syncthreads();
}
__device__ __forceinline__ void row_role(char* smem, int wave_id_, int rbase, int par, const float* mod, const char* P, const u16* x, u16* x1, char* h2c,
                                         unsigned* cnt, unsigned* tmo, int slot, unsigned long long* mts) {
  const int tid = opaque_tid(), lane = tid & 63, wave = WAVE_ID;
  if (tid < 80) *(volatile LAS unsigned*)(smem + 16384 + tid * 4) = 0u;
  {
    static_assert(TLP && SB % 2 == 0, "");
    unsigned z = SENT; asm volatile("" : "+v"(z));
    const int r4 = rbase + 4 * par + (wave & 3);
    for (int bb = SB + (wave >> 2); bb < NSEG; bb += 2)
#pragma unroll
      for (int i = 0; i < 2; ++i) {
        st16s(uni((const char*)x1 + (size_t)bb * 65536), (unsigned)(r4 * DM * 2 + i * 1024 + lane * 16), u32x4{z, z, z, z});
        st16s(uni(h2c + (size_t)bb * 65536), (unsigned)(r4 * 128 + (lane & 7) * 16 + (lane >> 3) * 4096 + i * 32768), u32x4{z, z, z, z});
      }
    WAIT_V(0);
  }
  row_table(smem, mod, cnt, tmo, tid);
  const int rb = rbase + wave + 32 * par;
  const rsrc_t rp = mkrs(P), rr = mkrs(x);
  const unsigned l16 = lane * 16;
  const unsigned pwo = (unsigned)(PWO + (lane & 31)) * 128;
  unsigned* myp = cnt + (PLN + (rb >> 3)) * 32 + wave;
  f32x2 a[2][4];
  const int gp = wave >> 2, r4 = rbase + 4 * par + (wave & 3);
  volatile LAS unsigned* lwb = (volatile LAS unsigned*)(smem + 16384 + gp * 8);
  if (gp && lane == 0) st4_sc1(myp, 1u);
  f32x2 tg[8], th[8], tc[8];
#pragma unroll
  for (int i = 0; i < 4; ++i) {
    const char* mt = smem + lane * 16 + i * 1024;
    const f32x4 g_ = *(const f32x4*)mt, h_ = *(const f32x4*)(mt + 4096), c_ = *(const f32x4*)(mt + 8192);
    tg[2 * i] = f32x2{g_[0], g_[1]}; tg[2 * i + 1] = f32x2{g_[2], g_[3]}; th[2 * i] = f32x2{h_[0], h_[1]}; th[2 * i + 1] = f32x2{h_[2], h_[3]};
    tc[2 * i] = f32x2{c_[0], c_[1]}; tc[2 * i + 1] = f32x2{c_[2], c_[3]};
  }
  const unsigned xo = (unsigned)(r4 * DM * 2 + lane * 16), ho = (unsigned)(r4 * 128 + (((lane & 7) ^ (r4 & 7)) << 4) + (lane >> 3) * 4096);
  const unsigned po = (l16 >> 6) * 2048 + (l16 & 48);
  u32x4 rv[2];
#pragma unroll
  for (int i = 0; i < 2; ++i) rv[i] = __builtin_amdgcn_raw_buffer_load_b128(rr, xo + i * 1024, (unsigned)gp * 65536u, 0);
  unsigned wb = 0;
  for (int bb = gp; bb < NSEG; bb += 2) {
    f32x2 xf[8];
#pragma unroll
    for (int e = 0; e < 8; ++e) xf[e] = f32x2{bflo(rv[e >> 2][e & 3]), bfhi(rv[e >> 2][e & 3])};
    asm volatile("" : "+v"(xf[0]), "+v"(xf[1]), "+v"(xf[2]), "+v"(xf[3]), "+v"(xf[4]), "+v"(xf[5]), "+v"(xf[6]), "+v"(xf[7]));
    if (wb <= (unsigned)bb) {
      if ((wave & 3) == 0) {
        wb = (unsigned)(bb + 1);
        for (int spins = 0;; ++spins) {
          unsigned v, u;
          ld_fb(v, u, cnt, pwo);
          if (__all(v > (unsigned)bb + 2u)) { v = rowmin16(v); wb = min(rdl(v, 0), rdl(v, 16)); break; }
          if (__all(v > (unsigned)bb) || bells(v > (unsigned)bb, u, bb, MARL)) break;
          __builtin_amdgcn_s_sleep(2);
          if (give_up(spins, tmo, 11)) break;
        }
        lwb[0] = wb;
      } else {
        while ((wb = __builtin_amdgcn_readfirstlane(lwb[0])) <= (unsigned)bb) __builtin_amdgcn_s_sleep(1);
      }
    }
    WTM(slot, bb, 0);
    bool fin = bb < SB;
    for (int spins = 0;; ++spins) {
      u32x4 pp[2][4];
#pragma unroll
      for (int i = 0; i < 2; ++i)
#pragma unroll
        for (int q = 0; q < 4; ++q) pp[i][q] = __builtin_amdgcn_raw_buffer_load_b128(rp, po + i * 32768, (unsigned)(bb * 32 * PROW + q * 65536 + r4 * 64), 16);
      sum4(pp[0], a[0]); sum4(pp[1], a[1]);
      if (ln_row(fin, bb, myp + 16, a, xf, tg, th, tc, uni((const char*)x1 + (size_t)bb * 65536), uni(h2c + (size_t)bb * 65536), xo, ho)) break;
      fin = __all(ld4_sc1((const char*)cnt + pwo) > (unsigned)bb) || give_up(spins, tmo, 11);
    }
    WTM(slot, bb, 3);
#pragma unroll
    for (int i = 0; i < 2; ++i) rv[i] = __builtin_amdgcn_raw_buffer_load_b128(rr, xo + i * 1024, (unsigned)min(bb + 2, NSEG - 1) * 65536u, 0);
    WAIT_V(2);
    unsigned o = 0;
    if (lane == 0) o = __hip_atomic_fetch_add((LAS unsigned*)(smem + 16448 + bb * 4), 1u, __ATOMIC_RELAXED, __HIP_MEMORY_SCOPE_WORKGROUP);
    if (__builtin_amdgcn_readfirstlane(o) == 3u) {
      if (lane < 4) st4_sc1(myp - (wave & 3) + lane, (unsigned)(bb + 2));
      WTM(slot, bb, 2);
    }
  }
}
#define CALF 1
#define NCS 6
__device__ __forceinline__ void l1_role(char* smem, int wave_id_, bool fst, int c, const u16* __restrict__ x, const float* __restrict__ mod,
                                        const u16* __restrict__ mem0, char* __restrict__ h1c, unsigned*& cnt, u64 tag, unsigned* tmo, unsigned long long* mts) {
  const int tid = opaque_tid(), lane = tid & 63, wave = WAVE_ID;
  char* ws = (char*)mod - OFF_MOD;
  u32x4 xr[7][2];
  const int row0 = (fst ? 0 : 256) + 8 * c + wave;
  const u16* src = x + (size_t)row0 * DM + lane * 8;
  xr[0][0] = ldnt<2>(src); xr[0][1] = ldnt<2>(src + 512);
  volatile LAS unsigned* sg = (volatile LAS unsigned*)(smem + L_SLOT + 784);
  if (!fst) {
#pragma unroll
    for (int pp = 1; pp < 7; ++pp) { xr[pp][0] = ldnt<2>(src + (size_t)pp * 256 * DM); xr[pp][1] = ldnt<2>(src + (size_t)pp * 256 * DM + 512); }
  } else if (wave >= 5) {
    char* cal = xch_base(ws) + ((size_t)(wave == 5 ? 0 : wave - 3) << 21) + (126 + (c >> 4)) * 16384 + (c & 15) * 512;
    st16_sc1(cal + (lane >> 5) * 8192 + (lane & 31) * 16, u32x4{SENT, SENT, SENT, SENT});
    WAIT_V(0);
    unsigned best[2] = {1u << 30, 1u << 30};
    const char* sb = uni(cal);
#pragma unroll
    for (int r = 1; r < NCS; ++r) {
      unsigned long long t0, t1; unsigned v;
      asm volatile("s_memtime %0\n\ts_waitcnt lgkmcnt(0)\n\ts_load_dword %2, %3, %4 glc\n\ts_waitcnt lgkmcnt(0)\n\ts_memtime %1\n\ts_waitcnt lgkmcnt(0)"
                   : "=&s"(t0), "=&s"(t1), "=&s"(v) : "s"(sb), "i"((r & 1) * 8192 + (r >> 1) * 128) : "memory");
      if (r > 1) best[r & 1] = min(best[r & 1], (unsigned)(t1 - t0));
    }
    unsigned xcc; asm volatile("s_getreg_b32 %0, hwreg(HW_REG_XCC_ID)" : "=s"(xcc));
    const unsigned df = max(best[0], best[1]) - min(best[0], best[1]);
    sg[wave - 5] = df < 110u || df > 400u ? 0u : (((best[1] < best[0]) != ((xcc & 4) != 0)) ? 1u : 8u) << (wave - 5);
  }
  if (CALF) BAR();
  if (!fst || wave >= 5) {
  } else if (wave < 3) {
    const int b = c + 32 * wave;
    if (b < 89) {
      const char* p = uni(xch_base(ws) + (b < 48 ? (size_t)((b >> 4) * 32 + (b & 15)) * 16384
                                        : b < 56 ? ((size_t)3 << 21) + (size_t)(8 + ((b - 48) >> 2) * 8 + (b & 3)) * 16384
                                        : b < 84 ? ((size_t)3 << 21) + (size_t)(b - 56 + 24) * 16384 : ((size_t)4 << 21) + (size_t)(b - 84) * 16384));
      const unsigned v = b < 80 ? SENT : 0u;
#pragma unroll
      for (int i = 0; i < 16; ++i) st16s(p, (unsigned)(i * 1024 + lane * 16), u32x4{v, v, v, v});
    } else if (b == 89 && lane == 0) st4_sc1(tmo, 0u);
    WAIT_V(0);
  } else {
    const int pc = (c & 7) * 2 + (wave & 1), g = lane >> 4, hf = (lane >> 2) & 1, row = ((lane >> 3) & 1) * 4 + (lane & 3);
    u32x4 v = *(const u32x4*)(mem0 + row * DM + 32 * (2 * pc + hf) + 8 * g);
    v.x = nosent(v.x); v.y = nosent(v.y); v.z = nosent(v.z); v.w = nosent(v.w);
    st16_sc1(xch_base(ws) + ((size_t)3 << 21) + (size_t)(((c >> 3) & 1) * 2 + (pc >> 3)) * 16384 + (c >> 4) * 8192 + (pc & 7) * 1024 + lane * 16, v);
    WAIT_V(0);
  }
  BAR();
  if (fst && tid == 0) st8_sc1(ws + OFF_INIT + c * 128, u32x2{(unsigned)tag << 6 | sg[0] | sg[1] | sg[2], (unsigned)(tag >> 26)});
  if (fst && wave == 0) STS(ws, 128 + (c >> 3) * 32 + 24 + (c & 7), 2);
  {
    f32x4 v;
    for (int spins = 0; spins < SPIN_CAP; ++spins) {
      u32x4 m[4];
      asm volatile("s_nop 4\n\tglobal_load_dwordx4 %0, %4, %6 sc1\n\tglobal_load_dwordx4 %1, %4, %6 offset:16 sc1\n\t"
                   "global_load_dwordx4 %2, %5, %6 sc1\n\tglobal_load_dwordx4 %3, %5, %6 offset:16 sc1\n\ts_waitcnt vmcnt(0)"
                   : "=&v"(m[0]), "=&v"(m[1]), "=&v"(m[2]), "=&v"(m[3]) : "v"((unsigned)tid * 32), "v"((unsigned)tid * 32 + 16384), "s"(mod) : "memory");
      bool ok = true;
#pragma unroll
      for (int i = 0; i < 4; ++i) ok = ok && m[i].y == (unsigned)tag && m[i].w == tag2(tag);
      v = f32x4{__uint_as_float(m[0].x) + __uint_as_float(m[2].x), __uint_as_float(m[0].z) + __uint_as_float(m[2].z),
                __uint_as_float(m[1].x) + __uint_as_float(m[3].x), __uint_as_float(m[1].z) + __uint_as_float(m[3].z)};
      if (__all(ok)) break;
      __builtin_amdgcn_s_sleep(4);
    }
    v = f32x4{bfr(v[0]), bfr(v[1]), bfr(v[2]), bfr(v[3])};
    const int a = tid >> 8, cc = tid & 255;
    if (a) v = f32x4{bfr(1.0f + v[0]), bfr(1.0f + v[1]), bfr(1.0f + v[2]), bfr(1.0f + v[3])};
    *(f32x4*)(smem + 4096 + a * 4096 + ((cc >> 7) * 2 + (cc & 1)) * 1024 + ((cc >> 1) & 63) * 16) = v;
  }
  BAR();
  if (fst && wave == 0) STS(ws, 128 + (c >> 3) * 32 + 24 + (c & 7), 3);
#pragma unroll
  for (int p = 0; p < 7; ++p) {
    if (p > 0 && fst) break;
    f32x2 v[2][4];
#pragma unroll
    for (int i = 0; i < 2; ++i)
#pragma unroll
      for (int e = 0; e < 4; ++e) v[i][e] = f32x2{bflo(xr[p][i][e]), bfhi(xr[p][i][e])};
    const f32x2 s2 = ((v[0][0] + v[0][1]) + (v[0][2] + v[0][3])) + ((v[1][0] + v[1][1]) + (v[1][2] + v[1][3]));
    const float mean = wavesum(s2[0] + s2[1]) * (1.0f / DM);
    f32x2 q2 = f32x2{0, 0};
#pragma unroll
    for (int i = 0; i < 8; ++i) { const f32x2 d = v[i >> 2][i & 3] - f32x2{mean, mean}; q2 = pkfma(d, d, q2); }
    const float q = wavesum(q2[0] + q2[1]);
    if (p == 0 && fst && wave == 0) STS((char*)tmo - OFF_TMO, 128 + (c >> 3) * 32 + 24 + (c & 7), 5);
    const float mb = bfr(mean), vb = bfr(q * (1.0f / DM));
    const float rden = 1.0f / bfr(sqrtf(bfr(vb + 1e-5f)));
    const int row = 256 * p + row0, rb = row & 31;
    char* dst = h1c + (size_t)(row >> 5) * 65536 + (lane >> 3) * 4096 + rb * 128 + (((lane & 7) ^ (rb & 7)) << 4);
    const char* ml = smem + lane * 16;
#pragma unroll
    for (int i = 0; i < 2; ++i) st16_sc1(dst + i * 32768, norm8(v[i], ml + i * 2048, mb, rden));
  }
  cnt = cnt_of(ws, init_wait(ws, tag));
  WAIT_V(0);
  __builtin_amdgcn_s_barrier();
  if (tid == 0) st4_sc1(cnt + ((fst ? PL1 : PL1B) + c) * 32, fst ? 8u : 7u);
  if (wave == 0) STS(ws, 128 + (c >> 3) * 32 + 24 + (c & 7), fst ? 6 : 7);
}
__device__ __forceinline__ void mod2_job(char* smem, int wave_id_, int c, const float* __restrict__ mod, const u16* __restrict__ cvec, const u16* __restrict__ wmod, unsigned* cnt) {
  const int tid = opaque_tid();
  {
    const float sva = bf2f(cvec[tid]), svb = bf2f(cvec[tid + 512]);
    float* sv = (float*)(smem + 12288);
    sv[tid] = bfr(sva / (1.0f + __expf(-sva))); sv[tid + 512] = bfr(svb / (1.0f + __expf(-svb)));
  }
  __syncthreads();
  {
    const int cg = tid & 15, r0 = tid >> 4;
    const u16* wp = wmod + (size_t)r0 * (6 * DM) + 2 * DM + 128 * c + cg * 8;
    const float* sv = (const float*)(smem + 12288) + r0;
    f32x2 acc[4] = {f32x2{0, 0}, f32x2{0, 0}, f32x2{0, 0}, f32x2{0, 0}};
#pragma unroll 1
    for (int o = 0; o < 4; ++o) {
      u32x4 w[8];
#pragma unroll
      for (int i = 0; i < 8; ++i) w[i] = ldnt<1>(wp + (size_t)(o * 8 + i) * 32 * (6 * DM));
#pragma unroll
      for (int i = 0; i < 8; ++i) {
        const float s = sv[(o * 8 + i) * 32];
#pragma unroll
        for (int e = 0; e < 4; ++e) acc[e] = pkfma(f32x2{s, s}, f32x2{bflo(w[i][e]), bfhi(w[i][e])}, acc[e]);
      }
    }
    float* red = (float*)(smem + 16384);
    *(f32x4*)(red + r0 * 128 + cg * 8) = f32x4{acc[0][0], acc[0][1], acc[1][0], acc[1][1]};
    *(f32x4*)(red + r0 * 128 + cg * 8 + 4) = f32x4{acc[2][0], acc[2][1], acc[3][0], acc[3][1]};
    __syncthreads();
    if (tid < 128) {
      float s = 0;
#pragma unroll
      for (int r = 0; r < 32; ++r) s += red[r * 128 + tid];
      st4_sc1((char*)mod + OFF_MOD2 + (128 * c + tid) * 4, __builtin_bit_cast(unsigned, s));
    }
    WAIT_V(0);
    __builtin_amdgcn_s_barrier();
    if (tid == 0) st4_sc1(cnt + (PL1 + c) * 32 + 1, 1u);
  }
}
enum { R_QKV = 0, R_PART = 1, R_F1 = 2, R_PART2 = 3 };
struct Dep {
  const char* base; unsigned voff; unsigned mul, add;
};
__device__ __forceinline__ unsigned dep_eval(unsigned v, const Dep& d) {
  v = rowmin16(v * d.mul + (d.add + 64u));
  return min(min(rdl(v, 0), rdl(v, 16)), min(rdl(v, 32), rdl(v, 48))) - 64u;
}
typedef unsigned u32x32 __attribute__((ext_vector_type(32)));
typedef unsigned u32x16 __attribute__((ext_vector_type(16)));
__device__ __forceinline__ void fast_ld0(const u32x32 (&W)[4], u32x16& pd, unsigned ea, unsigned da, int& b, int& lf, int cp, unsigned up, unsigned sa, unsigned mu, unsigned ad,
    unsigned rw, unsigned ab, const char* p1, const char* p0, unsigned l1, unsigned l0, const unsigned (&av)[8], int la) {
  asm volatile("s_setprio 0\n\t"
    "s_cmp_eq_u32 %[la], 3\n\t"
    "s_waitcnt vmcnt(12)\n\t"
    "s_cbranch_scc1 20f\n\t"
    "s_waitcnt vmcnt(4)\n\t"
    "20:\n\t"
    "s_barrier\n\t"
    "ds_read_b128 v[96:99], %[ea] offset:0\n\t"
    "ds_read_b128 v[100:103], %[ea] offset:2048\n\t"
    "ds_read_b128 v[104:107], %[da] offset:0\n\t"
    "ds_read_b128 v[108:111], %[da] offset:2048\n\t"
    "ds_read_b128 v[112:115], %[ea] offset:4096\n\t"
    "ds_read_b128 v[116:119], %[ea] offset:6144\n\t"
    "s_branch 2f\n\t"
    ".p2align 6\n\t"
    "1:\n\t"
    "s_cmp_eq_u32 %[la], 3\n\t"
    "s_waitcnt vmcnt(12)\n\t"
    "s_cbranch_scc1 21f\n\t"
    "s_waitcnt vmcnt(4)\n\t"
    "21:\n\t"
    "s_barrier\n\t"
    "2:\n\t"
    "s_waitcnt lgkmcnt(4)\n\t"
    "v_mfma_f32_16x16x32_bf16 v[64:67], v[128:131], v[96:99], 0\n\t"
    "ds_read_b128 v[120:123], %[da] offset:4096\n\t"
    "s_add_u32 m0, %[l1], 0\n\t"
    "ds_write_b128 %[rw], v[80:83] offset:0\n\t"
    "v_mfma_f32_16x16x32_bf16 v[68:71], v[132:135], v[96:99], 0\n\t"
    "ds_read_b128 v[124:127], %[da] offset:6144\n\t"
    "global_load_lds_dwordx4 %[av0], s[36:37] sc1\n\t"
    "ds_write_b128 %[rw], v[84:87] offset:1024\n\t"
    "v_mfma_f32_16x16x32_bf16 v[72:75], v[128:131], v[100:103], 0\n\t"
    "s_add_u32 m0, %[l1], 32768\n\t"
    "ds_write_b128 %[rw], v[88:91] offset:2048\n\t"
    "v_mfma_f32_16x16x32_bf16 v[76:79], v[132:135], v[100:103], 0\n\t"
    "global_load_lds_dwordx4 %[av1], s[36:37] sc1\n\t"
    "ds_write_b128 %[rw], v[92:95] offset:3072\n\t"
    "s_waitcnt lgkmcnt(8)\n\t"
    "v_mfma_f32_16x16x32_bf16 v[64:67], v[136:139], v[104:107], v[64:67]\n\t"
    "ds_read_b128 v[96:99], %[ea] offset:8192\n\t"
    "s_add_u32 m0, %[l1], 4096\n\t"
    "v_mfma_f32_16x16x32_bf16 v[68:71], v[140:143], v[104:107], v[68:71]\n\t"
    "ds_read_b128 v[100:103], %[ea] offset:10240\n\t"
    "global_load_lds_dwordx4 %[av2], s[36:37] sc1\n\t"
    "v_mfma_f32_16x16x32_bf16 v[72:75], v[136:139], v[108:111], v[72:75]\n\t"
    "s_add_u32 m0, %[l1], 36864\n\t"
    "v_mfma_f32_16x16x32_bf16 v[76:79], v[140:143], v[108:111], v[76:79]\n\t"
    "global_load_lds_dwordx4 %[av3], s[36:37] sc1\n\t"
    "s_waitcnt lgkmcnt(8)\n\t"
    "v_mfma_f32_16x16x32_bf16 v[64:67], v[144:147], v[112:115], v[64:67]\n\t"
    "ds_read_b128 v[104:107], %[da] offset:8192\n\t"
    "s_add_u32 m0, %[l1], 8192\n\t"
    "v_mfma_f32_16x16x32_bf16 v[68:71], v[148:151], v[112:115], v[68:71]\n\t"
    "ds_read_b128 v[108:111], %[da] offset:10240\n\t"
    "global_load_lds_dwordx4 %[av4], s[36:37] sc1\n\t"
    "v_mfma_f32_16x16x32_bf16 v[72:75], v[144:147], v[116:119], v[72:75]\n\t"
    "s_add_u32 m0, %[l1], 40960\n\t"
    "v_mfma_f32_16x16x32_bf16 v[76:79], v[148:151], v[116:119], v[76:79]\n\t"
    "global_load_lds_dwordx4 %[av5], s[36:37] sc1\n\t"
    "s_waitcnt lgkmcnt(7)\n\t"
    "v_mfma_f32_16x16x32_bf16 v[64:67], v[152:155], v[120:123], v[64:67]\n\t"
    "ds_read_b128 v[112:115], %[ea] offset:12288\n\t"
    "s_add_u32 m0, %[l1], 12288\n\t"
    "v_mfma_f32_16x16x32_bf16 v[68:71], v[156:159], v[120:123], v[68:71]\n\t"
    "ds_read_b128 v[116:119], %[ea] offset:14336\n\t"
    "global_load_lds_dwordx4 %[av6], s[36:37] sc1\n\t"
    "v_mfma_f32_16x16x32_bf16 v[72:75], v[152:155], v[124:127], v[72:75]\n\t"
    "s_add_u32 m0, %[l1], 45056\n\t"
    "v_mfma_f32_16x16x32_bf16 v[76:79], v[156:159], v[124:127], v[76:79]\n\t"
    "global_load_lds_dwordx4 %[av7], s[36:37] sc1\n\t"
    "s_waitcnt lgkmcnt(4)\n\t"
    "v_mfma_f32_16x16x32_bf16 v[64:67], v[160:163], v[96:99], v[64:67]\n\t"
    "ds_read_b128 v[120:123], %[da] offset:12288\n\t"
    "v_mfma_f32_16x16x32_bf16 v[68:71], v[164:167], v[96:99], v[68:71]\n\t"
    "ds_read_b128 v[124:127], %[da] offset:14336\n\t"
    "v_mfma_f32_16x16x32_bf16 v[72:75], v[160:163], v[100:103], v[72:75]\n\t"
    "v_mfma_f32_16x16x32_bf16 v[76:79], v[164:167], v[100:103], v[76:79]\n\t"
    "s_waitcnt lgkmcnt(4)\n\t"
    "v_mfma_f32_16x16x32_bf16 v[64:67], v[168:171], v[104:107], v[64:67]\n\t"
    "ds_read_b128 v[96:99], %[ea] offset:16384\n\t"
    "v_mfma_f32_16x16x32_bf16 v[68:71], v[172:175], v[104:107], v[68:71]\n\t"
    "ds_read_b128 v[100:103], %[ea] offset:18432\n\t"
    "v_mfma_f32_16x16x32_bf16 v[72:75], v[168:171], v[108:111], v[72:75]\n\t"
    "v_mfma_f32_16x16x32_bf16 v[76:79], v[172:175], v[108:111], v[76:79]\n\t"
    "s_waitcnt lgkmcnt(4)\n\t"
    "v_mfma_f32_16x16x32_bf16 v[64:67], v[176:179], v[112:115], v[64:67]\n\t"
    "ds_read_b128 v[104:107], %[da] offset:16384\n\t"
    "v_mfma_f32_16x16x32_bf16 v[68:71], v[180:183], v[112:115], v[68:71]\n\t"
    "ds_read_b128 v[108:111], %[da] offset:18432\n\t"
    "v_mfma_f32_16x16x32_bf16 v[72:75], v[176:179], v[116:119], v[72:75]\n\t"
    "v_mfma_f32_16x16x32_bf16 v[76:79], v[180:183], v[116:119], v[76:79]\n\t"
    "s_waitcnt lgkmcnt(4)\n\t"
    "v_mfma_f32_16x16x32_bf16 v[64:67], v[184:187], v[120:123], v[64:67]\n\t"
    "ds_read_b128 v[112:115], %[ea] offset:20480\n\t"
    "v_mfma_f32_16x16x32_bf16 v[68:71], v[188:191], v[120:123], v[68:71]\n\t"
    "ds_read_b128 v[116:119], %[ea] offset:22528\n\t"
    "v_mfma_f32_16x16x32_bf16 v[72:75], v[184:187], v[124:127], v[72:75]\n\t"
    "v_mfma_f32_16x16x32_bf16 v[76:79], v[188:191], v[124:127], v[76:79]\n\t"
    "s_cmp_eq_u32 %[la], 3\n\t"
    "s_waitcnt vmcnt(12)\n\t"
    "s_cbranch_scc1 22f\n\t"
    "s_waitcnt vmcnt(4)\n\t"
    "22:\n\t"
    "s_barrier\n\t"
    "s_waitcnt lgkmcnt(4)\n\t"
    "v_mfma_f32_16x16x32_bf16 v[64:67], v[192:195], v[96:99], v[64:67]\n\t"
    "ds_read_b128 v[120:123], %[da] offset:20480\n\t"
    "s_add_u32 m0, %[l0], 0\n\t"
    "v_mfma_f32_16x16x32_bf16 v[68:71], v[196:199], v[96:99], v[68:71]\n\t"
    "ds_read_b128 v[124:127], %[da] offset:22528\n\t"
    "global_load_lds_dwordx4 %[av0], s[38:39] sc1\n\t"
    "v_mfma_f32_16x16x32_bf16 v[72:75], v[192:195], v[100:103], v[72:75]\n\t"
    "s_add_u32 m0, %[l0], 32768\n\t"
    "v_mfma_f32_16x16x32_bf16 v[76:79], v[196:199], v[100:103], v[76:79]\n\t"
    "global_load_lds_dwordx4 %[av1], s[38:39] sc1\n\t"
    "s_waitcnt lgkmcnt(4)\n\t"
    "v_mfma_f32_16x16x32_bf16 v[64:67], v[200:203], v[104:107], v[64:67]\n\t"
    "ds_read_b128 v[96:99], %[ea] offset:24576\n\t"
    "s_add_u32 m0, %[l0], 4096\n\t"
    "v_mfma_f32_16x16x32_bf16 v[68:71], v[204:207], v[104:107], v[68:71]\n\t"
    "ds_read_b128 v[100:103], %[ea] offset:26624\n\t"
    "global_load_lds_dwordx4 %[av2], s[38:39] sc1\n\t"
    "v_mfma_f32_16x16x32_bf16 v[72:75], v[200:203], v[108:111], v[72:75]\n\t"
    "s_add_u32 m0, %[l0], 36864\n\t"
    "v_mfma_f32_16x16x32_bf16 v[76:79], v[204:207], v[108:111], v[76:79]\n\t"
    "global_load_lds_dwordx4 %[av3], s[38:39] sc1\n\t"
    "s_add_u32 s36, s36, %[ab]\n\t"
    "s_addc_u32 s37, s37, 0\n\t"
    "s_waitcnt lgkmcnt(4)\n\t"
    "v_mfma_f32_16x16x32_bf16 v[64:67], v[208:211], v[112:115], v[64:67]\n\t"
    "ds_read_b128 v[104:107], %[da] offset:24576\n\t"
    "s_add_u32 m0, %[l0], 8192\n\t"
    "v_mfma_f32_16x16x32_bf16 v[68:71], v[212:215], v[112:115], v[68:71]\n\t"
    "ds_read_b128 v[108:111], %[da] offset:26624\n\t"
    "global_load_lds_dwordx4 %[av4], s[38:39] sc1\n\t"
    "v_mfma_f32_16x16x32_bf16 v[72:75], v[208:211], v[116:119], v[72:75]\n\t"
    "ds_read_b32 v39, %[sa]\n\t"
    "s_add_u32 m0, %[l0], 40960\n\t"
    "v_mfma_f32_16x16x32_bf16 v[76:79], v[212:215], v[116:119], v[76:79]\n\t"
    "global_load_lds_dwordx4 %[av5], s[38:39] sc1\n\t"
    "s_xor_b32 %[l1], %[l1], 0x10000\n\t"
    "s_waitcnt lgkmcnt(5)\n\t"
    "v_mfma_f32_16x16x32_bf16 v[64:67], v[216:219], v[120:123], v[64:67]\n\t"
    "ds_read_b128 v[112:115], %[ea] offset:28672\n\t"
    "s_add_u32 m0, %[l0], 12288\n\t"
    "v_mfma_f32_16x16x32_bf16 v[68:71], v[220:223], v[120:123], v[68:71]\n\t"
    "ds_read_b128 v[116:119], %[ea] offset:30720\n\t"
    "global_load_lds_dwordx4 %[av6], s[38:39] sc1\n\t"
    "v_mfma_f32_16x16x32_bf16 v[72:75], v[216:219], v[124:127], v[72:75]\n\t"
    "v_xor_b32 %[ea], 0x10000, %[ea]\n\t"
    "s_add_u32 m0, %[l0], 45056\n\t"
    "v_mfma_f32_16x16x32_bf16 v[76:79], v[220:223], v[124:127], v[76:79]\n\t"
    "global_load_lds_dwordx4 %[av7], s[38:39] sc1\n\t"
    "s_waitcnt lgkmcnt(5)\n\t"
    "v_mfma_f32_16x16x32_bf16 v[64:67], v[224:227], v[96:99], v[64:67]\n\t"
    "ds_read_b128 v[120:123], %[da] offset:28672\n\t"
    "v_mfma_f32_16x16x32_bf16 v[68:71], v[228:231], v[96:99], v[68:71]\n\t"
    "ds_read_b128 v[124:127], %[da] offset:30720\n\t"
    "v_mfma_f32_16x16x32_bf16 v[72:75], v[224:227], v[100:103], v[72:75]\n\t"
    "v_xor_b32 %[da], 0x10000, %[da]\n\t"
    "v_mfma_f32_16x16x32_bf16 v[76:79], v[228:231], v[100:103], v[76:79]\n\t"
    "s_waitcnt lgkmcnt(5)\n\t"
    "v_mfma_f32_16x16x32_bf16 v[64:67], v[232:235], v[104:107], v[64:67]\n\t"
    "ds_read_b128 v[96:99], %[ea] offset:0\n\t"
    "v_mfma_f32_16x16x32_bf16 v[68:71], v[236:239], v[104:107], v[68:71]\n\t"
    "ds_read_b128 v[100:103], %[ea] offset:2048\n\t"
    "v_mfma_f32_16x16x32_bf16 v[72:75], v[232:235], v[108:111], v[72:75]\n\t"
    "v_mfma_f32_16x16x32_bf16 v[76:79], v[236:239], v[108:111], v[76:79]\n\t"
    "s_waitcnt lgkmcnt(4)\n\t"
    "v_mfma_f32_16x16x32_bf16 v[64:67], v[240:243], v[112:115], v[64:67]\n\t"
    "ds_read_b128 v[104:107], %[da] offset:0\n\t"
    "v_mad_u32_u24 v39, v39, %[mu], %[ad]\n\t"
    "v_mfma_f32_16x16x32_bf16 v[68:71], v[244:247], v[112:115], v[68:71]\n\t"
    "ds_read_b128 v[108:111], %[da] offset:2048\n\t"
    "v_cmp_ge_i32 vcc, %[lf], v39\n\t"
    "s_cmp_eq_u64 vcc, 0\n\t"
    "s_addc_u32 %[lf], %[lf], 0\n\t"
    "v_mfma_f32_16x16x32_bf16 v[72:75], v[240:243], v[116:119], v[72:75]\n\t"
    "v_mfma_f32_16x16x32_bf16 v[76:79], v[244:247], v[116:119], v[76:79]\n\t"
    "v_cmp_ge_i32 vcc, %[lf], v39\n\t"
    "s_cmp_eq_u64 vcc, 0\n\t"
    "s_addc_u32 %[lf], %[lf], 0\n\t"
    "s_waitcnt lgkmcnt(4)\n\t"
    "v_mfma_f32_16x16x32_bf16 v[80:83], v[248:251], v[120:123], v[64:67]\n\t"
    "ds_read_b128 v[112:115], %[ea] offset:4096\n\t"
    "s_min_i32 %[lf], %[lf], %[cp]\n\t"
    "s_add_u32 %[b], %[b], 1\n\t"
    "v_mfma_f32_16x16x32_bf16 v[84:87], v[252:255], v[120:123], v[68:71]\n\t"
    "ds_read_b128 v[116:119], %[ea] offset:6144\n\t"
    "v_mfma_f32_16x16x32_bf16 v[88:91], v[248:251], v[124:127], v[72:75]\n\t"
    "v_mfma_f32_16x16x32_bf16 v[92:95], v[252:255], v[124:127], v[76:79]\n\t"
    "s_add_u32 s38, s38, %[ab]\n\t"
    "s_addc_u32 s39, s39, 0\n\t"
    "s_xor_b32 %[l0], %[l0], 0x10000\n\t"
    "s_sub_u32 m0, %[lf], %[b]\n\t"
    "s_sub_u32 m0, m0, 1\n\t"
    "s_cmp_lt_u32 m0, %[up]\n\t"
    "s_cbranch_scc1 1b\n\t"
    "s_waitcnt lgkmcnt(0)\n\t"
    "s_nop 4"
    : "=&{v[80:95]}"(pd), [ea] "+&v"(ea), [da] "+&v"(da), [b] "+&s"(b), [lf] "+&s"(lf), "+{s[36:37]}"(p1), "+{s[38:39]}"(p0), [sa] "+&v"(sa), [l1] "+&s"(l1), [l0] "+&s"(l0)
    : [rw] "v"(rw), [ab] "s"(ab), [cp] "s"(cp), [mu] "s"(mu), [ad] "v"(ad), [up] "s"(up), "{v[128:159]}"(W[0]), "{v[160:191]}"(W[1]), "{v[192:223]}"(W[2]), "{v[224:255]}"(W[3]), [av0] "v"(av[0]), [av1] "v"(av[1]), [av2] "v"(av[2]), [av3] "v"(av[3]), [av4] "v"(av[4]), [av5] "v"(av[5]), [av6] "v"(av[6]), [av7] "v"(av[7]), [la] "s"(la)
    : "v39", "v64", "v65", "v66", "v67", "v68", "v69", "v70", "v71", "v72", "v73", "v74", "v75", "v76", "v77", "v78", "v79", "v96", "v97", "v98", "v99", "v100", "v101", "v102", "v103", "v104", "v105", "v106", "v107", "v108", "v109", "v110", "v111", "v112", "v113", "v114", "v115", "v116", "v117", "v118", "v119", "v120", "v121", "v122", "v123", "v124", "v125", "v126", "v127", "vcc", "scc", "m0", "memory");
}
__device__ __forceinline__ void fast_ld1(const u32x32 (&W)[4], u32x16& pd, unsigned ea, unsigned da, int& b, int& lf, int cp, unsigned up, unsigned sa, unsigned mu, unsigned ad,
    unsigned rw, unsigned ab, const char* p1, const char* p0, unsigned l1, unsigned l0, const unsigned (&av)[8], int la) {
  asm volatile("s_setprio 0\n\t"
    "s_cmp_eq_u32 %[la], 3\n\t"
    "s_waitcnt vmcnt(12)\n\t"
    "s_cbranch_scc1 20f\n\t"
    "s_waitcnt vmcnt(4)\n\t"
    "20:\n\t"
    "s_barrier\n\t"
    "ds_read_b128 v[96:99], %[ea] offset:0\n\t"
    "ds_read_b128 v[100:103], %[ea] offset:2048\n\t"
    "ds_read_b128 v[104:107], %[da] offset:0\n\t"
    "ds_read_b128 v[108:111], %[da] offset:2048\n\t"
    "ds_read_b128 v[112:115], %[ea] offset:4096\n\t"
    "ds_read_b128 v[116:119], %[ea] offset:6144\n\t"
    "s_branch 2f\n\t"
    ".p2align 6\n\t"
    "1:\n\t"
    "s_cmp_eq_u32 %[la], 3\n\t"
    "s_waitcnt vmcnt(12)\n\t"
    "s_cbranch_scc1 21f\n\t"
    "s_waitcnt vmcnt(4)\n\t"
    "21:\n\t"
    "s_barrier\n\t"
    "2:\n\t"
    "s_waitcnt lgkmcnt(4)\n\t"
    "v_mfma_f32_16x16x32_bf16 v[64:67], v[128:131], v[96:99], 0\n\t"
    "ds_read_b128 v[120:123], %[da] offset:4096\n\t"
    "s_add_u32 m0, %[l1], 0\n\t"
    "ds_write_b128 %[rw], v[80:83] offset:0\n\t"
    "v_mfma_f32_16x16x32_bf16 v[68:71], v[132:135], v[96:99], 0\n\t"
    "ds_read_b128 v[124:127], %[da] offset:6144\n\t"
    "global_load_lds_dwordx4 %[av0], s[36:37] sc1\n\t"
    "ds_write_b128 %[rw], v[84:87] offset:1024\n\t"
    "v_mfma_f32_16x16x32_bf16 v[72:75], v[128:131], v[100:103], 0\n\t"
    "s_add_u32 m0, %[l1], 32768\n\t"
    "ds_write_b128 %[rw], v[88:91] offset:2048\n\t"
    "v_mfma_f32_16x16x32_bf16 v[76:79], v[132:135], v[100:103], 0\n\t"
    "global_load_lds_dwordx4 %[av1], s[36:37] sc1\n\t"
    "ds_write_b128 %[rw], v[92:95] offset:3072\n\t"
    "s_waitcnt lgkmcnt(8)\n\t"
    "v_mfma_f32_16x16x32_bf16 v[64:67], v[136:139], v[104:107], v[64:67]\n\t"
    "ds_read_b128 v[96:99], %[ea] offset:8192\n\t"
    "s_add_u32 m0, %[l1], 4096\n\t"
    "v_mfma_f32_16x16x32_bf16 v[68:71], v[140:143], v[104:107], v[68:71]\n\t"
    "ds_read_b128 v[100:103], %[ea] offset:10240\n\t"
    "global_load_lds_dwordx4 %[av2], s[36:37] sc1\n\t"
    "v_mfma_f32_16x16x32_bf16 v[72:75], v[136:139], v[108:111], v[72:75]\n\t"
    "s_add_u32 m0, %[l1], 36864\n\t"
    "v_mfma_f32_16x16x32_bf16 v[76:79], v[140:143], v[108:111], v[76:79]\n\t"
    "global_load_lds_dwordx4 %[av3], s[36:37] sc1\n\t"
    "s_waitcnt lgkmcnt(8)\n\t"
    "v_mfma_f32_16x16x32_bf16 v[64:67], v[144:147], v[112:115], v[64:67]\n\t"
    "ds_read_b128 v[104:107], %[da] offset:8192\n\t"
    "s_add_u32 m0, %[l1], 8192\n\t"
    "v_mfma_f32_16x16x32_bf16 v[68:71], v[148:151], v[112:115], v[68:71]\n\t"
    "ds_read_b128 v[108:111], %[da] offset:10240\n\t"
    "global_load_lds_dwordx4 %[av4], s[36:37] sc1\n\t"
    "v_mfma_f32_16x16x32_bf16 v[72:75], v[144:147], v[116:119], v[72:75]\n\t"
    "s_add_u32 m0, %[l1], 40960\n\t"
    "v_mfma_f32_16x16x32_bf16 v[76:79], v[148:151], v[116:119], v[76:79]\n\t"
    "global_load_lds_dwordx4 %[av5], s[36:37] sc1\n\t"
    "s_waitcnt lgkmcnt(7)\n\t"
    "v_mfma_f32_16x16x32_bf16 v[64:67], v[152:155], v[120:123], v[64:67]\n\t"
    "ds_read_b128 v[112:115], %[ea] offset:12288\n\t"
    "s_add_u32 m0, %[l1], 12288\n\t"
    "v_mfma_f32_16x16x32_bf16 v[68:71], v[156:159], v[120:123], v[68:71]\n\t"
    "ds_read_b128 v[116:119], %[ea] offset:14336\n\t"
    "global_load_lds_dwordx4 %[av6], s[36:37] sc1\n\t"
    "v_mfma_f32_16x16x32_bf16 v[72:75], v[152:155], v[124:127], v[72:75]\n\t"
    "s_add_u32 m0, %[l1], 45056\n\t"
    "v_mfma_f32_16x16x32_bf16 v[76:79], v[156:159], v[124:127], v[76:79]\n\t"
    "global_load_lds_dwordx4 %[av7], s[36:37] sc1\n\t"
    "s_waitcnt lgkmcnt(4)\n\t"
    "v_mfma_f32_16x16x32_bf16 v[64:67], v[160:163], v[96:99], v[64:67]\n\t"
    "ds_read_b128 v[120:123], %[da] offset:12288\n\t"
    "v_mfma_f32_16x16x32_bf16 v[68:71], v[164:167], v[96:99], v[68:71]\n\t"
    "ds_read_b128 v[124:127], %[da] offset:14336\n\t"
    "v_mfma_f32_16x16x32_bf16 v[72:75], v[160:163], v[100:103], v[72:75]\n\t"
    "v_mfma_f32_16x16x32_bf16 v[76:79], v[164:167], v[100:103], v[76:79]\n\t"
    "s_waitcnt lgkmcnt(4)\n\t"
    "v_mfma_f32_16x16x32_bf16 v[64:67], v[168:171], v[104:107], v[64:67]\n\t"
    "ds_read_b128 v[96:99], %[ea] offset:16384\n\t"
    "v_mfma_f32_16x16x32_bf16 v[68:71], v[172:175], v[104:107], v[68:71]\n\t"
    "ds_read_b128 v[100:103], %[ea] offset:18432\n\t"
    "v_mfma_f32_16x16x32_bf16 v[72:75], v[168:171], v[108:111], v[72:75]\n\t"
    "v_mfma_f32_16x16x32_bf16 v[76:79], v[172:175], v[108:111], v[76:79]\n\t"
    "s_waitcnt lgkmcnt(4)\n\t"
    "v_mfma_f32_16x16x32_bf16 v[64:67], v[176:179], v[112:115], v[64:67]\n\t"
    "ds_read_b128 v[104:107], %[da] offset:16384\n\t"
    "v_mfma_f32_16x16x32_bf16 v[68:71], v[180:183], v[112:115], v[68:71]\n\t"
    "ds_read_b128 v[108:111], %[da] offset:18432\n\t"
    "v_mfma_f32_16x16x32_bf16 v[72:75], v[176:179], v[116:119], v[72:75]\n\t"
    "v_mfma_f32_16x16x32_bf16 v[76:79], v[180:183], v[116:119], v[76:79]\n\t"
    "s_waitcnt lgkmcnt(4)\n\t"
    "v_mfma_f32_16x16x32_bf16 v[64:67], v[184:187], v[120:123], v[64:67]\n\t"
    "ds_read_b128 v[112:115], %[ea] offset:20480\n\t"
    "v_mfma_f32_16x16x32_bf16 v[68:71], v[188:191], v[120:123], v[68:71]\n\t"
    "ds_read_b128 v[116:119], %[ea] offset:22528\n\t"
    "v_mfma_f32_16x16x32_bf16 v[72:75], v[184:187], v[124:127], v[72:75]\n\t"
    "v_mfma_f32_16x16x32_bf16 v[76:79], v[188:191], v[124:127], v[76:79]\n\t"
    "s_cmp_eq_u32 %[la], 3\n\t"
    "s_waitcnt vmcnt(12)\n\t"
    "s_cbranch_scc1 22f\n\t"
    "s_waitcnt vmcnt(4)\n\t"
    "22:\n\t"
    "s_barrier\n\t"
    "s_waitcnt lgkmcnt(4)\n\t"
    "v_mfma_f32_16x16x32_bf16 v[64:67], v[192:195], v[96:99], v[64:67]\n\t"
    "ds_read_b128 v[120:123], %[da] offset:20480\n\t"
    "s_add_u32 m0, %[l0], 0\n\t"
    "v_mfma_f32_16x16x32_bf16 v[68:71], v[196:199], v[96:99], v[68:71]\n\t"
    "ds_read_b128 v[124:127], %[da] offset:22528\n\t"
    "global_load_lds_dwordx4 %[av0], s[38:39] sc1\n\t"
    "v_mfma_f32_16x16x32_bf16 v[72:75], v[192:195], v[100:103], v[72:75]\n\t"
    "s_add_u32 m0, %[l0], 32768\n\t"
    "v_mfma_f32_16x16x32_bf16 v[76:79], v[196:199], v[100:103], v[76:79]\n\t"
    "global_load_lds_dwordx4 %[av1], s[38:39] sc1\n\t"
    "s_waitcnt lgkmcnt(4)\n\t"
    "v_mfma_f32_16x16x32_bf16 v[64:67], v[200:203], v[104:107], v[64:67]\n\t"
    "ds_read_b128 v[96:99], %[ea] offset:24576\n\t"
    "s_add_u32 m0, %[l0], 4096\n\t"
    "v_mfma_f32_16x16x32_bf16 v[68:71], v[204:207], v[104:107], v[68:71]\n\t"
    "ds_read_b128 v[100:103], %[ea] offset:26624\n\t"
    "global_load_lds_dwordx4 %[av2], s[38:39] sc1\n\t"
    "v_mfma_f32_16x16x32_bf16 v[72:75], v[200:203], v[108:111], v[72:75]\n\t"
    "s_add_u32 m0, %[l0], 36864\n\t"
    "v_mfma_f32_16x16x32_bf16 v[76:79], v[204:207], v[108:111], v[76:79]\n\t"
    "global_load_lds_dwordx4 %[av3], s[38:39] sc1\n\t"
    "s_add_u32 s36, s36, %[ab]\n\t"
    "s_addc_u32 s37, s37, 0\n\t"
    "s_waitcnt lgkmcnt(4)\n\t"
    "v_mfma_f32_16x16x32_bf16 v[64:67], v[208:211], v[112:115], v[64:67]\n\t"
    "ds_read_b128 v[104:107], %[da] offset:24576\n\t"
    "s_add_u32 m0, %[l0], 8192\n\t"
    "v_mfma_f32_16x16x32_bf16 v[68:71], v[212:215], v[112:115], v[68:71]\n\t"
    "ds_read_b128 v[108:111], %[da] offset:26624\n\t"
    "global_load_lds_dwordx4 %[av4], s[38:39] sc1\n\t"
    "v_mfma_f32_16x16x32_bf16 v[72:75], v[208:211], v[116:119], v[72:75]\n\t"
    "ds_read_b32 v39, %[sa]\n\t"
    "s_add_u32 m0, %[l0], 40960\n\t"
    "v_mfma_f32_16x16x32_bf16 v[76:79], v[212:215], v[116:119], v[76:79]\n\t"
    "global_load_lds_dwordx4 %[av5], s[38:39] sc1\n\t"
    "s_xor_b32 %[l1], %[l1], 0x10000\n\t"
    "s_waitcnt lgkmcnt(5)\n\t"
    "v_mfma_f32_16x16x32_bf16 v[64:67], v[216:219], v[120:123], v[64:67]\n\t"
    "ds_read_b128 v[112:115], %[ea] offset:28672\n\t"
    "s_add_u32 m0, %[l0], 12288\n\t"
    "v_mfma_f32_16x16x32_bf16 v[68:71], v[220:223], v[120:123], v[68:71]\n\t"
    "ds_read_b128 v[116:119], %[ea] offset:30720\n\t"
    "global_load_lds_dwordx4 %[av6], s[38:39] sc1\n\t"
    "v_mfma_f32_16x16x32_bf16 v[72:75], v[216:219], v[124:127], v[72:75]\n\t"
    "v_xor_b32 %[ea], 0x10000, %[ea]\n\t"
    "s_add_u32 m0, %[l0], 45056\n\t"
    "v_mfma_f32_16x16x32_bf16 v[76:79], v[220:223], v[124:127], v[76:79]\n\t"
    "global_load_lds_dwordx4 %[av7], s[38:39] sc1\n\t"
    "s_waitcnt lgkmcnt(5)\n\t"
    "v_mfma_f32_16x16x32_bf16 v[64:67], v[224:227], v[96:99], v[64:67]\n\t"
    "ds_read_b128 v[120:123], %[da] offset:28672\n\t"
    "v_mfma_f32_16x16x32_bf16 v[68:71], v[228:231], v[96:99], v[68:71]\n\t"
    "ds_read_b128 v[124:127], %[da] offset:30720\n\t"
    "v_mfma_f32_16x16x32_bf16 v[72:75], v[224:227], v[100:103], v[72:75]\n\t"
    "v_xor_b32 %[da], 0x10000, %[da]\n\t"
    "v_mfma_f32_16x16x32_bf16 v[76:79], v[228:231], v[100:103], v[76:79]\n\t"
    "s_waitcnt lgkmcnt(5)\n\t"
    "v_mfma_f32_16x16x32_bf16 v[64:67], v[232:235], v[104:107], v[64:67]\n\t"
    "ds_read_b128 v[96:99], %[ea] offset:0\n\t"
    "v_mfma_f32_16x16x32_bf16 v[68:71], v[236:239], v[104:107], v[68:71]\n\t"
    "ds_read_b128 v[100:103], %[ea] offset:2048\n\t"
    "v_mfma_f32_16x16x32_bf16 v[72:75], v[232:235], v[108:111], v[72:75]\n\t"
    "v_mfma_f32_16x16x32_bf16 v[76:79], v[236:239], v[108:111], v[76:79]\n\t"
    "s_waitcnt lgkmcnt(4)\n\t"
    "v_mfma_f32_16x16x32_bf16 v[64:67], v[240:243], v[112:115], v[64:67]\n\t"
    "ds_read_b128 v[104:107], %[da] offset:0\n\t"
    "v_mad_u32_u24 v39, v39, %[mu], %[ad]\n\t"
    "v_mfma_f32_16x16x32_bf16 v[68:71], v[244:247], v[112:115], v[68:71]\n\t"
    "ds_read_b128 v[108:111], %[da] offset:2048\n\t"
    "v_cmp_ge_i32 vcc, %[lf], v39\n\t"
    "s_cmp_eq_u64 vcc, 0\n\t"
    "s_addc_u32 %[lf], %[lf], 0\n\t"
    "v_mfma_f32_16x16x32_bf16 v[72:75], v[240:243], v[116:119], v[72:75]\n\t"
    "v_mfma_f32_16x16x32_bf16 v[76:79], v[244:247], v[116:119], v[76:79]\n\t"
    "v_cmp_ge_i32 vcc, %[lf], v39\n\t"
    "s_cmp_eq_u64 vcc, 0\n\t"
    "s_addc_u32 %[lf], %[lf], 0\n\t"
    "s_waitcnt lgkmcnt(4)\n\t"
    "v_mfma_f32_16x16x32_bf16 v[80:83], v[248:251], v[120:123], v[64:67]\n\t"
    "ds_read_b128 v[112:115], %[ea] offset:4096\n\t"
    "s_min_i32 %[lf], %[lf], %[cp]\n\t"
    "s_add_u32 %[b], %[b], 1\n\t"
    "v_mfma_f32_16x16x32_bf16 v[84:87], v[252:255], v[120:123], v[68:71]\n\t"
    "ds_read_b128 v[116:119], %[ea] offset:6144\n\t"
    "v_mfma_f32_16x16x32_bf16 v[88:91], v[248:251], v[124:127], v[72:75]\n\t"
    "v_mfma_f32_16x16x32_bf16 v[92:95], v[252:255], v[124:127], v[76:79]\n\t"
    "s_add_u32 s38, s38, %[ab]\n\t"
    "s_addc_u32 s39, s39, 0\n\t"
    "s_xor_b32 %[l0], %[l0], 0x10000\n\t"
    "s_sub_u32 m0, %[lf], %[b]\n\t"
    "s_sub_u32 m0, m0, 1\n\t"
    "s_cmp_lt_u32 m0, %[up]\n\t"
    "s_cbranch_scc1 1b\n\t"
    "s_waitcnt lgkmcnt(0)\n\t"
    "s_nop 4"
    : "=&{v[80:95]}"(pd), [ea] "+&v"(ea), [da] "+&v"(da), [b] "+&s"(b), [lf] "+&s"(lf), "+{s[36:37]}"(p1), "+{s[38:39]}"(p0), [sa] "+&v"(sa), [l1] "+&s"(l1), [l0] "+&s"(l0)
    : [rw] "v"(rw), [ab] "s"(ab), [cp] "s"(cp), [mu] "s"(mu), [ad] "v"(ad), [up] "s"(up), "{v[128:159]}"(W[0]), "{v[160:191]}"(W[1]), "{v[192:223]}"(W[2]), "{v[224:255]}"(W[3]), [av0] "v"(av[0]), [av1] "v"(av[1]), [av2] "v"(av[2]), [av3] "v"(av[3]), [av4] "v"(av[4]), [av5] "v"(av[5]), [av6] "v"(av[6]), [av7] "v"(av[7]), [la] "s"(la)
    : "v39", "v64", "v65", "v66", "v67", "v68", "v69", "v70", "v71", "v72", "v73", "v74", "v75", "v76", "v77", "v78", "v79", "v96", "v97", "v98", "v99", "v100", "v101", "v102", "v103", "v104", "v105", "v106", "v107", "v108", "v109", "v110", "v111", "v112", "v113", "v114", "v115", "v116", "v117", "v118", "v119", "v120", "v121", "v122", "v123", "v124", "v125", "v126", "v127", "vcc", "scc", "m0", "memory");
}
__device__ __forceinline__ void fast_ld2(const u32x32 (&W)[4], u32x16& pd, unsigned ea, unsigned da, int& b, int& lf, int cp, unsigned up, unsigned sa, unsigned mu, unsigned ad,
    unsigned rw, unsigned ab, const char* p1, const char* p0, unsigned l1, unsigned l0, const unsigned (&av)[8], int la) {
  asm volatile("s_setprio 0\n\t"
    "s_cmp_eq_u32 %[la], 3\n\t"
    "s_waitcnt vmcnt(6)\n\t"
    "s_cbranch_scc1 20f\n\t"
    "s_waitcnt vmcnt(2)\n\t"
    "20:\n\t"
    "s_barrier\n\t"
    "ds_read_b128 v[96:99], %[ea] offset:0\n\t"
    "ds_read_b128 v[100:103], %[ea] offset:2048\n\t"
    "ds_read_b128 v[104:107], %[da] offset:0\n\t"
    "ds_read_b128 v[108:111], %[da] offset:2048\n\t"
    "ds_read_b128 v[112:115], %[ea] offset:4096\n\t"
    "ds_read_b128 v[116:119], %[ea] offset:6144\n\t"
    "s_branch 2f\n\t"
    ".p2align 6\n\t"
    "1:\n\t"
    "s_cmp_eq_u32 %[la], 3\n\t"
    "s_waitcnt vmcnt(6)\n\t"
    "s_cbranch_scc1 21f\n\t"
    "s_waitcnt vmcnt(2)\n\t"
    "21:\n\t"
    "s_barrier\n\t"
    "2:\n\t"
    "s_waitcnt lgkmcnt(4)\n\t"
    "v_mfma_f32_16x16x32_bf16 v[64:67], v[128:131], v[96:99], 0\n\t"
    "ds_read_b128 v[120:123], %[da] offset:4096\n\t"
    "s_add_u32 m0, %[l1], 0\n\t"
    "ds_write_b128 %[rw], v[80:83] offset:0\n\t"
    "v_mfma_f32_16x16x32_bf16 v[68:71], v[132:135], v[96:99], 0\n\t"
    "ds_read_b128 v[124:127], %[da] offset:6144\n\t"
    "global_load_lds_dwordx4 %[av0], s[36:37] sc1\n\t"
    "ds_write_b128 %[rw], v[84:87] offset:1024\n\t"
    "v_mfma_f32_16x16x32_bf16 v[72:75], v[128:131], v[100:103], 0\n\t"
    "ds_write_b128 %[rw], v[88:91] offset:2048\n\t"
    "v_mfma_f32_16x16x32_bf16 v[76:79], v[132:135], v[100:103], 0\n\t"
    "ds_write_b128 %[rw], v[92:95] offset:3072\n\t"
    "s_waitcnt lgkmcnt(8)\n\t"
    "v_mfma_f32_16x16x32_bf16 v[64:67], v[136:139], v[104:107], v[64:67]\n\t"
    "ds_read_b128 v[96:99], %[ea] offset:8192\n\t"
    "s_add_u32 m0, %[l1], 4096\n\t"
    "v_mfma_f32_16x16x32_bf16 v[68:71], v[140:143], v[104:107], v[68:71]\n\t"
    "ds_read_b128 v[100:103], %[ea] offset:10240\n\t"
    "global_load_lds_dwordx4 %[av2], s[36:37] sc1\n\t"
    "v_mfma_f32_16x16x32_bf16 v[72:75], v[136:139], v[108:111], v[72:75]\n\t"
    "v_mfma_f32_16x16x32_bf16 v[76:79], v[140:143], v[108:111], v[76:79]\n\t"
    "s_waitcnt lgkmcnt(8)\n\t"
    "v_mfma_f32_16x16x32_bf16 v[64:67], v[144:147], v[112:115], v[64:67]\n\t"
    "ds_read_b128 v[104:107], %[da] offset:8192\n\t"
    "s_add_u32 m0, %[l1], 8192\n\t"
    "v_mfma_f32_16x16x32_bf16 v[68:71], v[148:151], v[112:115], v[68:71]\n\t"
    "ds_read_b128 v[108:111], %[da] offset:10240\n\t"
    "global_load_lds_dwordx4 %[av4], s[36:37] sc1\n\t"
    "v_mfma_f32_16x16x32_bf16 v[72:75], v[144:147], v[116:119], v[72:75]\n\t"
    "v_mfma_f32_16x16x32_bf16 v[76:79], v[148:151], v[116:119], v[76:79]\n\t"
    "s_waitcnt lgkmcnt(7)\n\t"
    "v_mfma_f32_16x16x32_bf16 v[64:67], v[152:155], v[120:123], v[64:67]\n\t"
    "ds_read_b128 v[112:115], %[ea] offset:12288\n\t"
    "s_add_u32 m0, %[l1], 12288\n\t"
    "v_mfma_f32_16x16x32_bf16 v[68:71], v[156:159], v[120:123], v[68:71]\n\t"
    "ds_read_b128 v[116:119], %[ea] offset:14336\n\t"
    "global_load_lds_dwordx4 %[av6], s[36:37] sc1\n\t"
    "v_mfma_f32_16x16x32_bf16 v[72:75], v[152:155], v[124:127], v[72:75]\n\t"
    "v_mfma_f32_16x16x32_bf16 v[76:79], v[156:159], v[124:127], v[76:79]\n\t"
    "s_waitcnt lgkmcnt(4)\n\t"
    "v_mfma_f32_16x16x32_bf16 v[64:67], v[160:163], v[96:99], v[64:67]\n\t"
    "ds_read_b128 v[120:123], %[da] offset:12288\n\t"
    "v_mfma_f32_16x16x32_bf16 v[68:71], v[164:167], v[96:99], v[68:71]\n\t"
    "ds_read_b128 v[124:127], %[da] offset:14336\n\t"
    "v_mfma_f32_16x16x32_bf16 v[72:75], v[160:163], v[100:103], v[72:75]\n\t"
    "v_mfma_f32_16x16x32_bf16 v[76:79], v[164:167], v[100:103], v[76:79]\n\t"
    "s_waitcnt lgkmcnt(4)\n\t"
    "v_mfma_f32_16x16x32_bf16 v[64:67], v[168:171], v[104:107], v[64:67]\n\t"
    "ds_read_b128 v[96:99], %[ea] offset:16384\n\t"
    "v_mfma_f32_16x16x32_bf16 v[68:71], v[172:175], v[104:107], v[68:71]\n\t"
    "ds_read_b128 v[100:103], %[ea] offset:18432\n\t"
    "v_mfma_f32_16x16x32_bf16 v[72:75], v[168:171], v[108:111], v[72:75]\n\t"
    "v_mfma_f32_16x16x32_bf16 v[76:79], v[172:175], v[108:111], v[76:79]\n\t"
    "s_waitcnt lgkmcnt(4)\n\t"
    "v_mfma_f32_16x16x32_bf16 v[64:67], v[176:179], v[112:115], v[64:67]\n\t"
    "ds_read_b128 v[104:107], %[da] offset:16384\n\t"
    "v_mfma_f32_16x16x32_bf16 v[68:71], v[180:183], v[112:115], v[68:71]\n\t"
    "ds_read_b128 v[108:111], %[da] offset:18432\n\t"
    "v_mfma_f32_16x16x32_bf16 v[72:75], v[176:179], v[116:119], v[72:75]\n\t"
    "v_mfma_f32_16x16x32_bf16 v[76:79], v[180:183], v[116:119], v[76:79]\n\t"
    "s_waitcnt lgkmcnt(4)\n\t"
    "v_mfma_f32_16x16x32_bf16 v[64:67], v[184:187], v[120:123], v[64:67]\n\t"
    "ds_read_b128 v[112:115], %[ea] offset:20480\n\t"
    "v_mfma_f32_16x16x32_bf16 v[68:71], v[188:191], v[120:123], v[68:71]\n\t"
    "ds_read_b128 v[116:119], %[ea] offset:22528\n\t"
    "v_mfma_f32_16x16x32_bf16 v[72:75], v[184:187], v[124:127], v[72:75]\n\t"
    "v_mfma_f32_16x16x32_bf16 v[76:79], v[188:191], v[124:127], v[76:79]\n\t"
    "s_cmp_eq_u32 %[la], 3\n\t"
    "s_waitcnt vmcnt(6)\n\t"
    "s_cbranch_scc1 22f\n\t"
    "s_waitcnt vmcnt(2)\n\t"
    "22:\n\t"
    "s_barrier\n\t"
    "s_waitcnt lgkmcnt(4)\n\t"
    "v_mfma_f32_16x16x32_bf16 v[64:67], v[192:195], v[96:99], v[64:67]\n\t"
    "ds_read_b128 v[120:123], %[da] offset:20480\n\t"
    "s_add_u32 m0, %[l0], 0\n\t"
    "v_mfma_f32_16x16x32_bf16 v[68:71], v[196:199], v[96:99], v[68:71]\n\t"
    "ds_read_b128 v[124:127], %[da] offset:22528\n\t"
    "global_load_lds_dwordx4 %[av0], s[38:39] sc1\n\t"
    "v_mfma_f32_16x16x32_bf16 v[72:75], v[192:195], v[100:103], v[72:75]\n\t"
    "v_mfma_f32_16x16x32_bf16 v[76:79], v[196:199], v[100:103], v[76:79]\n\t"
    "s_waitcnt lgkmcnt(4)\n\t"
    "v_mfma_f32_16x16x32_bf16 v[64:67], v[200:203], v[104:107], v[64:67]\n\t"
    "ds_read_b128 v[96:99], %[ea] offset:24576\n\t"
    "s_add_u32 m0, %[l0], 4096\n\t"
    "v_mfma_f32_16x16x32_bf16 v[68:71], v[204:207], v[104:107], v[68:71]\n\t"
    "ds_read_b128 v[100:103], %[ea] offset:26624\n\t"
    "global_load_lds_dwordx4 %[av2], s[38:39] sc1\n\t"
    "v_mfma_f32_16x16x32_bf16 v[72:75], v[200:203], v[108:111], v[72:75]\n\t"
    "v_mfma_f32_16x16x32_bf16 v[76:79], v[204:207], v[108:111], v[76:79]\n\t"
    "s_add_u32 s36, s36, %[ab]\n\t"
    "s_addc_u32 s37, s37, 0\n\t"
    "s_waitcnt lgkmcnt(4)\n\t"
    "v_mfma_f32_16x16x32_bf16 v[64:67], v[208:211], v[112:115], v[64:67]\n\t"
    "ds_read_b128 v[104:107], %[da] offset:24576\n\t"
    "s_add_u32 m0, %[l0], 8192\n\t"
    "v_mfma_f32_16x16x32_bf16 v[68:71], v[212:215], v[112:115], v[68:71]\n\t"
    "ds_read_b128 v[108:111], %[da] offset:26624\n\t"
    "global_load_lds_dwordx4 %[av4], s[38:39] sc1\n\t"
    "v_mfma_f32_16x16x32_bf16 v[72:75], v[208:211], v[116:119], v[72:75]\n\t"
    "ds_read_b32 v39, %[sa]\n\t"
    "v_mfma_f32_16x16x32_bf16 v[76:79], v[212:215], v[116:119], v[76:79]\n\t"
    "s_xor_b32 %[l1], %[l1], 0x10000\n\t"
    "s_waitcnt lgkmcnt(5)\n\t"
    "v_mfma_f32_16x16x32_bf16 v[64:67], v[216:219], v[120:123], v[64:67]\n\t"
    "ds_read_b128 v[112:115], %[ea] offset:28672\n\t"
    "s_add_u32 m0, %[l0], 12288\n\t"
    "v_mfma_f32_16x16x32_bf16 v[68:71], v[220:223], v[120:123], v[68:71]\n\t"
    "ds_read_b128 v[116:119], %[ea] offset:30720\n\t"
    "global_load_lds_dwordx4 %[av6], s[38:39] sc1\n\t"
    "v_mfma_f32_16x16x32_bf16 v[72:75], v[216:219], v[124:127], v[72:75]\n\t"
    "v_xor_b32 %[ea], 0x10000, %[ea]\n\t"
    "v_mfma_f32_16x16x32_bf16 v[76:79], v[220:223], v[124:127], v[76:79]\n\t"
    "s_waitcnt lgkmcnt(5)\n\t"
    "v_mfma_f32_16x16x32_bf16 v[64:67], v[224:227], v[96:99], v[64:67]\n\t"
    "ds_read_b128 v[120:123], %[da] offset:28672\n\t"
    "v_mfma_f32_16x16x32_bf16 v[68:71], v[228:231], v[96:99], v[68:71]\n\t"
    "ds_read_b128 v[124:127], %[da] offset:30720\n\t"
    "v_mfma_f32_16x16x32_bf16 v[72:75], v[224:227], v[100:103], v[72:75]\n\t"
    "v_xor_b32 %[da], 0x10000, %[da]\n\t"
    "v_mfma_f32_16x16x32_bf16 v[76:79], v[228:231], v[100:103], v[76:79]\n\t"
    "s_waitcnt lgkmcnt(5)\n\t"
    "v_mfma_f32_16x16x32_bf16 v[64:67], v[232:235], v[104:107], v[64:67]\n\t"
    "ds_read_b128 v[96:99], %[ea] offset:0\n\t"
    "v_mfma_f32_16x16x32_bf16 v[68:71], v[236:239], v[104:107], v[68:71]\n\t"
    "ds_read_b128 v[100:103], %[ea] offset:2048\n\t"
    "v_mfma_f32_16x16x32_bf16 v[72:75], v[232:235], v[108:111], v[72:75]\n\t"
    "v_mfma_f32_16x16x32_bf16 v[76:79], v[236:239], v[108:111], v[76:79]\n\t"
    "s_waitcnt lgkmcnt(4)\n\t"
    "v_mfma_f32_16x16x32_bf16 v[64:67], v[240:243], v[112:115], v[64:67]\n\t"
    "ds_read_b128 v[104:107], %[da] offset:0\n\t"
    "v_mad_u32_u24 v39, v39, %[mu], %[ad]\n\t"
    "v_mfma_f32_16x16x32_bf16 v[68:71], v[244:247], v[112:115], v[68:71]\n\t"
    "ds_read_b128 v[108:111], %[da] offset:2048\n\t"
    "v_cmp_ge_i32 vcc, %[lf], v39\n\t"
    "s_cmp_eq_u64 vcc, 0\n\t"
    "s_addc_u32 %[lf], %[lf], 0\n\t"
    "v_mfma_f32_16x16x32_bf16 v[72:75], v[240:243], v[116:119], v[72:75]\n\t"
    "v_mfma_f32_16x16x32_bf16 v[76:79], v[244:247], v[116:119], v[76:79]\n\t"
    "v_cmp_ge_i32 vcc, %[lf], v39\n\t"
    "s_cmp_eq_u64 vcc, 0\n\t"
    "s_addc_u32 %[lf], %[lf], 0\n\t"
    "s_waitcnt lgkmcnt(4)\n\t"
    "v_mfma_f32_16x16x32_bf16 v[80:83], v[248:251], v[120:123], v[64:67]\n\t"
    "ds_read_b128 v[112:115], %[ea] offset:4096\n\t"
    "s_min_i32 %[lf], %[lf], %[cp]\n\t"
    "s_add_u32 %[b], %[b], 1\n\t"
    "v_mfma_f32_16x16x32_bf16 v[84:87], v[252:255], v[120:123], v[68:71]\n\t"
    "ds_read_b128 v[116:119], %[ea] offset:6144\n\t"
    "v_mfma_f32_16x16x32_bf16 v[88:91], v[248:251], v[124:127], v[72:75]\n\t"
    "v_mfma_f32_16x16x32_bf16 v[92:95], v[252:255], v[124:127], v[76:79]\n\t"
    "s_add_u32 s38, s38, %[ab]\n\t"
    "s_addc_u32 s39, s39, 0\n\t"
    "s_xor_b32 %[l0], %[l0], 0x10000\n\t"
    "s_sub_u32 m0, %[lf], %[b]\n\t"
    "s_sub_u32 m0, m0, 1\n\t"
    "s_cmp_lt_u32 m0, %[up]\n\t"
    "s_cbranch_scc1 1b\n\t"
    "s_waitcnt lgkmcnt(0)\n\t"
    "s_nop 4"
    : "=&{v[80:95]}"(pd), [ea] "+&v"(ea), [da] "+&v"(da), [b] "+&s"(b), [lf] "+&s"(lf), "+{s[36:37]}"(p1), "+{s[38:39]}"(p0), [sa] "+&v"(sa), [l1] "+&s"(l1), [l0] "+&s"(l0)
    : [rw] "v"(rw), [ab] "s"(ab), [cp] "s"(cp), [mu] "s"(mu), [ad] "v"(ad), [up] "s"(up), "{v[128:159]}"(W[0]), "{v[160:191]}"(W[1]), "{v[192:223]}"(W[2]), "{v[224:255]}"(W[3]), [av0] "v"(av[0]), [av1] "v"(av[1]), [av2] "v"(av[2]), [av3] "v"(av[3]), [av4] "v"(av[4]), [av5] "v"(av[5]), [av6] "v"(av[6]), [av7] "v"(av[7]), [la] "s"(la)
    : "v39", "v64", "v65", "v66", "v67", "v68", "v69", "v70", "v71", "v72", "v73", "v74", "v75", "v76", "v77", "v78", "v79", "v96", "v97", "v98", "v99", "v100", "v101", "v102", "v103", "v104", "v105", "v106", "v107", "v108", "v109", "v110", "v111", "v112", "v113", "v114", "v115", "v116", "v117", "v118", "v119", "v120", "v121", "v122", "v123", "v124", "v125", "v126", "v127", "vcc", "scc", "m0", "memory");
}
__device__ __forceinline__ void fast_st0(const u32x32 (&W)[4], u32x16& pd, unsigned ea, unsigned da, int& b, int& lf, int cp, unsigned up, unsigned sa, unsigned mu, unsigned ad,
    unsigned rw, unsigned ab, const char* p1, const char* p0, unsigned co0, unsigned co1, int po, int pf, unsigned w0, unsigned sl, unsigned dv, const char* db, const char* pg) {
  unsigned st;
  asm volatile("s_barrier\n\t"
    "ds_read_b128 v[96:99], %[ea] offset:0\n\t"
    "ds_read_b128 v[100:103], %[ea] offset:2048\n\t"
    "ds_read_b128 v[104:107], %[da] offset:0\n\t"
    "ds_read_b128 v[108:111], %[da] offset:2048\n\t"
    "ds_read_b128 v[112:115], %[ea] offset:4096\n\t"
    "ds_read_b128 v[116:119], %[ea] offset:6144\n\t"
    "s_branch 2f\n\t"
    ".p2align 6\n\t"
    "1:\n\t"
    "s_barrier\n\t"
    "2:\n\t"
    "s_waitcnt lgkmcnt(4)\n\t"
    "v_mfma_f32_16x16x32_bf16 v[64:67], v[128:131], v[96:99], 0\n\t"
    "ds_read_b128 v[120:123], %[da] offset:4096\n\t"
    "v_mfma_f32_16x16x32_bf16 v[68:71], v[132:135], v[96:99], 0\n\t"
    "ds_read_b128 v[124:127], %[da] offset:6144\n\t"
    "v_mfma_f32_16x16x32_bf16 v[72:75], v[128:131], v[100:103], 0\n\t"
    "v_mfma_f32_16x16x32_bf16 v[76:79], v[132:135], v[100:103], 0\n\t"
    "s_waitcnt lgkmcnt(4)\n\t"
    "v_mfma_f32_16x16x32_bf16 v[64:67], v[136:139], v[104:107], v[64:67]\n\t"
    "ds_read_b128 v[96:99], %[ea] offset:8192\n\t"
    "v_mfma_f32_16x16x32_bf16 v[68:71], v[140:143], v[104:107], v[68:71]\n\t"
    "ds_read_b128 v[100:103], %[ea] offset:10240\n\t"
    "v_mfma_f32_16x16x32_bf16 v[72:75], v[136:139], v[108:111], v[72:75]\n\t"
    "v_mfma_f32_16x16x32_bf16 v[76:79], v[140:143], v[108:111], v[76:79]\n\t"
    "s_cmp_eq_u32 %[w0], 1\n\t"
    "s_cbranch_scc1 7f\n\t"
    "8:\n\t"
    "s_waitcnt lgkmcnt(4)\n\t"
    "v_mfma_f32_16x16x32_bf16 v[64:67], v[144:147], v[112:115], v[64:67]\n\t"
    "ds_read_b128 v[104:107], %[da] offset:8192\n\t"
    "v_mfma_f32_16x16x32_bf16 v[68:71], v[148:151], v[112:115], v[68:71]\n\t"
    "ds_read_b128 v[108:111], %[da] offset:10240\n\t"
    "v_mfma_f32_16x16x32_bf16 v[72:75], v[144:147], v[116:119], v[72:75]\n\t"
    "v_mfma_f32_16x16x32_bf16 v[76:79], v[148:151], v[116:119], v[76:79]\n\t"
    "s_waitcnt lgkmcnt(4)\n\t"
    "v_mfma_f32_16x16x32_bf16 v[64:67], v[152:155], v[120:123], v[64:67]\n\t"
    "ds_read_b128 v[112:115], %[ea] offset:12288\n\t"
    "v_mfma_f32_16x16x32_bf16 v[68:71], v[156:159], v[120:123], v[68:71]\n\t"
    "ds_read_b128 v[116:119], %[ea] offset:14336\n\t"
    "v_mfma_f32_16x16x32_bf16 v[72:75], v[152:155], v[124:127], v[72:75]\n\t"
    "v_mfma_f32_16x16x32_bf16 v[76:79], v[156:159], v[124:127], v[76:79]\n\t"
    "s_waitcnt lgkmcnt(4)\n\t"
    "v_mfma_f32_16x16x32_bf16 v[64:67], v[160:163], v[96:99], v[64:67]\n\t"
    "ds_read_b128 v[120:123], %[da] offset:12288\n\t"
    "v_mfma_f32_16x16x32_bf16 v[68:71], v[164:167], v[96:99], v[68:71]\n\t"
    "ds_read_b128 v[124:127], %[da] offset:14336\n\t"
    "v_mfma_f32_16x16x32_bf16 v[72:75], v[160:163], v[100:103], v[72:75]\n\t"
    "v_mfma_f32_16x16x32_bf16 v[76:79], v[164:167], v[100:103], v[76:79]\n\t"
    "s_waitcnt lgkmcnt(4)\n\t"
    "v_mfma_f32_16x16x32_bf16 v[64:67], v[168:171], v[104:107], v[64:67]\n\t"
    "ds_read_b128 v[96:99], %[ea] offset:16384\n\t"
    "v_mfma_f32_16x16x32_bf16 v[68:71], v[172:175], v[104:107], v[68:71]\n\t"
    "ds_read_b128 v[100:103], %[ea] offset:18432\n\t"
    "v_mfma_f32_16x16x32_bf16 v[72:75], v[168:171], v[108:111], v[72:75]\n\t"
    "v_mfma_f32_16x16x32_bf16 v[76:79], v[172:175], v[108:111], v[76:79]\n\t"
    "s_waitcnt lgkmcnt(4)\n\t"
    "v_mfma_f32_16x16x32_bf16 v[64:67], v[176:179], v[112:115], v[64:67]\n\t"
    "ds_read_b128 v[104:107], %[da] offset:16384\n\t"
    "v_mfma_f32_16x16x32_bf16 v[68:71], v[180:183], v[112:115], v[68:71]\n\t"
    "ds_read_b128 v[108:111], %[da] offset:18432\n\t"
    "v_mfma_f32_16x16x32_bf16 v[72:75], v[176:179], v[116:119], v[72:75]\n\t"
    "v_mfma_f32_16x16x32_bf16 v[76:79], v[180:183], v[116:119], v[76:79]\n\t"
    "s_waitcnt lgkmcnt(4)\n\t"
    "v_mfma_f32_16x16x32_bf16 v[64:67], v[184:187], v[120:123], v[64:67]\n\t"
    "ds_read_b128 v[112:115], %[ea] offset:20480\n\t"
    "v_mfma_f32_16x16x32_bf16 v[68:71], v[188:191], v[120:123], v[68:71]\n\t"
    "ds_read_b128 v[116:119], %[ea] offset:22528\n\t"
    "v_mfma_f32_16x16x32_bf16 v[72:75], v[184:187], v[124:127], v[72:75]\n\t"
    "v_mfma_f32_16x16x32_bf16 v[76:79], v[188:191], v[124:127], v[76:79]\n\t"
    "s_waitcnt vmcnt(0)\n\t"
    "s_barrier\n\t"
    "s_waitcnt lgkmcnt(4)\n\t"
    "v_mfma_f32_16x16x32_bf16 v[64:67], v[192:195], v[96:99], v[64:67]\n\t"
    "ds_read_b128 v[120:123], %[da] offset:20480\n\t"
    "v_mfma_f32_16x16x32_bf16 v[68:71], v[196:199], v[96:99], v[68:71]\n\t"
    "ds_read_b128 v[124:127], %[da] offset:22528\n\t"
    "v_mfma_f32_16x16x32_bf16 v[72:75], v[192:195], v[100:103], v[72:75]\n\t"
    "ds_read_b128 v[48:51], %[rw] offset:0\n\t"
    "v_mfma_f32_16x16x32_bf16 v[76:79], v[196:199], v[100:103], v[76:79]\n\t"
    "ds_read_b128 v[52:55], %[rw] offset:1024\n\t"
    "s_cmp_eq_u32 %[w0], 0\n\t"
    "s_cbranch_scc1 5f\n\t"
    "6:\n\t"
    "s_waitcnt lgkmcnt(6)\n\t"
    "v_mfma_f32_16x16x32_bf16 v[64:67], v[200:203], v[104:107], v[64:67]\n\t"
    "ds_read_b128 v[96:99], %[ea] offset:24576\n\t"
    "v_mfma_f32_16x16x32_bf16 v[68:71], v[204:207], v[104:107], v[68:71]\n\t"
    "ds_read_b128 v[100:103], %[ea] offset:26624\n\t"
    "v_mfma_f32_16x16x32_bf16 v[72:75], v[200:203], v[108:111], v[72:75]\n\t"
    "ds_read_b128 v[56:59], %[rw] offset:2048\n\t"
    "v_mfma_f32_16x16x32_bf16 v[76:79], v[204:207], v[108:111], v[76:79]\n\t"
    "ds_read_b128 v[60:63], %[rw] offset:3072\n\t"
    "s_waitcnt lgkmcnt(0)\n\t"
    "v_mfma_f32_16x16x32_bf16 v[64:67], v[208:211], v[112:115], v[64:67]\n\t"
    "ds_read_b128 v[104:107], %[da] offset:24576\n\t"
    "v_add_f32 v48, v48, v80\n\t"
    "v_add_f32 v49, v49, v81\n\t"
    "v_mfma_f32_16x16x32_bf16 v[68:71], v[212:215], v[112:115], v[68:71]\n\t"
    "ds_read_b128 v[108:111], %[da] offset:26624\n\t"
    "v_add_f32 v50, v50, v82\n\t"
    "v_add_f32 v51, v51, v83\n\t"
    "v_mfma_f32_16x16x32_bf16 v[72:75], v[208:211], v[116:119], v[72:75]\n\t"
    "ds_read_b32 v39, %[sa]\n\t"
    "v_add_f32 v52, v52, v84\n\t"
    "v_mfma_f32_16x16x32_bf16 v[76:79], v[212:215], v[116:119], v[76:79]\n\t"
    "v_add_f32 v53, v53, v85\n\t"
    "v_add_f32 v54, v54, v86\n\t"
    "v_mfma_f32_16x16x32_bf16 v[64:67], v[216:219], v[120:123], v[64:67]\n\t"
    "ds_read_b128 v[112:115], %[ea] offset:28672\n\t"
    "v_add_f32 v55, v55, v87\n\t"
    "v_add_f32 v56, v56, v88\n\t"
    "v_mfma_f32_16x16x32_bf16 v[68:71], v[220:223], v[120:123], v[68:71]\n\t"
    "ds_read_b128 v[116:119], %[ea] offset:30720\n\t"
    "v_add_f32 v57, v57, v89\n\t"
    "v_mfma_f32_16x16x32_bf16 v[72:75], v[216:219], v[124:127], v[72:75]\n\t"
    "v_xor_b32 %[ea], 0x10000, %[ea]\n\t"
    "v_add_f32 v58, v58, v90\n\t"
    "v_add_f32 v59, v59, v91\n\t"
    "v_mfma_f32_16x16x32_bf16 v[76:79], v[220:223], v[124:127], v[76:79]\n\t"
    "v_add_f32 v60, v60, v92\n\t"
    "v_mfma_f32_16x16x32_bf16 v[64:67], v[224:227], v[96:99], v[64:67]\n\t"
    "ds_read_b128 v[120:123], %[da] offset:28672\n\t"
    "v_add_f32 v61, v61, v93\n\t"
    "v_add_f32 v62, v62, v94\n\t"
    "v_mfma_f32_16x16x32_bf16 v[68:71], v[228:231], v[96:99], v[68:71]\n\t"
    "ds_read_b128 v[124:127], %[da] offset:30720\n\t"
    "v_add_f32 v63, v63, v95\n\t"
    "v_cvt_pk_bf16_f32 v40, v48, v49\n\t"
    "v_mfma_f32_16x16x32_bf16 v[72:75], v[224:227], v[100:103], v[72:75]\n\t"
    "v_xor_b32 %[da], 0x10000, %[da]\n\t"
    "v_cvt_pk_bf16_f32 v41, v50, v51\n\t"
    "v_mfma_f32_16x16x32_bf16 v[76:79], v[228:231], v[100:103], v[76:79]\n\t"
    "v_cvt_pk_bf16_f32 v42, v52, v53\n\t"
    "v_cvt_pk_bf16_f32 v43, v54, v55\n\t"
    "s_waitcnt lgkmcnt(5)\n\t"
    "v_mfma_f32_16x16x32_bf16 v[64:67], v[232:235], v[104:107], v[64:67]\n\t"
    "ds_read_b128 v[96:99], %[ea] offset:0\n\t"
    "global_store_dwordx4 %[co0], v[40:43], s[36:37] sc1\n\t"
    "v_cvt_pk_bf16_f32 v44, v56, v57\n\t"
    "v_mfma_f32_16x16x32_bf16 v[68:71], v[236:239], v[104:107], v[68:71]\n\t"
    "ds_read_b128 v[100:103], %[ea] offset:2048\n\t"
    "v_cvt_pk_bf16_f32 v45, v58, v59\n\t"
    "v_mfma_f32_16x16x32_bf16 v[72:75], v[232:235], v[108:111], v[72:75]\n\t"
    "v_cvt_pk_bf16_f32 v46, v60, v61\n\t"
    "v_cvt_pk_bf16_f32 v47, v62, v63\n\t"
    "v_mfma_f32_16x16x32_bf16 v[76:79], v[236:239], v[108:111], v[76:79]\n\t"
    "global_store_dwordx4 %[co1], v[44:47], s[36:37] sc1\n\t"
    "s_waitcnt lgkmcnt(4)\n\t"
    "v_mfma_f32_16x16x32_bf16 v[64:67], v[240:243], v[112:115], v[64:67]\n\t"
    "ds_read_b128 v[104:107], %[da] offset:0\n\t"
    "v_mad_u32_u24 v39, v39, %[mu], %[ad]\n\t"
    "v_mfma_f32_16x16x32_bf16 v[68:71], v[244:247], v[112:115], v[68:71]\n\t"
    "ds_read_b128 v[108:111], %[da] offset:2048\n\t"
    "v_cmp_ge_i32 vcc, %[lf], v39\n\t"
    "s_cmp_eq_u64 vcc, 0\n\t"
    "s_addc_u32 %[lf], %[lf], 0\n\t"
    "v_mfma_f32_16x16x32_bf16 v[72:75], v[240:243], v[116:119], v[72:75]\n\t"
    "v_mfma_f32_16x16x32_bf16 v[76:79], v[244:247], v[116:119], v[76:79]\n\t"
    "v_cmp_ge_i32 vcc, %[lf], v39\n\t"
    "s_cmp_eq_u64 vcc, 0\n\t"
    "s_addc_u32 %[lf], %[lf], 0\n\t"
    "s_waitcnt lgkmcnt(4)\n\t"
    "v_mfma_f32_16x16x32_bf16 v[80:83], v[248:251], v[120:123], v[64:67]\n\t"
    "ds_read_b128 v[112:115], %[ea] offset:4096\n\t"
    "s_min_i32 %[lf], %[lf], %[cp]\n\t"
    "s_add_u32 %[b], %[b], 1\n\t"
    "v_mfma_f32_16x16x32_bf16 v[84:87], v[252:255], v[120:123], v[68:71]\n\t"
    "ds_read_b128 v[116:119], %[ea] offset:6144\n\t"
    "v_mfma_f32_16x16x32_bf16 v[88:91], v[248:251], v[124:127], v[72:75]\n\t"
    "v_mfma_f32_16x16x32_bf16 v[92:95], v[252:255], v[124:127], v[76:79]\n\t"
    "s_mov_b64 s[36:37], s[38:39]\n\t"
    "s_add_u32 s38, s38, %[ab]\n\t"
    "s_addc_u32 s39, s39, 0\n\t"
    "s_sub_u32 m0, %[lf], %[b]\n\t"
    "s_sub_u32 m0, m0, 1\n\t"
    "s_cmp_lt_u32 m0, %[up]\n\t"
    "s_cbranch_scc1 1b\n\t"
    "s_waitcnt lgkmcnt(0)\n\t"
    "s_nop 4\n\t"
    "s_branch 9f\n\t"
    "5:\n\t"
    "s_add_u32 %[st], %[b], %[po]\n\t"
    "s_max_i32 %[st], %[st], %[p0]\n\t"
    "v_mov_b32 v39, %[st]\n\t"
    "v_mov_b32 v38, 0\n\t"
    "s_mov_b64 exec, 1\n\t"
    "global_store_dword v38, v39, %[pg] sc1\n\t"
    "s_mov_b64 exec, -1\n\t"
    "s_mov_b32 %[p0], %[p1]\n\t"
    "s_branch 6b\n\t"
    "7:\n\t"
    "s_cmp_ge_i32 %[lf], %[cp]\n\t"
    "s_cbranch_scc1 8b\n\t"
    "s_mov_b32 m0, %[sl]\n\t"
    "s_nop 0\n\t"
    "global_load_lds_dword %[dv], %[db] sc1\n\t"
    "s_branch 8b\n\t"
    "9:"
    : "=&{v[80:95]}"(pd), [ea] "+&v"(ea), [da] "+&v"(da), [b] "+&s"(b), [lf] "+&s"(lf), "+{s[36:37]}"(p1), "+{s[38:39]}"(p0), [sa] "+&v"(sa), [st] "=&s"(st), [sl] "+&s"(sl), [p0] "+&s"(pf)
    : [rw] "v"(rw), [ab] "s"(ab), [cp] "s"(cp), [mu] "s"(mu), [ad] "v"(ad), [up] "s"(up), "{v[128:159]}"(W[0]), "{v[160:191]}"(W[1]), "{v[192:223]}"(W[2]), "{v[224:255]}"(W[3]), [co0] "v"(co0), [w0] "s"(w0), [dv] "v"(dv), [db] "s"(db), [pg] "s"(pg), [po] "s"(po - 1), [p1] "s"(b + po), [co1] "v"(co1)
    : "v38", "v39", "v40", "v41", "v42", "v43", "v44", "v45", "v46", "v47", "v48", "v49", "v50", "v51", "v52", "v53", "v54", "v55", "v56", "v57", "v58", "v59", "v60", "v61", "v62", "v63", "v64", "v65", "v66", "v67", "v68", "v69", "v70", "v71", "v72", "v73", "v74", "v75", "v76", "v77", "v78", "v79", "v96", "v97", "v98", "v99", "v100", "v101", "v102", "v103", "v104", "v105", "v106", "v107", "v108", "v109", "v110", "v111", "v112", "v113", "v114", "v115", "v116", "v117", "v118", "v119", "v120", "v121", "v122", "v123", "v124", "v125", "v126", "v127", "vcc", "scc", "m0", "memory");
}
__device__ __forceinline__ void fast_st1(const u32x32 (&W)[4], u32x16& pd, unsigned ea, unsigned da, int& b, int& lf, int cp, unsigned up, unsigned sa, unsigned mu, unsigned ad,
    unsigned rw, unsigned ab, const char* p1, const char* p0, unsigned co0, unsigned co1, int po, int pf, unsigned w0, unsigned sl, unsigned dv, const char* db, const char* pg) {
  unsigned st;
  asm volatile("s_barrier\n\t"
    "ds_read_b128 v[96:99], %[ea] offset:0\n\t"
    "ds_read_b128 v[100:103], %[ea] offset:2048\n\t"
    "ds_read_b128 v[104:107], %[da] offset:0\n\t"
    "ds_read_b128 v[108:111], %[da] offset:2048\n\t"
    "ds_read_b128 v[112:115], %[ea] offset:4096\n\t"
    "ds_read_b128 v[116:119], %[ea] offset:6144\n\t"
    "s_branch 2f\n\t"
    ".p2align 6\n\t"
    "1:\n\t"
    "s_barrier\n\t"
    "2:\n\t"
    "s_waitcnt lgkmcnt(4)\n\t"
    "v_mfma_f32_16x16x32_bf16 v[64:67], v[128:131], v[96:99], 0\n\t"
    "ds_read_b128 v[120:123], %[da] offset:4096\n\t"
    "v_mfma_f32_16x16x32_bf16 v[68:71], v[132:135], v[96:99], 0\n\t"
    "ds_read_b128 v[124:127], %[da] offset:6144\n\t"
    "v_mfma_f32_16x16x32_bf16 v[72:75], v[128:131], v[100:103], 0\n\t"
    "v_mfma_f32_16x16x32_bf16 v[76:79], v[132:135], v[100:103], 0\n\t"
    "s_waitcnt lgkmcnt(4)\n\t"
    "v_mfma_f32_16x16x32_bf16 v[64:67], v[136:139], v[104:107], v[64:67]\n\t"
    "ds_read_b128 v[96:99], %[ea] offset:8192\n\t"
    "v_mfma_f32_16x16x32_bf16 v[68:71], v[140:143], v[104:107], v[68:71]\n\t"
    "ds_read_b128 v[100:103], %[ea] offset:10240\n\t"
    "v_mfma_f32_16x16x32_bf16 v[72:75], v[136:139], v[108:111], v[72:75]\n\t"
    "v_mfma_f32_16x16x32_bf16 v[76:79], v[140:143], v[108:111], v[76:79]\n\t"
    "s_cmp_eq_u32 %[w0], 1\n\t"
    "s_cbranch_scc1 7f\n\t"
    "8:\n\t"
    "s_waitcnt lgkmcnt(4)\n\t"
    "v_mfma_f32_16x16x32_bf16 v[64:67], v[144:147], v[112:115], v[64:67]\n\t"
    "ds_read_b128 v[104:107], %[da] offset:8192\n\t"
    "v_mfma_f32_16x16x32_bf16 v[68:71], v[148:151], v[112:115], v[68:71]\n\t"
    "ds_read_b128 v[108:111], %[da] offset:10240\n\t"
    "v_mfma_f32_16x16x32_bf16 v[72:75], v[144:147], v[116:119], v[72:75]\n\t"
    "v_mfma_f32_16x16x32_bf16 v[76:79], v[148:151], v[116:119], v[76:79]\n\t"
    "s_waitcnt lgkmcnt(4)\n\t"
    "v_mfma_f32_16x16x32_bf16 v[64:67], v[152:155], v[120:123], v[64:67]\n\t"
    "ds_read_b128 v[112:115], %[ea] offset:12288\n\t"
    "v_mfma_f32_16x16x32_bf16 v[68:71], v[156:159], v[120:123], v[68:71]\n\t"
    "ds_read_b128 v[116:119], %[ea] offset:14336\n\t"
    "v_mfma_f32_16x16x32_bf16 v[72:75], v[152:155], v[124:127], v[72:75]\n\t"
    "v_mfma_f32_16x16x32_bf16 v[76:79], v[156:159], v[124:127], v[76:79]\n\t"
    "s_waitcnt lgkmcnt(4)\n\t"
    "v_mfma_f32_16x16x32_bf16 v[64:67], v[160:163], v[96:99], v[64:67]\n\t"
    "ds_read_b128 v[120:123], %[da] offset:12288\n\t"
    "v_mfma_f32_16x16x32_bf16 v[68:71], v[164:167], v[96:99], v[68:71]\n\t"
    "ds_read_b128 v[124:127], %[da] offset:14336\n\t"
    "v_mfma_f32_16x16x32_bf16 v[72:75], v[160:163], v[100:103], v[72:75]\n\t"
    "v_mfma_f32_16x16x32_bf16 v[76:79], v[164:167], v[100:103], v[76:79]\n\t"
    "s_waitcnt lgkmcnt(4)\n\t"
    "v_mfma_f32_16x16x32_bf16 v[64:67], v[168:171], v[104:107], v[64:67]\n\t"
    "ds_read_b128 v[96:99], %[ea] offset:16384\n\t"
    "v_mfma_f32_16x16x32_bf16 v[68:71], v[172:175], v[104:107], v[68:71]\n\t"
    "ds_read_b128 v[100:103], %[ea] offset:18432\n\t"
    "v_mfma_f32_16x16x32_bf16 v[72:75], v[168:171], v[108:111], v[72:75]\n\t"
    "v_mfma_f32_16x16x32_bf16 v[76:79], v[172:175], v[108:111], v[76:79]\n\t"
    "s_waitcnt lgkmcnt(4)\n\t"
    "v_mfma_f32_16x16x32_bf16 v[64:67], v[176:179], v[112:115], v[64:67]\n\t"
    "ds_read_b128 v[104:107], %[da] offset:16384\n\t"
    "v_mfma_f32_16x16x32_bf16 v[68:71], v[180:183], v[112:115], v[68:71]\n\t"
    "ds_read_b128 v[108:111], %[da] offset:18432\n\t"
    "v_mfma_f32_16x16x32_bf16 v[72:75], v[176:179], v[116:119], v[72:75]\n\t"
    "v_mfma_f32_16x16x32_bf16 v[76:79], v[180:183], v[116:119], v[76:79]\n\t"
    "s_waitcnt lgkmcnt(4)\n\t"
    "v_mfma_f32_16x16x32_bf16 v[64:67], v[184:187], v[120:123], v[64:67]\n\t"
    "ds_read_b128 v[112:115], %[ea] offset:20480\n\t"
    "v_mfma_f32_16x16x32_bf16 v[68:71], v[188:191], v[120:123], v[68:71]\n\t"
    "ds_read_b128 v[116:119], %[ea] offset:22528\n\t"
    "v_mfma_f32_16x16x32_bf16 v[72:75], v[184:187], v[124:127], v[72:75]\n\t"
    "v_mfma_f32_16x16x32_bf16 v[76:79], v[188:191], v[124:127], v[76:79]\n\t"
    "s_waitcnt vmcnt(0)\n\t"
    "s_barrier\n\t"
    "s_waitcnt lgkmcnt(4)\n\t"
    "v_mfma_f32_16x16x32_bf16 v[64:67], v[192:195], v[96:99], v[64:67]\n\t"
    "ds_read_b128 v[120:123], %[da] offset:20480\n\t"
    "v_mfma_f32_16x16x32_bf16 v[68:71], v[196:199], v[96:99], v[68:71]\n\t"
    "ds_read_b128 v[124:127], %[da] offset:22528\n\t"
    "v_mfma_f32_16x16x32_bf16 v[72:75], v[192:195], v[100:103], v[72:75]\n\t"
    "ds_read_b128 v[48:51], %[rw] offset:0\n\t"
    "v_mfma_f32_16x16x32_bf16 v[76:79], v[196:199], v[100:103], v[76:79]\n\t"
    "ds_read_b128 v[52:55], %[rw] offset:1024\n\t"
    "s_cmp_eq_u32 %[w0], 0\n\t"
    "s_cbranch_scc1 5f\n\t"
    "6:\n\t"
    "s_waitcnt lgkmcnt(6)\n\t"
    "v_mfma_f32_16x16x32_bf16 v[64:67], v[200:203], v[104:107], v[64:67]\n\t"
    "ds_read_b128 v[96:99], %[ea] offset:24576\n\t"
    "v_mfma_f32_16x16x32_bf16 v[68:71], v[204:207], v[104:107], v[68:71]\n\t"
    "ds_read_b128 v[100:103], %[ea] offset:26624\n\t"
    "v_mfma_f32_16x16x32_bf16 v[72:75], v[200:203], v[108:111], v[72:75]\n\t"
    "ds_read_b128 v[56:59], %[rw] offset:2048\n\t"
    "v_mfma_f32_16x16x32_bf16 v[76:79], v[204:207], v[108:111], v[76:79]\n\t"
    "ds_read_b128 v[60:63], %[rw] offset:3072\n\t"
    "s_waitcnt lgkmcnt(0)\n\t"
    "v_mfma_f32_16x16x32_bf16 v[64:67], v[208:211], v[112:115], v[64:67]\n\t"
    "ds_read_b128 v[104:107], %[da] offset:24576\n\t"
    "v_add_f32 v48, v48, v80\n\t"
    "v_add_f32 v49, v49, v81\n\t"
    "v_add_f32 v50, v50, v82\n\t"
    "v_mfma_f32_16x16x32_bf16 v[68:71], v[212:215], v[112:115], v[68:71]\n\t"
    "ds_read_b128 v[108:111], %[da] offset:26624\n\t"
    "v_add_f32 v51, v51, v83\n\t"
    "v_add_f32 v52, v52, v84\n\t"
    "v_add_f32 v53, v53, v85\n\t"
    "v_mfma_f32_16x16x32_bf16 v[72:75], v[208:211], v[116:119], v[72:75]\n\t"
    "ds_read_b32 v39, %[sa]\n\t"
    "v_add_f32 v54, v54, v86\n\t"
    "v_add_f32 v55, v55, v87\n\t"
    "v_add_f32 v56, v56, v88\n\t"
    "v_mfma_f32_16x16x32_bf16 v[76:79], v[212:215], v[116:119], v[76:79]\n\t"
    "v_add_f32 v57, v57, v89\n\t"
    "v_add_f32 v58, v58, v90\n\t"
    "v_add_f32 v59, v59, v91\n\t"
    "v_mfma_f32_16x16x32_bf16 v[64:67], v[216:219], v[120:123], v[64:67]\n\t"
    "ds_read_b128 v[112:115], %[ea] offset:28672\n\t"
    "v_add_f32 v60, v60, v92\n\t"
    "v_add_f32 v61, v61, v93\n\t"
    "v_add_f32 v62, v62, v94\n\t"
    "v_mfma_f32_16x16x32_bf16 v[68:71], v[220:223], v[120:123], v[68:71]\n\t"
    "ds_read_b128 v[116:119], %[ea] offset:30720\n\t"
    "v_add_f32 v63, v63, v95\n\t"
    "v_mul_f32 v40, %[nl], v48\n\t"
    "v_mul_f32 v41, %[nl], v49\n\t"
    "v_mfma_f32_16x16x32_bf16 v[72:75], v[216:219], v[124:127], v[72:75]\n\t"
    "v_xor_b32 %[ea], 0x10000, %[ea]\n\t"
    "v_mul_f32 v42, %[nl], v50\n\t"
    "v_mul_f32 v43, %[nl], v51\n\t"
    "v_mul_f32 v44, %[nl], v56\n\t"
    "v_mfma_f32_16x16x32_bf16 v[76:79], v[220:223], v[124:127], v[76:79]\n\t"
    "v_mul_f32 v45, %[nl], v57\n\t"
    "v_mul_f32 v46, %[nl], v58\n\t"
    "v_mul_f32 v47, %[nl], v59\n\t"
    "v_mfma_f32_16x16x32_bf16 v[64:67], v[224:227], v[96:99], v[64:67]\n\t"
    "ds_read_b128 v[120:123], %[da] offset:28672\n\t"
    "v_exp_f32 v40, v40\n\t"
    "v_exp_f32 v41, v41\n\t"
    "v_exp_f32 v42, v42\n\t"
    "v_mfma_f32_16x16x32_bf16 v[68:71], v[228:231], v[96:99], v[68:71]\n\t"
    "ds_read_b128 v[124:127], %[da] offset:30720\n\t"
    "v_exp_f32 v43, v43\n\t"
    "v_exp_f32 v44, v44\n\t"
    "v_exp_f32 v45, v45\n\t"
    "v_mfma_f32_16x16x32_bf16 v[72:75], v[224:227], v[100:103], v[72:75]\n\t"
    "v_xor_b32 %[da], 0x10000, %[da]\n\t"
    "v_exp_f32 v46, v46\n\t"
    "v_exp_f32 v47, v47\n\t"
    "v_add_f32 v40, 1.0, v40\n\t"
    "v_mfma_f32_16x16x32_bf16 v[76:79], v[228:231], v[100:103], v[76:79]\n\t"
    "v_add_f32 v41, 1.0, v41\n\t"
    "v_add_f32 v42, 1.0, v42\n\t"
    "v_add_f32 v43, 1.0, v43\n\t"
    "s_waitcnt lgkmcnt(5)\n\t"
    "v_mfma_f32_16x16x32_bf16 v[64:67], v[232:235], v[104:107], v[64:67]\n\t"
    "ds_read_b128 v[96:99], %[ea] offset:0\n\t"
    "v_add_f32 v44, 1.0, v44\n\t"
    "v_add_f32 v45, 1.0, v45\n\t"
    "v_add_f32 v46, 1.0, v46\n\t"
    "v_mfma_f32_16x16x32_bf16 v[68:71], v[236:239], v[104:107], v[68:71]\n\t"
    "ds_read_b128 v[100:103], %[ea] offset:2048\n\t"
    "v_add_f32 v47, 1.0, v47\n\t"
    "v_rcp_f32 v40, v40\n\t"
    "v_rcp_f32 v41, v41\n\t"
    "v_mfma_f32_16x16x32_bf16 v[72:75], v[232:235], v[108:111], v[72:75]\n\t"
    "v_rcp_f32 v42, v42\n\t"
    "v_rcp_f32 v43, v43\n\t"
    "v_rcp_f32 v44, v44\n\t"
    "v_mfma_f32_16x16x32_bf16 v[76:79], v[236:239], v[108:111], v[76:79]\n\t"
    "v_rcp_f32 v45, v45\n\t"
    "v_rcp_f32 v46, v46\n\t"
    "v_rcp_f32 v47, v47\n\t"
    "s_waitcnt lgkmcnt(4)\n\t"
    "v_mfma_f32_16x16x32_bf16 v[64:67], v[240:243], v[112:115], v[64:67]\n\t"
    "ds_read_b128 v[104:107], %[da] offset:0\n\t"
    "v_mad_u32_u24 v39, v39, %[mu], %[ad]\n\t"
    "v_mul_f32 v40, v48, v40\n\t"
    "v_mul_f32 v41, v49, v41\n\t"
    "v_mul_f32 v42, v50, v42\n\t"
    "v_mfma_f32_16x16x32_bf16 v[68:71], v[244:247], v[112:115], v[68:71]\n\t"
    "ds_read_b128 v[108:111], %[da] offset:2048\n\t"
    "v_cmp_ge_i32 vcc, %[lf], v39\n\t"
    "s_cmp_eq_u64 vcc, 0\n\t"
    "s_addc_u32 %[lf], %[lf], 0\n\t"
    "v_mul_f32 v43, v51, v43\n\t"
    "v_mul_f32 v44, v56, v44\n\t"
    "v_mul_f32 v45, v57, v45\n\t"
    "v_mfma_f32_16x16x32_bf16 v[72:75], v[240:243], v[116:119], v[72:75]\n\t"
    "v_mul_f32 v46, v58, v46\n\t"
    "v_mul_f32 v47, v59, v47\n\t"
    "v_mul_f32 v40, v40, v52\n\t"
    "v_mfma_f32_16x16x32_bf16 v[76:79], v[244:247], v[116:119], v[76:79]\n\t"
    "v_cmp_ge_i32 vcc, %[lf], v39\n\t"
    "s_cmp_eq_u64 vcc, 0\n\t"
    "s_addc_u32 %[lf], %[lf], 0\n\t"
    "v_mul_f32 v41, v41, v53\n\t"
    "v_mul_f32 v42, v42, v54\n\t"
    "v_mul_f32 v43, v43, v55\n\t"
    "s_waitcnt lgkmcnt(4)\n\t"
    "v_mfma_f32_16x16x32_bf16 v[80:83], v[248:251], v[120:123], v[64:67]\n\t"
    "ds_read_b128 v[112:115], %[ea] offset:4096\n\t"
    "s_min_i32 %[lf], %[lf], %[cp]\n\t"
    "s_add_u32 %[b], %[b], 1\n\t"
    "v_mul_f32 v44, v44, v60\n\t"
    "v_mul_f32 v45, v45, v61\n\t"
    "v_mul_f32 v46, v46, v62\n\t"
    "v_mfma_f32_16x16x32_bf16 v[84:87], v[252:255], v[120:123], v[68:71]\n\t"
    "ds_read_b128 v[116:119], %[ea] offset:6144\n\t"
    "v_mul_f32 v47, v47, v63\n\t"
    "v_cvt_pk_bf16_f32 v48, v40, v41\n\t"
    "v_cvt_pk_bf16_f32 v49, v42, v43\n\t"
    "v_mfma_f32_16x16x32_bf16 v[88:91], v[248:251], v[124:127], v[72:75]\n\t"
    "v_cvt_pk_bf16_f32 v50, v44, v45\n\t"
    "v_cvt_pk_bf16_f32 v51, v46, v47\n\t"
    "s_nop 1\n\t"
    "v_mfma_f32_16x16x32_bf16 v[92:95], v[252:255], v[124:127], v[76:79]\n\t"
    "v_permlane16_swap_b32 v48, v50\n\t"
    "v_permlane16_swap_b32 v49, v51\n\t"
    "global_store_dwordx4 %[co0], v[48:51], s[36:37] sc1\n\t"
    "s_mov_b64 s[36:37], s[38:39]\n\t"
    "s_add_u32 s38, s38, %[ab]\n\t"
    "s_addc_u32 s39, s39, 0\n\t"
    "s_sub_u32 m0, %[lf], %[b]\n\t"
    "s_sub_u32 m0, m0, 1\n\t"
    "s_cmp_lt_u32 m0, %[up]\n\t"
    "s_cbranch_scc1 1b\n\t"
    "s_waitcnt lgkmcnt(0)\n\t"
    "s_nop 4\n\t"
    "s_branch 9f\n\t"
    "5:\n\t"
    "s_add_u32 %[st], %[b], %[po]\n\t"
    "s_max_i32 %[st], %[st], %[p0]\n\t"
    "v_mov_b32 v39, %[st]\n\t"
    "v_mov_b32 v38, 0\n\t"
    "s_mov_b64 exec, 1\n\t"
    "global_store_dword v38, v39, %[pg] sc1\n\t"
    "s_mov_b64 exec, -1\n\t"
    "s_mov_b32 %[p0], %[p1]\n\t"
    "s_branch 6b\n\t"
    "7:\n\t"
    "s_cmp_ge_i32 %[lf], %[cp]\n\t"
    "s_cbranch_scc1 8b\n\t"
    "s_mov_b32 m0, %[sl]\n\t"
    "s_nop 0\n\t"
    "global_load_lds_dword %[dv], %[db] sc1\n\t"
    "s_branch 8b\n\t"
    "9:"
    : "=&{v[80:95]}"(pd), [ea] "+&v"(ea), [da] "+&v"(da), [b] "+&s"(b), [lf] "+&s"(lf), "+{s[36:37]}"(p1), "+{s[38:39]}"(p0), [sa] "+&v"(sa), [st] "=&s"(st), [sl] "+&s"(sl), [p0] "+&s"(pf)
    : [rw] "v"(rw), [ab] "s"(ab), [cp] "s"(cp), [mu] "s"(mu), [ad] "v"(ad), [up] "s"(up), "{v[128:159]}"(W[0]), "{v[160:191]}"(W[1]), "{v[192:223]}"(W[2]), "{v[224:255]}"(W[3]), [co0] "v"(co0), [w0] "s"(w0), [dv] "v"(dv), [db] "s"(db), [pg] "s"(pg), [po] "s"(po - 1), [p1] "s"(b + po), [nl] "s"(-LOG2E)
    : "v38", "v39", "v40", "v41", "v42", "v43", "v44", "v45", "v46", "v47", "v48", "v49", "v50", "v51", "v52", "v53", "v54", "v55", "v56", "v57", "v58", "v59", "v60", "v61", "v62", "v63", "v64", "v65", "v66", "v67", "v68", "v69", "v70", "v71", "v72", "v73", "v74", "v75", "v76", "v77", "v78", "v79", "v96", "v97", "v98", "v99", "v100", "v101", "v102", "v103", "v104", "v105", "v106", "v107", "v108", "v109", "v110", "v111", "v112", "v113", "v114", "v115", "v116", "v117", "v118", "v119", "v120", "v121", "v122", "v123", "v124", "v125", "v126", "v127", "vcc", "scc", "m0", "memory");
}
__device__ __forceinline__ void fast_st2(const u32x32 (&W)[4], u32x16& pd, unsigned ea, unsigned da, int& b, int& lf, int cp, unsigned up, unsigned sa, unsigned mu, unsigned ad,
    unsigned rw, unsigned ab, const char* p1, const char* p0, unsigned co0, unsigned co1, int po, int pf, unsigned w0, unsigned sl, unsigned dv, const char* db, const char* pg) {
  unsigned st;
  asm volatile("s_barrier\n\t"
    "ds_read_b128 v[96:99], %[ea] offset:0\n\t"
    "ds_read_b128 v[100:103], %[ea] offset:2048\n\t"
    "ds_read_b128 v[104:107], %[da] offset:0\n\t"
    "ds_read_b128 v[108:111], %[da] offset:2048\n\t"
    "ds_read_b128 v[112:115], %[ea] offset:4096\n\t"
    "ds_read_b128 v[116:119], %[ea] offset:6144\n\t"
    "s_branch 2f\n\t"
    ".p2align 6\n\t"
    "1:\n\t"
    "s_barrier\n\t"
    "2:\n\t"
    "s_waitcnt lgkmcnt(4)\n\t"
    "v_mfma_f32_16x16x32_bf16 v[64:67], v[128:131], v[96:99], 0\n\t"
    "ds_read_b128 v[120:123], %[da] offset:4096\n\t"
    "v_mfma_f32_16x16x32_bf16 v[68:71], v[132:135], v[96:99], 0\n\t"
    "ds_read_b128 v[124:127], %[da] offset:6144\n\t"
    "v_mfma_f32_16x16x32_bf16 v[72:75], v[128:131], v[100:103], 0\n\t"
    "v_mfma_f32_16x16x32_bf16 v[76:79], v[132:135], v[100:103], 0\n\t"
    "s_waitcnt lgkmcnt(4)\n\t"
    "v_mfma_f32_16x16x32_bf16 v[64:67], v[136:139], v[104:107], v[64:67]\n\t"
    "ds_read_b128 v[96:99], %[ea] offset:8192\n\t"
    "v_mfma_f32_16x16x32_bf16 v[68:71], v[140:143], v[104:107], v[68:71]\n\t"
    "ds_read_b128 v[100:103], %[ea] offset:10240\n\t"
    "v_mfma_f32_16x16x32_bf16 v[72:75], v[136:139], v[108:111], v[72:75]\n\t"
    "v_mfma_f32_16x16x32_bf16 v[76:79], v[140:143], v[108:111], v[76:79]\n\t"
    "s_cmp_eq_u32 %[w0], 1\n\t"
    "s_cbranch_scc1 7f\n\t"
    "8:\n\t"
    "s_waitcnt lgkmcnt(4)\n\t"
    "v_mfma_f32_16x16x32_bf16 v[64:67], v[144:147], v[112:115], v[64:67]\n\t"
    "ds_read_b128 v[104:107], %[da] offset:8192\n\t"
    "v_mfma_f32_16x16x32_bf16 v[68:71], v[148:151], v[112:115], v[68:71]\n\t"
    "ds_read_b128 v[108:111], %[da] offset:10240\n\t"
    "v_mfma_f32_16x16x32_bf16 v[72:75], v[144:147], v[116:119], v[72:75]\n\t"
    "v_mfma_f32_16x16x32_bf16 v[76:79], v[148:151], v[116:119], v[76:79]\n\t"
    "s_waitcnt lgkmcnt(4)\n\t"
    "v_mfma_f32_16x16x32_bf16 v[64:67], v[152:155], v[120:123], v[64:67]\n\t"
    "ds_read_b128 v[112:115], %[ea] offset:12288\n\t"
    "v_mfma_f32_16x16x32_bf16 v[68:71], v[156:159], v[120:123], v[68:71]\n\t"
    "ds_read_b128 v[116:119], %[ea] offset:14336\n\t"
    "v_mfma_f32_16x16x32_bf16 v[72:75], v[152:155], v[124:127], v[72:75]\n\t"
    "v_mfma_f32_16x16x32_bf16 v[76:79], v[156:159], v[124:127], v[76:79]\n\t"
    "s_waitcnt lgkmcnt(4)\n\t"
    "v_mfma_f32_16x16x32_bf16 v[64:67], v[160:163], v[96:99], v[64:67]\n\t"
    "ds_read_b128 v[120:123], %[da] offset:12288\n\t"
    "v_mfma_f32_16x16x32_bf16 v[68:71], v[164:167], v[96:99], v[68:71]\n\t"
    "ds_read_b128 v[124:127], %[da] offset:14336\n\t"
    "v_mfma_f32_16x16x32_bf16 v[72:75], v[160:163], v[100:103], v[72:75]\n\t"
    "v_mfma_f32_16x16x32_bf16 v[76:79], v[164:167], v[100:103], v[76:79]\n\t"
    "s_waitcnt lgkmcnt(4)\n\t"
    "v_mfma_f32_16x16x32_bf16 v[64:67], v[168:171], v[104:107], v[64:67]\n\t"
    "ds_read_b128 v[96:99], %[ea] offset:16384\n\t"
    "v_mfma_f32_16x16x32_bf16 v[68:71], v[172:175], v[104:107], v[68:71]\n\t"
    "ds_read_b128 v[100:103], %[ea] offset:18432\n\t"
    "v_mfma_f32_16x16x32_bf16 v[72:75], v[168:171], v[108:111], v[72:75]\n\t"
    "v_mfma_f32_16x16x32_bf16 v[76:79], v[172:175], v[108:111], v[76:79]\n\t"
    "s_waitcnt lgkmcnt(4)\n\t"
    "v_mfma_f32_16x16x32_bf16 v[64:67], v[176:179], v[112:115], v[64:67]\n\t"
    "ds_read_b128 v[104:107], %[da] offset:16384\n\t"
    "v_mfma_f32_16x16x32_bf16 v[68:71], v[180:183], v[112:115], v[68:71]\n\t"
    "ds_read_b128 v[108:111], %[da] offset:18432\n\t"
    "v_mfma_f32_16x16x32_bf16 v[72:75], v[176:179], v[116:119], v[72:75]\n\t"
    "v_mfma_f32_16x16x32_bf16 v[76:79], v[180:183], v[116:119], v[76:79]\n\t"
    "s_waitcnt lgkmcnt(4)\n\t"
    "v_mfma_f32_16x16x32_bf16 v[64:67], v[184:187], v[120:123], v[64:67]\n\t"
    "ds_read_b128 v[112:115], %[ea] offset:20480\n\t"
    "v_mfma_f32_16x16x32_bf16 v[68:71], v[188:191], v[120:123], v[68:71]\n\t"
    "ds_read_b128 v[116:119], %[ea] offset:22528\n\t"
    "v_mfma_f32_16x16x32_bf16 v[72:75], v[184:187], v[124:127], v[72:75]\n\t"
    "v_mfma_f32_16x16x32_bf16 v[76:79], v[188:191], v[124:127], v[76:79]\n\t"
    "s_waitcnt vmcnt(0)\n\t"
    "s_barrier\n\t"
    "s_waitcnt lgkmcnt(4)\n\t"
    "v_mfma_f32_16x16x32_bf16 v[64:67], v[192:195], v[96:99], v[64:67]\n\t"
    "ds_read_b128 v[120:123], %[da] offset:20480\n\t"
    "v_mfma_f32_16x16x32_bf16 v[68:71], v[196:199], v[96:99], v[68:71]\n\t"
    "ds_read_b128 v[124:127], %[da] offset:22528\n\t"
    "v_mfma_f32_16x16x32_bf16 v[72:75], v[192:195], v[100:103], v[72:75]\n\t"
    "ds_read_b128 v[48:51], %[rw] offset:0\n\t"
    "v_mfma_f32_16x16x32_bf16 v[76:79], v[196:199], v[100:103], v[76:79]\n\t"
    "ds_read_b128 v[52:55], %[rw] offset:1024\n\t"
    "s_cmp_eq_u32 %[w0], 0\n\t"
    "s_cbranch_scc1 5f\n\t"
    "6:\n\t"
    "s_waitcnt lgkmcnt(6)\n\t"
    "v_mfma_f32_16x16x32_bf16 v[64:67], v[200:203], v[104:107], v[64:67]\n\t"
    "ds_read_b128 v[96:99], %[ea] offset:24576\n\t"
    "v_mfma_f32_16x16x32_bf16 v[68:71], v[204:207], v[104:107], v[68:71]\n\t"
    "ds_read_b128 v[100:103], %[ea] offset:26624\n\t"
    "v_mfma_f32_16x16x32_bf16 v[72:75], v[200:203], v[108:111], v[72:75]\n\t"
    "ds_read_b128 v[56:59], %[rw] offset:2048\n\t"
    "v_mfma_f32_16x16x32_bf16 v[76:79], v[204:207], v[108:111], v[76:79]\n\t"
    "ds_read_b128 v[60:63], %[rw] offset:3072\n\t"
    "s_waitcnt lgkmcnt(0)\n\t"
    "v_mfma_f32_16x16x32_bf16 v[64:67], v[208:211], v[112:115], v[64:67]\n\t"
    "ds_read_b128 v[104:107], %[da] offset:24576\n\t"
    "v_cvt_pk_bf16_f32 v40, v80, v81\n\t"
    "v_cvt_pk_bf16_f32 v41, v82, v83\n\t"
    "v_mfma_f32_16x16x32_bf16 v[68:71], v[212:215], v[112:115], v[68:71]\n\t"
    "ds_read_b128 v[108:111], %[da] offset:26624\n\t"
    "v_cvt_pk_bf16_f32 v42, v84, v85\n\t"
    "v_mfma_f32_16x16x32_bf16 v[72:75], v[208:211], v[116:119], v[72:75]\n\t"
    "ds_read_b32 v39, %[sa]\n\t"
    "v_cvt_pk_bf16_f32 v43, v86, v87\n\t"
    "v_mfma_f32_16x16x32_bf16 v[76:79], v[212:215], v[116:119], v[76:79]\n\t"
    "global_store_dwordx4 %[co0], v[40:43], s[36:37] sc1\n\t"
    "v_mfma_f32_16x16x32_bf16 v[64:67], v[216:219], v[120:123], v[64:67]\n\t"
    "ds_read_b128 v[112:115], %[ea] offset:28672\n\t"
    "v_cvt_pk_bf16_f32 v44, v88, v89\n\t"
    "v_cvt_pk_bf16_f32 v45, v90, v91\n\t"
    "v_mfma_f32_16x16x32_bf16 v[68:71], v[220:223], v[120:123], v[68:71]\n\t"
    "ds_read_b128 v[116:119], %[ea] offset:30720\n\t"
    "v_cvt_pk_bf16_f32 v46, v92, v93\n\t"
    "v_mfma_f32_16x16x32_bf16 v[72:75], v[216:219], v[124:127], v[72:75]\n\t"
    "v_xor_b32 %[ea], 0x10000, %[ea]\n\t"
    "v_cvt_pk_bf16_f32 v47, v94, v95\n\t"
    "v_mfma_f32_16x16x32_bf16 v[76:79], v[220:223], v[124:127], v[76:79]\n\t"
    "global_store_dwordx4 %[co1], v[44:47], s[36:37] sc1\n\t"
    "v_mfma_f32_16x16x32_bf16 v[64:67], v[224:227], v[96:99], v[64:67]\n\t"
    "ds_read_b128 v[120:123], %[da] offset:28672\n\t"
    "v_cvt_pk_bf16_f32 v48, v48, v49\n\t"
    "v_cvt_pk_bf16_f32 v49, v50, v51\n\t"
    "v_mfma_f32_16x16x32_bf16 v[68:71], v[228:231], v[96:99], v[68:71]\n\t"
    "ds_read_b128 v[124:127], %[da] offset:30720\n\t"
    "v_cvt_pk_bf16_f32 v50, v52, v53\n\t"
    "v_mfma_f32_16x16x32_bf16 v[72:75], v[224:227], v[100:103], v[72:75]\n\t"
    "v_xor_b32 %[da], 0x10000, %[da]\n\t"
    "v_cvt_pk_bf16_f32 v51, v54, v55\n\t"
    "v_mfma_f32_16x16x32_bf16 v[76:79], v[228:231], v[100:103], v[76:79]\n\t"
    "global_store_dwordx4 %[co0], v[48:51], s[36:37] offset:2048 sc1\n\t"
    "s_waitcnt lgkmcnt(5)\n\t"
    "v_mfma_f32_16x16x32_bf16 v[64:67], v[232:235], v[104:107], v[64:67]\n\t"
    "ds_read_b128 v[96:99], %[ea] offset:0\n\t"
    "v_cvt_pk_bf16_f32 v56, v56, v57\n\t"
    "v_cvt_pk_bf16_f32 v57, v58, v59\n\t"
    "v_mfma_f32_16x16x32_bf16 v[68:71], v[236:239], v[104:107], v[68:71]\n\t"
    "ds_read_b128 v[100:103], %[ea] offset:2048\n\t"
    "v_cvt_pk_bf16_f32 v58, v60, v61\n\t"
    "v_mfma_f32_16x16x32_bf16 v[72:75], v[232:235], v[108:111], v[72:75]\n\t"
    "v_cvt_pk_bf16_f32 v59, v62, v63\n\t"
    "v_mfma_f32_16x16x32_bf16 v[76:79], v[236:239], v[108:111], v[76:79]\n\t"
    "global_store_dwordx4 %[co1], v[56:59], s[36:37] offset:2048 sc1\n\t"
    "s_waitcnt lgkmcnt(4)\n\t"
    "v_mfma_f32_16x16x32_bf16 v[64:67], v[240:243], v[112:115], v[64:67]\n\t"
    "ds_read_b128 v[104:107], %[da] offset:0\n\t"
    "v_mad_u32_u24 v39, v39, %[mu], %[ad]\n\t"
    "v_mfma_f32_16x16x32_bf16 v[68:71], v[244:247], v[112:115], v[68:71]\n\t"
    "ds_read_b128 v[108:111], %[da] offset:2048\n\t"
    "v_cmp_ge_i32 vcc, %[lf], v39\n\t"
    "s_cmp_eq_u64 vcc, 0\n\t"
    "s_addc_u32 %[lf], %[lf], 0\n\t"
    "v_mfma_f32_16x16x32_bf16 v[72:75], v[240:243], v[116:119], v[72:75]\n\t"
    "v_mfma_f32_16x16x32_bf16 v[76:79], v[244:247], v[116:119], v[76:79]\n\t"
    "v_cmp_ge_i32 vcc, %[lf], v39\n\t"
    "s_cmp_eq_u64 vcc, 0\n\t"
    "s_addc_u32 %[lf], %[lf], 0\n\t"
    "s_waitcnt lgkmcnt(4)\n\t"
    "v_mfma_f32_16x16x32_bf16 v[80:83], v[248:251], v[120:123], v[64:67]\n\t"
    "ds_read_b128 v[112:115], %[ea] offset:4096\n\t"
    "s_min_i32 %[lf], %[lf], %[cp]\n\t"
    "s_add_u32 %[b], %[b], 1\n\t"
    "v_mfma_f32_16x16x32_bf16 v[84:87], v[252:255], v[120:123], v[68:71]\n\t"
    "ds_read_b128 v[116:119], %[ea] offset:6144\n\t"
    "v_mfma_f32_16x16x32_bf16 v[88:91], v[248:251], v[124:127], v[72:75]\n\t"
    "v_mfma_f32_16x16x32_bf16 v[92:95], v[252:255], v[124:127], v[76:79]\n\t"
    "s_mov_b64 s[36:37], s[38:39]\n\t"
    "s_add_u32 s38, s38, %[ab]\n\t"
    "s_addc_u32 s39, s39, 0\n\t"
    "s_sub_u32 m0, %[lf], %[b]\n\t"
    "s_sub_u32 m0, m0, 1\n\t"
    "s_cmp_lt_u32 m0, %[up]\n\t"
    "s_cbranch_scc1 1b\n\t"
    "s_waitcnt lgkmcnt(0)\n\t"
    "s_nop 4\n\t"
    "s_branch 9f\n\t"
    "5:\n\t"
    "s_add_u32 %[st], %[b], %[po]\n\t"
    "s_max_i32 %[st], %[st], %[p0]\n\t"
    "v_mov_b32 v39, %[st]\n\t"
    "v_mov_b32 v38, 0\n\t"
    "s_mov_b64 exec, 1\n\t"
    "global_store_dword v38, v39, %[pg] sc1\n\t"
    "s_mov_b64 exec, -1\n\t"
    "s_mov_b32 %[p0], %[p1]\n\t"
    "s_branch 6b\n\t"
    "7:\n\t"
    "s_cmp_ge_i32 %[lf], %[cp]\n\t"
    "s_cbranch_scc1 8b\n\t"
    "s_mov_b32 m0, %[sl]\n\t"
    "s_nop 0\n\t"
    "global_load_lds_dword %[dv], %[db] sc1\n\t"
    "s_branch 8b\n\t"
    "9:"
    : "=&{v[80:95]}"(pd), [ea] "+&v"(ea), [da] "+&v"(da), [b] "+&s"(b), [lf] "+&s"(lf), "+{s[36:37]}"(p1), "+{s[38:39]}"(p0), [sa] "+&v"(sa), [st] "=&s"(st), [sl] "+&s"(sl), [p0] "+&s"(pf)
    : [rw] "v"(rw), [ab] "s"(ab), [cp] "s"(cp), [mu] "s"(mu), [ad] "v"(ad), [up] "s"(up), "{v[128:159]}"(W[0]), "{v[160:191]}"(W[1]), "{v[192:223]}"(W[2]), "{v[224:255]}"(W[3]), [co0] "v"(co0), [w0] "s"(w0), [dv] "v"(dv), [db] "s"(db), [pg] "s"(pg), [po] "s"(po - 1), [p1] "s"(b + po), [co1] "v"(co1)
    : "v38", "v39", "v40", "v41", "v42", "v43", "v44", "v45", "v46", "v47", "v48", "v49", "v50", "v51", "v52", "v53", "v54", "v55", "v56", "v57", "v58", "v59", "v60", "v61", "v62", "v63", "v64", "v65", "v66", "v67", "v68", "v69", "v70", "v71", "v72", "v73", "v74", "v75", "v76", "v77", "v78", "v79", "v96", "v97", "v98", "v99", "v100", "v101", "v102", "v103", "v104", "v105", "v106", "v107", "v108", "v109", "v110", "v111", "v112", "v113", "v114", "v115", "v116", "v117", "v118", "v119", "v120", "v121", "v122", "v123", "v124", "v125", "v126", "v127", "vcc", "scc", "m0", "memory");
}
__device__ __forceinline__ void lean_ld0(const u32x32 (&W)[4], unsigned ea, unsigned da, unsigned x, unsigned lb, const char* s0, const char* s1, const unsigned (&av)[8], unsigned ps, unsigned dv, const char* db, unsigned rw, unsigned fa, unsigned fv,
    unsigned ga, unsigned mu, unsigned ad, int th, int tb, unsigned& sv, unsigned& su, unsigned& got) {
  asm volatile("s_setprio 2\n\t"
    "v_mov_b32 v76, %[ga]\n\t"
    "ds_read_b32 v76, v76 offset:24\n\t"
    "ds_read_b128 v[96:99], %[ea] offset:0\n\t"
    "ds_read_b128 v[100:103], %[ea] offset:2048\n\t"
    "ds_read_b128 v[104:107], %[da] offset:0\n\t"
    "ds_read_b128 v[108:111], %[da] offset:2048\n\t"
    "ds_read_b128 v[112:115], %[ea] offset:4096\n\t"
    "ds_read_b128 v[116:119], %[ea] offset:6144\n\t"
    "s_waitcnt lgkmcnt(6)\n\t"
    "v_cmp_ne_u32 vcc, 0, v76\n\t"
    "s_mov_b32 %[got], -1\n\t"
    "s_cbranch_vccnz 12f\n\t"
    "s_waitcnt lgkmcnt(4)\n\t"
    "v_mfma_f32_16x16x32_bf16 v[80:83], v[128:131], v[96:99], 0\n\t"
    "ds_read_b128 v[120:123], %[da] offset:4096\n\t"
    "v_mfma_f32_16x16x32_bf16 v[84:87], v[132:135], v[96:99], 0\n\t"
    "ds_read_b128 v[124:127], %[da] offset:6144\n\t"
    "v_mfma_f32_16x16x32_bf16 v[88:91], v[128:131], v[100:103], 0\n\t"
    "v_mfma_f32_16x16x32_bf16 v[92:95], v[132:135], v[100:103], 0\n\t"
    "s_waitcnt vmcnt(24)\n\t"
    "s_cmp_lt_u32 %[x], 2\n\t"
    "s_cbranch_scc0 41f\n\t"
    "s_waitcnt vmcnt(8)\n\t"
    "41:\n\t"
    "s_barrier\n\t"
    "s_waitcnt lgkmcnt(4)\n\t"
    "v_mfma_f32_16x16x32_bf16 v[80:83], v[136:139], v[104:107], v[80:83]\n\t"
    "ds_read_b128 v[96:99], %[ea] offset:8192\n\t"
    "v_mfma_f32_16x16x32_bf16 v[84:87], v[140:143], v[104:107], v[84:87]\n\t"
    "ds_read_b128 v[100:103], %[ea] offset:10240\n\t"
    "v_mfma_f32_16x16x32_bf16 v[88:91], v[136:139], v[108:111], v[88:91]\n\t"
    "v_mfma_f32_16x16x32_bf16 v[92:95], v[140:143], v[108:111], v[92:95]\n\t"
    "s_waitcnt lgkmcnt(4)\n\t"
    "v_mfma_f32_16x16x32_bf16 v[80:83], v[144:147], v[112:115], v[80:83]\n\t"
    "ds_read_b128 v[104:107], %[da] offset:8192\n\t"
    "v_mfma_f32_16x16x32_bf16 v[84:87], v[148:151], v[112:115], v[84:87]\n\t"
    "ds_read_b128 v[108:111], %[da] offset:10240\n\t"
    "v_mfma_f32_16x16x32_bf16 v[88:91], v[144:147], v[116:119], v[88:91]\n\t"
    "v_mfma_f32_16x16x32_bf16 v[92:95], v[148:151], v[116:119], v[92:95]\n\t"
    "s_waitcnt lgkmcnt(4)\n\t"
    "v_mfma_f32_16x16x32_bf16 v[80:83], v[152:155], v[120:123], v[80:83]\n\t"
    "ds_read_b128 v[112:115], %[ea] offset:12288\n\t"
    "v_mfma_f32_16x16x32_bf16 v[84:87], v[156:159], v[120:123], v[84:87]\n\t"
    "ds_read_b128 v[116:119], %[ea] offset:14336\n\t"
    "v_mfma_f32_16x16x32_bf16 v[88:91], v[152:155], v[124:127], v[88:91]\n\t"
    "v_mfma_f32_16x16x32_bf16 v[92:95], v[156:159], v[124:127], v[92:95]\n\t"
    "s_waitcnt lgkmcnt(4)\n\t"
    "v_mfma_f32_16x16x32_bf16 v[80:83], v[160:163], v[96:99], v[80:83]\n\t"
    "ds_read_b128 v[120:123], %[da] offset:12288\n\t"
    "v_mfma_f32_16x16x32_bf16 v[84:87], v[164:167], v[96:99], v[84:87]\n\t"
    "ds_read_b128 v[124:127], %[da] offset:14336\n\t"
    "v_mfma_f32_16x16x32_bf16 v[88:91], v[160:163], v[100:103], v[88:91]\n\t"
    "v_mfma_f32_16x16x32_bf16 v[92:95], v[164:167], v[100:103], v[92:95]\n\t"
    "s_waitcnt vmcnt(20)\n\t"
    "s_cmp_lt_u32 %[x], 2\n\t"
    "s_cbranch_scc0 45f\n\t"
    "s_waitcnt vmcnt(4)\n\t"
    "45:\n\t"
    "s_barrier\n\t"
    "s_waitcnt lgkmcnt(4)\n\t"
    "v_mfma_f32_16x16x32_bf16 v[80:83], v[168:171], v[104:107], v[80:83]\n\t"
    "ds_read_b128 v[96:99], %[ea] offset:16384\n\t"
    "v_mfma_f32_16x16x32_bf16 v[84:87], v[172:175], v[104:107], v[84:87]\n\t"
    "ds_read_b128 v[100:103], %[ea] offset:18432\n\t"
    "v_mfma_f32_16x16x32_bf16 v[88:91], v[168:171], v[108:111], v[88:91]\n\t"
    "v_mfma_f32_16x16x32_bf16 v[92:95], v[172:175], v[108:111], v[92:95]\n\t"
    "s_waitcnt lgkmcnt(4)\n\t"
    "v_mfma_f32_16x16x32_bf16 v[80:83], v[176:179], v[112:115], v[80:83]\n\t"
    "ds_read_b128 v[104:107], %[da] offset:16384\n\t"
    "v_mfma_f32_16x16x32_bf16 v[84:87], v[180:183], v[112:115], v[84:87]\n\t"
    "ds_read_b128 v[108:111], %[da] offset:18432\n\t"
    "v_mfma_f32_16x16x32_bf16 v[88:91], v[176:179], v[116:119], v[88:91]\n\t"
    "v_mfma_f32_16x16x32_bf16 v[92:95], v[180:183], v[116:119], v[92:95]\n\t"
    "s_waitcnt lgkmcnt(4)\n\t"
    "v_mfma_f32_16x16x32_bf16 v[80:83], v[184:187], v[120:123], v[80:83]\n\t"
    "ds_read_b128 v[112:115], %[ea] offset:20480\n\t"
    "v_mfma_f32_16x16x32_bf16 v[84:87], v[188:191], v[120:123], v[84:87]\n\t"
    "ds_read_b128 v[116:119], %[ea] offset:22528\n\t"
    "v_mfma_f32_16x16x32_bf16 v[88:91], v[184:187], v[124:127], v[88:91]\n\t"
    "v_mfma_f32_16x16x32_bf16 v[92:95], v[188:191], v[124:127], v[92:95]\n\t"
    "s_waitcnt lgkmcnt(4)\n\t"
    "v_mfma_f32_16x16x32_bf16 v[80:83], v[192:195], v[96:99], v[80:83]\n\t"
    "ds_read_b128 v[120:123], %[da] offset:20480\n\t"
    "v_mfma_f32_16x16x32_bf16 v[84:87], v[196:199], v[96:99], v[84:87]\n\t"
    "ds_read_b128 v[124:127], %[da] offset:22528\n\t"
    "v_mfma_f32_16x16x32_bf16 v[88:91], v[192:195], v[100:103], v[88:91]\n\t"
    "v_mfma_f32_16x16x32_bf16 v[92:95], v[196:199], v[100:103], v[92:95]\n\t"
    "s_waitcnt vmcnt(16)\n\t"
    "s_cmp_lt_u32 %[x], 2\n\t"
    "s_cbranch_scc0 49f\n\t"
    "s_waitcnt vmcnt(0)\n\t"
    "49:\n\t"
    "s_barrier\n\t"
    "s_waitcnt lgkmcnt(4)\n\t"
    "v_mfma_f32_16x16x32_bf16 v[80:83], v[200:203], v[104:107], v[80:83]\n\t"
    "ds_read_b128 v[96:99], %[ea] offset:24576\n\t"
    "v_mfma_f32_16x16x32_bf16 v[84:87], v[204:207], v[104:107], v[84:87]\n\t"
    "ds_read_b128 v[100:103], %[ea] offset:26624\n\t"
    "v_mfma_f32_16x16x32_bf16 v[88:91], v[200:203], v[108:111], v[88:91]\n\t"
    "v_mfma_f32_16x16x32_bf16 v[92:95], v[204:207], v[108:111], v[92:95]\n\t"
    "s_waitcnt lgkmcnt(4)\n\t"
    "v_mfma_f32_16x16x32_bf16 v[80:83], v[208:211], v[112:115], v[80:83]\n\t"
    "ds_read_b128 v[104:107], %[da] offset:24576\n\t"
    "v_mfma_f32_16x16x32_bf16 v[84:87], v[212:215], v[112:115], v[84:87]\n\t"
    "ds_read_b128 v[108:111], %[da] offset:26624\n\t"
    "v_mfma_f32_16x16x32_bf16 v[88:91], v[208:211], v[116:119], v[88:91]\n\t"
    "v_mfma_f32_16x16x32_bf16 v[92:95], v[212:215], v[116:119], v[92:95]\n\t"
    "s_cmp_eq_u32 %[ps], 0\n\t"
    "s_cbranch_scc1 10f\n\t"
    "global_load_dword %[sv], %[dv], %[db] sc1\n\t"
    "global_load_dword %[su], %[dv], %[db] offset:64 sc1\n\t"
    "10:\n\t"
    "s_waitcnt lgkmcnt(4)\n\t"
    "v_mfma_f32_16x16x32_bf16 v[80:83], v[216:219], v[120:123], v[80:83]\n\t"
    "ds_read_b128 v[112:115], %[ea] offset:28672\n\t"
    "v_mfma_f32_16x16x32_bf16 v[84:87], v[220:223], v[120:123], v[84:87]\n\t"
    "ds_read_b128 v[116:119], %[ea] offset:30720\n\t"
    "v_mfma_f32_16x16x32_bf16 v[88:91], v[216:219], v[124:127], v[88:91]\n\t"
    "v_mfma_f32_16x16x32_bf16 v[92:95], v[220:223], v[124:127], v[92:95]\n\t"
    "s_waitcnt lgkmcnt(4)\n\t"
    "v_mfma_f32_16x16x32_bf16 v[80:83], v[224:227], v[96:99], v[80:83]\n\t"
    "ds_read_b128 v[120:123], %[da] offset:28672\n\t"
    "v_mfma_f32_16x16x32_bf16 v[84:87], v[228:231], v[96:99], v[84:87]\n\t"
    "ds_read_b128 v[124:127], %[da] offset:30720\n\t"
    "v_mfma_f32_16x16x32_bf16 v[88:91], v[224:227], v[100:103], v[88:91]\n\t"
    "v_mfma_f32_16x16x32_bf16 v[92:95], v[228:231], v[100:103], v[92:95]\n\t"
    "s_waitcnt lgkmcnt(4)\n\t"
    "v_mfma_f32_16x16x32_bf16 v[80:83], v[232:235], v[104:107], v[80:83]\n\t"
    "v_mfma_f32_16x16x32_bf16 v[84:87], v[236:239], v[104:107], v[84:87]\n\t"
    "v_mfma_f32_16x16x32_bf16 v[88:91], v[232:235], v[108:111], v[88:91]\n\t"
    "v_mfma_f32_16x16x32_bf16 v[92:95], v[236:239], v[108:111], v[92:95]\n\t"
    "s_waitcnt lgkmcnt(2)\n\t"
    "v_mfma_f32_16x16x32_bf16 v[80:83], v[240:243], v[112:115], v[80:83]\n\t"
    "v_mfma_f32_16x16x32_bf16 v[84:87], v[244:247], v[112:115], v[84:87]\n\t"
    "v_mfma_f32_16x16x32_bf16 v[88:91], v[240:243], v[116:119], v[88:91]\n\t"
    "v_mfma_f32_16x16x32_bf16 v[92:95], v[244:247], v[116:119], v[92:95]\n\t"
    "s_waitcnt lgkmcnt(0)\n\t"
    "v_mfma_f32_16x16x32_bf16 v[80:83], v[248:251], v[120:123], v[80:83]\n\t"
    "s_memrealtime s[36:37]\n\t"
    "v_mfma_f32_16x16x32_bf16 v[84:87], v[252:255], v[120:123], v[84:87]\n\t"
    "v_mfma_f32_16x16x32_bf16 v[88:91], v[248:251], v[124:127], v[88:91]\n\t"
    "v_mfma_f32_16x16x32_bf16 v[92:95], v[252:255], v[124:127], v[92:95]\n\t"
    "s_mov_b32 %[got], 0\n\t"
    "v_mov_b32 v76, %[fa]\n\t"
    "v_mov_b32 v77, %[fv]\n\t"
    "v_add_u32 v78, 1, v77\n\t"
    "v_mov_b32 v79, %[ga]\n\t"
    "s_waitcnt lgkmcnt(0)\n\t"
    "ds_write_b128 %[rw], v[80:83] offset:0\n\t"
    "ds_write_b128 %[rw], v[84:87] offset:1024\n\t"
    "ds_write_b128 %[rw], v[88:91] offset:2048\n\t"
    "ds_write_b128 %[rw], v[92:95] offset:3072\n\t"
    "ds_write_b32 v76, v77\n\t"
    "s_cmp_eq_u32 %[ps], 0\n\t"
    "s_cbranch_scc1 9f\n\t"
    "s_waitcnt vmcnt(0)\n\t"
    "v_mad_u32_u24 v76, %[sv], %[mu], %[ad]\n\t"
    "v_cmp_ge_i32 vcc, %[th], v76\n\t"
    "s_cbranch_vccz 11f\n\t"
    "s_sub_u32 s36, s36, 20\n\t"
    "v_sub_u32 v76, s36, %[su]\n\t"
    "v_lshrrev_b32 v77, 24, %[su]\n\t"
    "v_lshlrev_b32 v76, 8, v76\n\t"
    "v_ashrrev_i32 v76, 31, v76\n\t"
    "v_add_u32 v77, v77, v76\n\t"
    "v_cmp_lt_i32 s[36:37], %[tb], v77\n\t"
    "s_andn2_b64 vcc, vcc, s[36:37]\n\t"
    "s_cbranch_scc1 9f\n\t"
    "11:\n\t"
    "ds_write2_b32 v79, v78, v78 offset1:1\n\t"
    "s_add_u32 m0, %[lb], 0\n\t"
    "s_nop 0\n\t"
    "global_load_lds_dwordx4 %[av0], %[s0] sc1\n\t"
    "s_add_u32 m0, %[lb], 32768\n\t"
    "s_nop 0\n\t"
    "global_load_lds_dwordx4 %[av1], %[s0] sc1\n\t"
    "s_add_u32 m0, %[lb], 4096\n\t"
    "s_nop 0\n\t"
    "global_load_lds_dwordx4 %[av2], %[s0] sc1\n\t"
    "s_add_u32 m0, %[lb], 36864\n\t"
    "s_nop 0\n\t"
    "global_load_lds_dwordx4 %[av3], %[s0] sc1\n\t"
    "s_add_u32 m0, %[lb], 8192\n\t"
    "s_nop 0\n\t"
    "global_load_lds_dwordx4 %[av4], %[s0] sc1\n\t"
    "s_add_u32 m0, %[lb], 40960\n\t"
    "s_nop 0\n\t"
    "global_load_lds_dwordx4 %[av5], %[s0] sc1\n\t"
    "s_add_u32 m0, %[lb], 12288\n\t"
    "s_nop 0\n\t"
    "global_load_lds_dwordx4 %[av6], %[s0] sc1\n\t"
    "s_add_u32 m0, %[lb], 45056\n\t"
    "s_nop 0\n\t"
    "global_load_lds_dwordx4 %[av7], %[s0] sc1\n\t"
    "s_add_u32 m0, %[lb], 16384\n\t"
    "s_nop 0\n\t"
    "global_load_lds_dwordx4 %[av0], %[s1] sc1\n\t"
    "s_add_u32 m0, %[lb], 49152\n\t"
    "s_nop 0\n\t"
    "global_load_lds_dwordx4 %[av1], %[s1] sc1\n\t"
    "s_add_u32 m0, %[lb], 20480\n\t"
    "s_nop 0\n\t"
    "global_load_lds_dwordx4 %[av2], %[s1] sc1\n\t"
    "s_add_u32 m0, %[lb], 53248\n\t"
    "s_nop 0\n\t"
    "global_load_lds_dwordx4 %[av3], %[s1] sc1\n\t"
    "s_add_u32 m0, %[lb], 24576\n\t"
    "s_nop 0\n\t"
    "global_load_lds_dwordx4 %[av4], %[s1] sc1\n\t"
    "s_add_u32 m0, %[lb], 57344\n\t"
    "s_nop 0\n\t"
    "global_load_lds_dwordx4 %[av5], %[s1] sc1\n\t"
    "s_add_u32 m0, %[lb], 28672\n\t"
    "s_nop 0\n\t"
    "global_load_lds_dwordx4 %[av6], %[s1] sc1\n\t"
    "s_add_u32 m0, %[lb], 61440\n\t"
    "s_nop 0\n\t"
    "global_load_lds_dwordx4 %[av7], %[s1] sc1\n\t"
    "s_mov_b32 %[got], 1\n\t"
    "s_branch 9f\n\t"
    "12:\n\t"
    "s_waitcnt lgkmcnt(0)\n\t"
    "9:"
    : [sv] "+&v"(sv), [su] "+&v"(su), [got] "=&s"(got)
    : [ea] "v"(ea), [da] "v"(da), [x] "s"(x), "{v[128:159]}"(W[0]), "{v[160:191]}"(W[1]), "{v[192:223]}"(W[2]), "{v[224:255]}"(W[3]), [lb] "s"(lb), [s0] "s"(s0), [s1] "s"(s1), [av0] "v"(av[0]), [av1] "v"(av[1]), [av2] "v"(av[2]), [av3] "v"(av[3]), [av4] "v"(av[4]), [av5] "v"(av[5]), [av6] "v"(av[6]), [av7] "v"(av[7]), [ps] "s"(ps), [dv] "v"(dv), [db] "s"(db), [rw] "v"(rw), [fa] "s"(fa), [fv] "s"(fv), [ga] "s"(ga), [mu] "s"(mu), [ad] "v"(ad), [th] "s"(th), [tb] "s"(tb)
    : "v96", "v97", "v98", "v99", "v100", "v101", "v102", "v103", "v104", "v105", "v106", "v107", "v108", "v109", "v110", "v111", "v112", "v113", "v114", "v115", "v116", "v117", "v118", "v119", "v120", "v121", "v122", "v123", "v124", "v125", "v126", "v127", "v76", "v77", "v78", "v79", "v80", "v81", "v82", "v83", "v84", "v85", "v86", "v87", "v88", "v89", "v90", "v91", "v92", "v93", "v94", "v95", "vcc", "scc", "m0", "memory", "s36", "s37", "s38", "s39");
}
__device__ __forceinline__ void lean_dm0(const u32x32 (&W)[4], unsigned ea, unsigned da, unsigned x, unsigned lb, const char* s0, const char* s1, const unsigned (&av)[8], unsigned ps, unsigned dv, const char* db, unsigned rw, unsigned fa, unsigned fv,
    unsigned ga, unsigned mu, unsigned ad, int th, int tb, unsigned& sv, unsigned& su, unsigned& got) {
  asm volatile("s_setprio 2\n\t"
    "v_mov_b32 v76, %[ga]\n\t"
    "ds_read_b32 v76, v76 offset:24\n\t"
    "ds_read_b128 v[96:99], %[ea] offset:0\n\t"
    "ds_read_b128 v[100:103], %[ea] offset:2048\n\t"
    "ds_read_b128 v[104:107], %[da] offset:0\n\t"
    "ds_read_b128 v[108:111], %[da] offset:2048\n\t"
    "ds_read_b128 v[112:115], %[ea] offset:4096\n\t"
    "ds_read_b128 v[116:119], %[ea] offset:6144\n\t"
    "s_waitcnt lgkmcnt(6)\n\t"
    "v_cmp_ne_u32 vcc, 0, v76\n\t"
    "s_mov_b32 %[got], -1\n\t"
    "s_cbranch_vccnz 12f\n\t"
    "s_waitcnt lgkmcnt(4)\n\t"
    "v_mfma_f32_16x16x32_bf16 v[80:83], v[128:131], v[96:99], 0\n\t"
    "ds_read_b128 v[120:123], %[da] offset:4096\n\t"
    "s_add_u32 m0, %[lb], 0\n\t"
    "v_mfma_f32_16x16x32_bf16 v[84:87], v[132:135], v[96:99], 0\n\t"
    "ds_read_b128 v[124:127], %[da] offset:6144\n\t"
    "global_load_lds_dwordx4 %[av0], %[s0] sc1\n\t"
    "v_mfma_f32_16x16x32_bf16 v[88:91], v[128:131], v[100:103], 0\n\t"
    "s_add_u32 m0, %[lb], 32768\n\t"
    "v_mfma_f32_16x16x32_bf16 v[92:95], v[132:135], v[100:103], 0\n\t"
    "global_load_lds_dwordx4 %[av1], %[s0] sc1\n\t"
    "s_waitcnt vmcnt(10)\n\t"
    "s_barrier\n\t"
    "s_waitcnt lgkmcnt(4)\n\t"
    "v_mfma_f32_16x16x32_bf16 v[80:83], v[136:139], v[104:107], v[80:83]\n\t"
    "ds_read_b128 v[96:99], %[ea] offset:8192\n\t"
    "s_add_u32 m0, %[lb], 4096\n\t"
    "v_mfma_f32_16x16x32_bf16 v[84:87], v[140:143], v[104:107], v[84:87]\n\t"
    "ds_read_b128 v[100:103], %[ea] offset:10240\n\t"
    "global_load_lds_dwordx4 %[av2], %[s0] sc1\n\t"
    "v_mfma_f32_16x16x32_bf16 v[88:91], v[136:139], v[108:111], v[88:91]\n\t"
    "s_add_u32 m0, %[lb], 36864\n\t"
    "v_mfma_f32_16x16x32_bf16 v[92:95], v[140:143], v[108:111], v[92:95]\n\t"
    "global_load_lds_dwordx4 %[av3], %[s0] sc1\n\t"
    "s_waitcnt lgkmcnt(4)\n\t"
    "v_mfma_f32_16x16x32_bf16 v[80:83], v[144:147], v[112:115], v[80:83]\n\t"
    "ds_read_b128 v[104:107], %[da] offset:8192\n\t"
    "s_add_u32 m0, %[lb], 8192\n\t"
    "v_mfma_f32_16x16x32_bf16 v[84:87], v[148:151], v[112:115], v[84:87]\n\t"
    "ds_read_b128 v[108:111], %[da] offset:10240\n\t"
    "global_load_lds_dwordx4 %[av4], %[s0] sc1\n\t"
    "v_mfma_f32_16x16x32_bf16 v[88:91], v[144:147], v[116:119], v[88:91]\n\t"
    "s_add_u32 m0, %[lb], 40960\n\t"
    "v_mfma_f32_16x16x32_bf16 v[92:95], v[148:151], v[116:119], v[92:95]\n\t"
    "global_load_lds_dwordx4 %[av5], %[s0] sc1\n\t"
    "s_waitcnt lgkmcnt(4)\n\t"
    "v_mfma_f32_16x16x32_bf16 v[80:83], v[152:155], v[120:123], v[80:83]\n\t"
    "ds_read_b128 v[112:115], %[ea] offset:12288\n\t"
    "s_add_u32 m0, %[lb], 12288\n\t"
    "v_mfma_f32_16x16x32_bf16 v[84:87], v[156:159], v[120:123], v[84:87]\n\t"
    "ds_read_b128 v[116:119], %[ea] offset:14336\n\t"
    "global_load_lds_dwordx4 %[av6], %[s0] sc1\n\t"
    "v_mfma_f32_16x16x32_bf16 v[88:91], v[152:155], v[124:127], v[88:91]\n\t"
    "s_add_u32 m0, %[lb], 45056\n\t"
    "v_mfma_f32_16x16x32_bf16 v[92:95], v[156:159], v[124:127], v[92:95]\n\t"
    "global_load_lds_dwordx4 %[av7], %[s0] sc1\n\t"
    "s_waitcnt lgkmcnt(4)\n\t"
    "v_mfma_f32_16x16x32_bf16 v[80:83], v[160:163], v[96:99], v[80:83]\n\t"
    "ds_read_b128 v[120:123], %[da] offset:12288\n\t"
    "s_add_u32 m0, %[lb], 16384\n\t"
    "v_mfma_f32_16x16x32_bf16 v[84:87], v[164:167], v[96:99], v[84:87]\n\t"
    "ds_read_b128 v[124:127], %[da] offset:14336\n\t"
    "global_load_lds_dwordx4 %[av0], %[s1] sc1\n\t"
    "v_mfma_f32_16x16x32_bf16 v[88:91], v[160:163], v[100:103], v[88:91]\n\t"
    "s_add_u32 m0, %[lb], 49152\n\t"
    "v_mfma_f32_16x16x32_bf16 v[92:95], v[164:167], v[100:103], v[92:95]\n\t"
    "global_load_lds_dwordx4 %[av1], %[s1] sc1\n\t"
    "s_waitcnt vmcnt(14)\n\t"
    "s_barrier\n\t"
    "s_waitcnt lgkmcnt(4)\n\t"
    "v_mfma_f32_16x16x32_bf16 v[80:83], v[168:171], v[104:107], v[80:83]\n\t"
    "ds_read_b128 v[96:99], %[ea] offset:16384\n\t"
    "s_add_u32 m0, %[lb], 20480\n\t"
    "v_mfma_f32_16x16x32_bf16 v[84:87], v[172:175], v[104:107], v[84:87]\n\t"
    "ds_read_b128 v[100:103], %[ea] offset:18432\n\t"
    "global_load_lds_dwordx4 %[av2], %[s1] sc1\n\t"
    "v_mfma_f32_16x16x32_bf16 v[88:91], v[168:171], v[108:111], v[88:91]\n\t"
    "s_add_u32 m0, %[lb], 53248\n\t"
    "v_mfma_f32_16x16x32_bf16 v[92:95], v[172:175], v[108:111], v[92:95]\n\t"
    "global_load_lds_dwordx4 %[av3], %[s1] sc1\n\t"
    "s_waitcnt lgkmcnt(4)\n\t"
    "v_mfma_f32_16x16x32_bf16 v[80:83], v[176:179], v[112:115], v[80:83]\n\t"
    "ds_read_b128 v[104:107], %[da] offset:16384\n\t"
    "s_add_u32 m0, %[lb], 24576\n\t"
    "v_mfma_f32_16x16x32_bf16 v[84:87], v[180:183], v[112:115], v[84:87]\n\t"
    "ds_read_b128 v[108:111], %[da] offset:18432\n\t"
    "global_load_lds_dwordx4 %[av4], %[s1] sc1\n\t"
    "v_mfma_f32_16x16x32_bf16 v[88:91], v[176:179], v[116:119], v[88:91]\n\t"
    "s_add_u32 m0, %[lb], 57344\n\t"
    "v_mfma_f32_16x16x32_bf16 v[92:95], v[180:183], v[116:119], v[92:95]\n\t"
    "global_load_lds_dwordx4 %[av5], %[s1] sc1\n\t"
    "s_waitcnt lgkmcnt(4)\n\t"
    "v_mfma_f32_16x16x32_bf16 v[80:83], v[184:187], v[120:123], v[80:83]\n\t"
    "ds_read_b128 v[112:115], %[ea] offset:20480\n\t"
    "s_add_u32 m0, %[lb], 28672\n\t"
    "v_mfma_f32_16x16x32_bf16 v[84:87], v[188:191], v[120:123], v[84:87]\n\t"
    "ds_read_b128 v[116:119], %[ea] offset:22528\n\t"
    "global_load_lds_dwordx4 %[av6], %[s1] sc1\n\t"
    "v_mfma_f32_16x16x32_bf16 v[88:91], v[184:187], v[124:127], v[88:91]\n\t"
    "s_add_u32 m0, %[lb], 61440\n\t"
    "v_mfma_f32_16x16x32_bf16 v[92:95], v[188:191], v[124:127], v[92:95]\n\t"
    "global_load_lds_dwordx4 %[av7], %[s1] sc1\n\t"
    "s_waitcnt lgkmcnt(4)\n\t"
    "v_mfma_f32_16x16x32_bf16 v[80:83], v[192:195], v[96:99], v[80:83]\n\t"
    "ds_read_b128 v[120:123], %[da] offset:20480\n\t"
    "v_mfma_f32_16x16x32_bf16 v[84:87], v[196:199], v[96:99], v[84:87]\n\t"
    "ds_read_b128 v[124:127], %[da] offset:22528\n\t"
    "v_mfma_f32_16x16x32_bf16 v[88:91], v[192:195], v[100:103], v[88:91]\n\t"
    "v_mfma_f32_16x16x32_bf16 v[92:95], v[196:199], v[100:103], v[92:95]\n\t"
    "s_waitcnt vmcnt(16)\n\t"
    "s_barrier\n\t"
    "s_waitcnt lgkmcnt(4)\n\t"
    "v_mfma_f32_16x16x32_bf16 v[80:83], v[200:203], v[104:107], v[80:83]\n\t"
    "ds_read_b128 v[96:99], %[ea] offset:24576\n\t"
    "v_mfma_f32_16x16x32_bf16 v[84:87], v[204:207], v[104:107], v[84:87]\n\t"
    "ds_read_b128 v[100:103], %[ea] offset:26624\n\t"
    "v_mfma_f32_16x16x32_bf16 v[88:91], v[200:203], v[108:111], v[88:91]\n\t"
    "v_mfma_f32_16x16x32_bf16 v[92:95], v[204:207], v[108:111], v[92:95]\n\t"
    "s_waitcnt lgkmcnt(4)\n\t"
    "v_mfma_f32_16x16x32_bf16 v[80:83], v[208:211], v[112:115], v[80:83]\n\t"
    "ds_read_b128 v[104:107], %[da] offset:24576\n\t"
    "v_mfma_f32_16x16x32_bf16 v[84:87], v[212:215], v[112:115], v[84:87]\n\t"
    "ds_read_b128 v[108:111], %[da] offset:26624\n\t"
    "v_mfma_f32_16x16x32_bf16 v[88:91], v[208:211], v[116:119], v[88:91]\n\t"
    "v_mfma_f32_16x16x32_bf16 v[92:95], v[212:215], v[116:119], v[92:95]\n\t"
    "s_waitcnt lgkmcnt(4)\n\t"
    "v_mfma_f32_16x16x32_bf16 v[80:83], v[216:219], v[120:123], v[80:83]\n\t"
    "ds_read_b128 v[112:115], %[ea] offset:28672\n\t"
    "v_mfma_f32_16x16x32_bf16 v[84:87], v[220:223], v[120:123], v[84:87]\n\t"
    "ds_read_b128 v[116:119], %[ea] offset:30720\n\t"
    "v_mfma_f32_16x16x32_bf16 v[88:91], v[216:219], v[124:127], v[88:91]\n\t"
    "v_mfma_f32_16x16x32_bf16 v[92:95], v[220:223], v[124:127], v[92:95]\n\t"
    "s_waitcnt lgkmcnt(4)\n\t"
    "v_mfma_f32_16x16x32_bf16 v[80:83], v[224:227], v[96:99], v[80:83]\n\t"
    "ds_read_b128 v[120:123], %[da] offset:28672\n\t"
    "v_mfma_f32_16x16x32_bf16 v[84:87], v[228:231], v[96:99], v[84:87]\n\t"
    "ds_read_b128 v[124:127], %[da] offset:30720\n\t"
    "v_mfma_f32_16x16x32_bf16 v[88:91], v[224:227], v[100:103], v[88:91]\n\t"
    "v_mfma_f32_16x16x32_bf16 v[92:95], v[228:231], v[100:103], v[92:95]\n\t"
    "s_waitcnt lgkmcnt(4)\n\t"
    "v_mfma_f32_16x16x32_bf16 v[80:83], v[232:235], v[104:107], v[80:83]\n\t"
    "v_mfma_f32_16x16x32_bf16 v[84:87], v[236:239], v[104:107], v[84:87]\n\t"
    "v_mfma_f32_16x16x32_bf16 v[88:91], v[232:235], v[108:111], v[88:91]\n\t"
    "v_mfma_f32_16x16x32_bf16 v[92:95], v[236:239], v[108:111], v[92:95]\n\t"
    "s_waitcnt lgkmcnt(2)\n\t"
    "v_mfma_f32_16x16x32_bf16 v[80:83], v[240:243], v[112:115], v[80:83]\n\t"
    "v_mfma_f32_16x16x32_bf16 v[84:87], v[244:247], v[112:115], v[84:87]\n\t"
    "v_mfma_f32_16x16x32_bf16 v[88:91], v[240:243], v[116:119], v[88:91]\n\t"
    "v_mfma_f32_16x16x32_bf16 v[92:95], v[244:247], v[116:119], v[92:95]\n\t"
    "s_waitcnt lgkmcnt(0)\n\t"
    "v_mfma_f32_16x16x32_bf16 v[80:83], v[248:251], v[120:123], v[80:83]\n\t"
    "v_mfma_f32_16x16x32_bf16 v[84:87], v[252:255], v[120:123], v[84:87]\n\t"
    "v_mfma_f32_16x16x32_bf16 v[88:91], v[248:251], v[124:127], v[88:91]\n\t"
    "v_mfma_f32_16x16x32_bf16 v[92:95], v[252:255], v[124:127], v[92:95]\n\t"
    "s_cmp_eq_u32 %[ps], 0\n\t"
    "s_cbranch_scc1 10f\n\t"
    "global_load_dword %[sv], %[dv], %[db] sc1\n\t"
    "global_load_dword %[su], %[dv], %[db] offset:64 sc1\n\t"
    "10:\n\t"
    "s_mov_b32 %[got], 0\n\t"
    "v_mov_b32 v76, %[fa]\n\t"
    "v_mov_b32 v77, %[fv]\n\t"
    "v_add_u32 v78, 1, v77\n\t"
    "v_mov_b32 v79, %[ga]\n\t"
    "ds_write_b128 %[rw], v[80:83] offset:0\n\t"
    "ds_write_b128 %[rw], v[84:87] offset:1024\n\t"
    "ds_write_b128 %[rw], v[88:91] offset:2048\n\t"
    "ds_write_b128 %[rw], v[92:95] offset:3072\n\t"
    "ds_write_b32 v76, v77\n\t"
    "s_cmp_eq_u32 %[ps], 0\n\t"
    "s_cbranch_scc1 9f\n\t"
    "s_waitcnt vmcnt(0)\n\t"
    "s_branch 9f\n\t"
    "12:\n\t"
    "s_waitcnt lgkmcnt(0)\n\t"
    "9:"
    : [sv] "+&v"(sv), [su] "+&v"(su), [got] "=&s"(got)
    : [ea] "v"(ea), [da] "v"(da), [x] "s"(x), "{v[128:159]}"(W[0]), "{v[160:191]}"(W[1]), "{v[192:223]}"(W[2]), "{v[224:255]}"(W[3]), [lb] "s"(lb), [s0] "s"(s0), [s1] "s"(s1), [av0] "v"(av[0]), [av1] "v"(av[1]), [av2] "v"(av[2]), [av3] "v"(av[3]), [av4] "v"(av[4]), [av5] "v"(av[5]), [av6] "v"(av[6]), [av7] "v"(av[7]), [ps] "s"(ps), [dv] "v"(dv), [db] "s"(db), [rw] "v"(rw), [fa] "s"(fa), [fv] "s"(fv), [ga] "s"(ga), [mu] "s"(mu), [ad] "v"(ad), [th] "s"(th), [tb] "s"(tb)
    : "v96", "v97", "v98", "v99", "v100", "v101", "v102", "v103", "v104", "v105", "v106", "v107", "v108", "v109", "v110", "v111", "v112", "v113", "v114", "v115", "v116", "v117", "v118", "v119", "v120", "v121", "v122", "v123", "v124", "v125", "v126", "v127", "v76", "v77", "v78", "v79", "v80", "v81", "v82", "v83", "v84", "v85", "v86", "v87", "v88", "v89", "v90", "v91", "v92", "v93", "v94", "v95", "vcc", "scc", "m0", "memory");
}
__device__ __forceinline__ void lean_ld1(const u32x32 (&W)[4], unsigned ea, unsigned da, unsigned x, unsigned lb, const char* s0, const char* s1, const unsigned (&av)[8], unsigned ps, unsigned dv, const char* db, unsigned rw, unsigned fa, unsigned fv,
    unsigned ga, unsigned mu, unsigned ad, int th, int tb, unsigned& sv, unsigned& su, unsigned& got) {
  asm volatile("s_setprio 2\n\t"
    "v_mov_b32 v76, %[ga]\n\t"
    "ds_read_b32 v76, v76 offset:24\n\t"
    "ds_read_b128 v[96:99], %[ea] offset:0\n\t"
    "ds_read_b128 v[100:103], %[ea] offset:2048\n\t"
    "ds_read_b128 v[104:107], %[da] offset:0\n\t"
    "ds_read_b128 v[108:111], %[da] offset:2048\n\t"
    "ds_read_b128 v[112:115], %[ea] offset:4096\n\t"
    "ds_read_b128 v[116:119], %[ea] offset:6144\n\t"
    "s_waitcnt lgkmcnt(6)\n\t"
    "v_cmp_ne_u32 vcc, 0, v76\n\t"
    "s_mov_b32 %[got], -1\n\t"
    "s_cbranch_vccnz 12f\n\t"
    "s_waitcnt lgkmcnt(4)\n\t"
    "v_mfma_f32_16x16x32_bf16 v[80:83], v[128:131], v[96:99], 0\n\t"
    "ds_read_b128 v[120:123], %[da] offset:4096\n\t"
    "v_mfma_f32_16x16x32_bf16 v[84:87], v[132:135], v[96:99], 0\n\t"
    "ds_read_b128 v[124:127], %[da] offset:6144\n\t"
    "v_mfma_f32_16x16x32_bf16 v[88:91], v[128:131], v[100:103], 0\n\t"
    "v_mfma_f32_16x16x32_bf16 v[92:95], v[132:135], v[100:103], 0\n\t"
    "s_waitcnt vmcnt(12)\n\t"
    "s_cmp_lt_u32 %[x], 2\n\t"
    "s_cbranch_scc0 41f\n\t"
    "s_waitcnt vmcnt(4)\n\t"
    "41:\n\t"
    "s_barrier\n\t"
    "s_waitcnt lgkmcnt(4)\n\t"
    "v_mfma_f32_16x16x32_bf16 v[80:83], v[136:139], v[104:107], v[80:83]\n\t"
    "ds_read_b128 v[96:99], %[ea] offset:8192\n\t"
    "v_mfma_f32_16x16x32_bf16 v[84:87], v[140:143], v[104:107], v[84:87]\n\t"
    "ds_read_b128 v[100:103], %[ea] offset:10240\n\t"
    "v_mfma_f32_16x16x32_bf16 v[88:91], v[136:139], v[108:111], v[88:91]\n\t"
    "v_mfma_f32_16x16x32_bf16 v[92:95], v[140:143], v[108:111], v[92:95]\n\t"
    "s_waitcnt lgkmcnt(4)\n\t"
    "v_mfma_f32_16x16x32_bf16 v[80:83], v[144:147], v[112:115], v[80:83]\n\t"
    "ds_read_b128 v[104:107], %[da] offset:8192\n\t"
    "v_mfma_f32_16x16x32_bf16 v[84:87], v[148:151], v[112:115], v[84:87]\n\t"
    "ds_read_b128 v[108:111], %[da] offset:10240\n\t"
    "v_mfma_f32_16x16x32_bf16 v[88:91], v[144:147], v[116:119], v[88:91]\n\t"
    "v_mfma_f32_16x16x32_bf16 v[92:95], v[148:151], v[116:119], v[92:95]\n\t"
    "s_waitcnt lgkmcnt(4)\n\t"
    "v_mfma_f32_16x16x32_bf16 v[80:83], v[152:155], v[120:123], v[80:83]\n\t"
    "ds_read_b128 v[112:115], %[ea] offset:12288\n\t"
    "v_mfma_f32_16x16x32_bf16 v[84:87], v[156:159], v[120:123], v[84:87]\n\t"
    "ds_read_b128 v[116:119], %[ea] offset:14336\n\t"
    "v_mfma_f32_16x16x32_bf16 v[88:91], v[152:155], v[124:127], v[88:91]\n\t"
    "v_mfma_f32_16x16x32_bf16 v[92:95], v[156:159], v[124:127], v[92:95]\n\t"
    "s_waitcnt lgkmcnt(4)\n\t"
    "v_mfma_f32_16x16x32_bf16 v[80:83], v[160:163], v[96:99], v[80:83]\n\t"
    "ds_read_b128 v[120:123], %[da] offset:12288\n\t"
    "v_mfma_f32_16x16x32_bf16 v[84:87], v[164:167], v[96:99], v[84:87]\n\t"
    "ds_read_b128 v[124:127], %[da] offset:14336\n\t"
    "v_mfma_f32_16x16x32_bf16 v[88:91], v[160:163], v[100:103], v[88:91]\n\t"
    "v_mfma_f32_16x16x32_bf16 v[92:95], v[164:167], v[100:103], v[92:95]\n\t"
    "s_waitcnt vmcnt(10)\n\t"
    "s_cmp_lt_u32 %[x], 2\n\t"
    "s_cbranch_scc0 45f\n\t"
    "s_waitcnt vmcnt(2)\n\t"
    "45:\n\t"
    "s_barrier\n\t"
    "s_waitcnt lgkmcnt(4)\n\t"
    "v_mfma_f32_16x16x32_bf16 v[80:83], v[168:171], v[104:107], v[80:83]\n\t"
    "ds_read_b128 v[96:99], %[ea] offset:16384\n\t"
    "v_mfma_f32_16x16x32_bf16 v[84:87], v[172:175], v[104:107], v[84:87]\n\t"
    "ds_read_b128 v[100:103], %[ea] offset:18432\n\t"
    "v_mfma_f32_16x16x32_bf16 v[88:91], v[168:171], v[108:111], v[88:91]\n\t"
    "v_mfma_f32_16x16x32_bf16 v[92:95], v[172:175], v[108:111], v[92:95]\n\t"
    "s_waitcnt lgkmcnt(4)\n\t"
    "v_mfma_f32_16x16x32_bf16 v[80:83], v[176:179], v[112:115], v[80:83]\n\t"
    "ds_read_b128 v[104:107], %[da] offset:16384\n\t"
    "v_mfma_f32_16x16x32_bf16 v[84:87], v[180:183], v[112:115], v[84:87]\n\t"
    "ds_read_b128 v[108:111], %[da] offset:18432\n\t"
    "v_mfma_f32_16x16x32_bf16 v[88:91], v[176:179], v[116:119], v[88:91]\n\t"
    "v_mfma_f32_16x16x32_bf16 v[92:95], v[180:183], v[116:119], v[92:95]\n\t"
    "s_waitcnt lgkmcnt(4)\n\t"
    "v_mfma_f32_16x16x32_bf16 v[80:83], v[184:187], v[120:123], v[80:83]\n\t"
    "ds_read_b128 v[112:115], %[ea] offset:20480\n\t"
    "v_mfma_f32_16x16x32_bf16 v[84:87], v[188:191], v[120:123], v[84:87]\n\t"
    "ds_read_b128 v[116:119], %[ea] offset:22528\n\t"
    "v_mfma_f32_16x16x32_bf16 v[88:91], v[184:187], v[124:127], v[88:91]\n\t"
    "v_mfma_f32_16x16x32_bf16 v[92:95], v[188:191], v[124:127], v[92:95]\n\t"
    "s_waitcnt lgkmcnt(4)\n\t"
    "v_mfma_f32_16x16x32_bf16 v[80:83], v[192:195], v[96:99], v[80:83]\n\t"
    "ds_read_b128 v[120:123], %[da] offset:20480\n\t"
    "v_mfma_f32_16x16x32_bf16 v[84:87], v[196:199], v[96:99], v[84:87]\n\t"
    "ds_read_b128 v[124:127], %[da] offset:22528\n\t"
    "v_mfma_f32_16x16x32_bf16 v[88:91], v[192:195], v[100:103], v[88:91]\n\t"
    "v_mfma_f32_16x16x32_bf16 v[92:95], v[196:199], v[100:103], v[92:95]\n\t"
    "s_waitcnt vmcnt(8)\n\t"
    "s_cmp_lt_u32 %[x], 2\n\t"
    "s_cbranch_scc0 49f\n\t"
    "s_waitcnt vmcnt(0)\n\t"
    "49:\n\t"
    "s_barrier\n\t"
    "s_waitcnt lgkmcnt(4)\n\t"
    "v_mfma_f32_16x16x32_bf16 v[80:83], v[200:203], v[104:107], v[80:83]\n\t"
    "ds_read_b128 v[96:99], %[ea] offset:24576\n\t"
    "v_mfma_f32_16x16x32_bf16 v[84:87], v[204:207], v[104:107], v[84:87]\n\t"
    "ds_read_b128 v[100:103], %[ea] offset:26624\n\t"
    "v_mfma_f32_16x16x32_bf16 v[88:91], v[200:203], v[108:111], v[88:91]\n\t"
    "v_mfma_f32_16x16x32_bf16 v[92:95], v[204:207], v[108:111], v[92:95]\n\t"
    "s_waitcnt lgkmcnt(4)\n\t"
    "v_mfma_f32_16x16x32_bf16 v[80:83], v[208:211], v[112:115], v[80:83]\n\t"
    "ds_read_b128 v[104:107], %[da] offset:24576\n\t"
    "v_mfma_f32_16x16x32_bf16 v[84:87], v[212:215], v[112:115], v[84:87]\n\t"
    "ds_read_b128 v[108:111], %[da] offset:26624\n\t"
    "v_mfma_f32_16x16x32_bf16 v[88:91], v[208:211], v[116:119], v[88:91]\n\t"
    "v_mfma_f32_16x16x32_bf16 v[92:95], v[212:215], v[116:119], v[92:95]\n\t"
    "s_cmp_eq_u32 %[ps], 0\n\t"
    "s_cbranch_scc1 10f\n\t"
    "global_load_dword %[sv], %[dv], %[db] sc1\n\t"
    "global_load_dword %[su], %[dv], %[db] offset:64 sc1\n\t"
    "10:\n\t"
    "s_waitcnt lgkmcnt(4)\n\t"
    "v_mfma_f32_16x16x32_bf16 v[80:83], v[216:219], v[120:123], v[80:83]\n\t"
    "ds_read_b128 v[112:115], %[ea] offset:28672\n\t"
    "v_mfma_f32_16x16x32_bf16 v[84:87], v[220:223], v[120:123], v[84:87]\n\t"
    "ds_read_b128 v[116:119], %[ea] offset:30720\n\t"
    "v_mfma_f32_16x16x32_bf16 v[88:91], v[216:219], v[124:127], v[88:91]\n\t"
    "v_mfma_f32_16x16x32_bf16 v[92:95], v[220:223], v[124:127], v[92:95]\n\t"
    "s_waitcnt lgkmcnt(4)\n\t"
    "v_mfma_f32_16x16x32_bf16 v[80:83], v[224:227], v[96:99], v[80:83]\n\t"
    "ds_read_b128 v[120:123], %[da] offset:28672\n\t"
    "v_mfma_f32_16x16x32_bf16 v[84:87], v[228:231], v[96:99], v[84:87]\n\t"
    "ds_read_b128 v[124:127], %[da] offset:30720\n\t"
    "v_mfma_f32_16x16x32_bf16 v[88:91], v[224:227], v[100:103], v[88:91]\n\t"
    "v_mfma_f32_16x16x32_bf16 v[92:95], v[228:231], v[100:103], v[92:95]\n\t"
    "s_waitcnt lgkmcnt(4)\n\t"
    "v_mfma_f32_16x16x32_bf16 v[80:83], v[232:235], v[104:107], v[80:83]\n\t"
    "v_mfma_f32_16x16x32_bf16 v[84:87], v[236:239], v[104:107], v[84:87]\n\t"
    "v_mfma_f32_16x16x32_bf16 v[88:91], v[232:235], v[108:111], v[88:91]\n\t"
    "v_mfma_f32_16x16x32_bf16 v[92:95], v[236:239], v[108:111], v[92:95]\n\t"
    "s_waitcnt lgkmcnt(2)\n\t"
    "v_mfma_f32_16x16x32_bf16 v[80:83], v[240:243], v[112:115], v[80:83]\n\t"
    "v_mfma_f32_16x16x32_bf16 v[84:87], v[244:247], v[112:115], v[84:87]\n\t"
    "v_mfma_f32_16x16x32_bf16 v[88:91], v[240:243], v[116:119], v[88:91]\n\t"
    "v_mfma_f32_16x16x32_bf16 v[92:95], v[244:247], v[116:119], v[92:95]\n\t"
    "s_waitcnt lgkmcnt(0)\n\t"
    "v_mfma_f32_16x16x32_bf16 v[80:83], v[248:251], v[120:123], v[80:83]\n\t"
    "s_memrealtime s[36:37]\n\t"
    "v_mfma_f32_16x16x32_bf16 v[84:87], v[252:255], v[120:123], v[84:87]\n\t"
    "v_mfma_f32_16x16x32_bf16 v[88:91], v[248:251], v[124:127], v[88:91]\n\t"
    "v_mfma_f32_16x16x32_bf16 v[92:95], v[252:255], v[124:127], v[92:95]\n\t"
    "s_mov_b32 %[got], 0\n\t"
    "v_mov_b32 v76, %[fa]\n\t"
    "v_mov_b32 v77, %[fv]\n\t"
    "v_add_u32 v78, 1, v77\n\t"
    "v_mov_b32 v79, %[ga]\n\t"
    "s_waitcnt lgkmcnt(0)\n\t"
    "ds_write_b128 %[rw], v[80:83] offset:0\n\t"
    "ds_write_b128 %[rw], v[84:87] offset:1024\n\t"
    "ds_write_b128 %[rw], v[88:91] offset:2048\n\t"
    "ds_write_b128 %[rw], v[92:95] offset:3072\n\t"
    "ds_write_b32 v76, v77\n\t"
    "s_cmp_eq_u32 %[ps], 0\n\t"
    "s_cbranch_scc1 9f\n\t"
    "s_waitcnt vmcnt(0)\n\t"
    "v_mad_u32_u24 v76, %[sv], %[mu], %[ad]\n\t"
    "v_cmp_ge_i32 vcc, %[th], v76\n\t"
    "s_cbranch_vccz 11f\n\t"
    "s_sub_u32 s36, s36, 20\n\t"
    "v_sub_u32 v76, s36, %[su]\n\t"
    "v_lshrrev_b32 v77, 24, %[su]\n\t"
    "v_lshlrev_b32 v76, 8, v76\n\t"
    "v_ashrrev_i32 v76, 31, v76\n\t"
    "v_add_u32 v77, v77, v76\n\t"
    "v_cmp_lt_i32 s[36:37], %[tb], v77\n\t"
    "s_andn2_b64 vcc, vcc, s[36:37]\n\t"
    "s_cbranch_scc1 9f\n\t"
    "11:\n\t"
    "ds_write2_b32 v79, v78, v78 offset1:1\n\t"
    "s_add_u32 m0, %[lb], 0\n\t"
    "s_nop 0\n\t"
    "global_load_lds_dwordx4 %[av0], %[s0] sc1\n\t"
    "s_add_u32 m0, %[lb], 4096\n\t"
    "s_nop 0\n\t"
    "global_load_lds_dwordx4 %[av2], %[s0] sc1\n\t"
    "s_add_u32 m0, %[lb], 8192\n\t"
    "s_nop 0\n\t"
    "global_load_lds_dwordx4 %[av4], %[s0] sc1\n\t"
    "s_add_u32 m0, %[lb], 12288\n\t"
    "s_nop 0\n\t"
    "global_load_lds_dwordx4 %[av6], %[s0] sc1\n\t"
    "s_add_u32 m0, %[lb], 16384\n\t"
    "s_nop 0\n\t"
    "global_load_lds_dwordx4 %[av0], %[s1] sc1\n\t"
    "s_add_u32 m0, %[lb], 20480\n\t"
    "s_nop 0\n\t"
    "global_load_lds_dwordx4 %[av2], %[s1] sc1\n\t"
    "s_add_u32 m0, %[lb], 24576\n\t"
    "s_nop 0\n\t"
    "global_load_lds_dwordx4 %[av4], %[s1] sc1\n\t"
    "s_add_u32 m0, %[lb], 28672\n\t"
    "s_nop 0\n\t"
    "global_load_lds_dwordx4 %[av6], %[s1] sc1\n\t"
    "s_mov_b32 %[got], 1\n\t"
    "s_branch 9f\n\t"
    "12:\n\t"
    "s_waitcnt lgkmcnt(0)\n\t"
    "9:"
    : [sv] "+&v"(sv), [su] "+&v"(su), [got] "=&s"(got)
    : [ea] "v"(ea), [da] "v"(da), [x] "s"(x), "{v[128:159]}"(W[0]), "{v[160:191]}"(W[1]), "{v[192:223]}"(W[2]), "{v[224:255]}"(W[3]), [lb] "s"(lb), [s0] "s"(s0), [s1] "s"(s1), [av0] "v"(av[0]), [av1] "v"(av[1]), [av2] "v"(av[2]), [av3] "v"(av[3]), [av4] "v"(av[4]), [av5] "v"(av[5]), [av6] "v"(av[6]), [av7] "v"(av[7]), [ps] "s"(ps), [dv] "v"(dv), [db] "s"(db), [rw] "v"(rw), [fa] "s"(fa), [fv] "s"(fv), [ga] "s"(ga), [mu] "s"(mu), [ad] "v"(ad), [th] "s"(th), [tb] "s"(tb)
    : "v96", "v97", "v98", "v99", "v100", "v101", "v102", "v103", "v104", "v105", "v106", "v107", "v108", "v109", "v110", "v111", "v112", "v113", "v114", "v115", "v116", "v117", "v118", "v119", "v120", "v121", "v122", "v123", "v124", "v125", "v126", "v127", "v76", "v77", "v78", "v79", "v80", "v81", "v82", "v83", "v84", "v85", "v86", "v87", "v88", "v89", "v90", "v91", "v92", "v93", "v94", "v95", "vcc", "scc", "m0", "memory", "s36", "s37", "s38", "s39");
}
__device__ __forceinline__ void lean_dm1(const u32x32 (&W)[4], unsigned ea, unsigned da, unsigned x, unsigned lb, const char* s0, const char* s1, const unsigned (&av)[8], unsigned ps, unsigned dv, const char* db, unsigned rw, unsigned fa, unsigned fv,
    unsigned ga, unsigned mu, unsigned ad, int th, int tb, unsigned& sv, unsigned& su, unsigned& got) {
  asm volatile("s_setprio 2\n\t"
    "v_mov_b32 v76, %[ga]\n\t"
    "ds_read_b32 v76, v76 offset:24\n\t"
    "ds_read_b128 v[96:99], %[ea] offset:0\n\t"
    "ds_read_b128 v[100:103], %[ea] offset:2048\n\t"
    "ds_read_b128 v[104:107], %[da] offset:0\n\t"
    "ds_read_b128 v[108:111], %[da] offset:2048\n\t"
    "ds_read_b128 v[112:115], %[ea] offset:4096\n\t"
    "ds_read_b128 v[116:119], %[ea] offset:6144\n\t"
    "s_waitcnt lgkmcnt(6)\n\t"
    "v_cmp_ne_u32 vcc, 0, v76\n\t"
    "s_mov_b32 %[got], -1\n\t"
    "s_cbranch_vccnz 12f\n\t"
    "s_waitcnt lgkmcnt(4)\n\t"
    "v_mfma_f32_16x16x32_bf16 v[80:83], v[128:131], v[96:99], 0\n\t"
    "ds_read_b128 v[120:123], %[da] offset:4096\n\t"
    "s_add_u32 m0, %[lb], 0\n\t"
    "v_mfma_f32_16x16x32_bf16 v[84:87], v[132:135], v[96:99], 0\n\t"
    "ds_read_b128 v[124:127], %[da] offset:6144\n\t"
    "global_load_lds_dwordx4 %[av0], %[s0] sc1\n\t"
    "v_mfma_f32_16x16x32_bf16 v[88:91], v[128:131], v[100:103], 0\n\t"
    "v_mfma_f32_16x16x32_bf16 v[92:95], v[132:135], v[100:103], 0\n\t"
    "s_waitcnt vmcnt(5)\n\t"
    "s_barrier\n\t"
    "s_waitcnt lgkmcnt(4)\n\t"
    "v_mfma_f32_16x16x32_bf16 v[80:83], v[136:139], v[104:107], v[80:83]\n\t"
    "ds_read_b128 v[96:99], %[ea] offset:8192\n\t"
    "s_add_u32 m0, %[lb], 4096\n\t"
    "v_mfma_f32_16x16x32_bf16 v[84:87], v[140:143], v[104:107], v[84:87]\n\t"
    "ds_read_b128 v[100:103], %[ea] offset:10240\n\t"
    "global_load_lds_dwordx4 %[av2], %[s0] sc1\n\t"
    "v_mfma_f32_16x16x32_bf16 v[88:91], v[136:139], v[108:111], v[88:91]\n\t"
    "v_mfma_f32_16x16x32_bf16 v[92:95], v[140:143], v[108:111], v[92:95]\n\t"
    "s_waitcnt lgkmcnt(4)\n\t"
    "v_mfma_f32_16x16x32_bf16 v[80:83], v[144:147], v[112:115], v[80:83]\n\t"
    "ds_read_b128 v[104:107], %[da] offset:8192\n\t"
    "s_add_u32 m0, %[lb], 8192\n\t"
    "v_mfma_f32_16x16x32_bf16 v[84:87], v[148:151], v[112:115], v[84:87]\n\t"
    "ds_read_b128 v[108:111], %[da] offset:10240\n\t"
    "global_load_lds_dwordx4 %[av4], %[s0] sc1\n\t"
    "v_mfma_f32_16x16x32_bf16 v[88:91], v[144:147], v[116:119], v[88:91]\n\t"
    "v_mfma_f32_16x16x32_bf16 v[92:95], v[148:151], v[116:119], v[92:95]\n\t"
    "s_waitcnt lgkmcnt(4)\n\t"
    "v_mfma_f32_16x16x32_bf16 v[80:83], v[152:155], v[120:123], v[80:83]\n\t"
    "ds_read_b128 v[112:115], %[ea] offset:12288\n\t"
    "s_add_u32 m0, %[lb], 12288\n\t"
    "v_mfma_f32_16x16x32_bf16 v[84:87], v[156:159], v[120:123], v[84:87]\n\t"
    "ds_read_b128 v[116:119], %[ea] offset:14336\n\t"
    "global_load_lds_dwordx4 %[av6], %[s0] sc1\n\t"
    "v_mfma_f32_16x16x32_bf16 v[88:91], v[152:155], v[124:127], v[88:91]\n\t"
    "v_mfma_f32_16x16x32_bf16 v[92:95], v[156:159], v[124:127], v[92:95]\n\t"
    "s_waitcnt lgkmcnt(4)\n\t"
    "v_mfma_f32_16x16x32_bf16 v[80:83], v[160:163], v[96:99], v[80:83]\n\t"
    "ds_read_b128 v[120:123], %[da] offset:12288\n\t"
    "s_add_u32 m0, %[lb], 16384\n\t"
    "v_mfma_f32_16x16x32_bf16 v[84:87], v[164:167], v[96:99], v[84:87]\n\t"
    "ds_read_b128 v[124:127], %[da] offset:14336\n\t"
    "global_load_lds_dwordx4 %[av0], %[s1] sc1\n\t"
    "v_mfma_f32_16x16x32_bf16 v[88:91], v[160:163], v[100:103], v[88:91]\n\t"
    "v_mfma_f32_16x16x32_bf16 v[92:95], v[164:167], v[100:103], v[92:95]\n\t"
    "s_waitcnt vmcnt(7)\n\t"
    "s_barrier\n\t"
    "s_waitcnt lgkmcnt(4)\n\t"
    "v_mfma_f32_16x16x32_bf16 v[80:83], v[168:171], v[104:107], v[80:83]\n\t"
    "ds_read_b128 v[96:99], %[ea] offset:16384\n\t"
    "s_add_u32 m0, %[lb], 20480\n\t"
    "v_mfma_f32_16x16x32_bf16 v[84:87], v[172:175], v[104:107], v[84:87]\n\t"
    "ds_read_b128 v[100:103], %[ea] offset:18432\n\t"
    "global_load_lds_dwordx4 %[av2], %[s1] sc1\n\t"
    "v_mfma_f32_16x16x32_bf16 v[88:91], v[168:171], v[108:111], v[88:91]\n\t"
    "v_mfma_f32_16x16x32_bf16 v[92:95], v[172:175], v[108:111], v[92:95]\n\t"
    "s_waitcnt lgkmcnt(4)\n\t"
    "v_mfma_f32_16x16x32_bf16 v[80:83], v[176:179], v[112:115], v[80:83]\n\t"
    "ds_read_b128 v[104:107], %[da] offset:16384\n\t"
    "s_add_u32 m0, %[lb], 24576\n\t"
    "v_mfma_f32_16x16x32_bf16 v[84:87], v[180:183], v[112:115], v[84:87]\n\t"
    "ds_read_b128 v[108:111], %[da] offset:18432\n\t"
    "global_load_lds_dwordx4 %[av4], %[s1] sc1\n\t"
    "v_mfma_f32_16x16x32_bf16 v[88:91], v[176:179], v[116:119], v[88:91]\n\t"
    "v_mfma_f32_16x16x32_bf16 v[92:95], v[180:183], v[116:119], v[92:95]\n\t"
    "s_waitcnt lgkmcnt(4)\n\t"
    "v_mfma_f32_16x16x32_bf16 v[80:83], v[184:187], v[120:123], v[80:83]\n\t"
    "ds_read_b128 v[112:115], %[ea] offset:20480\n\t"
    "s_add_u32 m0, %[lb], 28672\n\t"
    "v_mfma_f32_16x16x32_bf16 v[84:87], v[188:191], v[120:123], v[84:87]\n\t"
    "ds_read_b128 v[116:119], %[ea] offset:22528\n\t"
    "global_load_lds_dwordx4 %[av6], %[s1] sc1\n\t"
    "v_mfma_f32_16x16x32_bf16 v[88:91], v[184:187], v[124:127], v[88:91]\n\t"
    "v_mfma_f32_16x16x32_bf16 v[92:95], v[188:191], v[124:127], v[92:95]\n\t"
    "s_waitcnt lgkmcnt(4)\n\t"
    "v_mfma_f32_16x16x32_bf16 v[80:83], v[192:195], v[96:99], v[80:83]\n\t"
    "ds_read_b128 v[120:123], %[da] offset:20480\n\t"
    "v_mfma_f32_16x16x32_bf16 v[84:87], v[196:199], v[96:99], v[84:87]\n\t"
    "ds_read_b128 v[124:127], %[da] offset:22528\n\t"
    "v_mfma_f32_16x16x32_bf16 v[88:91], v[192:195], v[100:103], v[88:91]\n\t"
    "v_mfma_f32_16x16x32_bf16 v[92:95], v[196:199], v[100:103], v[92:95]\n\t"
    "s_waitcnt vmcnt(8)\n\t"
    "s_barrier\n\t"
    "s_waitcnt lgkmcnt(4)\n\t"
    "v_mfma_f32_16x16x32_bf16 v[80:83], v[200:203], v[104:107], v[80:83]\n\t"
    "ds_read_b128 v[96:99], %[ea] offset:24576\n\t"
    "v_mfma_f32_16x16x32_bf16 v[84:87], v[204:207], v[104:107], v[84:87]\n\t"
    "ds_read_b128 v[100:103], %[ea] offset:26624\n\t"
    "v_mfma_f32_16x16x32_bf16 v[88:91], v[200:203], v[108:111], v[88:91]\n\t"
    "v_mfma_f32_16x16x32_bf16 v[92:95], v[204:207], v[108:111], v[92:95]\n\t"
    "s_waitcnt lgkmcnt(4)\n\t"
    "v_mfma_f32_16x16x32_bf16 v[80:83], v[208:211], v[112:115], v[80:83]\n\t"
    "ds_read_b128 v[104:107], %[da] offset:24576\n\t"
    "v_mfma_f32_16x16x32_bf16 v[84:87], v[212:215], v[112:115], v[84:87]\n\t"
    "ds_read_b128 v[108:111], %[da] offset:26624\n\t"
    "v_mfma_f32_16x16x32_bf16 v[88:91], v[208:211], v[116:119], v[88:91]\n\t"
    "v_mfma_f32_16x16x32_bf16 v[92:95], v[212:215], v[116:119], v[92:95]\n\t"
    "s_waitcnt lgkmcnt(4)\n\t"
    "v_mfma_f32_16x16x32_bf16 v[80:83], v[216:219], v[120:123], v[80:83]\n\t"
    "ds_read_b128 v[112:115], %[ea] offset:28672\n\t"
    "v_mfma_f32_16x16x32_bf16 v[84:87], v[220:223], v[120:123], v[84:87]\n\t"
    "ds_read_b128 v[116:119], %[ea] offset:30720\n\t"
    "v_mfma_f32_16x16x32_bf16 v[88:91], v[216:219], v[124:127], v[88:91]\n\t"
    "v_mfma_f32_16x16x32_bf16 v[92:95], v[220:223], v[124:127], v[92:95]\n\t"
    "s_waitcnt lgkmcnt(4)\n\t"
    "v_mfma_f32_16x16x32_bf16 v[80:83], v[224:227], v[96:99], v[80:83]\n\t"
    "ds_read_b128 v[120:123], %[da] offset:28672\n\t"
    "v_mfma_f32_16x16x32_bf16 v[84:87], v[228:231], v[96:99], v[84:87]\n\t"
    "ds_read_b128 v[124:127], %[da] offset:30720\n\t"
    "v_mfma_f32_16x16x32_bf16 v[88:91], v[224:227], v[100:103], v[88:91]\n\t"
    "v_mfma_f32_16x16x32_bf16 v[92:95], v[228:231], v[100:103], v[92:95]\n\t"
    "s_waitcnt lgkmcnt(4)\n\t"
    "v_mfma_f32_16x16x32_bf16 v[80:83], v[232:235], v[104:107], v[80:83]\n\t"
    "v_mfma_f32_16x16x32_bf16 v[84:87], v[236:239], v[104:107], v[84:87]\n\t"
    "v_mfma_f32_16x16x32_bf16 v[88:91], v[232:235], v[108:111], v[88:91]\n\t"
    "v_mfma_f32_16x16x32_bf16 v[92:95], v[236:239], v[108:111], v[92:95]\n\t"
    "s_waitcnt lgkmcnt(2)\n\t"
    "v_mfma_f32_16x16x32_bf16 v[80:83], v[240:243], v[112:115], v[80:83]\n\t"
    "v_mfma_f32_16x16x32_bf16 v[84:87], v[244:247], v[112:115], v[84:87]\n\t"
    "v_mfma_f32_16x16x32_bf16 v[88:91], v[240:243], v[116:119], v[88:91]\n\t"
    "v_mfma_f32_16x16x32_bf16 v[92:95], v[244:247], v[116:119], v[92:95]\n\t"
    "s_waitcnt lgkmcnt(0)\n\t"
    "v_mfma_f32_16x16x32_bf16 v[80:83], v[248:251], v[120:123], v[80:83]\n\t"
    "v_mfma_f32_16x16x32_bf16 v[84:87], v[252:255], v[120:123], v[84:87]\n\t"
    "v_mfma_f32_16x16x32_bf16 v[88:91], v[248:251], v[124:127], v[88:91]\n\t"
    "v_mfma_f32_16x16x32_bf16 v[92:95], v[252:255], v[124:127], v[92:95]\n\t"
    "s_cmp_eq_u32 %[ps], 0\n\t"
    "s_cbranch_scc1 10f\n\t"
    "global_load_dword %[sv], %[dv], %[db] sc1\n\t"
    "global_load_dword %[su], %[dv], %[db] offset:64 sc1\n\t"
    "10:\n\t"
    "s_mov_b32 %[got], 0\n\t"
    "v_mov_b32 v76, %[fa]\n\t"
    "v_mov_b32 v77, %[fv]\n\t"
    "v_add_u32 v78, 1, v77\n\t"
    "v_mov_b32 v79, %[ga]\n\t"
    "ds_write_b128 %[rw], v[80:83] offset:0\n\t"
    "ds_write_b128 %[rw], v[84:87] offset:1024\n\t"
    "ds_write_b128 %[rw], v[88:91] offset:2048\n\t"
    "ds_write_b128 %[rw], v[92:95] offset:3072\n\t"
    "ds_write_b32 v76, v77\n\t"
    "s_cmp_eq_u32 %[ps], 0\n\t"
    "s_cbranch_scc1 9f\n\t"
    "s_waitcnt vmcnt(0)\n\t"
    "s_branch 9f\n\t"
    "12:\n\t"
    "s_waitcnt lgkmcnt(0)\n\t"
    "9:"
    : [sv] "+&v"(sv), [su] "+&v"(su), [got] "=&s"(got)
    : [ea] "v"(ea), [da] "v"(da), [x] "s"(x), "{v[128:159]}"(W[0]), "{v[160:191]}"(W[1]), "{v[192:223]}"(W[2]), "{v[224:255]}"(W[3]), [lb] "s"(lb), [s0] "s"(s0), [s1] "s"(s1), [av0] "v"(av[0]), [av1] "v"(av[1]), [av2] "v"(av[2]), [av3] "v"(av[3]), [av4] "v"(av[4]), [av5] "v"(av[5]), [av6] "v"(av[6]), [av7] "v"(av[7]), [ps] "s"(ps), [dv] "v"(dv), [db] "s"(db), [rw] "v"(rw), [fa] "s"(fa), [fv] "s"(fv), [ga] "s"(ga), [mu] "s"(mu), [ad] "v"(ad), [th] "s"(th), [tb] "s"(tb)
    : "v96", "v97", "v98", "v99", "v100", "v101", "v102", "v103", "v104", "v105", "v106", "v107", "v108", "v109", "v110", "v111", "v112", "v113", "v114", "v115", "v116", "v117", "v118", "v119", "v120", "v121", "v122", "v123", "v124", "v125", "v126", "v127", "v76", "v77", "v78", "v79", "v80", "v81", "v82", "v83", "v84", "v85", "v86", "v87", "v88", "v89", "v90", "v91", "v92", "v93", "v94", "v95", "vcc", "scc", "m0", "memory");
}
__device__ __forceinline__ void lean_st0(const u32x32 (&W)[4], unsigned ea, unsigned da, unsigned x, u32x16& pd, unsigned pv, unsigned cn, const char* pg, unsigned bl, unsigned& tk, unsigned& st) {
  asm volatile("v_mov_b32 v76, %[cn]\n\t"
    "ds_read_b32 v76, v76 offset:28\n\t"
    "ds_read_b128 v[96:99], %[ea] offset:0\n\t"
    "ds_read_b128 v[100:103], %[ea] offset:2048\n\t"
    "ds_read_b128 v[104:107], %[da] offset:0\n\t"
    "ds_read_b128 v[108:111], %[da] offset:2048\n\t"
    "ds_read_b128 v[112:115], %[ea] offset:4096\n\t"
    "ds_read_b128 v[116:119], %[ea] offset:6144\n\t"
    "s_waitcnt lgkmcnt(6)\n\t"
    "v_cmp_ne_u32 vcc, 0, v76\n\t"
    "s_mov_b32 %[st], -1\n\t"
    "s_cbranch_vccnz 12f\n\t"
    "s_waitcnt lgkmcnt(4)\n\t"
    "v_mfma_f32_16x16x32_bf16 v[80:83], v[128:131], v[96:99], 0\n\t"
    "ds_read_b128 v[120:123], %[da] offset:4096\n\t"
    "v_mfma_f32_16x16x32_bf16 v[84:87], v[132:135], v[96:99], 0\n\t"
    "ds_read_b128 v[124:127], %[da] offset:6144\n\t"
    "v_mfma_f32_16x16x32_bf16 v[88:91], v[128:131], v[100:103], 0\n\t"
    "v_mfma_f32_16x16x32_bf16 v[92:95], v[132:135], v[100:103], 0\n\t"
    "s_barrier\n\t"
    "s_bitcmp1_b32 %[x], 2\n\t"
    "s_cbranch_scc1 7f\n\t"
    "8:\n\t"
    "s_bitcmp1_b32 %[x], 3\n\t"
    "s_cbranch_scc1 13f\n\t"
    "14:\n\t"
    "s_waitcnt lgkmcnt(4)\n\t"
    "v_mfma_f32_16x16x32_bf16 v[80:83], v[136:139], v[104:107], v[80:83]\n\t"
    "ds_read_b128 v[96:99], %[ea] offset:8192\n\t"
    "v_mfma_f32_16x16x32_bf16 v[84:87], v[140:143], v[104:107], v[84:87]\n\t"
    "ds_read_b128 v[100:103], %[ea] offset:10240\n\t"
    "v_mfma_f32_16x16x32_bf16 v[88:91], v[136:139], v[108:111], v[88:91]\n\t"
    "v_mfma_f32_16x16x32_bf16 v[92:95], v[140:143], v[108:111], v[92:95]\n\t"
    "s_waitcnt lgkmcnt(4)\n\t"
    "v_mfma_f32_16x16x32_bf16 v[80:83], v[144:147], v[112:115], v[80:83]\n\t"
    "ds_read_b128 v[104:107], %[da] offset:8192\n\t"
    "v_mfma_f32_16x16x32_bf16 v[84:87], v[148:151], v[112:115], v[84:87]\n\t"
    "ds_read_b128 v[108:111], %[da] offset:10240\n\t"
    "v_mfma_f32_16x16x32_bf16 v[88:91], v[144:147], v[116:119], v[88:91]\n\t"
    "v_mfma_f32_16x16x32_bf16 v[92:95], v[148:151], v[116:119], v[92:95]\n\t"
    "s_waitcnt lgkmcnt(4)\n\t"
    "v_mfma_f32_16x16x32_bf16 v[80:83], v[152:155], v[120:123], v[80:83]\n\t"
    "ds_read_b128 v[112:115], %[ea] offset:12288\n\t"
    "v_mfma_f32_16x16x32_bf16 v[84:87], v[156:159], v[120:123], v[84:87]\n\t"
    "ds_read_b128 v[116:119], %[ea] offset:14336\n\t"
    "v_mfma_f32_16x16x32_bf16 v[88:91], v[152:155], v[124:127], v[88:91]\n\t"
    "v_mfma_f32_16x16x32_bf16 v[92:95], v[156:159], v[124:127], v[92:95]\n\t"
    "s_waitcnt lgkmcnt(4)\n\t"
    "v_mfma_f32_16x16x32_bf16 v[80:83], v[160:163], v[96:99], v[80:83]\n\t"
    "ds_read_b128 v[120:123], %[da] offset:12288\n\t"
    "v_mfma_f32_16x16x32_bf16 v[84:87], v[164:167], v[96:99], v[84:87]\n\t"
    "ds_read_b128 v[124:127], %[da] offset:14336\n\t"
    "v_mfma_f32_16x16x32_bf16 v[88:91], v[160:163], v[100:103], v[88:91]\n\t"
    "v_mfma_f32_16x16x32_bf16 v[92:95], v[164:167], v[100:103], v[92:95]\n\t"
    "s_barrier\n\t"
    "s_waitcnt lgkmcnt(4)\n\t"
    "v_mfma_f32_16x16x32_bf16 v[80:83], v[168:171], v[104:107], v[80:83]\n\t"
    "ds_read_b128 v[96:99], %[ea] offset:16384\n\t"
    "v_mfma_f32_16x16x32_bf16 v[84:87], v[172:175], v[104:107], v[84:87]\n\t"
    "ds_read_b128 v[100:103], %[ea] offset:18432\n\t"
    "v_mfma_f32_16x16x32_bf16 v[88:91], v[168:171], v[108:111], v[88:91]\n\t"
    "v_mfma_f32_16x16x32_bf16 v[92:95], v[172:175], v[108:111], v[92:95]\n\t"
    "s_waitcnt lgkmcnt(4)\n\t"
    "v_mfma_f32_16x16x32_bf16 v[80:83], v[176:179], v[112:115], v[80:83]\n\t"
    "ds_read_b128 v[104:107], %[da] offset:16384\n\t"
    "v_mfma_f32_16x16x32_bf16 v[84:87], v[180:183], v[112:115], v[84:87]\n\t"
    "ds_read_b128 v[108:111], %[da] offset:18432\n\t"
    "v_mfma_f32_16x16x32_bf16 v[88:91], v[176:179], v[116:119], v[88:91]\n\t"
    "v_mfma_f32_16x16x32_bf16 v[92:95], v[180:183], v[116:119], v[92:95]\n\t"
    "s_waitcnt lgkmcnt(4)\n\t"
    "v_mfma_f32_16x16x32_bf16 v[80:83], v[184:187], v[120:123], v[80:83]\n\t"
    "ds_read_b128 v[112:115], %[ea] offset:20480\n\t"
    "v_mfma_f32_16x16x32_bf16 v[84:87], v[188:191], v[120:123], v[84:87]\n\t"
    "ds_read_b128 v[116:119], %[ea] offset:22528\n\t"
    "v_mfma_f32_16x16x32_bf16 v[88:91], v[184:187], v[124:127], v[88:91]\n\t"
    "v_mfma_f32_16x16x32_bf16 v[92:95], v[188:191], v[124:127], v[92:95]\n\t"
    "s_waitcnt lgkmcnt(4)\n\t"
    "v_mfma_f32_16x16x32_bf16 v[80:83], v[192:195], v[96:99], v[80:83]\n\t"
    "ds_read_b128 v[120:123], %[da] offset:20480\n\t"
    "v_mfma_f32_16x16x32_bf16 v[84:87], v[196:199], v[96:99], v[84:87]\n\t"
    "ds_read_b128 v[124:127], %[da] offset:22528\n\t"
    "v_mfma_f32_16x16x32_bf16 v[88:91], v[192:195], v[100:103], v[88:91]\n\t"
    "v_mfma_f32_16x16x32_bf16 v[92:95], v[196:199], v[100:103], v[92:95]\n\t"
    "s_barrier\n\t"
    "s_and_b32 %[st], %[x], 3\n\t"
    "s_cbranch_scc1 3f\n\t"
    "4:\n\t"
    "s_waitcnt lgkmcnt(4)\n\t"
    "v_mfma_f32_16x16x32_bf16 v[80:83], v[200:203], v[104:107], v[80:83]\n\t"
    "ds_read_b128 v[96:99], %[ea] offset:24576\n\t"
    "v_mfma_f32_16x16x32_bf16 v[84:87], v[204:207], v[104:107], v[84:87]\n\t"
    "ds_read_b128 v[100:103], %[ea] offset:26624\n\t"
    "v_mfma_f32_16x16x32_bf16 v[88:91], v[200:203], v[108:111], v[88:91]\n\t"
    "v_mfma_f32_16x16x32_bf16 v[92:95], v[204:207], v[108:111], v[92:95]\n\t"
    "s_waitcnt lgkmcnt(4)\n\t"
    "v_mfma_f32_16x16x32_bf16 v[80:83], v[208:211], v[112:115], v[80:83]\n\t"
    "ds_read_b128 v[104:107], %[da] offset:24576\n\t"
    "v_mfma_f32_16x16x32_bf16 v[84:87], v[212:215], v[112:115], v[84:87]\n\t"
    "ds_read_b128 v[108:111], %[da] offset:26624\n\t"
    "v_mfma_f32_16x16x32_bf16 v[88:91], v[208:211], v[116:119], v[88:91]\n\t"
    "v_mfma_f32_16x16x32_bf16 v[92:95], v[212:215], v[116:119], v[92:95]\n\t"
    "s_waitcnt lgkmcnt(4)\n\t"
    "v_mfma_f32_16x16x32_bf16 v[80:83], v[216:219], v[120:123], v[80:83]\n\t"
    "ds_read_b128 v[112:115], %[ea] offset:28672\n\t"
    "v_mfma_f32_16x16x32_bf16 v[84:87], v[220:223], v[120:123], v[84:87]\n\t"
    "ds_read_b128 v[116:119], %[ea] offset:30720\n\t"
    "v_mfma_f32_16x16x32_bf16 v[88:91], v[216:219], v[124:127], v[88:91]\n\t"
    "v_mfma_f32_16x16x32_bf16 v[92:95], v[220:223], v[124:127], v[92:95]\n\t"
    "s_waitcnt lgkmcnt(4)\n\t"
    "v_mfma_f32_16x16x32_bf16 v[80:83], v[224:227], v[96:99], v[80:83]\n\t"
    "ds_read_b128 v[120:123], %[da] offset:28672\n\t"
    "v_mfma_f32_16x16x32_bf16 v[84:87], v[228:231], v[96:99], v[84:87]\n\t"
    "ds_read_b128 v[124:127], %[da] offset:30720\n\t"
    "v_mfma_f32_16x16x32_bf16 v[88:91], v[224:227], v[100:103], v[88:91]\n\t"
    "v_mfma_f32_16x16x32_bf16 v[92:95], v[228:231], v[100:103], v[92:95]\n\t"
    "s_waitcnt lgkmcnt(4)\n\t"
    "v_mfma_f32_16x16x32_bf16 v[80:83], v[232:235], v[104:107], v[80:83]\n\t"
    "v_mfma_f32_16x16x32_bf16 v[84:87], v[236:239], v[104:107], v[84:87]\n\t"
    "v_mfma_f32_16x16x32_bf16 v[88:91], v[232:235], v[108:111], v[88:91]\n\t"
    "v_mfma_f32_16x16x32_bf16 v[92:95], v[236:239], v[108:111], v[92:95]\n\t"
    "s_waitcnt lgkmcnt(2)\n\t"
    "v_mfma_f32_16x16x32_bf16 v[80:83], v[240:243], v[112:115], v[80:83]\n\t"
    "v_mfma_f32_16x16x32_bf16 v[84:87], v[244:247], v[112:115], v[84:87]\n\t"
    "v_mfma_f32_16x16x32_bf16 v[88:91], v[240:243], v[116:119], v[88:91]\n\t"
    "v_mfma_f32_16x16x32_bf16 v[92:95], v[244:247], v[116:119], v[92:95]\n\t"
    "s_waitcnt lgkmcnt(0)\n\t"
    "v_mfma_f32_16x16x32_bf16 v[80:83], v[248:251], v[120:123], v[80:83]\n\t"
    "v_mfma_f32_16x16x32_bf16 v[84:87], v[252:255], v[120:123], v[84:87]\n\t"
    "v_mfma_f32_16x16x32_bf16 v[88:91], v[248:251], v[124:127], v[88:91]\n\t"
    "v_mfma_f32_16x16x32_bf16 v[92:95], v[252:255], v[124:127], v[92:95]\n\t"
    "s_mov_b32 %[st], 0\n\t"
    "s_nop 5\n\t"
    "s_branch 9f\n\t"
    "13:\n\t"
    "s_memrealtime s[36:37]\n\t"
    "v_mov_b32 v79, 0\n\t"
    "s_waitcnt lgkmcnt(0)\n\t"
    "s_mov_b32 %[tk], s36\n\t"
    "s_add_u32 s36, s36, %[bl]\n\t"
    "s_and_b32 s36, s36, 0xffffff\n\t"
    "s_and_b32 s37, %[bl], 0xff000000\n\t"
    "s_or_b32 s36, s36, s37\n\t"
    "v_mov_b32 v78, s36\n\t"
    "s_mov_b64 exec, 1\n\t"
    "global_store_dword v79, v78, %[pg] offset:64 sc1\n\t"
    "s_mov_b64 exec, -1\n\t"
    "s_branch 14b\n\t"
    "3:\n\t"
    "7:\n\t"
    "s_and_b32 %[st], %[pv], 1\n\t"
    "s_mul_i32 %[st], %[st], 36\n\t"
    "s_add_u32 %[st], %[st], %[cn]\n\t"
    "v_mov_b32 v76, %[st]\n\t"
    "v_mov_b32 v77, 1\n\t"
    "v_mov_b32 v78, %[pv]\n\t"
    "v_mov_b32 v79, 0\n\t"
    "s_waitcnt vmcnt(0)\n\t"
    "s_mov_b64 exec, 1\n\t"
    "ds_add_rtn_u32 v77, v76, v77\n\t"
    "s_waitcnt lgkmcnt(0)\n\t"
    "v_readfirstlane_b32 %[st], v77\n\t"
    "s_and_b32 %[st], %[st], 3\n\t"
    "s_cmp_lg_u32 %[st], 3\n\t"
    "s_cbranch_scc1 6f\n\t"
    "global_store_dword v79, v78, %[pg] sc1\n\t"
    "6:\n\t"
    "s_mov_b64 exec, -1\n\t"
    "s_bitcmp1_b32 %[x], 2\n\t"
    "s_cbranch_scc1 8b\n\t"
    "s_branch 4b\n\t"
    "12:\n\t"
    "s_waitcnt lgkmcnt(0)\n\t"
    "9:"
    : "=&{v[80:95]}"(pd), [st] "=&s"(st), [tk] "+&s"(tk)
    : [ea] "v"(ea), [da] "v"(da), [x] "s"(x), "{v[128:159]}"(W[0]), "{v[160:191]}"(W[1]), "{v[192:223]}"(W[2]), "{v[224:255]}"(W[3]), [pv] "s"(pv), [cn] "s"(cn), [pg] "s"(pg), [bl] "s"(bl)
    : "v96", "v97", "v98", "v99", "v100", "v101", "v102", "v103", "v104", "v105", "v106", "v107", "v108", "v109", "v110", "v111", "v112", "v113", "v114", "v115", "v116", "v117", "v118", "v119", "v120", "v121", "v122", "v123", "v124", "v125", "v126", "v127", "v76", "v77", "v78", "v79", "vcc", "scc", "m0", "memory", "s36", "s37", "s38", "s39");
}
#define LEAN_QB 1
#define PREK 10
#define NEARLY 16
__device__ __forceinline__ void fin_ld(const u32x16& pd, unsigned rw, unsigned fa, unsigned fv) {
  asm volatile("ds_write_b128 %[rw], v[80:83] offset:0\n\t"
    "ds_write_b128 %[rw], v[84:87] offset:1024\n\t"
    "ds_write_b128 %[rw], v[88:91] offset:2048\n\t"
    "ds_write_b128 %[rw], v[92:95] offset:3072\n\t"
    "ds_write_b32 %[fa], %[fv]"
    :
    : [rw] "v"(rw), "{v[80:95]}"(pd), [fa] "v"(fa), [fv] "v"(fv)
    : "memory");
}
__device__ __forceinline__ void fin_st0(const u32x16& pd, unsigned rw, unsigned fa, unsigned fv, const char* cb, unsigned co0, unsigned co1, unsigned pa, unsigned& bad, unsigned& pk) {
  asm volatile("1:\n\t"
    "ds_read_b32 v39, %[fa]\n\t"
    "ds_read_b128 v[48:51], %[rw] offset:0\n\t"
    "ds_read_b128 v[52:55], %[rw] offset:1024\n\t"
    "ds_read_b128 v[56:59], %[rw] offset:2048\n\t"
    "ds_read_b128 v[60:63], %[rw] offset:3072\n\t"
    "s_waitcnt lgkmcnt(2)\n\t"
    "v_cmp_ne_u32 vcc, v39, %[fv]\n\t"
    "s_cbranch_vccz 2f\n\t"
    "s_waitcnt lgkmcnt(0)\n\t"
    "s_branch 1b\n\t"
    "2:\n\t"
    "v_add_f32 v48, v48, v80\n\t"
    "v_add_f32 v49, v49, v81\n\t"
    "v_add_f32 v50, v50, v82\n\t"
    "v_add_f32 v51, v51, v83\n\t"
    "v_add_f32 v52, v52, v84\n\t"
    "v_add_f32 v53, v53, v85\n\t"
    "v_add_f32 v54, v54, v86\n\t"
    "v_add_f32 v55, v55, v87\n\t"
    "v_cvt_pk_bf16_f32 v40, v48, v49\n\t"
    "v_cvt_pk_bf16_f32 v41, v50, v51\n\t"
    "v_cvt_pk_bf16_f32 v42, v52, v53\n\t"
    "v_cvt_pk_bf16_f32 v43, v54, v55\n\t"
    "global_store_dwordx4 %[co0], v[40:43], %[cb] sc1\n\t"
    "s_waitcnt lgkmcnt(0)\n\t"
    "v_mov_b32 v39, %[pa]\n\t"
    "ds_read_b32 v39, v39\n\t"
    "v_add_f32 v56, v56, v88\n\t"
    "v_add_f32 v57, v57, v89\n\t"
    "v_add_f32 v58, v58, v90\n\t"
    "v_add_f32 v59, v59, v91\n\t"
    "v_add_f32 v60, v60, v92\n\t"
    "v_add_f32 v61, v61, v93\n\t"
    "v_add_f32 v62, v62, v94\n\t"
    "v_add_f32 v63, v63, v95\n\t"
    "v_cvt_pk_bf16_f32 v44, v56, v57\n\t"
    "v_cvt_pk_bf16_f32 v45, v58, v59\n\t"
    "v_cvt_pk_bf16_f32 v46, v60, v61\n\t"
    "v_cvt_pk_bf16_f32 v47, v62, v63\n\t"
    "global_store_dwordx4 %[co1], v[44:47], %[cb] sc1\n\t"
    "v_cmp_u_f32 vcc, v48, v56\n\t"
    "s_waitcnt lgkmcnt(0)\n\t"
    "s_or_b32 %[bad], vcc_lo, vcc_hi\n\t"
    "v_readfirstlane_b32 %[pk], v39"
    : [bad] "=&s"(bad), [pk] "=&s"(pk)
    : [rw] "v"(rw), "{v[80:95]}"(pd), [fa] "v"(fa), [fv] "v"(fv), [cb] "s"(cb), [co0] "v"(co0), [co1] "v"(co1), [pa] "s"(pa)
    : "v39", "v40", "v41", "v42", "v43", "v44", "v45", "v46", "v47", "v48", "v49", "v50", "v51", "v52", "v53", "v54", "v55", "v56", "v57", "v58", "v59", "v60", "v61", "v62", "v63", "vcc", "scc", "memory");
}
__device__ __forceinline__ void fin_st1(const u32x16& pd, unsigned rw, unsigned fa, unsigned fv, const char* cb, unsigned co0, unsigned co1, unsigned pa, unsigned& bad, unsigned& pk) {
  asm volatile("1:\n\t"
    "ds_read_b32 v39, %[fa]\n\t"
    "ds_read_b128 v[48:51], %[rw] offset:0\n\t"
    "ds_read_b128 v[52:55], %[rw] offset:1024\n\t"
    "ds_read_b128 v[56:59], %[rw] offset:2048\n\t"
    "ds_read_b128 v[60:63], %[rw] offset:3072\n\t"
    "s_waitcnt lgkmcnt(2)\n\t"
    "v_cmp_ne_u32 vcc, v39, %[fv]\n\t"
    "s_cbranch_vccz 2f\n\t"
    "s_waitcnt lgkmcnt(0)\n\t"
    "s_branch 1b\n\t"
    "2:\n\t"
    "v_add_f32 v48, v48, v80\n\t"
    "v_add_f32 v49, v49, v81\n\t"
    "v_add_f32 v50, v50, v82\n\t"
    "v_add_f32 v51, v51, v83\n\t"
    "v_add_f32 v52, v52, v84\n\t"
    "v_add_f32 v53, v53, v85\n\t"
    "v_add_f32 v54, v54, v86\n\t"
    "v_add_f32 v55, v55, v87\n\t"
    "s_waitcnt lgkmcnt(0)\n\t"
    "v_mov_b32 v39, %[pa]\n\t"
    "ds_read_b32 v39, v39\n\t"
    "v_add_f32 v56, v56, v88\n\t"
    "v_add_f32 v57, v57, v89\n\t"
    "v_add_f32 v58, v58, v90\n\t"
    "v_add_f32 v59, v59, v91\n\t"
    "v_add_f32 v60, v60, v92\n\t"
    "v_add_f32 v61, v61, v93\n\t"
    "v_add_f32 v62, v62, v94\n\t"
    "v_add_f32 v63, v63, v95\n\t"
    "v_mul_f32 v40, %[nl], v48\n\t"
    "v_mul_f32 v41, %[nl], v49\n\t"
    "v_mul_f32 v42, %[nl], v50\n\t"
    "v_mul_f32 v43, %[nl], v51\n\t"
    "v_mul_f32 v44, %[nl], v56\n\t"
    "v_mul_f32 v45, %[nl], v57\n\t"
    "v_mul_f32 v46, %[nl], v58\n\t"
    "v_mul_f32 v47, %[nl], v59\n\t"
    "v_exp_f32 v40, v40\n\t"
    "v_exp_f32 v41, v41\n\t"
    "v_exp_f32 v42, v42\n\t"
    "v_exp_f32 v43, v43\n\t"
    "v_exp_f32 v44, v44\n\t"
    "v_exp_f32 v45, v45\n\t"
    "v_exp_f32 v46, v46\n\t"
    "v_exp_f32 v47, v47\n\t"
    "v_add_f32 v40, 1.0, v40\n\t"
    "v_add_f32 v41, 1.0, v41\n\t"
    "v_add_f32 v42, 1.0, v42\n\t"
    "v_add_f32 v43, 1.0, v43\n\t"
    "v_add_f32 v44, 1.0, v44\n\t"
    "v_add_f32 v45, 1.0, v45\n\t"
    "v_add_f32 v46, 1.0, v46\n\t"
    "v_add_f32 v47, 1.0, v47\n\t"
    "v_rcp_f32 v40, v40\n\t"
    "v_rcp_f32 v41, v41\n\t"
    "v_rcp_f32 v42, v42\n\t"
    "v_rcp_f32 v43, v43\n\t"
    "v_rcp_f32 v44, v44\n\t"
    "v_rcp_f32 v45, v45\n\t"
    "v_rcp_f32 v46, v46\n\t"
    "v_rcp_f32 v47, v47\n\t"
    "v_mul_f32 v40, v48, v40\n\t"
    "v_mul_f32 v41, v49, v41\n\t"
    "v_mul_f32 v42, v50, v42\n\t"
    "v_mul_f32 v43, v51, v43\n\t"
    "v_mul_f32 v44, v56, v44\n\t"
    "v_mul_f32 v45, v57, v45\n\t"
    "v_mul_f32 v46, v58, v46\n\t"
    "v_mul_f32 v47, v59, v47\n\t"
    "v_mul_f32 v40, v40, v52\n\t"
    "v_mul_f32 v41, v41, v53\n\t"
    "v_mul_f32 v42, v42, v54\n\t"
    "v_mul_f32 v43, v43, v55\n\t"
    "v_mul_f32 v44, v44, v60\n\t"
    "v_mul_f32 v45, v45, v61\n\t"
    "v_mul_f32 v46, v46, v62\n\t"
    "v_mul_f32 v47, v47, v63\n\t"
    "v_cvt_pk_bf16_f32 v48, v40, v41\n\t"
    "v_cvt_pk_bf16_f32 v49, v42, v43\n\t"
    "v_cvt_pk_bf16_f32 v50, v44, v45\n\t"
    "v_cvt_pk_bf16_f32 v51, v46, v47\n\t"
    "s_nop 1\n\t"
    "v_permlane16_swap_b32 v48, v50\n\t"
    "v_permlane16_swap_b32 v49, v51\n\t"
    "global_store_dwordx4 %[co0], v[48:51], %[cb] sc1\n\t"
    "v_cmp_u_f32 vcc, v52, v60\n\t"
    "s_waitcnt lgkmcnt(0)\n\t"
    "s_or_b32 %[bad], vcc_lo, vcc_hi\n\t"
    "v_readfirstlane_b32 %[pk], v39"
    : [bad] "=&s"(bad), [pk] "=&s"(pk)
    : [rw] "v"(rw), "{v[80:95]}"(pd), [fa] "v"(fa), [fv] "v"(fv), [cb] "s"(cb), [co0] "v"(co0), [nl] "s"(-LOG2E), [pa] "s"(pa)
    : "v39", "v40", "v41", "v42", "v43", "v44", "v45", "v46", "v47", "v48", "v49", "v50", "v51", "v52", "v53", "v54", "v55", "v56", "v57", "v58", "v59", "v60", "v61", "v62", "v63", "vcc", "scc", "memory");
}
__device__ __forceinline__ void fin_st2(const u32x16& pd, unsigned rw, unsigned fa, unsigned fv, const char* cb, unsigned co0, unsigned co1, unsigned pa, unsigned& bad, unsigned& pk) {
  asm volatile("s_nop 1\n\t"
    "v_cvt_pk_bf16_f32 v40, v80, v81\n\t"
    "v_cvt_pk_bf16_f32 v41, v82, v83\n\t"
    "v_cvt_pk_bf16_f32 v42, v84, v85\n\t"
    "v_cvt_pk_bf16_f32 v43, v86, v87\n\t"
    "global_store_dwordx4 %[co0], v[40:43], %[cb] sc1\n\t"
    "v_cvt_pk_bf16_f32 v44, v88, v89\n\t"
    "v_cvt_pk_bf16_f32 v45, v90, v91\n\t"
    "v_cvt_pk_bf16_f32 v46, v92, v93\n\t"
    "v_cvt_pk_bf16_f32 v47, v94, v95\n\t"
    "global_store_dwordx4 %[co1], v[44:47], %[cb] sc1\n\t"
    "1:\n\t"
    "ds_read_b32 v39, %[fa]\n\t"
    "ds_read_b128 v[48:51], %[rw] offset:0\n\t"
    "ds_read_b128 v[52:55], %[rw] offset:1024\n\t"
    "ds_read_b128 v[56:59], %[rw] offset:2048\n\t"
    "ds_read_b128 v[60:63], %[rw] offset:3072\n\t"
    "s_waitcnt lgkmcnt(2)\n\t"
    "v_cmp_ne_u32 vcc, v39, %[fv]\n\t"
    "s_cbranch_vccz 2f\n\t"
    "s_waitcnt lgkmcnt(0)\n\t"
    "s_branch 1b\n\t"
    "2:\n\t"
    "v_cvt_pk_bf16_f32 v48, v48, v49\n\t"
    "v_cvt_pk_bf16_f32 v49, v50, v51\n\t"
    "v_cvt_pk_bf16_f32 v50, v52, v53\n\t"
    "v_cvt_pk_bf16_f32 v51, v54, v55\n\t"
    "global_store_dwordx4 %[co0], v[48:51], %[cb] offset:2048 sc1\n\t"
    "s_waitcnt lgkmcnt(0)\n\t"
    "v_mov_b32 v39, %[pa]\n\t"
    "ds_read_b32 v39, v39\n\t"
    "v_cvt_pk_bf16_f32 v56, v56, v57\n\t"
    "v_cvt_pk_bf16_f32 v57, v58, v59\n\t"
    "v_cvt_pk_bf16_f32 v58, v60, v61\n\t"
    "v_cvt_pk_bf16_f32 v59, v62, v63\n\t"
    "global_store_dwordx4 %[co1], v[56:59], %[cb] offset:2048 sc1\n\t"
    "v_cmp_u_f32 vcc, v80, v88\n\t"
    "s_waitcnt lgkmcnt(0)\n\t"
    "s_or_b32 %[bad], vcc_lo, vcc_hi\n\t"
    "v_readfirstlane_b32 %[pk], v39"
    : [bad] "=&s"(bad), [pk] "=&s"(pk)
    : [rw] "v"(rw), "{v[80:95]}"(pd), [fa] "v"(fa), [fv] "v"(fv), [cb] "s"(cb), [co0] "v"(co0), [co1] "v"(co1), [pa] "s"(pa)
    : "v39", "v40", "v41", "v42", "v43", "v44", "v45", "v46", "v47", "v48", "v49", "v50", "v51", "v52", "v53", "v54", "v55", "v56", "v57", "v58", "v59", "v60", "v61", "v62", "v63", "vcc", "scc", "memory");
}
template <int R> __device__ __forceinline__ void gw_load(u32x32& w, unsigned a0, unsigned a1);
template <> __device__ __forceinline__ void gw_load<0>(u32x32& w, unsigned a0, unsigned a1) {
  asm volatile("ds_read_b64_tr_b16 v[128:129], %1 offset:0\n\t"
    "ds_read_b64_tr_b16 v[130:131], %1 offset:512\n\t"
    "ds_read_b64_tr_b16 v[132:133], %2 offset:0\n\t"
    "ds_read_b64_tr_b16 v[134:135], %2 offset:512\n\t"
    "ds_read_b64_tr_b16 v[136:137], %1 offset:4096\n\t"
    "ds_read_b64_tr_b16 v[138:139], %1 offset:4608\n\t"
    "ds_read_b64_tr_b16 v[140:141], %2 offset:4096\n\t"
    "ds_read_b64_tr_b16 v[142:143], %2 offset:4608\n\t"
    "s_waitcnt lgkmcnt(0)\n\t"
    "ds_read_b64_tr_b16 v[144:145], %1 offset:16384\n\t"
    "ds_read_b64_tr_b16 v[146:147], %1 offset:16896\n\t"
    "ds_read_b64_tr_b16 v[148:149], %2 offset:16384\n\t"
    "ds_read_b64_tr_b16 v[150:151], %2 offset:16896\n\t"
    "ds_read_b64_tr_b16 v[152:153], %1 offset:20480\n\t"
    "ds_read_b64_tr_b16 v[154:155], %1 offset:20992\n\t"
    "ds_read_b64_tr_b16 v[156:157], %2 offset:20480\n\t"
    "ds_read_b64_tr_b16 v[158:159], %2 offset:20992\n\t"
    "s_waitcnt lgkmcnt(0)"
    : "=&{v[128:159]}"(w) : "v"(a0), "v"(a1) : "memory");
}
template <> __device__ __forceinline__ void gw_load<1>(u32x32& w, unsigned a0, unsigned a1) {
  asm volatile("ds_read_b64_tr_b16 v[160:161], %1 offset:0\n\t"
    "ds_read_b64_tr_b16 v[162:163], %1 offset:512\n\t"
    "ds_read_b64_tr_b16 v[164:165], %2 offset:0\n\t"
    "ds_read_b64_tr_b16 v[166:167], %2 offset:512\n\t"
    "ds_read_b64_tr_b16 v[168:169], %1 offset:4096\n\t"
    "ds_read_b64_tr_b16 v[170:171], %1 offset:4608\n\t"
    "ds_read_b64_tr_b16 v[172:173], %2 offset:4096\n\t"
    "ds_read_b64_tr_b16 v[174:175], %2 offset:4608\n\t"
    "s_waitcnt lgkmcnt(0)\n\t"
    "ds_read_b64_tr_b16 v[176:177], %1 offset:16384\n\t"
    "ds_read_b64_tr_b16 v[178:179], %1 offset:16896\n\t"
    "ds_read_b64_tr_b16 v[180:181], %2 offset:16384\n\t"
    "ds_read_b64_tr_b16 v[182:183], %2 offset:16896\n\t"
    "ds_read_b64_tr_b16 v[184:185], %1 offset:20480\n\t"
    "ds_read_b64_tr_b16 v[186:187], %1 offset:20992\n\t"
    "ds_read_b64_tr_b16 v[188:189], %2 offset:20480\n\t"
    "ds_read_b64_tr_b16 v[190:191], %2 offset:20992\n\t"
    "s_waitcnt lgkmcnt(0)"
    : "=&{v[160:191]}"(w) : "v"(a0), "v"(a1) : "memory");
}
template <> __device__ __forceinline__ void gw_load<2>(u32x32& w, unsigned a0, unsigned a1) {
  asm volatile("ds_read_b64_tr_b16 v[192:193], %1 offset:0\n\t"
    "ds_read_b64_tr_b16 v[194:195], %1 offset:512\n\t"
    "ds_read_b64_tr_b16 v[196:197], %2 offset:0\n\t"
    "ds_read_b64_tr_b16 v[198:199], %2 offset:512\n\t"
    "ds_read_b64_tr_b16 v[200:201], %1 offset:4096\n\t"
    "ds_read_b64_tr_b16 v[202:203], %1 offset:4608\n\t"
    "ds_read_b64_tr_b16 v[204:205], %2 offset:4096\n\t"
    "ds_read_b64_tr_b16 v[206:207], %2 offset:4608\n\t"
    "s_waitcnt lgkmcnt(0)\n\t"
    "ds_read_b64_tr_b16 v[208:209], %1 offset:16384\n\t"
    "ds_read_b64_tr_b16 v[210:211], %1 offset:16896\n\t"
    "ds_read_b64_tr_b16 v[212:213], %2 offset:16384\n\t"
    "ds_read_b64_tr_b16 v[214:215], %2 offset:16896\n\t"
    "ds_read_b64_tr_b16 v[216:217], %1 offset:20480\n\t"
    "ds_read_b64_tr_b16 v[218:219], %1 offset:20992\n\t"
    "ds_read_b64_tr_b16 v[220:221], %2 offset:20480\n\t"
    "ds_read_b64_tr_b16 v[222:223], %2 offset:20992\n\t"
    "s_waitcnt lgkmcnt(0)"
    : "=&{v[192:223]}"(w) : "v"(a0), "v"(a1) : "memory");
}
template <> __device__ __forceinline__ void gw_load<3>(u32x32& w, unsigned a0, unsigned a1) {
  asm volatile("ds_read_b64_tr_b16 v[224:225], %1 offset:0\n\t"
    "ds_read_b64_tr_b16 v[226:227], %1 offset:512\n\t"
    "ds_read_b64_tr_b16 v[228:229], %2 offset:0\n\t"
    "ds_read_b64_tr_b16 v[230:231], %2 offset:512\n\t"
    "ds_read_b64_tr_b16 v[232:233], %1 offset:4096\n\t"
    "ds_read_b64_tr_b16 v[234:235], %1 offset:4608\n\t"
    "ds_read_b64_tr_b16 v[236:237], %2 offset:4096\n\t"
    "ds_read_b64_tr_b16 v[238:239], %2 offset:4608\n\t"
    "s_waitcnt lgkmcnt(0)\n\t"
    "ds_read_b64_tr_b16 v[240:241], %1 offset:16384\n\t"
    "ds_read_b64_tr_b16 v[242:243], %1 offset:16896\n\t"
    "ds_read_b64_tr_b16 v[244:245], %2 offset:16384\n\t"
    "ds_read_b64_tr_b16 v[246:247], %2 offset:16896\n\t"
    "ds_read_b64_tr_b16 v[248:249], %1 offset:20480\n\t"
    "ds_read_b64_tr_b16 v[250:251], %1 offset:20992\n\t"
    "ds_read_b64_tr_b16 v[252:253], %2 offset:20480\n\t"
    "ds_read_b64_tr_b16 v[254:255], %2 offset:20992\n\t"
    "s_waitcnt lgkmcnt(0)"
    : "=&{v[224:255]}"(w) : "v"(a0), "v"(a1) : "memory");
}
template <int I, int E, class F> __device__ __forceinline__ void sfor(F&& f) { if constexpr (I < E) { f(std::integral_constant<int, I>{}); sfor<I + 1, E>(f); } }
template <int ROLE, int KQ, class F>
__device__ __forceinline__ void wload(char* smem, int wave_id_, const u16* W0, const u16* W1, int ldb, u32x32 (&bw)[4], int rot, F&& hook) {
  constexpr int ABUF = 65536;
  const int tid = opaque_tid(), lane = tid & 63, wave = WAVE_ID;
  const int kq = KQ == 2 ? wave >> 2 : 0, nq = KQ == 2 ? wave & 3 : wave;
  const int g = lane >> 4, r16 = lane & 15, q4 = r16 >> 2, p4 = lane & 3;
  const unsigned ldsb = __builtin_amdgcn_readfirstlane((unsigned)(size_t)(LAS char*)smem);
  {
    const int srow = 8 * wave + (lane >> 3), ch = (lane & 7) ^ bswz(srow);
    const unsigned voff = (unsigned)(srow * ldb + ch * 8) * 2;
    const char* w0 = uni((const char*)W0); const char* w1 = uni((const char*)(ROLE == R_F1 ? W1 : W0 + 64));
    const size_t kb = (size_t)64 * ldb * 2;
    auto wstage = [&](int r) {
      const unsigned ld = ldsb + (r & 1) * ABUF + wave * 1024;
#pragma unroll
      for (int i = 0; i < 8; ++i) {
        const int kbi = KQ == 2 ? (ROLE == R_PART2 ? 0 : (i >> 2) * 8) + ((2 * r + ((i >> 1) & 1)) ^ rot) : 2 * r + (i >> 2);
        const char* src = KQ == 2 ? ((i & 1) ? w1 : w0) + kbi * kb + (ROLE == R_PART2 ? (i >> 2) * 256 : 0) : w0 + kbi * kb + (i & 3) * 128;
        unsigned keep;
        asm volatile("s_mov_b32 %0, m0\n\ts_mov_b32 m0, %3\n\ts_nop 2\n\tglobal_load_lds_dwordx4 %1, %2" WNT_ "\n\ts_mov_b32 m0, %0"
                     : "=&s"(keep) : "v"(voff), "s"(src), "s"(ld + i * 8192) : "memory");
      }
    };
    const int brow = 8 * g + q4, bs = bswz(brow);
    unsigned la[2];
#pragma unroll
    for (int j = 0; j < 2; ++j) {
      int blk, c16, sub;
      if (ROLE == R_F1) { blk = j; c16 = nq * 2 + (p4 >> 1); sub = (p4 & 1) * 8; }
      else { blk = nq >> 1; c16 = (nq & 1) * 4 + p4; sub = j * 8; }
      int set = kq;
      if (ROLE == R_PART2 && TLP) { set = nq >> 1; blk = nq & 1; c16 = kq * 4 + p4; }
      la[j] = (unsigned)((KQ == 2 ? set * 4 + blk : blk) * 8192 + brow * 128 + ((c16 ^ bs) << 4) + sub);
    }
    hook(4);
    wstage(0); wstage(1);
    sfor<0, 4>([&](auto r_c) __attribute__((always_inline)) {
      constexpr int r = decltype(r_c)::value;
      if (r < 3) WAIT_V(8); else WAIT_V(0);
      __builtin_amdgcn_s_barrier(); asm volatile("" ::: "memory");
      gw_load<r>(bw[r], ldsb + (r & 1) * ABUF + la[0], ldsb + (r & 1) * ABUF + la[1]);
      BAR();
      if (r + 2 < 4) wstage(r + 2);
      hook(r);
    });
  }
}
#define DSR2(d0, d1, a0_, a1_, off) asm volatile("ds_read_b128 %0, %2 offset:%4\n\tds_read_b128 %1, %3 offset:%4" : "=&v"(d0), "=&v"(d1) : "v"(a0_), "v"(a1_), "i"(off))
template <int N> __device__ __forceinline__ void waitl(u32x4& a, u32x4& b) { asm volatile("s_waitcnt lgkmcnt(%2)" : "+v"(a), "+v"(b) : "i"(N)); }
__device__ __forceinline__ void glds1(const char* sbase, unsigned voff, unsigned lds) {
  unsigned keep;
  asm volatile("s_mov_b32 %0, m0\n\ts_mov_b32 m0, %3\n\ts_nop 2\n\tglobal_load_lds_dwordx4 %1, %2 sc1\n\ts_mov_b32 m0, %0"
               : "=&s"(keep) : "v"(voff), "s"(sbase), "s"(lds) : "memory");
}
#define XS(i) do { } while (0)
#define XFL(b_) do { } while (0)
#define XA_ 0
#define XF_ 0
#define XE_ 0
#define XP_ 0
#define PUBK 9
#define LATE 0
#define NOWP 0
#define TB 48
#define TDM 1
#define TDA 15
#define TPUB 1
#define DUR0 160
#define PUP 4
#define PDQ 5
#define PDM 2
#define PDEEP 5
template <int ROLE>
__device__ __forceinline__ void gemm7(char* smem, int wave_id_, const u16* W0, const u16* W1, int ldb, const char* A_, size_t a_blk, Dep dep,
                                      char* C_, size_t c_blk, unsigned*& cnt, int myw, unsigned* tmo, int slot, unsigned long long* mts,
                                      bool gl, int nimm = 0, int nseg = NSEG, int wm0 = 0, int pofs = 0, u64 tag = 0) {
  dep.base = (const char*)cnt;
  unsigned* myprog = cnt + myw;
  volatile LAS unsigned* wsl = (volatile LAS unsigned*)(smem + L_SLOT + 512);
  if (opaque_tid() < 144) *(volatile LAS unsigned*)(smem + L_SLOT + opaque_tid() * 4) = 0u;
  const int tid = opaque_tid(), lane = tid & 63, wave = WAVE_ID;
  const int kq = wave >> 2, nq = wave & 3;
  const unsigned ldsb = __builtin_amdgcn_readfirstlane((unsigned)(size_t)(LAS char*)smem);
  constexpr int rot = 0;
  const char* A = uni(A_) + nq * 1024;
  const char* Cu = uni(C_);
  constexpr int PD = PDM;
  constexpr bool P2C = ROLE == R_PART2;
  int pw = 0;
  int wm = wm0, nh = 0;
  constexpr bool SPEC = ROLE != R_QKV;
  auto lgo = [&](int b, unsigned& v, unsigned& u, bool pre, bool force) {
    if (wave == 4) {
      for (int spins = 0; __builtin_amdgcn_readfirstlane(wsl[2]) != (unsigned)(b + 1); ++spins) {
        if (!pre) ld_fb(v, u, dep.base, dep.voff);
        const bool ok = (int)(v * dep.mul + dep.add) > b;
        if (__all(ok)) break;
        if (SPEC && !force && bells(ok, u, b, MARG)) break;
        if (!pre) __builtin_amdgcn_s_sleep(2);
        pre = false;
        if (give_up(spins, tmo, 9)) { v = 255u; break; }
      }
      wsl[2] = (unsigned)(b + 1);
    } else {
      while (__builtin_amdgcn_readfirstlane(wsl[2]) != (unsigned)(b + 1)) __builtin_amdgcn_s_sleep(1);
    }
  };
  auto lwm = [&](int b, unsigned v) {
    unsigned w;
    if (wave == 4) {
      const int w2 = (int)dep_eval(v, dep);
      w = (unsigned)min(max(max(w2, wm), b + 1), 255);
      wsl[0] = ((unsigned)(b + 1) << 8) | w;
    } else {
      for (;;) {
        w = __builtin_amdgcn_readfirstlane(wsl[0]);
        if ((w >> 8) == (unsigned)(b + 1)) break;
        __builtin_amdgcn_s_sleep(1);
      }
      w &= 255u;
    }
    return (int)w;
  };
  auto pubw = [&](int n) {
    WAIT_V(0);
    if (lane == 0) {
      const unsigned o = __hip_atomic_fetch_add((LAS unsigned*)(smem + L_SLOT + 516 + ((n + pofs) & 1) * 36), 1u, __ATOMIC_RELAXED, __HIP_MEMORY_SCOPE_WORKGROUP);
      if ((o & 3u) == 3u) {
        st4_sc1(myprog, (unsigned)(n + pofs));
      }
    }
    asm volatile("s_waitcnt lgkmcnt(0)" ::: "memory");
  };
  auto hsrc = [&](int hh) { return A + (size_t)(hh >> 1) * a_blk + ((hh ^ (rot >> 2)) & 1) * 16384; };
  auto hlds = [&](int hh) { return ldsb + (unsigned)(((hh >> 1) & 1) * 65536 + (hh & 1) * 16384 + nq * 1024); };
#define POFF(i_) (((i_) & 1) * 32768 + ((i_) >> 1) * 4096)
#define PIECE(s_, l_, i_) asm volatile("s_add_u32 m0, %2, %3\n\ts_nop 0\n\tglobal_load_lds_dwordx4 %0, %1 sc1" :: "v"(av[i_]), "s"(s_), "s"(l_), "i"(POFF(i_)) : "memory", "scc", "m0")
  int na = SB;
  auto arm = [&](int bb) {
    unsigned z = SENT; asm volatile("" : "+v"(z));
    constexpr int N = ROLE == R_F1 ? 1 : (P2C ? 4 : 2);
    const unsigned o = nq * N * 1024 + lane_id() * 16;
    const char* p = Cu + (size_t)bb * c_blk;
#pragma unroll
    for (int i = 0; i < N; ++i) st16s(p, o + i * 1024, u32x4{z, z, z, z});
  };
  u32x32 BW[4];
  wload<ROLE, 2>(smem, wave_id_, W0, W1, ldb, BW, rot, [&](int r) {
    if (ROLE == R_QKV && nimm > 0 && wave == 0) STS((char*)tmo - OFF_TMO, 128 + slot, 3 + r);
    if (SPEC && r == 2 && (ROLE != R_PART || gl)) { for (na += kq; na < NSEG; na += 2) arm(na); na = NSEG; }
    if (ROLE == R_QKV && nimm > 0 && r == 0 && wave == 4) init_issue((char*)tmo - OFF_TMO, ldsb + L_RED);
    if (ROLE == R_QKV && nimm > 0 && r == 2) {
      cnt = cnt_of((char*)tmo - OFF_TMO, init_get((char*)tmo - OFF_TMO, tag, smem + L_RED));
      dep.base = (const char*)cnt; myprog = cnt + myw;
      if (kq) {
        unsigned v0 = 0, u0;
        lgo(0, v0, u0, false, true);
        wm = lwm(0, v0);
        const unsigned l16 = lane_id() * 16;
#pragma unroll
        for (int i = 0; i < 16; ++i) glds1(hsrc(i >> 3) + (i & 1) * 32768 + ((((i & 7) >> 1) ^ rot) & 3) * 4096, l16, hlds(i >> 3) + POFF(i & 7));
      }
      nh = 2;
    }
  });
  if (ROLE == R_QKV && nimm > 0 && !kq) wm = (int)(__builtin_amdgcn_readfirstlane(wsl[0]) & 255u);
  if (ROLE == R_QKV && nimm > 0 && opaque_tid() == 0) STS((char*)tmo - OFF_TMO, 128 + slot, 0);
  if (wave == 0) WFB();
  auto body = [&](auto ldr_c) __attribute__((always_inline)) {
    constexpr bool LDR = decltype(ldr_c)::value;
    if (XP_ == 0 ? !LDR : (XP_ == 1 ? LDR : false)) __builtin_amdgcn_s_setprio(1);
    unsigned av[8];
#pragma unroll
    for (int i = 0; i < 8; ++i) av[i] = lane * 16 + (P2C ? 0 : (i & 1) * 32768) + (((i >> 1) ^ rot) & 3) * 4096;
    auto issue_all = [&](int hh) {
      if (LDR) {
        const char* s_ = hsrc(hh); const unsigned l_ = hlds(hh);
#pragma unroll
        for (int i = 0; i < 8; ++i) if (!(P2C && (i & 1))) PIECE(s_, l_, i);
      }
    };
    int b = 0;
    bool pre = false;
    unsigned smu = 0, tk = 0;
    unsigned smp = 0, got = 0;
    while (b < nseg) {
      const bool fast = (wm < nseg ? wm : nseg) >= b + PD && b >= nimm && nh <= 2 * b + 2;
      if (SPEC && b >= TB && !fast) break;
      const bool pl = wm <= b || pre, cold = nh < 2 * b + 2, nh3 = nh == 2 * b + 3;
      if (!fast) {
        XS(0);
        if (LDR && wm <= b && !got) lgo(b, smp, smu, false, true);
        XS(1);
        if (got) nh += NEARLY / 8;
        { const int upto = nh3 ? 2 * b + 4 : 2 * b + 2; for (; nh < upto; ++nh) issue_all(nh); }
        if (LDR && pl) wm = lwm(b, smp);
        XS(2);
      }
      const int ln = lane_id(), g = ln >> 4, r16 = ln & 15;
      const unsigned e0 = ldsb + (unsigned)((P2C ? 0 : kq * 32768) + (b & 1) * 65536 + (gl ? (g >> 1) * 512 + r16 * 32 + (g & 1) * 16 : r16 * 128 + ((g ^ (r16 & 7)) << 4)));
      const unsigned d0 = e0 ^ (gl ? 1024u : 64u);
      const unsigned rw = ldsb + L_RED + nq * 4096 + ln * 16;
      const unsigned co0 = ROLE == R_F1 ? (unsigned)((g & 1) * 2048 + nq * 512 + r16 * 32 + (g >> 1) * 16)
                         : ((ROLE == R_QKV ? TLQ : TLP) ? (unsigned)(nq * (P2C ? 4096 : 2048) + r16 * 64 + g * 16)
                         : (ROLE == R_QKV ? (unsigned)(r16 * 256 + (((4 * nq + g) ^ r16) << 4)) : (unsigned)(r16 * PROW + (32 * nq + 8 * g) * 2)));
      const unsigned co1 = co0 + ((ROLE == R_QKV ? TLQ : TLP) ? 1024u : (ROLE == R_QKV ? 4096u : 16u * PROW));
      u32x16 pv_;
      bool pr2 = false;

      if (fast) {
        __builtin_amdgcn_s_barrier(); asm volatile("" ::: "memory");
        const int LA = (wm < nseg ? wm : nseg) >= b + (ROLE == R_QKV ? PDQ : PDEEP) ? 3 : 2;
        for (; nh < 2 * b + LA; ++nh) issue_all(nh);
        int lf = (wm < nseg ? wm : nseg) - (LA - 1), cp = nseg - (LA - 1);
        const unsigned sa = ldsb + L_SLOT + ln * 4, ad = dep.add - (unsigned)(LA - 1);
        const unsigned up = LA == 3 ? 1000u : (unsigned)PUP;
#define FARGS BW, pv_, e0, d0, b, lf, cp, up, sa, dep.mul, ad, rw, (unsigned)a_blk, hsrc(2 * b + LA), hsrc(2 * b + LA + 1), hlds(2 * b + LA), hlds(2 * b + LA + 1), av
        if constexpr (LDR) (P2C ? fast_ld2 : (ROLE == R_F1 ? fast_ld1 : fast_ld0))(FARGS, LA);
#undef FARGS
        else {
          const char* c0_ = uni((const char*)tmo - OFF_TMO + OFF_DUMP); const char* c1_ = Cu + (size_t)b * c_blk;
          const int sf = b + pofs;
          const char* pg = uni((const char*)myprog);
          (ROLE == R_F1 ? fast_st1 : (P2C ? fast_st2 : fast_st0))(BW, pv_, e0, d0, b, lf, cp, up, sa, dep.mul, ad, rw, (unsigned)c_blk, c0_, c1_, co0, co1, pofs, sf, (unsigned)wave,
                                                                   ldsb + L_SLOT, dep.voff, dep.base, pg);
        }
        wm = lf + LA - 1 > wm ? lf + LA - 1 : wm;
        nh = 2 * b + 2;
        --b;
        __builtin_amdgcn_s_barrier(); asm volatile("" ::: "memory");
      } else {
        if (LDR) { if (nh >= 2 * b + 4) { if (P2C) WAIT_V(14); else WAIT_V(28); } else { if (P2C) WAIT_V(6); else WAIT_V(12); } }
        BAR();
        XS(3);
        if (ROLE == R_QKV && nimm > 0 && b < 2 && opaque_tid() == 0) STS((char*)tmo - OFF_TMO, 128 + slot, 1 + b);
        if constexpr (!LDR) {
          const unsigned wv = wsl[0];
          unsigned st;
          lean_st0(BW, e0, d0, __builtin_amdgcn_readfirstlane(pw > 0 ? (cold ? 4u : 1u) : 0u), pv_, (unsigned)(pw + pofs), ldsb + L_SLOT + 516, uni((const char*)myprog), 0u, tk, st);
          if (pl) wm = (int)(__builtin_amdgcn_readfirstlane(wv) & 255u);
        }
        const bool dm = nh == 2 * b + 2 && wm > b + 1 && b + 1 < nseg;
        pr2 = b + 1 < nseg && wm < nseg;
        if constexpr (LDR) {
          const bool er = nh == 2 * b + 2 && !dm;
          const unsigned ps = pr2 && (wave == 4 || er) ? (unsigned)(b + 1) : 0u;
#define LARGS BW, e0, d0, (unsigned)(nh - 2 * b - 2), hlds(nh), hsrc(nh), hsrc(nh + 1), av, ps, dep.voff, dep.base, \
              rw, ldsb + L_SLOT + 528 + nq * 4, (unsigned)(b + 1), ldsb + L_SLOT + 520, dep.mul, dep.add, er ? b + 1 : 0x7FFFFFFF, 0x7FFFFFFF, smp, smu, got
          if (dm) (P2C ? lean_dm1 : lean_dm0)(LARGS); else (P2C ? lean_ld1 : lean_ld0)(LARGS);
#undef LARGS
          got = __builtin_amdgcn_readfirstlane(got);
        }
        if (dm) nh += 2;
        XS(5);
      }
      unsigned bad = 0, pk = 0;
      if constexpr (LDR) { if (fast) fin_ld(pv_, rw, ldsb + L_SLOT + 528 + nq * 4, (unsigned)(b + 1)); }
      else (ROLE == R_F1 ? fin_st1 : (P2C ? fin_st2 : fin_st0))(pv_, rw, ldsb + L_SLOT + 528 + nq * 4, (unsigned)(b + 1), Cu + (size_t)b * c_blk, co0, co1, ldsb + L_SLOT + 524, bad, pk);
      XS(6);
      pre = pr2;
      bool now = (nh < 2 * b + 4 && wm < b + 1 + PD) || b + 1 >= nseg || b < nimm;
      if (!LDR && now) {
        if (NOWP == 0 && b >= nimm && pk == (unsigned)(b + 2)) now = false;
        else pubw(b + 1);
      }
      XS(7);
      XFL(b);
      pw = now ? 0 : b + 1;
      if (SPEC && !LDR && !fast && b >= 16 && na < NSEG && na > b + 1) { arm(na); arm(na + 1); na += 2; }
      ++b;
    }
    if (SPEC && b < nseg) {
      static_assert(!SPEC || (NEARLY == 16 && TLP), "");
      const int ln = lane_id(), g = ln >> 4, r16 = ln & 15;
      const unsigned e0 = ldsb + (unsigned)((P2C ? 0 : kq * 32768) + (gl ? (g >> 1) * 512 + r16 * 32 + (g & 1) * 16 : r16 * 128 + ((g ^ (r16 & 7)) << 4)));
      const unsigned rw = ldsb + L_RED + nq * 4096 + ln * 16;
      const unsigned co0 = ROLE == R_F1 ? (unsigned)((g & 1) * 2048 + nq * 512 + r16 * 32 + (g >> 1) * 16) : (unsigned)(nq * (P2C ? 4096 : 2048) + r16 * 64 + g * 16);
      const char* pg = uni((const char*)myprog);
      bool force = false, dmx = false;
      unsigned dur = DUR0, dp = 0, x8 = __builtin_amdgcn_readfirstlane(wave == 0 ? 8u : 0u);
      if (nh >= 2 * b + 2) got = 1;
      pre = false;
      for (;;) {
        unsigned rd;
        XS(0);
        if (LDR && b < nseg) {
          if (!got) { lgo(b, smp, smu, pre, force); issue_all(2 * b); issue_all(2 * b + 1); }
          XS(2);
          if (P2C) WAIT_V(6); else WAIT_V(12);
        }
        if (!LDR && b >= SB && na <= NSEG) { for (na = max(na, b); na < NSEG; ++na) arm(na); WAIT_V(0); ++na; }
        const unsigned ea = e0 + (b & 1) * 65536;
        BAR();
        XS(3);
        if (b >= nseg) rd = __builtin_amdgcn_readfirstlane(wsl[8]);
        else if constexpr (LDR) {
          const int th = b + 1 < nseg ? b + 1 : 0;
#define LARGS BW, ea, ea ^ (gl ? 1024u : 64u), 0u, hlds(2 * b + 2), hsrc(2 * b + 2), hsrc(2 * b + 3), av, (unsigned)th, dep.voff, dep.base, \
              rw, ldsb + L_SLOT + 528 + nq * 4, (unsigned)(b + 1), ldsb + L_SLOT + 520, dep.mul, dep.add, th, th >= SB ? th : 0x7FFFFFFF, smp, smu, got
          if (dmx) {
            *(volatile LAS u32x2*)(wsl + 2) = u32x2{(unsigned)(b + 2), (unsigned)(b + 2)};
            (P2C ? lean_dm1 : lean_dm0)(LARGS);
          } else (P2C ? lean_ld1 : lean_ld0)(LARGS);
#undef LARGS
          got = __builtin_amdgcn_readfirstlane(got) | (dmx ? 1u : 0u);
          rd = got >> 1;
          dmx = TDM && got == 1u && b + 2 < nseg
             && __all((int)(smp * dep.mul + dep.add) > b + 2 || (b + 2 >= SB && (int)(smu >> 24) + ((int)((rt_now() + (dmx ? TDA : 0) - MARG - smu) << 8) >> 31) > b + 2));
          pre = true;
        } else {
          u32x16 pv_;
          lean_st0(BW, ea, ea ^ (gl ? 1024u : 64u), b >= SB - 2 ? (unsigned)min(pw, 1) | x8 : (unsigned)min(pw, 1) * TPUB, pv_, (unsigned)pw, ldsb + L_SLOT + 516, pg, (unsigned)(b + 1) << 24 | dur, tk, rd);
          rd = __builtin_amdgcn_readfirstlane(rd);
          XS(5);
          if (!rd) {
            unsigned bad, pk;
            (ROLE == R_F1 ? fin_st1 : (P2C ? fin_st2 : fin_st0))(pv_, rw, ldsb + L_SLOT + 528 + nq * 4, (unsigned)(b + 1), Cu + (size_t)b * c_blk, co0, co0 + 1024u,
                                                                 ldsb + L_SLOT + 524, bad, pk);
            XS(6);
            pw = b + 1;
            if (x8 && b >= SB - 2) { const unsigned d = min(rt_now() - __builtin_amdgcn_readfirstlane(tk), 300u); dur = max(d, dp); dp = d; }
            if (bad && !force && b >= SB) wsl[8] = 1u;
            else if (b == SB - 1 || (pk != (unsigned)(b + 2) && __builtin_amdgcn_readfirstlane(wsl[2]) != (unsigned)(b + 2))) {
              pubw(b + 1);
              pw = 0;
            }
            if (na < NSEG && na > b + 1) { arm(na); arm(na + 1); na += 2; }
          }
        }
        XS(7);
        if (rd) {
          WAIT_V(0);
          BAR();
          if (wave == 4 && ln < 11) wsl[ln] = 0u;
          BAR();
          --b; pw = 0; pre = false; got = 0; force = true;
          continue;
        }
        if (b >= nseg) break;
        force = false;
        ++b;
      }
    }
    __builtin_amdgcn_s_setprio(0);
  };
  if (kq) body(std::true_type{}); else body(std::false_type{});
#undef PIECE
#undef POFF
  WAIT_V(0);
  BAR();
}
__device__ __forceinline__ void fin_role(char* smem, int wave_id_, int fidx, const float* mod, const char* P2, const u16* x1, u16* out, unsigned* cnt, unsigned* tmo,
                                         unsigned long long* mts) {
  const int tid = opaque_tid(), lane = tid & 63, wave = WAVE_ID;
  row_table(smem + L_GB, mod, cnt, tmo, tid);
  volatile LAS unsigned* wsl = (volatile LAS unsigned*)(smem + L_SLOT + 512);
  int wf = 0, par = 0;
  const unsigned pf2 = (unsigned)(PF2 + ((lane & 31) < 28 ? (lane & 31) : 0)) * 128, pln = (unsigned)((PLN + (lane >> 3)) * 128 + (lane & 7) * 4);
  for (int nf = fidx >> 3; nf < NSEG; nf += 2) {
    if (wf <= nf) {
      if (wave == 0) {
        unsigned w;
        for (int spins = 0;; ++spins) {
          unsigned v, u;
          ld_fb(v, u, cnt, pf2);
          if (!__all(v > (unsigned)nf) && bells(v > (unsigned)nf, u, nf, MARF)) { w = 0; break; }
          v = rowmin16(v);
          w = min(rdl(v, 0), rdl(v, 16));
          if ((int)w > nf) break;
          __builtin_amdgcn_s_sleep(2);
          if (give_up(spins, tmo, 9)) { w = NSEG; break; }
        }
        if (lane == 0) wsl[par] = w;
      }
      BAR();
      wf = (int)__builtin_amdgcn_readfirstlane(wsl[par]);
      par ^= 1;
    }
    int ln = lane; asm volatile("" : "+v"(ln));
    if (wave == 0) WTS(256 + 8 + fidx, nf / 2, 0);
    bool fin = nf < SB;
    for (int spins = 0; !fin_half(fin, P2, x1, smem + L_GB + 12288, out, nf * 32 + 4 * (fidx & 7) + (wave >> 1), wave & 1, ln); ++spins)
      fin = __all(ld4_sc1((const char*)cnt + pf2) > (unsigned)nf && ld4_sc1((const char*)cnt + pln) > (unsigned)nf) || give_up(spins, tmo, 9);
    if (wave == 0) WTS(256 + 8 + fidx, nf / 2, 2);
  }
}
#define LD8(r, vo, sb) asm volatile("global_load_dwordx4 %0, %8, %12 sc1\n\tglobal_load_dwordx4 %1, %9, %12 sc1\n\t" \
    "global_load_dwordx4 %2, %10, %12 sc1\n\tglobal_load_dwordx4 %3, %11, %12 sc1\n\t" \
    "global_load_dwordx4 %4, %8, %13 sc1\n\tglobal_load_dwordx4 %5, %9, %13 sc1\n\t" \
    "global_load_dwordx4 %6, %10, %13 sc1\n\tglobal_load_dwordx4 %7, %11, %13 sc1" \
    : "=&v"(r[0][0]), "=&v"(r[0][1]), "=&v"(r[0][2]), "=&v"(r[0][3]), "=&v"(r[1][0]), "=&v"(r[1][1]), "=&v"(r[1][2]), "=&v"(r[1][3]) \
    : "v"(vo), "v"(vo ^ (TLQ ? 2048u : 64u)), "v"(vo ^ (TLQ ? 4096u : 128u)), "v"(vo ^ (TLQ ? 6144u : 192u)), "s"(sb), "s"((sb) + (TLQ ? 1024 : 4096)) : "memory")
#define TIE8(r) "+v"(r[0][0]), "+v"(r[0][1]), "+v"(r[0][2]), "+v"(r[0][3]), "+v"(r[1][0]), "+v"(r[1][1]), "+v"(r[1][2]), "+v"(r[1][3])
#define SADL 255
#define DSA 20
#define SASL 8
__device__ __forceinline__ void sa_role(char* smem, int wave_id_, int hbase, const char* __restrict__ Q, const char* __restrict__ Kx, const char* __restrict__ V,
                                        const char* kvm, char* __restrict__ O, unsigned* cnt, unsigned* tmo, int slot, unsigned long long* mts) {
  constexpr float LS = LOG2E * SCALE;
  const int tid = opaque_tid(), wave = WAVE_ID;
  const int h = hbase + (wave & 1), s0 = wave >> 1;
  char* vl = smem + wave * 17408;
  const unsigned ldsw = __builtin_amdgcn_readfirstlane((unsigned)(size_t)(LAS char*)vl);
  unsigned avail = 0;
  {
    static_assert(SB % 4 == 0, "");
    unsigned z = SENT; asm volatile("" : "+v"(z));
    for (int s = SB + s0; s < NSEG; s += 4)
#pragma unroll
      for (int i = 0; i < 8; ++i) st16s(uni(O + ((size_t)s * 64 + 2 * h) * 4096), (unsigned)(i * 1024 + (tid & 63) * 16), u32x4{z, z, z, z});
  }
  for (int s = s0, it = 1; s < NSEG; s += 4, ++it) {
    u32x4 qv[2][4], kv[2][4];
    int lane = tid & 63; asm volatile("" : "+v"(lane));
    const int g = lane >> 4, r16 = lane & 15, q4 = r16 >> 2, p4 = lane & 3;
    auto request = [&](int sr) {
      const size_t o = ((size_t)sr * NH + h) * 8192;
      const char* qs = uni(Q + o); const char* ks = uni(Kx + o); const char* vs = uni(V + o);
      const unsigned fo = TLQ ? (unsigned)(r16 * 64 + g * 16) : (unsigned)(r16 * 256 + ((g ^ r16) << 4));
      LD8(qv, fo, qs); LD8(kv, fo, ks);
      head_to_lds(vs, lane, ldsw);
    };
    {
      for (int spins = 0; avail <= (unsigned)s; ++spins) {
        const unsigned p = ld4_sc1(cnt + (PQKV + h) * 32 + (lane & 15));
        avail = min(min(qkv_avail(p, 0), qkv_avail(p, 1)), qkv_avail(p, 2));
        if (avail > (unsigned)s) break;
        __builtin_amdgcn_s_sleep(32);
        if (give_up(spins, tmo, 10)) break;
      }
      request(s);
      asm volatile("s_waitcnt vmcnt(0)" : TIE8(qv), TIE8(kv) :: "memory");
    }
    WTM(slot, s, 0);
    f32x4 s00 = f32x4{0, 0, 0, 0}, s01 = s00, s11 = s00;
#pragma unroll
    for (int ks = 0; ks < 4; ++ks) {
      s00 = MFMA32(asbf8(kv[0][ks]), asbf8(qv[0][ks]), s00);
      s01 = MFMA32(asbf8(kv[0][ks]), asbf8(qv[1][ks]), s01);
      s11 = MFMA32(asbf8(kv[1][ks]), asbf8(qv[1][ks]), s11);
    }
    float mx[2], sum[2];
    u32x4 pseg[2];
    {
#pragma unroll
      for (int r = 0; r < 4; ++r) { const bool ok = 4 * g + r <= r16; s00[r] = ok ? s00[r] : -3.0e38f; s11[r] = ok ? s11[r] : -3.0e38f; }
      mx[0] = red16_32_max(max3(max3(s00[0], s00[1], s00[2]), s00[3], -1.0e38f));
      mx[1] = red16_32_max(max3(max3(fmaxf(fmaxf(s01[0], s01[1]), fmaxf(s01[2], s01[3])), s11[0], -1.0e38f), max3(s11[1], s11[2], s11[3]), -1.0e38f));
      const float n0 = -mx[0] * LS, n1 = -mx[1] * LS;
#pragma unroll
      for (int r = 0; r < 4; ++r) {
        s00[r] = __builtin_amdgcn_exp2f(s00[r] * LS + n0); s01[r] = __builtin_amdgcn_exp2f(s01[r] * LS + n1); s11[r] = __builtin_amdgcn_exp2f(s11[r] * LS + n1);
      }
      sum[0] = (s00[0] + s00[1]) + (s00[2] + s00[3]);
      sum[1] = ((s01[0] + s01[1]) + (s01[2] + s01[3])) + ((s11[0] + s11[1]) + (s11[2] + s11[3]));
      pseg[0] = u32x4{pack2(s00[0], s00[1]), pack2(s00[2], s00[3]), 0u, 0u};
      pseg[1] = u32x4{pack2(s01[0], s01[1]), pack2(s01[2], s01[3]), pack2(s11[0], s11[1]), pack2(s11[2], s11[3])};
    }
    f32x4 o[8][2];
#pragma unroll
    for (int dt = 0; dt < 8; ++dt) {
      const int row0 = 4 * g + q4;
      const unsigned xo = (unsigned)(row0 * 256 + (((4 * (dt >> 1) + p4) ^ row0) << 4) + (dt & 1) * 8);
      const u32x2 lo = trread(vl, xo), hi = trread(vl, xo + 4096);
      o[dt][0] = MFMA32(asbf8(u32x4{lo.x, lo.y, 0u, 0u}), asbf8(pseg[0]), (f32x4{0, 0, 0, 0}));
      o[dt][1] = MFMA32(asbf8(u32x4{lo.x, lo.y, hi.x, hi.y}), asbf8(pseg[1]), (f32x4{0, 0, 0, 0}));
    }
    float n0[2]; f32x4 sm[2];
#pragma unroll
    for (int b = 0; b < 2; ++b) { const float t = red16_32_add(sum[b]); sm[b] = f32x4{t, t, t, t}; n0[b] = -mx[b] * LS; }
    unsigned oa = (unsigned)(r16 * 128 + ((g ^ (r16 & 7)) << 4)), ka = (unsigned)(g * 256 + (r16 & 7) * 16), va = (unsigned)lane * 16;
    unsigned one = g < 2 ? 0x3F803F80u : 0u;
    u32x4 kmv[4], vmv[4];
#pragma unroll
    for (int i = 0; i < 4; ++i) vmv[i] = u32x4{0u, 0u, 0u, 0u};
#pragma unroll
    for (int dt = 0; dt < 8; ++dt) asm volatile("" : "+v"(o[dt][0]), "+v"(o[dt][1]), "+v"(vmv[dt >> 1]));
    asm volatile("s_waitcnt lgkmcnt(0)" : "+v"(sm[0]), "+v"(sm[1]), "+v"(n0[0]), "+v"(n0[1]), "+v"(oa), "+v"(ka), "+v"(va), "+v"(one) :: "memory");
    unsigned pg;
    for (int spins = 0;; ++spins) {
      pg = ld4_sc1(cnt + (PROG + h) * 32 + ((lane & 4) ? 5 : (lane & 3)));
      if (__all((pg >> 24) + (s >= SDIR ? 1u : 0u) > (unsigned)s)) break;
      __builtin_amdgcn_s_sleep(SASL);
      if (give_up(spins, tmo, 6)) break;
    }
    if (__all((pg >> 24) == (unsigned)s)) wait_rt(latest(pg), SADL);
    const char* mk = uni(kvm + ((size_t)s * NH + h) * 4096);
    for (int spins = 0;; ++spins) {
      asm volatile("global_load_dwordx4 %0, %8, %10 sc1\n\tglobal_load_dwordx4 %1, %8, %10 offset:128 sc1\n\t"
                   "global_load_dwordx4 %2, %8, %10 offset:1024 sc1\n\tglobal_load_dwordx4 %3, %8, %10 offset:1152 sc1\n\ts_mov_b32 exec_hi, 0\n\t"
                   "global_load_dwordx4 %4, %9, %10 offset:2048 sc1\n\tglobal_load_dwordx4 %5, %9, %10 offset:2560 sc1\n\t"
                   "global_load_dwordx4 %6, %9, %10 offset:3072 sc1\n\tglobal_load_dwordx4 %7, %9, %10 offset:3584 sc1\n\ts_mov_b32 exec_hi, -1\n\ts_waitcnt vmcnt(0)"
                   : "=&v"(kmv[0]), "=&v"(kmv[1]), "=&v"(kmv[2]), "=&v"(kmv[3]), "+v"(vmv[0]), "+v"(vmv[1]), "+v"(vmv[2]), "+v"(vmv[3])
                   : "v"(ka), "v"(va), "s"(mk) : "memory");
      unsigned mx_ = 0;
#pragma unroll
      for (int i = 0; i < 4; ++i) mx_ = max(max(max(max(mx_, kmv[i].x), kmv[i].y), max(kmv[i].z, kmv[i].w)), max(max(vmv[i].x, vmv[i].y), max(vmv[i].z, vmv[i].w)));
      if (__all(mx_ != SENT)) break;
      __builtin_amdgcn_s_sleep(2);
      if (give_up(spins, tmo, 6)) break;
    }
    WTM(slot, s, 1);
    f32x4 tt[2] = {f32x4{0, 0, 0, 0}, f32x4{0, 0, 0, 0}};
#pragma unroll
    for (int ks = 0; ks < 4; ++ks) { tt[0] = MFMA32(asbf8(kmv[ks]), asbf8(qv[0][ks]), tt[0]); tt[1] = MFMA32(asbf8(kmv[ks]), asbf8(qv[1][ks]), tt[1]); }
#pragma unroll
    for (int b = 0; b < 2; ++b)
#pragma unroll
      for (int r = 0; r < 4; ++r) tt[b][r] = tt[b][r] * LS + n0[b];
    if (!__all(max3(max3(tt[0][0], tt[0][1], tt[0][2]), max3(tt[0][3], tt[1][0], tt[1][1]), max2(tt[1][2], tt[1][3])) <= 40.0f)) {
#pragma unroll
      for (int b = 0; b < 2; ++b) {
        float m = max3(max3(tt[b][0], tt[b][1], tt[b][2]), tt[b][3], 0.0f);
        { float x, y; sw16(m, x, y); m = max2(x, y); }
        const float f = __builtin_amdgcn_exp2f(-m);
        tt[b] -= m; sm[b] *= f;
#pragma unroll
        for (int dt = 0; dt < 8; ++dt) o[dt][b] *= f;
      }
    }
    u32x2 pm[2]; float inv[2];
#pragma unroll
    for (int b = 0; b < 2; ++b) {
      pm[b] = u32x2{pack2(__builtin_amdgcn_exp2f(tt[b][0]), __builtin_amdgcn_exp2f(tt[b][1])), pack2(__builtin_amdgcn_exp2f(tt[b][2]), __builtin_amdgcn_exp2f(tt[b][3]))};
      sm[b] = __builtin_amdgcn_mfma_f32_16x16x16bf16_1k(ass4(u32x2{one, one}), ass4(pm[b]), sm[b], 0, 0, 0);
    }
#pragma unroll
    for (int dt = 0; dt < 8; ++dt)
#pragma unroll
      for (int b = 0; b < 2; ++b) o[dt][b] = __builtin_amdgcn_mfma_f32_16x16x16bf16_1k(ass4((dt & 1) ? u32x2{vmv[dt >> 1].z, vmv[dt >> 1].w} : u32x2{vmv[dt >> 1].x, vmv[dt >> 1].y}), ass4(pm[b]), o[dt][b], 0, 0, 0);
    inv[0] = __builtin_amdgcn_rcpf(sm[0][0]); inv[1] = __builtin_amdgcn_rcpf(sm[1][0]);
    const char* od = uni(O + ((size_t)s * 64 + 2 * h) * 4096);
#pragma unroll
    for (int j = 0; j < 4; ++j) {
#pragma unroll
      for (int b = 0; b < 2; ++b) {
        const f32x4 r0 = o[2 * j][b] * inv[b], r1 = o[2 * j + 1][b] * inv[b];
        st16s(od + (j >> 1) * 4096 + b * 2048, oa ^ ((j & 1) * 64u), u32x4{pack2(r0[0], r0[1]), pack2(r0[2], r0[3]), pack2(r1[0], r1[1]), pack2(r1[2], r1[3])});
      }
    }
    if (s >= SB && lane == 0) st4_sc1(cnt + (PSA + (hbase >> 1)) * 32 + 16 + wave, bell(s, rt_now() + DSA));
    WTM(slot, s, 3);
    WAIT_V(0);
    if (lane == 0) st4_sc1(cnt + (PSA + (hbase >> 1)) * 32 + wave, (unsigned)it);
    WTM(slot, s, 2);
  }
}
#undef LD8
#undef TIE8

constexpr int Q_MEM = 0;
constexpr int Q_K   = 16384;
constexpr int Q_V   = 24576;
constexpr int Q_QK  = 32768;
constexpr int Q_TM  = 36864;
constexpr int Q_CB  = 40960;
constexpr int Q_RING = 32768;
constexpr int V_P1  = 0;
constexpr int V_P2  = 16384;
constexpr int NCHAIN = 96;
#define VHN 2
#define ROTH 0
#define NQQ 6
#define NQK 4
#define E_AG 9
#define RDYI -2
#define ROTW 2
#define DQU 6
#define DQM 18
#define DQD 1
#define ARSEL (xg >> 1)
#define ORSEL (xg >> 1)
#define D_Q 105
#define D_O 4
#define E_B 6
#define E_O 1900
#define KDEL 5
#define VPOS 0

typedef __bf16 bf16x16 __attribute__((ext_vector_type(16)));
typedef unsigned u32x8 __attribute__((ext_vector_type(8)));
__device__ __forceinline__ f32x4 SMF(u32x4 a, u32x8 b, f32x4 c, int idx) {
  return __builtin_amdgcn_smfmac_f32_16x16x64_bf16(__builtin_bit_cast(bf16x8, a), __builtin_bit_cast(bf16x16, b), c, idx, 0, 0);
}
__device__ __forceinline__ float fold32(float a, float b) {
  const auto p = __builtin_amdgcn_permlane32_swap(__float_as_uint(a), __float_as_uint(b), false, false);
  return __uint_as_float(p[0]) + __uint_as_float(p[1]);
}
__device__ __forceinline__ f32x2 rowsum(f32x4 x) { return f32x2{fold32(x[0], x[2]), fold32(x[1], x[3])}; }
__device__ __forceinline__ unsigned nosent1(unsigned v) { return v < 0xFFFFFFFEu ? v : 0xFFFFFFFEu; }
__device__ __forceinline__ void st4s(const char* sb, unsigned vo, unsigned v) { asm volatile("global_store_dword %0, %1, %2 sc1\n\ts_nop 0" :: "v"(vo), "v"(v), "s"(sb) : "memory"); }
__device__ __forceinline__ void st8s(const char* sb, unsigned vo, u32x2 v) { asm volatile("global_store_dwordx2 %0, %1, %2 sc1\n\ts_nop 1" :: "v"(vo), "v"(v), "s"(sb) : "memory"); }
__device__ __forceinline__ void gldsw(const char* sbase, unsigned voff, unsigned lds) {
  unsigned keep;
  asm volatile("s_mov_b32 %0, m0\n\ts_mov_b32 m0, %3\n\ts_nop 2\n\tglobal_load_lds_dwordx4 %1, %2" CNT_ "\n\ts_mov_b32 m0, %0"
               : "=&s"(keep) : "v"(voff), "s"(sbase), "s"(lds) : "memory");
}
__device__ __forceinline__ void wait_clk(unsigned t0, unsigned d) {
  while ((unsigned)__builtin_amdgcn_s_memtime() - t0 < d) __builtin_amdgcn_s_sleep(1);
}
__device__ __forceinline__ unsigned bell_time(const char* a, unsigned vo, unsigned tag, unsigned cap) {
  unsigned v;
  for (unsigned spins = 0;; ++spins) {
    asm volatile("global_load_dword %0, %1, %2 sc1\n\ts_waitcnt vmcnt(0)" : "=&v"(v) : "v"(vo), "s"(a) : "memory");
    if (__all((v >> 24) >= tag) || spins >= cap) break;
  }
  const unsigned now = (unsigned)__builtin_amdgcn_s_memrealtime() + 250u;
  const unsigned age = rowmin16((now - v) & 0xFFFFFFu);
  return now - min(min(rdl(age, 0), rdl(age, 16)), 500u);
}
#define WBAR() do { __builtin_amdgcn_s_barrier(); asm volatile("" ::: "memory"); } while (0)
#define TN(k, v) do { } while (0)
__device__ __forceinline__ void stage_proj(const char* src, unsigned lds, int tid, int wave) {
  const int row = tid >> 4, f = 2 * ((row & 1) | (((row >> 2) & 1) << 1));
#pragma unroll
  for (int j = 0; j < 4; ++j)
    gldsw(src + (size_t)j * 32 * HD * 2, (unsigned)(row * HD * 2 + (((tid & 15) ^ f ^ ((j & 1) << 3)) << 4)), lds + (wave * 64 + 512 * j) * 16);
}
__device__ __forceinline__ unsigned proj_base(int T, int lane) {
  const int g = lane >> 4, q4 = (lane & 15) >> 2, p4 = lane & 3, fs = 2 * ((q4 & 1) | ((g & 1) << 1) | ((q4 >> 1) << 2));
  return (unsigned)((4 * g + (q4 & 1) + 32 * (q4 >> 1)) * 256 + (((2 * T) ^ fs) << 4) + p4 * 8);
}
__device__ __forceinline__ u32x8 proj_frag(const char* smem, unsigned a) {
  const u32x2 t0 = trread(smem, a), t1 = trread(smem, a + 512), t2 = trread(smem, a + 4096), t3 = trread(smem, a + 4608);
  return u32x8{t0.x, t0.y, t1.x, t1.y, t2.x, t2.y, t3.x, t3.y};
}
__device__ __forceinline__ void flag_frags(unsigned fa, unsigned a, unsigned want, bf16x8 (&f)[4]) {
  u32x2 t[8];
  for (;;) {
    u32x4 fl;
    asm volatile("ds_read_b128 %0, %9\n\t"
                 "ds_read_b64_tr_b16 %1, %10\n\tds_read_b64_tr_b16 %2, %10 offset:64\n\t"
                 "ds_read_b64_tr_b16 %3, %10 offset:512\n\tds_read_b64_tr_b16 %4, %10 offset:576\n\t"
                 "ds_read_b64_tr_b16 %5, %10 offset:1024\n\tds_read_b64_tr_b16 %6, %10 offset:1088\n\t"
                 "ds_read_b64_tr_b16 %7, %10 offset:1536\n\tds_read_b64_tr_b16 %8, %10 offset:1600\n\ts_waitcnt lgkmcnt(0)"
                 : "=&v"(fl), "=&v"(t[0]), "=&v"(t[1]), "=&v"(t[2]), "=&v"(t[3]), "=&v"(t[4]), "=&v"(t[5]), "=&v"(t[6]), "=&v"(t[7])
                 : "v"(fa), "v"(a) : "memory");
    if (__builtin_amdgcn_readfirstlane(min(min(fl.x, fl.y), min(fl.z, fl.w))) >= want) break;
  }
#pragma unroll
  for (int ks = 0; ks < 4; ++ks) f[ks] = asbf8(u32x4{t[2 * ks].x, t[2 * ks].y, t[2 * ks + 1].x, t[2 * ks + 1].y});
}
__device__ __forceinline__ void wait_flags(const char* p, unsigned want) {
  for (;;) {
    const u32x4 fl = *(volatile LAS u32x4*)p;
    if (__builtin_amdgcn_readfirstlane(min(min(fl.x, fl.y), min(fl.z, fl.w))) >= want) break;
  }
}
__device__ __forceinline__ f32x4 zero4() { float z = 0.0f; asm volatile("" : "+v"(z)); return f32x4{z, z, z, z}; }
__device__ __forceinline__ unsigned lds_word(const char* p) { return __builtin_amdgcn_readfirstlane(*(volatile LAS unsigned*)p); }

#define MFMA16(a, b, c) __builtin_amdgcn_mfma_f32_16x16x16bf16_1k((a), (b), (c), 0, 0, 0)
#define UF(x) __uint_as_float(x)
__device__ __forceinline__ unsigned umax3(unsigned a, unsigned b, unsigned c) { return max(max(a, b), c); }
__device__ __forceinline__ f32x2 logit2(float a, float b) {
  return f32x2{a, b};
}
constexpr float LSC = LOG2E * SCALE;

struct QKW { u32x32 w0, w1, w2, w3, w4, w5; u32x8 x; };
template <int W_> __device__ __forceinline__ void qk_step(const QKW& W, const char* ag, const char* vs, const char* od, const char* kv, unsigned& cap, unsigned& sp,
                                                          unsigned long long& tc, unsigned long long* ts, unsigned& d0, unsigned& d1);
template <> __device__ __forceinline__ void qk_step<0>(const QKW& W, const char* ag, const char* vs, const char* od, const char* kv, unsigned& cap, unsigned& sp,
                                                        unsigned long long& tc, unsigned long long* ts, unsigned& d0, unsigned& d1) {
  unsigned st, bg;
  asm volatile("v_mbcnt_lo_u32_b32 v60, -1, 0\n\t"
    "v_mbcnt_hi_u32_b32 v60, -1, v60\n\t"
    "v_lshlrev_b32 v6, 4, v60\n\t"
    "s_mov_b32 %[sp], 0\n\t"
    "s_setprio 3\n\t"
    "global_load_dwordx4 v[40:43], v6, %[ag] sc1\n\t"
    "global_load_dwordx4 v[44:47], v6, %[ag] offset:1024 sc1\n\t"
    "s_memtime %[tc]\n\t"
    "v_add_u32 v7, 40960, v6\n\t"
    "v_add_u32 v8, 98304, v6\n\t"
    "ds_read_b128 v[24:27], v7 offset:0\n\t"
    "ds_read_b128 v[28:31], v7 offset:1024\n\t"
    "ds_read_b128 v[32:35], v7 offset:8192\n\t"
    "ds_read_b128 v[36:39], v7 offset:9216\n\t"
    "v_lshrrev_b32 v48, 4, v60\n\t"
    "v_and_b32 v49, 15, v60\n\t"
    "v_and_b32 v61, 4, v60\n\t"
    "v_mul_u32_u24 v61, 0x2aaa, v61\n\t"
    "v_lshrrev_b32 v9, 1, v60\n\t"
    "v_and_b32 v9, 2, v9\n\t"
    "v_or3_b32 v9, v9, v61, %[c44]\n\t"
    "v_add_u32 v14, 0, v6\n\t"
    "v_lshlrev_b32 v61, 2, v48\n\t"
    "v_lshl_or_b32 v10, v49, 4, v61\n\t"
    "v_lshrrev_b32 v50, 2, v49\n\t"
    "v_and_b32 v51, 3, v60\n\t"
    "v_and_b32 v61, 1, v60\n\t"
    "v_lshlrev_b32 v61, 3, v61\n\t"
    "v_lshl_or_b32 v11, v50, 4, v61\n\t"
    "v_lshl_or_b32 v11, v48, 7, v11\n\t"
    "v_xor_b32 v12, v48, v49\n\t"
    "v_lshlrev_b32 v12, 4, v12\n\t"
    "v_lshl_or_b32 v12, v49, 8, v12\n\t"
    "v_lshl_or_b32 v50, v48, 2, v50\n\t"
    "v_or_b32 v51, 0, v51\n\t"
    "v_xor_b32 v51, v51, v50\n\t"
    "v_lshlrev_b32 v13, 4, v51\n\t"
    "v_lshl_or_b32 v13, v50, 8, v13\n\t"
    "v_and_b32 v61, 3, v60\n\t"
    "v_lshlrev_b32 v61, 4, v61\n\t"
    "v_lshl_or_b32 v15, v48, 6, v61\n\t"
    "v_and_b32 v61, 8, v60\n\t"
    "v_lshl_or_b32 v15, v61, 14, v15\n\t"
    "v_and_b32 v61, 4, v60\n\t"
    "v_lshl_or_b32 v15, v61, 8, v15\n\t"
    "v_add_u32 v15, 0, v15\n\t"
    "3:\n\t"
    "v_mov_b32 v16, 0\n\t"
    "v_mov_b32 v17, 0\n\t"
    "v_mov_b32 v18, 0\n\t"
    "v_mov_b32 v19, 0\n\t"
    "v_mov_b32 v20, 0\n\t"
    "v_mov_b32 v21, 0\n\t"
    "v_mov_b32 v22, 0\n\t"
    "v_mov_b32 v23, 0\n\t"
    "s_waitcnt vmcnt(0) lgkmcnt(0)\n\t"
    "14:\n\t"
    "ds_write_b128 v6, v[40:43] offset:0\n\t"
    "ds_write_b128 v6, v[44:47] offset:1024\n\t"
    "v_smfmac_f32_16x16x64_bf16 v[16:19], v[40:43], v[64:71], v9\n\t"
    "v_max3_u32 v60, v40, v41, v42\n\t"
    "v_max3_u32 v61, v43, v44, v45\n\t"
    "v_smfmac_f32_16x16x64_bf16 v[20:23], v[40:43], v[72:79], v9\n\t"
    "v_max3_u32 v60, v60, v46, v47\n\t"
    "v_max_u32 v60, v60, v61\n\t"
    "v_cmp_eq_u32 vcc, -1, v60\n\t"
    "s_cbranch_vccnz 1f\n\t"
    "2:\n\t"
    "s_waitcnt lgkmcnt(0)\n\t"
    "s_barrier\n\t"
    "ds_read_b128 v[40:43], v6 offset:12288\n\t"
    "ds_read_b128 v[48:51], v6 offset:2048\n\t"
    "v_smfmac_f32_16x16x64_bf16 v[16:19], v[44:47], v[80:87], v9\n\t"
    "ds_read_b128 v[52:55], v6 offset:3072\n\t"
    "ds_read_b128 v[56:59], v6 offset:4096\n\t"
    "v_smfmac_f32_16x16x64_bf16 v[20:23], v[44:47], v[88:95], v9\n\t"
    "ds_read_b128 v[60:63], v6 offset:13312\n\t"
    "ds_read_b128 v[44:47], v6 offset:5120\n\t"
    "s_waitcnt lgkmcnt(5)\n\t"
    "v_smfmac_f32_16x16x64_bf16 v[16:19], v[40:43], v[24:31], v9\n\t"
    "ds_read_b128 v[24:27], v7 offset:16384\n\t"
    "ds_read_b128 v[28:31], v7 offset:17408\n\t"
    "v_smfmac_f32_16x16x64_bf16 v[20:23], v[40:43], v[32:39], v9\n\t"
    "ds_read_b128 v[32:35], v7 offset:24576\n\t"
    "ds_read_b128 v[36:39], v7 offset:25600\n\t"
    "ds_read_b128 v[40:43], v6 offset:6144\n\t"
    "s_waitcnt lgkmcnt(9)\n\t"
    "v_smfmac_f32_16x16x64_bf16 v[16:19], v[48:51], v[96:103], v9\n\t"
    "v_smfmac_f32_16x16x64_bf16 v[20:23], v[48:51], v[104:111], v9\n\t"
    "ds_read_b128 v[48:51], v6 offset:7168\n\t"
    "s_waitcnt lgkmcnt(9)\n\t"
    "v_smfmac_f32_16x16x64_bf16 v[16:19], v[52:55], v[112:119], v9\n\t"
    "v_smfmac_f32_16x16x64_bf16 v[20:23], v[52:55], v[120:127], v9\n\t"
    "ds_read_b128 v[52:55], v6 offset:14336\n\t"
    "s_waitcnt lgkmcnt(9)\n\t"
    "v_smfmac_f32_16x16x64_bf16 v[16:19], v[56:59], v[128:135], v9\n\t"
    "v_smfmac_f32_16x16x64_bf16 v[20:23], v[56:59], v[136:143], v9\n\t"
    "ds_read_b128 v[56:59], v6 offset:8192\n\t"
    "s_waitcnt lgkmcnt(6)\n\t"
    "v_smfmac_f32_16x16x64_bf16 v[16:19], v[60:63], v[24:31], v9\n\t"
    "ds_read_b128 v[24:27], v8 offset:0\n\t"
    "ds_read_b128 v[28:31], v8 offset:1024\n\t"
    "s_waitcnt lgkmcnt(6)\n\t"
    "v_smfmac_f32_16x16x64_bf16 v[20:23], v[60:63], v[32:39], v9\n\t"
    "ds_read_b128 v[32:35], v8 offset:8192\n\t"
    "ds_read_b128 v[36:39], v8 offset:9216\n\t"
    "ds_read_b128 v[60:63], v6 offset:9216\n\t"
    "v_smfmac_f32_16x16x64_bf16 v[16:19], v[44:47], v[144:151], v9\n\t"
    "v_smfmac_f32_16x16x64_bf16 v[20:23], v[44:47], v[152:159], v9\n\t"
    "ds_read_b128 v[44:47], v6 offset:10240\n\t"
    "s_waitcnt lgkmcnt(9)\n\t"
    "v_smfmac_f32_16x16x64_bf16 v[16:19], v[40:43], v[160:167], v9\n\t"
    "v_smfmac_f32_16x16x64_bf16 v[20:23], v[40:43], v[168:175], v9\n\t"
    "ds_read_b128 v[40:43], v6 offset:15360\n\t"
    "s_waitcnt lgkmcnt(9)\n\t"
    "v_smfmac_f32_16x16x64_bf16 v[16:19], v[48:51], v[176:183], v9\n\t"
    "v_smfmac_f32_16x16x64_bf16 v[20:23], v[48:51], v[184:191], v9\n\t"
    "ds_read_b128 v[48:51], v6 offset:11264\n\t"
    "s_waitcnt lgkmcnt(6)\n\t"
    "v_smfmac_f32_16x16x64_bf16 v[16:19], v[52:55], v[24:31], v9\n\t"
    "ds_read_b128 v[24:27], v8 offset:16384\n\t"
    "ds_read_b128 v[28:31], v8 offset:17408\n\t"
    "s_waitcnt lgkmcnt(6)\n\t"
    "v_smfmac_f32_16x16x64_bf16 v[20:23], v[52:55], v[32:39], v9\n\t"
    "ds_read_b128 v[32:35], v8 offset:24576\n\t"
    "ds_read_b128 v[36:39], v8 offset:25600\n\t"
    "v_smfmac_f32_16x16x64_bf16 v[16:19], v[56:59], v[192:199], v9\n\t"
    "v_smfmac_f32_16x16x64_bf16 v[20:23], v[56:59], v[200:207], v9\n\t"
    "s_waitcnt lgkmcnt(7)\n\t"
    "v_smfmac_f32_16x16x64_bf16 v[16:19], v[60:63], v[208:215], v9\n\t"
    "v_smfmac_f32_16x16x64_bf16 v[20:23], v[60:63], v[216:223], v9\n\t"
    "s_waitcnt lgkmcnt(6)\n\t"
    "v_smfmac_f32_16x16x64_bf16 v[16:19], v[44:47], v[224:231], v9\n\t"
    "v_smfmac_f32_16x16x64_bf16 v[20:23], v[44:47], v[232:239], v9\n\t"
    "s_waitcnt lgkmcnt(2)\n\t"
    "v_smfmac_f32_16x16x64_bf16 v[16:19], v[40:43], v[24:31], v9\n\t"
    "s_waitcnt lgkmcnt(0)\n\t"
    "v_smfmac_f32_16x16x64_bf16 v[20:23], v[40:43], v[32:39], v9\n\t"
    "v_smfmac_f32_16x16x64_bf16 v[16:19], v[48:51], v[240:247], v9\n\t"
    "v_smfmac_f32_16x16x64_bf16 v[20:23], v[48:51], v[248:255], v9\n\t"
    "v_xor_b32 v7, 64, v12\n\t"
    "v_xor_b32 v8, 0x80, v12\n\t"
    "v_xor_b32 v6, 0xc0, v12\n\t"
    "ds_read_b128 v[24:27], v12 offset:16384\n\t"
    "ds_read_b128 v[28:31], v12 offset:20480\n\t"
    "ds_read_b128 v[32:35], v7 offset:16384\n\t"
    "ds_read_b128 v[36:39], v7 offset:20480\n\t"
    "v_permlane16_swap_b32 v16, v18\n\t"
    "v_permlane16_swap_b32 v17, v19\n\t"
    "v_permlane16_swap_b32 v20, v22\n\t"
    "v_permlane16_swap_b32 v21, v23\n\t"
    "v_add_f32 v16, v16, v18\n\t"
    "v_add_f32 v17, v17, v19\n\t"
    "v_add_f32 v20, v20, v22\n\t"
    "v_mul_f32 v16, %[lsc], v16\n\t"
    "v_mul_f32 v17, %[lsc], v17\n\t"
    "v_cvt_pk_bf16_f32 v16, v16, v17\n\t"
    "v_add_f32 v21, v21, v23\n\t"
    "ds_write_b32 v10, v16 offset:32768\n\t"
    "v_mul_f32 v20, %[lsc], v20\n\t"
    "v_mul_f32 v21, %[lsc], v21\n\t"
    "v_cvt_pk_bf16_f32 v20, v20, v21\n\t"
    "ds_write_b32 v10, v20 offset:33024\n\t"
    "ds_read_b128 v[40:43], v8 offset:16384\n\t"
    "ds_read_b128 v[44:47], v8 offset:20480\n\t"
    "ds_read_b128 v[48:51], v6 offset:16384\n\t"
    "ds_read_b128 v[52:55], v6 offset:20480\n\t"
    "s_waitcnt lgkmcnt(4)\n\t"
    "s_barrier\n\t"
    "ds_read_b64_tr_b16 v[16:17], v11 offset:32768\n\t"
    "ds_read_b64_tr_b16 v[18:19], v11 offset:32832\n\t"
    "ds_read_b64_tr_b16 v[20:21], v11 offset:33280\n\t"
    "ds_read_b64_tr_b16 v[22:23], v11 offset:33344\n\t"
    "ds_read_b64_tr_b16 v[56:57], v11 offset:33792\n\t"
    "ds_read_b64_tr_b16 v[58:59], v11 offset:33856\n\t"
    "ds_read_b64_tr_b16 v[60:61], v11 offset:34304\n\t"
    "ds_read_b64_tr_b16 v[62:63], v11 offset:34368\n\t"
    "v_mov_b32 v6, 0\n\t"
    "v_mov_b32 v7, 0\n\t"
    "v_mov_b32 v8, 0\n\t"
    "v_mov_b32 v9, 0\n\t"
    "s_mov_b32 exec_hi, 0\n\t"
    "global_load_dwordx4 v[6:9], v14, %[vs] sc1\n\t"
    "s_mov_b32 exec_hi, -1\n\t"
    "s_waitcnt lgkmcnt(6)\n\t"
    "v_mfma_f32_16x16x32_bf16 v[24:27], v[24:27], v[16:19], 0\n\t"
    "v_mfma_f32_16x16x32_bf16 v[28:31], v[28:31], v[16:19], 0\n\t"
    "s_waitcnt lgkmcnt(4)\n\t"
    "v_mfma_f32_16x16x32_bf16 v[24:27], v[32:35], v[20:23], v[24:27]\n\t"
    "v_mfma_f32_16x16x32_bf16 v[28:31], v[36:39], v[20:23], v[28:31]\n\t"
    "ds_read_b64_tr_b16 v[32:33], v13 offset:24576\n\t"
    "ds_read_b64_tr_b16 v[34:35], v13 offset:28672\n\t"
    "ds_read_b64_tr_b16 v[36:37], v13 offset:24584\n\t"
    "ds_read_b64_tr_b16 v[38:39], v13 offset:28680\n\t"
    "s_waitcnt lgkmcnt(6)\n\t"
    "v_mfma_f32_16x16x32_bf16 v[24:27], v[40:43], v[56:59], v[24:27]\n\t"
    "v_mfma_f32_16x16x32_bf16 v[28:31], v[44:47], v[56:59], v[28:31]\n\t"
    "s_waitcnt lgkmcnt(4)\n\t"
    "v_mfma_f32_16x16x32_bf16 v[24:27], v[48:51], v[60:63], v[24:27]\n\t"
    "v_mfma_f32_16x16x32_bf16 v[28:31], v[52:55], v[60:63], v[28:31]\n\t"
    "s_setprio 1\n\t"
    "v_mov_b32 v52, %[one]\n\t"
    "v_mov_b32 v53, %[one]\n\t"
    "v_mov_b32 v54, %[one]\n\t"
    "v_mov_b32 v55, %[one]\n\t"
    "s_nop 2\n\t"
    "v_max3_f32 v10, |v24|, |v25|, |v26|\n\t"
    "v_max3_f32 v40, |v27|, |v28|, |v29|\n\t"
    "v_max3_f32 v10, v10, |v30|, |v31|\n\t"
    "v_max_f32 v10, v10, v40\n\t"
    "v_cmp_nge_f32 vcc, %[big], v10\n\t"
    "s_mov_b32 %[bg], %[big]\n\t"
    "v_mov_b32 v10, 0\n\t"
    "s_cbranch_vccnz 12f\n\t"
    "13:\n\t"
    "v_exp_f32 v24, v24\n\t"
    "v_exp_f32 v25, v25\n\t"
    "v_exp_f32 v26, v26\n\t"
    "v_exp_f32 v27, v27\n\t"
    "v_exp_f32 v28, v28\n\t"
    "v_exp_f32 v29, v29\n\t"
    "v_exp_f32 v30, v30\n\t"
    "v_exp_f32 v31, v31\n\t"
    "v_cvt_pk_bf16_f32 v40, v24, v25\n\t"
    "v_cvt_pk_bf16_f32 v41, v26, v27\n\t"
    "v_cvt_pk_bf16_f32 v42, v28, v29\n\t"
    "v_cvt_pk_bf16_f32 v43, v30, v31\n\t"
    "s_setprio 3\n\t"
    "s_waitcnt lgkmcnt(0)\n\t"
    "s_nop 0\n\t"
    "v_mfma_f32_16x16x32_bf16 v[44:47], v[32:35], v[40:43], 0\n\t"
    "v_mfma_f32_16x16x32_bf16 v[48:51], v[36:39], v[40:43], 0\n\t"
    "v_mfma_f32_16x16x32_bf16 v[52:55], v[52:55], v[40:43], 0\n\t"
    "v_mov_b32 v12, 0\n\t"
    "v_mov_b32 v13, 0\n\t"
    "s_mov_b32 exec_hi, 0\n\t"
    "v_mov_b32 v12, %[one]\n\t"
    "v_mov_b32 v13, %[one]\n\t"
    "s_mov_b32 exec_hi, -1\n\t"
    "s_barrier\n\t"
    "ds_read_b64_tr_b16 v[24:25], v11 offset:34816\n\t"
    "ds_read_b64_tr_b16 v[26:27], v11 offset:34880\n\t"
    "ds_read_b64_tr_b16 v[28:29], v11 offset:35328\n\t"
    "ds_read_b64_tr_b16 v[30:31], v11 offset:35392\n\t"
    "ds_read_b64_tr_b16 v[32:33], v11 offset:35840\n\t"
    "ds_read_b64_tr_b16 v[34:35], v11 offset:35904\n\t"
    "ds_read_b64_tr_b16 v[36:37], v11 offset:36352\n\t"
    "ds_read_b64_tr_b16 v[38:39], v11 offset:36416\n\t"
    "s_waitcnt lgkmcnt(6)\n\t"
    "v_mfma_f32_16x16x32_bf16 v[40:43], v[24:27], v[16:19], 0\n\t"
    "s_waitcnt lgkmcnt(4)\n\t"
    "v_mfma_f32_16x16x32_bf16 v[40:43], v[28:31], v[20:23], v[40:43]\n\t"
    "s_waitcnt lgkmcnt(2)\n\t"
    "v_mfma_f32_16x16x32_bf16 v[40:43], v[32:35], v[56:59], v[40:43]\n\t"
    "s_waitcnt lgkmcnt(0)\n\t"
    "v_mfma_f32_16x16x32_bf16 v[40:43], v[36:39], v[60:63], v[40:43]\n\t"
    "5:\n\t"
    "s_waitcnt vmcnt(0)\n\t"
    "v_max3_u32 v11, v6, v7, v8\n\t"
    "v_max_u32 v11, v11, v9\n\t"
    "v_cmp_eq_u32 vcc, -1, v11\n\t"
    "s_cbranch_vccnz 4f\n\t"
    "6:\n\t"
    "s_nop 2\n\t"
    "v_exp_f32 v28, v40\n\t"
    "v_max3_f32 v11, v40, v41, v42\n\t"
    "v_exp_f32 v29, v41\n\t"
    "v_max_f32 v11, v11, v43\n\t"
    "v_exp_f32 v30, v42\n\t"
    "v_cmp_nge_f32 vcc, %[bg], v11\n\t"
    "v_exp_f32 v31, v43\n\t"
    "s_cbranch_vccnz 10f\n\t"
    "11:\n\t"
    "v_cvt_pk_bf16_f32 v24, v28, v29\n\t"
    "v_cvt_pk_bf16_f32 v25, v30, v31\n\t"
    "s_nop 1\n\t"
    "v_mfma_f32_16x16x16_bf16 v[52:55], v[12:13], v[24:25], v[52:55]\n\t"
    "v_mfma_f32_16x16x16_bf16 v[44:47], v[6:7], v[24:25], v[44:47]\n\t"
    "v_mfma_f32_16x16x16_bf16 v[48:51], v[8:9], v[24:25], v[48:51]\n\t"
    "s_nop 5\n\t"
    "v_rcp_f32 v26, v52\n\t"
    "s_nop 0\n\t"
    "v_pk_mul_f32 v[44:45], v[44:45], v[26:27] op_sel_hi:[1,0]\n\t"
    "v_pk_mul_f32 v[46:47], v[46:47], v[26:27] op_sel_hi:[1,0]\n\t"
    "v_pk_mul_f32 v[48:49], v[48:49], v[26:27] op_sel_hi:[1,0]\n\t"
    "v_pk_mul_f32 v[50:51], v[50:51], v[26:27] op_sel_hi:[1,0]\n\t"
    "v_cvt_pk_bf16_f32 v36, v44, v45\n\t"
    "v_cvt_pk_bf16_f32 v37, v46, v47\n\t"
    "v_cvt_pk_bf16_f32 v38, v48, v49\n\t"
    "v_cvt_pk_bf16_f32 v39, v50, v51\n\t"
    "v_min_u32 v36, -2, v36\n\t"
    "v_min_u32 v37, -2, v37\n\t"
    "v_min_u32 v38, -2, v38\n\t"
    "v_min_u32 v39, -2, v39\n\t"
    "global_store_dwordx4 v15, v[36:39], %[od] sc1\n\t"
    "s_barrier\n\t"
    "s_mov_b32 exec_hi, 0\n\t"
    "global_store_dwordx4 v14, v[6:9], %[kv] sc1\n\t"
    "s_mov_b32 exec_hi, -1\n\t"
    "s_nop 0\n\t"
    "s_waitcnt lgkmcnt(0)\n\t"
    "s_branch 9f\n\t"
    "1:\n\t"
    "s_add_u32 %[sp], %[sp], 1\n\t"
    "s_cmp_ge_u32 %[sp], %[cap]\n\t"
    "s_cbranch_scc1 7f\n\t"
    "global_load_dwordx4 v[40:43], v6, %[ag] sc1\n\t"
    "global_load_dwordx4 v[44:47], v6, %[ag] offset:1024 sc1\n\t"
    "s_waitcnt lgkmcnt(0)\n\t"
    "s_memtime %[tc]\n\t"
    "s_nop 7\n\t"
    "s_branch 3b\n\t"
    "7:\n\t"
    "s_mov_b32 %[cap], 0\n\t"
    "s_branch 2b\n\t"
    "4:\n\t"
    "s_add_u32 %[sp], %[sp], 0x10000\n\t"
    "s_lshr_b32 %[st], %[sp], 16\n\t"
    "s_cmp_ge_u32 %[st], %[cap]\n\t"
    "s_cbranch_scc1 8f\n\t"
    "s_mov_b32 exec_hi, 0\n\t"
    "global_load_dwordx4 v[6:9], v14, %[vs] sc1\n\t"
    "s_mov_b32 exec_hi, -1\n\t"
    "s_branch 5b\n\t"
    "8:\n\t"
    "s_mov_b32 %[cap], 0\n\t"
    "s_branch 6b\n\t"
    "10:\n\t"
    "v_max_f32 v11, v11, v10\n\t"
    "v_mov_b32 v26, v11\n\t"
    "s_nop 1\n\t"
    "v_permlane16_swap_b32 v11, v26\n\t"
    "v_max_f32 v11, v11, v26\n\t"
    "v_sub_f32 v28, v40, v11\n\t"
    "v_sub_f32 v29, v41, v11\n\t"
    "v_sub_f32 v30, v42, v11\n\t"
    "v_sub_f32 v31, v43, v11\n\t"
    "v_sub_f32 v11, v10, v11\n\t"
    "v_exp_f32 v11, v11\n\t"
    "v_exp_f32 v28, v28\n\t"
    "v_exp_f32 v29, v29\n\t"
    "v_exp_f32 v30, v30\n\t"
    "v_exp_f32 v31, v31\n\t"
    "v_mul_f32 v44, v44, v11\n\t"
    "v_mul_f32 v45, v45, v11\n\t"
    "v_mul_f32 v46, v46, v11\n\t"
    "v_mul_f32 v47, v47, v11\n\t"
    "v_mul_f32 v48, v48, v11\n\t"
    "v_mul_f32 v49, v49, v11\n\t"
    "v_mul_f32 v50, v50, v11\n\t"
    "v_mul_f32 v51, v51, v11\n\t"
    "v_mul_f32 v52, v52, v11\n\t"
    "v_mul_f32 v53, v53, v11\n\t"
    "v_mul_f32 v54, v54, v11\n\t"
    "v_mul_f32 v55, v55, v11\n\t"
    "s_branch 11b\n\t"
    "12:\n\t"
    "v_max3_f32 v10, v24, v25, v26\n\t"
    "v_max3_f32 v40, v27, v28, v29\n\t"
    "v_max3_f32 v10, v10, v30, v31\n\t"
    "v_max3_f32 v10, v10, v40, %[neg]\n\t"
    "v_mov_b32 v40, v10\n\t"
    "s_nop 1\n\t"
    "v_permlane16_swap_b32 v10, v40\n\t"
    "v_max_f32 v10, v10, v40\n\t"
    "v_mov_b32 v40, v10\n\t"
    "s_nop 1\n\t"
    "v_permlane32_swap_b32 v10, v40\n\t"
    "v_max_f32 v10, v10, v40\n\t"
    "v_sub_f32 v24, v24, v10\n\t"
    "v_sub_f32 v25, v25, v10\n\t"
    "v_sub_f32 v26, v26, v10\n\t"
    "v_sub_f32 v27, v27, v10\n\t"
    "v_sub_f32 v28, v28, v10\n\t"
    "v_sub_f32 v29, v29, v10\n\t"
    "v_sub_f32 v30, v30, v10\n\t"
    "v_sub_f32 v31, v31, v10\n\t"
    "s_mov_b32 %[bg], 0x7fc00000\n\t"
    "s_branch 13b\n\t"
    "9:"
    : [tc] "=&s"(tc), [sp] "=&s"(sp), [cap] "+s"(cap), [st] "=&s"(st), [bg] "=&s"(bg)
    : [ag] "s"(ag), [c44] "s"(0x4444u), [kv] "s"(kv), [vs] "s"(vs), [od] "s"(od), [od2] "s"(od + 262144), [neg] "s"(-1.0e38f), [lsc] "s"(LSC), [one] "s"(0x3F803F80u), [big] "s"(24.0f), "{v[64:95]}"(W.w0), "{v[96:127]}"(W.w1), "{v[128:159]}"(W.w2), "{v[160:191]}"(W.w3), "{v[192:223]}"(W.w4), "{v[224:255]}"(W.w5)
    : "v6", "v7", "v8", "v9", "v10", "v11", "v12", "v13", "v14", "v15", "v16", "v17", "v18", "v19", "v20", "v21", "v22", "v23", "v24", "v25", "v26", "v27", "v28", "v29", "v30", "v31", "v32", "v33", "v34", "v35", "v36", "v37", "v38", "v39", "v40", "v41", "v42", "v43", "v44", "v45", "v46", "v47", "v48", "v49", "v50", "v51", "v52", "v53", "v54", "v55", "v56", "v57", "v58", "v59", "v60", "v61", "v62", "v63", "vcc", "scc", "memory");
}
template <> __device__ __forceinline__ void qk_step<1>(const QKW& W, const char* ag, const char* vs, const char* od, const char* kv, unsigned& cap, unsigned& sp,
                                                        unsigned long long& tc, unsigned long long* ts, unsigned& d0, unsigned& d1) {
  unsigned st, bg;
  asm volatile("v_mbcnt_lo_u32_b32 v60, -1, 0\n\t"
    "v_mbcnt_hi_u32_b32 v60, -1, v60\n\t"
    "v_lshlrev_b32 v6, 4, v60\n\t"
    "s_mov_b32 %[sp], 0\n\t"
    "s_setprio 3\n\t"
    "global_load_dwordx4 v[40:43], v6, %[ag] sc1\n\t"
    "global_load_dwordx4 v[44:47], v6, %[ag] offset:1024 sc1\n\t"
    "s_memtime %[tc]\n\t"
    "v_add_u32 v7, 43008, v6\n\t"
    "v_add_u32 v8, 100352, v6\n\t"
    "ds_read_b128 v[24:27], v7 offset:0\n\t"
    "ds_read_b128 v[28:31], v7 offset:1024\n\t"
    "ds_read_b128 v[32:35], v7 offset:8192\n\t"
    "ds_read_b128 v[36:39], v7 offset:9216\n\t"
    "v_lshrrev_b32 v48, 4, v60\n\t"
    "v_and_b32 v49, 15, v60\n\t"
    "v_and_b32 v61, 4, v60\n\t"
    "v_mul_u32_u24 v61, 0x2aaa, v61\n\t"
    "v_lshrrev_b32 v9, 1, v60\n\t"
    "v_and_b32 v9, 2, v9\n\t"
    "v_or3_b32 v9, v9, v61, %[c44]\n\t"
    "v_add_u32 v14, 512, v6\n\t"
    "v_lshlrev_b32 v61, 2, v48\n\t"
    "v_lshl_or_b32 v10, v49, 4, v61\n\t"
    "v_lshrrev_b32 v50, 2, v49\n\t"
    "v_and_b32 v51, 3, v60\n\t"
    "v_and_b32 v61, 1, v60\n\t"
    "v_lshlrev_b32 v61, 3, v61\n\t"
    "v_lshl_or_b32 v11, v50, 4, v61\n\t"
    "v_lshl_or_b32 v11, v48, 7, v11\n\t"
    "v_xor_b32 v12, v48, v49\n\t"
    "v_lshlrev_b32 v12, 4, v12\n\t"
    "v_lshl_or_b32 v12, v49, 8, v12\n\t"
    "v_lshl_or_b32 v50, v48, 2, v50\n\t"
    "v_or_b32 v51, 4, v51\n\t"
    "v_xor_b32 v51, v51, v50\n\t"
    "v_lshlrev_b32 v13, 4, v51\n\t"
    "v_lshl_or_b32 v13, v50, 8, v13\n\t"
    "v_and_b32 v61, 3, v60\n\t"
    "v_lshlrev_b32 v61, 4, v61\n\t"
    "v_lshl_or_b32 v15, v48, 6, v61\n\t"
    "v_and_b32 v61, 8, v60\n\t"
    "v_lshl_or_b32 v15, v61, 14, v15\n\t"
    "v_and_b32 v61, 4, v60\n\t"
    "v_lshl_or_b32 v15, v61, 8, v15\n\t"
    "v_add_u32 v15, 256, v15\n\t"
    "3:\n\t"
    "v_mov_b32 v16, 0\n\t"
    "v_mov_b32 v17, 0\n\t"
    "v_mov_b32 v18, 0\n\t"
    "v_mov_b32 v19, 0\n\t"
    "v_mov_b32 v20, 0\n\t"
    "v_mov_b32 v21, 0\n\t"
    "v_mov_b32 v22, 0\n\t"
    "v_mov_b32 v23, 0\n\t"
    "s_waitcnt vmcnt(0) lgkmcnt(0)\n\t"
    "14:\n\t"
    "ds_write_b128 v6, v[40:43] offset:2048\n\t"
    "ds_write_b128 v6, v[44:47] offset:3072\n\t"
    "v_smfmac_f32_16x16x64_bf16 v[16:19], v[40:43], v[96:103], v9\n\t"
    "v_max3_u32 v60, v40, v41, v42\n\t"
    "v_max3_u32 v61, v43, v44, v45\n\t"
    "v_smfmac_f32_16x16x64_bf16 v[20:23], v[40:43], v[104:111], v9\n\t"
    "v_max3_u32 v60, v60, v46, v47\n\t"
    "v_max_u32 v60, v60, v61\n\t"
    "v_cmp_eq_u32 vcc, -1, v60\n\t"
    "s_cbranch_vccnz 1f\n\t"
    "2:\n\t"
    "s_waitcnt lgkmcnt(0)\n\t"
    "s_barrier\n\t"
    "ds_read_b128 v[40:43], v6 offset:12288\n\t"
    "ds_read_b128 v[48:51], v6 offset:0\n\t"
    "v_smfmac_f32_16x16x64_bf16 v[16:19], v[44:47], v[112:119], v9\n\t"
    "ds_read_b128 v[52:55], v6 offset:1024\n\t"
    "ds_read_b128 v[56:59], v6 offset:4096\n\t"
    "v_smfmac_f32_16x16x64_bf16 v[20:23], v[44:47], v[120:127], v9\n\t"
    "ds_read_b128 v[60:63], v6 offset:13312\n\t"
    "ds_read_b128 v[44:47], v6 offset:5120\n\t"
    "s_waitcnt lgkmcnt(5)\n\t"
    "v_smfmac_f32_16x16x64_bf16 v[16:19], v[40:43], v[24:31], v9\n\t"
    "ds_read_b128 v[24:27], v7 offset:16384\n\t"
    "ds_read_b128 v[28:31], v7 offset:17408\n\t"
    "v_smfmac_f32_16x16x64_bf16 v[20:23], v[40:43], v[32:39], v9\n\t"
    "ds_read_b128 v[32:35], v7 offset:24576\n\t"
    "ds_read_b128 v[36:39], v7 offset:25600\n\t"
    "ds_read_b128 v[40:43], v6 offset:6144\n\t"
    "s_waitcnt lgkmcnt(9)\n\t"
    "v_smfmac_f32_16x16x64_bf16 v[16:19], v[48:51], v[64:71], v9\n\t"
    "v_smfmac_f32_16x16x64_bf16 v[20:23], v[48:51], v[72:79], v9\n\t"
    "ds_read_b128 v[48:51], v6 offset:7168\n\t"
    "s_waitcnt lgkmcnt(9)\n\t"
    "v_smfmac_f32_16x16x64_bf16 v[16:19], v[52:55], v[80:87], v9\n\t"
    "v_smfmac_f32_16x16x64_bf16 v[20:23], v[52:55], v[88:95], v9\n\t"
    "ds_read_b128 v[52:55], v6 offset:14336\n\t"
    "s_waitcnt lgkmcnt(9)\n\t"
    "v_smfmac_f32_16x16x64_bf16 v[16:19], v[56:59], v[128:135], v9\n\t"
    "v_smfmac_f32_16x16x64_bf16 v[20:23], v[56:59], v[136:143], v9\n\t"
    "ds_read_b128 v[56:59], v6 offset:8192\n\t"
    "s_waitcnt lgkmcnt(6)\n\t"
    "v_smfmac_f32_16x16x64_bf16 v[16:19], v[60:63], v[24:31], v9\n\t"
    "ds_read_b128 v[24:27], v8 offset:0\n\t"
    "ds_read_b128 v[28:31], v8 offset:1024\n\t"
    "s_waitcnt lgkmcnt(6)\n\t"
    "v_smfmac_f32_16x16x64_bf16 v[20:23], v[60:63], v[32:39], v9\n\t"
    "ds_read_b128 v[32:35], v8 offset:8192\n\t"
    "ds_read_b128 v[36:39], v8 offset:9216\n\t"
    "ds_read_b128 v[60:63], v6 offset:9216\n\t"
    "v_smfmac_f32_16x16x64_bf16 v[16:19], v[44:47], v[144:151], v9\n\t"
    "v_smfmac_f32_16x16x64_bf16 v[20:23], v[44:47], v[152:159], v9\n\t"
    "ds_read_b128 v[44:47], v6 offset:10240\n\t"
    "s_waitcnt lgkmcnt(9)\n\t"
    "v_smfmac_f32_16x16x64_bf16 v[16:19], v[40:43], v[160:167], v9\n\t"
    "v_smfmac_f32_16x16x64_bf16 v[20:23], v[40:43], v[168:175], v9\n\t"
    "ds_read_b128 v[40:43], v6 offset:15360\n\t"
    "s_waitcnt lgkmcnt(9)\n\t"
    "v_smfmac_f32_16x16x64_bf16 v[16:19], v[48:51], v[176:183], v9\n\t"
    "v_smfmac_f32_16x16x64_bf16 v[20:23], v[48:51], v[184:191], v9\n\t"
    "ds_read_b128 v[48:51], v6 offset:11264\n\t"
    "s_waitcnt lgkmcnt(6)\n\t"
    "v_smfmac_f32_16x16x64_bf16 v[16:19], v[52:55], v[24:31], v9\n\t"
    "ds_read_b128 v[24:27], v8 offset:16384\n\t"
    "ds_read_b128 v[28:31], v8 offset:17408\n\t"
    "s_waitcnt lgkmcnt(6)\n\t"
    "v_smfmac_f32_16x16x64_bf16 v[20:23], v[52:55], v[32:39], v9\n\t"
    "ds_read_b128 v[32:35], v8 offset:24576\n\t"
    "ds_read_b128 v[36:39], v8 offset:25600\n\t"
    "v_smfmac_f32_16x16x64_bf16 v[16:19], v[56:59], v[192:199], v9\n\t"
    "v_smfmac_f32_16x16x64_bf16 v[20:23], v[56:59], v[200:207], v9\n\t"
    "s_waitcnt lgkmcnt(7)\n\t"
    "v_smfmac_f32_16x16x64_bf16 v[16:19], v[60:63], v[208:215], v9\n\t"
    "v_smfmac_f32_16x16x64_bf16 v[20:23], v[60:63], v[216:223], v9\n\t"
    "s_waitcnt lgkmcnt(6)\n\t"
    "v_smfmac_f32_16x16x64_bf16 v[16:19], v[44:47], v[224:231], v9\n\t"
    "v_smfmac_f32_16x16x64_bf16 v[20:23], v[44:47], v[232:239], v9\n\t"
    "s_waitcnt lgkmcnt(2)\n\t"
    "v_smfmac_f32_16x16x64_bf16 v[16:19], v[40:43], v[24:31], v9\n\t"
    "s_waitcnt lgkmcnt(0)\n\t"
    "v_smfmac_f32_16x16x64_bf16 v[20:23], v[40:43], v[32:39], v9\n\t"
    "v_smfmac_f32_16x16x64_bf16 v[16:19], v[48:51], v[240:247], v9\n\t"
    "v_smfmac_f32_16x16x64_bf16 v[20:23], v[48:51], v[248:255], v9\n\t"
    "v_xor_b32 v7, 64, v12\n\t"
    "v_xor_b32 v8, 0x80, v12\n\t"
    "v_xor_b32 v6, 0xc0, v12\n\t"
    "ds_read_b128 v[24:27], v12 offset:16384\n\t"
    "ds_read_b128 v[28:31], v12 offset:20480\n\t"
    "ds_read_b128 v[32:35], v7 offset:16384\n\t"
    "ds_read_b128 v[36:39], v7 offset:20480\n\t"
    "v_permlane16_swap_b32 v16, v18\n\t"
    "v_permlane16_swap_b32 v17, v19\n\t"
    "v_permlane16_swap_b32 v20, v22\n\t"
    "v_permlane16_swap_b32 v21, v23\n\t"
    "v_add_f32 v16, v16, v18\n\t"
    "v_add_f32 v17, v17, v19\n\t"
    "v_add_f32 v20, v20, v22\n\t"
    "v_mul_f32 v16, %[lsc], v16\n\t"
    "v_mul_f32 v17, %[lsc], v17\n\t"
    "v_cvt_pk_bf16_f32 v16, v16, v17\n\t"
    "v_add_f32 v21, v21, v23\n\t"
    "ds_write_b32 v10, v16 offset:33280\n\t"
    "v_mul_f32 v20, %[lsc], v20\n\t"
    "v_mul_f32 v21, %[lsc], v21\n\t"
    "v_cvt_pk_bf16_f32 v20, v20, v21\n\t"
    "ds_write_b32 v10, v20 offset:33536\n\t"
    "ds_read_b128 v[40:43], v8 offset:16384\n\t"
    "ds_read_b128 v[44:47], v8 offset:20480\n\t"
    "ds_read_b128 v[48:51], v6 offset:16384\n\t"
    "ds_read_b128 v[52:55], v6 offset:20480\n\t"
    "s_waitcnt lgkmcnt(4)\n\t"
    "s_barrier\n\t"
    "ds_read_b64_tr_b16 v[16:17], v11 offset:32768\n\t"
    "ds_read_b64_tr_b16 v[18:19], v11 offset:32832\n\t"
    "ds_read_b64_tr_b16 v[20:21], v11 offset:33280\n\t"
    "ds_read_b64_tr_b16 v[22:23], v11 offset:33344\n\t"
    "ds_read_b64_tr_b16 v[56:57], v11 offset:33792\n\t"
    "ds_read_b64_tr_b16 v[58:59], v11 offset:33856\n\t"
    "ds_read_b64_tr_b16 v[60:61], v11 offset:34304\n\t"
    "ds_read_b64_tr_b16 v[62:63], v11 offset:34368\n\t"
    "v_mov_b32 v6, 0\n\t"
    "v_mov_b32 v7, 0\n\t"
    "v_mov_b32 v8, 0\n\t"
    "v_mov_b32 v9, 0\n\t"
    "s_mov_b32 exec_hi, 0\n\t"
    "global_load_dwordx4 v[6:9], v14, %[vs] sc1\n\t"
    "s_mov_b32 exec_hi, -1\n\t"
    "s_waitcnt lgkmcnt(6)\n\t"
    "v_mfma_f32_16x16x32_bf16 v[24:27], v[24:27], v[16:19], 0\n\t"
    "v_mfma_f32_16x16x32_bf16 v[28:31], v[28:31], v[16:19], 0\n\t"
    "s_waitcnt lgkmcnt(4)\n\t"
    "v_mfma_f32_16x16x32_bf16 v[24:27], v[32:35], v[20:23], v[24:27]\n\t"
    "v_mfma_f32_16x16x32_bf16 v[28:31], v[36:39], v[20:23], v[28:31]\n\t"
    "ds_read_b64_tr_b16 v[32:33], v13 offset:24576\n\t"
    "ds_read_b64_tr_b16 v[34:35], v13 offset:28672\n\t"
    "ds_read_b64_tr_b16 v[36:37], v13 offset:24584\n\t"
    "ds_read_b64_tr_b16 v[38:39], v13 offset:28680\n\t"
    "s_waitcnt lgkmcnt(6)\n\t"
    "v_mfma_f32_16x16x32_bf16 v[24:27], v[40:43], v[56:59], v[24:27]\n\t"
    "v_mfma_f32_16x16x32_bf16 v[28:31], v[44:47], v[56:59], v[28:31]\n\t"
    "s_waitcnt lgkmcnt(4)\n\t"
    "v_mfma_f32_16x16x32_bf16 v[24:27], v[48:51], v[60:63], v[24:27]\n\t"
    "v_mfma_f32_16x16x32_bf16 v[28:31], v[52:55], v[60:63], v[28:31]\n\t"
    "s_setprio 1\n\t"
    "v_mov_b32 v52, %[one]\n\t"
    "v_mov_b32 v53, %[one]\n\t"
    "v_mov_b32 v54, %[one]\n\t"
    "v_mov_b32 v55, %[one]\n\t"
    "s_nop 2\n\t"
    "v_max3_f32 v10, |v24|, |v25|, |v26|\n\t"
    "v_max3_f32 v40, |v27|, |v28|, |v29|\n\t"
    "v_max3_f32 v10, v10, |v30|, |v31|\n\t"
    "v_max_f32 v10, v10, v40\n\t"
    "v_cmp_nge_f32 vcc, %[big], v10\n\t"
    "s_mov_b32 %[bg], %[big]\n\t"
    "v_mov_b32 v10, 0\n\t"
    "s_cbranch_vccnz 12f\n\t"
    "13:\n\t"
    "v_exp_f32 v24, v24\n\t"
    "v_exp_f32 v25, v25\n\t"
    "v_exp_f32 v26, v26\n\t"
    "v_exp_f32 v27, v27\n\t"
    "v_exp_f32 v28, v28\n\t"
    "v_exp_f32 v29, v29\n\t"
    "v_exp_f32 v30, v30\n\t"
    "v_exp_f32 v31, v31\n\t"
    "v_cvt_pk_bf16_f32 v40, v24, v25\n\t"
    "v_cvt_pk_bf16_f32 v41, v26, v27\n\t"
    "v_cvt_pk_bf16_f32 v42, v28, v29\n\t"
    "v_cvt_pk_bf16_f32 v43, v30, v31\n\t"
    "s_setprio 3\n\t"
    "s_waitcnt lgkmcnt(0)\n\t"
    "s_nop 0\n\t"
    "v_mfma_f32_16x16x32_bf16 v[44:47], v[32:35], v[40:43], 0\n\t"
    "v_mfma_f32_16x16x32_bf16 v[48:51], v[36:39], v[40:43], 0\n\t"
    "v_mfma_f32_16x16x32_bf16 v[52:55], v[52:55], v[40:43], 0\n\t"
    "v_mov_b32 v12, 0\n\t"
    "v_mov_b32 v13, 0\n\t"
    "s_mov_b32 exec_hi, 0\n\t"
    "v_mov_b32 v12, %[one]\n\t"
    "v_mov_b32 v13, %[one]\n\t"
    "s_mov_b32 exec_hi, -1\n\t"
    "s_barrier\n\t"
    "ds_read_b64_tr_b16 v[24:25], v11 offset:34816\n\t"
    "ds_read_b64_tr_b16 v[26:27], v11 offset:34880\n\t"
    "ds_read_b64_tr_b16 v[28:29], v11 offset:35328\n\t"
    "ds_read_b64_tr_b16 v[30:31], v11 offset:35392\n\t"
    "ds_read_b64_tr_b16 v[32:33], v11 offset:35840\n\t"
    "ds_read_b64_tr_b16 v[34:35], v11 offset:35904\n\t"
    "ds_read_b64_tr_b16 v[36:37], v11 offset:36352\n\t"
    "ds_read_b64_tr_b16 v[38:39], v11 offset:36416\n\t"
    "s_waitcnt lgkmcnt(6)\n\t"
    "v_mfma_f32_16x16x32_bf16 v[40:43], v[24:27], v[16:19], 0\n\t"
    "s_waitcnt lgkmcnt(4)\n\t"
    "v_mfma_f32_16x16x32_bf16 v[40:43], v[28:31], v[20:23], v[40:43]\n\t"
    "s_waitcnt lgkmcnt(2)\n\t"
    "v_mfma_f32_16x16x32_bf16 v[40:43], v[32:35], v[56:59], v[40:43]\n\t"
    "s_waitcnt lgkmcnt(0)\n\t"
    "v_mfma_f32_16x16x32_bf16 v[40:43], v[36:39], v[60:63], v[40:43]\n\t"
    "5:\n\t"
    "s_waitcnt vmcnt(0)\n\t"
    "v_max3_u32 v11, v6, v7, v8\n\t"
    "v_max_u32 v11, v11, v9\n\t"
    "v_cmp_eq_u32 vcc, -1, v11\n\t"
    "s_cbranch_vccnz 4f\n\t"
    "6:\n\t"
    "s_nop 2\n\t"
    "v_exp_f32 v28, v40\n\t"
    "v_max3_f32 v11, v40, v41, v42\n\t"
    "v_exp_f32 v29, v41\n\t"
    "v_max_f32 v11, v11, v43\n\t"
    "v_exp_f32 v30, v42\n\t"
    "v_cmp_nge_f32 vcc, %[bg], v11\n\t"
    "v_exp_f32 v31, v43\n\t"
    "s_cbranch_vccnz 10f\n\t"
    "11:\n\t"
    "v_cvt_pk_bf16_f32 v24, v28, v29\n\t"
    "v_cvt_pk_bf16_f32 v25, v30, v31\n\t"
    "s_nop 1\n\t"
    "v_mfma_f32_16x16x16_bf16 v[52:55], v[12:13], v[24:25], v[52:55]\n\t"
    "v_mfma_f32_16x16x16_bf16 v[44:47], v[6:7], v[24:25], v[44:47]\n\t"
    "v_mfma_f32_16x16x16_bf16 v[48:51], v[8:9], v[24:25], v[48:51]\n\t"
    "s_nop 5\n\t"
    "v_rcp_f32 v26, v52\n\t"
    "s_nop 0\n\t"
    "v_pk_mul_f32 v[44:45], v[44:45], v[26:27] op_sel_hi:[1,0]\n\t"
    "v_pk_mul_f32 v[46:47], v[46:47], v[26:27] op_sel_hi:[1,0]\n\t"
    "v_pk_mul_f32 v[48:49], v[48:49], v[26:27] op_sel_hi:[1,0]\n\t"
    "v_pk_mul_f32 v[50:51], v[50:51], v[26:27] op_sel_hi:[1,0]\n\t"
    "v_cvt_pk_bf16_f32 v36, v44, v45\n\t"
    "v_cvt_pk_bf16_f32 v37, v46, v47\n\t"
    "v_cvt_pk_bf16_f32 v38, v48, v49\n\t"
    "v_cvt_pk_bf16_f32 v39, v50, v51\n\t"
    "v_min_u32 v36, -2, v36\n\t"
    "v_min_u32 v37, -2, v37\n\t"
    "v_min_u32 v38, -2, v38\n\t"
    "v_min_u32 v39, -2, v39\n\t"
    "global_store_dwordx4 v15, v[36:39], %[od] sc1\n\t"
    "s_barrier\n\t"
    "s_mov_b32 exec_hi, 0\n\t"
    "global_store_dwordx4 v14, v[6:9], %[kv] sc1\n\t"
    "s_mov_b32 exec_hi, -1\n\t"
    "s_nop 0\n\t"
    "s_waitcnt lgkmcnt(0)\n\t"
    "s_branch 9f\n\t"
    "1:\n\t"
    "s_add_u32 %[sp], %[sp], 1\n\t"
    "s_cmp_ge_u32 %[sp], %[cap]\n\t"
    "s_cbranch_scc1 7f\n\t"
    "global_load_dwordx4 v[40:43], v6, %[ag] sc1\n\t"
    "global_load_dwordx4 v[44:47], v6, %[ag] offset:1024 sc1\n\t"
    "s_waitcnt lgkmcnt(0)\n\t"
    "s_memtime %[tc]\n\t"
    "s_nop 7\n\t"
    "s_branch 3b\n\t"
    "7:\n\t"
    "s_mov_b32 %[cap], 0\n\t"
    "s_branch 2b\n\t"
    "4:\n\t"
    "s_add_u32 %[sp], %[sp], 0x10000\n\t"
    "s_lshr_b32 %[st], %[sp], 16\n\t"
    "s_cmp_ge_u32 %[st], %[cap]\n\t"
    "s_cbranch_scc1 8f\n\t"
    "s_mov_b32 exec_hi, 0\n\t"
    "global_load_dwordx4 v[6:9], v14, %[vs] sc1\n\t"
    "s_mov_b32 exec_hi, -1\n\t"
    "s_branch 5b\n\t"
    "8:\n\t"
    "s_mov_b32 %[cap], 0\n\t"
    "s_branch 6b\n\t"
    "10:\n\t"
    "v_max_f32 v11, v11, v10\n\t"
    "v_mov_b32 v26, v11\n\t"
    "s_nop 1\n\t"
    "v_permlane16_swap_b32 v11, v26\n\t"
    "v_max_f32 v11, v11, v26\n\t"
    "v_sub_f32 v28, v40, v11\n\t"
    "v_sub_f32 v29, v41, v11\n\t"
    "v_sub_f32 v30, v42, v11\n\t"
    "v_sub_f32 v31, v43, v11\n\t"
    "v_sub_f32 v11, v10, v11\n\t"
    "v_exp_f32 v11, v11\n\t"
    "v_exp_f32 v28, v28\n\t"
    "v_exp_f32 v29, v29\n\t"
    "v_exp_f32 v30, v30\n\t"
    "v_exp_f32 v31, v31\n\t"
    "v_mul_f32 v44, v44, v11\n\t"
    "v_mul_f32 v45, v45, v11\n\t"
    "v_mul_f32 v46, v46, v11\n\t"
    "v_mul_f32 v47, v47, v11\n\t"
    "v_mul_f32 v48, v48, v11\n\t"
    "v_mul_f32 v49, v49, v11\n\t"
    "v_mul_f32 v50, v50, v11\n\t"
    "v_mul_f32 v51, v51, v11\n\t"
    "v_mul_f32 v52, v52, v11\n\t"
    "v_mul_f32 v53, v53, v11\n\t"
    "v_mul_f32 v54, v54, v11\n\t"
    "v_mul_f32 v55, v55, v11\n\t"
    "s_branch 11b\n\t"
    "12:\n\t"
    "v_max3_f32 v10, v24, v25, v26\n\t"
    "v_max3_f32 v40, v27, v28, v29\n\t"
    "v_max3_f32 v10, v10, v30, v31\n\t"
    "v_max3_f32 v10, v10, v40, %[neg]\n\t"
    "v_mov_b32 v40, v10\n\t"
    "s_nop 1\n\t"
    "v_permlane16_swap_b32 v10, v40\n\t"
    "v_max_f32 v10, v10, v40\n\t"
    "v_mov_b32 v40, v10\n\t"
    "s_nop 1\n\t"
    "v_permlane32_swap_b32 v10, v40\n\t"
    "v_max_f32 v10, v10, v40\n\t"
    "v_sub_f32 v24, v24, v10\n\t"
    "v_sub_f32 v25, v25, v10\n\t"
    "v_sub_f32 v26, v26, v10\n\t"
    "v_sub_f32 v27, v27, v10\n\t"
    "v_sub_f32 v28, v28, v10\n\t"
    "v_sub_f32 v29, v29, v10\n\t"
    "v_sub_f32 v30, v30, v10\n\t"
    "v_sub_f32 v31, v31, v10\n\t"
    "s_mov_b32 %[bg], 0x7fc00000\n\t"
    "s_branch 13b\n\t"
    "9:"
    : [tc] "=&s"(tc), [sp] "=&s"(sp), [cap] "+s"(cap), [st] "=&s"(st), [bg] "=&s"(bg)
    : [ag] "s"(ag), [c44] "s"(0x4444u), [kv] "s"(kv), [vs] "s"(vs), [od] "s"(od), [od2] "s"(od + 262144), [neg] "s"(-1.0e38f), [lsc] "s"(LSC), [one] "s"(0x3F803F80u), [big] "s"(24.0f), "{v[64:95]}"(W.w0), "{v[96:127]}"(W.w1), "{v[128:159]}"(W.w2), "{v[160:191]}"(W.w3), "{v[192:223]}"(W.w4), "{v[224:255]}"(W.w5)
    : "v6", "v7", "v8", "v9", "v10", "v11", "v12", "v13", "v14", "v15", "v16", "v17", "v18", "v19", "v20", "v21", "v22", "v23", "v24", "v25", "v26", "v27", "v28", "v29", "v30", "v31", "v32", "v33", "v34", "v35", "v36", "v37", "v38", "v39", "v40", "v41", "v42", "v43", "v44", "v45", "v46", "v47", "v48", "v49", "v50", "v51", "v52", "v53", "v54", "v55", "v56", "v57", "v58", "v59", "v60", "v61", "v62", "v63", "vcc", "scc", "memory");
}
template <> __device__ __forceinline__ void qk_step<2>(const QKW& W, const char* ag, const char* vs, const char* od, const char* kv, unsigned& cap, unsigned& sp,
                                                        unsigned long long& tc, unsigned long long* ts, unsigned& d0, unsigned& d1) {
  unsigned st, bg;
  asm volatile("v_mbcnt_lo_u32_b32 v60, -1, 0\n\t"
    "v_mbcnt_hi_u32_b32 v60, -1, v60\n\t"
    "v_lshlrev_b32 v6, 4, v60\n\t"
    "s_mov_b32 %[sp], 0\n\t"
    "s_setprio 3\n\t"
    "global_load_dwordx4 v[40:43], v6, %[ag] sc1\n\t"
    "global_load_dwordx4 v[44:47], v6, %[ag] offset:1024 sc1\n\t"
    "s_memtime %[tc]\n\t"
    "v_add_u32 v7, 45056, v6\n\t"
    "v_add_u32 v8, 102400, v6\n\t"
    "ds_read_b128 v[24:27], v7 offset:0\n\t"
    "ds_read_b128 v[28:31], v7 offset:1024\n\t"
    "ds_read_b128 v[32:35], v7 offset:8192\n\t"
    "ds_read_b128 v[36:39], v7 offset:9216\n\t"
    "v_lshrrev_b32 v48, 4, v60\n\t"
    "v_and_b32 v49, 15, v60\n\t"
    "v_and_b32 v61, 4, v60\n\t"
    "v_mul_u32_u24 v61, 0x2aaa, v61\n\t"
    "v_lshrrev_b32 v9, 1, v60\n\t"
    "v_and_b32 v9, 2, v9\n\t"
    "v_or3_b32 v9, v9, v61, %[c44]\n\t"
    "v_add_u32 v14, 1024, v6\n\t"
    "v_lshlrev_b32 v61, 2, v48\n\t"
    "v_lshl_or_b32 v10, v49, 4, v61\n\t"
    "v_lshrrev_b32 v50, 2, v49\n\t"
    "v_and_b32 v51, 3, v60\n\t"
    "v_and_b32 v61, 1, v60\n\t"
    "v_lshlrev_b32 v61, 3, v61\n\t"
    "v_lshl_or_b32 v11, v50, 4, v61\n\t"
    "v_lshl_or_b32 v11, v48, 7, v11\n\t"
    "v_xor_b32 v12, v48, v49\n\t"
    "v_lshlrev_b32 v12, 4, v12\n\t"
    "v_lshl_or_b32 v12, v49, 8, v12\n\t"
    "v_lshl_or_b32 v50, v48, 2, v50\n\t"
    "v_or_b32 v51, 8, v51\n\t"
    "v_xor_b32 v51, v51, v50\n\t"
    "v_lshlrev_b32 v13, 4, v51\n\t"
    "v_lshl_or_b32 v13, v50, 8, v13\n\t"
    "v_and_b32 v61, 3, v60\n\t"
    "v_lshlrev_b32 v61, 4, v61\n\t"
    "v_lshl_or_b32 v15, v48, 6, v61\n\t"
    "v_and_b32 v61, 8, v60\n\t"
    "v_lshl_or_b32 v15, v61, 14, v15\n\t"
    "v_and_b32 v61, 4, v60\n\t"
    "v_lshl_or_b32 v15, v61, 8, v15\n\t"
    "v_add_u32 v15, 512, v15\n\t"
    "3:\n\t"
    "v_mov_b32 v16, 0\n\t"
    "v_mov_b32 v17, 0\n\t"
    "v_mov_b32 v18, 0\n\t"
    "v_mov_b32 v19, 0\n\t"
    "v_mov_b32 v20, 0\n\t"
    "v_mov_b32 v21, 0\n\t"
    "v_mov_b32 v22, 0\n\t"
    "v_mov_b32 v23, 0\n\t"
    "s_waitcnt vmcnt(0) lgkmcnt(0)\n\t"
    "14:\n\t"
    "ds_write_b128 v6, v[40:43] offset:4096\n\t"
    "ds_write_b128 v6, v[44:47] offset:5120\n\t"
    "v_smfmac_f32_16x16x64_bf16 v[16:19], v[40:43], v[128:135], v9\n\t"
    "v_max3_u32 v60, v40, v41, v42\n\t"
    "v_max3_u32 v61, v43, v44, v45\n\t"
    "v_smfmac_f32_16x16x64_bf16 v[20:23], v[40:43], v[136:143], v9\n\t"
    "v_max3_u32 v60, v60, v46, v47\n\t"
    "v_max_u32 v60, v60, v61\n\t"
    "v_cmp_eq_u32 vcc, -1, v60\n\t"
    "s_cbranch_vccnz 1f\n\t"
    "2:\n\t"
    "s_waitcnt lgkmcnt(0)\n\t"
    "s_barrier\n\t"
    "ds_read_b128 v[40:43], v6 offset:12288\n\t"
    "ds_read_b128 v[48:51], v6 offset:0\n\t"
    "v_smfmac_f32_16x16x64_bf16 v[16:19], v[44:47], v[144:151], v9\n\t"
    "ds_read_b128 v[52:55], v6 offset:1024\n\t"
    "ds_read_b128 v[56:59], v6 offset:2048\n\t"
    "v_smfmac_f32_16x16x64_bf16 v[20:23], v[44:47], v[152:159], v9\n\t"
    "ds_read_b128 v[60:63], v6 offset:13312\n\t"
    "ds_read_b128 v[44:47], v6 offset:3072\n\t"
    "s_waitcnt lgkmcnt(5)\n\t"
    "v_smfmac_f32_16x16x64_bf16 v[16:19], v[40:43], v[24:31], v9\n\t"
    "ds_read_b128 v[24:27], v7 offset:16384\n\t"
    "ds_read_b128 v[28:31], v7 offset:17408\n\t"
    "v_smfmac_f32_16x16x64_bf16 v[20:23], v[40:43], v[32:39], v9\n\t"
    "ds_read_b128 v[32:35], v7 offset:24576\n\t"
    "ds_read_b128 v[36:39], v7 offset:25600\n\t"
    "ds_read_b128 v[40:43], v6 offset:6144\n\t"
    "s_waitcnt lgkmcnt(9)\n\t"
    "v_smfmac_f32_16x16x64_bf16 v[16:19], v[48:51], v[64:71], v9\n\t"
    "v_smfmac_f32_16x16x64_bf16 v[20:23], v[48:51], v[72:79], v9\n\t"
    "ds_read_b128 v[48:51], v6 offset:7168\n\t"
    "s_waitcnt lgkmcnt(9)\n\t"
    "v_smfmac_f32_16x16x64_bf16 v[16:19], v[52:55], v[80:87], v9\n\t"
    "v_smfmac_f32_16x16x64_bf16 v[20:23], v[52:55], v[88:95], v9\n\t"
    "ds_read_b128 v[52:55], v6 offset:14336\n\t"
    "s_waitcnt lgkmcnt(9)\n\t"
    "v_smfmac_f32_16x16x64_bf16 v[16:19], v[56:59], v[96:103], v9\n\t"
    "v_smfmac_f32_16x16x64_bf16 v[20:23], v[56:59], v[104:111], v9\n\t"
    "ds_read_b128 v[56:59], v6 offset:8192\n\t"
    "s_waitcnt lgkmcnt(6)\n\t"
    "v_smfmac_f32_16x16x64_bf16 v[16:19], v[60:63], v[24:31], v9\n\t"
    "ds_read_b128 v[24:27], v8 offset:0\n\t"
    "ds_read_b128 v[28:31], v8 offset:1024\n\t"
    "s_waitcnt lgkmcnt(6)\n\t"
    "v_smfmac_f32_16x16x64_bf16 v[20:23], v[60:63], v[32:39], v9\n\t"
    "ds_read_b128 v[32:35], v8 offset:8192\n\t"
    "ds_read_b128 v[36:39], v8 offset:9216\n\t"
    "ds_read_b128 v[60:63], v6 offset:9216\n\t"
    "v_smfmac_f32_16x16x64_bf16 v[16:19], v[44:47], v[112:119], v9\n\t"
    "v_smfmac_f32_16x16x64_bf16 v[20:23], v[44:47], v[120:127], v9\n\t"
    "ds_read_b128 v[44:47], v6 offset:10240\n\t"
    "s_waitcnt lgkmcnt(9)\n\t"
    "v_smfmac_f32_16x16x64_bf16 v[16:19], v[40:43], v[160:167], v9\n\t"
    "v_smfmac_f32_16x16x64_bf16 v[20:23], v[40:43], v[168:175], v9\n\t"
    "ds_read_b128 v[40:43], v6 offset:15360\n\t"
    "s_waitcnt lgkmcnt(9)\n\t"
    "v_smfmac_f32_16x16x64_bf16 v[16:19], v[48:51], v[176:183], v9\n\t"
    "v_smfmac_f32_16x16x64_bf16 v[20:23], v[48:51], v[184:191], v9\n\t"
    "ds_read_b128 v[48:51], v6 offset:11264\n\t"
    "s_waitcnt lgkmcnt(6)\n\t"
    "v_smfmac_f32_16x16x64_bf16 v[16:19], v[52:55], v[24:31], v9\n\t"
    "ds_read_b128 v[24:27], v8 offset:16384\n\t"
    "ds_read_b128 v[28:31], v8 offset:17408\n\t"
    "s_waitcnt lgkmcnt(6)\n\t"
    "v_smfmac_f32_16x16x64_bf16 v[20:23], v[52:55], v[32:39], v9\n\t"
    "ds_read_b128 v[32:35], v8 offset:24576\n\t"
    "ds_read_b128 v[36:39], v8 offset:25600\n\t"
    "v_smfmac_f32_16x16x64_bf16 v[16:19], v[56:59], v[192:199], v9\n\t"
    "v_smfmac_f32_16x16x64_bf16 v[20:23], v[56:59], v[200:207], v9\n\t"
    "s_waitcnt lgkmcnt(7)\n\t"
    "v_smfmac_f32_16x16x64_bf16 v[16:19], v[60:63], v[208:215], v9\n\t"
    "v_smfmac_f32_16x16x64_bf16 v[20:23], v[60:63], v[216:223], v9\n\t"
    "s_waitcnt lgkmcnt(6)\n\t"
    "v_smfmac_f32_16x16x64_bf16 v[16:19], v[44:47], v[224:231], v9\n\t"
    "v_smfmac_f32_16x16x64_bf16 v[20:23], v[44:47], v[232:239], v9\n\t"
    "s_waitcnt lgkmcnt(2)\n\t"
    "v_smfmac_f32_16x16x64_bf16 v[16:19], v[40:43], v[24:31], v9\n\t"
    "s_waitcnt lgkmcnt(0)\n\t"
    "v_smfmac_f32_16x16x64_bf16 v[20:23], v[40:43], v[32:39], v9\n\t"
    "v_smfmac_f32_16x16x64_bf16 v[16:19], v[48:51], v[240:247], v9\n\t"
    "v_smfmac_f32_16x16x64_bf16 v[20:23], v[48:51], v[248:255], v9\n\t"
    "v_xor_b32 v7, 64, v12\n\t"
    "v_xor_b32 v8, 0x80, v12\n\t"
    "v_xor_b32 v6, 0xc0, v12\n\t"
    "ds_read_b128 v[24:27], v12 offset:16384\n\t"
    "ds_read_b128 v[28:31], v12 offset:20480\n\t"
    "ds_read_b128 v[32:35], v7 offset:16384\n\t"
    "ds_read_b128 v[36:39], v7 offset:20480\n\t"
    "v_permlane16_swap_b32 v16, v18\n\t"
    "v_permlane16_swap_b32 v17, v19\n\t"
    "v_permlane16_swap_b32 v20, v22\n\t"
    "v_permlane16_swap_b32 v21, v23\n\t"
    "v_add_f32 v16, v16, v18\n\t"
    "v_add_f32 v17, v17, v19\n\t"
    "v_add_f32 v20, v20, v22\n\t"
    "v_mul_f32 v16, %[lsc], v16\n\t"
    "v_mul_f32 v17, %[lsc], v17\n\t"
    "v_cvt_pk_bf16_f32 v16, v16, v17\n\t"
    "v_add_f32 v21, v21, v23\n\t"
    "ds_write_b32 v10, v16 offset:33792\n\t"
    "v_mul_f32 v20, %[lsc], v20\n\t"
    "v_mul_f32 v21, %[lsc], v21\n\t"
    "v_cvt_pk_bf16_f32 v20, v20, v21\n\t"
    "ds_write_b32 v10, v20 offset:34048\n\t"
    "ds_read_b128 v[40:43], v8 offset:16384\n\t"
    "ds_read_b128 v[44:47], v8 offset:20480\n\t"
    "ds_read_b128 v[48:51], v6 offset:16384\n\t"
    "ds_read_b128 v[52:55], v6 offset:20480\n\t"
    "s_waitcnt lgkmcnt(4)\n\t"
    "s_barrier\n\t"
    "ds_read_b64_tr_b16 v[16:17], v11 offset:32768\n\t"
    "ds_read_b64_tr_b16 v[18:19], v11 offset:32832\n\t"
    "ds_read_b64_tr_b16 v[20:21], v11 offset:33280\n\t"
    "ds_read_b64_tr_b16 v[22:23], v11 offset:33344\n\t"
    "ds_read_b64_tr_b16 v[56:57], v11 offset:33792\n\t"
    "ds_read_b64_tr_b16 v[58:59], v11 offset:33856\n\t"
    "ds_read_b64_tr_b16 v[60:61], v11 offset:34304\n\t"
    "ds_read_b64_tr_b16 v[62:63], v11 offset:34368\n\t"
    "v_mov_b32 v6, 0\n\t"
    "v_mov_b32 v7, 0\n\t"
    "v_mov_b32 v8, 0\n\t"
    "v_mov_b32 v9, 0\n\t"
    "s_mov_b32 exec_hi, 0\n\t"
    "global_load_dwordx4 v[6:9], v14, %[vs] sc1\n\t"
    "s_mov_b32 exec_hi, -1\n\t"
    "s_waitcnt lgkmcnt(6)\n\t"
    "v_mfma_f32_16x16x32_bf16 v[24:27], v[24:27], v[16:19], 0\n\t"
    "v_mfma_f32_16x16x32_bf16 v[28:31], v[28:31], v[16:19], 0\n\t"
    "s_waitcnt lgkmcnt(4)\n\t"
    "v_mfma_f32_16x16x32_bf16 v[24:27], v[32:35], v[20:23], v[24:27]\n\t"
    "v_mfma_f32_16x16x32_bf16 v[28:31], v[36:39], v[20:23], v[28:31]\n\t"
    "ds_read_b64_tr_b16 v[32:33], v13 offset:24576\n\t"
    "ds_read_b64_tr_b16 v[34:35], v13 offset:28672\n\t"
    "ds_read_b64_tr_b16 v[36:37], v13 offset:24584\n\t"
    "ds_read_b64_tr_b16 v[38:39], v13 offset:28680\n\t"
    "s_waitcnt lgkmcnt(6)\n\t"
    "v_mfma_f32_16x16x32_bf16 v[24:27], v[40:43], v[56:59], v[24:27]\n\t"
    "v_mfma_f32_16x16x32_bf16 v[28:31], v[44:47], v[56:59], v[28:31]\n\t"
    "s_waitcnt lgkmcnt(4)\n\t"
    "v_mfma_f32_16x16x32_bf16 v[24:27], v[48:51], v[60:63], v[24:27]\n\t"
    "v_mfma_f32_16x16x32_bf16 v[28:31], v[52:55], v[60:63], v[28:31]\n\t"
    "s_setprio 1\n\t"
    "v_mov_b32 v52, %[one]\n\t"
    "v_mov_b32 v53, %[one]\n\t"
    "v_mov_b32 v54, %[one]\n\t"
    "v_mov_b32 v55, %[one]\n\t"
    "s_nop 2\n\t"
    "v_max3_f32 v10, |v24|, |v25|, |v26|\n\t"
    "v_max3_f32 v40, |v27|, |v28|, |v29|\n\t"
    "v_max3_f32 v10, v10, |v30|, |v31|\n\t"
    "v_max_f32 v10, v10, v40\n\t"
    "v_cmp_nge_f32 vcc, %[big], v10\n\t"
    "s_mov_b32 %[bg], %[big]\n\t"
    "v_mov_b32 v10, 0\n\t"
    "s_cbranch_vccnz 12f\n\t"
    "13:\n\t"
    "v_exp_f32 v24, v24\n\t"
    "v_exp_f32 v25, v25\n\t"
    "v_exp_f32 v26, v26\n\t"
    "v_exp_f32 v27, v27\n\t"
    "v_exp_f32 v28, v28\n\t"
    "v_exp_f32 v29, v29\n\t"
    "v_exp_f32 v30, v30\n\t"
    "v_exp_f32 v31, v31\n\t"
    "v_cvt_pk_bf16_f32 v40, v24, v25\n\t"
    "v_cvt_pk_bf16_f32 v41, v26, v27\n\t"
    "v_cvt_pk_bf16_f32 v42, v28, v29\n\t"
    "v_cvt_pk_bf16_f32 v43, v30, v31\n\t"
    "s_setprio 3\n\t"
    "s_waitcnt lgkmcnt(0)\n\t"
    "s_nop 0\n\t"
    "v_mfma_f32_16x16x32_bf16 v[44:47], v[32:35], v[40:43], 0\n\t"
    "v_mfma_f32_16x16x32_bf16 v[48:51], v[36:39], v[40:43], 0\n\t"
    "v_mfma_f32_16x16x32_bf16 v[52:55], v[52:55], v[40:43], 0\n\t"
    "v_mov_b32 v12, 0\n\t"
    "v_mov_b32 v13, 0\n\t"
    "s_mov_b32 exec_hi, 0\n\t"
    "v_mov_b32 v12, %[one]\n\t"
    "v_mov_b32 v13, %[one]\n\t"
    "s_mov_b32 exec_hi, -1\n\t"
    "s_barrier\n\t"
    "ds_read_b64_tr_b16 v[24:25], v11 offset:34816\n\t"
    "ds_read_b64_tr_b16 v[26:27], v11 offset:34880\n\t"
    "ds_read_b64_tr_b16 v[28:29], v11 offset:35328\n\t"
    "ds_read_b64_tr_b16 v[30:31], v11 offset:35392\n\t"
    "ds_read_b64_tr_b16 v[32:33], v11 offset:35840\n\t"
    "ds_read_b64_tr_b16 v[34:35], v11 offset:35904\n\t"
    "ds_read_b64_tr_b16 v[36:37], v11 offset:36352\n\t"
    "ds_read_b64_tr_b16 v[38:39], v11 offset:36416\n\t"
    "s_waitcnt lgkmcnt(6)\n\t"
    "v_mfma_f32_16x16x32_bf16 v[40:43], v[24:27], v[16:19], 0\n\t"
    "s_waitcnt lgkmcnt(4)\n\t"
    "v_mfma_f32_16x16x32_bf16 v[40:43], v[28:31], v[20:23], v[40:43]\n\t"
    "s_waitcnt lgkmcnt(2)\n\t"
    "v_mfma_f32_16x16x32_bf16 v[40:43], v[32:35], v[56:59], v[40:43]\n\t"
    "s_waitcnt lgkmcnt(0)\n\t"
    "v_mfma_f32_16x16x32_bf16 v[40:43], v[36:39], v[60:63], v[40:43]\n\t"
    "5:\n\t"
    "s_waitcnt vmcnt(0)\n\t"
    "v_max3_u32 v11, v6, v7, v8\n\t"
    "v_max_u32 v11, v11, v9\n\t"
    "v_cmp_eq_u32 vcc, -1, v11\n\t"
    "s_cbranch_vccnz 4f\n\t"
    "6:\n\t"
    "s_nop 2\n\t"
    "v_exp_f32 v28, v40\n\t"
    "v_max3_f32 v11, v40, v41, v42\n\t"
    "v_exp_f32 v29, v41\n\t"
    "v_max_f32 v11, v11, v43\n\t"
    "v_exp_f32 v30, v42\n\t"
    "v_cmp_nge_f32 vcc, %[bg], v11\n\t"
    "v_exp_f32 v31, v43\n\t"
    "s_cbranch_vccnz 10f\n\t"
    "11:\n\t"
    "v_cvt_pk_bf16_f32 v24, v28, v29\n\t"
    "v_cvt_pk_bf16_f32 v25, v30, v31\n\t"
    "s_nop 1\n\t"
    "v_mfma_f32_16x16x16_bf16 v[52:55], v[12:13], v[24:25], v[52:55]\n\t"
    "v_mfma_f32_16x16x16_bf16 v[44:47], v[6:7], v[24:25], v[44:47]\n\t"
    "v_mfma_f32_16x16x16_bf16 v[48:51], v[8:9], v[24:25], v[48:51]\n\t"
    "s_nop 5\n\t"
    "v_rcp_f32 v26, v52\n\t"
    "s_nop 0\n\t"
    "v_pk_mul_f32 v[44:45], v[44:45], v[26:27] op_sel_hi:[1,0]\n\t"
    "v_pk_mul_f32 v[46:47], v[46:47], v[26:27] op_sel_hi:[1,0]\n\t"
    "v_pk_mul_f32 v[48:49], v[48:49], v[26:27] op_sel_hi:[1,0]\n\t"
    "v_pk_mul_f32 v[50:51], v[50:51], v[26:27] op_sel_hi:[1,0]\n\t"
    "v_cvt_pk_bf16_f32 v36, v44, v45\n\t"
    "v_cvt_pk_bf16_f32 v37, v46, v47\n\t"
    "v_cvt_pk_bf16_f32 v38, v48, v49\n\t"
    "v_cvt_pk_bf16_f32 v39, v50, v51\n\t"
    "v_min_u32 v36, -2, v36\n\t"
    "v_min_u32 v37, -2, v37\n\t"
    "v_min_u32 v38, -2, v38\n\t"
    "v_min_u32 v39, -2, v39\n\t"
    "global_store_dwordx4 v15, v[36:39], %[od] sc1\n\t"
    "s_barrier\n\t"
    "s_mov_b32 exec_hi, 0\n\t"
    "global_store_dwordx4 v14, v[6:9], %[kv] sc1\n\t"
    "s_mov_b32 exec_hi, -1\n\t"
    "s_nop 0\n\t"
    "s_waitcnt lgkmcnt(0)\n\t"
    "s_branch 9f\n\t"
    "1:\n\t"
    "s_add_u32 %[sp], %[sp], 1\n\t"
    "s_cmp_ge_u32 %[sp], %[cap]\n\t"
    "s_cbranch_scc1 7f\n\t"
    "global_load_dwordx4 v[40:43], v6, %[ag] sc1\n\t"
    "global_load_dwordx4 v[44:47], v6, %[ag] offset:1024 sc1\n\t"
    "s_waitcnt lgkmcnt(0)\n\t"
    "s_memtime %[tc]\n\t"
    "s_nop 7\n\t"
    "s_branch 3b\n\t"
    "7:\n\t"
    "s_mov_b32 %[cap], 0\n\t"
    "s_branch 2b\n\t"
    "4:\n\t"
    "s_add_u32 %[sp], %[sp], 0x10000\n\t"
    "s_lshr_b32 %[st], %[sp], 16\n\t"
    "s_cmp_ge_u32 %[st], %[cap]\n\t"
    "s_cbranch_scc1 8f\n\t"
    "s_mov_b32 exec_hi, 0\n\t"
    "global_load_dwordx4 v[6:9], v14, %[vs] sc1\n\t"
    "s_mov_b32 exec_hi, -1\n\t"
    "s_branch 5b\n\t"
    "8:\n\t"
    "s_mov_b32 %[cap], 0\n\t"
    "s_branch 6b\n\t"
    "10:\n\t"
    "v_max_f32 v11, v11, v10\n\t"
    "v_mov_b32 v26, v11\n\t"
    "s_nop 1\n\t"
    "v_permlane16_swap_b32 v11, v26\n\t"
    "v_max_f32 v11, v11, v26\n\t"
    "v_sub_f32 v28, v40, v11\n\t"
    "v_sub_f32 v29, v41, v11\n\t"
    "v_sub_f32 v30, v42, v11\n\t"
    "v_sub_f32 v31, v43, v11\n\t"
    "v_sub_f32 v11, v10, v11\n\t"
    "v_exp_f32 v11, v11\n\t"
    "v_exp_f32 v28, v28\n\t"
    "v_exp_f32 v29, v29\n\t"
    "v_exp_f32 v30, v30\n\t"
    "v_exp_f32 v31, v31\n\t"
    "v_mul_f32 v44, v44, v11\n\t"
    "v_mul_f32 v45, v45, v11\n\t"
    "v_mul_f32 v46, v46, v11\n\t"
    "v_mul_f32 v47, v47, v11\n\t"
    "v_mul_f32 v48, v48, v11\n\t"
    "v_mul_f32 v49, v49, v11\n\t"
    "v_mul_f32 v50, v50, v11\n\t"
    "v_mul_f32 v51, v51, v11\n\t"
    "v_mul_f32 v52, v52, v11\n\t"
    "v_mul_f32 v53, v53, v11\n\t"
    "v_mul_f32 v54, v54, v11\n\t"
    "v_mul_f32 v55, v55, v11\n\t"
    "s_branch 11b\n\t"
    "12:\n\t"
    "v_max3_f32 v10, v24, v25, v26\n\t"
    "v_max3_f32 v40, v27, v28, v29\n\t"
    "v_max3_f32 v10, v10, v30, v31\n\t"
    "v_max3_f32 v10, v10, v40, %[neg]\n\t"
    "v_mov_b32 v40, v10\n\t"
    "s_nop 1\n\t"
    "v_permlane16_swap_b32 v10, v40\n\t"
    "v_max_f32 v10, v10, v40\n\t"
    "v_mov_b32 v40, v10\n\t"
    "s_nop 1\n\t"
    "v_permlane32_swap_b32 v10, v40\n\t"
    "v_max_f32 v10, v10, v40\n\t"
    "v_sub_f32 v24, v24, v10\n\t"
    "v_sub_f32 v25, v25, v10\n\t"
    "v_sub_f32 v26, v26, v10\n\t"
    "v_sub_f32 v27, v27, v10\n\t"
    "v_sub_f32 v28, v28, v10\n\t"
    "v_sub_f32 v29, v29, v10\n\t"
    "v_sub_f32 v30, v30, v10\n\t"
    "v_sub_f32 v31, v31, v10\n\t"
    "s_mov_b32 %[bg], 0x7fc00000\n\t"
    "s_branch 13b\n\t"
    "9:"
    : [tc] "=&s"(tc), [sp] "=&s"(sp), [cap] "+s"(cap), [st] "=&s"(st), [bg] "=&s"(bg)
    : [ag] "s"(ag), [c44] "s"(0x4444u), [kv] "s"(kv), [vs] "s"(vs), [od] "s"(od), [od2] "s"(od + 262144), [neg] "s"(-1.0e38f), [lsc] "s"(LSC), [one] "s"(0x3F803F80u), [big] "s"(24.0f), "{v[64:95]}"(W.w0), "{v[96:127]}"(W.w1), "{v[128:159]}"(W.w2), "{v[160:191]}"(W.w3), "{v[192:223]}"(W.w4), "{v[224:255]}"(W.w5)
    : "v6", "v7", "v8", "v9", "v10", "v11", "v12", "v13", "v14", "v15", "v16", "v17", "v18", "v19", "v20", "v21", "v22", "v23", "v24", "v25", "v26", "v27", "v28", "v29", "v30", "v31", "v32", "v33", "v34", "v35", "v36", "v37", "v38", "v39", "v40", "v41", "v42", "v43", "v44", "v45", "v46", "v47", "v48", "v49", "v50", "v51", "v52", "v53", "v54", "v55", "v56", "v57", "v58", "v59", "v60", "v61", "v62", "v63", "vcc", "scc", "memory");
}
template <> __device__ __forceinline__ void qk_step<3>(const QKW& W, const char* ag, const char* vs, const char* od, const char* kv, unsigned& cap, unsigned& sp,
                                                        unsigned long long& tc, unsigned long long* ts, unsigned& d0, unsigned& d1) {
  unsigned st, bg;
  asm volatile("v_mbcnt_lo_u32_b32 v60, -1, 0\n\t"
    "v_mbcnt_hi_u32_b32 v60, -1, v60\n\t"
    "v_lshlrev_b32 v6, 4, v60\n\t"
    "s_mov_b32 %[sp], 0\n\t"
    "s_setprio 3\n\t"
    "global_load_dwordx4 v[40:43], v6, %[ag] sc1\n\t"
    "global_load_dwordx4 v[44:47], v6, %[ag] offset:1024 sc1\n\t"
    "s_memtime %[tc]\n\t"
    "v_add_u32 v7, 47104, v6\n\t"
    "v_add_u32 v8, 104448, v6\n\t"
    "ds_read_b128 v[24:27], v7 offset:0\n\t"
    "ds_read_b128 v[28:31], v7 offset:1024\n\t"
    "ds_read_b128 v[32:35], v7 offset:8192\n\t"
    "ds_read_b128 v[36:39], v7 offset:9216\n\t"
    "v_lshrrev_b32 v48, 4, v60\n\t"
    "v_and_b32 v49, 15, v60\n\t"
    "v_and_b32 v61, 4, v60\n\t"
    "v_mul_u32_u24 v61, 0x2aaa, v61\n\t"
    "v_lshrrev_b32 v9, 1, v60\n\t"
    "v_and_b32 v9, 2, v9\n\t"
    "v_or3_b32 v9, v9, v61, %[c44]\n\t"
    "v_add_u32 v14, 1536, v6\n\t"
    "v_lshlrev_b32 v61, 2, v48\n\t"
    "v_lshl_or_b32 v10, v49, 4, v61\n\t"
    "v_lshrrev_b32 v50, 2, v49\n\t"
    "v_and_b32 v51, 3, v60\n\t"
    "v_and_b32 v61, 1, v60\n\t"
    "v_lshlrev_b32 v61, 3, v61\n\t"
    "v_lshl_or_b32 v11, v50, 4, v61\n\t"
    "v_lshl_or_b32 v11, v48, 7, v11\n\t"
    "v_xor_b32 v12, v48, v49\n\t"
    "v_lshlrev_b32 v12, 4, v12\n\t"
    "v_lshl_or_b32 v12, v49, 8, v12\n\t"
    "v_lshl_or_b32 v50, v48, 2, v50\n\t"
    "v_or_b32 v51, 12, v51\n\t"
    "v_xor_b32 v51, v51, v50\n\t"
    "v_lshlrev_b32 v13, 4, v51\n\t"
    "v_lshl_or_b32 v13, v50, 8, v13\n\t"
    "v_and_b32 v61, 3, v60\n\t"
    "v_lshlrev_b32 v61, 4, v61\n\t"
    "v_lshl_or_b32 v15, v48, 6, v61\n\t"
    "v_and_b32 v61, 8, v60\n\t"
    "v_lshl_or_b32 v15, v61, 14, v15\n\t"
    "v_and_b32 v61, 4, v60\n\t"
    "v_lshl_or_b32 v15, v61, 8, v15\n\t"
    "v_add_u32 v15, 768, v15\n\t"
    "3:\n\t"
    "v_mov_b32 v16, 0\n\t"
    "v_mov_b32 v17, 0\n\t"
    "v_mov_b32 v18, 0\n\t"
    "v_mov_b32 v19, 0\n\t"
    "v_mov_b32 v20, 0\n\t"
    "v_mov_b32 v21, 0\n\t"
    "v_mov_b32 v22, 0\n\t"
    "v_mov_b32 v23, 0\n\t"
    "s_waitcnt vmcnt(0) lgkmcnt(0)\n\t"
    "14:\n\t"
    "ds_write_b128 v6, v[40:43] offset:6144\n\t"
    "ds_write_b128 v6, v[44:47] offset:7168\n\t"
    "v_smfmac_f32_16x16x64_bf16 v[16:19], v[40:43], v[160:167], v9\n\t"
    "v_max3_u32 v60, v40, v41, v42\n\t"
    "v_max3_u32 v61, v43, v44, v45\n\t"
    "v_smfmac_f32_16x16x64_bf16 v[20:23], v[40:43], v[168:175], v9\n\t"
    "v_max3_u32 v60, v60, v46, v47\n\t"
    "v_max_u32 v60, v60, v61\n\t"
    "v_cmp_eq_u32 vcc, -1, v60\n\t"
    "s_cbranch_vccnz 1f\n\t"
    "2:\n\t"
    "s_waitcnt lgkmcnt(0)\n\t"
    "s_barrier\n\t"
    "ds_read_b128 v[40:43], v6 offset:12288\n\t"
    "ds_read_b128 v[48:51], v6 offset:0\n\t"
    "v_smfmac_f32_16x16x64_bf16 v[16:19], v[44:47], v[176:183], v9\n\t"
    "ds_read_b128 v[52:55], v6 offset:1024\n\t"
    "ds_read_b128 v[56:59], v6 offset:2048\n\t"
    "v_smfmac_f32_16x16x64_bf16 v[20:23], v[44:47], v[184:191], v9\n\t"
    "ds_read_b128 v[60:63], v6 offset:13312\n\t"
    "ds_read_b128 v[44:47], v6 offset:3072\n\t"
    "s_waitcnt lgkmcnt(5)\n\t"
    "v_smfmac_f32_16x16x64_bf16 v[16:19], v[40:43], v[24:31], v9\n\t"
    "ds_read_b128 v[24:27], v7 offset:16384\n\t"
    "ds_read_b128 v[28:31], v7 offset:17408\n\t"
    "v_smfmac_f32_16x16x64_bf16 v[20:23], v[40:43], v[32:39], v9\n\t"
    "ds_read_b128 v[32:35], v7 offset:24576\n\t"
    "ds_read_b128 v[36:39], v7 offset:25600\n\t"
    "ds_read_b128 v[40:43], v6 offset:4096\n\t"
    "s_waitcnt lgkmcnt(9)\n\t"
    "v_smfmac_f32_16x16x64_bf16 v[16:19], v[48:51], v[64:71], v9\n\t"
    "v_smfmac_f32_16x16x64_bf16 v[20:23], v[48:51], v[72:79], v9\n\t"
    "ds_read_b128 v[48:51], v6 offset:5120\n\t"
    "s_waitcnt lgkmcnt(9)\n\t"
    "v_smfmac_f32_16x16x64_bf16 v[16:19], v[52:55], v[80:87], v9\n\t"
    "v_smfmac_f32_16x16x64_bf16 v[20:23], v[52:55], v[88:95], v9\n\t"
    "ds_read_b128 v[52:55], v6 offset:14336\n\t"
    "s_waitcnt lgkmcnt(9)\n\t"
    "v_smfmac_f32_16x16x64_bf16 v[16:19], v[56:59], v[96:103], v9\n\t"
    "v_smfmac_f32_16x16x64_bf16 v[20:23], v[56:59], v[104:111], v9\n\t"
    "ds_read_b128 v[56:59], v6 offset:8192\n\t"
    "s_waitcnt lgkmcnt(6)\n\t"
    "v_smfmac_f32_16x16x64_bf16 v[16:19], v[60:63], v[24:31], v9\n\t"
    "ds_read_b128 v[24:27], v8 offset:0\n\t"
    "ds_read_b128 v[28:31], v8 offset:1024\n\t"
    "s_waitcnt lgkmcnt(6)\n\t"
    "v_smfmac_f32_16x16x64_bf16 v[20:23], v[60:63], v[32:39], v9\n\t"
    "ds_read_b128 v[32:35], v8 offset:8192\n\t"
    "ds_read_b128 v[36:39], v8 offset:9216\n\t"
    "ds_read_b128 v[60:63], v6 offset:9216\n\t"
    "v_smfmac_f32_16x16x64_bf16 v[16:19], v[44:47], v[112:119], v9\n\t"
    "v_smfmac_f32_16x16x64_bf16 v[20:23], v[44:47], v[120:127], v9\n\t"
    "ds_read_b128 v[44:47], v6 offset:10240\n\t"
    "s_waitcnt lgkmcnt(9)\n\t"
    "v_smfmac_f32_16x16x64_bf16 v[16:19], v[40:43], v[128:135], v9\n\t"
    "v_smfmac_f32_16x16x64_bf16 v[20:23], v[40:43], v[136:143], v9\n\t"
    "ds_read_b128 v[40:43], v6 offset:15360\n\t"
    "s_waitcnt lgkmcnt(9)\n\t"
    "v_smfmac_f32_16x16x64_bf16 v[16:19], v[48:51], v[144:151], v9\n\t"
    "v_smfmac_f32_16x16x64_bf16 v[20:23], v[48:51], v[152:159], v9\n\t"
    "ds_read_b128 v[48:51], v6 offset:11264\n\t"
    "s_waitcnt lgkmcnt(6)\n\t"
    "v_smfmac_f32_16x16x64_bf16 v[16:19], v[52:55], v[24:31], v9\n\t"
    "ds_read_b128 v[24:27], v8 offset:16384\n\t"
    "ds_read_b128 v[28:31], v8 offset:17408\n\t"
    "s_waitcnt lgkmcnt(6)\n\t"
    "v_smfmac_f32_16x16x64_bf16 v[20:23], v[52:55], v[32:39], v9\n\t"
    "ds_read_b128 v[32:35], v8 offset:24576\n\t"
    "ds_read_b128 v[36:39], v8 offset:25600\n\t"
    "v_smfmac_f32_16x16x64_bf16 v[16:19], v[56:59], v[192:199], v9\n\t"
    "v_smfmac_f32_16x16x64_bf16 v[20:23], v[56:59], v[200:207], v9\n\t"
    "s_waitcnt lgkmcnt(7)\n\t"
    "v_smfmac_f32_16x16x64_bf16 v[16:19], v[60:63], v[208:215], v9\n\t"
    "v_smfmac_f32_16x16x64_bf16 v[20:23], v[60:63], v[216:223], v9\n\t"
    "s_waitcnt lgkmcnt(6)\n\t"
    "v_smfmac_f32_16x16x64_bf16 v[16:19], v[44:47], v[224:231], v9\n\t"
    "v_smfmac_f32_16x16x64_bf16 v[20:23], v[44:47], v[232:239], v9\n\t"
    "s_waitcnt lgkmcnt(2)\n\t"
    "v_smfmac_f32_16x16x64_bf16 v[16:19], v[40:43], v[24:31], v9\n\t"
    "s_waitcnt lgkmcnt(0)\n\t"
    "v_smfmac_f32_16x16x64_bf16 v[20:23], v[40:43], v[32:39], v9\n\t"
    "v_smfmac_f32_16x16x64_bf16 v[16:19], v[48:51], v[240:247], v9\n\t"
    "v_smfmac_f32_16x16x64_bf16 v[20:23], v[48:51], v[248:255], v9\n\t"
    "v_xor_b32 v7, 64, v12\n\t"
    "v_xor_b32 v8, 0x80, v12\n\t"
    "v_xor_b32 v6, 0xc0, v12\n\t"
    "ds_read_b128 v[24:27], v12 offset:16384\n\t"
    "ds_read_b128 v[28:31], v12 offset:20480\n\t"
    "ds_read_b128 v[32:35], v7 offset:16384\n\t"
    "ds_read_b128 v[36:39], v7 offset:20480\n\t"
    "v_permlane16_swap_b32 v16, v18\n\t"
    "v_permlane16_swap_b32 v17, v19\n\t"
    "v_permlane16_swap_b32 v20, v22\n\t"
    "v_permlane16_swap_b32 v21, v23\n\t"
    "v_add_f32 v16, v16, v18\n\t"
    "v_add_f32 v17, v17, v19\n\t"
    "v_add_f32 v20, v20, v22\n\t"
    "v_mul_f32 v16, %[lsc], v16\n\t"
    "v_mul_f32 v17, %[lsc], v17\n\t"
    "v_cvt_pk_bf16_f32 v16, v16, v17\n\t"
    "v_add_f32 v21, v21, v23\n\t"
    "ds_write_b32 v10, v16 offset:34304\n\t"
    "v_mul_f32 v20, %[lsc], v20\n\t"
    "v_mul_f32 v21, %[lsc], v21\n\t"
    "v_cvt_pk_bf16_f32 v20, v20, v21\n\t"
    "ds_write_b32 v10, v20 offset:34560\n\t"
    "ds_read_b128 v[40:43], v8 offset:16384\n\t"
    "ds_read_b128 v[44:47], v8 offset:20480\n\t"
    "ds_read_b128 v[48:51], v6 offset:16384\n\t"
    "ds_read_b128 v[52:55], v6 offset:20480\n\t"
    "s_waitcnt lgkmcnt(4)\n\t"
    "s_barrier\n\t"
    "ds_read_b64_tr_b16 v[16:17], v11 offset:32768\n\t"
    "ds_read_b64_tr_b16 v[18:19], v11 offset:32832\n\t"
    "ds_read_b64_tr_b16 v[20:21], v11 offset:33280\n\t"
    "ds_read_b64_tr_b16 v[22:23], v11 offset:33344\n\t"
    "ds_read_b64_tr_b16 v[56:57], v11 offset:33792\n\t"
    "ds_read_b64_tr_b16 v[58:59], v11 offset:33856\n\t"
    "ds_read_b64_tr_b16 v[60:61], v11 offset:34304\n\t"
    "ds_read_b64_tr_b16 v[62:63], v11 offset:34368\n\t"
    "v_mov_b32 v6, 0\n\t"
    "v_mov_b32 v7, 0\n\t"
    "v_mov_b32 v8, 0\n\t"
    "v_mov_b32 v9, 0\n\t"
    "s_mov_b32 exec_hi, 0\n\t"
    "global_load_dwordx4 v[6:9], v14, %[vs] sc1\n\t"
    "s_mov_b32 exec_hi, -1\n\t"
    "s_waitcnt lgkmcnt(6)\n\t"
    "v_mfma_f32_16x16x32_bf16 v[24:27], v[24:27], v[16:19], 0\n\t"
    "v_mfma_f32_16x16x32_bf16 v[28:31], v[28:31], v[16:19], 0\n\t"
    "s_waitcnt lgkmcnt(4)\n\t"
    "v_mfma_f32_16x16x32_bf16 v[24:27], v[32:35], v[20:23], v[24:27]\n\t"
    "v_mfma_f32_16x16x32_bf16 v[28:31], v[36:39], v[20:23], v[28:31]\n\t"
    "ds_read_b64_tr_b16 v[32:33], v13 offset:24576\n\t"
    "ds_read_b64_tr_b16 v[34:35], v13 offset:28672\n\t"
    "ds_read_b64_tr_b16 v[36:37], v13 offset:24584\n\t"
    "ds_read_b64_tr_b16 v[38:39], v13 offset:28680\n\t"
    "s_waitcnt lgkmcnt(6)\n\t"
    "v_mfma_f32_16x16x32_bf16 v[24:27], v[40:43], v[56:59], v[24:27]\n\t"
    "v_mfma_f32_16x16x32_bf16 v[28:31], v[44:47], v[56:59], v[28:31]\n\t"
    "s_waitcnt lgkmcnt(4)\n\t"
    "v_mfma_f32_16x16x32_bf16 v[24:27], v[48:51], v[60:63], v[24:27]\n\t"
    "v_mfma_f32_16x16x32_bf16 v[28:31], v[52:55], v[60:63], v[28:31]\n\t"
    "s_setprio 1\n\t"
    "v_mov_b32 v52, %[one]\n\t"
    "v_mov_b32 v53, %[one]\n\t"
    "v_mov_b32 v54, %[one]\n\t"
    "v_mov_b32 v55, %[one]\n\t"
    "s_nop 2\n\t"
    "v_max3_f32 v10, |v24|, |v25|, |v26|\n\t"
    "v_max3_f32 v40, |v27|, |v28|, |v29|\n\t"
    "v_max3_f32 v10, v10, |v30|, |v31|\n\t"
    "v_max_f32 v10, v10, v40\n\t"
    "v_cmp_nge_f32 vcc, %[big], v10\n\t"
    "s_mov_b32 %[bg], %[big]\n\t"
    "v_mov_b32 v10, 0\n\t"
    "s_cbranch_vccnz 12f\n\t"
    "13:\n\t"
    "v_exp_f32 v24, v24\n\t"
    "v_exp_f32 v25, v25\n\t"
    "v_exp_f32 v26, v26\n\t"
    "v_exp_f32 v27, v27\n\t"
    "v_exp_f32 v28, v28\n\t"
    "v_exp_f32 v29, v29\n\t"
    "v_exp_f32 v30, v30\n\t"
    "v_exp_f32 v31, v31\n\t"
    "v_cvt_pk_bf16_f32 v40, v24, v25\n\t"
    "v_cvt_pk_bf16_f32 v41, v26, v27\n\t"
    "v_cvt_pk_bf16_f32 v42, v28, v29\n\t"
    "v_cvt_pk_bf16_f32 v43, v30, v31\n\t"
    "s_setprio 3\n\t"
    "s_waitcnt lgkmcnt(0)\n\t"
    "s_nop 0\n\t"
    "v_mfma_f32_16x16x32_bf16 v[44:47], v[32:35], v[40:43], 0\n\t"
    "v_mfma_f32_16x16x32_bf16 v[48:51], v[36:39], v[40:43], 0\n\t"
    "v_mfma_f32_16x16x32_bf16 v[52:55], v[52:55], v[40:43], 0\n\t"
    "v_mov_b32 v12, 0\n\t"
    "v_mov_b32 v13, 0\n\t"
    "s_mov_b32 exec_hi, 0\n\t"
    "v_mov_b32 v12, %[one]\n\t"
    "v_mov_b32 v13, %[one]\n\t"
    "s_mov_b32 exec_hi, -1\n\t"
    "s_barrier\n\t"
    "ds_read_b64_tr_b16 v[24:25], v11 offset:34816\n\t"
    "ds_read_b64_tr_b16 v[26:27], v11 offset:34880\n\t"
    "ds_read_b64_tr_b16 v[28:29], v11 offset:35328\n\t"
    "ds_read_b64_tr_b16 v[30:31], v11 offset:35392\n\t"
    "ds_read_b64_tr_b16 v[32:33], v11 offset:35840\n\t"
    "ds_read_b64_tr_b16 v[34:35], v11 offset:35904\n\t"
    "ds_read_b64_tr_b16 v[36:37], v11 offset:36352\n\t"
    "ds_read_b64_tr_b16 v[38:39], v11 offset:36416\n\t"
    "s_waitcnt lgkmcnt(6)\n\t"
    "v_mfma_f32_16x16x32_bf16 v[40:43], v[24:27], v[16:19], 0\n\t"
    "s_waitcnt lgkmcnt(4)\n\t"
    "v_mfma_f32_16x16x32_bf16 v[40:43], v[28:31], v[20:23], v[40:43]\n\t"
    "s_waitcnt lgkmcnt(2)\n\t"
    "v_mfma_f32_16x16x32_bf16 v[40:43], v[32:35], v[56:59], v[40:43]\n\t"
    "s_waitcnt lgkmcnt(0)\n\t"
    "v_mfma_f32_16x16x32_bf16 v[40:43], v[36:39], v[60:63], v[40:43]\n\t"
    "5:\n\t"
    "s_waitcnt vmcnt(0)\n\t"
    "v_max3_u32 v11, v6, v7, v8\n\t"
    "v_max_u32 v11, v11, v9\n\t"
    "v_cmp_eq_u32 vcc, -1, v11\n\t"
    "s_cbranch_vccnz 4f\n\t"
    "6:\n\t"
    "s_nop 2\n\t"
    "v_exp_f32 v28, v40\n\t"
    "v_max3_f32 v11, v40, v41, v42\n\t"
    "v_exp_f32 v29, v41\n\t"
    "v_max_f32 v11, v11, v43\n\t"
    "v_exp_f32 v30, v42\n\t"
    "v_cmp_nge_f32 vcc, %[bg], v11\n\t"
    "v_exp_f32 v31, v43\n\t"
    "s_cbranch_vccnz 10f\n\t"
    "11:\n\t"
    "v_cvt_pk_bf16_f32 v24, v28, v29\n\t"
    "v_cvt_pk_bf16_f32 v25, v30, v31\n\t"
    "s_nop 1\n\t"
    "v_mfma_f32_16x16x16_bf16 v[52:55], v[12:13], v[24:25], v[52:55]\n\t"
    "v_mfma_f32_16x16x16_bf16 v[44:47], v[6:7], v[24:25], v[44:47]\n\t"
    "v_mfma_f32_16x16x16_bf16 v[48:51], v[8:9], v[24:25], v[48:51]\n\t"
    "s_nop 5\n\t"
    "v_rcp_f32 v26, v52\n\t"
    "s_nop 0\n\t"
    "v_pk_mul_f32 v[44:45], v[44:45], v[26:27] op_sel_hi:[1,0]\n\t"
    "v_pk_mul_f32 v[46:47], v[46:47], v[26:27] op_sel_hi:[1,0]\n\t"
    "v_pk_mul_f32 v[48:49], v[48:49], v[26:27] op_sel_hi:[1,0]\n\t"
    "v_pk_mul_f32 v[50:51], v[50:51], v[26:27] op_sel_hi:[1,0]\n\t"
    "v_cvt_pk_bf16_f32 v36, v44, v45\n\t"
    "v_cvt_pk_bf16_f32 v37, v46, v47\n\t"
    "v_cvt_pk_bf16_f32 v38, v48, v49\n\t"
    "v_cvt_pk_bf16_f32 v39, v50, v51\n\t"
    "v_min_u32 v36, -2, v36\n\t"
    "v_min_u32 v37, -2, v37\n\t"
    "v_min_u32 v38, -2, v38\n\t"
    "v_min_u32 v39, -2, v39\n\t"
    "global_store_dwordx4 v15, v[36:39], %[od] sc1\n\t"
    "s_barrier\n\t"
    "s_mov_b32 exec_hi, 0\n\t"
    "global_store_dwordx4 v14, v[6:9], %[kv] sc1\n\t"
    "s_mov_b32 exec_hi, -1\n\t"
    "s_nop 0\n\t"
    "s_waitcnt lgkmcnt(0)\n\t"
    "s_branch 9f\n\t"
    "1:\n\t"
    "s_add_u32 %[sp], %[sp], 1\n\t"
    "s_cmp_ge_u32 %[sp], %[cap]\n\t"
    "s_cbranch_scc1 7f\n\t"
    "global_load_dwordx4 v[40:43], v6, %[ag] sc1\n\t"
    "global_load_dwordx4 v[44:47], v6, %[ag] offset:1024 sc1\n\t"
    "s_waitcnt lgkmcnt(0)\n\t"
    "s_memtime %[tc]\n\t"
    "s_nop 7\n\t"
    "s_branch 3b\n\t"
    "7:\n\t"
    "s_mov_b32 %[cap], 0\n\t"
    "s_branch 2b\n\t"
    "4:\n\t"
    "s_add_u32 %[sp], %[sp], 0x10000\n\t"
    "s_lshr_b32 %[st], %[sp], 16\n\t"
    "s_cmp_ge_u32 %[st], %[cap]\n\t"
    "s_cbranch_scc1 8f\n\t"
    "s_mov_b32 exec_hi, 0\n\t"
    "global_load_dwordx4 v[6:9], v14, %[vs] sc1\n\t"
    "s_mov_b32 exec_hi, -1\n\t"
    "s_branch 5b\n\t"
    "8:\n\t"
    "s_mov_b32 %[cap], 0\n\t"
    "s_branch 6b\n\t"
    "10:\n\t"
    "v_max_f32 v11, v11, v10\n\t"
    "v_mov_b32 v26, v11\n\t"
    "s_nop 1\n\t"
    "v_permlane16_swap_b32 v11, v26\n\t"
    "v_max_f32 v11, v11, v26\n\t"
    "v_sub_f32 v28, v40, v11\n\t"
    "v_sub_f32 v29, v41, v11\n\t"
    "v_sub_f32 v30, v42, v11\n\t"
    "v_sub_f32 v31, v43, v11\n\t"
    "v_sub_f32 v11, v10, v11\n\t"
    "v_exp_f32 v11, v11\n\t"
    "v_exp_f32 v28, v28\n\t"
    "v_exp_f32 v29, v29\n\t"
    "v_exp_f32 v30, v30\n\t"
    "v_exp_f32 v31, v31\n\t"
    "v_mul_f32 v44, v44, v11\n\t"
    "v_mul_f32 v45, v45, v11\n\t"
    "v_mul_f32 v46, v46, v11\n\t"
    "v_mul_f32 v47, v47, v11\n\t"
    "v_mul_f32 v48, v48, v11\n\t"
    "v_mul_f32 v49, v49, v11\n\t"
    "v_mul_f32 v50, v50, v11\n\t"
    "v_mul_f32 v51, v51, v11\n\t"
    "v_mul_f32 v52, v52, v11\n\t"
    "v_mul_f32 v53, v53, v11\n\t"
    "v_mul_f32 v54, v54, v11\n\t"
    "v_mul_f32 v55, v55, v11\n\t"
    "s_branch 11b\n\t"
    "12:\n\t"
    "v_max3_f32 v10, v24, v25, v26\n\t"
    "v_max3_f32 v40, v27, v28, v29\n\t"
    "v_max3_f32 v10, v10, v30, v31\n\t"
    "v_max3_f32 v10, v10, v40, %[neg]\n\t"
    "v_mov_b32 v40, v10\n\t"
    "s_nop 1\n\t"
    "v_permlane16_swap_b32 v10, v40\n\t"
    "v_max_f32 v10, v10, v40\n\t"
    "v_mov_b32 v40, v10\n\t"
    "s_nop 1\n\t"
    "v_permlane32_swap_b32 v10, v40\n\t"
    "v_max_f32 v10, v10, v40\n\t"
    "v_sub_f32 v24, v24, v10\n\t"
    "v_sub_f32 v25, v25, v10\n\t"
    "v_sub_f32 v26, v26, v10\n\t"
    "v_sub_f32 v27, v27, v10\n\t"
    "v_sub_f32 v28, v28, v10\n\t"
    "v_sub_f32 v29, v29, v10\n\t"
    "v_sub_f32 v30, v30, v10\n\t"
    "v_sub_f32 v31, v31, v10\n\t"
    "s_mov_b32 %[bg], 0x7fc00000\n\t"
    "s_branch 13b\n\t"
    "9:"
    : [tc] "=&s"(tc), [sp] "=&s"(sp), [cap] "+s"(cap), [st] "=&s"(st), [bg] "=&s"(bg)
    : [ag] "s"(ag), [c44] "s"(0x4444u), [kv] "s"(kv), [vs] "s"(vs), [od] "s"(od), [od2] "s"(od + 262144), [neg] "s"(-1.0e38f), [lsc] "s"(LSC), [one] "s"(0x3F803F80u), [big] "s"(24.0f), "{v[64:95]}"(W.w0), "{v[96:127]}"(W.w1), "{v[128:159]}"(W.w2), "{v[160:191]}"(W.w3), "{v[192:223]}"(W.w4), "{v[224:255]}"(W.w5)
    : "v6", "v7", "v8", "v9", "v10", "v11", "v12", "v13", "v14", "v15", "v16", "v17", "v18", "v19", "v20", "v21", "v22", "v23", "v24", "v25", "v26", "v27", "v28", "v29", "v30", "v31", "v32", "v33", "v34", "v35", "v36", "v37", "v38", "v39", "v40", "v41", "v42", "v43", "v44", "v45", "v46", "v47", "v48", "v49", "v50", "v51", "v52", "v53", "v54", "v55", "v56", "v57", "v58", "v59", "v60", "v61", "v62", "v63", "vcc", "scc", "memory");
}
template <> __device__ __forceinline__ void qk_step<4>(const QKW& W, const char* ag, const char* vs, const char* od, const char* kv, unsigned& cap, unsigned& sp,
                                                        unsigned long long& tc, unsigned long long* ts, unsigned& d0, unsigned& d1) {
  unsigned st, bg;
  asm volatile("v_mbcnt_lo_u32_b32 v52, -1, 0\n\t"
    "v_mbcnt_hi_u32_b32 v52, -1, v52\n\t"
    "v_lshlrev_b32 v6, 4, v52\n\t"
    "s_mov_b32 %[sp], 0\n\t"
    "global_load_dwordx4 v[36:39], v6, %[ag] sc1\n\t"
    "global_load_dwordx4 v[40:43], v6, %[ag] offset:1024 sc1\n\t"
    "s_memtime %[tc]\n\t"
    "v_add_u32 v7, 73728, v6\n\t"
    "v_add_u32 v8, 131072, v6\n\t"
    "ds_read_b128 v[20:23], v7 offset:8192\n\t"
    "ds_read_b128 v[24:27], v7 offset:9216\n\t"
    "ds_read_b128 v[28:31], v7 offset:0\n\t"
    "ds_read_b128 v[32:35], v7 offset:1024\n\t"
    "v_lshrrev_b32 v44, 4, v52\n\t"
    "v_and_b32 v45, 15, v52\n\t"
    "v_and_b32 v53, 4, v52\n\t"
    "v_mul_u32_u24 v53, 0x2aaa, v53\n\t"
    "v_lshrrev_b32 v9, 1, v52\n\t"
    "v_and_b32 v9, 2, v9\n\t"
    "v_or3_b32 v9, v9, v53, %[c44]\n\t"
    "v_lshlrev_b32 v53, 2, v44\n\t"
    "v_lshl_or_b32 v10, v45, 4, v53\n\t"
    "3:\n\t"
    "v_mov_b32 v12, 0\n\t"
    "v_mov_b32 v13, 0\n\t"
    "v_mov_b32 v14, 0\n\t"
    "v_mov_b32 v15, 0\n\t"
    "v_mov_b32 v16, 0\n\t"
    "v_mov_b32 v17, 0\n\t"
    "v_mov_b32 v18, 0\n\t"
    "v_mov_b32 v19, 0\n\t"
    "s_waitcnt vmcnt(0) lgkmcnt(0)\n\t"
    "14:\n\t"
    "ds_write_b128 v6, v[36:39] offset:8192\n\t"
    "ds_write_b128 v6, v[40:43] offset:9216\n\t"
    "v_smfmac_f32_16x16x64_bf16 v[12:15], v[36:39], v[128:135], v9\n\t"
    "v_max3_u32 v52, v36, v37, v38\n\t"
    "v_max3_u32 v53, v39, v40, v41\n\t"
    "v_smfmac_f32_16x16x64_bf16 v[16:19], v[36:39], v[136:143], v9\n\t"
    "v_max3_u32 v52, v52, v42, v43\n\t"
    "v_max_u32 v52, v52, v53\n\t"
    "v_cmp_eq_u32 vcc, -1, v52\n\t"
    "s_cbranch_vccnz 1f\n\t"
    "2:\n\t"
    "s_waitcnt lgkmcnt(0)\n\t"
    "s_barrier\n\t"
    "s_memrealtime s[36:37]\n\t"
    "ds_read_b128 v[36:39], v6 offset:0\n\t"
    "ds_read_b128 v[44:47], v6 offset:4096\n\t"
    "v_smfmac_f32_16x16x64_bf16 v[12:15], v[40:43], v[144:151], v9\n\t"
    "ds_read_b128 v[48:51], v6 offset:5120\n\t"
    "ds_read_b128 v[52:55], v6 offset:6144\n\t"
    "v_smfmac_f32_16x16x64_bf16 v[16:19], v[40:43], v[152:159], v9\n\t"
    "ds_read_b128 v[40:43], v6 offset:1024\n\t"
    "s_waitcnt lgkmcnt(0)\n\t"
    "s_mov_b32 s38, s36\n\t"
    "s_min_u32 s37, %[d0], %[d1]\n\t"
    "s_add_u32 s36, s36, s37\n\t"
    "s_and_b32 s36, s36, 0xffffff\n\t"
    "s_or_b32 s36, s36, %[sn]\n\t"
    "v_lshlrev_b32 v11, 10, v6\n\t"
    "v_mov_b32 v5, s36\n\t"
    "s_mov_b64 exec, 15\n\t"
    "global_store_dword v11, v5, %[vs] offset:-4096 sc1\n\t"
    "s_mov_b64 exec, -1\n\t"
    "s_sleep 0\n\t"
    "v_smfmac_f32_16x16x64_bf16 v[12:15], v[36:39], v[56:63], v9\n\t"
    "v_smfmac_f32_16x16x64_bf16 v[16:19], v[36:39], v[28:35], v9\n\t"
    "ds_read_b128 v[28:31], v7 offset:16384\n\t"
    "ds_read_b128 v[32:35], v7 offset:17408\n\t"
    "ds_read_b128 v[36:39], v6 offset:7168\n\t"
    "v_smfmac_f32_16x16x64_bf16 v[12:15], v[44:47], v[64:71], v9\n\t"
    "v_smfmac_f32_16x16x64_bf16 v[16:19], v[44:47], v[72:79], v9\n\t"
    "ds_read_b128 v[44:47], v6 offset:10240\n\t"
    "v_smfmac_f32_16x16x64_bf16 v[12:15], v[48:51], v[80:87], v9\n\t"
    "v_smfmac_f32_16x16x64_bf16 v[16:19], v[48:51], v[88:95], v9\n\t"
    "ds_read_b128 v[48:51], v6 offset:11264\n\t"
    "v_smfmac_f32_16x16x64_bf16 v[12:15], v[52:55], v[96:103], v9\n\t"
    "v_smfmac_f32_16x16x64_bf16 v[16:19], v[52:55], v[104:111], v9\n\t"
    "ds_read_b128 v[52:55], v6 offset:2048\n\t"
    "v_smfmac_f32_16x16x64_bf16 v[12:15], v[40:43], v[20:27], v9\n\t"
    "ds_read_b128 v[20:23], v8 offset:0\n\t"
    "ds_read_b128 v[24:27], v8 offset:1024\n\t"
    "s_waitcnt lgkmcnt(6)\n\t"
    "v_smfmac_f32_16x16x64_bf16 v[16:19], v[40:43], v[28:35], v9\n\t"
    "ds_read_b128 v[28:31], v8 offset:8192\n\t"
    "ds_read_b128 v[32:35], v8 offset:9216\n\t"
    "ds_read_b128 v[40:43], v6 offset:12288\n\t"
    "s_waitcnt lgkmcnt(8)\n\t"
    "v_smfmac_f32_16x16x64_bf16 v[12:15], v[36:39], v[112:119], v9\n\t"
    "v_smfmac_f32_16x16x64_bf16 v[16:19], v[36:39], v[120:127], v9\n\t"
    "ds_read_b128 v[36:39], v6 offset:13312\n\t"
    "s_waitcnt lgkmcnt(8)\n\t"
    "v_smfmac_f32_16x16x64_bf16 v[12:15], v[44:47], v[160:167], v9\n\t"
    "v_smfmac_f32_16x16x64_bf16 v[16:19], v[44:47], v[168:175], v9\n\t"
    "ds_read_b128 v[44:47], v6 offset:14336\n\t"
    "s_barrier\n\t"
    "s_setprio 2\n\t"
    "s_waitcnt lgkmcnt(8)\n\t"
    "v_smfmac_f32_16x16x64_bf16 v[12:15], v[48:51], v[176:183], v9\n\t"
    "v_smfmac_f32_16x16x64_bf16 v[16:19], v[48:51], v[184:191], v9\n\t"
    "ds_read_b128 v[48:51], v6 offset:3072\n\t"
    "s_waitcnt lgkmcnt(6)\n\t"
    "v_smfmac_f32_16x16x64_bf16 v[12:15], v[52:55], v[20:27], v9\n\t"
    "ds_read_b128 v[20:23], v8 offset:16384\n\t"
    "ds_read_b128 v[24:27], v8 offset:17408\n\t"
    "s_waitcnt lgkmcnt(6)\n\t"
    "v_smfmac_f32_16x16x64_bf16 v[16:19], v[52:55], v[28:35], v9\n\t"
    "ds_read_b128 v[28:31], v8 offset:24576\n\t"
    "ds_read_b128 v[32:35], v8 offset:25600\n\t"
    "ds_read_b128 v[52:55], v6 offset:15360\n\t"
    "s_waitcnt lgkmcnt(8)\n\t"
    "v_smfmac_f32_16x16x64_bf16 v[12:15], v[40:43], v[192:199], v9\n\t"
    "v_smfmac_f32_16x16x64_bf16 v[16:19], v[40:43], v[200:207], v9\n\t"
    "s_waitcnt lgkmcnt(7)\n\t"
    "v_smfmac_f32_16x16x64_bf16 v[12:15], v[36:39], v[208:215], v9\n\t"
    "v_smfmac_f32_16x16x64_bf16 v[16:19], v[36:39], v[216:223], v9\n\t"
    "s_waitcnt lgkmcnt(6)\n\t"
    "v_smfmac_f32_16x16x64_bf16 v[12:15], v[44:47], v[224:231], v9\n\t"
    "v_smfmac_f32_16x16x64_bf16 v[16:19], v[44:47], v[232:239], v9\n\t"
    "s_waitcnt lgkmcnt(3)\n\t"
    "v_smfmac_f32_16x16x64_bf16 v[12:15], v[48:51], v[20:27], v9\n\t"
    "s_waitcnt lgkmcnt(1)\n\t"
    "v_smfmac_f32_16x16x64_bf16 v[16:19], v[48:51], v[28:35], v9\n\t"
    "s_waitcnt lgkmcnt(0)\n\t"
    "v_smfmac_f32_16x16x64_bf16 v[12:15], v[52:55], v[240:247], v9\n\t"
    "v_smfmac_f32_16x16x64_bf16 v[16:19], v[52:55], v[248:255], v9\n\t"
    "s_nop 6\n\t"
    "v_permlane16_swap_b32 v12, v14\n\t"
    "v_permlane16_swap_b32 v13, v15\n\t"
    "v_permlane16_swap_b32 v16, v18\n\t"
    "v_permlane16_swap_b32 v17, v19\n\t"
    "v_add_f32 v12, v12, v14\n\t"
    "v_add_f32 v13, v13, v15\n\t"
    "v_add_f32 v16, v16, v18\n\t"
    "v_cvt_pk_bf16_f32 v12, v12, v13\n\t"
    "v_add_f32 v17, v17, v19\n\t"
    "ds_write_b32 v10, v12 offset:34816\n\t"
    "v_cvt_pk_bf16_f32 v16, v16, v17\n\t"
    "ds_write_b32 v10, v16 offset:35072\n\t"
    "s_waitcnt lgkmcnt(0)\n\t"
    "s_setprio 0\n\t"
    "s_barrier\n\t"
    "s_memrealtime s[36:37]\n\t"
    "s_waitcnt lgkmcnt(0)\n\t"
    "s_and_b32 s36, s36, 0xffffff\n\t"
    "s_or_b32 s36, s36, %[sn]\n\t"
    "v_lshlrev_b32 v7, 10, v6\n\t"
    "v_mov_b32 v8, s36\n\t"
    "s_mov_b64 exec, 15\n\t"
    "global_store_dword v7, v8, %[vs] offset:0 sc1\n\t"
    "s_mov_b64 exec, -1\n\t"
    "s_barrier\n\t"
    "s_memrealtime s[36:37]\n\t"
    "s_mov_b32 %[d1], %[d0]\n\t"
    "s_waitcnt lgkmcnt(0)\n\t"
    "s_sub_u32 %[d0], s36, s38\n\t"
    "s_min_u32 %[d0], %[d0], 200\n\t"
    "s_waitcnt lgkmcnt(0)\n\t"
    "s_branch 9f\n\t"
    "1:\n\t"
    "s_add_u32 %[sp], %[sp], 1\n\t"
    "s_cmp_ge_u32 %[sp], %[cap]\n\t"
    "s_cbranch_scc1 7f\n\t"
    "global_load_dwordx4 v[36:39], v6, %[ag] sc1\n\t"
    "global_load_dwordx4 v[40:43], v6, %[ag] offset:1024 sc1\n\t"
    "s_waitcnt lgkmcnt(0)\n\t"
    "s_memtime %[tc]\n\t"
    "s_nop 7\n\t"
    "s_branch 3b\n\t"
    "7:\n\t"
    "s_mov_b32 %[cap], 0\n\t"
    "s_branch 2b\n\t"
    "9:"
    : [tc] "=&s"(tc), [sp] "=&s"(sp), [cap] "+s"(cap), [d0] "+s"(d0), [d1] "+s"(d1)
    : [ag] "s"(ag), [c44] "s"(0x4444u), [vs] "s"(vs), [sn] "s"((unsigned)(size_t)od), "{v[64:95]}"(W.w0), "{v[96:127]}"(W.w1), "{v[128:159]}"(W.w2), "{v[160:191]}"(W.w3), "{v[192:223]}"(W.w4), "{v[224:255]}"(W.w5), "{v[56:63]}"(W.x)
    : "v5", "v6", "v7", "v8", "v9", "v10", "v11", "v12", "v13", "v14", "v15", "v16", "v17", "v18", "v19", "v20", "v21", "v22", "v23", "v24", "v25", "v26", "v27", "v28", "v29", "v30", "v31", "v32", "v33", "v34", "v35", "v36", "v37", "v38", "v39", "v40", "v41", "v42", "v43", "v44", "v45", "v46", "v47", "v48", "v49", "v50", "v51", "v52", "v53", "v54", "v55", "vcc", "scc", "memory", "s36", "s37", "s38");
}
template <> __device__ __forceinline__ void qk_step<5>(const QKW& W, const char* ag, const char* vs, const char* od, const char* kv, unsigned& cap, unsigned& sp,
                                                        unsigned long long& tc, unsigned long long* ts, unsigned& d0, unsigned& d1) {
  unsigned st, bg;
  asm volatile("v_mbcnt_lo_u32_b32 v52, -1, 0\n\t"
    "v_mbcnt_hi_u32_b32 v52, -1, v52\n\t"
    "v_lshlrev_b32 v6, 4, v52\n\t"
    "s_mov_b32 %[sp], 0\n\t"
    "global_load_dwordx4 v[36:39], v6, %[ag] sc1\n\t"
    "global_load_dwordx4 v[40:43], v6, %[ag] offset:1024 sc1\n\t"
    "s_memtime %[tc]\n\t"
    "v_add_u32 v7, 75776, v6\n\t"
    "v_add_u32 v8, 133120, v6\n\t"
    "ds_read_b128 v[20:23], v7 offset:8192\n\t"
    "ds_read_b128 v[24:27], v7 offset:9216\n\t"
    "ds_read_b128 v[28:31], v7 offset:0\n\t"
    "ds_read_b128 v[32:35], v7 offset:1024\n\t"
    "v_lshrrev_b32 v44, 4, v52\n\t"
    "v_and_b32 v45, 15, v52\n\t"
    "v_and_b32 v53, 4, v52\n\t"
    "v_mul_u32_u24 v53, 0x2aaa, v53\n\t"
    "v_lshrrev_b32 v9, 1, v52\n\t"
    "v_and_b32 v9, 2, v9\n\t"
    "v_or3_b32 v9, v9, v53, %[c44]\n\t"
    "v_lshlrev_b32 v53, 2, v44\n\t"
    "v_lshl_or_b32 v10, v45, 4, v53\n\t"
    "v_and_b32 v53, 1, v52\n\t"
    "v_lshlrev_b32 v53, 3, v53\n\t"
    "v_lshl_or_b32 v11, v44, 7, v53\n\t"
    "v_and_b32 v53, 12, v52\n\t"
    "v_lshl_or_b32 v11, v53, 2, v11\n\t"
    "v_and_b32 v53, 2, v52\n\t"
    "v_lshl_or_b32 v11, v53, 8, v11\n\t"
    "3:\n\t"
    "v_mov_b32 v12, 0\n\t"
    "v_mov_b32 v13, 0\n\t"
    "v_mov_b32 v14, 0\n\t"
    "v_mov_b32 v15, 0\n\t"
    "v_mov_b32 v16, 0\n\t"
    "v_mov_b32 v17, 0\n\t"
    "v_mov_b32 v18, 0\n\t"
    "v_mov_b32 v19, 0\n\t"
    "s_waitcnt vmcnt(0) lgkmcnt(0)\n\t"
    "14:\n\t"
    "ds_write_b128 v6, v[36:39] offset:10240\n\t"
    "ds_write_b128 v6, v[40:43] offset:11264\n\t"
    "v_smfmac_f32_16x16x64_bf16 v[12:15], v[36:39], v[160:167], v9\n\t"
    "v_max3_u32 v52, v36, v37, v38\n\t"
    "v_max3_u32 v53, v39, v40, v41\n\t"
    "v_smfmac_f32_16x16x64_bf16 v[16:19], v[36:39], v[168:175], v9\n\t"
    "v_max3_u32 v52, v52, v42, v43\n\t"
    "v_max_u32 v52, v52, v53\n\t"
    "v_cmp_eq_u32 vcc, -1, v52\n\t"
    "s_cbranch_vccnz 1f\n\t"
    "2:\n\t"
    "s_waitcnt lgkmcnt(0)\n\t"
    "s_barrier\n\t"
    "ds_read_b128 v[36:39], v6 offset:0\n\t"
    "ds_read_b128 v[44:47], v6 offset:4096\n\t"
    "v_smfmac_f32_16x16x64_bf16 v[12:15], v[40:43], v[176:183], v9\n\t"
    "ds_read_b128 v[48:51], v6 offset:5120\n\t"
    "ds_read_b128 v[52:55], v6 offset:6144\n\t"
    "v_smfmac_f32_16x16x64_bf16 v[16:19], v[40:43], v[184:191], v9\n\t"
    "ds_read_b128 v[40:43], v6 offset:1024\n\t"
    "s_sleep 0\n\t"
    "s_waitcnt lgkmcnt(4)\n\t"
    "v_smfmac_f32_16x16x64_bf16 v[12:15], v[36:39], v[56:63], v9\n\t"
    "v_smfmac_f32_16x16x64_bf16 v[16:19], v[36:39], v[28:35], v9\n\t"
    "ds_read_b128 v[28:31], v7 offset:16384\n\t"
    "ds_read_b128 v[32:35], v7 offset:17408\n\t"
    "ds_read_b128 v[36:39], v6 offset:7168\n\t"
    "s_waitcnt lgkmcnt(6)\n\t"
    "v_smfmac_f32_16x16x64_bf16 v[12:15], v[44:47], v[64:71], v9\n\t"
    "v_smfmac_f32_16x16x64_bf16 v[16:19], v[44:47], v[72:79], v9\n\t"
    "ds_read_b128 v[44:47], v6 offset:8192\n\t"
    "s_waitcnt lgkmcnt(6)\n\t"
    "v_smfmac_f32_16x16x64_bf16 v[12:15], v[48:51], v[80:87], v9\n\t"
    "v_smfmac_f32_16x16x64_bf16 v[16:19], v[48:51], v[88:95], v9\n\t"
    "ds_read_b128 v[48:51], v6 offset:9216\n\t"
    "s_waitcnt lgkmcnt(6)\n\t"
    "v_smfmac_f32_16x16x64_bf16 v[12:15], v[52:55], v[96:103], v9\n\t"
    "v_smfmac_f32_16x16x64_bf16 v[16:19], v[52:55], v[104:111], v9\n\t"
    "ds_read_b128 v[52:55], v6 offset:2048\n\t"
    "s_waitcnt lgkmcnt(6)\n\t"
    "v_smfmac_f32_16x16x64_bf16 v[12:15], v[40:43], v[20:27], v9\n\t"
    "ds_read_b128 v[20:23], v8 offset:0\n\t"
    "ds_read_b128 v[24:27], v8 offset:1024\n\t"
    "s_waitcnt lgkmcnt(6)\n\t"
    "v_smfmac_f32_16x16x64_bf16 v[16:19], v[40:43], v[28:35], v9\n\t"
    "ds_read_b128 v[28:31], v8 offset:8192\n\t"
    "ds_read_b128 v[32:35], v8 offset:9216\n\t"
    "ds_read_b128 v[40:43], v6 offset:12288\n\t"
    "s_waitcnt lgkmcnt(8)\n\t"
    "v_smfmac_f32_16x16x64_bf16 v[12:15], v[36:39], v[112:119], v9\n\t"
    "v_smfmac_f32_16x16x64_bf16 v[16:19], v[36:39], v[120:127], v9\n\t"
    "ds_read_b128 v[36:39], v6 offset:13312\n\t"
    "s_waitcnt lgkmcnt(8)\n\t"
    "v_smfmac_f32_16x16x64_bf16 v[12:15], v[44:47], v[128:135], v9\n\t"
    "v_smfmac_f32_16x16x64_bf16 v[16:19], v[44:47], v[136:143], v9\n\t"
    "ds_read_b128 v[44:47], v6 offset:14336\n\t"
    "s_barrier\n\t"
    "s_setprio 2\n\t"
    "s_waitcnt lgkmcnt(8)\n\t"
    "v_smfmac_f32_16x16x64_bf16 v[12:15], v[48:51], v[144:151], v9\n\t"
    "v_smfmac_f32_16x16x64_bf16 v[16:19], v[48:51], v[152:159], v9\n\t"
    "ds_read_b128 v[48:51], v6 offset:3072\n\t"
    "s_waitcnt lgkmcnt(6)\n\t"
    "v_smfmac_f32_16x16x64_bf16 v[12:15], v[52:55], v[20:27], v9\n\t"
    "ds_read_b128 v[20:23], v8 offset:16384\n\t"
    "ds_read_b128 v[24:27], v8 offset:17408\n\t"
    "s_waitcnt lgkmcnt(6)\n\t"
    "v_smfmac_f32_16x16x64_bf16 v[16:19], v[52:55], v[28:35], v9\n\t"
    "ds_read_b128 v[28:31], v8 offset:24576\n\t"
    "ds_read_b128 v[32:35], v8 offset:25600\n\t"
    "ds_read_b128 v[52:55], v6 offset:15360\n\t"
    "s_waitcnt lgkmcnt(8)\n\t"
    "v_smfmac_f32_16x16x64_bf16 v[12:15], v[40:43], v[192:199], v9\n\t"
    "v_smfmac_f32_16x16x64_bf16 v[16:19], v[40:43], v[200:207], v9\n\t"
    "s_waitcnt lgkmcnt(7)\n\t"
    "v_smfmac_f32_16x16x64_bf16 v[12:15], v[36:39], v[208:215], v9\n\t"
    "v_smfmac_f32_16x16x64_bf16 v[16:19], v[36:39], v[216:223], v9\n\t"
    "s_waitcnt lgkmcnt(6)\n\t"
    "v_smfmac_f32_16x16x64_bf16 v[12:15], v[44:47], v[224:231], v9\n\t"
    "v_smfmac_f32_16x16x64_bf16 v[16:19], v[44:47], v[232:239], v9\n\t"
    "s_waitcnt lgkmcnt(3)\n\t"
    "v_smfmac_f32_16x16x64_bf16 v[12:15], v[48:51], v[20:27], v9\n\t"
    "s_waitcnt lgkmcnt(1)\n\t"
    "v_smfmac_f32_16x16x64_bf16 v[16:19], v[48:51], v[28:35], v9\n\t"
    "s_waitcnt lgkmcnt(0)\n\t"
    "v_smfmac_f32_16x16x64_bf16 v[12:15], v[52:55], v[240:247], v9\n\t"
    "v_smfmac_f32_16x16x64_bf16 v[16:19], v[52:55], v[248:255], v9\n\t"
    "s_nop 6\n\t"
    "v_permlane16_swap_b32 v12, v14\n\t"
    "v_permlane16_swap_b32 v13, v15\n\t"
    "v_permlane16_swap_b32 v16, v18\n\t"
    "v_permlane16_swap_b32 v17, v19\n\t"
    "v_add_f32 v12, v12, v14\n\t"
    "v_add_f32 v13, v13, v15\n\t"
    "v_add_f32 v16, v16, v18\n\t"
    "v_cvt_pk_bf16_f32 v12, v12, v13\n\t"
    "v_add_f32 v17, v17, v19\n\t"
    "ds_write_b32 v10, v12 offset:35328\n\t"
    "v_cvt_pk_bf16_f32 v16, v16, v17\n\t"
    "ds_write_b32 v10, v16 offset:35584\n\t"
    "s_waitcnt lgkmcnt(0)\n\t"
    "s_setprio 0\n\t"
    "s_barrier\n\t"
    "ds_read_b64_tr_b16 v[36:37], v11 offset:34816\n\t"
    "ds_read_b64_tr_b16 v[38:39], v11 offset:34880\n\t"
    "ds_read_b64_tr_b16 v[40:41], v11 offset:35840\n\t"
    "ds_read_b64_tr_b16 v[42:43], v11 offset:35904\n\t"
    "s_waitcnt lgkmcnt(0)\n\t"
    "v_min_u32 v36, -2, v36\n\t"
    "v_min_u32 v37, -2, v37\n\t"
    "v_min_u32 v38, -2, v38\n\t"
    "v_min_u32 v39, -2, v39\n\t"
    "v_min_u32 v40, -2, v40\n\t"
    "v_min_u32 v41, -2, v41\n\t"
    "v_min_u32 v42, -2, v42\n\t"
    "v_min_u32 v43, -2, v43\n\t"
    "global_store_dwordx4 v6, v[36:39], %[vs] sc1\n\t"
    "global_store_dwordx4 v6, v[40:43], %[vs] offset:1024 sc1\n\t"
    "s_nop 1\n\t"
    "s_barrier\n\t"
    "s_waitcnt lgkmcnt(0)\n\t"
    "s_branch 9f\n\t"
    "1:\n\t"
    "s_add_u32 %[sp], %[sp], 1\n\t"
    "s_cmp_ge_u32 %[sp], %[cap]\n\t"
    "s_cbranch_scc1 7f\n\t"
    "global_load_dwordx4 v[36:39], v6, %[ag] sc1\n\t"
    "global_load_dwordx4 v[40:43], v6, %[ag] offset:1024 sc1\n\t"
    "s_waitcnt lgkmcnt(0)\n\t"
    "s_memtime %[tc]\n\t"
    "s_nop 7\n\t"
    "s_branch 3b\n\t"
    "7:\n\t"
    "s_mov_b32 %[cap], 0\n\t"
    "s_branch 2b\n\t"
    "9:"
    : [tc] "=&s"(tc), [sp] "=&s"(sp), [cap] "+s"(cap)
    : [ag] "s"(ag), [c44] "s"(0x4444u), [vs] "s"(vs), "{v[64:95]}"(W.w0), "{v[96:127]}"(W.w1), "{v[128:159]}"(W.w2), "{v[160:191]}"(W.w3), "{v[192:223]}"(W.w4), "{v[224:255]}"(W.w5), "{v[56:63]}"(W.x)
    : "v6", "v7", "v8", "v9", "v10", "v11", "v12", "v13", "v14", "v15", "v16", "v17", "v18", "v19", "v20", "v21", "v22", "v23", "v24", "v25", "v26", "v27", "v28", "v29", "v30", "v31", "v32", "v33", "v34", "v35", "v36", "v37", "v38", "v39", "v40", "v41", "v42", "v43", "v44", "v45", "v46", "v47", "v48", "v49", "v50", "v51", "v52", "v53", "v54", "v55", "vcc", "scc", "memory");
}
template <> __device__ __forceinline__ void qk_step<6>(const QKW& W, const char* ag, const char* vs, const char* od, const char* kv, unsigned& cap, unsigned& sp,
                                                        unsigned long long& tc, unsigned long long* ts, unsigned& d0, unsigned& d1) {
  unsigned st, bg;
  asm volatile("v_mbcnt_lo_u32_b32 v52, -1, 0\n\t"
    "v_mbcnt_hi_u32_b32 v52, -1, v52\n\t"
    "v_lshlrev_b32 v6, 4, v52\n\t"
    "s_mov_b32 %[sp], 0\n\t"
    "global_load_dwordx4 v[36:39], v6, %[ag] sc1\n\t"
    "global_load_dwordx4 v[40:43], v6, %[ag] offset:1024 sc1\n\t"
    "s_memtime %[tc]\n\t"
    "v_add_u32 v7, 77824, v6\n\t"
    "v_add_u32 v8, 135168, v6\n\t"
    "ds_read_b128 v[20:23], v7 offset:8192\n\t"
    "ds_read_b128 v[24:27], v7 offset:9216\n\t"
    "ds_read_b128 v[28:31], v7 offset:0\n\t"
    "ds_read_b128 v[32:35], v7 offset:1024\n\t"
    "v_lshrrev_b32 v44, 4, v52\n\t"
    "v_and_b32 v45, 15, v52\n\t"
    "v_and_b32 v53, 4, v52\n\t"
    "v_mul_u32_u24 v53, 0x2aaa, v53\n\t"
    "v_lshrrev_b32 v9, 1, v52\n\t"
    "v_and_b32 v9, 2, v9\n\t"
    "v_or3_b32 v9, v9, v53, %[c44]\n\t"
    "v_lshlrev_b32 v53, 2, v44\n\t"
    "v_lshl_or_b32 v10, v45, 4, v53\n\t"
    "3:\n\t"
    "v_mov_b32 v12, 0\n\t"
    "v_mov_b32 v13, 0\n\t"
    "v_mov_b32 v14, 0\n\t"
    "v_mov_b32 v15, 0\n\t"
    "v_mov_b32 v16, 0\n\t"
    "v_mov_b32 v17, 0\n\t"
    "v_mov_b32 v18, 0\n\t"
    "v_mov_b32 v19, 0\n\t"
    "s_waitcnt vmcnt(0) lgkmcnt(0)\n\t"
    "14:\n\t"
    "ds_write_b128 v6, v[36:39] offset:12288\n\t"
    "ds_write_b128 v6, v[40:43] offset:13312\n\t"
    "v_smfmac_f32_16x16x64_bf16 v[12:15], v[36:39], v[192:199], v9\n\t"
    "v_max3_u32 v52, v36, v37, v38\n\t"
    "v_max3_u32 v53, v39, v40, v41\n\t"
    "v_smfmac_f32_16x16x64_bf16 v[16:19], v[36:39], v[200:207], v9\n\t"
    "v_max3_u32 v52, v52, v42, v43\n\t"
    "v_max_u32 v52, v52, v53\n\t"
    "v_cmp_eq_u32 vcc, -1, v52\n\t"
    "s_cbranch_vccnz 1f\n\t"
    "2:\n\t"
    "s_waitcnt lgkmcnt(0)\n\t"
    "s_barrier\n\t"
    "ds_read_b128 v[36:39], v6 offset:0\n\t"
    "ds_read_b128 v[44:47], v6 offset:4096\n\t"
    "v_smfmac_f32_16x16x64_bf16 v[12:15], v[40:43], v[208:215], v9\n\t"
    "ds_read_b128 v[48:51], v6 offset:5120\n\t"
    "ds_read_b128 v[52:55], v6 offset:6144\n\t"
    "v_smfmac_f32_16x16x64_bf16 v[16:19], v[40:43], v[216:223], v9\n\t"
    "ds_read_b128 v[40:43], v6 offset:1024\n\t"
    "s_sleep 0\n\t"
    "s_waitcnt lgkmcnt(4)\n\t"
    "v_smfmac_f32_16x16x64_bf16 v[12:15], v[36:39], v[56:63], v9\n\t"
    "v_smfmac_f32_16x16x64_bf16 v[16:19], v[36:39], v[28:35], v9\n\t"
    "ds_read_b128 v[28:31], v7 offset:16384\n\t"
    "ds_read_b128 v[32:35], v7 offset:17408\n\t"
    "ds_read_b128 v[36:39], v6 offset:7168\n\t"
    "s_waitcnt lgkmcnt(6)\n\t"
    "v_smfmac_f32_16x16x64_bf16 v[12:15], v[44:47], v[64:71], v9\n\t"
    "v_smfmac_f32_16x16x64_bf16 v[16:19], v[44:47], v[72:79], v9\n\t"
    "ds_read_b128 v[44:47], v6 offset:8192\n\t"
    "s_waitcnt lgkmcnt(6)\n\t"
    "v_smfmac_f32_16x16x64_bf16 v[12:15], v[48:51], v[80:87], v9\n\t"
    "v_smfmac_f32_16x16x64_bf16 v[16:19], v[48:51], v[88:95], v9\n\t"
    "ds_read_b128 v[48:51], v6 offset:9216\n\t"
    "s_waitcnt lgkmcnt(6)\n\t"
    "v_smfmac_f32_16x16x64_bf16 v[12:15], v[52:55], v[96:103], v9\n\t"
    "v_smfmac_f32_16x16x64_bf16 v[16:19], v[52:55], v[104:111], v9\n\t"
    "ds_read_b128 v[52:55], v6 offset:2048\n\t"
    "s_waitcnt lgkmcnt(6)\n\t"
    "v_smfmac_f32_16x16x64_bf16 v[12:15], v[40:43], v[20:27], v9\n\t"
    "ds_read_b128 v[20:23], v8 offset:0\n\t"
    "ds_read_b128 v[24:27], v8 offset:1024\n\t"
    "s_waitcnt lgkmcnt(6)\n\t"
    "v_smfmac_f32_16x16x64_bf16 v[16:19], v[40:43], v[28:35], v9\n\t"
    "ds_read_b128 v[28:31], v8 offset:8192\n\t"
    "ds_read_b128 v[32:35], v8 offset:9216\n\t"
    "ds_read_b128 v[40:43], v6 offset:10240\n\t"
    "s_waitcnt lgkmcnt(8)\n\t"
    "v_smfmac_f32_16x16x64_bf16 v[12:15], v[36:39], v[112:119], v9\n\t"
    "v_smfmac_f32_16x16x64_bf16 v[16:19], v[36:39], v[120:127], v9\n\t"
    "ds_read_b128 v[36:39], v6 offset:11264\n\t"
    "s_waitcnt lgkmcnt(8)\n\t"
    "v_smfmac_f32_16x16x64_bf16 v[12:15], v[44:47], v[128:135], v9\n\t"
    "v_smfmac_f32_16x16x64_bf16 v[16:19], v[44:47], v[136:143], v9\n\t"
    "ds_read_b128 v[44:47], v6 offset:14336\n\t"
    "s_barrier\n\t"
    "s_setprio 2\n\t"
    "s_waitcnt lgkmcnt(8)\n\t"
    "v_smfmac_f32_16x16x64_bf16 v[12:15], v[48:51], v[144:151], v9\n\t"
    "v_smfmac_f32_16x16x64_bf16 v[16:19], v[48:51], v[152:159], v9\n\t"
    "ds_read_b128 v[48:51], v6 offset:3072\n\t"
    "s_waitcnt lgkmcnt(6)\n\t"
    "v_smfmac_f32_16x16x64_bf16 v[12:15], v[52:55], v[20:27], v9\n\t"
    "ds_read_b128 v[20:23], v8 offset:16384\n\t"
    "ds_read_b128 v[24:27], v8 offset:17408\n\t"
    "s_waitcnt lgkmcnt(6)\n\t"
    "v_smfmac_f32_16x16x64_bf16 v[16:19], v[52:55], v[28:35], v9\n\t"
    "ds_read_b128 v[28:31], v8 offset:24576\n\t"
    "ds_read_b128 v[32:35], v8 offset:25600\n\t"
    "ds_read_b128 v[52:55], v6 offset:15360\n\t"
    "s_waitcnt lgkmcnt(8)\n\t"
    "v_smfmac_f32_16x16x64_bf16 v[12:15], v[40:43], v[160:167], v9\n\t"
    "v_smfmac_f32_16x16x64_bf16 v[16:19], v[40:43], v[168:175], v9\n\t"
    "s_waitcnt lgkmcnt(7)\n\t"
    "v_smfmac_f32_16x16x64_bf16 v[12:15], v[36:39], v[176:183], v9\n\t"
    "v_smfmac_f32_16x16x64_bf16 v[16:19], v[36:39], v[184:191], v9\n\t"
    "s_waitcnt lgkmcnt(6)\n\t"
    "v_smfmac_f32_16x16x64_bf16 v[12:15], v[44:47], v[224:231], v9\n\t"
    "v_smfmac_f32_16x16x64_bf16 v[16:19], v[44:47], v[232:239], v9\n\t"
    "s_waitcnt lgkmcnt(3)\n\t"
    "v_smfmac_f32_16x16x64_bf16 v[12:15], v[48:51], v[20:27], v9\n\t"
    "s_waitcnt lgkmcnt(1)\n\t"
    "v_smfmac_f32_16x16x64_bf16 v[16:19], v[48:51], v[28:35], v9\n\t"
    "s_waitcnt lgkmcnt(0)\n\t"
    "v_smfmac_f32_16x16x64_bf16 v[12:15], v[52:55], v[240:247], v9\n\t"
    "v_smfmac_f32_16x16x64_bf16 v[16:19], v[52:55], v[248:255], v9\n\t"
    "s_nop 6\n\t"
    "v_permlane16_swap_b32 v12, v14\n\t"
    "v_permlane16_swap_b32 v13, v15\n\t"
    "v_permlane16_swap_b32 v16, v18\n\t"
    "v_permlane16_swap_b32 v17, v19\n\t"
    "v_add_f32 v12, v12, v14\n\t"
    "v_add_f32 v13, v13, v15\n\t"
    "v_add_f32 v16, v16, v18\n\t"
    "v_cvt_pk_bf16_f32 v12, v12, v13\n\t"
    "v_add_f32 v17, v17, v19\n\t"
    "ds_write_b32 v10, v12 offset:35840\n\t"
    "v_cvt_pk_bf16_f32 v16, v16, v17\n\t"
    "ds_write_b32 v10, v16 offset:36096\n\t"
    "s_waitcnt lgkmcnt(0)\n\t"
    "s_setprio 0\n\t"
    "s_barrier\n\t"
    "s_barrier\n\t"
    "s_waitcnt lgkmcnt(0)\n\t"
    "s_branch 9f\n\t"
    "1:\n\t"
    "s_add_u32 %[sp], %[sp], 1\n\t"
    "s_cmp_ge_u32 %[sp], %[cap]\n\t"
    "s_cbranch_scc1 7f\n\t"
    "global_load_dwordx4 v[36:39], v6, %[ag] sc1\n\t"
    "global_load_dwordx4 v[40:43], v6, %[ag] offset:1024 sc1\n\t"
    "s_waitcnt lgkmcnt(0)\n\t"
    "s_memtime %[tc]\n\t"
    "s_nop 7\n\t"
    "s_branch 3b\n\t"
    "7:\n\t"
    "s_mov_b32 %[cap], 0\n\t"
    "s_branch 2b\n\t"
    "9:"
    : [tc] "=&s"(tc), [sp] "=&s"(sp), [cap] "+s"(cap)
    : [ag] "s"(ag), [c44] "s"(0x4444u), "{v[64:95]}"(W.w0), "{v[96:127]}"(W.w1), "{v[128:159]}"(W.w2), "{v[160:191]}"(W.w3), "{v[192:223]}"(W.w4), "{v[224:255]}"(W.w5), "{v[56:63]}"(W.x)
    : "v6", "v7", "v8", "v9", "v10", "v11", "v12", "v13", "v14", "v15", "v16", "v17", "v18", "v19", "v20", "v21", "v22", "v23", "v24", "v25", "v26", "v27", "v28", "v29", "v30", "v31", "v32", "v33", "v34", "v35", "v36", "v37", "v38", "v39", "v40", "v41", "v42", "v43", "v44", "v45", "v46", "v47", "v48", "v49", "v50", "v51", "v52", "v53", "v54", "v55", "vcc", "scc", "memory");
}
template <> __device__ __forceinline__ void qk_step<7>(const QKW& W, const char* ag, const char* vs, const char* od, const char* kv, unsigned& cap, unsigned& sp,
                                                        unsigned long long& tc, unsigned long long* ts, unsigned& d0, unsigned& d1) {
  unsigned st, bg;
  asm volatile("v_mbcnt_lo_u32_b32 v52, -1, 0\n\t"
    "v_mbcnt_hi_u32_b32 v52, -1, v52\n\t"
    "v_lshlrev_b32 v6, 4, v52\n\t"
    "s_mov_b32 %[sp], 0\n\t"
    "global_load_dwordx4 v[36:39], v6, %[ag] sc1\n\t"
    "global_load_dwordx4 v[40:43], v6, %[ag] offset:1024 sc1\n\t"
    "s_memtime %[tc]\n\t"
    "v_add_u32 v7, 79872, v6\n\t"
    "v_add_u32 v8, 137216, v6\n\t"
    "ds_read_b128 v[20:23], v7 offset:8192\n\t"
    "ds_read_b128 v[24:27], v7 offset:9216\n\t"
    "ds_read_b128 v[28:31], v7 offset:0\n\t"
    "ds_read_b128 v[32:35], v7 offset:1024\n\t"
    "v_lshrrev_b32 v44, 4, v52\n\t"
    "v_and_b32 v45, 15, v52\n\t"
    "v_and_b32 v53, 4, v52\n\t"
    "v_mul_u32_u24 v53, 0x2aaa, v53\n\t"
    "v_lshrrev_b32 v9, 1, v52\n\t"
    "v_and_b32 v9, 2, v9\n\t"
    "v_or3_b32 v9, v9, v53, %[c44]\n\t"
    "v_lshlrev_b32 v53, 2, v44\n\t"
    "v_lshl_or_b32 v10, v45, 4, v53\n\t"
    "3:\n\t"
    "v_mov_b32 v12, 0\n\t"
    "v_mov_b32 v13, 0\n\t"
    "v_mov_b32 v14, 0\n\t"
    "v_mov_b32 v15, 0\n\t"
    "v_mov_b32 v16, 0\n\t"
    "v_mov_b32 v17, 0\n\t"
    "v_mov_b32 v18, 0\n\t"
    "v_mov_b32 v19, 0\n\t"
    "s_waitcnt vmcnt(0) lgkmcnt(0)\n\t"
    "14:\n\t"
    "ds_write_b128 v6, v[36:39] offset:14336\n\t"
    "ds_write_b128 v6, v[40:43] offset:15360\n\t"
    "v_smfmac_f32_16x16x64_bf16 v[12:15], v[36:39], v[224:231], v9\n\t"
    "v_max3_u32 v52, v36, v37, v38\n\t"
    "v_max3_u32 v53, v39, v40, v41\n\t"
    "v_smfmac_f32_16x16x64_bf16 v[16:19], v[36:39], v[232:239], v9\n\t"
    "v_max3_u32 v52, v52, v42, v43\n\t"
    "v_max_u32 v52, v52, v53\n\t"
    "v_cmp_eq_u32 vcc, -1, v52\n\t"
    "s_cbranch_vccnz 1f\n\t"
    "2:\n\t"
    "s_waitcnt lgkmcnt(0)\n\t"
    "s_barrier\n\t"
    "ds_read_b128 v[36:39], v6 offset:0\n\t"
    "ds_read_b128 v[44:47], v6 offset:4096\n\t"
    "v_smfmac_f32_16x16x64_bf16 v[12:15], v[40:43], v[240:247], v9\n\t"
    "ds_read_b128 v[48:51], v6 offset:5120\n\t"
    "ds_read_b128 v[52:55], v6 offset:6144\n\t"
    "v_smfmac_f32_16x16x64_bf16 v[16:19], v[40:43], v[248:255], v9\n\t"
    "ds_read_b128 v[40:43], v6 offset:1024\n\t"
    "s_sleep 0\n\t"
    "s_waitcnt lgkmcnt(4)\n\t"
    "v_smfmac_f32_16x16x64_bf16 v[12:15], v[36:39], v[56:63], v9\n\t"
    "v_smfmac_f32_16x16x64_bf16 v[16:19], v[36:39], v[28:35], v9\n\t"
    "ds_read_b128 v[28:31], v7 offset:16384\n\t"
    "ds_read_b128 v[32:35], v7 offset:17408\n\t"
    "ds_read_b128 v[36:39], v6 offset:7168\n\t"
    "s_waitcnt lgkmcnt(6)\n\t"
    "v_smfmac_f32_16x16x64_bf16 v[12:15], v[44:47], v[64:71], v9\n\t"
    "v_smfmac_f32_16x16x64_bf16 v[16:19], v[44:47], v[72:79], v9\n\t"
    "ds_read_b128 v[44:47], v6 offset:8192\n\t"
    "s_waitcnt lgkmcnt(6)\n\t"
    "v_smfmac_f32_16x16x64_bf16 v[12:15], v[48:51], v[80:87], v9\n\t"
    "v_smfmac_f32_16x16x64_bf16 v[16:19], v[48:51], v[88:95], v9\n\t"
    "ds_read_b128 v[48:51], v6 offset:9216\n\t"
    "s_waitcnt lgkmcnt(6)\n\t"
    "v_smfmac_f32_16x16x64_bf16 v[12:15], v[52:55], v[96:103], v9\n\t"
    "v_smfmac_f32_16x16x64_bf16 v[16:19], v[52:55], v[104:111], v9\n\t"
    "ds_read_b128 v[52:55], v6 offset:2048\n\t"
    "s_waitcnt lgkmcnt(6)\n\t"
    "v_smfmac_f32_16x16x64_bf16 v[12:15], v[40:43], v[20:27], v9\n\t"
    "ds_read_b128 v[20:23], v8 offset:0\n\t"
    "ds_read_b128 v[24:27], v8 offset:1024\n\t"
    "s_waitcnt lgkmcnt(6)\n\t"
    "v_smfmac_f32_16x16x64_bf16 v[16:19], v[40:43], v[28:35], v9\n\t"
    "ds_read_b128 v[28:31], v8 offset:8192\n\t"
    "ds_read_b128 v[32:35], v8 offset:9216\n\t"
    "ds_read_b128 v[40:43], v6 offset:10240\n\t"
    "s_waitcnt lgkmcnt(8)\n\t"
    "v_smfmac_f32_16x16x64_bf16 v[12:15], v[36:39], v[112:119], v9\n\t"
    "v_smfmac_f32_16x16x64_bf16 v[16:19], v[36:39], v[120:127], v9\n\t"
    "ds_read_b128 v[36:39], v6 offset:11264\n\t"
    "s_waitcnt lgkmcnt(8)\n\t"
    "v_smfmac_f32_16x16x64_bf16 v[12:15], v[44:47], v[128:135], v9\n\t"
    "v_smfmac_f32_16x16x64_bf16 v[16:19], v[44:47], v[136:143], v9\n\t"
    "ds_read_b128 v[44:47], v6 offset:12288\n\t"
    "s_barrier\n\t"
    "s_setprio 2\n\t"
    "s_waitcnt lgkmcnt(8)\n\t"
    "v_smfmac_f32_16x16x64_bf16 v[12:15], v[48:51], v[144:151], v9\n\t"
    "v_smfmac_f32_16x16x64_bf16 v[16:19], v[48:51], v[152:159], v9\n\t"
    "ds_read_b128 v[48:51], v6 offset:3072\n\t"
    "s_waitcnt lgkmcnt(6)\n\t"
    "v_smfmac_f32_16x16x64_bf16 v[12:15], v[52:55], v[20:27], v9\n\t"
    "ds_read_b128 v[20:23], v8 offset:16384\n\t"
    "ds_read_b128 v[24:27], v8 offset:17408\n\t"
    "s_waitcnt lgkmcnt(6)\n\t"
    "v_smfmac_f32_16x16x64_bf16 v[16:19], v[52:55], v[28:35], v9\n\t"
    "ds_read_b128 v[28:31], v8 offset:24576\n\t"
    "ds_read_b128 v[32:35], v8 offset:25600\n\t"
    "ds_read_b128 v[52:55], v6 offset:13312\n\t"
    "s_waitcnt lgkmcnt(8)\n\t"
    "v_smfmac_f32_16x16x64_bf16 v[12:15], v[40:43], v[160:167], v9\n\t"
    "v_smfmac_f32_16x16x64_bf16 v[16:19], v[40:43], v[168:175], v9\n\t"
    "s_waitcnt lgkmcnt(7)\n\t"
    "v_smfmac_f32_16x16x64_bf16 v[12:15], v[36:39], v[176:183], v9\n\t"
    "v_smfmac_f32_16x16x64_bf16 v[16:19], v[36:39], v[184:191], v9\n\t"
    "s_waitcnt lgkmcnt(6)\n\t"
    "v_smfmac_f32_16x16x64_bf16 v[12:15], v[44:47], v[192:199], v9\n\t"
    "v_smfmac_f32_16x16x64_bf16 v[16:19], v[44:47], v[200:207], v9\n\t"
    "s_waitcnt lgkmcnt(3)\n\t"
    "v_smfmac_f32_16x16x64_bf16 v[12:15], v[48:51], v[20:27], v9\n\t"
    "s_waitcnt lgkmcnt(1)\n\t"
    "v_smfmac_f32_16x16x64_bf16 v[16:19], v[48:51], v[28:35], v9\n\t"
    "s_waitcnt lgkmcnt(0)\n\t"
    "v_smfmac_f32_16x16x64_bf16 v[12:15], v[52:55], v[208:215], v9\n\t"
    "v_smfmac_f32_16x16x64_bf16 v[16:19], v[52:55], v[216:223], v9\n\t"
    "s_nop 6\n\t"
    "v_permlane16_swap_b32 v12, v14\n\t"
    "v_permlane16_swap_b32 v13, v15\n\t"
    "v_permlane16_swap_b32 v16, v18\n\t"
    "v_permlane16_swap_b32 v17, v19\n\t"
    "v_add_f32 v12, v12, v14\n\t"
    "v_add_f32 v13, v13, v15\n\t"
    "v_add_f32 v16, v16, v18\n\t"
    "v_cvt_pk_bf16_f32 v12, v12, v13\n\t"
    "v_add_f32 v17, v17, v19\n\t"
    "ds_write_b32 v10, v12 offset:36352\n\t"
    "v_cvt_pk_bf16_f32 v16, v16, v17\n\t"
    "ds_write_b32 v10, v16 offset:36608\n\t"
    "s_waitcnt lgkmcnt(0)\n\t"
    "s_setprio 0\n\t"
    "s_barrier\n\t"
    "s_barrier\n\t"
    "s_waitcnt lgkmcnt(0)\n\t"
    "s_branch 9f\n\t"
    "1:\n\t"
    "s_add_u32 %[sp], %[sp], 1\n\t"
    "s_cmp_ge_u32 %[sp], %[cap]\n\t"
    "s_cbranch_scc1 7f\n\t"
    "global_load_dwordx4 v[36:39], v6, %[ag] sc1\n\t"
    "global_load_dwordx4 v[40:43], v6, %[ag] offset:1024 sc1\n\t"
    "s_waitcnt lgkmcnt(0)\n\t"
    "s_memtime %[tc]\n\t"
    "s_nop 7\n\t"
    "s_branch 3b\n\t"
    "7:\n\t"
    "s_mov_b32 %[cap], 0\n\t"
    "s_branch 2b\n\t"
    "9:"
    : [tc] "=&s"(tc), [sp] "=&s"(sp), [cap] "+s"(cap)
    : [ag] "s"(ag), [c44] "s"(0x4444u), "{v[64:95]}"(W.w0), "{v[96:127]}"(W.w1), "{v[128:159]}"(W.w2), "{v[160:191]}"(W.w3), "{v[192:223]}"(W.w4), "{v[224:255]}"(W.w5), "{v[56:63]}"(W.x)
    : "v6", "v7", "v8", "v9", "v10", "v11", "v12", "v13", "v14", "v15", "v16", "v17", "v18", "v19", "v20", "v21", "v22", "v23", "v24", "v25", "v26", "v27", "v28", "v29", "v30", "v31", "v32", "v33", "v34", "v35", "v36", "v37", "v38", "v39", "v40", "v41", "v42", "v43", "v44", "v45", "v46", "v47", "v48", "v49", "v50", "v51", "v52", "v53", "v54", "v55", "vcc", "scc", "memory");
}
template <int I> __device__ __forceinline__ void qk_wload(u32x32& w, unsigned a0, unsigned a1);
template <> __device__ __forceinline__ void qk_wload<0>(u32x32& w, unsigned a0, unsigned a1) {
  asm volatile("ds_read_b64_tr_b16 v[64:65], %1 offset:0\n\t"
    "ds_read_b64_tr_b16 v[66:67], %1 offset:512\n\t"
    "ds_read_b64_tr_b16 v[68:69], %1 offset:4096\n\t"
    "ds_read_b64_tr_b16 v[70:71], %1 offset:4608\n\t"
    "ds_read_b64_tr_b16 v[72:73], %2 offset:0\n\t"
    "ds_read_b64_tr_b16 v[74:75], %2 offset:512\n\t"
    "ds_read_b64_tr_b16 v[76:77], %2 offset:4096\n\t"
    "ds_read_b64_tr_b16 v[78:79], %2 offset:4608\n\t"
    "s_waitcnt lgkmcnt(0)\n\t"
    "ds_read_b64_tr_b16 v[80:81], %1 offset:16384\n\t"
    "ds_read_b64_tr_b16 v[82:83], %1 offset:16896\n\t"
    "ds_read_b64_tr_b16 v[84:85], %1 offset:20480\n\t"
    "ds_read_b64_tr_b16 v[86:87], %1 offset:20992\n\t"
    "ds_read_b64_tr_b16 v[88:89], %2 offset:16384\n\t"
    "ds_read_b64_tr_b16 v[90:91], %2 offset:16896\n\t"
    "ds_read_b64_tr_b16 v[92:93], %2 offset:20480\n\t"
    "ds_read_b64_tr_b16 v[94:95], %2 offset:20992\n\t"
    "s_waitcnt lgkmcnt(0)"
    : "=&{v[64:95]}"(w) : "v"(a0), "v"(a1) : "memory");
}
template <> __device__ __forceinline__ void qk_wload<1>(u32x32& w, unsigned a0, unsigned a1) {
  asm volatile("ds_read_b64_tr_b16 v[96:97], %1 offset:0\n\t"
    "ds_read_b64_tr_b16 v[98:99], %1 offset:512\n\t"
    "ds_read_b64_tr_b16 v[100:101], %1 offset:4096\n\t"
    "ds_read_b64_tr_b16 v[102:103], %1 offset:4608\n\t"
    "ds_read_b64_tr_b16 v[104:105], %2 offset:0\n\t"
    "ds_read_b64_tr_b16 v[106:107], %2 offset:512\n\t"
    "ds_read_b64_tr_b16 v[108:109], %2 offset:4096\n\t"
    "ds_read_b64_tr_b16 v[110:111], %2 offset:4608\n\t"
    "s_waitcnt lgkmcnt(0)\n\t"
    "ds_read_b64_tr_b16 v[112:113], %1 offset:16384\n\t"
    "ds_read_b64_tr_b16 v[114:115], %1 offset:16896\n\t"
    "ds_read_b64_tr_b16 v[116:117], %1 offset:20480\n\t"
    "ds_read_b64_tr_b16 v[118:119], %1 offset:20992\n\t"
    "ds_read_b64_tr_b16 v[120:121], %2 offset:16384\n\t"
    "ds_read_b64_tr_b16 v[122:123], %2 offset:16896\n\t"
    "ds_read_b64_tr_b16 v[124:125], %2 offset:20480\n\t"
    "ds_read_b64_tr_b16 v[126:127], %2 offset:20992\n\t"
    "s_waitcnt lgkmcnt(0)"
    : "=&{v[96:127]}"(w) : "v"(a0), "v"(a1) : "memory");
}
template <> __device__ __forceinline__ void qk_wload<2>(u32x32& w, unsigned a0, unsigned a1) {
  asm volatile("ds_read_b64_tr_b16 v[128:129], %1 offset:0\n\t"
    "ds_read_b64_tr_b16 v[130:131], %1 offset:512\n\t"
    "ds_read_b64_tr_b16 v[132:133], %1 offset:4096\n\t"
    "ds_read_b64_tr_b16 v[134:135], %1 offset:4608\n\t"
    "ds_read_b64_tr_b16 v[136:137], %2 offset:0\n\t"
    "ds_read_b64_tr_b16 v[138:139], %2 offset:512\n\t"
    "ds_read_b64_tr_b16 v[140:141], %2 offset:4096\n\t"
    "ds_read_b64_tr_b16 v[142:143], %2 offset:4608\n\t"
    "s_waitcnt lgkmcnt(0)\n\t"
    "ds_read_b64_tr_b16 v[144:145], %1 offset:16384\n\t"
    "ds_read_b64_tr_b16 v[146:147], %1 offset:16896\n\t"
    "ds_read_b64_tr_b16 v[148:149], %1 offset:20480\n\t"
    "ds_read_b64_tr_b16 v[150:151], %1 offset:20992\n\t"
    "ds_read_b64_tr_b16 v[152:153], %2 offset:16384\n\t"
    "ds_read_b64_tr_b16 v[154:155], %2 offset:16896\n\t"
    "ds_read_b64_tr_b16 v[156:157], %2 offset:20480\n\t"
    "ds_read_b64_tr_b16 v[158:159], %2 offset:20992\n\t"
    "s_waitcnt lgkmcnt(0)"
    : "=&{v[128:159]}"(w) : "v"(a0), "v"(a1) : "memory");
}
template <> __device__ __forceinline__ void qk_wload<3>(u32x32& w, unsigned a0, unsigned a1) {
  asm volatile("ds_read_b64_tr_b16 v[160:161], %1 offset:0\n\t"
    "ds_read_b64_tr_b16 v[162:163], %1 offset:512\n\t"
    "ds_read_b64_tr_b16 v[164:165], %1 offset:4096\n\t"
    "ds_read_b64_tr_b16 v[166:167], %1 offset:4608\n\t"
    "ds_read_b64_tr_b16 v[168:169], %2 offset:0\n\t"
    "ds_read_b64_tr_b16 v[170:171], %2 offset:512\n\t"
    "ds_read_b64_tr_b16 v[172:173], %2 offset:4096\n\t"
    "ds_read_b64_tr_b16 v[174:175], %2 offset:4608\n\t"
    "s_waitcnt lgkmcnt(0)\n\t"
    "ds_read_b64_tr_b16 v[176:177], %1 offset:16384\n\t"
    "ds_read_b64_tr_b16 v[178:179], %1 offset:16896\n\t"
    "ds_read_b64_tr_b16 v[180:181], %1 offset:20480\n\t"
    "ds_read_b64_tr_b16 v[182:183], %1 offset:20992\n\t"
    "ds_read_b64_tr_b16 v[184:185], %2 offset:16384\n\t"
    "ds_read_b64_tr_b16 v[186:187], %2 offset:16896\n\t"
    "ds_read_b64_tr_b16 v[188:189], %2 offset:20480\n\t"
    "ds_read_b64_tr_b16 v[190:191], %2 offset:20992\n\t"
    "s_waitcnt lgkmcnt(0)"
    : "=&{v[160:191]}"(w) : "v"(a0), "v"(a1) : "memory");
}
template <> __device__ __forceinline__ void qk_wload<4>(u32x32& w, unsigned a0, unsigned a1) {
  asm volatile("ds_read_b64_tr_b16 v[192:193], %1 offset:0\n\t"
    "ds_read_b64_tr_b16 v[194:195], %1 offset:512\n\t"
    "ds_read_b64_tr_b16 v[196:197], %1 offset:4096\n\t"
    "ds_read_b64_tr_b16 v[198:199], %1 offset:4608\n\t"
    "ds_read_b64_tr_b16 v[200:201], %2 offset:0\n\t"
    "ds_read_b64_tr_b16 v[202:203], %2 offset:512\n\t"
    "ds_read_b64_tr_b16 v[204:205], %2 offset:4096\n\t"
    "ds_read_b64_tr_b16 v[206:207], %2 offset:4608\n\t"
    "s_waitcnt lgkmcnt(0)\n\t"
    "ds_read_b64_tr_b16 v[208:209], %1 offset:16384\n\t"
    "ds_read_b64_tr_b16 v[210:211], %1 offset:16896\n\t"
    "ds_read_b64_tr_b16 v[212:213], %1 offset:20480\n\t"
    "ds_read_b64_tr_b16 v[214:215], %1 offset:20992\n\t"
    "ds_read_b64_tr_b16 v[216:217], %2 offset:16384\n\t"
    "ds_read_b64_tr_b16 v[218:219], %2 offset:16896\n\t"
    "ds_read_b64_tr_b16 v[220:221], %2 offset:20480\n\t"
    "ds_read_b64_tr_b16 v[222:223], %2 offset:20992\n\t"
    "s_waitcnt lgkmcnt(0)"
    : "=&{v[192:223]}"(w) : "v"(a0), "v"(a1) : "memory");
}
template <> __device__ __forceinline__ void qk_wload<5>(u32x32& w, unsigned a0, unsigned a1) {
  asm volatile("ds_read_b64_tr_b16 v[224:225], %1 offset:0\n\t"
    "ds_read_b64_tr_b16 v[226:227], %1 offset:512\n\t"
    "ds_read_b64_tr_b16 v[228:229], %1 offset:4096\n\t"
    "ds_read_b64_tr_b16 v[230:231], %1 offset:4608\n\t"
    "ds_read_b64_tr_b16 v[232:233], %2 offset:0\n\t"
    "ds_read_b64_tr_b16 v[234:235], %2 offset:512\n\t"
    "ds_read_b64_tr_b16 v[236:237], %2 offset:4096\n\t"
    "ds_read_b64_tr_b16 v[238:239], %2 offset:4608\n\t"
    "s_waitcnt lgkmcnt(0)\n\t"
    "ds_read_b64_tr_b16 v[240:241], %1 offset:16384\n\t"
    "ds_read_b64_tr_b16 v[242:243], %1 offset:16896\n\t"
    "ds_read_b64_tr_b16 v[244:245], %1 offset:20480\n\t"
    "ds_read_b64_tr_b16 v[246:247], %1 offset:20992\n\t"
    "ds_read_b64_tr_b16 v[248:249], %2 offset:16384\n\t"
    "ds_read_b64_tr_b16 v[250:251], %2 offset:16896\n\t"
    "ds_read_b64_tr_b16 v[252:253], %2 offset:20480\n\t"
    "ds_read_b64_tr_b16 v[254:255], %2 offset:20992\n\t"
    "s_waitcnt lgkmcnt(0)"
    : "=&{v[224:255]}"(w) : "v"(a0), "v"(a1) : "memory");
}
template <int I> __device__ __forceinline__ void vr_wload(u32x32& w, unsigned a);
template <> __device__ __forceinline__ void vr_wload<0>(u32x32& w, unsigned a) {
  asm volatile("ds_read_b64_tr_b16 v[64:65], %1 offset:0\n\t"
    "ds_read_b64_tr_b16 v[66:67], %1 offset:256\n\t"
    "ds_read_b64_tr_b16 v[68:69], %1 offset:2048\n\t"
    "ds_read_b64_tr_b16 v[70:71], %1 offset:2304\n\t"
    "ds_read_b64_tr_b16 v[72:73], %1 offset:32\n\t"
    "ds_read_b64_tr_b16 v[74:75], %1 offset:288\n\t"
    "ds_read_b64_tr_b16 v[76:77], %1 offset:2080\n\t"
    "ds_read_b64_tr_b16 v[78:79], %1 offset:2336\n\t"
    "s_waitcnt lgkmcnt(0)\n\t"
    "ds_read_b64_tr_b16 v[80:81], %1 offset:64\n\t"
    "ds_read_b64_tr_b16 v[82:83], %1 offset:320\n\t"
    "ds_read_b64_tr_b16 v[84:85], %1 offset:2112\n\t"
    "ds_read_b64_tr_b16 v[86:87], %1 offset:2368\n\t"
    "ds_read_b64_tr_b16 v[88:89], %1 offset:96\n\t"
    "ds_read_b64_tr_b16 v[90:91], %1 offset:352\n\t"
    "ds_read_b64_tr_b16 v[92:93], %1 offset:2144\n\t"
    "ds_read_b64_tr_b16 v[94:95], %1 offset:2400\n\t"
    "s_waitcnt lgkmcnt(0)"
    : "=&{v[64:95]}"(w) : "v"(a) : "memory");
}
template <> __device__ __forceinline__ void vr_wload<1>(u32x32& w, unsigned a) {
  asm volatile("ds_read_b64_tr_b16 v[96:97], %1 offset:8192\n\t"
    "ds_read_b64_tr_b16 v[98:99], %1 offset:8448\n\t"
    "ds_read_b64_tr_b16 v[100:101], %1 offset:10240\n\t"
    "ds_read_b64_tr_b16 v[102:103], %1 offset:10496\n\t"
    "ds_read_b64_tr_b16 v[104:105], %1 offset:8224\n\t"
    "ds_read_b64_tr_b16 v[106:107], %1 offset:8480\n\t"
    "ds_read_b64_tr_b16 v[108:109], %1 offset:10272\n\t"
    "ds_read_b64_tr_b16 v[110:111], %1 offset:10528\n\t"
    "s_waitcnt lgkmcnt(0)\n\t"
    "ds_read_b64_tr_b16 v[112:113], %1 offset:8256\n\t"
    "ds_read_b64_tr_b16 v[114:115], %1 offset:8512\n\t"
    "ds_read_b64_tr_b16 v[116:117], %1 offset:10304\n\t"
    "ds_read_b64_tr_b16 v[118:119], %1 offset:10560\n\t"
    "ds_read_b64_tr_b16 v[120:121], %1 offset:8288\n\t"
    "ds_read_b64_tr_b16 v[122:123], %1 offset:8544\n\t"
    "ds_read_b64_tr_b16 v[124:125], %1 offset:10336\n\t"
    "ds_read_b64_tr_b16 v[126:127], %1 offset:10592\n\t"
    "s_waitcnt lgkmcnt(0)"
    : "=&{v[96:127]}"(w) : "v"(a) : "memory");
}
template <> __device__ __forceinline__ void vr_wload<2>(u32x32& w, unsigned a) {
  asm volatile("ds_read_b64_tr_b16 v[128:129], %1 offset:0\n\t"
    "ds_read_b64_tr_b16 v[130:131], %1 offset:128\n\t"
    "ds_read_b64_tr_b16 v[132:133], %1 offset:1024\n\t"
    "ds_read_b64_tr_b16 v[134:135], %1 offset:1152\n\t"
    "ds_read_b64_tr_b16 v[136:137], %1 offset:32\n\t"
    "ds_read_b64_tr_b16 v[138:139], %1 offset:160\n\t"
    "ds_read_b64_tr_b16 v[140:141], %1 offset:1056\n\t"
    "ds_read_b64_tr_b16 v[142:143], %1 offset:1184\n\t"
    "s_waitcnt lgkmcnt(0)\n\t"
    "ds_read_b64_tr_b16 v[144:145], %1 offset:4096\n\t"
    "ds_read_b64_tr_b16 v[146:147], %1 offset:4224\n\t"
    "ds_read_b64_tr_b16 v[148:149], %1 offset:5120\n\t"
    "ds_read_b64_tr_b16 v[150:151], %1 offset:5248\n\t"
    "ds_read_b64_tr_b16 v[152:153], %1 offset:4128\n\t"
    "ds_read_b64_tr_b16 v[154:155], %1 offset:4256\n\t"
    "ds_read_b64_tr_b16 v[156:157], %1 offset:5152\n\t"
    "ds_read_b64_tr_b16 v[158:159], %1 offset:5280\n\t"
    "s_waitcnt lgkmcnt(0)"
    : "=&{v[128:159]}"(w) : "v"(a) : "memory");
}
template <> __device__ __forceinline__ void vr_wload<3>(u32x32& w, unsigned a) {
  asm volatile("ds_read_b64_tr_b16 v[160:161], %1 offset:8192\n\t"
    "ds_read_b64_tr_b16 v[162:163], %1 offset:8320\n\t"
    "ds_read_b64_tr_b16 v[164:165], %1 offset:9216\n\t"
    "ds_read_b64_tr_b16 v[166:167], %1 offset:9344\n\t"
    "ds_read_b64_tr_b16 v[168:169], %1 offset:8224\n\t"
    "ds_read_b64_tr_b16 v[170:171], %1 offset:8352\n\t"
    "ds_read_b64_tr_b16 v[172:173], %1 offset:9248\n\t"
    "ds_read_b64_tr_b16 v[174:175], %1 offset:9376\n\t"
    "s_waitcnt lgkmcnt(0)\n\t"
    "ds_read_b64_tr_b16 v[176:177], %1 offset:12288\n\t"
    "ds_read_b64_tr_b16 v[178:179], %1 offset:12416\n\t"
    "ds_read_b64_tr_b16 v[180:181], %1 offset:13312\n\t"
    "ds_read_b64_tr_b16 v[182:183], %1 offset:13440\n\t"
    "ds_read_b64_tr_b16 v[184:185], %1 offset:12320\n\t"
    "ds_read_b64_tr_b16 v[186:187], %1 offset:12448\n\t"
    "ds_read_b64_tr_b16 v[188:189], %1 offset:13344\n\t"
    "ds_read_b64_tr_b16 v[190:191], %1 offset:13472\n\t"
    "s_waitcnt lgkmcnt(0)"
    : "=&{v[160:191]}"(w) : "v"(a) : "memory");
}
template <> __device__ __forceinline__ void vr_wload<4>(u32x32& w, unsigned a) {
  asm volatile("ds_read_b64_tr_b16 v[192:193], %1 offset:0\n\t"
    "ds_read_b64_tr_b16 v[194:195], %1 offset:128\n\t"
    "ds_read_b64_tr_b16 v[196:197], %1 offset:1024\n\t"
    "ds_read_b64_tr_b16 v[198:199], %1 offset:1152\n\t"
    "ds_read_b64_tr_b16 v[200:201], %1 offset:32\n\t"
    "ds_read_b64_tr_b16 v[202:203], %1 offset:160\n\t"
    "ds_read_b64_tr_b16 v[204:205], %1 offset:1056\n\t"
    "ds_read_b64_tr_b16 v[206:207], %1 offset:1184\n\t"
    "s_waitcnt lgkmcnt(0)\n\t"
    "ds_read_b64_tr_b16 v[208:209], %1 offset:4096\n\t"
    "ds_read_b64_tr_b16 v[210:211], %1 offset:4224\n\t"
    "ds_read_b64_tr_b16 v[212:213], %1 offset:5120\n\t"
    "ds_read_b64_tr_b16 v[214:215], %1 offset:5248\n\t"
    "ds_read_b64_tr_b16 v[216:217], %1 offset:4128\n\t"
    "ds_read_b64_tr_b16 v[218:219], %1 offset:4256\n\t"
    "ds_read_b64_tr_b16 v[220:221], %1 offset:5152\n\t"
    "ds_read_b64_tr_b16 v[222:223], %1 offset:5280\n\t"
    "s_waitcnt lgkmcnt(0)"
    : "=&{v[192:223]}"(w) : "v"(a) : "memory");
}
template <> __device__ __forceinline__ void vr_wload<5>(u32x32& w, unsigned a) {
  asm volatile("ds_read_b64_tr_b16 v[224:225], %1 offset:8192\n\t"
    "ds_read_b64_tr_b16 v[226:227], %1 offset:8320\n\t"
    "ds_read_b64_tr_b16 v[228:229], %1 offset:9216\n\t"
    "ds_read_b64_tr_b16 v[230:231], %1 offset:9344\n\t"
    "ds_read_b64_tr_b16 v[232:233], %1 offset:8224\n\t"
    "ds_read_b64_tr_b16 v[234:235], %1 offset:8352\n\t"
    "ds_read_b64_tr_b16 v[236:237], %1 offset:9248\n\t"
    "ds_read_b64_tr_b16 v[238:239], %1 offset:9376\n\t"
    "s_waitcnt lgkmcnt(0)\n\t"
    "ds_read_b64_tr_b16 v[240:241], %1 offset:12288\n\t"
    "ds_read_b64_tr_b16 v[242:243], %1 offset:12416\n\t"
    "ds_read_b64_tr_b16 v[244:245], %1 offset:13312\n\t"
    "ds_read_b64_tr_b16 v[246:247], %1 offset:13440\n\t"
    "ds_read_b64_tr_b16 v[248:249], %1 offset:12320\n\t"
    "ds_read_b64_tr_b16 v[250:251], %1 offset:12448\n\t"
    "ds_read_b64_tr_b16 v[252:253], %1 offset:13344\n\t"
    "ds_read_b64_tr_b16 v[254:255], %1 offset:13472\n\t"
    "s_waitcnt lgkmcnt(0)"
    : "=&{v[224:255]}"(w) : "v"(a) : "memory");
}
#define OREP 2
#define AREP 2
struct VRW { u32x32 v0, v1, o0, o1, o2, o3; };
__device__ __forceinline__ void vr_v(const VRW& W, const char* ag, const char* vd, unsigned wa, unsigned ra, unsigned& cap, unsigned& sp, unsigned long long& tc, unsigned long long* ts) {
  asm volatile("v_mbcnt_lo_u32_b32 v16, -1, 0\n\t"
    "v_mbcnt_hi_u32_b32 v16, -1, v16\n\t"
    "v_lshlrev_b32 v17, 4, v16\n\t"
    "s_mov_b32 %[sp], 0\n\t"
    "global_load_dwordx4 v[24:27], v17, %[ag] sc1\n\t"
    "global_load_dwordx4 v[28:31], v17, %[ag] offset:1024 sc1\n\t"
    "s_memtime %[tc]\n\t"
    "v_and_b32 v22, 4, v16\n\t"
    "v_mul_u32_u24 v22, 0x2aaa, v22\n\t"
    "v_lshrrev_b32 v18, 1, v16\n\t"
    "v_and_b32 v18, 2, v18\n\t"
    "v_or3_b32 v18, v18, v22, %[c44]\n\t"
    "v_lshl_add_u32 v19, v16, 3, %[wa]\n\t"
    "v_lshl_add_u32 v20, v16, 3, %[ra]\n\t"
    "v_and_b32 v22, 40, v16\n\t"
    "v_lshlrev_b32 v21, 3, v22\n\t"
    "v_and_b32 v22, 3, v16\n\t"
    "v_lshl_or_b32 v21, v22, 4, v21\n\t"
    "v_and_b32 v22, 4, v16\n\t"
    "v_lshl_or_b32 v21, v22, 1, v21\n\t"
    "v_lshrrev_b32 v22, 2, v16\n\t"
    "v_and_or_b32 v21, v22, 4, v21\n\t"
    "3:\n\t"
    "v_mov_b32 v32, 0\n\t"
    "v_mov_b32 v33, 0\n\t"
    "v_mov_b32 v34, 0\n\t"
    "v_mov_b32 v35, 0\n\t"
    "v_mov_b32 v36, 0\n\t"
    "v_mov_b32 v37, 0\n\t"
    "v_mov_b32 v38, 0\n\t"
    "v_mov_b32 v39, 0\n\t"
    "v_mov_b32 v40, 0\n\t"
    "v_mov_b32 v41, 0\n\t"
    "v_mov_b32 v42, 0\n\t"
    "v_mov_b32 v43, 0\n\t"
    "v_mov_b32 v44, 0\n\t"
    "v_mov_b32 v45, 0\n\t"
    "v_mov_b32 v46, 0\n\t"
    "v_mov_b32 v47, 0\n\t"
    "s_waitcnt vmcnt(0) lgkmcnt(0)\n\t"
    "v_smfmac_f32_16x16x64_bf16 v[32:35], v[24:27], v[64:71], v18\n\t"
    "v_smfmac_f32_16x16x64_bf16 v[32:35], v[28:31], v[96:103], v18\n\t"
    "v_max3_u32 v22, v24, v25, v26\n\t"
    "v_max3_u32 v23, v27, v28, v29\n\t"
    "v_smfmac_f32_16x16x64_bf16 v[36:39], v[24:27], v[72:79], v18\n\t"
    "v_max3_u32 v22, v22, v30, v31\n\t"
    "v_max_u32 v22, v22, v23\n\t"
    "v_smfmac_f32_16x16x64_bf16 v[36:39], v[28:31], v[104:111], v18\n\t"
    "v_cmp_eq_u32 vcc, -1, v22\n\t"
    "s_cbranch_vccnz 1f\n\t"
    "2:\n\t"
    "v_smfmac_f32_16x16x64_bf16 v[40:43], v[24:27], v[80:87], v18\n\t"
    "v_permlane16_swap_b32 v32, v34\n\t"
    "v_permlane16_swap_b32 v33, v35\n\t"
    "v_smfmac_f32_16x16x64_bf16 v[40:43], v[28:31], v[112:119], v18\n\t"
    "v_pk_add_f32 v[32:33], v[32:33], v[34:35]\n\t"
    "ds_write_b64 v19, v[32:33] offset:0\n\t"
    "v_smfmac_f32_16x16x64_bf16 v[44:47], v[24:27], v[88:95], v18\n\t"
    "s_nop 0\n\t"
    "v_permlane16_swap_b32 v36, v38\n\t"
    "v_permlane16_swap_b32 v37, v39\n\t"
    "v_smfmac_f32_16x16x64_bf16 v[44:47], v[28:31], v[120:127], v18\n\t"
    "v_pk_add_f32 v[36:37], v[36:37], v[38:39]\n\t"
    "ds_write_b64 v19, v[36:37] offset:512\n\t"
    "v_permlane16_swap_b32 v40, v42\n\t"
    "v_permlane16_swap_b32 v41, v43\n\t"
    "v_pk_add_f32 v[40:41], v[40:41], v[42:43]\n\t"
    "ds_write_b64 v19, v[40:41] offset:1024\n\t"
    "s_nop 1\n\t"
    "v_permlane16_swap_b32 v44, v46\n\t"
    "v_permlane16_swap_b32 v45, v47\n\t"
    "v_pk_add_f32 v[44:45], v[44:45], v[46:47]\n\t"
    "ds_write_b64 v19, v[44:45] offset:1536\n\t"
    "s_cmp_ge_u32 %[wa], 8192\n\t"
    "s_waitcnt lgkmcnt(0)\n\t"
    "s_barrier\n\t"
    "s_cbranch_scc1 9f\n\t"
    "ds_read_b64 v[48:49], v20 offset:0\n\t"
    "ds_read_b64 v[50:51], v20 offset:2048\n\t"
    "ds_read_b64 v[52:53], v20 offset:4096\n\t"
    "ds_read_b64 v[54:55], v20 offset:6144\n\t"
    "ds_read_b64 v[56:57], v20 offset:8192\n\t"
    "ds_read_b64 v[58:59], v20 offset:10240\n\t"
    "ds_read_b64 v[60:61], v20 offset:12288\n\t"
    "ds_read_b64 v[62:63], v20 offset:14336\n\t"
    "s_waitcnt lgkmcnt(6)\n\t"
    "v_pk_add_f32 v[48:49], v[48:49], v[50:51]\n\t"
    "s_waitcnt lgkmcnt(4)\n\t"
    "v_pk_add_f32 v[52:53], v[52:53], v[54:55]\n\t"
    "v_pk_add_f32 v[48:49], v[48:49], v[52:53]\n\t"
    "s_waitcnt lgkmcnt(2)\n\t"
    "v_pk_add_f32 v[56:57], v[56:57], v[58:59]\n\t"
    "s_waitcnt lgkmcnt(0)\n\t"
    "v_pk_add_f32 v[60:61], v[60:61], v[62:63]\n\t"
    "v_pk_add_f32 v[56:57], v[56:57], v[60:61]\n\t"
    "v_pk_add_f32 v[48:49], v[48:49], v[56:57]\n\t"
    "v_cvt_pk_bf16_f32 v22, v48, v49\n\t"
    "v_min_u32 v22, -2, v22\n\t"
    "global_store_dword v21, v22, %[vd] sc1\n\t"
    "s_waitcnt lgkmcnt(0)\n\t"
    "s_branch 9f\n\t"
    "1:\n\t"
    "s_add_u32 %[sp], %[sp], 1\n\t"
    "s_cmp_ge_u32 %[sp], %[cap]\n\t"
    "s_cbranch_scc1 7f\n\t"
    "global_load_dwordx4 v[24:27], v17, %[ag] sc1\n\t"
    "global_load_dwordx4 v[28:31], v17, %[ag] offset:1024 sc1\n\t"
    "s_waitcnt lgkmcnt(0)\n\t"
    "s_memtime %[tc]\n\t"
    "s_nop 7\n\t"
    "s_branch 3b\n\t"
    "7:\n\t"
    "s_mov_b32 %[cap], 0\n\t"
    "s_branch 2b\n\t"
    "9:"
    : [tc] "=&s"(tc), [sp] "=&s"(sp), [cap] "+s"(cap)
    : [ag] "s"(ag), [vd] "s"(vd), [wa] "s"(wa), [ra] "s"(ra), [c44] "s"(0x4444u), "{v[64:95]}"(W.v0), "{v[96:127]}"(W.v1), "{v[128:159]}"(W.o0), "{v[160:191]}"(W.o1), "{v[192:223]}"(W.o2), "{v[224:255]}"(W.o3)
    : "v16", "v17", "v18", "v19", "v20", "v21", "v22", "v23", "v24", "v25", "v26", "v27", "v28", "v29", "v30", "v31", "v32", "v33", "v34", "v35", "v36", "v37", "v38", "v39", "v40", "v41", "v42", "v43", "v44", "v45", "v46", "v47", "v48", "v49", "v50", "v51", "v52", "v53", "v54", "v55", "v56", "v57", "v58", "v59", "v60", "v61", "v62", "v63", "vcc", "scc", "memory");
}
__device__ __forceinline__ void vr_r(const VRW& W, const char* os, const char* ad, unsigned wa, unsigned jx, unsigned& cap, unsigned& sp, unsigned long long* ts) {
  asm volatile("v_mbcnt_lo_u32_b32 v16, -1, 0\n\t"
    "v_mbcnt_hi_u32_b32 v16, -1, v16\n\t"
    "v_lshl_or_b32 v17, v16, 4, %[jx]\n\t"
    "global_load_dwordx4 v[24:27], v17, %[os] sc1\n\t"
    "v_xor_b32 v18, 2048, v17\n\t"
    "global_load_dwordx4 v[28:31], v18, %[os] sc1\n\t"
    "v_xor_b32 v57, 4096, v17\n\t"
    "global_load_dwordx4 v[32:35], v57, %[os] sc1\n\t"
    "v_xor_b32 v58, 6144, v17\n\t"
    "global_load_dwordx4 v[36:39], v58, %[os] sc1\n\t"
    "s_mov_b32 %[sp], 0\n\t"
    "v_and_b32 v23, 4, v16\n\t"
    "v_mul_u32_u24 v23, 0x2aaa, v23\n\t"
    "v_lshrrev_b32 v19, 1, v16\n\t"
    "v_and_b32 v19, 2, v19\n\t"
    "v_or3_b32 v19, v19, v23, %[c44]\n\t"
    "v_and_b32 v23, 15, v16\n\t"
    "v_lshl_add_u32 v20, v23, 2, %[wa]\n\t"
    "v_and_b32 v23, 48, v16\n\t"
    "v_lshl_add_u32 v20, v23, 3, v20\n\t"
    "v_and_b32 v23, 32, v16\n\t"
    "v_and_b32 v21, 28, v16\n\t"
    "v_lshlrev_b32 v21, 4, v21\n\t"
    "v_or_b32 v21, v21, v23\n\t"
    "v_lshlrev_b32 v22, 3, v23\n\t"
    "v_and_b32 v23, 3, v16\n\t"
    "v_lshl_or_b32 v21, v23, 3, v21\n\t"
    "v_and_b32 v23, 31, v16\n\t"
    "v_lshl_or_b32 v22, v23, 2, v22\n\t"
    "3:\n\t"
    "v_mov_b32 v40, 0\n\t"
    "v_mov_b32 v41, 0\n\t"
    "v_mov_b32 v42, 0\n\t"
    "v_mov_b32 v43, 0\n\t"
    "v_mov_b32 v44, 0\n\t"
    "v_mov_b32 v45, 0\n\t"
    "v_mov_b32 v46, 0\n\t"
    "v_mov_b32 v47, 0\n\t"
    "v_mov_b32 v48, 0\n\t"
    "v_mov_b32 v49, 0\n\t"
    "v_mov_b32 v50, 0\n\t"
    "v_mov_b32 v51, 0\n\t"
    "v_mov_b32 v52, 0\n\t"
    "v_mov_b32 v53, 0\n\t"
    "v_mov_b32 v54, 0\n\t"
    "v_mov_b32 v55, 0\n\t"
    "v_mov_b32 v56, 0\n\t"
    "s_waitcnt vmcnt(3)\n\t"
    "v_smfmac_f32_16x16x64_bf16 v[44:47], v[24:27], v[144:151], v19\n\t"
    "v_max3_u32 v56, v56, v24, v25\n\t"
    "v_smfmac_f32_16x16x64_bf16 v[48:51], v[24:27], v[136:143], v19\n\t"
    "v_max3_u32 v56, v56, v26, v27\n\t"
    "v_smfmac_f32_16x16x64_bf16 v[40:43], v[24:27], v[128:135], v19\n\t"
    "v_smfmac_f32_16x16x64_bf16 v[52:55], v[24:27], v[152:159], v19\n\t"
    "s_waitcnt vmcnt(2)\n\t"
    "v_smfmac_f32_16x16x64_bf16 v[44:47], v[28:31], v[176:183], v19\n\t"
    "v_max3_u32 v56, v56, v28, v29\n\t"
    "v_smfmac_f32_16x16x64_bf16 v[48:51], v[28:31], v[168:175], v19\n\t"
    "v_max3_u32 v56, v56, v30, v31\n\t"
    "v_smfmac_f32_16x16x64_bf16 v[40:43], v[28:31], v[160:167], v19\n\t"
    "v_smfmac_f32_16x16x64_bf16 v[52:55], v[28:31], v[184:191], v19\n\t"
    "s_waitcnt vmcnt(1)\n\t"
    "v_smfmac_f32_16x16x64_bf16 v[44:47], v[32:35], v[208:215], v19\n\t"
    "v_max3_u32 v56, v56, v32, v33\n\t"
    "v_smfmac_f32_16x16x64_bf16 v[48:51], v[32:35], v[200:207], v19\n\t"
    "v_max3_u32 v56, v56, v34, v35\n\t"
    "v_smfmac_f32_16x16x64_bf16 v[40:43], v[32:35], v[192:199], v19\n\t"
    "v_smfmac_f32_16x16x64_bf16 v[52:55], v[32:35], v[216:223], v19\n\t"
    "s_waitcnt vmcnt(0)\n\t"
    "v_smfmac_f32_16x16x64_bf16 v[44:47], v[36:39], v[240:247], v19\n\t"
    "v_max3_u32 v56, v56, v36, v37\n\t"
    "v_smfmac_f32_16x16x64_bf16 v[48:51], v[36:39], v[232:239], v19\n\t"
    "v_max3_u32 v56, v56, v38, v39\n\t"
    "v_smfmac_f32_16x16x64_bf16 v[40:43], v[36:39], v[224:231], v19\n\t"
    "v_cmp_eq_u32 vcc, -1, v56\n\t"
    "v_smfmac_f32_16x16x64_bf16 v[52:55], v[36:39], v[248:255], v19\n\t"
    "s_cbranch_vccnz 4f\n\t"
    "5:\n\t"
    "s_nop 2\n\t"
    "v_permlane32_swap_b32 v44, v48\n\t"
    "v_permlane32_swap_b32 v45, v49\n\t"
    "v_permlane32_swap_b32 v46, v50\n\t"
    "v_permlane32_swap_b32 v47, v51\n\t"
    "s_mov_b32 exec_hi, 0\n\t"
    "v_pk_add_f32 v[40:41], v[40:41], v[48:49]\n\t"
    "v_pk_add_f32 v[42:43], v[42:43], v[50:51]\n\t"
    "s_mov_b64 exec, %[hi]\n\t"
    "v_pk_add_f32 v[40:41], v[44:45], v[52:53]\n\t"
    "v_pk_add_f32 v[42:43], v[46:47], v[54:55]\n\t"
    "s_mov_b32 exec_lo, -1\n\t"
    "s_nop 0\n\t"
    "v_permlane16_swap_b32 v40, v42\n\t"
    "v_permlane16_swap_b32 v41, v43\n\t"
    "v_add_f32 v40, v40, v42\n\t"
    "v_add_f32 v41, v41, v43\n\t"
    "ds_write_b32 v20, v40 offset:16384\n\t"
    "ds_write_b32 v20, v41 offset:16448\n\t"
    "s_cmp_ge_u32 %[wa], 1024\n\t"
    "s_waitcnt lgkmcnt(0)\n\t"
    "s_barrier\n\t"
    "s_cbranch_scc1 9f\n\t"
    "ds_read_b64 v[24:25], v21 offset:16384\n\t"
    "ds_read_b64 v[26:27], v21 offset:16896\n\t"
    "ds_read_b64 v[28:29], v21 offset:17408\n\t"
    "ds_read_b64 v[30:31], v21 offset:17920\n\t"
    "ds_read_b64 v[32:33], v21 offset:18432\n\t"
    "ds_read_b64 v[34:35], v21 offset:18944\n\t"
    "ds_read_b64 v[36:37], v21 offset:19456\n\t"
    "ds_read_b64 v[38:39], v21 offset:19968\n\t"
    "s_waitcnt lgkmcnt(6)\n\t"
    "v_pk_add_f32 v[24:25], v[24:25], v[26:27]\n\t"
    "s_waitcnt lgkmcnt(4)\n\t"
    "v_pk_add_f32 v[28:29], v[28:29], v[30:31]\n\t"
    "v_pk_add_f32 v[24:25], v[24:25], v[28:29]\n\t"
    "s_waitcnt lgkmcnt(2)\n\t"
    "v_pk_add_f32 v[32:33], v[32:33], v[34:35]\n\t"
    "s_waitcnt lgkmcnt(0)\n\t"
    "v_pk_add_f32 v[36:37], v[36:37], v[38:39]\n\t"
    "v_pk_add_f32 v[32:33], v[32:33], v[36:37]\n\t"
    "v_pk_add_f32 v[24:25], v[24:25], v[32:33]\n\t"
    "v_cvt_pk_bf16_f32 v23, v24, v25\n\t"
    "v_min_u32 v23, -2, v23\n\t"
    "global_store_dword v22, v23, %[ad] sc1\n\t"
    "s_waitcnt lgkmcnt(0)\n\t"
    "s_branch 9f\n\t"
    "4:\n\t"
    "s_add_u32 %[sp], %[sp], 1\n\t"
    "s_cmp_ge_u32 %[sp], %[cap]\n\t"
    "s_cbranch_scc1 7f\n\t"
    "global_load_dwordx4 v[24:27], v17, %[os] sc1\n\t"
    "global_load_dwordx4 v[28:31], v18, %[os] sc1\n\t"
    "global_load_dwordx4 v[32:35], v57, %[os] sc1\n\t"
    "global_load_dwordx4 v[36:39], v58, %[os] sc1\n\t"
    "s_nop 7\n\t"
    "s_branch 3b\n\t"
    "7:\n\t"
    "s_mov_b32 %[cap], 0\n\t"
    "s_branch 5b\n\t"
    "9:"
    : [sp] "=&s"(sp), [cap] "+s"(cap)
    : [os] "s"(os), [ad] "s"(ad), [wa] "s"(wa), [jx] "s"(jx), [c44] "s"(0x4444u), [hi] "s"(0xFFFFFFFF00000000ull), "{v[64:95]}"(W.v0), "{v[96:127]}"(W.v1), "{v[128:159]}"(W.o0), "{v[160:191]}"(W.o1), "{v[192:223]}"(W.o2), "{v[224:255]}"(W.o3)
    : "v16", "v17", "v18", "v19", "v20", "v21", "v22", "v23", "v24", "v25", "v26", "v27", "v28", "v29", "v30", "v31", "v32", "v33", "v34", "v35", "v36", "v37", "v38", "v39", "v40", "v41", "v42", "v43", "v44", "v45", "v46", "v47", "v48", "v49", "v50", "v51", "v52", "v53", "v54", "v55", "v56", "v57", "v58", "v59", "v60", "v61", "v62", "v63", "vcc", "scc", "memory");
}
__device__ __forceinline__ void qk_body(char* smem, int wave_id_, int xg, int h, int wg, const u16* __restrict__ Wq, const u16* __restrict__ Wk,
                                        const char* __restrict__ Kx, const char* __restrict__ V, char* ws, unsigned* tmo, u64 tag) {
  const int wave = WAVE_ID;
  const int ty = wave >> 2, w4 = wave & 3;
  const char* wq = uni((const char*)(Wq + h * DH)); const char* wk = uni((const char*)(Wk + h * DH));
  auto stage = [&](int u) {
    stage_proj((u & 1) ? wk + (size_t)(((u >> 1) + 2) & 7) * 128 * HD * 2 : wq + (size_t)(u >> 1) * 128 * HD * 2, ((u + 4) % 5) * 32768, wave * 64 + lane_id(), wave);
  };
  QKW W;
  if (wave == 0) STS(ws, wg, 7);
  stage(0); stage(1); stage(2); stage(3);
#define WLD(i, w) do { WAIT_V(12); WBAR(); stage(2 * (i) + 4); if (ty) { WAIT_V(12); WBAR(); stage(2 * (i) + 5); } \
    { const int l_ = lane_id(); const unsigned o_ = ty ? ((2 * (i) + 5) % 5) * 32768 : ((2 * (i) + 4) % 5) * 32768; qk_wload<i>(w, proj_base(2 * w4, l_) + o_, proj_base(2 * w4 + 1, l_) + o_); } \
    if (!ty) { WAIT_V(12); WBAR(); stage(2 * (i) + 5); } if (wave == 0) STS(ws, wg, 1 + (i)); } while (0)
  WLD(0, W.w0); WLD(1, W.w1); WLD(2, W.w2); WLD(3, W.w3); WLD(4, W.w4); WLD(5, W.w5);
#undef WLD
  unsigned in = 0;
#pragma unroll
  for (int i = 0; i < 2; ++i) {
    if (i == 0) WAIT_V(8); else WAIT_V(0);
    WBAR();
    if (i == 0 && wave == 1) init_issue(ws, Q_V);
    if (i == 1) in = init_get(ws, tag, smem + Q_V);
    const int lane = lane_id();
    const unsigned o = Q_RING + ty * 32768 + i * 65536, f0 = proj_base(2 * w4, lane) + o, f1 = proj_base(2 * w4 + 1, lane) + o;
    u32x8 x[4] = {proj_frag(smem, f0), proj_frag(smem, f1), proj_frag(smem, f0 + 16384), proj_frag(smem, f1 + 16384)};
    asm volatile("" : "+v"(x[0]), "+v"(x[1]), "+v"(x[2]), "+v"(x[3]));
    BAR();
    char* d = smem + Q_CB + i * 57344 + ty * (i ? 32768 : 24576) + w4 * 2048 + lane * 16;
#pragma unroll
    for (int j = 0; j < 4; ++j)
      if (i || j || !ty) { *(u32x4*)(d + j * 8192) = u32x4{x[j][0], x[j][1], x[j][2], x[j][3]}; *(u32x4*)(d + j * 8192 + 1024) = u32x4{x[j][4], x[j][5], x[j][6], x[j][7]}; }
    if (i == 0 && ty) { *(u32x4*)(smem + Q_MEM + wave * 2048 + lane * 16) = u32x4{x[0][0], x[0][1], x[0][2], x[0][3]}; *(u32x4*)(smem + Q_MEM + wave * 2048 + 1024 + lane * 16) = u32x4{x[0][4], x[0][5], x[0][6], x[0][7]}; }
  }
  if (wave == 4) *(volatile LAS u32x2*)(smem + Q_TM) = u32x2{0u, 0u};
  BAR();
  if (wave == 0) STS(ws, wg, 0);
  unsigned* cnt = cnt_of(ws, in);
  const char* xb = uni(xch_base(ws) + ((size_t)3 << 21) + (size_t)((in & 2u) << 12));
  const char* oh = uni(xch_base(ws) + (size_t)((in & 1u) << 13) + (size_t)(h >> 2) * 16384 + (h & 3) * 2048);
  auto rearm = [&](int ps) {
    unsigned z = SENT; asm volatile("" : "+v"(z));
    const unsigned l16 = lane_id() * 16;
#pragma unroll
    for (int r = 0; r < 2 * OREP; ++r) st16s(oh + (size_t)(ps * 4 + (r >> 1)) * 131072, (r & 1) * 1024u + l16, u32x4{z, z, z, z});
  };
  const char* dbw = xb + (size_t)48 * 16384 + 4096 + h * 128;
  unsigned avail = 0;
  auto stage_kv = [&](int s) {
    if (avail <= (unsigned)s) {
      for (int spins = 0;; ++spins) {
        const unsigned p = ld4_sc1(cnt + (PQKV + h) * 32 + (lane_id() & 15));
        avail = min(qkv_avail(p, 1), qkv_avail(p, 2));
        if (avail > (unsigned)s) break;
        __builtin_amdgcn_s_sleep(8);
        if (give_up(spins, tmo, 5)) break;
      }
    }
    const char* ks = uni(Kx + ((size_t)s * NH + h) * 8192); const char* vs = uni(V + ((size_t)s * NH + h) * 8192);
    const int lane = lane_id();
    head_to_lds(ks, lane, Q_K); head_to_lds(vs, lane, Q_V);
  };
  auto loop = [&](auto w_c) __attribute__((always_inline)) {
    constexpr int WV = decltype(w_c)::value;
    if (WV >= 4) asm volatile("ds_read_b128 v[56:59], %1\n\tds_read_b128 v[60:63], %1 offset:1024\n\ts_waitcnt lgkmcnt(0)" : "=&{v[56:63]}"(W.x) : "v"(Q_MEM + WV * 2048 + lane_id() * 16) : "memory");
    if (WV == 6) stage_kv(0);
    if (WV < 4) __builtin_amdgcn_s_setprio(2);
    unsigned t0 = 0, cap = 60000, d0 = 105, d1 = 200;
    int dq = D_Q;
    int sl = 0;
    for (int s = 0; s < NSEG; ++s) {
      TSDECL;
      const char* ag = xb + (size_t)((sl * 4 + (AREP == 4 ? xg : (AREP == 2 ? ARSEL : 0))) * 2 + (WV >> 2)) * 16384 + (WV & 3) * 2048;
      const char* vs = WV == 4 ? dbw : WV == 5 ? ws + OFF_KVM + ((size_t)s * NH + h) * 4096 : xb + (size_t)(24 + sl * 8 + (h >> 2)) * 16384 + (h & 3) * 2048;
      const char* od = WV == 4 ? (const char*)(size_t)((unsigned)(s + 1) << 24) : oh + (size_t)sl * 524288;
      if (s > 0) wait_rt(t0, dq);
      TS(0);
      unsigned long long tc; unsigned sp;
      qk_step<WV>(W, ag, vs, od, ws + OFF_KVM + ((size_t)s * NH + h) * 4096 + 2048, cap, sp, tc, nullptr, d0, d1);
      dq = (sp & 0xFFFFu) ? min(dq + DQU, D_Q + DQM) : max(dq - DQD, D_Q);
      if (WV < 4 || WV == 5) {
        const unsigned pv = (unsigned)(s + 1) << 24 | (rt_now() & 0xFFFFFFu);
        WAIT_V(0);
        if (lane_id() == 0) st4_sc1(cnt + (PROG + h) * 32 + WV, pv);
      }
      if (WV == 6 && s + 1 < NSEG - 1) stage_kv(s + 1);
      if (WV == 7 && s > 0 && s < NSEG - 1) rearm(sl == 0 ? 2 : sl - 1);
      if (WV == 7 && s >= 8 && s < 8 + NSEG - SDIR) {
        unsigned z = SENT; asm volatile("" : "+v"(z));
        const char* kd = ws + OFF_KVM + ((size_t)(s - 8 + SDIR) * NH + h) * 4096;
#pragma unroll
        for (int r = 0; r < 4; ++r) st16s(kd, r * 1024u + lane_id() * 16, u32x4{z, z, z, z});
      }
      if (s < NSEG - 1) {
        if (WV == 4) {
          delay64<E_B>();
          t0 = bell_time(xb + (size_t)(48 + xg) * 16384 + 4096, (lane_id() & 31) * 128, (unsigned)(s + 1), cap);
          *(volatile LAS u32x2*)(smem + Q_TM) = u32x2{(unsigned)(s + 1), t0};
        } else {
          for (;;) {
            const u32x2 v = *(volatile LAS u32x2*)(smem + Q_TM);
            t0 = __builtin_amdgcn_readfirstlane(v.y);
            if (__builtin_amdgcn_readfirstlane(v.x) == (unsigned)(s + 1)) break;
            __builtin_amdgcn_s_sleep(1);
          }
        }
      }
      sl = sl == 2 ? 0 : sl + 1;
    }
    if (cap == 0 && lane_id() == 0) st4_sc1(tmo, 1u);
  };
  switch (wave) {
    case 0: loop(std::integral_constant<int, 0>{}); break;
    case 1: loop(std::integral_constant<int, 1>{}); break;
    case 2: loop(std::integral_constant<int, 2>{}); break;
    case 3: loop(std::integral_constant<int, 3>{}); break;
    case 4: loop(std::integral_constant<int, 4>{}); break;
    case 5: loop(std::integral_constant<int, 5>{}); break;
    case 6: loop(std::integral_constant<int, 6>{}); break;
    default: loop(std::integral_constant<int, 7>{}); break;
  }
  __builtin_amdgcn_s_setprio(0);
}

__device__ __forceinline__ void vr_body(char* smem, int wave_id_, int xg, int h, int c, int r, int wg, const u16* __restrict__ Wv, const u16* __restrict__ Wo,
                                        const u16* __restrict__ Wq, const u16* __restrict__ Wk, char* ws, unsigned* tmo, u64 tag) {
  const int wave = WAVE_ID;
  const unsigned po = (unsigned)(r >> 5) * 16384u + ((r >> 2) & 7) * 1024 + (r & 1) * 512 + ((r >> 1) & 1) * 128;
  const unsigned vo_ = (unsigned)(24 + (h >> 2)) * 16384u + (h & 3) * 2048 + c * 1024;
  const int hg = (wave + ROTW * (r & 3)) & 7;
  const int jr = ROTH ? r & 3 : 0;
  VRW W;
  {
    const int lane = lane_id();
    const int g = lane >> 4, q4 = (lane & 15) >> 2, p4 = lane & 3, fr = 4 * g + (q4 & 1) + 32 * (q4 >> 1);
    const unsigned lw = wave * 16384;
    const char* sv_ = uni((const char*)(Wv + (size_t)(128 * wave) * HD + h * DH + 64 * c));
    const char* so = uni((const char*)(Wo + (size_t)(512 * hg) * DM + 64 * (r >> 2) + 16 * (r & 1)));
    const unsigned vo = (unsigned)((32 * (lane >> 5) + ((lane >> 2) & 7)) * DM * 2 + ((lane >> 1) & 1) * 64 + (lane & 1) * 16);
    auto issue = [&](int p) {
#pragma unroll
      for (int i = 0; i < 8; ++i) {
        if (p >= 2 && (NTM & 8)) gldsw(so + (size_t)(128 * ((p - 2) ^ jr) + 64 * (i & 1) + 8 * (i >> 1)) * DM * 2, vo, lw + (p & 1) * 8192 + i * 1024);
        else if (p >= 2) glds16s<0>(so + (size_t)(128 * ((p - 2) ^ jr) + 64 * (i & 1) + 8 * (i >> 1)) * DM * 2, vo, lw + (p & 1) * 8192 + i * 1024);
        else gldsw(sv_ + (size_t)(8 * p + i) * 8 * HD * 2, (unsigned)((lane >> 3) * HD * 2 + (lane & 7) * 16), lw + (p & 1) * 8192 + i * 1024);
      }
    };
#define VTS(p) do { } while (0)
    if (wave == 0) STS(ws, wg, 7);
    issue(0); issue(1);
    const unsigned av = lw + (unsigned)(fr * 128 + p4 * 8), ao = lw + (unsigned)(fr * 64 + p4 * 8);
    WAIT_V(8); VTS(0); vr_wload<0>(W.v0, av); issue(2);
    WAIT_V(8); VTS(1); vr_wload<1>(W.v1, av); issue(3);
    WAIT_V(8); VTS(2); vr_wload<2>(W.o0, ao); issue(4);
    WAIT_V(8); vr_wload<3>(W.o1, ao); issue(5);
    if (wave == 0) init_issue(ws, 131072);
    WAIT_V(8); vr_wload<4>(W.o2, ao);
    WAIT_V(0); VTS(3);
    if (VHN) {
      const int rl = r - 20 * xg, hn = ((xg == 1 ? rl : rl - (rl >= (xg ? 8 : 16) ? 4 : 0)) * 8 + wave) * (6 * VHN) + (lane >> 1);
      const int pm = hn / 384, hr = hn - pm * 384, p = 8 - VHN + (pm >> 1), hh = 12 * xg + 1 + 4 * (hr >> 7);
      const u16* hp = ((pm & 1) ? Wk : Wq) + (size_t)(((pm & 1) ? (p + 2) & 7 : p) * 128 + (hr & 127)) * HD + min(hh, 31) * DH + (lane & 1) * 64;
      if (((r >> 2) & 7) != 3 && lane < 12 * VHN && pm < 2 * VHN) asm volatile("s_mov_b32 m0, %0\n\ts_nop 0\n\tglobal_load_lds_dword %1, off" :: "s"(155648 + wave * 256), "v"(hp) : "memory", "m0");
    }
    vr_wload<5>(W.o3, ao);
#undef VTS
  }
  if (wave == 0) STS(ws, wg, 5);
  if (wave == 0) STS(ws, wg, 6);
  BAR();
  if (wave == 0) STS(ws, wg, 0);
  const unsigned in = init_get(ws, tag, smem + 131072);
  const char* xb = uni(xch_base(ws) + ((size_t)3 << 21) + (size_t)((in & 2u) << 12));
  const char* ob = uni(xch_base(ws) + (size_t)((in & 1u) << 13));
  auto rearm = [&](int ps, bool ag) {
    const int lane = lane_id();
    unsigned z = SENT; asm volatile("" : "+v"(z));
    const u32x4 sv = u32x4{z, z, z, z};
    if (ag && lane < 16 * AREP) st16s(xb, (unsigned)(ps * 8 + (lane >> 4) * 2) * 16384u + po + ((lane >> 3) & 1) * 256 + (lane & 7) * 16, sv);
    st16s(xb, (unsigned)ps * 131072u + vo_ + lane * 16, sv);
  };
  __builtin_amdgcn_s_setprio(2);
  unsigned cap = 60000;
  int sl = 0;
  for (int s = 0; s < NSEG; ++s) {
    TSDECL;
    const int sn = sl == 2 ? 0 : sl + 1;
    const char* ag = xb + (size_t)((sl * 4 + (AREP == 4 ? xg : (AREP == 2 ? ARSEL : 0))) * 2 + (wave >> 2)) * 16384 + (wave & 3) * 2048;
    const char* vd = xb + (size_t)sl * 131072 + vo_ + (wave & 2) * 256 + (wave & 1) * 128;
    const char* os = ob + (size_t)((sl * 4 + (OREP == 4 ? xg : ORSEL)) * 8 + hg) * 16384 + ((r >> 1) & 1) * 1024;
    const char* ad = xb + (size_t)((sn * 4 + (wave & 3)) * 2) * 16384 + po;
    if (s > 0) delay64<E_AG>();
    TS(0);
    unsigned long long tc; unsigned sp;
    unsigned long long* tsp = nullptr;
    vr_v(W, ag, vd, wave * 2048, (wave & 3) * 512, cap, sp, tc, tsp);
    TN(12, sp + 1);
    if (s == NSEG - 1) break;
    if (wave == 7 && s > 0) rearm(sl == 0 ? 2 : sl - 1, true);
    wait_clk((unsigned)tc, E_O);
    TS(3);
    const unsigned tb = bell_time(xb + (size_t)(48 + xg) * 16384 + hg * 512, (lane_id() & 3) * 128, (unsigned)(s + 1), cap);
    wait_rt(tb, D_O);
    TS(4);
    vr_r(W, os, ad, wave * 512, jr * 2048, cap, sp, tsp);
    TN(14, (sp & 0xFFFF) + 1);
    if (wave == 1) TSFLUSH(0x6078);
    sl = sn;
  }
  __builtin_amdgcn_s_setprio(0);
  if (cap == 0 && lane_id() == 0) st4_sc1(tmo, 2u);
}
struct ModIn { u32x4 w[8]; unsigned cv; };
__device__ __forceinline__ void mod_issue(ModIn& I, int wave_id_, int j, const u16* __restrict__ c, const u16* __restrict__ wmod) {
  const int tid = opaque_tid(), k0 = (j >> 5) * 512 + (tid >> 3);
  I.cv = c[k0 + 64 * (tid & 7)];
#pragma unroll
  for (int i = 0; i < 8; ++i) I.w[i] = ldnt<1>(wmod + (size_t)(k0 + 64 * i) * (6 * DM) + (j & 31) * 64 + (tid & 7) * 8);
}
__device__ __forceinline__ void mod_job(char* smem, int wave_id_, int j, u64 tag, const ModIn& I, char* ws) {
  const int tid = opaque_tid(), lane = tid & 63, wave = WAVE_ID, cg = tid & 7;
  const float cv = bflo(I.cv), sl = bfr(cv / (1.0f + __expf(-cv)));
  float acc[8] = {0, 0, 0, 0, 0, 0, 0, 0};
#pragma unroll
  for (int i = 0; i < 8; ++i) {
    const float sv = __shfl(sl, (lane & 56) + i);
#pragma unroll
    for (int e = 0; e < 4; ++e) { acc[2 * e] += sv * bflo(I.w[i][e]); acc[2 * e + 1] += sv * bfhi(I.w[i][e]); }
  }
#pragma unroll
  for (int e = 0; e < 8; ++e) acc[e] = red16_32_add(add_ror8(acc[e]));
  float* red = (float*)(smem + L_GB);
  if (lane < 8) { *(f32x4*)(red + wave * 64 + cg * 8) = f32x4{acc[0], acc[1], acc[2], acc[3]}; *(f32x4*)(red + wave * 64 + cg * 8 + 4) = f32x4{acc[4], acc[5], acc[6], acc[7]}; }
  __syncthreads();
  if (tid < 32) {
    f32x2 s = f32x2{0, 0};
#pragma unroll
    for (int r = 0; r < 8; ++r) s += *(const f32x2*)(red + r * 64 + tid * 2);
    st16_sc1(ws + OFF_MOD + (j * 32 + tid) * 16, u32x4{__float_as_uint(s[0]), (unsigned)tag, __float_as_uint(s[1]), tag2(tag)});
  }
  BAR();
}
#define NIMM 3
constexpr unsigned TK(int m, int hl, int part, int bo, int be) { return (unsigned)(m | hl << 2 | part << 5 | bo << 8 | be << 16); }
#define QX 30
#define CA 36
#define CB 42
#define C2 46
constexpr unsigned task_tab(int t, int it) {
  return t >= 8 && t < 13 ? (it == 0 ? TK(1, t - 8, 0, 0, 64) : it == 1 ? (t < 12 ? TK(2, t - 4, 1, C2, 64) : TK(1, 5, 1, C2, 64)) : 0)
       : it > 0 ? 0
       : t < 8 ? TK(0, t, 1, QX, 64)
       : t < 16 ? TK(1, t - 8, 0, 0, C2)
       : t < 24 ? TK(2, t - 16, 0, 0, t < 18 ? CA : (t < 20 ? CB : C2))
       : t < 26 ? TK(2, t - 24, 1, CA, 64)
       : t == 26 ? TK(1, 6, 1, C2, (C2 + 64) / 2) : t == 27 ? TK(1, 6, 2, (C2 + 64) / 2, 64)
       : t < 30 ? TK(2, t - 26, 1, CB, 64)
       : t == 30 ? TK(1, 7, 1, C2, (C2 + 64) / 2) : t == 31 ? TK(1, 7, 2, (C2 + 64) / 2, 64)
       : TK(0, t - 32, 0, 0, QX);
}
template <int IT, int I = 0> __device__ __forceinline__ unsigned task_sel(int t) {
  if constexpr (I == 40) return 0u;
  else { unsigned r = task_sel<IT, I + 1>(t); constexpr unsigned c = task_tab(I, IT); if (c) asm volatile("s_cmp_eq_u32 %1, %2\n\ts_cselect_b32 %0, %3, %0" : "+s"(r) : "s"(t), "i"(I), "i"(c) : "scc"); return r; }
}
__global__ void __launch_bounds__(512, 2) k_mega(const u16* __restrict__ Wq, const u16* __restrict__ Wk, const u16* __restrict__ Wv,
                                                 const u16* __restrict__ Wo, const u16* __restrict__ w1, const u16* __restrict__ w3,
                                                 const u16* __restrict__ w2, const u16* __restrict__ x, const u16* __restrict__ cvec,
                                                 const u16* __restrict__ wmod, const u16* __restrict__ mem0, char* ws, u16* out) {
  extern __shared__ __attribute__((aligned(16))) char smem[];
  int wave_id_ = __builtin_amdgcn_readfirstlane((int)threadIdx.x >> 6);
  unsigned bid = blockIdx.x;
  asm volatile("" : "+s"(wave_id_), "+s"(bid));
  unsigned* tmo = (unsigned*)(ws + OFF_TMO);
  const unsigned tst = (unsigned)__builtin_amdgcn_s_memrealtime();
  unsigned long long* mts = nullptr;
  const float* mod = (const float*)(ws + OFF_MOD);
  volatile LAS unsigned* tslot = (volatile LAS unsigned*)(smem + L_SLOT + 768);
#define CPF 131072
  if (CPF && wave_id_ == 1) {
    u64 pc; asm volatile("s_getpc_b64 %0" : "=s"(pc));
    const unsigned o = (((bid >> 3) & 31) * 64 + lane_id()) * 64;
    if (o < CPF) asm volatile("s_mov_b32 m0, %0\n\ts_nop 0\n\tglobal_load_lds_dword %1, %2" :: "s"(155648 + 1024), "v"(o), "s"(pc & ~(u64)127) : "memory", "m0");
  }
  unsigned xcc; asm volatile("s_getreg_b32 %0, hwreg(HW_REG_XCC_ID)" : "=s"(xcc));
  xcc &= 7;
  const unsigned pred = xcc * 32 + ((bid >> 3) & 31);
  ModIn mi;
  auto modj = [](unsigned sl) { return sl >= 128 && ((sl + 8) & 31) < 16 ? (int)(((sl >> 5) & 3) * 16 + (sl & 15)) : -1; };
  if (modj(pred) >= 0) mod_issue(mi, wave_id_, modj(pred), cvec, wmod);
  u64 tag;
  asm volatile("s_load_dwordx2 %0, %1, 0x0\n\ts_waitcnt lgkmcnt(0)" : "=s"(tag) : "s"(ws + OFF_TAG) : "memory");
  tag += 1;
  if (opaque_tid() == 0) {
    unsigned sl = pred;
    const unsigned s0 = sl;
    for (unsigned i = 1; i <= 256; ++i) {
      if (__hip_atomic_exchange((u64*)(ws + OFF_CLAIM + sl * 128), tag, __ATOMIC_RELAXED, __HIP_MEMORY_SCOPE_AGENT) != tag) break;
      sl = i == 256 ? 0xFFFFu : (((s0 + (i & ~31u)) & 0xE0u) | ((s0 + i) & 31u));
    }
    *tslot = sl;
  }
  BAR();
  const int slot = (int)__builtin_amdgcn_readfirstlane(*tslot);
  BAR();
  if (slot >= 256) return;
  if (modj(slot) >= 0) {
    if (slot != (int)pred) mod_issue(mi, wave_id_, modj(slot), cvec, wmod);
    mod_job(smem, wave_id_, modj(slot), tag, mi, ws);
  }
  unsigned* cnt = nullptr;
  const int xg = slot >> 5, t = xg == 3 ? 24 + (slot & 7) : slot & 31;
  if (xg < 3) {
    const int nq = xg == 2 ? 8 : 12;
    if (t < nq) { const int h = xg * 12 + t; qk_body(smem, wave_id_, xg, h, (h >> 3) * 32 + (h & 7), Wq, Wk, ws + OFF_K, ws + OFF_V, ws, tmo, tag); }
    else { const int r = xg * 20 + t - nq; vr_body(smem, wave_id_, xg, r >> 1, r & 1, r, (r >> 4) * 32 + 8 + (r & 15), Wv, Wo, Wq, Wk, ws, tmo, tag); }
    return;
  }
  const bool cx = xg == 3;
  const int xq = cx ? (slot >> 3) & 3 : xg & 3, wsl = xq * 32 + t;
#define GDEL 1200
#define GDX 1200
  if (cx || t < 8) l1_role(smem, wave_id_, cx, xq * 8 + (t & 7), x, mod, mem0, ws + OFF_H1, cnt, tag, tmo, mts);
#define KH 0
  if (KH && !cx && t >= 24) {
    const int it = (t & 7) * 8 + wave_id_, lane = lane_id();
    touch(((it & 16) ? Wk : Wv) + (size_t)(256 + (it & 8) * 64 + (it & 7) * 32) * HD + (8 * xq + 1 + 4 * (it >> 5)) * DH,
          (unsigned)((lane >> 1) * HD * 2 + (lane & 1) * 128), 155648 + wave_id_ * 256);
  }
  if (t < 8 || t >= 24) {
    while ((unsigned)__builtin_amdgcn_s_memrealtime() - tst < (cx ? GDX : GDEL)) __builtin_amdgcn_s_sleep(8);
    if (!cx && t >= 24) {
      cnt = cnt_of(ws, init_wait(ws, tag));
      mod2_job(smem, wave_id_, xq * 8 + t - 24, mod, cvec, wmod, cnt);
    }
  }
  auto u2_of = [&]() { return cx ? -1 : (t >= 13 && t < 16 ? t - 13 : (t >= 20 && t < 24 ? t - 17 : -1)); };
  auto f_of = [&]() {
    int fl = t < 8 ? t : (t < 18 ? t - 4 : (t < 20 ? t - 10 : t - 14));
    const int sa = (3 + 2 * xq) & 7, sb = sa + 8 < 14 ? sa + 8 : 13;
    if (fl == 12) fl = sa; else if (fl == sa) fl = 12;
    else if (fl == 13) fl = sb; else if (fl == sb) fl = 13;
    return (int)__builtin_amdgcn_readfirstlane(14 * xq + fl);
  };
  unsigned tk = task_sel<0>(cx ? t + 8 : t), t2 = task_sel<1>(cx ? t + 8 : t), t3 = task_sel<2>(cx ? t + 8 : t);
  for (int it = 0; tk != 0; ++it, tk = t2, t2 = t3, t3 = 0) {
    const int m = tk & 3, hl = (tk >> 2) & 7, part = (tk >> 5) & 3, bo = (tk >> 8) & 255, ns = (int)(tk >> 16) - bo, h = 8 * xq + hl;
    const bool first = m != 0 && bo == 0, late = it > 0;
    const int lane = lane_id();
    const Dep d{nullptr, (unsigned)((lane < 32 ? PL1 : PL1B - 32) + lane) * 128, 8u, (unsigned)((lane < 32 ? 0 : 8) - bo)};
    if (wave_id_ == 0 && late) wait_l1(cnt + (PL1B - PL1) * 32, 7u, tmo);
    BAR();
    gemm7<R_QKV>(smem, wave_id_, (m == 0 ? Wq : (m == 1 ? Wk : Wv)) + h * DH, nullptr, HD, ws + OFF_H1 + (size_t)bo * 65536, 65536, d,
                    ws + (m == 0 ? OFF_Q : (m == 1 ? OFF_K : OFF_V)) + h * 8192 + (size_t)bo * 32 * 8192, 32 * 8192,
                    cnt, (PQKV + h) * 32 + 4 * part + m, tmo, wsl, mts, false, first ? NIMM : 0, ns, late ? ns : 0, bo * 257, tag);
  }
  __syncthreads();
  const int u2 = u2_of();
  if (cx || (u2 >= 0 && (xq < 3 || u2 < 3))) {
    const int ks = cx || xq < 3 ? xq : u2, cn = cx ? (t + 1) & 7 : (xq < 3 ? u2 : 7);
    const int lane = lane_id();
    const Dep d{nullptr, (unsigned)(cx ? (PSA + 4 * xq + ((lane >> 3) & 3)) * 128 + (lane & 7) * 4 : (PF1 + 16 * ks + (lane & 15)) * 128), cx ? 4u : 1u, cx ? (unsigned)((lane >> 1) & 3) : 0u};
    gemm7<R_PART>(smem, wave_id_, (cx ? Wo : w2) + (size_t)ks * 1024 * DM + cn * 128, nullptr, DM, ws + (cx ? OFF_O : OFF_G) + ks * 16 * 4096, (cx ? 64 : 56) * 4096, d,
                  ws + (cx ? OFF_P : OFF_P2) + (ks * DM + cn * 128) * (TLP ? 64 : 2), (size_t)32 * PROW, cnt, ((cx ? PWO : PF2) + ks * 8 + cn) * 32, tmo,
                  128 + xq * 32 + (cx ? cn : 22 + u2), mts, !cx);
  } else if (u2 >= 0) {
    const int c4 = u2 - 3;
    const int lane = lane_id();
    const Dep d{nullptr, (unsigned)(PF1 + 48 + (lane & 7)) * 128, 1u, 0u};
    gemm7<R_PART2>(smem, wave_id_, w2 + (size_t)3072 * DM + c4 * 256, nullptr, DM, ws + OFF_G + 48 * 4096, 56 * 4096, d,
                   ws + OFF_P2 + (3 * DM + c4 * 256) * (TLP ? 64 : 2), (size_t)32 * PROW, cnt, (PF2 + 24 + c4) * 32, tmo, 128 + xq * 32 + 22 + u2, mts, true);
  } else if (t < 8 || (t >= 16 && t < 20) || t == 24 || t == 25) {
    int f = f_of();
    asm volatile("" : "+s"(f));
    const int lane = lane_id();
    const Dep d{nullptr, (unsigned)((PLN + (lane >> 3)) * 128 + (lane & 7) * 4), 1u, 0u};
    gemm7<R_F1>(smem, wave_id_, w1 + 64 * f, w3 + 64 * f, FF, ws + OFF_H2, 65536, d, ws + OFF_G + f * 4096, 56 * 4096, cnt, (PF1 + f) * 32, tmo, 128 + xq * 18 + 8 + f, mts, false);
  } else if (t >= 9 && t < 13) {
    fin_role(smem, wave_id_, xq * 4 + t - 9, mod, ws + OFF_P2, (const u16*)(ws + OFF_X1), out, cnt, tmo, mts);
    if (xq == 3 && t == 12 && opaque_tid() == 0) {
      const u32x4 v = ld16_sc1(ws + OFF_TAG);
      const u64 tn = (((u64)v.y << 32) | v.x) + 1;
      st8_sc1(ws + OFF_TAG, u32x2{(unsigned)tn, (unsigned)(tn >> 32)});
    }
  } else if (t == 28 || t == 29) {
    row_role(smem, wave_id_, 8 * xq, t - 28, mod, ws + OFF_P, x, (u16*)(ws + OFF_X1), ws + OFF_H2, cnt, tmo, 256 + xq, mts);
  } else if (t >= 26) {
    sa_role(smem, wave_id_, 8 * xq + 2 * (t >= 30 ? t - 30 : t - 24), ws + OFF_Q, ws + OFF_K, ws + OFF_V, ws + OFF_KVM, ws + OFF_O, cnt, tmo, 128 + xq * 32 + 30 + (t & 1), mts);
  }
}

extern "C" void kernel_launch(void* const* d_in, const int* in_sizes, int n_in, void* d_out, int out_size, void* d_ws, size_t ws_size,
                              hipStream_t stream) {
  const u16* x    = (const u16*)d_in[0];
  const u16* mem0 = (const u16*)d_in[1];
  const u16* Wq   = (const u16*)d_in[2];
  const u16* Wk   = (const u16*)d_in[3];
  const u16* Wv   = (const u16*)d_in[4];
  const u16* Wo   = (const u16*)d_in[5];
  const u16* w1   = (const u16*)d_in[8];
  const u16* w3   = (const u16*)d_in[9];
  const u16* w2   = (const u16*)d_in[10];
  const u16* c    = (const u16*)d_in[11];
  const u16* wmod = (const u16*)d_in[12];
#define WSHIFT 0
  char* ws = (char*)d_ws + ((size_t)WSHIFT << 20);
  hipFuncSetAttribute((const void*)k_mega, hipFuncAttributeMaxDynamicSharedMemorySize, MEGA_LDS);
  k_mega<<<256, 512, MEGA_LDS, stream>>>(Wq, Wk, Wv, Wo, w1, w3, w2, x, c, wmod, mem0, ws, (u16*)d_out);
}
```

```cpp
#include <hip/hip_runtime.h>
#include <hip/hip_bf16.h>
#include <stdint.h>
#include <type_traits>
#pragma clang diagnostic ignored "-Winline-asm"
#pragma clang diagnostic ignored "-Wunused-value"

typedef unsigned short u16;
typedef short s16x4 __attribute__((ext_vector_type(4)));
typedef __bf16 bf16x8 __attribute__((ext_vector_type(8)));
typedef float f32x4 __attribute__((ext_vector_type(4)));
typedef float f32x2 __attribute__((ext_vector_type(2)));
typedef unsigned u32x4 __attribute__((ext_vector_type(4)));
typedef unsigned u32x2 __attribute__((ext_vector_type(2)));
#define LAS __attribute__((address_space(3)))

constexpr int SEQ = 2048, DM = 1024, NH = 32, DH = 128, HD = 4096, NMEM = 8, SEG = 32, NSEG = 64, FF = 3584;
constexpr unsigned SENT = 0xFFFFFFFFu;

__device__ __forceinline__ int lane_id() { int l; asm volatile("v_mbcnt_lo_u32_b32 %0, -1, 0\n\tv_mbcnt_hi_u32_b32 %0, -1, %0" : "=v"(l)); return l; }
#define WAVE_ID wave_id_
#define opaque_tid() (WAVE_ID * 64 + lane_id())
__device__ __forceinline__ float bf2f(u16 v) { return __builtin_bit_cast(float, (unsigned)v << 16); }
__device__ __forceinline__ float bflo(unsigned v) { return __builtin_bit_cast(float, v << 16); }
__device__ __forceinline__ float bfhi(unsigned v) { return __builtin_bit_cast(float, v & 0xffff0000u); }
__device__ __forceinline__ float bfr(float x) { return (float)(__bf16)x; }
__device__ __forceinline__ unsigned pack2(float a, float b) {
  typedef __bf16 bf16x2 __attribute__((ext_vector_type(2)));
  bf16x2 v; v[0] = (__bf16)a; v[1] = (__bf16)b; return __builtin_bit_cast(unsigned, v);
}
__device__ __forceinline__ unsigned nosent(unsigned v) { return v == SENT ? 0x7FC07FC0u : v; }
__device__ __forceinline__ bf16x8 asbf8(u32x4 v) { return __builtin_bit_cast(bf16x8, v); }
__device__ __forceinline__ s16x4 ass4(u32x2 v) { return __builtin_bit_cast(s16x4, v); }
__device__ __forceinline__ u32x2 trread(const char* lds_base, unsigned off) {
  s16x4 v = __builtin_amdgcn_ds_read_tr16_b64_v4i16((LAS s16x4*)(lds_base + off));
  return __builtin_bit_cast(u32x2, v);
}
__device__ __forceinline__ void glds16(const void* gsrc, unsigned lds_dst) {
  unsigned keep;
  asm volatile("s_mov_b32 %0, m0\n\ts_mov_b32 m0, %2\n\ts_nop 0\n\tglobal_load_lds_dwordx4 %1, off\n\ts_mov_b32 m0, %0"
               : "=&s"(keep) : "v"(gsrc), "s"(lds_dst) : "memory");
}
#define CNT_ " nt"
#define WNT_ " nt"
__device__ __forceinline__ void glds16_w(const void* gsrc, unsigned lds_dst) {
  unsigned keep;
  asm volatile("s_mov_b32 %0, m0\n\ts_mov_b32 m0, %2\n\ts_nop 0\n\tglobal_load_lds_dwordx4 %1, off" CNT_ "\n\ts_mov_b32 m0, %0"
               : "=&s"(keep) : "v"(gsrc), "s"(lds_dst) : "memory");
}
__device__ __forceinline__ void glds16_sc1(const void* gsrc, unsigned lds_dst) {
  unsigned keep;
  asm volatile("s_mov_b32 %0, m0\n\ts_mov_b32 m0, %2\n\ts_nop 0\n\tglobal_load_lds_dwordx4 %1, off sc1\n\ts_mov_b32 m0, %0"
               : "=&s"(keep) : "v"(gsrc), "s"(lds_dst) : "memory");
}
#define NTM 1
template <int B> __device__ __forceinline__ u32x4 ldnt(const void* p) { if (NTM & B) return __builtin_nontemporal_load((const u32x4*)p); else return *(const u32x4*)p; }
#define MFMA32(a, b, c) __builtin_amdgcn_mfma_f32_16x16x32_bf16((a), (b), (c), 0, 0, 0)

__device__ __forceinline__ void st16_sc1(void* p, u32x4 v) { asm volatile("global_store_dwordx4 %0, %1, off sc1\n\ts_nop 1" :: "v"(p), "v"(v) : "memory"); }
__device__ __forceinline__ void st8_sc1(void* p, u32x2 v) { asm volatile("global_store_dwordx2 %0, %1, off sc1\n\ts_nop 1" :: "v"(p), "v"(v) : "memory"); }
template <bool WT> __device__ __forceinline__ void st8(void* p, u32x2 v) { if (WT) st8_sc1(p, v); else *(u32x2*)p = v; }
template <bool WT> __device__ __forceinline__ void st16(void* p, u32x4 v) { if (WT) st16_sc1(p, v); else *(u32x4*)p = v; }
__device__ __forceinline__ u32x4 ld16_sc1(const void* p) {
  u32x4 r; asm volatile("global_load_dwordx4 %0, %1, off sc1\n\ts_waitcnt vmcnt(0)" : "=&v"(r) : "v"(p) : "memory"); return r;
}
__device__ __forceinline__ void ld16x4_sc1(const void* p, u32x4& a, u32x4& b, u32x4& c, u32x4& d) {
  asm volatile("global_load_dwordx4 %0, %4, off sc1\n\tglobal_load_dwordx4 %1, %4, off offset:1024 sc1\n\t"
               "global_load_dwordx4 %2, %4, off offset:2048 sc1\n\tglobal_load_dwordx4 %3, %4, off offset:3072 sc1\n\ts_waitcnt vmcnt(0)"
               : "=&v"(a), "=&v"(b), "=&v"(c), "=&v"(d) : "v"(p) : "memory");
}
typedef __amdgpu_buffer_rsrc_t rsrc_t;
__device__ __forceinline__ rsrc_t mkrs(const void* p) { return __builtin_amdgcn_make_buffer_rsrc((void*)p, 0, 0x7FFFFFFF, 0x00020000); }
__device__ __forceinline__ u32x4 bld16(rsrc_t r, unsigned off) { return __builtin_amdgcn_raw_buffer_load_b128(r, off, 0, 16); }
__device__ __forceinline__ u32x2 bld8(rsrc_t r, unsigned off) { return __builtin_amdgcn_raw_buffer_load_b64(r, off, 0, 16); }
__device__ __forceinline__ bool ok4(u32x4 v) { return v.x != SENT && v.y != SENT && v.z != SENT && v.w != SENT; }
constexpr int SPIN_CAP = 2000000;
__device__ __forceinline__ bool give_up(int spins, unsigned* tmo, unsigned code) {
  if ((spins & 4095) != 4095) return false;
  if (spins > SPIN_CAP) { __hip_atomic_store(tmo, code, __ATOMIC_RELAXED, __HIP_MEMORY_SCOPE_AGENT); return true; }
  return __hip_atomic_load(tmo, __ATOMIC_RELAXED, __HIP_MEMORY_SCOPE_AGENT) != 0u;
}
template <bool ACQ = true>
__device__ __forceinline__ void wait_cnt(unsigned* c, unsigned n, unsigned* tmo) {
  for (int spins = 0;; ++spins) {
    const unsigned v = __hip_atomic_load(c, __ATOMIC_RELAXED, __HIP_MEMORY_SCOPE_AGENT);
    if (v >= n) break;
    __builtin_amdgcn_s_sleep(8);
    if (give_up(spins, tmo, 5)) break;
  }
  if (ACQ) {
    __builtin_amdgcn_fence(__ATOMIC_ACQUIRE, "agent");
    asm volatile("s_waitcnt vmcnt(0)" ::: "memory");
  }
}

__device__ __forceinline__ const char* uni(const char* p) {
  const unsigned long long v = (unsigned long long)p;
  const unsigned lo = __builtin_amdgcn_readfirstlane((unsigned)v), hi = __builtin_amdgcn_readfirstlane((unsigned)(v >> 32));
  return (const char*)(((unsigned long long)hi << 32) | lo);
}
#define TSCLK "s_memrealtime"
#define TC(k) do { } while (0)
#define TS(k) do { } while (0)
#define TSDECL do { } while (0)
#define TSFLUSH(mask) do { } while (0)

constexpr size_t OFF_MOD  = 0;
constexpr size_t OFF_MOD2 = 65536;
constexpr size_t OFF_TMO  = 98304;
constexpr size_t OFF_TAG  = 131072;
constexpr size_t OFF_CLAIM = 135168;
constexpr size_t OFF_INIT = 196608;
constexpr size_t OFF_H1   = 262144;
constexpr size_t OFF_Q    = OFF_H1 + (size_t)SEQ * DM * 2;
constexpr size_t OFF_K    = OFF_Q + (size_t)SEQ * HD * 2;
constexpr size_t OFF_V    = OFF_K + (size_t)SEQ * HD * 2;
constexpr size_t OFF_O    = OFF_V + (size_t)SEQ * HD * 2;
constexpr size_t OFF_X1   = OFF_O + (size_t)SEQ * HD * 2;
constexpr size_t OFF_H2   = OFF_X1 + (size_t)SEQ * DM * 2;
constexpr size_t OFF_G    = OFF_H2 + (size_t)SEQ * DM * 2;
constexpr size_t OFF_KVM  = (size_t)92 << 20;
constexpr size_t OFF_XCH  = (size_t)100 << 20;
constexpr size_t OFF_P    = (size_t)112 << 20;
constexpr size_t OFF_P2   = (size_t)128 << 20;
constexpr size_t OFF_DUMP = (size_t)144 << 20;
constexpr int PROW = 4 * DM * 2;
#define TLQ 1
#define TLP 1
static_assert(OFF_G + (size_t)SEQ * FF * 2 <= OFF_KVM, "layout");
__device__ __forceinline__ char* xch_base(char* ws) { return (char*)(((size_t)(ws + OFF_XCH) + ((2u << 20) - 1)) & ~(size_t)((2u << 20) - 1)); }
#define XPAR 0
constexpr int PROG = 16;
constexpr int PQKV = 64;
constexpr int PL1 = 96;
constexpr int PL1B = 350;
constexpr int PSA = 128;
constexpr int PWO = 192;
constexpr int PLN = 224;
constexpr int PF2 = 320;
constexpr int PF1 = 448;
#define SDIR 40
#define SB 56
#define MARG 20
#define MARL 15
#define MARF 8
#define HB 61
#define MARH 35
#define SAW 0
constexpr int L_RED = 131072;
constexpr int L_SLOT = L_RED + 16384;
constexpr int L_GB = L_SLOT + 1024;
constexpr int MEGA_LDS = 163840;
static_assert(L_GB + 12288 + 2048 <= MEGA_LDS, "lds");
#define XPARC 0

__device__ __forceinline__ int bswz(int row) { return (((row >> 1) & 1) | (((row >> 3) & 1) << 1)) << 1; }
template <int OFF, bool SC1 = false>
__device__ __forceinline__ void glds16s(const char* sbase, unsigned voff, unsigned lds_base) {
  unsigned keep;
  if (SC1)
    asm volatile("s_mov_b32 %0, m0\n\ts_add_u32 m0, %3, %4\n\ts_nop 2\n\tglobal_load_lds_dwordx4 %1, %2 sc1\n\ts_mov_b32 m0, %0"
                 : "=&s"(keep) : "v"(voff), "s"(sbase), "s"(lds_base), "i"(OFF) : "memory", "scc");
  else
    asm volatile("s_mov_b32 %0, m0\n\ts_add_u32 m0, %3, %4\n\ts_nop 2\n\tglobal_load_lds_dwordx4 %1, %2\n\ts_mov_b32 m0, %0"
                 : "=&s"(keep) : "v"(voff), "s"(sbase), "s"(lds_base), "i"(OFF) : "memory", "scc");
}
template <int OFF>
__device__ __forceinline__ void glds16x2s(const char* sbase, unsigned voff, unsigned lds_base) {
  unsigned keep;
  asm volatile("s_mov_b32 %0, m0\n\ts_add_u32 m0, %3, %4\n\ts_nop 2\n\tglobal_load_lds_dwordx4 %1, %2 sc1\n\tglobal_load_lds_dwordx4 %1, %2 offset:1024 sc1\n\ts_mov_b32 m0, %0"
               : "=&s"(keep) : "v"(voff), "s"(sbase), "s"(lds_base), "i"(OFF) : "memory", "scc");
}
__device__ __forceinline__ void st16s(const char* sb, unsigned vo, u32x4 v) { asm volatile("global_store_dwordx4 %0, %1, %2 sc1\n\ts_nop 1" :: "v"(vo), "v"(v), "s"(sb) : "memory"); }
__device__ __forceinline__ void head_to_lds(const char* src, int lane, unsigned lds) {
  if (TLQ) {
    const int c = (lane & 15) ^ (lane >> 4);
    const unsigned v0 = (unsigned)((c >> 2) * 2048 + (lane >> 4) * 64 + (c & 3) * 16);
#pragma unroll
    for (int i = 0; i < 8; ++i) glds16s<0, true>(src + i * 256, v0 ^ ((i & 3) * 2048), lds + i * 1024);
  } else {
#pragma unroll
    for (int i = 0; i < 4; ++i) glds16x2s<0>(src + i * 2048, (unsigned)lane * 16, lds + i * 2048);
  }
}
#define TCHP ""
__device__ __forceinline__ void touch(const void* sb, unsigned vo, unsigned lds) {
  asm volatile("s_mov_b32 m0, %0\n\ts_nop 0\n\tglobal_load_lds_dword %1, %2" TCHP :: "s"(lds), "v"(vo), "s"(sb) : "memory", "m0");
}
#define WAIT_V(n) asm volatile("s_waitcnt vmcnt(" #n ")" ::: "memory")
#define BAR() do { asm volatile("s_waitcnt lgkmcnt(0)" ::: "memory"); __builtin_amdgcn_s_barrier(); asm volatile("" ::: "memory"); } while (0)
__device__ __forceinline__ void st4_sc1(void* p, unsigned v) { asm volatile("global_store_dword %0, %1, off sc1" :: "v"(p), "v"(v) : "memory"); }
__device__ __forceinline__ unsigned ld4_sc1(const void* p) {
  unsigned r; asm volatile("global_load_dword %0, %1, off sc1\n\ts_waitcnt vmcnt(0)" : "=&v"(r) : "v"(p) : "memory"); return r;
}
template <int CTRL> __device__ __forceinline__ unsigned dppu(unsigned x) { return (unsigned)__builtin_amdgcn_update_dpp(0, (int)x, CTRL, 0xf, 0xf, false); }
__device__ __forceinline__ unsigned rowmin16(unsigned v) {
  v = min(v, dppu<0x128>(v)); v = min(v, dppu<0x124>(v)); v = min(v, dppu<0x122>(v)); v = min(v, dppu<0x121>(v));
  return v;
}
__device__ __forceinline__ unsigned rdl(unsigned v, int l) { return (unsigned)__builtin_amdgcn_readlane((int)v, l); }
__device__ __forceinline__ unsigned qkv_avail(unsigned v, int m) {
  unsigned a = rdl(v, m);
#pragma unroll
  for (int j = 1; j < 4; ++j) { const unsigned p = rdl(v, 4 * j + m); if ((p >> 8) <= a) a = max(a, p & 255u); }
  return a;
}
constexpr float SCALE = 0.08838834764831845f;
constexpr float LOG2E = 1.4426950408889634f;
template <int N> __device__ __forceinline__ void delay64() {
  if (N >= 16) { __builtin_amdgcn_s_sleep(15); delay64<(N >= 16 ? N - 15 : 0)>(); } else if (N > 0) __builtin_amdgcn_s_sleep(N);
}
__device__ __forceinline__ void sw16(float x, float& a, float& b) {
  a = x; b = x; asm volatile("s_nop 1\n\tv_permlane16_swap_b32 %0, %1" : "+v"(a), "+v"(b));
}
__device__ __forceinline__ void sw32(float x, float& a, float& b) {
  a = x; b = x; asm volatile("s_nop 1\n\tv_permlane32_swap_b32 %0, %1" : "+v"(a), "+v"(b));
}
__device__ __forceinline__ float red16_32_max(float x) {
  float a, b; sw16(x, a, b); x = fmaxf(a, b); sw32(x, a, b); return fmaxf(a, b);
}
__device__ __forceinline__ float red16_32_add(float x) {
  float a, b; sw16(x, a, b); x = a + b; sw32(x, a, b); return a + b;
}
__device__ __forceinline__ unsigned cvtpk(float a, float b) { unsigned r; asm("v_cvt_pk_bf16_f32 %0, %1, %2" : "=v"(r) : "v"(a), "v"(b)); return r; }
__device__ __forceinline__ float max3(float a, float b, float c) { float r; asm("v_max3_f32 %0, %1, %2, %3" : "=v"(r) : "v"(a), "v"(b), "v"(c)); return r; }
__device__ __forceinline__ float max2(float a, float b) { float r; asm("v_max_f32 %0, %1, %2" : "=v"(r) : "v"(a), "v"(b)); return r; }
__device__ __forceinline__ unsigned ror8u(unsigned x) { return (unsigned)__builtin_amdgcn_update_dpp(0, (int)x, 0x128, 0xf, 0xf, false); }
__device__ __forceinline__ float ror8(float x) { return __builtin_bit_cast(float, ror8u(__builtin_bit_cast(unsigned, x))); }
__device__ __forceinline__ float add_ror8(float x) {
  return x + __builtin_bit_cast(float, __builtin_amdgcn_update_dpp(0, __builtin_bit_cast(int, x), 0x128, 0xf, 0xf, false));
}

__device__ __forceinline__ unsigned rt_now() { return (unsigned)__builtin_amdgcn_s_memrealtime(); }
__device__ __forceinline__ unsigned latest(unsigned v) {
  const unsigned now = rt_now() + 400u, age = rowmin16((now - v) & 0xFFFFFFu);
  return now - min(min(min(rdl(age, 0), rdl(age, 16)), min(rdl(age, 32), rdl(age, 48))), 900u);
}
__device__ __forceinline__ void wait_rt(unsigned t0, int d) {
  while ((int)(rt_now() - t0) < d) { }
}
__device__ __forceinline__ bool bells(bool ok, unsigned u, int b, int marg) {
  if (b < SB || !__all(ok || (int)(u >> 24) > b)) return false;
  wait_rt(latest(!ok && (int)(u >> 24) == b + 1 ? u : rt_now() - 400u), marg);
  return true;
}
__device__ __forceinline__ void ld_fb(unsigned& v, unsigned& u, const void* sb, unsigned vo) {
  asm volatile("global_load_dword %0, %2, %3 sc1\n\tglobal_load_dword %1, %2, %3 offset:64 sc1\n\ts_waitcnt vmcnt(0)" : "=&v"(v), "=&v"(u) : "v"(vo), "s"(sb) : "memory");
}
__device__ __forceinline__ unsigned bell(int b, unsigned t) { return (unsigned)(b + 1) << 24 | (t & 0xFFFFFFu); }
typedef unsigned long long u64;
__device__ __forceinline__ unsigned tag2(u64 tag) { return (unsigned)(tag >> 32) ^ ((unsigned)tag * 0x9E3779B1u); }
__device__ __forceinline__ unsigned init_eval(u32x4 v, u64 tag) {
  if (!__all((v.x & ~63u) == (unsigned)tag << 6 && v.y == (unsigned)(tag >> 26))) return 0u;
  unsigned r = 8u;
#pragma unroll
  for (int c = 0; c < 3; ++c) r |= __popcll(__ballot(v.x & (1u << c))) > __popcll(__ballot(v.x & (8u << c))) ? 1u << c : 0u;
  return r ^ (XPAR * 3u + XPARC * 4u);
}
__device__ __forceinline__ unsigned init_wait(const char* ws, u64 tag) {
  for (int spins = 0; spins < SPIN_CAP; ++spins) {
    const unsigned r = init_eval(ld16_sc1(ws + OFF_INIT + (lane_id() & 31) * 128), tag);
    if (r) return r;
    __builtin_amdgcn_s_sleep(4);
  }
  return 8u;
}
__device__ __forceinline__ void init_issue(const char* ws, unsigned lds) { glds16s<0, true>(uni(ws + OFF_INIT), (unsigned)(lane_id() & 31) * 128u, lds); }
__device__ __forceinline__ unsigned init_get(const char* ws, u64 tag, const char* copy) {
  const unsigned r = init_eval(*(volatile LAS u32x4*)(copy + lane_id() * 16), tag);
  return r ? r : init_wait(ws, tag);
}
__device__ __forceinline__ unsigned* cnt_of(char* ws, unsigned in) { return (unsigned*)uni(xch_base(ws) + ((size_t)4 << 21) + (size_t)((in & 4u) << 11)); }
__device__ __forceinline__ void wait_l1(const unsigned* cnt, unsigned n, unsigned* tmo) {
  const int lane = lane_id();
  for (int spins = 0;; ++spins) {
    const unsigned v = ld4_sc1(cnt + (PL1 + (lane & 31)) * 32);
    if (__all(v >= n)) break;
    __builtin_amdgcn_s_sleep(8);
    if (give_up(spins, tmo, 12)) break;
  }
}
#define GKV 1
__device__ __forceinline__ void wait_k0(const unsigned* cnt, int h, unsigned* tmo) {
  for (int spins = 0;; ++spins) {
    const int l = lane_id();
    const unsigned v = ld4_sc1(cnt + (PQKV + h) * 32 + 1 + (l & 1));
    if (__all(v >= (unsigned)GKV)) break;
    __builtin_amdgcn_s_sleep(32);
    if (give_up(spins, tmo, 13)) break;
  }
}
#define WCLK(slot, b) do { } while (0)
#define WTM(slot, b, k) do { } while (0)
#define WTS(slot, b, k) do { } while (0)
#define WFB() do { } while (0)
#define STS(ws_, slot_, k_) do { } while (0)
typedef __bf16 bf16x2 __attribute__((ext_vector_type(2)));
__device__ __forceinline__ f32x2 pkfma(f32x2 a, f32x2 b, f32x2 c) { return __builtin_elementwise_fma(a, b, c); }
__device__ __forceinline__ float wavesum(float v) {
  v += __builtin_bit_cast(float, dppu<0x128>(__builtin_bit_cast(unsigned, v))); v += __builtin_bit_cast(float, dppu<0x124>(__builtin_bit_cast(unsigned, v)));
  v += __builtin_bit_cast(float, dppu<0x122>(__builtin_bit_cast(unsigned, v))); v += __builtin_bit_cast(float, dppu<0x121>(__builtin_bit_cast(unsigned, v)));
  return red16_32_add(v);
}
__device__ __forceinline__ float adds(unsigned v, unsigned sel, float c) { return __builtin_amdgcn_fdot2_f32_bf16(__builtin_bit_cast(bf16x2, v), __builtin_bit_cast(bf16x2, sel), c, false); }
__device__ __forceinline__ void sum4(const u32x4 (&p)[4], f32x2 (&a)[4]) {
  unsigned slo = 0x00003F80u, shi = 0x3F800000u;
  asm("" : "+s"(slo), "+s"(shi));
#pragma unroll
  for (int e = 0; e < 4; ++e) {
    float lo = bflo(p[0][e]), hi = bfhi(p[0][e]);
#pragma unroll
    for (int q = 1; q < 4; ++q) { lo = adds(p[q][e], slo, lo); hi = adds(p[q][e], shi, hi); }
    a[e] = f32x2{lo, hi};
  }
}
__device__ __forceinline__ u32x4 norm8(const f32x2 (&v)[4], const char* mt, float mb, float rden) {
  const f32x4 h0 = *(const f32x4*)(mt + 4096), h1 = *(const f32x4*)(mt + 4096 + 1024), c0 = *(const f32x4*)(mt + 8192), c1 = *(const f32x4*)(mt + 8192 + 1024);
  const f32x2 m2 = f32x2{mb, mb}, d2 = f32x2{rden, rden};
  const f32x2 r0 = pkfma(v[0] - m2, f32x2{c0[0], c0[1]} * d2, f32x2{h0[0], h0[1]}), r1 = pkfma(v[1] - m2, f32x2{c0[2], c0[3]} * d2, f32x2{h0[2], h0[3]});
  const f32x2 r2 = pkfma(v[2] - m2, f32x2{c1[0], c1[1]} * d2, f32x2{h1[0], h1[1]}), r3 = pkfma(v[3] - m2, f32x2{c1[2], c1[3]} * d2, f32x2{h1[2], h1[3]});
  return u32x4{pack2(r0[0], r0[1]), pack2(r1[0], r1[1]), pack2(r2[0], r2[1]), pack2(r3[0], r3[1])};
}
__device__ __forceinline__ void half_load(const char* P, const u16* res, int row, int hf, int lane, f32x2 (&a)[4], u32x4& rv) {
  const rsrc_t rp = mkrs(P), rr = mkrs(res);
  const unsigned po = TLP ? (unsigned)((row >> 5) * 32 * PROW + (hf * 16 + (lane >> 2)) * 2048 + (row & 31) * 64 + (lane & 3) * 16) : (unsigned)(row * PROW + hf * 1024 + lane * 16), ro = (unsigned)(row * DM * 2 + hf * 1024 + lane * 16);
  u32x4 p[4];
#pragma unroll
  for (int q = 0; q < 4; ++q) p[q] = bld16(rp, po + q * (TLP ? 65536 : 2048));
  if (res) rv = bld16(rr, ro);
  sum4(p, a);
}
__device__ __forceinline__ bool fin_half(bool fin, const char* P, const u16* res, const char* gbt, u16* __restrict__ o1, int row, int hf, int lane) {
  f32x2 a[4]; u32x4 rv;
  half_load(P, res, row, hf, lane, a, rv);
  const u32x4 gv = *(const u32x4*)(gbt + hf * 1024 + lane * 16);
  u32x4 o; f32x2 t = f32x2{0, 0};
#pragma unroll
  for (int e = 0; e < 4; ++e) { const f32x2 s = pkfma(f32x2{bflo(gv[e]), bfhi(gv[e])}, a[e], f32x2{bflo(rv[e]), bfhi(rv[e])}); o[e] = pack2(s[0], s[1]); t += s; }
  if (!fin && !__all(t[0] == t[0] && t[1] == t[1])) return false;
  st16_sc1(o1 + (size_t)row * DM + hf * 512 + lane * 8, o);
  return true;
}
#define DLN 10
#define LNE 1
template <int CTRL> __device__ __forceinline__ float dppf(float x) { return __builtin_bit_cast(float, dppu<CTRL>(__builtin_bit_cast(unsigned, x))); }
__device__ __forceinline__ bool ln_row(bool fin, int bb, unsigned* bl, const f32x2 (&a)[2][4], const f32x2 (&xf)[8], const f32x2 (&tg)[8], const f32x2 (&th)[8], const f32x2 (&tc)[8],
                                       const char* x1, const char* h2, unsigned xo, unsigned ho) {
  f32x2 v[8]; u32x4 o[2];
#pragma unroll
  for (int e = 0; e < 8; ++e) { const f32x2 t = pkfma(tg[e], a[e >> 2][e & 3], xf[e]); const unsigned u = pack2(t[0], t[1]); o[e >> 2][e & 3] = u; v[e] = f32x2{bflo(u), bfhi(u)}; }
  const float pv = __builtin_bit_cast(float, __builtin_amdgcn_readfirstlane(__float_as_int(v[0][0])));
  f32x2 d[8];
#pragma unroll
  for (int e = 0; e < 8; ++e) d[e] = v[e] - f32x2{pv, pv};
  const f32x2 s2 = ((d[0] + d[1]) + (d[2] + d[3])) + ((d[4] + d[5]) + (d[6] + d[7]));
  const f32x2 q2 = pkfma(d[3], d[3], pkfma(d[2], d[2], pkfma(d[1], d[1], d[0] * d[0]))) + pkfma(d[7], d[7], pkfma(d[6], d[6], pkfma(d[5], d[5], d[4] * d[4])));
  float s = s2[0] + s2[1], q = q2[0] + q2[1];
  if (!fin && !__all(s == s)) return false;
  if (LNE && bb >= SB && lane_id() == 0) st4_sc1(bl, bell(bb, rt_now() + DLN + 30));
  s += dppf<0x128>(s); q += dppf<0x128>(q); s += dppf<0x124>(s); q += dppf<0x124>(q);
  s += dppf<0x122>(s); q += dppf<0x122>(q); s += dppf<0x121>(s); q += dppf<0x121>(q);
  asm volatile("s_nop 1\n\tv_permlane16_swap_b32 %0, %1" : "+v"(s), "+v"(q));
  s += q;
  { float x, y; sw32(s, x, y); s = x + y; }
  const float sh = __builtin_bit_cast(float, rdl(__builtin_bit_cast(unsigned, s), 0)) * (1.0f / DM), sq = __builtin_bit_cast(float, rdl(__builtin_bit_cast(unsigned, s), 16));
  const float mb = bfr(pv + sh), vb = bfr(fmaxf(sq * (1.0f / DM) - sh * sh, 0.0f));
  const float rden = __builtin_amdgcn_rcpf(bfr(__builtin_amdgcn_sqrtf(bfr(vb + 1e-5f))));
#pragma unroll
  for (int i = 0; i < 2; ++i) {
    u32x4 r;
#pragma unroll
    for (int e = 0; e < 4; ++e) { const f32x2 t = pkfma(v[4 * i + e] - f32x2{mb, mb}, tc[4 * i + e] * f32x2{rden, rden}, th[4 * i + e]); r[e] = pack2(t[0], t[1]); }
    st16s(h2, ho + i * 32768, r);
  }
  if (!LNE && bb >= SB && lane_id() == 0) st4_sc1(bl, bell(bb, rt_now() + DLN));
  st16s(x1, xo, o[0]); st16s(x1, xo + 1024, o[1]);
  return true;
}
__device__ __forceinline__ void row_table(char* tab, const float* mod, const unsigned* cnt, unsigned* tmo, int tid) {
  wait_l1(cnt + 1, 1u, tmo);
  const rsrc_t rm = mkrs((const char*)mod + OFF_MOD2);
#pragma unroll
  for (int i = 0; i < 2; ++i) {
    const int u = tid + 512 * i, a = u >> 8, c = u & 255;
    f32x4 v = __builtin_bit_cast(f32x4, bld16(rm, (unsigned)u * 16));
    v = f32x4{bfr(v[0]), bfr(v[1]), bfr(v[2]), bfr(v[3])};
    if (a == 2) v = f32x4{bfr(1.0f + v[0]), bfr(1.0f + v[1]), bfr(1.0f + v[2]), bfr(1.0f + v[3])};
    if (a < 3) *(f32x4*)(tab + a * 4096 + ((c >> 7) * 2 + (c & 1)) * 1024 + ((c >> 1) & 63) * 16) = v;
    else *(u32x2*)(tab + 12288 + c * 8) = u32x2{pack2(v[0], v[1]), pack2(v[2], v[3])};
  }
  __syncthreads();
}
__device__ __forceinline__ void row_role(char* smem, int wave_id_, int rbase, int par, const float* mod, const char* P, const u16* x, u16* x1, char* h2c,
                                         unsigned* cnt, unsigned* tmo, int slot, unsigned long long* mts) {
  const int tid = opaque_tid(), lane = tid & 63, wave = WAVE_ID;
  if (tid < 80) *(volatile LAS unsigned*)(smem + 16384 + tid * 4) = 0u;
  {
    static_assert(TLP && SB % 2 == 0, "");
    unsigned z = SENT; asm volatile("" : "+v"(z));
    const int r4 = rbase + 4 * par + (wave & 3);
    for (int bb = SB + (wave >> 2); bb < NSEG; bb += 2)
#pragma unroll
      for (int i = 0; i < 2; ++i) {
        st16s(uni((const char*)x1 + (size_t)bb * 65536), (unsigned)(r4 * DM * 2 + i * 1024 + lane * 16), u32x4{z, z, z, z});
        st16s(uni(h2c + (size_t)bb * 65536), (unsigned)(r4 * 128 + (lane & 7) * 16 + (lane >> 3) * 4096 + i * 32768), u32x4{z, z, z, z});
      }
    WAIT_V(0);
  }
  row_table(smem, mod, cnt, tmo, tid);
  const int rb = rbase + wave + 32 * par;
  const rsrc_t rp = mkrs(P), rr = mkrs(x);
  const unsigned l16 = lane * 16;
  const unsigned pwo = (unsigned)(PWO + (lane & 31)) * 128;
  unsigned* myp = cnt + (PLN + (rb >> 3)) * 32 + wave;
  f32x2 a[2][4];
  const int gp = wave >> 2, r4 = rbase + 4 * par + (wave & 3);
  volatile LAS unsigned* lwb = (volatile LAS unsigned*)(smem + 16384 + gp * 8);
  if (gp && lane == 0) st4_sc1(myp, 1u);
  f32x2 tg[8], th[8], tc[8];
#pragma unroll
  for (int i = 0; i < 4; ++i) {
    const char* mt = smem + lane * 16 + i * 1024;
    const f32x4 g_ = *(const f32x4*)mt, h_ = *(const f32x4*)(mt + 4096), c_ = *(const f32x4*)(mt + 8192);
    tg[2 * i] = f32x2{g_[0], g_[1]}; tg[2 * i + 1] = f32x2{g_[2], g_[3]}; th[2 * i] = f32x2{h_[0], h_[1]}; th[2 * i + 1] = f32x2{h_[2], h_[3]};
    tc[2 * i] = f32x2{c_[0], c_[1]}; tc[2 * i + 1] = f32x2{c_[2], c_[3]};
  }
  const unsigned xo = (unsigned)(r4 * DM * 2 + lane * 16), ho = (unsigned)(r4 * 128 + (((lane & 7) ^ (r4 & 7)) << 4) + (lane >> 3) * 4096);
  const unsigned po = (l16 >> 6) * 2048 + (l16 & 48);
  u32x4 rv[2];
#pragma unroll
  for (int i = 0; i < 2; ++i) rv[i] = __builtin_amdgcn_raw_buffer_load_b128(rr, xo + i * 1024, (unsigned)gp * 65536u, 0);
  unsigned wb = 0;
  for (int bb = gp; bb < NSEG; bb += 2) {
    f32x2 xf[8];
#pragma unroll
    for (int e = 0; e < 8; ++e) xf[e] = f32x2{bflo(rv[e >> 2][e & 3]), bfhi(rv[e >> 2][e & 3])};
    asm volatile("" : "+v"(xf[0]), "+v"(xf[1]), "+v"(xf[2]), "+v"(xf[3]), "+v"(xf[4]), "+v"(xf[5]), "+v"(xf[6]), "+v"(xf[7]));
    if (wb <= (unsigned)bb) {
      if ((wave & 3) == 0) {
        wb = (unsigned)(bb + 1);
        for (int spins = 0;; ++spins) {
          unsigned v, u;
          ld_fb(v, u, cnt, pwo);
          if (__all(v > (unsigned)bb + 2u)) { v = rowmin16(v); wb = min(rdl(v, 0), rdl(v, 16)); break; }
          if (__all(v > (unsigned)bb) || bells(v > (unsigned)bb, u, bb, MARL)) break;
          __builtin_amdgcn_s_sleep(2);
          if (give_up(spins, tmo, 11)) break;
        }
        lwb[0] = wb;
      } else {
        while ((wb = __builtin_amdgcn_readfirstlane(lwb[0])) <= (unsigned)bb) __builtin_amdgcn_s_sleep(1);
      }
    }
    WTM(slot, bb, 0);
    bool fin = bb < SB;
    for (int spins = 0;; ++spins) {
      u32x4 pp[2][4];
#pragma unroll
      for (int i = 0; i < 2; ++i)
#pragma unroll
        for (int q = 0; q < 4; ++q) pp[i][q] = __builtin_amdgcn_raw_buffer_load_b128(rp, po + i * 32768, (unsigned)(bb * 32 * PROW + q * 65536 + r4 * 64), 16);
      sum4(pp[0], a[0]); sum4(pp[1], a[1]);
      if (ln_row(fin, bb, myp + 16, a, xf, tg, th, tc, uni((const char*)x1 + (size_t)bb * 65536), uni(h2c + (size_t)bb * 65536), xo, ho)) break;
      fin = __all(ld4_sc1((const char*)cnt + pwo) > (unsigned)bb) || give_up(spins, tmo, 11);
    }
    WTM(slot, bb, 3);
#pragma unroll
    for (int i = 0; i < 2; ++i) rv[i] = __builtin_amdgcn_raw_buffer_load_b128(rr, xo + i * 1024, (unsigned)min(bb + 2, NSEG - 1) * 65536u, 0);
    WAIT_V(2);
    unsigned o = 0;
    if (lane == 0) o = __hip_atomic_fetch_add((LAS unsigned*)(smem + 16448 + bb * 4), 1u, __ATOMIC_RELAXED, __HIP_MEMORY_SCOPE_WORKGROUP);
    if (__builtin_amdgcn_readfirstlane(o) == 3u) {
      if (lane < 4) st4_sc1(myp - (wave & 3) + lane, (unsigned)(bb + 2));
      WTM(slot, bb, 2);
    }
  }
}
#define CALF 1
#define NCS 6
__device__ __forceinline__ void l1_role(char* smem, int wave_id_, bool fst, int c, const u16* __restrict__ x, const float* __restrict__ mod,
                                        const u16* __restrict__ mem0, char* __restrict__ h1c, unsigned*& cnt, u64 tag, unsigned* tmo, unsigned long long* mts) {
  const int tid = opaque_tid(), lane = tid & 63, wave = WAVE_ID;
  char* ws = (char*)mod - OFF_MOD;
  u32x4 xr[7][2];
  const int row0 = (fst ? 0 : 256) + 8 * c + wave;
  const u16* src = x + (size_t)row0 * DM + lane * 8;
  xr[0][0] = ldnt<2>(src); xr[0][1] = ldnt<2>(src + 512);
  volatile LAS unsigned* sg = (volatile LAS unsigned*)(smem + L_SLOT + 784);
  if (!fst) {
#pragma unroll
    for (int pp = 1; pp < 7; ++pp) { xr[pp][0] = ldnt<2>(src + (size_t)pp * 256 * DM); xr[pp][1] = ldnt<2>(src + (size_t)pp * 256 * DM + 512); }
  } else if (wave >= 5) {
    char* cal = xch_base(ws) + ((size_t)(wave == 5 ? 0 : wave - 3) << 21) + (126 + (c >> 4)) * 16384 + (c & 15) * 512;
    st16_sc1(cal + (lane >> 5) * 8192 + (lane & 31) * 16, u32x4{SENT, SENT, SENT, SENT});
    WAIT_V(0);
    unsigned best[2] = {1u << 30, 1u << 30};
    const char* sb = uni(cal);
#pragma unroll
    for (int r = 1; r < NCS; ++r) {
      unsigned long long t0, t1; unsigned v;
      asm volatile("s_memtime %0\n\ts_waitcnt lgkmcnt(0)\n\ts_load_dword %2, %3, %4 glc\n\ts_waitcnt lgkmcnt(0)\n\ts_memtime %1\n\ts_waitcnt lgkmcnt(0)"
                   : "=&s"(t0), "=&s"(t1), "=&s"(v) : "s"(sb), "i"((r & 1) * 8192 + (r >> 1) * 128) : "memory");
      if (r > 1) best[r & 1] = min(best[r & 1], (unsigned)(t1 - t0));
    }
    unsigned xcc; asm volatile("s_getreg_b32 %0, hwreg(HW_REG_XCC_ID)" : "=s"(xcc));
    const unsigned df = max(best[0], best[1]) - min(best[0], best[1]);
    sg[wave - 5] = df < 110u || df > 400u ? 0u : (((best[1] < best[0]) != ((xcc & 4) != 0)) ? 1u : 8u) << (wave - 5);
  }
  if (CALF) BAR();
  if (!fst || wave >= 5) {
  } else if (wave < 3) {
    const int b = c + 32 * wave;
    if (b < 89) {
      const char* p = uni(xch_base(ws) + (b < 48 ? (size_t)((b >> 4) * 32 + (b & 15)) * 16384
                                        : b < 56 ? ((size_t)3 << 21) + (size_t)(8 + ((b - 48) >> 2) * 8 + (b & 3)) * 16384
                                        : b < 84 ? ((size_t)3 << 21) + (size_t)(b - 56 + 24) * 16384 : ((size_t)4 << 21) + (size_t)(b - 84) * 16384));
      const unsigned v = b < 80 ? SENT : 0u;
#pragma unroll
      for (int i = 0; i < 16; ++i) st16s(p, (unsigned)(i * 1024 + lane * 16), u32x4{v, v, v, v});
    } else if (b == 89 && lane == 0) st4_sc1(tmo, 0u);
    WAIT_V(0);
  } else {
    const int pc = (c & 7) * 2 + (wave & 1), g = lane >> 4, hf = (lane >> 2) & 1, row = ((lane >> 3) & 1) * 4 + (lane & 3);
    u32x4 v = *(const u32x4*)(mem0 + row * DM + 32 * (2 * pc + hf) + 8 * g);
    v.x = nosent(v.x); v.y = nosent(v.y); v.z = nosent(v.z); v.w = nosent(v.w);
    st16_sc1(xch_base(ws) + ((size_t)3 << 21) + (size_t)(((c >> 3) & 1) * 2 + (pc >> 3)) * 16384 + (c >> 4) * 8192 + (pc & 7) * 1024 + lane * 16, v);
    WAIT_V(0);
  }
  BAR();
  if (fst && tid == 0) st8_sc1(ws + OFF_INIT + c * 128, u32x2{(unsigned)tag << 6 | sg[0] | sg[1] | sg[2], (unsigned)(tag >> 26)});
  if (fst && wave == 0) STS(ws, 128 + (c >> 3) * 32 + 24 + (c & 7), 2);
  {
    f32x4 v;
    for (int spins = 0; spins < SPIN_CAP; ++spins) {
      u32x4 m[4];
      asm volatile("s_nop 4\n\tglobal_load_dwordx4 %0, %4, %6 sc1\n\tglobal_load_dwordx4 %1, %4, %6 offset:16 sc1\n\t"
                   "global_load_dwordx4 %2, %5, %6 sc1\n\tglobal_load_dwordx4 %3, %5, %6 offset:16 sc1\n\ts_waitcnt vmcnt(0)"
                   : "=&v"(m[0]), "=&v"(m[1]), "=&v"(m[2]), "=&v"(m[3]) : "v"((unsigned)tid * 32), "v"((unsigned)tid * 32 + 16384), "s"(mod) : "memory");
      bool ok = true;
#pragma unroll
      for (int i = 0; i < 4; ++i) ok = ok && m[i].y == (unsigned)tag && m[i].w == tag2(tag);
      v = f32x4{__uint_as_float(m[0].x) + __uint_as_float(m[2].x), __uint_as_float(m[0].z) + __uint_as_float(m[2].z),
                __uint_as_float(m[1].x) + __uint_as_float(m[3].x), __uint_as_float(m[1].z) + __uint_as_float(m[3].z)};
      if (__all(ok)) break;
      __builtin_amdgcn_s_sleep(4);
    }
    v = f32x4{bfr(v[0]), bfr(v[1]), bfr(v[2]), bfr(v[3])};
    const int a = tid >> 8, cc = tid & 255;
    if (a) v = f32x4{bfr(1.0f + v[0]), bfr(1.0f + v[1]), bfr(1.0f + v[2]), bfr(1.0f + v[3])};
    *(f32x4*)(smem + 4096 + a * 4096 + ((cc >> 7) * 2 + (cc & 1)) * 1024 + ((cc >> 1) & 63) * 16) = v;
  }
  BAR();
  if (fst && wave == 0) STS(ws, 128 + (c >> 3) * 32 + 24 + (c & 7), 3);
#pragma unroll
  for (int p = 0; p < 7; ++p) {
    if (p > 0 && fst) break;
    f32x2 v[2][4];
#pragma unroll
    for (int i = 0; i < 2; ++i)
#pragma unroll
      for (int e = 0; e < 4; ++e) v[i][e] = f32x2{bflo(xr[p][i][e]), bfhi(xr[p][i][e])};
    const f32x2 s2 = ((v[0][0] + v[0][1]) + (v[0][2] + v[0][3])) + ((v[1][0] + v[1][1]) + (v[1][2] + v[1][3]));
    const float mean = wavesum(s2[0] + s2[1]) * (1.0f / DM);
    f32x2 q2 = f32x2{0, 0};
#pragma unroll
    for (int i = 0; i < 8; ++i) { const f32x2 d = v[i >> 2][i & 3] - f32x2{mean, mean}; q2 = pkfma(d, d, q2); }
    const float q = wavesum(q2[0] + q2[1]);
    if (p == 0 && fst && wave == 0) STS((char*)tmo - OFF_TMO, 128 + (c >> 3) * 32 + 24 + (c & 7), 5);
    const float mb = bfr(mean), vb = bfr(q * (1.0f / DM));
    const float rden = 1.0f / bfr(sqrtf(bfr(vb + 1e-5f)));
    const int row = 256 * p + row0, rb = row & 31;
    char* dst = h1c + (size_t)(row >> 5) * 65536 + (lane >> 3) * 4096 + rb * 128 + (((lane & 7) ^ (rb & 7)) << 4);
    const char* ml = smem + lane * 16;
#pragma unroll
    for (int i = 0; i < 2; ++i) st16_sc1(dst + i * 32768, norm8(v[i], ml + i * 2048, mb, rden));
  }
  cnt = cnt_of(ws, init_wait(ws, tag));
  WAIT_V(0);
  __builtin_amdgcn_s_barrier();
  if (tid == 0) st4_sc1(cnt + ((fst ? PL1 : PL1B) + c) * 32, fst ? 8u : 7u);
  if (wave == 0) STS(ws, 128 + (c >> 3) * 32 + 24 + (c & 7), fst ? 6 : 7);
}
__device__ __forceinline__ void mod2_job(char* smem, int wave_id_, int c, const float* __restrict__ mod, const u16* __restrict__ cvec, const u16* __restrict__ wmod, unsigned* cnt) {
  const int tid = opaque_tid();
  {
    const float sva = bf2f(cvec[tid]), svb = bf2f(cvec[tid + 512]);
    float* sv = (float*)(smem + 12288);
    sv[tid] = bfr(sva / (1.0f + __expf(-sva))); sv[tid + 512] = bfr(svb / (1.0f + __expf(-svb)));
  }
  __syncthreads();
  {
    const int cg = tid & 15, r0 = tid >> 4;
    const u16* wp = wmod + (size_t)r0 * (6 * DM) + 2 * DM + 128 * c + cg * 8;
    const float* sv = (const float*)(smem + 12288) + r0;
    f32x2 acc[4] = {f32x2{0, 0}, f32x2{0, 0}, f32x2{0, 0}, f32x2{0, 0}};
#pragma unroll 1
    for (int o = 0; o < 4; ++o) {
      u32x4 w[8];
#pragma unroll
      for (int i = 0; i < 8; ++i) w[i] = ldnt<1>(wp + (size_t)(o * 8 + i) * 32 * (6 * DM));
#pragma unroll
      for (int i = 0; i < 8; ++i) {
        const float s = sv[(o * 8 + i) * 32];
#pragma unroll
        for (int e = 0; e < 4; ++e) acc[e] = pkfma(f32x2{s, s}, f32x2{bflo(w[i][e]), bfhi(w[i][e])}, acc[e]);
      }
    }
    float* red = (float*)(smem + 16384);
    *(f32x4*)(red + r0 * 128 + cg * 8) = f32x4{acc[0][0], acc[0][1], acc[1][0], acc[1][1]};
    *(f32x4*)(red + r0 * 128 + cg * 8 + 4) = f32x4{acc[2][0], acc[2][1], acc[3][0], acc[3][1]};
    __syncthreads();
    if (tid < 128) {
      float s = 0;
#pragma unroll
      for (int r = 0; r < 32; ++r) s += red[r * 128 + tid];
      st4_sc1((char*)mod + OFF_MOD2 + (128 * c + tid) * 4, __builtin_bit_cast(unsigned, s));
    }
    WAIT_V(0);
    __builtin_amdgcn_s_barrier();
    if (tid == 0) st4_sc1(cnt + (PL1 + c) * 32 + 1, 1u);
  }
}
enum { R_QKV = 0, R_PART = 1, R_F1 = 2, R_PART2 = 3 };
struct Dep {
  const char* base; unsigned voff; unsigned mul, add;
};
__device__ __forceinline__ unsigned dep_eval(unsigned v, const Dep& d) {
  v = rowmin16(v * d.mul + (d.add + 64u));
  return min(min(rdl(v, 0), rdl(v, 16)), min(rdl(v, 32), rdl(v, 48))) - 64u;
}
typedef unsigned u32x32 __attribute__((ext_vector_type(32)));
typedef unsigned u32x16 __attribute__((ext_vector_type(16)));
__device__ __forceinline__ void fast_ld0(const u32x32 (&W)[4], u32x16& pd, unsigned ea, unsigned da, int& b, int& lf, int cp, unsigned up, unsigned sa, unsigned mu, unsigned ad,
    unsigned rw, unsigned ab, const char* p1, const char* p0, unsigned l1, unsigned l0, const unsigned (&av)[8], int la) {
  asm volatile("s_setprio 0\n\t"
    "s_cmp_eq_u32 %[la], 3\n\t"
    "s_waitcnt vmcnt(12)\n\t"
    "s_cbranch_scc1 20f\n\t"
    "s_waitcnt vmcnt(4)\n\t"
    "20:\n\t"
    "s_barrier\n\t"
    "ds_read_b128 v[96:99], %[ea] offset:0\n\t"
    "ds_read_b128 v[100:103], %[ea] offset:2048\n\t"
    "ds_read_b128 v[104:107], %[da] offset:0\n\t"
    "ds_read_b128 v[108:111], %[da] offset:2048\n\t"
    "ds_read_b128 v[112:115], %[ea] offset:4096\n\t"
    "ds_read_b128 v[116:119], %[ea] offset:6144\n\t"
    "s_branch 2f\n\t"
    ".p2align 6\n\t"
    "1:\n\t"
    "s_cmp_eq_u32 %[la], 3\n\t"
    "s_waitcnt vmcnt(12)\n\t"
    "s_cbranch_scc1 21f\n\t"
    "s_waitcnt vmcnt(4)\n\t"
    "21:\n\t"
    "s_barrier\n\t"
    "2:\n\t"
    "s_waitcnt lgkmcnt(4)\n\t"
    "v_mfma_f32_16x16x32_bf16 v[64:67], v[128:131], v[96:99], 0\n\t"
    "ds_read_b128 v[120:123], %[da] offset:4096\n\t"
    "s_add_u32 m0, %[l1], 0\n\t"
    "ds_write_b128 %[rw], v[80:83] offset:0\n\t"
    "v_mfma_f32_16x16x32_bf16 v[68:71], v[132:135], v[96:99], 0\n\t"
    "ds_read_b128 v[124:127], %[da] offset:6144\n\t"
    "global_load_lds_dwordx4 %[av0], s[36:37] sc1\n\t"
    "ds_write_b128 %[rw], v[84:87] offset:1024\n\t"
    "v_mfma_f32_16x16x32_bf16 v[72:75], v[128:131], v[100:103], 0\n\t"
    "s_add_u32 m0, %[l1], 32768\n\t"
    "ds_write_b128 %[rw], v[88:91] offset:2048\n\t"
    "v_mfma_f32_16x16x32_bf16 v[76:79], v[132:135], v[100:103], 0\n\t"
    "global_load_lds_dwordx4 %[av1], s[36:37] sc1\n\t"
    "ds_write_b128 %[rw], v[92:95] offset:3072\n\t"
    "s_waitcnt lgkmcnt(8)\n\t"
    "v_mfma_f32_16x16x32_bf16 v[64:67], v[136:139], v[104:107], v[64:67]\n\t"
    "ds_read_b128 v[96:99], %[ea] offset:8192\n\t"
    "s_add_u32 m0, %[l1], 4096\n\t"
    "v_mfma_f32_16x16x32_bf16 v[68:71], v[140:143], v[104:107], v[68:71]\n\t"
    "ds_read_b128 v[100:103], %[ea] offset:10240\n\t"
    "global_load_lds_dwordx4 %[av2], s[36:37] sc1\n\t"
    "v_mfma_f32_16x16x32_bf16 v[72:75], v[136:139], v[108:111], v[72:75]\n\t"
    "s_add_u32 m0, %[l1], 36864\n\t"
    "v_mfma_f32_16x16x32_bf16 v[76:79], v[140:143], v[108:111], v[76:79]\n\t"
    "global_load_lds_dwordx4 %[av3], s[36:37] sc1\n\t"
    "s_waitcnt lgkmcnt(8)\n\t"
    "v_mfma_f32_16x16x32_bf16 v[64:67], v[144:147], v[112:115], v[64:67]\n\t"
    "ds_read_b128 v[104:107], %[da] offset:8192\n\t"
    "s_add_u32 m0, %[l1], 8192\n\t"
    "v_mfma_f32_16x16x32_bf16 v[68:71], v[148:151], v[112:115], v[68:71]\n\t"
    "ds_read_b128 v[108:111], %[da] offset:10240\n\t"
    "global_load_lds_dwordx4 %[av4], s[36:37] sc1\n\t"
    "v_mfma_f32_16x16x32_bf16 v[72:75], v[144:147], v[116:119], v[72:75]\n\t"
    "s_add_u32 m0, %[l1], 40960\n\t"
    "v_mfma_f32_16x16x32_bf16 v[76:79], v[148:151], v[116:119], v[76:79]\n\t"
    "global_load_lds_dwordx4 %[av5], s[36:37] sc1\n\t"
    "s_waitcnt lgkmcnt(7)\n\t"
    "v_mfma_f32_16x16x32_bf16 v[64:67], v[152:155], v[120:123], v[64:67]\n\t"
    "ds_read_b128 v[112:115], %[ea] offset:12288\n\t"
    "s_add_u32 m0, %[l1], 12288\n\t"
    "v_mfma_f32_16x16x32_bf16 v[68:71], v[156:159], v[120:123], v[68:71]\n\t"
    "ds_read_b128 v[116:119], %[ea] offset:14336\n\t"
    "global_load_lds_dwordx4 %[av6], s[36:37] sc1\n\t"
    "v_mfma_f32_16x16x32_bf16 v[72:75], v[152:155], v[124:127], v[72:75]\n\t"
    "s_add_u32 m0, %[l1], 45056\n\t"
    "v_mfma_f32_16x16x32_bf16 v[76:79], v[156:159], v[124:127], v[76:79]\n\t"
    "global_load_lds_dwordx4 %[av7], s[36:37] sc1\n\t"
    "s_waitcnt lgkmcnt(4)\n\t"
    "v_mfma_f32_16x16x32_bf16 v[64:67], v[160:163], v[96:99], v[64:67]\n\t"
    "ds_read_b128 v[120:123], %[da] offset:12288\n\t"
    "v_mfma_f32_16x16x32_bf16 v[68:71], v[164:167], v[96:99], v[68:71]\n\t"
    "ds_read_b128 v[124:127], %[da] offset:14336\n\t"
    "v_mfma_f32_16x16x32_bf16 v[72:75], v[160:163], v[100:103], v[72:75]\n\t"
    "v_mfma_f32_16x16x32_bf16 v[76:79], v[164:167], v[100:103], v[76:79]\n\t"
    "s_waitcnt lgkmcnt(4)\n\t"
    "v_mfma_f32_16x16x32_bf16 v[64:67], v[168:171], v[104:107], v[64:67]\n\t"
    "ds_read_b128 v[96:99], %[ea] offset:16384\n\t"
    "v_mfma_f32_16x16x32_bf16 v[68:71], v[172:175], v[104:107], v[68:71]\n\t"
    "ds_read_b128 v[100:103], %[ea] offset:18432\n\t"
    "v_mfma_f32_16x16x32_bf16 v[72:75], v[168:171], v[108:111], v[72:75]\n\t"
    "v_mfma_f32_16x16x32_bf16 v[76:79], v[172:175], v[108:111], v[76:79]\n\t"
    "s_waitcnt lgkmcnt(4)\n\t"
    "v_mfma_f32_16x16x32_bf16 v[64:67], v[176:179], v[112:115], v[64:67]\n\t"
    "ds_read_b128 v[104:107], %[da] offset:16384\n\t"
    "v_mfma_f32_16x16x32_bf16 v[68:71], v[180:183], v[112:115], v[68:71]\n\t"
    "ds_read_b128 v[108:111], %[da] offset:18432\n\t"
    "v_mfma_f32_16x16x32_bf16 v[72:75], v[176:179], v[116:119], v[72:75]\n\t"
    "v_mfma_f32_16x16x32_bf16 v[76:79], v[180:183], v[116:119], v[76:79]\n\t"
    "s_waitcnt lgkmcnt(4)\n\t"
    "v_mfma_f32_16x16x32_bf16 v[64:67], v[184:187], v[120:123], v[64:67]\n\t"
    "ds_read_b128 v[112:115], %[ea] offset:20480\n\t"
    "v_mfma_f32_16x16x32_bf16 v[68:71], v[188:191], v[120:123], v[68:71]\n\t"
    "ds_read_b128 v[116:119], %[ea] offset:22528\n\t"
    "v_mfma_f32_16x16x32_bf16 v[72:75], v[184:187], v[124:127], v[72:75]\n\t"
    "v_mfma_f32_16x16x32_bf16 v[76:79], v[188:191], v[124:127], v[76:79]\n\t"
    "s_cmp_eq_u32 %[la], 3\n\t"
    "s_waitcnt vmcnt(12)\n\t"
    "s_cbranch_scc1 22f\n\t"
    "s_waitcnt vmcnt(4)\n\t"
    "22:\n\t"
    "s_barrier\n\t"
    "s_waitcnt lgkmcnt(4)\n\t"
    "v_mfma_f32_16x16x32_bf16 v[64:67], v[192:195], v[96:99], v[64:67]\n\t"
    "ds_read_b128 v[120:123], %[da] offset:20480\n\t"
    "s_add_u32 m0, %[l0], 0\n\t"
    "v_mfma_f32_16x16x32_bf16 v[68:71], v[196:199], v[96:99], v[68:71]\n\t"
    "ds_read_b128 v[124:127], %[da] offset:22528\n\t"
    "global_load_lds_dwordx4 %[av0], s[38:39] sc1\n\t"
    "v_mfma_f32_16x16x32_bf16 v[72:75], v[192:195], v[100:103], v[72:75]\n\t"
    "s_add_u32 m0, %[l0], 32768\n\t"
    "v_mfma_f32_16x16x32_bf16 v[76:79], v[196:199], v[100:103], v[76:79]\n\t"
    "global_load_lds_dwordx4 %[av1], s[38:39] sc1\n\t"
    "s_waitcnt lgkmcnt(4)\n\t"
    "v_mfma_f32_16x16x32_bf16 v[64:67], v[200:203], v[104:107], v[64:67]\n\t"
    "ds_read_b128 v[96:99], %[ea] offset:24576\n\t"
    "s_add_u32 m0, %[l0], 4096\n\t"
    "v_mfma_f32_16x16x32_bf16 v[68:71], v[204:207], v[104:107], v[68:71]\n\t"
    "ds_read_b128 v[100:103], %[ea] offset:26624\n\t"
    "global_load_lds_dwordx4 %[av2], s[38:39] sc1\n\t"
    "v_mfma_f32_16x16x32_bf16 v[72:75], v[200:203], v[108:111], v[72:75]\n\t"
    "s_add_u32 m0, %[l0], 36864\n\t"
    "v_mfma_f32_16x16x32_bf16 v[76:79], v[204:207], v[108:111], v[76:79]\n\t"
    "global_load_lds_dwordx4 %[av3], s[38:39] sc1\n\t"
    "s_add_u32 s36, s36, %[ab]\n\t"
    "s_addc_u32 s37, s37, 0\n\t"
    "s_waitcnt lgkmcnt(4)\n\t"
    "v_mfma_f32_16x16x32_bf16 v[64:67], v[208:211], v[112:115], v[64:67]\n\t"
    "ds_read_b128 v[104:107], %[da] offset:24576\n\t"
    "s_add_u32 m0, %[l0], 8192\n\t"
    "v_mfma_f32_16x16x32_bf16 v[68:71], v[212:215], v[112:115], v[68:71]\n\t"
    "ds_read_b128 v[108:111], %[da] offset:26624\n\t"
    "global_load_lds_dwordx4 %[av4], s[38:39] sc1\n\t"
    "v_mfma_f32_16x16x32_bf16 v[72:75], v[208:211], v[116:119], v[72:75]\n\t"
    "ds_read_b32 v39, %[sa]\n\t"
    "s_add_u32 m0, %[l0], 40960\n\t"
    "v_mfma_f32_16x16x32_bf16 v[76:79], v[212:215], v[116:119], v[76:79]\n\t"
    "global_load_lds_dwordx4 %[av5], s[38:39] sc1\n\t"
    "s_xor_b32 %[l1], %[l1], 0x10000\n\t"
    "s_waitcnt lgkmcnt(5)\n\t"
    "v_mfma_f32_16x16x32_bf16 v[64:67], v[216:219], v[120:123], v[64:67]\n\t"
    "ds_read_b128 v[112:115], %[ea] offset:28672\n\t"
    "s_add_u32 m0, %[l0], 12288\n\t"
    "v_mfma_f32_16x16x32_bf16 v[68:71], v[220:223], v[120:123], v[68:71]\n\t"
    "ds_read_b128 v[116:119], %[ea] offset:30720\n\t"
    "global_load_lds_dwordx4 %[av6], s[38:39] sc1\n\t"
    "v_mfma_f32_16x16x32_bf16 v[72:75], v[216:219], v[124:127], v[72:75]\n\t"
    "v_xor_b32 %[ea], 0x10000, %[ea]\n\t"
    "s_add_u32 m0, %[l0], 45056\n\t"
    "v_mfma_f32_16x16x32_bf16 v[76:79], v[220:223], v[124:127], v[76:79]\n\t"
    "global_load_lds_dwordx4 %[av7], s[38:39] sc1\n\t"
    "s_waitcnt lgkmcnt(5)\n\t"
    "v_mfma_f32_16x16x32_bf16 v[64:67], v[224:227], v[96:99], v[64:67]\n\t"
    "ds_read_b128 v[120:123], %[da] offset:28672\n\t"
    "v_mfma_f32_16x16x32_bf16 v[68:71], v[228:231], v[96:99], v[68:71]\n\t"
    "ds_read_b128 v[124:127], %[da] offset:30720\n\t"
    "v_mfma_f32_16x16x32_bf16 v[72:75], v[224:227], v[100:103], v[72:75]\n\t"
    "v_xor_b32 %[da], 0x10000, %[da]\n\t"
    "v_mfma_f32_16x16x32_bf16 v[76:79], v[228:231], v[100:103], v[76:79]\n\t"
    "s_waitcnt lgkmcnt(5)\n\t"
    "v_mfma_f32_16x16x32_bf16 v[64:67], v[232:235], v[104:107], v[64:67]\n\t"
    "ds_read_b128 v[96:99], %[ea] offset:0\n\t"
    "v_mfma_f32_16x16x32_bf16 v[68:71], v[236:239], v[104:107], v[68:71]\n\t"
    "ds_read_b128 v[100:103], %[ea] offset:2048\n\t"
    "v_mfma_f32_16x16x32_bf16 v[72:75], v[232:235], v[108:111], v[72:75]\n\t"
    "v_mfma_f32_16x16x32_bf16 v[76:79], v[236:239], v[108:111], v[76:79]\n\t"
    "s_waitcnt lgkmcnt(4)\n\t"
    "v_mfma_f32_16x16x32_bf16 v[64:67], v[240:243], v[112:115], v[64:67]\n\t"
    "ds_read_b128 v[104:107], %[da] offset:0\n\t"
    "v_mad_u32_u24 v39, v39, %[mu], %[ad]\n\t"
    "v_mfma_f32_16x16x32_bf16 v[68:71], v[244:247], v[112:115], v[68:71]\n\t"
    "ds_read_b128 v[108:111], %[da] offset:2048\n\t"
    "v_cmp_ge_i32 vcc, %[lf], v39\n\t"
    "s_cmp_eq_u64 vcc, 0\n\t"
    "s_addc_u32 %[lf], %[lf], 0\n\t"
    "v_mfma_f32_16x16x32_bf16 v[72:75], v[240:243], v[116:119], v[72:75]\n\t"
    "v_mfma_f32_16x16x32_bf16 v[76:79], v[244:247], v[116:119], v[76:79]\n\t"
    "v_cmp_ge_i32 vcc, %[lf], v39\n\t"
    "s_cmp_eq_u64 vcc, 0\n\t"
    "s_addc_u32 %[lf], %[lf], 0\n\t"
    "s_waitcnt lgkmcnt(4)\n\t"
    "v_mfma_f32_16x16x32_bf16 v[80:83], v[248:251], v[120:123], v[64:67]\n\t"
    "ds_read_b128 v[112:115], %[ea] offset:4096\n\t"
    "s_min_i32 %[lf], %[lf], %[cp]\n\t"
    "s_add_u32 %[b], %[b], 1\n\t"
    "v_mfma_f32_16x16x32_bf16 v[84:87], v[252:255], v[120:123], v[68:71]\n\t"
    "ds_read_b128 v[116:119], %[ea] offset:6144\n\t"
    "v_mfma_f32_16x16x32_bf16 v[88:91], v[248:251], v[124:127], v[72:75]\n\t"
    "v_mfma_f32_16x16x32_bf16 v[92:95], v[252:255], v[124:127], v[76:79]\n\t"
    "s_add_u32 s38, s38, %[ab]\n\t"
    "s_addc_u32 s39, s39, 0\n\t"
    "s_xor_b32 %[l0], %[l0], 0x10000\n\t"
    "s_sub_u32 m0, %[lf], %[b]\n\t"
    "s_sub_u32 m0, m0, 1\n\t"
    "s_cmp_lt_u32 m0, %[up]\n\t"
    "s_cbranch_scc1 1b\n\t"
    "s_waitcnt lgkmcnt(0)\n\t"
    "s_nop 4"
    : "=&{v[80:95]}"(pd), [ea] "+&v"(ea), [da] "+&v"(da), [b] "+&s"(b), [lf] "+&s"(lf), "+{s[36:37]}"(p1), "+{s[38:39]}"(p0), [sa] "+&v"(sa), [l1] "+&s"(l1), [l0] "+&s"(l0)
    : [rw] "v"(rw), [ab] "s"(ab), [cp] "s"(cp), [mu] "s"(mu), [ad] "v"(ad), [up] "s"(up), "{v[128:159]}"(W[0]), "{v[160:191]}"(W[1]), "{v[192:223]}"(W[2]), "{v[224:255]}"(W[3]), [av0] "v"(av[0]), [av1] "v"(av[1]), [av2] "v"(av[2]), [av3] "v"(av[3]), [av4] "v"(av[4]), [av5] "v"(av[5]), [av6] "v"(av[6]), [av7] "v"(av[7]), [la] "s"(la)
    : "v39", "v64", "v65", "v66", "v67", "v68", "v69", "v70", "v71", "v72", "v73", "v74", "v75", "v76", "v77", "v78", "v79", "v96", "v97", "v98", "v99", "v100", "v101", "v102", "v103", "v104", "v105", "v106", "v107", "v108", "v109", "v110", "v111", "v112", "v113", "v114", "v115", "v116", "v117", "v118", "v119", "v120", "v121", "v122", "v123", "v124", "v125", "v126", "v127", "vcc", "scc", "m0", "memory");
}
__device__ __forceinline__ void fast_ld1(const u32x32 (&W)[4], u32x16& pd, unsigned ea, unsigned da, int& b, int& lf, int cp, unsigned up, unsigned sa, unsigned mu, unsigned ad,
    unsigned rw, unsigned ab, const char* p1, const char* p0, unsigned l1, unsigned l0, const unsigned (&av)[8], int la) {
  asm volatile("s_setprio 0\n\t"
    "s_cmp_eq_u32 %[la], 3\n\t"
    "s_waitcnt vmcnt(12)\n\t"
    "s_cbranch_scc1 20f\n\t"
    "s_waitcnt vmcnt(4)\n\t"
    "20:\n\t"
    "s_barrier\n\t"
    "ds_read_b128 v[96:99], %[ea] offset:0\n\t"
    "ds_read_b128 v[100:103], %[ea] offset:2048\n\t"
    "ds_read_b128 v[104:107], %[da] offset:0\n\t"
    "ds_read_b128 v[108:111], %[da] offset:2048\n\t"
    "ds_read_b128 v[112:115], %[ea] offset:4096\n\t"
    "ds_read_b128 v[116:119], %[ea] offset:6144\n\t"
    "s_branch 2f\n\t"
    ".p2align 6\n\t"
    "1:\n\t"
    "s_cmp_eq_u32 %[la], 3\n\t"
    "s_waitcnt vmcnt(12)\n\t"
    "s_cbranch_scc1 21f\n\t"
    "s_waitcnt vmcnt(4)\n\t"
    "21:\n\t"
    "s_barrier\n\t"
    "2:\n\t"
    "s_waitcnt lgkmcnt(4)\n\t"
    "v_mfma_f32_16x16x32_bf16 v[64:67], v[128:131], v[96:99], 0\n\t"
    "ds_read_b128 v[120:123], %[da] offset:4096\n\t"
    "s_add_u32 m0, %[l1], 0\n\t"
    "ds_write_b128 %[rw], v[80:83] offset:0\n\t"
    "v_mfma_f32_16x16x32_bf16 v[68:71], v[132:135], v[96:99], 0\n\t"
    "ds_read_b128 v[124:127], %[da] offset:6144\n\t"
    "global_load_lds_dwordx4 %[av0], s[36:37] sc1\n\t"
    "ds_write_b128 %[rw], v[84:87] offset:1024\n\t"
    "v_mfma_f32_16x16x32_bf16 v[72:75], v[128:131], v[100:103], 0\n\t"
    "s_add_u32 m0, %[l1], 32768\n\t"
    "ds_write_b128 %[rw], v[88:91] offset:2048\n\t"
    "v_mfma_f32_16x16x32_bf16 v[76:79], v[132:135], v[100:103], 0\n\t"
    "global_load_lds_dwordx4 %[av1], s[36:37] sc1\n\t"
    "ds_write_b128 %[rw], v[92:95] offset:3072\n\t"
    "s_waitcnt lgkmcnt(8)\n\t"
    "v_mfma_f32_16x16x32_bf16 v[64:67], v[136:139], v[104:107], v[64:67]\n\t"
    "ds_read_b128 v[96:99], %[ea] offset:8192\n\t"
    "s_add_u32 m0, %[l1], 4096\n\t"
    "v_mfma_f32_16x16x32_bf16 v[68:71], v[140:143], v[104:107], v[68:71]\n\t"
    "ds_read_b128 v[100:103], %[ea] offset:10240\n\t"
    "global_load_lds_dwordx4 %[av2], s[36:37] sc1\n\t"
    "v_mfma_f32_16x16x32_bf16 v[72:75], v[136:139], v[108:111], v[72:75]\n\t"
    "s_add_u32 m0, %[l1], 36864\n\t"
    "v_mfma_f32_16x16x32_bf16 v[76:79], v[140:143], v[108:111], v[76:79]\n\t"
    "global_load_lds_dwordx4 %[av3], s[36:37] sc1\n\t"
    "s_waitcnt lgkmcnt(8)\n\t"
    "v_mfma_f32_16x16x32_bf16 v[64:67], v[144:147], v[112:115], v[64:67]\n\t"
    "ds_read_b128 v[104:107], %[da] offset:8192\n\t"
    "s_add_u32 m0, %[l1], 8192\n\t"
    "v_mfma_f32_16x16x32_bf16 v[68:71], v[148:151], v[112:115], v[68:71]\n\t"
    "ds_read_b128 v[108:111], %[da] offset:10240\n\t"
    "global_load_lds_dwordx4 %[av4], s[36:37] sc1\n\t"
    "v_mfma_f32_16x16x32_bf16 v[72:75], v[144:147], v[116:119], v[72:75]\n\t"
    "s_add_u32 m0, %[l1], 40960\n\t"
    "v_mfma_f32_16x16x32_bf16 v[76:79], v[148:151], v[116:119], v[76:79]\n\t"
    "global_load_lds_dwordx4 %[av5], s[36:37] sc1\n\t"
    "s_waitcnt lgkmcnt(7)\n\t"
    "v_mfma_f32_16x16x32_bf16 v[64:67], v[152:155], v[120:123], v[64:67]\n\t"
    "ds_read_b128 v[112:115], %[ea] offset:12288\n\t"
    "s_add_u32 m0, %[l1], 12288\n\t"
    "v_mfma_f32_16x16x32_bf16 v[68:71], v[156:159], v[120:123], v[68:71]\n\t"
    "ds_read_b128 v[116:119], %[ea] offset:14336\n\t"
    "global_load_lds_dwordx4 %[av6], s[36:37] sc1\n\t"
    "v_mfma_f32_16x16x32_bf16 v[72:75], v[152:155], v[124:127], v[72:75]\n\t"
    "s_add_u32 m0, %[l1], 45056\n\t"
    "v_mfma_f32_16x16x32_bf16 v[76:79], v[156:159], v[124:127], v[76:79]\n\t"
    "global_load_lds_dwordx4 %[av7], s[36:37] sc1\n\t"
    "s_waitcnt lgkmcnt(4)\n\t"
    "v_mfma_f32_16x16x32_bf16 v[64:67], v[160:163], v[96:99], v[64:67]\n\t"
    "ds_read_b128 v[120:123], %[da] offset:12288\n\t"
    "v_mfma_f32_16x16x32_bf16 v[68:71], v[164:167], v[96:99], v[68:71]\n\t"
    "ds_read_b128 v[124:127], %[da] offset:14336\n\t"
    "v_mfma_f32_16x16x32_bf16 v[72:75], v[160:163], v[100:103], v[72:75]\n\t"
    "v_mfma_f32_16x16x32_bf16 v[76:79], v[164:167], v[100:103], v[76:79]\n\t"
    "s_waitcnt lgkmcnt(4)\n\t"
    "v_mfma_f32_16x16x32_bf16 v[64:67], v[168:171], v[104:107], v[64:67]\n\t"
    "ds_read_b128 v[96:99], %[ea] offset:16384\n\t"
    "v_mfma_f32_16x16x32_bf16 v[68:71], v[172:175], v[104:107], v[68:71]\n\t"
    "ds_read_b128 v[100:103], %[ea] offset:18432\n\t"
    "v_mfma_f32_16x16x32_bf16 v[72:75], v[168:171], v[108:111], v[72:75]\n\t"
    "v_mfma_f32_16x16x32_bf16 v[76:79], v[172:175], v[108:111], v[76:79]\n\t"
    "s_waitcnt lgkmcnt(4)\n\t"
    "v_mfma_f32_16x16x32_bf16 v[64:67], v[176:179], v[112:115], v[64:67]\n\t"
    "ds_read_b128 v[104:107], %[da] offset:16384\n\t"
    "v_mfma_f32_16x16x32_bf16 v[68:71], v[180:183], v[112:115], v[68:71]\n\t"
    "ds_read_b128 v[108:111], %[da] offset:18432\n\t"
    "v_mfma_f32_16x16x32_bf16 v[72:75], v[176:179], v[116:119], v[72:75]\n\t"
    "v_mfma_f32_16x16x32_bf16 v[76:79], v[180:183], v[116:119], v[76:79]\n\t"
    "s_waitcnt lgkmcnt(4)\n\t"
    "v_mfma_f32_16x16x32_bf16 v[64:67], v[184:187], v[120:123], v[64:67]\n\t"
    "ds_read_b128 v[112:115], %[ea] offset:20480\n\t"
    "v_mfma_f32_16x16x32_bf16 v[68:71], v[188:191], v[120:123], v[68:71]\n\t"
    "ds_read_b128 v[116:119], %[ea] offset:22528\n\t"
    "v_mfma_f32_16x16x32_bf16 v[72:75], v[184:187], v[124:127], v[72:75]\n\t"
    "v_mfma_f32_16x16x32_bf16 v[76:79], v[188:191], v[124:127], v[76:79]\n\t"
    "s_cmp_eq_u32 %[la], 3\n\t"
    "s_waitcnt vmcnt(12)\n\t"
    "s_cbranch_scc1 22f\n\t"
    "s_waitcnt vmcnt(4)\n\t"
    "22:\n\t"
    "s_barrier\n\t"
    "s_waitcnt lgkmcnt(4)\n\t"
    "v_mfma_f32_16x16x32_bf16 v[64:67], v[192:195], v[96:99], v[64:67]\n\t"
    "ds_read_b128 v[120:123], %[da] offset:20480\n\t"
    "s_add_u32 m0, %[l0], 0\n\t"
    "v_mfma_f32_16x16x32_bf16 v[68:71], v[196:199], v[96:99], v[68:71]\n\t"
    "ds_read_b128 v[124:127], %[da] offset:22528\n\t"
    "global_load_lds_dwordx4 %[av0], s[38:39] sc1\n\t"
    "v_mfma_f32_16x16x32_bf16 v[72:75], v[192:195], v[100:103], v[72:75]\n\t"
    "s_add_u32 m0, %[l0], 32768\n\t"
    "v_mfma_f32_16x16x32_bf16 v[76:79], v[196:199], v[100:103], v[76:79]\n\t"
    "global_load_lds_dwordx4 %[av1], s[38:39] sc1\n\t"
    "s_waitcnt lgkmcnt(4)\n\t"
    "v_mfma_f32_16x16x32_bf16 v[64:67], v[200:203], v[104:107], v[64:67]\n\t"
    "ds_read_b128 v[96:99], %[ea] offset:24576\n\t"
    "s_add_u32 m0, %[l0], 4096\n\t"
    "v_mfma_f32_16x16x32_bf16 v[68:71], v[204:207], v[104:107], v[68:71]\n\t"
    "ds_read_b128 v[100:103], %[ea] offset:26624\n\t"
    "global_load_lds_dwordx4 %[av2], s[38:39] sc1\n\t"
    "v_mfma_f32_16x16x32_bf16 v[72:75], v[200:203], v[108:111], v[72:75]\n\t"
    "s_add_u32 m0, %[l0], 36864\n\t"
    "v_mfma_f32_16x16x32_bf16 v[76:79], v[204:207], v[108:111], v[76:79]\n\t"
    "global_load_lds_dwordx4 %[av3], s[38:39] sc1\n\t"
    "s_add_u32 s36, s36, %[ab]\n\t"
    "s_addc_u32 s37, s37, 0\n\t"
    "s_waitcnt lgkmcnt(4)\n\t"
    "v_mfma_f32_16x16x32_bf16 v[64:67], v[208:211], v[112:115], v[64:67]\n\t"
    "ds_read_b128 v[104:107], %[da] offset:24576\n\t"
    "s_add_u32 m0, %[l0], 8192\n\t"
    "v_mfma_f32_16x16x32_bf16 v[68:71], v[212:215], v[112:115], v[68:71]\n\t"
    "ds_read_b128 v[108:111], %[da] offset:26624\n\t"
    "global_load_lds_dwordx4 %[av4], s[38:39] sc1\n\t"
    "v_mfma_f32_16x16x32_bf16 v[72:75], v[208:211], v[116:119], v[72:75]\n\t"
    "ds_read_b32 v39, %[sa]\n\t"
    "s_add_u32 m0, %[l0], 40960\n\t"
    "v_mfma_f32_16x16x32_bf16 v[76:79], v[212:215], v[116:119], v[76:79]\n\t"
    "global_load_lds_dwordx4 %[av5], s[38:39] sc1\n\t"
    "s_xor_b32 %[l1], %[l1], 0x10000\n\t"
    "s_waitcnt lgkmcnt(5)\n\t"
    "v_mfma_f32_16x16x32_bf16 v[64:67], v[216:219], v[120:123], v[64:67]\n\t"
    "ds_read_b128 v[112:115], %[ea] offset:28672\n\t"
    "s_add_u32 m0, %[l0], 12288\n\t"
    "v_mfma_f32_16x16x32_bf16 v[68:71], v[220:223], v[120:123], v[68:71]\n\t"
    "ds_read_b128 v[116:119], %[ea] offset:30720\n\t"
    "global_load_lds_dwordx4 %[av6], s[38:39] sc1\n\t"
    "v_mfma_f32_16x16x32_bf16 v[72:75], v[216:219], v[124:127], v[72:75]\n\t"
    "v_xor_b32 %[ea], 0x10000, %[ea]\n\t"
    "s_add_u32 m0, %[l0], 45056\n\t"
    "v_mfma_f32_16x16x32_bf16 v[76:79], v[220:223], v[124:127], v[76:79]\n\t"
    "global_load_lds_dwordx4 %[av7], s[38:39] sc1\n\t"
    "s_waitcnt lgkmcnt(5)\n\t"
    "v_mfma_f32_16x16x32_bf16 v[64:67], v[224:227], v[96:99], v[64:67]\n\t"
    "ds_read_b128 v[120:123], %[da] offset:28672\n\t"
    "v_mfma_f32_16x16x32_bf16 v[68:71], v[228:231], v[96:99], v[68:71]\n\t"
    "ds_read_b128 v[124:127], %[da] offset:30720\n\t"
    "v_mfma_f32_16x16x32_bf16 v[72:75], v[224:227], v[100:103], v[72:75]\n\t"
    "v_xor_b32 %[da], 0x10000, %[da]\n\t"
    "v_mfma_f32_16x16x32_bf16 v[76:79], v[228:231], v[100:103], v[76:79]\n\t"
    "s_waitcnt lgkmcnt(5)\n\t"
    "v_mfma_f32_16x16x32_bf16 v[64:67], v[232:235], v[104:107], v[64:67]\n\t"
    "ds_read_b128 v[96:99], %[ea] offset:0\n\t"
    "v_mfma_f32_16x16x32_bf16 v[68:71], v[236:239], v[104:107], v[68:71]\n\t"
    "ds_read_b128 v[100:103], %[ea] offset:2048\n\t"
    "v_mfma_f32_16x16x32_bf16 v[72:75], v[232:235], v[108:111], v[72:75]\n\t"
    "v_mfma_f32_16x16x32_bf16 v[76:79], v[236:239], v[108:111], v[76:79]\n\t"
    "s_waitcnt lgkmcnt(4)\n\t"
    "v_mfma_f32_16x16x32_bf16 v[64:67], v[240:243], v[112:115], v[64:67]\n\t"
    "ds_read_b128 v[104:107], %[da] offset:0\n\t"
    "v_mad_u32_u24 v39, v39, %[mu], %[ad]\n\t"
    "v_mfma_f32_16x16x32_bf16 v[68:71], v[244:247], v[112:115], v[68:71]\n\t"
    "ds_read_b128 v[108:111], %[da] offset:2048\n\t"
    "v_cmp_ge_i32 vcc, %[lf], v39\n\t"
    "s_cmp_eq_u64 vcc, 0\n\t"
    "s_addc_u32 %[lf], %[lf], 0\n\t"
    "v_mfma_f32_16x16x32_bf16 v[72:75], v[240:243], v[116:119], v[72:75]\n\t"
    "v_mfma_f32_16x16x32_bf16 v[76:79], v[244:247], v[116:119], v[76:79]\n\t"
    "v_cmp_ge_i32 vcc, %[lf], v39\n\t"
    "s_cmp_eq_u64 vcc, 0\n\t"
    "s_addc_u32 %[lf], %[lf], 0\n\t"
    "s_waitcnt lgkmcnt(4)\n\t"
    "v_mfma_f32_16x16x32_bf16 v[80:83], v[248:251], v[120:123], v[64:67]\n\t"
    "ds_read_b128 v[112:115], %[ea] offset:4096\n\t"
    "s_min_i32 %[lf], %[lf], %[cp]\n\t"
    "s_add_u32 %[b], %[b], 1\n\t"
    "v_mfma_f32_16x16x32_bf16 v[84:87], v[252:255], v[120:123], v[68:71]\n\t"
    "ds_read_b128 v[116:119], %[ea] offset:6144\n\t"
    "v_mfma_f32_16x16x32_bf16 v[88:91], v[248:251], v[124:127], v[72:75]\n\t"
    "v_mfma_f32_16x16x32_bf16 v[92:95], v[252:255], v[124:127], v[76:79]\n\t"
    "s_add_u32 s38, s38, %[ab]\n\t"
    "s_addc_u32 s39, s39, 0\n\t"
    "s_xor_b32 %[l0], %[l0], 0x10000\n\t"
    "s_sub_u32 m0, %[lf], %[b]\n\t"
    "s_sub_u32 m0, m0, 1\n\t"
    "s_cmp_lt_u32 m0, %[up]\n\t"
    "s_cbranch_scc1 1b\n\t"
    "s_waitcnt lgkmcnt(0)\n\t"
    "s_nop 4"
    : "=&{v[80:95]}"(pd), [ea] "+&v"(ea), [da] "+&v"(da), [b] "+&s"(b), [lf] "+&s"(lf), "+{s[36:37]}"(p1), "+{s[38:39]}"(p0), [sa] "+&v"(sa), [l1] "+&s"(l1), [l0] "+&s"(l0)
    : [rw] "v"(rw), [ab] "s"(ab), [cp] "s"(cp), [mu] "s"(mu), [ad] "v"(ad), [up] "s"(up), "{v[128:159]}"(W[0]), "{v[160:191]}"(W[1]), "{v[192:223]}"(W[2]), "{v[224:255]}"(W[3]), [av0] "v"(av[0]), [av1] "v"(av[1]), [av2] "v"(av[2]), [av3] "v"(av[3]), [av4] "v"(av[4]), [av5] "v"(av[5]), [av6] "v"(av[6]), [av7] "v"(av[7]), [la] "s"(la)
    : "v39", "v64", "v65", "v66", "v67", "v68", "v69", "v70", "v71", "v72", "v73", "v74", "v75", "v76", "v77", "v78", "v79", "v96", "v97", "v98", "v99", "v100", "v101", "v102", "v103", "v104", "v105", "v106", "v107", "v108", "v109", "v110", "v111", "v112", "v113", "v114", "v115", "v116", "v117", "v118", "v119", "v120", "v121", "v122", "v123", "v124", "v125", "v126", "v127", "vcc", "scc", "m0", "memory");
}
__device__ __forceinline__ void fast_ld2(const u32x32 (&W)[4], u32x16& pd, unsigned ea, unsigned da, int& b, int& lf, int cp, unsigned up, unsigned sa, unsigned mu, unsigned ad,
    unsigned rw, unsigned ab, const char* p1, const char* p0, unsigned l1, unsigned l0, const unsigned (&av)[8], int la) {
  asm volatile("s_setprio 0\n\t"
    "s_cmp_eq_u32 %[la], 3\n\t"
    "s_waitcnt vmcnt(6)\n\t"
    "s_cbranch_scc1 20f\n\t"
    "s_waitcnt vmcnt(2)\n\t"
    "20:\n\t"
    "s_barrier\n\t"
    "ds_read_b128 v[96:99], %[ea] offset:0\n\t"
    "ds_read_b128 v[100:103], %[ea] offset:2048\n\t"
    "ds_read_b128 v[104:107], %[da] offset:0\n\t"
    "ds_read_b128 v[108:111], %[da] offset:2048\n\t"
    "ds_read_b128 v[112:115], %[ea] offset:4096\n\t"
    "ds_read_b128 v[116:119], %[ea] offset:6144\n\t"
    "s_branch 2f\n\t"
    ".p2align 6\n\t"
    "1:\n\t"
    "s_cmp_eq_u32 %[la], 3\n\t"
    "s_waitcnt vmcnt(6)\n\t"
    "s_cbranch_scc1 21f\n\t"
    "s_waitcnt vmcnt(2)\n\t"
    "21:\n\t"
    "s_barrier\n\t"
    "2:\n\t"
    "s_waitcnt lgkmcnt(4)\n\t"
    "v_mfma_f32_16x16x32_bf16 v[64:67], v[128:131], v[96:99], 0\n\t"
    "ds_read_b128 v[120:123], %[da] offset:4096\n\t"
    "s_add_u32 m0, %[l1], 0\n\t"
    "ds_write_b128 %[rw], v[80:83] offset:0\n\t"
    "v_mfma_f32_16x16x32_bf16 v[68:71], v[132:135], v[96:99], 0\n\t"
    "ds_read_b128 v[124:127], %[da] offset:6144\n\t"
    "global_load_lds_dwordx4 %[av0], s[36:37] sc1\n\t"
    "ds_write_b128 %[rw], v[84:87] offset:1024\n\t"
    "v_mfma_f32_16x16x32_bf16 v[72:75], v[128:131], v[100:103], 0\n\t"
    "ds_write_b128 %[rw], v[88:91] offset:2048\n\t"
    "v_mfma_f32_16x16x32_bf16 v[76:79], v[132:135], v[100:103], 0\n\t"
    "ds_write_b128 %[rw], v[92:95] offset:3072\n\t"
    "s_waitcnt lgkmcnt(8)\n\t"
    "v_mfma_f32_16x16x32_bf16 v[64:67], v[136:139], v[104:107], v[64:67]\n\t"
    "ds_read_b128 v[96:99], %[ea] offset:8192\n\t"
    "s_add_u32 m0, %[l1], 4096\n\t"
    "v_mfma_f32_16x16x32_bf16 v[68:71], v[140:143], v[104:107], v[68:71]\n\t"
    "ds_read_b128 v[100:103], %[ea] offset:10240\n\t"
    "global_load_lds_dwordx4 %[av2], s[36:37] sc1\n\t"
    "v_mfma_f32_16x16x32_bf16 v[72:75], v[136:139], v[108:111], v[72:75]\n\t"
    "v_mfma_f32_16x16x32_bf16 v[76:79], v[140:143], v[108:111], v[76:79]\n\t"
    "s_waitcnt lgkmcnt(8)\n\t"
    "v_mfma_f32_16x16x32_bf16 v[64:67], v[144:147], v[112:115], v[64:67]\n\t"
    "ds_read_b128 v[104:107], %[da] offset:8192\n\t"
    "s_add_u32 m0, %[l1], 8192\n\t"
    "v_mfma_f32_16x16x32_bf16 v[68:71], v[148:151], v[112:115], v[68:71]\n\t"
    "ds_read_b128 v[108:111], %[da] offset:10240\n\t"
    "global_load_lds_dwordx4 %[av4], s[36:37] sc1\n\t"
    "v_mfma_f32_16x16x32_bf16 v[72:75], v[144:147], v[116:119], v[72:75]\n\t"
    "v_mfma_f32_16x16x32_bf16 v[76:79], v[148:151], v[116:119], v[76:79]\n\t"
    "s_waitcnt lgkmcnt(7)\n\t"
    "v_mfma_f32_16x16x32_bf16 v[64:67], v[152:155], v[120:123], v[64:67]\n\t"
    "ds_read_b128 v[112:115], %[ea] offset:12288\n\t"
    "s_add_u32 m0, %[l1], 12288\n\t"
    "v_mfma_f32_16x16x32_bf16 v[68:71], v[156:159], v[120:123], v[68:71]\n\t"
    "ds_read_b128 v[116:119], %[ea] offset:14336\n\t"
    "global_load_lds_dwordx4 %[av6], s[36:37] sc1\n\t"
    "v_mfma_f32_16x16x32_bf16 v[72:75], v[152:155], v[124:127], v[72:75]\n\t"
    "v_mfma_f32_16x16x32_bf16 v[76:79], v[156:159], v[124:127], v[76:79]\n\t"
    "s_waitcnt lgkmcnt(4)\n\t"
    "v_mfma_f32_16x16x32_bf16 v[64:67], v[160:163], v[96:99], v[64:67]\n\t"
    "ds_read_b128 v[120:123], %[da] offset:12288\n\t"
    "v_mfma_f32_16x16x32_bf16 v[68:71], v[164:167], v[96:99], v[68:71]\n\t"
    "ds_read_b128 v[124:127], %[da] offset:14336\n\t"
    "v_mfma_f32_16x16x32_bf16 v[72:75], v[160:163], v[100:103], v[72:75]\n\t"
    "v_mfma_f32_16x16x32_bf16 v[76:79], v[164:167], v[100:103], v[76:79]\n\t"
    "s_waitcnt lgkmcnt(4)\n\t"
    "v_mfma_f32_16x16x32_bf16 v[64:67], v[168:171], v[104:107], v[64:67]\n\t"
    "ds_read_b128 v[96:99], %[ea] offset:16384\n\t"
    "v_mfma_f32_16x16x32_bf16 v[68:71], v[172:175], v[104:107], v[68:71]\n\t"
    "ds_read_b128 v[100:103], %[ea] offset:18432\n\t"
    "v_mfma_f32_16x16x32_bf16 v[72:75], v[168:171], v[108:111], v[72:75]\n\t"
    "v_mfma_f32_16x16x32_bf16 v[76:79], v[172:175], v[108:111], v[76:79]\n\t"
    "s_waitcnt lgkmcnt(4)\n\t"
    "v_mfma_f32_16x16x32_bf16 v[64:67], v[176:179], v[112:115], v[64:67]\n\t"
    "ds_read_b128 v[104:107], %[da] offset:16384\n\t"
    "v_mfma_f32_16x16x32_bf16 v[68:71], v[180:183], v[112:115], v[68:71]\n\t"
    "ds_read_b128 v[108:111], %[da] offset:18432\n\t"
    "v_mfma_f32_16x16x32_bf16 v[72:75], v[176:179], v[116:119], v[72:75]\n\t"
    "v_mfma_f32_16x16x32_bf16 v[76:79], v[180:183], v[116:119], v[76:79]\n\t"
    "s_waitcnt lgkmcnt(4)\n\t"
    "v_mfma_f32_16x16x32_bf16 v[64:67], v[184:187], v[120:123], v[64:67]\n\t"
    "ds_read_b128 v[112:115], %[ea] offset:20480\n\t"
    "v_mfma_f32_16x16x32_bf16 v[68:71], v[188:191], v[120:123], v[68:71]\n\t"
    "ds_read_b128 v[116:119], %[ea] offset:22528\n\t"
    "v_mfma_f32_16x16x32_bf16 v[72:75], v[184:187], v[124:127], v[72:75]\n\t"
    "v_mfma_f32_16x16x32_bf16 v[76:79], v[188:191], v[124:127], v[76:79]\n\t"
    "s_cmp_eq_u32 %[la], 3\n\t"
    "s_waitcnt vmcnt(6)\n\t"
    "s_cbranch_scc1 22f\n\t"
    "s_waitcnt vmcnt(2)\n\t"
    "22:\n\t"
    "s_barrier\n\t"
    "s_waitcnt lgkmcnt(4)\n\t"
    "v_mfma_f32_16x16x32_bf16 v[64:67], v[192:195], v[96:99], v[64:67]\n\t"
    "ds_read_b128 v[120:123], %[da] offset:20480\n\t"
    "s_add_u32 m0, %[l0], 0\n\t"
    "v_mfma_f32_16x16x32_bf16 v[68:71], v[196:199], v[96:99], v[68:71]\n\t"
    "ds_read_b128 v[124:127], %[da] offset:22528\n\t"
    "global_load_lds_dwordx4 %[av0], s[38:39] sc1\n\t"
    "v_mfma_f32_16x16x32_bf16 v[72:75], v[192:195], v[100:103], v[72:75]\n\t"
    "v_mfma_f32_16x16x32_bf16 v[76:79], v[196:199], v[100:103], v[76:79]\n\t"
    "s_waitcnt lgkmcnt(4)\n\t"
    "v_mfma_f32_16x16x32_bf16 v[64:67], v[200:203], v[104:107], v[64:67]\n\t"
    "ds_read_b128 v[96:99], %[ea] offset:24576\n\t"
    "s_add_u32 m0, %[l0], 4096\n\t"
    "v_mfma_f32_16x16x32_bf16 v[68:71], v[204:207], v[104:107], v[68:71]\n\t"
    "ds_read_b128 v[100:103], %[ea] offset:26624\n\t"
    "global_load_lds_dwordx4 %[av2], s[38:39] sc1\n\t"
    "v_mfma_f32_16x16x32_bf16 v[72:75], v[200:203], v[108:111], v[72:75]\n\t"
    "v_mfma_f32_16x16x32_bf16 v[76:79], v[204:207], v[108:111], v[76:79]\n\t"
    "s_add_u32 s36, s36, %[ab]\n\t"
    "s_addc_u32 s37, s37, 0\n\t"
    "s_waitcnt lgkmcnt(4)\n\t"
    "v_mfma_f32_16x16x32_bf16 v[64:67], v[208:211], v[112:115], v[64:67]\n\t"
    "ds_read_b128 v[104:107], %[da] offset:24576\n\t"
    "s_add_u32 m0, %[l0], 8192\n\t"
    "v_mfma_f32_16x16x32_bf16 v[68:71], v[212:215], v[112:115], v[68:71]\n\t"
    "ds_read_b128 v[108:111], %[da] offset:26624\n\t"
    "global_load_lds_dwordx4 %[av4], s[38:39] sc1\n\t"
    "v_mfma_f32_16x16x32_bf16 v[72:75], v[208:211], v[116:119], v[72:75]\n\t"
    "ds_read_b32 v39, %[sa]\n\t"
    "v_mfma_f32_16x16x32_bf16 v[76:79], v[212:215], v[116:119], v[76:79]\n\t"
    "s_xor_b32 %[l1], %[l1], 0x10000\n\t"
    "s_waitcnt lgkmcnt(5)\n\t"
    "v_mfma_f32_16x16x32_bf16 v[64:67], v[216:219], v[120:123], v[64:67]\n\t"
    "ds_read_b128 v[112:115], %[ea] offset:28672\n\t"
    "s_add_u32 m0, %[l0], 12288\n\t"
    "v_mfma_f32_16x16x32_bf16 v[68:71], v[220:223], v[120:123], v[68:71]\n\t"
    "ds_read_b128 v[116:119], %[ea] offset:30720\n\t"
    "global_load_lds_dwordx4 %[av6], s[38:39] sc1\n\t"
    "v_mfma_f32_16x16x32_bf16 v[72:75], v[216:219], v[124:127], v[72:75]\n\t"
    "v_xor_b32 %[ea], 0x10000, %[ea]\n\t"
    "v_mfma_f32_16x16x32_bf16 v[76:79], v[220:223], v[124:127], v[76:79]\n\t"
    "s_waitcnt lgkmcnt(5)\n\t"
    "v_mfma_f32_16x16x32_bf16 v[64:67], v[224:227], v[96:99], v[64:67]\n\t"
    "ds_read_b128 v[120:123], %[da] offset:28672\n\t"
    "v_mfma_f32_16x16x32_bf16 v[68:71], v[228:231], v[96:99], v[68:71]\n\t"
    "ds_read_b128 v[124:127], %[da] offset:30720\n\t"
    "v_mfma_f32_16x16x32_bf16 v[72:75], v[224:227], v[100:103], v[72:75]\n\t"
    "v_xor_b32 %[da], 0x10000, %[da]\n\t"
    "v_mfma_f32_16x16x32_bf16 v[76:79], v[228:231], v[100:103], v[76:79]\n\t"
    "s_waitcnt lgkmcnt(5)\n\t"
    "v_mfma_f32_16x16x32_bf16 v[64:67], v[232:235], v[104:107], v[64:67]\n\t"
    "ds_read_b128 v[96:99], %[ea] offset:0\n\t"
    "v_mfma_f32_16x16x32_bf16 v[68:71], v[236:239], v[104:107], v[68:71]\n\t"
    "ds_read_b128 v[100:103], %[ea] offset:2048\n\t"
    "v_mfma_f32_16x16x32_bf16 v[72:75], v[232:235], v[108:111], v[72:75]\n\t"
    "v_mfma_f32_16x16x32_bf16 v[76:79], v[236:239], v[108:111], v[76:79]\n\t"
    "s_waitcnt lgkmcnt(4)\n\t"
    "v_mfma_f32_16x16x32_bf16 v[64:67], v[240:243], v[112:115], v[64:67]\n\t"
    "ds_read_b128 v[104:107], %[da] offset:0\n\t"
    "v_mad_u32_u24 v39, v39, %[mu], %[ad]\n\t"
    "v_mfma_f32_16x16x32_bf16 v[68:71], v[244:247], v[112:115], v[68:71]\n\t"
    "ds_read_b128 v[108:111], %[da] offset:2048\n\t"
    "v_cmp_ge_i32 vcc, %[lf], v39\n\t"
    "s_cmp_eq_u64 vcc, 0\n\t"
    "s_addc_u32 %[lf], %[lf], 0\n\t"
    "v_mfma_f32_16x16x32_bf16 v[72:75], v[240:243], v[116:119], v[72:75]\n\t"
    "v_mfma_f32_16x16x32_bf16 v[76:79], v[244:247], v[116:119], v[76:79]\n\t"
    "v_cmp_ge_i32 vcc, %[lf], v39\n\t"
    "s_cmp_eq_u64 vcc, 0\n\t"
    "s_addc_u32 %[lf], %[lf], 0\n\t"
    "s_waitcnt lgkmcnt(4)\n\t"
    "v_mfma_f32_16x16x32_bf16 v[80:83], v[248:251], v[120:123], v[64:67]\n\t"
    "ds_read_b128 v[112:115], %[ea] offset:4096\n\t"
    "s_min_i32 %[lf], %[lf], %[cp]\n\t"
    "s_add_u32 %[b], %[b], 1\n\t"
    "v_mfma_f32_16x16x32_bf16 v[84:87], v[252:255], v[120:123], v[68:71]\n\t"
    "ds_read_b128 v[116:119], %[ea] offset:6144\n\t"
    "v_mfma_f32_16x16x32_bf16 v[88:91], v[248:251], v[124:127], v[72:75]\n\t"
    "v_mfma_f32_16x16x32_bf16 v[92:95], v[252:255], v[124:127], v[76:79]\n\t"
    "s_add_u32 s38, s38, %[ab]\n\t"
    "s_addc_u32 s39, s39, 0\n\t"
    "s_xor_b32 %[l0], %[l0], 0x10000\n\t"
    "s_sub_u32 m0, %[lf], %[b]\n\t"
    "s_sub_u32 m0, m0, 1\n\t"
    "s_cmp_lt_u32 m0, %[up]\n\t"
    "s_cbranch_scc1 1b\n\t"
    "s_waitcnt lgkmcnt(0)\n\t"
    "s_nop 4"
    : "=&{v[80:95]}"(pd), [ea] "+&v"(ea), [da] "+&v"(da), [b] "+&s"(b), [lf] "+&s"(lf), "+{s[36:37]}"(p1), "+{s[38:39]}"(p0), [sa] "+&v"(sa), [l1] "+&s"(l1), [l0] "+&s"(l0)
    : [rw] "v"(rw), [ab] "s"(ab), [cp] "s"(cp), [mu] "s"(mu), [ad] "v"(ad), [up] "s"(up), "{v[128:159]}"(W[0]), "{v[160:191]}"(W[1]), "{v[192:223]}"(W[2]), "{v[224:255]}"(W[3]), [av0] "v"(av[0]), [av1] "v"(av[1]), [av2] "v"(av[2]), [av3] "v"(av[3]), [av4] "v"(av[4]), [av5] "v"(av[5]), [av6] "v"(av[6]), [av7] "v"(av[7]), [la] "s"(la)
    : "v39", "v64", "v65", "v66", "v67", "v68", "v69", "v70", "v71", "v72", "v73", "v74", "v75", "v76", "v77", "v78", "v79", "v96", "v97", "v98", "v99", "v100", "v101", "v102", "v103", "v104", "v105", "v106", "v107", "v108", "v109", "v110", "v111", "v112", "v113", "v114", "v115", "v116", "v117", "v118", "v119", "v120", "v121", "v122", "v123", "v124", "v125", "v126", "v127", "vcc", "scc", "m0", "memory");
}
__device__ __forceinline__ void fast_st0(const u32x32 (&W)[4], u32x16& pd, unsigned ea, unsigned da, int& b, int& lf, int cp, unsigned up, unsigned sa, unsigned mu, unsigned ad,
    unsigned rw, unsigned ab, const char* p1, const char* p0, unsigned co0, unsigned co1, int po, int pf, unsigned w0, unsigned sl, unsigned dv, const char* db, const char* pg) {
  unsigned st;
  asm volatile("s_barrier\n\t"
    "ds_read_b128 v[96:99], %[ea] offset:0\n\t"
    "ds_read_b128 v[100:103], %[ea] offset:2048\n\t"
    "ds_read_b128 v[104:107], %[da] offset:0\n\t"
    "ds_read_b128 v[108:111], %[da] offset:2048\n\t"
    "ds_read_b128 v[112:115], %[ea] offset:4096\n\t"
    "ds_read_b128 v[116:119], %[ea] offset:6144\n\t"
    "s_branch 2f\n\t"
    ".p2align 6\n\t"
    "1:\n\t"
    "s_barrier\n\t"
    "2:\n\t"
    "s_waitcnt lgkmcnt(4)\n\t"
    "v_mfma_f32_16x16x32_bf16 v[64:67], v[128:131], v[96:99], 0\n\t"
    "ds_read_b128 v[120:123], %[da] offset:4096\n\t"
    "v_mfma_f32_16x16x32_bf16 v[68:71], v[132:135], v[96:99], 0\n\t"
    "ds_read_b128 v[124:127], %[da] offset:6144\n\t"
    "v_mfma_f32_16x16x32_bf16 v[72:75], v[128:131], v[100:103], 0\n\t"
    "v_mfma_f32_16x16x32_bf16 v[76:79], v[132:135], v[100:103], 0\n\t"
    "s_waitcnt lgkmcnt(4)\n\t"
    "v_mfma_f32_16x16x32_bf16 v[64:67], v[136:139], v[104:107], v[64:67]\n\t"
    "ds_read_b128 v[96:99], %[ea] offset:8192\n\t"
    "v_mfma_f32_16x16x32_bf16 v[68:71], v[140:143], v[104:107], v[68:71]\n\t"
    "ds_read_b128 v[100:103], %[ea] offset:10240\n\t"
    "v_mfma_f32_16x16x32_bf16 v[72:75], v[136:139], v[108:111], v[72:75]\n\t"
    "v_mfma_f32_16x16x32_bf16 v[76:79], v[140:143], v[108:111], v[76:79]\n\t"
    "s_cmp_eq_u32 %[w0], 1\n\t"
    "s_cbranch_scc1 7f\n\t"
    "8:\n\t"
    "s_waitcnt lgkmcnt(4)\n\t"
    "v_mfma_f32_16x16x32_bf16 v[64:67], v[144:147], v[112:115], v[64:67]\n\t"
    "ds_read_b128 v[104:107], %[da] offset:8192\n\t"
    "v_mfma_f32_16x16x32_bf16 v[68:71], v[148:151], v[112:115], v[68:71]\n\t"
    "ds_read_b128 v[108:111], %[da] offset:10240\n\t"
    "v_mfma_f32_16x16x32_bf16 v[72:75], v[144:147], v[116:119], v[72:75]\n\t"
    "v_mfma_f32_16x16x32_bf16 v[76:79], v[148:151], v[116:119], v[76:79]\n\t"
    "s_waitcnt lgkmcnt(4)\n\t"
    "v_mfma_f32_16x16x32_bf16 v[64:67], v[152:155], v[120:123], v[64:67]\n\t"
    "ds_read_b128 v[112:115], %[ea] offset:12288\n\t"
    "v_mfma_f32_16x16x32_bf16 v[68:71], v[156:159], v[120:123], v[68:71]\n\t"
    "ds_read_b128 v[116:119], %[ea] offset:14336\n\t"
    "v_mfma_f32_16x16x32_bf16 v[72:75], v[152:155], v[124:127], v[72:75]\n\t"
    "v_mfma_f32_16x16x32_bf16 v[76:79], v[156:159], v[124:127], v[76:79]\n\t"
    "s_waitcnt lgkmcnt(4)\n\t"
    "v_mfma_f32_16x16x32_bf16 v[64:67], v[160:163], v[96:99], v[64:67]\n\t"
    "ds_read_b128 v[120:123], %[da] offset:12288\n\t"
    "v_mfma_f32_16x16x32_bf16 v[68:71], v[164:167], v[96:99], v[68:71]\n\t"
    "ds_read_b128 v[124:127], %[da] offset:14336\n\t"
    "v_mfma_f32_16x16x32_bf16 v[72:75], v[160:163], v[100:103], v[72:75]\n\t"
    "v_mfma_f32_16x16x32_bf16 v[76:79], v[164:167], v[100:103], v[76:79]\n\t"
    "s_waitcnt lgkmcnt(4)\n\t"
    "v_mfma_f32_16x16x32_bf16 v[64:67], v[168:171], v[104:107], v[64:67]\n\t"
    "ds_read_b128 v[96:99], %[ea] offset:16384\n\t"
    "v_mfma_f32_16x16x32_bf16 v[68:71], v[172:175], v[104:107], v[68:71]\n\t"
    "ds_read_b128 v[100:103], %[ea] offset:18432\n\t"
    "v_mfma_f32_16x16x32_bf16 v[72:75], v[168:171], v[108:111], v[72:75]\n\t"
    "v_mfma_f32_16x16x32_bf16 v[76:79], v[172:175], v[108:111], v[76:79]\n\t"
    "s_waitcnt lgkmcnt(4)\n\t"
    "v_mfma_f32_16x16x32_bf16 v[64:67], v[176:179], v[112:115], v[64:67]\n\t"
    "ds_read_b128 v[104:107], %[da] offset:16384\n\t"
    "v_mfma_f32_16x16x32_bf16 v[68:71], v[180:183], v[112:115], v[68:71]\n\t"
    "ds_read_b128 v[108:111], %[da] offset:18432\n\t"
    "v_mfma_f32_16x16x32_bf16 v[72:75], v[176:179], v[116:119], v[72:75]\n\t"
    "v_mfma_f32_16x16x32_bf16 v[76:79], v[180:183], v[116:119], v[76:79]\n\t"
    "s_waitcnt lgkmcnt(4)\n\t"
    "v_mfma_f32_16x16x32_bf16 v[64:67], v[184:187], v[120:123], v[64:67]\n\t"
    "ds_read_b128 v[112:115], %[ea] offset:20480\n\t"
    "v_mfma_f32_16x16x32_bf16 v[68:71], v[188:191], v[120:123], v[68:71]\n\t"
    "ds_read_b128 v[116:119], %[ea] offset:22528\n\t"
    "v_mfma_f32_16x16x32_bf16 v[72:75], v[184:187], v[124:127], v[72:75]\n\t"
    "v_mfma_f32_16x16x32_bf16 v[76:79], v[188:191], v[124:127], v[76:79]\n\t"
    "s_waitcnt vmcnt(0)\n\t"
    "s_barrier\n\t"
    "s_waitcnt lgkmcnt(4)\n\t"
    "v_mfma_f32_16x16x32_bf16 v[64:67], v[192:195], v[96:99], v[64:67]\n\t"
    "ds_read_b128 v[120:123], %[da] offset:20480\n\t"
    "v_mfma_f32_16x16x32_bf16 v[68:71], v[196:199], v[96:99], v[68:71]\n\t"
    "ds_read_b128 v[124:127], %[da] offset:22528\n\t"
    "v_mfma_f32_16x16x32_bf16 v[72:75], v[192:195], v[100:103], v[72:75]\n\t"
    "ds_read_b128 v[48:51], %[rw] offset:0\n\t"
    "v_mfma_f32_16x16x32_bf16 v[76:79], v[196:199], v[100:103], v[76:79]\n\t"
    "ds_read_b128 v[52:55], %[rw] offset:1024\n\t"
    "s_cmp_eq_u32 %[w0], 0\n\t"
    "s_cbranch_scc1 5f\n\t"
    "6:\n\t"
    "s_waitcnt lgkmcnt(6)\n\t"
    "v_mfma_f32_16x16x32_bf16 v[64:67], v[200:203], v[104:107], v[64:67]\n\t"
    "ds_read_b128 v[96:99], %[ea] offset:24576\n\t"
    "v_mfma_f32_16x16x32_bf16 v[68:71], v[204:207], v[104:107], v[68:71]\n\t"
    "ds_read_b128 v[100:103], %[ea] offset:26624\n\t"
    "v_mfma_f32_16x16x32_bf16 v[72:75], v[200:203], v[108:111], v[72:75]\n\t"
    "ds_read_b128 v[56:59], %[rw] offset:2048\n\t"
    "v_mfma_f32_16x16x32_bf16 v[76:79], v[204:207], v[108:111], v[76:79]\n\t"
    "ds_read_b128 v[60:63], %[rw] offset:3072\n\t"
    "s_waitcnt lgkmcnt(0)\n\t"
    "v_mfma_f32_16x16x32_bf16 v[64:67], v[208:211], v[112:115], v[64:67]\n\t"
    "ds_read_b128 v[104:107], %[da] offset:24576\n\t"
    "v_add_f32 v48, v48, v80\n\t"
    "v_add_f32 v49, v49, v81\n\t"
    "v_mfma_f32_16x16x32_bf16 v[68:71], v[212:215], v[112:115], v[68:71]\n\t"
    "ds_read_b128 v[108:111], %[da] offset:26624\n\t"
    "v_add_f32 v50, v50, v82\n\t"
    "v_add_f32 v51, v51, v83\n\t"
    "v_mfma_f32_16x16x32_bf16 v[72:75], v[208:211], v[116:119], v[72:75]\n\t"
    "ds_read_b32 v39, %[sa]\n\t"
    "v_add_f32 v52, v52, v84\n\t"
    "v_mfma_f32_16x16x32_bf16 v[76:79], v[212:215], v[116:119], v[76:79]\n\t"
    "v_add_f32 v53, v53, v85\n\t"
    "v_add_f32 v54, v54, v86\n\t"
    "v_mfma_f32_16x16x32_bf16 v[64:67], v[216:219], v[120:123], v[64:67]\n\t"
    "ds_read_b128 v[112:115], %[ea] offset:28672\n\t"
    "v_add_f32 v55, v55, v87\n\t"
    "v_add_f32 v56, v56, v88\n\t"
    "v_mfma_f32_16x16x32_bf16 v[68:71], v[220:223], v[120:123], v[68:71]\n\t"
    "ds_read_b128 v[116:119], %[ea] offset:30720\n\t"
    "v_add_f32 v57, v57, v89\n\t"
    "v_mfma_f32_16x16x32_bf16 v[72:75], v[216:219], v[124:127], v[72:75]\n\t"
    "v_xor_b32 %[ea], 0x10000, %[ea]\n\t"
    "v_add_f32 v58, v58, v90\n\t"
    "v_add_f32 v59, v59, v91\n\t"
    "v_mfma_f32_16x16x32_bf16 v[76:79], v[220:223], v[124:127], v[76:79]\n\t"
    "v_add_f32 v60, v60, v92\n\t"
    "v_mfma_f32_16x16x32_bf16 v[64:67], v[224:227], v[96:99], v[64:67]\n\t"
    "ds_read_b128 v[120:123], %[da] offset:28672\n\t"
    "v_add_f32 v61, v61, v93\n\t"
    "v_add_f32 v62, v62, v94\n\t"
    "v_mfma_f32_16x16x32_bf16 v[68:71], v[228:231], v[96:99], v[68:71]\n\t"
    "ds_read_b128 v[124:127], %[da] offset:30720\n\t"
    "v_add_f32 v63, v63, v95\n\t"
    "v_cvt_pk_bf16_f32 v40, v48, v49\n\t"
    "v_mfma_f32_16x16x32_bf16 v[72:75], v[224:227], v[100:103], v[72:75]\n\t"
    "v_xor_b32 %[da], 0x10000, %[da]\n\t"
    "v_cvt_pk_bf16_f32 v41, v50, v51\n\t"
    "v_mfma_f32_16x16x32_bf16 v[76:79], v[228:231], v[100:103], v[76:79]\n\t"
    "v_cvt_pk_bf16_f32 v42, v52, v53\n\t"
    "v_cvt_pk_bf16_f32 v43, v54, v55\n\t"
    "s_waitcnt lgkmcnt(5)\n\t"
    "v_mfma_f32_16x16x32_bf16 v[64:67], v[232:235], v[104:107], v[64:67]\n\t"
    "ds_read_b128 v[96:99], %[ea] offset:0\n\t"
    "global_store_dwordx4 %[co0], v[40:43], s[36:37] sc1\n\t"
    "v_cvt_pk_bf16_f32 v44, v56, v57\n\t"
    "v_mfma_f32_16x16x32_bf16 v[68:71], v[236:239], v[104:107], v[68:71]\n\t"
    "ds_read_b128 v[100:103], %[ea] offset:2048\n\t"
    "v_cvt_pk_bf16_f32 v45, v58, v59\n\t"
    "v_mfma_f32_16x16x32_bf16 v[72:75], v[232:235], v[108:111], v[72:75]\n\t"
    "v_cvt_pk_bf16_f32 v46, v60, v61\n\t"
    "v_cvt_pk_bf16_f32 v47, v62, v63\n\t"
    "v_mfma_f32_16x16x32_bf16 v[76:79], v[236:239], v[108:111], v[76:79]\n\t"
    "global_store_dwordx4 %[co1], v[44:47], s[36:37] sc1\n\t"
    "s_waitcnt lgkmcnt(4)\n\t"
    "v_mfma_f32_16x16x32_bf16 v[64:67], v[240:243], v[112:115], v[64:67]\n\t"
    "ds_read_b128 v[104:107], %[da] offset:0\n\t"
    "v_mad_u32_u24 v39, v39, %[mu], %[ad]\n\t"
    "v_mfma_f32_16x16x32_bf16 v[68:71], v[244:247], v[112:115], v[68:71]\n\t"
    "ds_read_b128 v[108:111], %[da] offset:2048\n\t"
    "v_cmp_ge_i32 vcc, %[lf], v39\n\t"
    "s_cmp_eq_u64 vcc, 0\n\t"
    "s_addc_u32 %[lf], %[lf], 0\n\t"
    "v_mfma_f32_16x16x32_bf16 v[72:75], v[240:243], v[116:119], v[72:75]\n\t"
    "v_mfma_f32_16x16x32_bf16 v[76:79], v[244:247], v[116:119], v[76:79]\n\t"
    "v_cmp_ge_i32 vcc, %[lf], v39\n\t"
    "s_cmp_eq_u64 vcc, 0\n\t"
    "s_addc_u32 %[lf], %[lf], 0\n\t"
    "s_waitcnt lgkmcnt(4)\n\t"
    "v_mfma_f32_16x16x32_bf16 v[80:83], v[248:251], v[120:123], v[64:67]\n\t"
    "ds_read_b128 v[112:115], %[ea] offset:4096\n\t"
    "s_min_i32 %[lf], %[lf], %[cp]\n\t"
    "s_add_u32 %[b], %[b], 1\n\t"
    "v_mfma_f32_16x16x32_bf16 v[84:87], v[252:255], v[120:123], v[68:71]\n\t"
    "ds_read_b128 v[116:119], %[ea] offset:6144\n\t"
    "v_mfma_f32_16x16x32_bf16 v[88:91], v[248:251], v[124:127], v[72:75]\n\t"
    "v_mfma_f32_16x16x32_bf16 v[92:95], v[252:255], v[124:127], v[76:79]\n\t"
    "s_mov_b64 s[36:37], s[38:39]\n\t"
    "s_add_u32 s38, s38, %[ab]\n\t"
    "s_addc_u32 s39, s39, 0\n\t"
    "s_sub_u32 m0, %[lf], %[b]\n\t"
    "s_sub_u32 m0, m0, 1\n\t"
    "s_cmp_lt_u32 m0, %[up]\n\t"
    "s_cbranch_scc1 1b\n\t"
    "s_waitcnt lgkmcnt(0)\n\t"
    "s_nop 4\n\t"
    "s_branch 9f\n\t"
    "5:\n\t"
    "s_add_u32 %[st], %[b], %[po]\n\t"
    "s_max_i32 %[st], %[st], %[p0]\n\t"
    "v_mov_b32 v39, %[st]\n\t"
    "v_mov_b32 v38, 0\n\t"
    "s_mov_b64 exec, 1\n\t"
    "global_store_dword v38, v39, %[pg] sc1\n\t"
    "s_mov_b64 exec, -1\n\t"
    "s_mov_b32 %[p0], %[p1]\n\t"
    "s_branch 6b\n\t"
    "7:\n\t"
    "s_cmp_ge_i32 %[lf], %[cp]\n\t"
    "s_cbranch_scc1 8b\n\t"
    "s_mov_b32 m0, %[sl]\n\t"
    "s_nop 0\n\t"
    "global_load_lds_dword %[dv], %[db] sc1\n\t"
    "s_branch 8b\n\t"
    "9:"
    : "=&{v[80:95]}"(pd), [ea] "+&v"(ea), [da] "+&v"(da), [b] "+&s"(b), [lf] "+&s"(lf), "+{s[36:37]}"(p1), "+{s[38:39]}"(p0), [sa] "+&v"(sa), [st] "=&s"(st), [sl] "+&s"(sl), [p0] "+&s"(pf)
    : [rw] "v"(rw), [ab] "s"(ab), [cp] "s"(cp), [mu] "s"(mu), [ad] "v"(ad), [up] "s"(up), "{v[128:159]}"(W[0]), "{v[160:191]}"(W[1]), "{v[192:223]}"(W[2]), "{v[224:255]}"(W[3]), [co0] "v"(co0), [w0] "s"(w0), [dv] "v"(dv), [db] "s"(db), [pg] "s"(pg), [po] "s"(po - 1), [p1] "s"(b + po), [co1] "v"(co1)
    : "v38", "v39", "v40", "v41", "v42", "v43", "v44", "v45", "v46", "v47", "v48", "v49", "v50", "v51", "v52", "v53", "v54", "v55", "v56", "v57", "v58", "v59", "v60", "v61", "v62", "v63", "v64", "v65", "v66", "v67", "v68", "v69", "v70", "v71", "v72", "v73", "v74", "v75", "v76", "v77", "v78", "v79", "v96", "v97", "v98", "v99", "v100", "v101", "v102", "v103", "v104", "v105", "v106", "v107", "v108", "v109", "v110", "v111", "v112", "v113", "v114", "v115", "v116", "v117", "v118", "v119", "v120", "v121", "v122", "v123", "v124", "v125", "v126", "v127", "vcc", "scc", "m0", "memory");
}
__device__ __forceinline__ void fast_st1(const u32x32 (&W)[4], u32x16& pd, unsigned ea, unsigned da, int& b, int& lf, int cp, unsigned up, unsigned sa, unsigned mu, unsigned ad,
    unsigned rw, unsigned ab, const char* p1, const char* p0, unsigned co0, unsigned co1, int po, int pf, unsigned w0, unsigned sl, unsigned dv, const char* db, const char* pg) {
  unsigned st;
  asm volatile("s_barrier\n\t"
    "ds_read_b128 v[96:99], %[ea] offset:0\n\t"
    "ds_read_b128 v[100:103], %[ea] offset:2048\n\t"
    "ds_read_b128 v[104:107], %[da] offset:0\n\t"
    "ds_read_b128 v[108:111], %[da] offset:2048\n\t"
    "ds_read_b128 v[112:115], %[ea] offset:4096\n\t"
    "ds_read_b128 v[116:119], %[ea] offset:6144\n\t"
    "s_branch 2f\n\t"
    ".p2align 6\n\t"
    "1:\n\t"
    "s_barrier\n\t"
    "2:\n\t"
    "s_waitcnt lgkmcnt(4)\n\t"
    "v_mfma_f32_16x16x32_bf16 v[64:67], v[128:131], v[96:99], 0\n\t"
    "ds_read_b128 v[120:123], %[da] offset:4096\n\t"
    "v_mfma_f32_16x16x32_bf16 v[68:71], v[132:135], v[96:99], 0\n\t"
    "ds_read_b128 v[124:127], %[da] offset:6144\n\t"
    "v_mfma_f32_16x16x32_bf16 v[72:75], v[128:131], v[100:103], 0\n\t"
    "v_mfma_f32_16x16x32_bf16 v[76:79], v[132:135], v[100:103], 0\n\t"
    "s_waitcnt lgkmcnt(4)\n\t"
    "v_mfma_f32_16x16x32_bf16 v[64:67], v[136:139], v[104:107], v[64:67]\n\t"
    "ds_read_b128 v[96:99], %[ea] offset:8192\n\t"
    "v_mfma_f32_16x16x32_bf16 v[68:71], v[140:143], v[104:107], v[68:71]\n\t"
    "ds_read_b128 v[100:103], %[ea] offset:10240\n\t"
    "v_mfma_f32_16x16x32_bf16 v[72:75], v[136:139], v[108:111], v[72:75]\n\t"
    "v_mfma_f32_16x16x32_bf16 v[76:79], v[140:143], v[108:111], v[76:79]\n\t"
    "s_cmp_eq_u32 %[w0], 1\n\t"
    "s_cbranch_scc1 7f\n\t"
    "8:\n\t"
    "s_waitcnt lgkmcnt(4)\n\t"
    "v_mfma_f32_16x16x32_bf16 v[64:67], v[144:147], v[112:115], v[64:67]\n\t"
    "ds_read_b128 v[104:107], %[da] offset:8192\n\t"
    "v_mfma_f32_16x16x32_bf16 v[68:71], v[148:151], v[112:115], v[68:71]\n\t"
    "ds_read_b128 v[108:111], %[da] offset:10240\n\t"
    "v_mfma_f32_16x16x32_bf16 v[72:75], v[144:147], v[116:119], v[72:75]\n\t"
    "v_mfma_f32_16x16x32_bf16 v[76:79], v[148:151], v[116:119], v[76:79]\n\t"
    "s_waitcnt lgkmcnt(4)\n\t"
    "v_mfma_f32_16x16x32_bf16 v[64:67], v[152:155], v[120:123], v[64:67]\n\t"
    "ds_read_b128 v[112:115], %[ea] offset:12288\n\t"
    "v_mfma_f32_16x16x32_bf16 v[68:71], v[156:159], v[120:123], v[68:71]\n\t"
    "ds_read_b128 v[116:119], %[ea] offset:14336\n\t"
    "v_mfma_f32_16x16x32_bf16 v[72:75], v[152:155], v[124:127], v[72:75]\n\t"
    "v_mfma_f32_16x16x32_bf16 v[76:79], v[156:159], v[124:127], v[76:79]\n\t"
    "s_waitcnt lgkmcnt(4)\n\t"
    "v_mfma_f32_16x16x32_bf16 v[64:67], v[160:163], v[96:99], v[64:67]\n\t"
    "ds_read_b128 v[120:123], %[da] offset:12288\n\t"
    "v_mfma_f32_16x16x32_bf16 v[68:71], v[164:167], v[96:99], v[68:71]\n\t"
    "ds_read_b128 v[124:127], %[da] offset:14336\n\t"
    "v_mfma_f32_16x16x32_bf16 v[72:75], v[160:163], v[100:103], v[72:75]\n\t"
    "v_mfma_f32_16x16x32_bf16 v[76:79], v[164:167], v[100:103], v[76:79]\n\t"
    "s_waitcnt lgkmcnt(4)\n\t"
    "v_mfma_f32_16x16x32_bf16 v[64:67], v[168:171], v[104:107], v[64:67]\n\t"
    "ds_read_b128 v[96:99], %[ea] offset:16384\n\t"
    "v_mfma_f32_16x16x32_bf16 v[68:71], v[172:175], v[104:107], v[68:71]\n\t"
    "ds_read_b128 v[100:103], %[ea] offset:18432\n\t"
    "v_mfma_f32_16x16x32_bf16 v[72:75], v[168:171], v[108:111], v[72:75]\n\t"
    "v_mfma_f32_16x16x32_bf16 v[76:79], v[172:175], v[108:111], v[76:79]\n\t"
    "s_waitcnt lgkmcnt(4)\n\t"
    "v_mfma_f32_16x16x32_bf16 v[64:67], v[176:179], v[112:115], v[64:67]\n\t"
    "ds_read_b128 v[104:107], %[da] offset:16384\n\t"
    "v_mfma_f32_16x16x32_bf16 v[68:71], v[180:183], v[112:115], v[68:71]\n\t"
    "ds_read_b128 v[108:111], %[da] offset:18432\n\t"
    "v_mfma_f32_16x16x32_bf16 v[72:75], v[176:179], v[116:119], v[72:75]\n\t"
    "v_mfma_f32_16x16x32_bf16 v[76:79], v[180:183], v[116:119], v[76:79]\n\t"
    "s_waitcnt lgkmcnt(4)\n\t"
    "v_mfma_f32_16x16x32_bf16 v[64:67], v[184:187], v[120:123], v[64:67]\n\t"
    "ds_read_b128 v[112:115], %[ea] offset:20480\n\t"
    "v_mfma_f32_16x16x32_bf16 v[68:71], v[188:191], v[120:123], v[68:71]\n\t"
    "ds_read_b128 v[116:119], %[ea] offset:22528\n\t"
    "v_mfma_f32_16x16x32_bf16 v[72:75], v[184:187], v[124:127], v[72:75]\n\t"
    "v_mfma_f32_16x16x32_bf16 v[76:79], v[188:191], v[124:127], v[76:79]\n\t"
    "s_waitcnt vmcnt(0)\n\t"
    "s_barrier\n\t"
    "s_waitcnt lgkmcnt(4)\n\t"
    "v_mfma_f32_16x16x32_bf16 v[64:67], v[192:195], v[96:99], v[64:67]\n\t"
    "ds_read_b128 v[120:123], %[da] offset:20480\n\t"
    "v_mfma_f32_16x16x32_bf16 v[68:71], v[196:199], v[96:99], v[68:71]\n\t"
    "ds_read_b128 v[124:127], %[da] offset:22528\n\t"
    "v_mfma_f32_16x16x32_bf16 v[72:75], v[192:195], v[100:103], v[72:75]\n\t"
    "ds_read_b128 v[48:51], %[rw] offset:0\n\t"
    "v_mfma_f32_16x16x32_bf16 v[76:79], v[196:199], v[100:103], v[76:79]\n\t"
    "ds_read_b128 v[52:55], %[rw] offset:1024\n\t"
    "s_cmp_eq_u32 %[w0], 0\n\t"
    "s_cbranch_scc1 5f\n\t"
    "6:\n\t"
    "s_waitcnt lgkmcnt(6)\n\t"
    "v_mfma_f32_16x16x32_bf16 v[64:67], v[200:203], v[104:107], v[64:67]\n\t"
    "ds_read_b128 v[96:99], %[ea] offset:24576\n\t"
    "v_mfma_f32_16x16x32_bf16 v[68:71], v[204:207], v[104:107], v[68:71]\n\t"
    "ds_read_b128 v[100:103], %[ea] offset:26624\n\t"
    "v_mfma_f32_16x16x32_bf16 v[72:75], v[200:203], v[108:111], v[72:75]\n\t"
    "ds_read_b128 v[56:59], %[rw] offset:2048\n\t"
    "v_mfma_f32_16x16x32_bf16 v[76:79], v[204:207], v[108:111], v[76:79]\n\t"
    "ds_read_b128 v[60:63], %[rw] offset:3072\n\t"
    "s_waitcnt lgkmcnt(0)\n\t"
    "v_mfma_f32_16x16x32_bf16 v[64:67], v[208:211], v[112:115], v[64:67]\n\t"
    "ds_read_b128 v[104:107], %[da] offset:24576\n\t"
    "v_add_f32 v48, v48, v80\n\t"
    "v_add_f32 v49, v49, v81\n\t"
    "v_add_f32 v50, v50, v82\n\t"
    "v_mfma_f32_16x16x32_bf16 v[68:71], v[212:215], v[112:115], v[68:71]\n\t"
    "ds_read_b128 v[108:111], %[da] offset:26624\n\t"
    "v_add_f32 v51, v51, v83\n\t"
    "v_add_f32 v52, v52, v84\n\t"
    "v_add_f32 v53, v53, v85\n\t"
    "v_mfma_f32_16x16x32_bf16 v[72:75], v[208:211], v[116:119], v[72:75]\n\t"
    "ds_read_b32 v39, %[sa]\n\t"
    "v_add_f32 v54, v54, v86\n\t"
    "v_add_f32 v55, v55, v87\n\t"
    "v_add_f32 v56, v56, v88\n\t"
    "v_mfma_f32_16x16x32_bf16 v[76:79], v[212:215], v[116:119], v[76:79]\n\t"
    "v_add_f32 v57, v57, v89\n\t"
    "v_add_f32 v58, v58, v90\n\t"
    "v_add_f32 v59, v59, v91\n\t"
    "v_mfma_f32_16x16x32_bf16 v[64:67], v[216:219], v[120:123], v[64:67]\n\t"
    "ds_read_b128 v[112:115], %[ea] offset:28672\n\t"
    "v_add_f32 v60, v60, v92\n\t"
    "v_add_f32 v61, v61, v93\n\t"
    "v_add_f32 v62, v62, v94\n\t"
    "v_mfma_f32_16x16x32_bf16 v[68:71], v[220:223], v[120:123], v[68:71]\n\t"
    "ds_read_b128 v[116:119], %[ea] offset:30720\n\t"
    "v_add_f32 v63, v63, v95\n\t"
    "v_mul_f32 v40, %[nl], v48\n\t"
    "v_mul_f32 v41, %[nl], v49\n\t"
    "v_mfma_f32_16x16x32_bf16 v[72:75], v[216:219], v[124:127], v[72:75]\n\t"
    "v_xor_b32 %[ea], 0x10000, %[ea]\n\t"
    "v_mul_f32 v42, %[nl], v50\n\t"
    "v_mul_f32 v43, %[nl], v51\n\t"
    "v_mul_f32 v44, %[nl], v56\n\t"
    "v_mfma_f32_16x16x32_bf16 v[76:79], v[220:223], v[124:127], v[76:79]\n\t"
    "v_mul_f32 v45, %[nl], v57\n\t"
    "v_mul_f32 v46, %[nl], v58\n\t"
    "v_mul_f32 v47, %[nl], v59\n\t"
    "v_mfma_f32_16x16x32_bf16 v[64:67], v[224:227], v[96:99], v[64:67]\n\t"
    "ds_read_b128 v[120:123], %[da] offset:28672\n\t"
    "v_exp_f32 v40, v40\n\t"
    "v_exp_f32 v41, v41\n\t"
    "v_exp_f32 v42, v42\n\t"
    "v_mfma_f32_16x16x32_bf16 v[68:71], v[228:231], v[96:99], v[68:71]\n\t"
    "ds_read_b128 v[124:127], %[da] offset:30720\n\t"
    "v_exp_f32 v43, v43\n\t"
    "v_exp_f32 v44, v44\n\t"
    "v_exp_f32 v45, v45\n\t"
    "v_mfma_f32_16x16x32_bf16 v[72:75], v[224:227], v[100:103], v[72:75]\n\t"
    "v_xor_b32 %[da], 0x10000, %[da]\n\t"
    "v_exp_f32 v46, v46\n\t"
    "v_exp_f32 v47, v47\n\t"
    "v_add_f32 v40, 1.0, v40\n\t"
    "v_mfma_f32_16x16x32_bf16 v[76:79], v[228:231], v[100:103], v[76:79]\n\t"
    "v_add_f32 v41, 1.0, v41\n\t"
    "v_add_f32 v42, 1.0, v42\n\t"
    "v_add_f32 v43, 1.0, v43\n\t"
    "s_waitcnt lgkmcnt(5)\n\t"
    "v_mfma_f32_16x16x32_bf16 v[64:67], v[232:235], v[104:107], v[64:67]\n\t"
    "ds_read_b128 v[96:99], %[ea] offset:0\n\t"
    "v_add_f32 v44, 1.0, v44\n\t"
    "v_add_f32 v45, 1.0, v45\n\t"
    "v_add_f32 v46, 1.0, v46\n\t"
    "v_mfma_f32_16x16x32_bf16 v[68:71], v[236:239], v[104:107], v[68:71]\n\t"
    "ds_read_b128 v[100:103], %[ea] offset:2048\n\t"
    "v_add_f32 v47, 1.0, v47\n\t"
    "v_rcp_f32 v40, v40\n\t"
    "v_rcp_f32 v41, v41\n\t"
    "v_mfma_f32_16x16x32_bf16 v[72:75], v[232:235], v[108:111], v[72:75]\n\t"
    "v_rcp_f32 v42, v42\n\t"
    "v_rcp_f32 v43, v43\n\t"
    "v_rcp_f32 v44, v44\n\t"
    "v_mfma_f32_16x16x32_bf16 v[76:79], v[236:239], v[108:111], v[76:79]\n\t"
    "v_rcp_f32 v45, v45\n\t"
    "v_rcp_f32 v46, v46\n\t"
    "v_rcp_f32 v47, v47\n\t"
    "s_waitcnt lgkmcnt(4)\n\t"
    "v_mfma_f32_16x16x32_bf16 v[64:67], v[240:243], v[112:115], v[64:67]\n\t"
    "ds_read_b128 v[104:107], %[da] offset:0\n\t"
    "v_mad_u32_u24 v39, v39, %[mu], %[ad]\n\t"
    "v_mul_f32 v40, v48, v40\n\t"
    "v_mul_f32 v41, v49, v41\n\t"
    "v_mul_f32 v42, v50, v42\n\t"
    "v_mfma_f32_16x16x32_bf16 v[68:71], v[244:247], v[112:115], v[68:71]\n\t"
    "ds_read_b128 v[108:111], %[da] offset:2048\n\t"
    "v_cmp_ge_i32 vcc, %[lf], v39\n\t"
    "s_cmp_eq_u64 vcc, 0\n\t"
    "s_addc_u32 %[lf], %[lf], 0\n\t"
    "v_mul_f32 v43, v51, v43\n\t"
    "v_mul_f32 v44, v56, v44\n\t"
    "v_mul_f32 v45, v57, v45\n\t"
    "v_mfma_f32_16x16x32_bf16 v[72:75], v[240:243], v[116:119], v[72:75]\n\t"
    "v_mul_f32 v46, v58, v46\n\t"
    "v_mul_f32 v47, v59, v47\n\t"
    "v_mul_f32 v40, v40, v52\n\t"
    "v_mfma_f32_16x16x32_bf16 v[76:79], v[244:247], v[116:119], v[76:79]\n\t"
    "v_cmp_ge_i32 vcc, %[lf], v39\n\t"
    "s_cmp_eq_u64 vcc, 0\n\t"
    "s_addc_u32 %[lf], %[lf], 0\n\t"
    "v_mul_f32 v41, v41, v53\n\t"
    "v_mul_f32 v42, v42, v54\n\t"
    "v_mul_f32 v43, v43, v55\n\t"
    "s_waitcnt lgkmcnt(4)\n\t"
    "v_mfma_f32_16x16x32_bf16 v[80:83], v[248:251], v[120:123], v[64:67]\n\t"
    "ds_read_b128 v[112:115], %[ea] offset:4096\n\t"
    "s_min_i32 %[lf], %[lf], %[cp]\n\t"
    "s_add_u32 %[b], %[b], 1\n\t"
    "v_mul_f32 v44, v44, v60\n\t"
    "v_mul_f32 v45, v45, v61\n\t"
    "v_mul_f32 v46, v46, v62\n\t"
    "v_mfma_f32_16x16x32_bf16 v[84:87], v[252:255], v[120:123], v[68:71]\n\t"
    "ds_read_b128 v[116:119], %[ea] offset:6144\n\t"
    "v_mul_f32 v47, v47, v63\n\t"
    "v_cvt_pk_bf16_f32 v48, v40, v41\n\t"
    "v_cvt_pk_bf16_f32 v49, v42, v43\n\t"
    "v_mfma_f32_16x16x32_bf16 v[88:91], v[248:251], v[124:127], v[72:75]\n\t"
    "v_cvt_pk_bf16_f32 v50, v44, v45\n\t"
    "v_cvt_pk_bf16_f32 v51, v46, v47\n\t"
    "s_nop 1\n\t"
    "v_mfma_f32_16x16x32_bf16 v[92:95], v[252:255], v[124:127], v[76:79]\n\t"
    "v_permlane16_swap_b32 v48, v50\n\t"
    "v_permlane16_swap_b32 v49, v51\n\t"
    "global_store_dwordx4 %[co0], v[48:51], s[36:37] sc1\n\t"
    "s_mov_b64 s[36:37], s[38:39]\n\t"
    "s_add_u32 s38, s38, %[ab]\n\t"
    "s_addc_u32 s39, s39, 0\n\t"
    "s_sub_u32 m0, %[lf], %[b]\n\t"
    "s_sub_u32 m0, m0, 1\n\t"
    "s_cmp_lt_u32 m0, %[up]\n\t"
    "s_cbranch_scc1 1b\n\t"
    "s_waitcnt lgkmcnt(0)\n\t"
    "s_nop 4\n\t"
    "s_branch 9f\n\t"
    "5:\n\t"
    "s_add_u32 %[st], %[b], %[po]\n\t"
    "s_max_i32 %[st], %[st], %[p0]\n\t"
    "v_mov_b32 v39, %[st]\n\t"
    "v_mov_b32 v38, 0\n\t"
    "s_mov_b64 exec, 1\n\t"
    "global_store_dword v38, v39, %[pg] sc1\n\t"
    "s_mov_b64 exec, -1\n\t"
    "s_mov_b32 %[p0], %[p1]\n\t"
    "s_branch 6b\n\t"
    "7:\n\t"
    "s_cmp_ge_i32 %[lf], %[cp]\n\t"
    "s_cbranch_scc1 8b\n\t"
    "s_mov_b32 m0, %[sl]\n\t"
    "s_nop 0\n\t"
    "global_load_lds_dword %[dv], %[db] sc1\n\t"
    "s_branch 8b\n\t"
    "9:"
    : "=&{v[80:95]}"(pd), [ea] "+&v"(ea), [da] "+&v"(da), [b] "+&s"(b), [lf] "+&s"(lf), "+{s[36:37]}"(p1), "+{s[38:39]}"(p0), [sa] "+&v"(sa), [st] "=&s"(st), [sl] "+&s"(sl), [p0] "+&s"(pf)
    : [rw] "v"(rw), [ab] "s"(ab), [cp] "s"(cp), [mu] "s"(mu), [ad] "v"(ad), [up] "s"(up), "{v[128:159]}"(W[0]), "{v[160:191]}"(W[1]), "{v[192:223]}"(W[2]), "{v[224:255]}"(W[3]), [co0] "v"(co0), [w0] "s"(w0), [dv] "v"(dv), [db] "s"(db), [pg] "s"(pg), [po] "s"(po - 1), [p1] "s"(b + po), [nl] "s"(-LOG2E)
    : "v38", "v39", "v40", "v41", "v42", "v43", "v44", "v45", "v46", "v47", "v48", "v49", "v50", "v51", "v52", "v53", "v54", "v55", "v56", "v57", "v58", "v59", "v60", "v61", "v62", "v63", "v64", "v65", "v66", "v67", "v68", "v69", "v70", "v71", "v72", "v73", "v74", "v75", "v76", "v77", "v78", "v79", "v96", "v97", "v98", "v99", "v100", "v101", "v102", "v103", "v104", "v105", "v106", "v107", "v108", "v109", "v110", "v111", "v112", "v113", "v114", "v115", "v116", "v117", "v118", "v119", "v120", "v121", "v122", "v123", "v124", "v125", "v126", "v127", "vcc", "scc", "m0", "memory");
}
__device__ __forceinline__ void fast_st2(const u32x32 (&W)[4], u32x16& pd, unsigned ea, unsigned da, int& b, int& lf, int cp, unsigned up, unsigned sa, unsigned mu, unsigned ad,
    unsigned rw, unsigned ab, const char* p1, const char* p0, unsigned co0, unsigned co1, int po, int pf, unsigned w0, unsigned sl, unsigned dv, const char* db, const char* pg) {
  unsigned st;
  asm volatile("s_barrier\n\t"
    "ds_read_b128 v[96:99], %[ea] offset:0\n\t"
    "ds_read_b128 v[100:103], %[ea] offset:2048\n\t"
    "ds_read_b128 v[104:107], %[da] offset:0\n\t"
    "ds_read_b128 v[108:111], %[da] offset:2048\n\t"
    "ds_read_b128 v[112:115], %[ea] offset:4096\n\t"
    "ds_read_b128 v[116:119], %[ea] offset:6144\n\t"
    "s_branch 2f\n\t"
    ".p2align 6\n\t"
    "1:\n\t"
    "s_barrier\n\t"
    "2:\n\t"
    "s_waitcnt lgkmcnt(4)\n\t"
    "v_mfma_f32_16x16x32_bf16 v[64:67], v[128:131], v[96:99], 0\n\t"
    "ds_read_b128 v[120:123], %[da] offset:4096\n\t"
    "v_mfma_f32_16x16x32_bf16 v[68:71], v[132:135], v[96:99], 0\n\t"
    "ds_read_b128 v[124:127], %[da] offset:6144\n\t"
    "v_mfma_f32_16x16x32_bf16 v[72:75], v[128:131], v[100:103], 0\n\t"
    "v_mfma_f32_16x16x32_bf16 v[76:79], v[132:135], v[100:103], 0\n\t"
    "s_waitcnt lgkmcnt(4)\n\t"
    "v_mfma_f32_16x16x32_bf16 v[64:67], v[136:139], v[104:107], v[64:67]\n\t"
    "ds_read_b128 v[96:99], %[ea] offset:8192\n\t"
    "v_mfma_f32_16x16x32_bf16 v[68:71], v[140:143], v[104:107], v[68:71]\n\t"
    "ds_read_b128 v[100:103], %[ea] offset:10240\n\t"
    "v_mfma_f32_16x16x32_bf16 v[72:75], v[136:139], v[108:111], v[72:75]\n\t"
    "v_mfma_f32_16x16x32_bf16 v[76:79], v[140:143], v[108:111], v[76:79]\n\t"
    "s_cmp_eq_u32 %[w0], 1\n\t"
    "s_cbranch_scc1 7f\n\t"
    "8:\n\t"
    "s_waitcnt lgkmcnt(4)\n\t"
    "v_mfma_f32_16x16x32_bf16 v[64:67], v[144:147], v[112:115], v[64:67]\n\t"
    "ds_read_b128 v[104:107], %[da] offset:8192\n\t"
    "v_mfma_f32_16x16x32_bf16 v[68:71], v[148:151], v[112:115], v[68:71]\n\t"
    "ds_read_b128 v[108:111], %[da] offset:10240\n\t"
    "v_mfma_f32_16x16x32_bf16 v[72:75], v[144:147], v[116:119], v[72:75]\n\t"
    "v_mfma_f32_16x16x32_bf16 v[76:79], v[148:151], v[116:119], v[76:79]\n\t"
    "s_waitcnt lgkmcnt(4)\n\t"
    "v_mfma_f32_16x16x32_bf16 v[64:67], v[152:155], v[120:123], v[64:67]\n\t"
    "ds_read_b128 v[112:115], %[ea] offset:12288\n\t"
    "v_mfma_f32_16x16x32_bf16 v[68:71], v[156:159], v[120:123], v[68:71]\n\t"
    "ds_read_b128 v[116:119], %[ea] offset:14336\n\t"
    "v_mfma_f32_16x16x32_bf16 v[72:75], v[152:155], v[124:127], v[72:75]\n\t"
    "v_mfma_f32_16x16x32_bf16 v[76:79], v[156:159], v[124:127], v[76:79]\n\t"
    "s_waitcnt lgkmcnt(4)\n\t"
    "v_mfma_f32_16x16x32_bf16 v[64:67], v[160:163], v[96:99], v[64:67]\n\t"
    "ds_read_b128 v[120:123], %[da] offset:12288\n\t"
    "v_mfma_f32_16x16x32_bf16 v[68:71], v[164:167], v[96:99], v[68:71]\n\t"
    "ds_read_b128 v[124:127], %[da] offset:14336\n\t"
    "v_mfma_f32_16x16x32_bf16 v[72:75], v[160:163], v[100:103], v[72:75]\n\t"
    "v_mfma_f32_16x16x32_bf16 v[76:79], v[164:167], v[100:103], v[76:79]\n\t"
    "s_waitcnt lgkmcnt(4)\n\t"
    "v_mfma_f32_16x16x32_bf16 v[64:67], v[168:171], v[104:107], v[64:67]\n\t"
    "ds_read_b128 v[96:99], %[ea] offset:16384\n\t"
    "v_mfma_f32_16x16x32_bf16 v[68:71], v[172:175], v[104:107], v[68:71]\n\t"
    "ds_read_b128 v[100:103], %[ea] offset:18432\n\t"
    "v_mfma_f32_16x16x32_bf16 v[72:75], v[168:171], v[108:111], v[72:75]\n\t"
    "v_mfma_f32_16x16x32_bf16 v[76:79], v[172:175], v[108:111], v[76:79]\n\t"
    "s_waitcnt lgkmcnt(4)\n\t"
    "v_mfma_f32_16x16x32_bf16 v[64:67], v[176:179], v[112:115], v[64:67]\n\t"
    "ds_read_b128 v[104:107], %[da] offset:16384\n\t"
    "v_mfma_f32_16x16x32_bf16 v[68:71], v[180:183], v[112:115], v[68:71]\n\t"
    "ds_read_b128 v[108:111], %[da] offset:18432\n\t"
    "v_mfma_f32_16x16x32_bf16 v[72:75], v[176:179], v[116:119], v[72:75]\n\t"
    "v_mfma_f32_16x16x32_bf16 v[76:79], v[180:183], v[116:119], v[76:79]\n\t"
    "s_waitcnt lgkmcnt(4)\n\t"
    "v_mfma_f32_16x16x32_bf16 v[64:67], v[184:187], v[120:123], v[64:67]\n\t"
    "ds_read_b128 v[112:115], %[ea] offset:20480\n\t"
    "v_mfma_f32_16x16x32_bf16 v[68:71], v[188:191], v[120:123], v[68:71]\n\t"
    "ds_read_b128 v[116:119], %[ea] offset:22528\n\t"
    "v_mfma_f32_16x16x32_bf16 v[72:75], v[184:187], v[124:127], v[72:75]\n\t"
    "v_mfma_f32_16x16x32_bf16 v[76:79], v[188:191], v[124:127], v[76:79]\n\t"
    "s_waitcnt vmcnt(0)\n\t"
    "s_barrier\n\t"
    "s_waitcnt lgkmcnt(4)\n\t"
    "v_mfma_f32_16x16x32_bf16 v[64:67], v[192:195], v[96:99], v[64:67]\n\t"
    "ds_read_b128 v[120:123], %[da] offset:20480\n\t"
    "v_mfma_f32_16x16x32_bf16 v[68:71], v[196:199], v[96:99], v[68:71]\n\t"
    "ds_read_b128 v[124:127], %[da] offset:22528\n\t"
    "v_mfma_f32_16x16x32_bf16 v[72:75], v[192:195], v[100:103], v[72:75]\n\t"
    "ds_read_b128 v[48:51], %[rw] offset:0\n\t"
    "v_mfma_f32_16x16x32_bf16 v[76:79], v[196:199], v[100:103], v[76:79]\n\t"
    "ds_read_b128 v[52:55], %[rw] offset:1024\n\t"
    "s_cmp_eq_u32 %[w0], 0\n\t"
    "s_cbranch_scc1 5f\n\t"
    "6:\n\t"
    "s_waitcnt lgkmcnt(6)\n\t"
    "v_mfma_f32_16x16x32_bf16 v[64:67], v[200:203], v[104:107], v[64:67]\n\t"
    "ds_read_b128 v[96:99], %[ea] offset:24576\n\t"
    "v_mfma_f32_16x16x32_bf16 v[68:71], v[204:207], v[104:107], v[68:71]\n\t"
    "ds_read_b128 v[100:103], %[ea] offset:26624\n\t"
    "v_mfma_f32_16x16x32_bf16 v[72:75], v[200:203], v[108:111], v[72:75]\n\t"
    "ds_read_b128 v[56:59], %[rw] offset:2048\n\t"
    "v_mfma_f32_16x16x32_bf16 v[76:79], v[204:207], v[108:111], v[76:79]\n\t"
    "ds_read_b128 v[60:63], %[rw] offset:3072\n\t"
    "s_waitcnt lgkmcnt(0)\n\t"
    "v_mfma_f32_16x16x32_bf16 v[64:67], v[208:211], v[112:115], v[64:67]\n\t"
    "ds_read_b128 v[104:107], %[da] offset:24576\n\t"
    "v_cvt_pk_bf16_f32 v40, v80, v81\n\t"
    "v_cvt_pk_bf16_f32 v41, v82, v83\n\t"
    "v_mfma_f32_16x16x32_bf16 v[68:71], v[212:215], v[112:115], v[68:71]\n\t"
    "ds_read_b128 v[108:111], %[da] offset:26624\n\t"
    "v_cvt_pk_bf16_f32 v42, v84, v85\n\t"
    "v_mfma_f32_16x16x32_bf16 v[72:75], v[208:211], v[116:119], v[72:75]\n\t"
    "ds_read_b32 v39, %[sa]\n\t"
    "v_cvt_pk_bf16_f32 v43, v86, v87\n\t"
    "v_mfma_f32_16x16x32_bf16 v[76:79], v[212:215], v[116:119], v[76:79]\n\t"
    "global_store_dwordx4 %[co0], v[40:43], s[36:37] sc1\n\t"
    "v_mfma_f32_16x16x32_bf16 v[64:67], v[216:219], v[120:123], v[64:67]\n\t"
    "ds_read_b128 v[112:115], %[ea] offset:28672\n\t"
    "v_cvt_pk_bf16_f32 v44, v88, v89\n\t"
    "v_cvt_pk_bf16_f32 v45, v90, v91\n\t"
    "v_mfma_f32_16x16x32_bf16 v[68:71], v[220:223], v[120:123], v[68:71]\n\t"
    "ds_read_b128 v[116:119], %[ea] offset:30720\n\t"
    "v_cvt_pk_bf16_f32 v46, v92, v93\n\t"
    "v_mfma_f32_16x16x32_bf16 v[72:75], v[216:219], v[124:127], v[72:75]\n\t"
    "v_xor_b32 %[ea], 0x10000, %[ea]\n\t"
    "v_cvt_pk_bf16_f32 v47, v94, v95\n\t"
    "v_mfma_f32_16x16x32_bf16 v[76:79], v[220:223], v[124:127], v[76:79]\n\t"
    "global_store_dwordx4 %[co1], v[44:47], s[36:37] sc1\n\t"
    "v_mfma_f32_16x16x32_bf16 v[64:67], v[224:227], v[96:99], v[64:67]\n\t"
    "ds_read_b128 v[120:123], %[da] offset:28672\n\t"
    "v_cvt_pk_bf16_f32 v48, v48, v49\n\t"
    "v_cvt_pk_bf16_f32 v49, v50, v51\n\t"
    "v_mfma_f32_16x16x32_bf16 v[68:71], v[228:231], v[96:99], v[68:71]\n\t"
    "ds_read_b128 v[124:127], %[da] offset:30720\n\t"
    "v_cvt_pk_bf16_f32 v50, v52, v53\n\t"
    "v_mfma_f32_16x16x32_bf16 v[72:75], v[224:227], v[100:103], v[72:75]\n\t"
    "v_xor_b32 %[da], 0x10000, %[da]\n\t"
    "v_cvt_pk_bf16_f32 v51, v54, v55\n\t"
    "v_mfma_f32_16x16x32_bf16 v[76:79], v[228:231], v[100:103], v[76:79]\n\t"
    "global_store_dwordx4 %[co0], v[48:51], s[36:37] offset:2048 sc1\n\t"
    "s_waitcnt lgkmcnt(5)\n\t"
    "v_mfma_f32_16x16x32_bf16 v[64:67], v[232:235], v[104:107], v[64:67]\n\t"
    "ds_read_b128 v[96:99], %[ea] offset:0\n\t"
    "v_cvt_pk_bf16_f32 v56, v56, v57\n\t"
    "v_cvt_pk_bf16_f32 v57, v58, v59\n\t"
    "v_mfma_f32_16x16x32_bf16 v[68:71], v[236:239], v[104:107], v[68:71]\n\t"
    "ds_read_b128 v[100:103], %[ea] offset:2048\n\t"
    "v_cvt_pk_bf16_f32 v58, v60, v61\n\t"
    "v_mfma_f32_16x16x32_bf16 v[72:75], v[232:235], v[108:111], v[72:75]\n\t"
    "v_cvt_pk_bf16_f32 v59, v62, v63\n\t"
    "v_mfma_f32_16x16x32_bf16 v[76:79], v[236:239], v[108:111], v[76:79]\n\t"
    "global_store_dwordx4 %[co1], v[56:59], s[36:37] offset:2048 sc1\n\t"
    "s_waitcnt lgkmcnt(4)\n\t"
    "v_mfma_f32_16x16x32_bf16 v[64:67], v[240:243], v[112:115], v[64:67]\n\t"
    "ds_read_b128 v[104:107], %[da] offset:0\n\t"
    "v_mad_u32_u24 v39, v39, %[mu], %[ad]\n\t"
    "v_mfma_f32_16x16x32_bf16 v[68:71], v[244:247], v[112:115], v[68:71]\n\t"
    "ds_read_b128 v[108:111], %[da] offset:2048\n\t"
    "v_cmp_ge_i32 vcc, %[lf], v39\n\t"
    "s_cmp_eq_u64 vcc, 0\n\t"
    "s_addc_u32 %[lf], %[lf], 0\n\t"
    "v_mfma_f32_16x16x32_bf16 v[72:75], v[240:243], v[116:119], v[72:75]\n\t"
    "v_mfma_f32_16x16x32_bf16 v[76:79], v[244:247], v[116:119], v[76:79]\n\t"
    "v_cmp_ge_i32 vcc, %[lf], v39\n\t"
    "s_cmp_eq_u64 vcc, 0\n\t"
    "s_addc_u32 %[lf], %[lf], 0\n\t"
    "s_waitcnt lgkmcnt(4)\n\t"
    "v_mfma_f32_16x16x32_bf16 v[80:83], v[248:251], v[120:123], v[64:67]\n\t"
    "ds_read_b128 v[112:115], %[ea] offset:4096\n\t"
    "s_min_i32 %[lf], %[lf], %[cp]\n\t"
    "s_add_u32 %[b], %[b], 1\n\t"
    "v_mfma_f32_16x16x32_bf16 v[84:87], v[252:255], v[120:123], v[68:71]\n\t"
    "ds_read_b128 v[116:119], %[ea] offset:6144\n\t"
    "v_mfma_f32_16x16x32_bf16 v[88:91], v[248:251], v[124:127], v[72:75]\n\t"
    "v_mfma_f32_16x16x32_bf16 v[92:95], v[252:255], v[124:127], v[76:79]\n\t"
    "s_mov_b64 s[36:37], s[38:39]\n\t"
    "s_add_u32 s38, s38, %[ab]\n\t"
    "s_addc_u32 s39, s39, 0\n\t"
    "s_sub_u32 m0, %[lf], %[b]\n\t"
    "s_sub_u32 m0, m0, 1\n\t"
    "s_cmp_lt_u32 m0, %[up]\n\t"
    "s_cbranch_scc1 1b\n\t"
    "s_waitcnt lgkmcnt(0)\n\t"
    "s_nop 4\n\t"
    "s_branch 9f\n\t"
    "5:\n\t"
    "s_add_u32 %[st], %[b], %[po]\n\t"
    "s_max_i32 %[st], %[st], %[p0]\n\t"
    "v_mov_b32 v39, %[st]\n\t"
    "v_mov_b32 v38, 0\n\t"
    "s_mov_b64 exec, 1\n\t"
    "global_store_dword v38, v39, %[pg] sc1\n\t"
    "s_mov_b64 exec, -1\n\t"
    "s_mov_b32 %[p0], %[p1]\n\t"
    "s_branch 6b\n\t"
    "7:\n\t"
    "s_cmp_ge_i32 %[lf], %[cp]\n\t"
    "s_cbranch_scc1 8b\n\t"
    "s_mov_b32 m0, %[sl]\n\t"
    "s_nop 0\n\t"
    "global_load_lds_dword %[dv], %[db] sc1\n\t"
    "s_branch 8b\n\t"
    "9:"
    : "=&{v[80:95]}"(pd), [ea] "+&v"(ea), [da] "+&v"(da), [b] "+&s"(b), [lf] "+&s"(lf), "+{s[36:37]}"(p1), "+{s[38:39]}"(p0), [sa] "+&v"(sa), [st] "=&s"(st), [sl] "+&s"(sl), [p0] "+&s"(pf)
    : [rw] "v"(rw), [ab] "s"(ab), [cp] "s"(cp), [mu] "s"(mu), [ad] "v"(ad), [up] "s"(up), "{v[128:159]}"(W[0]), "{v[160:191]}"(W[1]), "{v[192:223]}"(W[2]), "{v[224:255]}"(W[3]), [co0] "v"(co0), [w0] "s"(w0), [dv] "v"(dv), [db] "s"(db), [pg] "s"(pg), [po] "s"(po - 1), [p1] "s"(b + po), [co1] "v"(co1)
    : "v38", "v39", "v40", "v41", "v42", "v43", "v44", "v45", "v46", "v47", "v48", "v49", "v50", "v51", "v52", "v53", "v54", "v55", "v56", "v57", "v58", "v59", "v60", "v61", "v62", "v63", "v64", "v65", "v66", "v67", "v68", "v69", "v70", "v71", "v72", "v73", "v74", "v75", "v76", "v77", "v78", "v79", "v96", "v97", "v98", "v99", "v100", "v101", "v102", "v103", "v104", "v105", "v106", "v107", "v108", "v109", "v110", "v111", "v112", "v113", "v114", "v115", "v116", "v117", "v118", "v119", "v120", "v121", "v122", "v123", "v124", "v125", "v126", "v127", "vcc", "scc", "m0", "memory");
}
__device__ __forceinline__ void lean_ld0(const u32x32 (&W)[4], unsigned ea, unsigned da, unsigned x, unsigned lb, const char* s0, const char* s1, const unsigned (&av)[8], unsigned ps, unsigned dv, const char* db, unsigned rw, unsigned fa, unsigned fv,
    unsigned ga, unsigned mu, unsigned ad, int th, int tb, unsigned& sv, unsigned& su, unsigned& got) {
  asm volatile("s_setprio 2\n\t"
    "v_mov_b32 v76, %[ga]\n\t"
    "ds_read_b32 v76, v76 offset:24\n\t"
    "ds_read_b128 v[96:99], %[ea] offset:0\n\t"
    "ds_read_b128 v[100:103], %[ea] offset:2048\n\t"
    "ds_read_b128 v[104:107], %[da] offset:0\n\t"
    "ds_read_b128 v[108:111], %[da] offset:2048\n\t"
    "ds_read_b128 v[112:115], %[ea] offset:4096\n\t"
    "ds_read_b128 v[116:119], %[ea] offset:6144\n\t"
    "s_waitcnt lgkmcnt(6)\n\t"
    "v_cmp_ne_u32 vcc, 0, v76\n\t"
    "s_mov_b32 %[got], -1\n\t"
    "s_cbranch_vccnz 12f\n\t"
    "s_waitcnt lgkmcnt(4)\n\t"
    "v_mfma_f32_16x16x32_bf16 v[80:83], v[128:131], v[96:99], 0\n\t"
    "ds_read_b128 v[120:123], %[da] offset:4096\n\t"
    "v_mfma_f32_16x16x32_bf16 v[84:87], v[132:135], v[96:99], 0\n\t"
    "ds_read_b128 v[124:127], %[da] offset:6144\n\t"
    "v_mfma_f32_16x16x32_bf16 v[88:91], v[128:131], v[100:103], 0\n\t"
    "v_mfma_f32_16x16x32_bf16 v[92:95], v[132:135], v[100:103], 0\n\t"
    "s_waitcnt vmcnt(24)\n\t"
    "s_cmp_lt_u32 %[x], 2\n\t"
    "s_cbranch_scc0 41f\n\t"
    "s_waitcnt vmcnt(8)\n\t"
    "41:\n\t"
    "s_barrier\n\t"
    "s_waitcnt lgkmcnt(4)\n\t"
    "v_mfma_f32_16x16x32_bf16 v[80:83], v[136:139], v[104:107], v[80:83]\n\t"
    "ds_read_b128 v[96:99], %[ea] offset:8192\n\t"
    "v_mfma_f32_16x16x32_bf16 v[84:87], v[140:143], v[104:107], v[84:87]\n\t"
    "ds_read_b128 v[100:103], %[ea] offset:10240\n\t"
    "v_mfma_f32_16x16x32_bf16 v[88:91], v[136:139], v[108:111], v[88:91]\n\t"
    "v_mfma_f32_16x16x32_bf16 v[92:95], v[140:143], v[108:111], v[92:95]\n\t"
    "s_waitcnt lgkmcnt(4)\n\t"
    "v_mfma_f32_16x16x32_bf16 v[80:83], v[144:147], v[112:115], v[80:83]\n\t"
    "ds_read_b128 v[104:107], %[da] offset:8192\n\t"
    "v_mfma_f32_16x16x32_bf16 v[84:87], v[148:151], v[112:115], v[84:87]\n\t"
    "ds_read_b128 v[108:111], %[da] offset:10240\n\t"
    "v_mfma_f32_16x16x32_bf16 v[88:91], v[144:147], v[116:119], v[88:91]\n\t"
    "v_mfma_f32_16x16x32_bf16 v[92:95], v[148:151], v[116:119], v[92:95]\n\t"
    "s_waitcnt lgkmcnt(4)\n\t"
    "v_mfma_f32_16x16x32_bf16 v[80:83], v[152:155], v[120:123], v[80:83]\n\t"
    "ds_read_b128 v[112:115], %[ea] offset:12288\n\t"
    "v_mfma_f32_16x16x32_bf16 v[84:87], v[156:159], v[120:123], v[84:87]\n\t"
    "ds_read_b128 v[116:119], %[ea] offset:14336\n\t"
    "v_mfma_f32_16x16x32_bf16 v[88:91], v[152:155], v[124:127], v[88:91]\n\t"
    "v_mfma_f32_16x16x32_bf16 v[92:95], v[156:159], v[124:127], v[92:95]\n\t"
    "s_waitcnt lgkmcnt(4)\n\t"
    "v_mfma_f32_16x16x32_bf16 v[80:83], v[160:163], v[96:99], v[80:83]\n\t"
    "ds_read_b128 v[120:123], %[da] offset:12288\n\t"
    "v_mfma_f32_16x16x32_bf16 v[84:87], v[164:167], v[96:99], v[84:87]\n\t"
    "ds_read_b128 v[124:127], %[da] offset:14336\n\t"
    "v_mfma_f32_16x16x32_bf16 v[88:91], v[160:163], v[100:103], v[88:91]\n\t"
    "v_mfma_f32_16x16x32_bf16 v[92:95], v[164:167], v[100:103], v[92:95]\n\t"
    "s_waitcnt vmcnt(20)\n\t"
    "s_cmp_lt_u32 %[x], 2\n\t"
    "s_cbranch_scc0 45f\n\t"
    "s_waitcnt vmcnt(4)\n\t"
    "45:\n\t"
    "s_barrier\n\t"
    "s_waitcnt lgkmcnt(4)\n\t"
    "v_mfma_f32_16x16x32_bf16 v[80:83], v[168:171], v[104:107], v[80:83]\n\t"
    "ds_read_b128 v[96:99], %[ea] offset:16384\n\t"
    "v_mfma_f32_16x16x32_bf16 v[84:87], v[172:175], v[104:107], v[84:87]\n\t"
    "ds_read_b128 v[100:103], %[ea] offset:18432\n\t"
    "v_mfma_f32_16x16x32_bf16 v[88:91], v[168:171], v[108:111], v[88:91]\n\t"
    "v_mfma_f32_16x16x32_bf16 v[92:95], v[172:175], v[108:111], v[92:95]\n\t"
    "s_waitcnt lgkmcnt(4)\n\t"
    "v_mfma_f32_16x16x32_bf16 v[80:83], v[176:179], v[112:115], v[80:83]\n\t"
    "ds_read_b128 v[104:107], %[da] offset:16384\n\t"
    "v_mfma_f32_16x16x32_bf16 v[84:87], v[180:183], v[112:115], v[84:87]\n\t"
    "ds_read_b128 v[108:111], %[da] offset:18432\n\t"
    "v_mfma_f32_16x16x32_bf16 v[88:91], v[176:179], v[116:119], v[88:91]\n\t"
    "v_mfma_f32_16x16x32_bf16 v[92:95], v[180:183], v[116:119], v[92:95]\n\t"
    "s_waitcnt lgkmcnt(4)\n\t"
    "v_mfma_f32_16x16x32_bf16 v[80:83], v[184:187], v[120:123], v[80:83]\n\t"
    "ds_read_b128 v[112:115], %[ea] offset:20480\n\t"
    "v_mfma_f32_16x16x32_bf16 v[84:87], v[188:191], v[120:123], v[84:87]\n\t"
    "ds_read_b128 v[116:119], %[ea] offset:22528\n\t"
    "v_mfma_f32_16x16x32_bf16 v[88:91], v[184:187], v[124:127], v[88:91]\n\t"
    "v_mfma_f32_16x16x32_bf16 v[92:95], v[188:191], v[124:127], v[92:95]\n\t"
    "s_waitcnt lgkmcnt(4)\n\t"
    "v_mfma_f32_16x16x32_bf16 v[80:83], v[192:195], v[96:99], v[80:83]\n\t"
    "ds_read_b128 v[120:123], %[da] offset:20480\n\t"
    "v_mfma_f32_16x16x32_bf16 v[84:87], v[196:199], v[96:99], v[84:87]\n\t"
    "ds_read_b128 v[124:127], %[da] offset:22528\n\t"
    "v_mfma_f32_16x16x32_bf16 v[88:91], v[192:195], v[100:103], v[88:91]\n\t"
    "v_mfma_f32_16x16x32_bf16 v[92:95], v[196:199], v[100:103], v[92:95]\n\t"
    "s_waitcnt vmcnt(16)\n\t"
    "s_cmp_lt_u32 %[x], 2\n\t"
    "s_cbranch_scc0 49f\n\t"
    "s_waitcnt vmcnt(0)\n\t"
    "49:\n\t"
    "s_barrier\n\t"
    "s_waitcnt lgkmcnt(4)\n\t"
    "v_mfma_f32_16x16x32_bf16 v[80:83], v[200:203], v[104:107], v[80:83]\n\t"
    "ds_read_b128 v[96:99], %[ea] offset:24576\n\t"
    "v_mfma_f32_16x16x32_bf16 v[84:87], v[204:207], v[104:107], v[84:87]\n\t"
    "ds_read_b128 v[100:103], %[ea] offset:26624\n\t"
    "v_mfma_f32_16x16x32_bf16 v[88:91], v[200:203], v[108:111], v[88:91]\n\t"
    "v_mfma_f32_16x16x32_bf16 v[92:95], v[204:207], v[108:111], v[92:95]\n\t"
    "s_waitcnt lgkmcnt(4)\n\t"
    "v_mfma_f32_16x16x32_bf16 v[80:83], v[208:211], v[112:115], v[80:83]\n\t"
    "ds_read_b128 v[104:107], %[da] offset:24576\n\t"
    "v_mfma_f32_16x16x32_bf16 v[84:87], v[212:215], v[112:115], v[84:87]\n\t"
    "ds_read_b128 v[108:111], %[da] offset:26624\n\t"
    "v_mfma_f32_16x16x32_bf16 v[88:91], v[208:211], v[116:119], v[88:91]\n\t"
    "v_mfma_f32_16x16x32_bf16 v[92:95], v[212:215], v[116:119], v[92:95]\n\t"
    "s_cmp_eq_u32 %[ps], 0\n\t"
    "s_cbranch_scc1 10f\n\t"
    "global_load_dword %[sv], %[dv], %[db] sc1\n\t"
    "global_load_dword %[su], %[dv], %[db] offset:64 sc1\n\t"
    "10:\n\t"
    "s_waitcnt lgkmcnt(4)\n\t"
    "v_mfma_f32_16x16x32_bf16 v[80:83], v[216:219], v[120:123], v[80:83]\n\t"
    "ds_read_b128 v[112:115], %[ea] offset:28672\n\t"
    "v_mfma_f32_16x16x32_bf16 v[84:87], v[220:223], v[120:123], v[84:87]\n\t"
    "ds_read_b128 v[116:119], %[ea] offset:30720\n\t"
    "v_mfma_f32_16x16x32_bf16 v[88:91], v[216:219], v[124:127], v[88:91]\n\t"
    "v_mfma_f32_16x16x32_bf16 v[92:95], v[220:223], v[124:127], v[92:95]\n\t"
    "s_waitcnt lgkmcnt(4)\n\t"
    "v_mfma_f32_16x16x32_bf16 v[80:83], v[224:227], v[96:99], v[80:83]\n\t"
    "ds_read_b128 v[120:123], %[da] offset:28672\n\t"
    "v_mfma_f32_16x16x32_bf16 v[84:87], v[228:231], v[96:99], v[84:87]\n\t"
    "ds_read_b128 v[124:127], %[da] offset:30720\n\t"
    "v_mfma_f32_16x16x32_bf16 v[88:91], v[224:227], v[100:103], v[88:91]\n\t"
    "v_mfma_f32_16x16x32_bf16 v[92:95], v[228:231], v[100:103], v[92:95]\n\t"
    "s_waitcnt lgkmcnt(4)\n\t"
    "v_mfma_f32_16x16x32_bf16 v[80:83], v[232:235], v[104:107], v[80:83]\n\t"
    "v_mfma_f32_16x16x32_bf16 v[84:87], v[236:239], v[104:107], v[84:87]\n\t"
    "v_mfma_f32_16x16x32_bf16 v[88:91], v[232:235], v[108:111], v[88:91]\n\t"
    "v_mfma_f32_16x16x32_bf16 v[92:95], v[236:239], v[108:111], v[92:95]\n\t"
    "s_waitcnt lgkmcnt(2)\n\t"
    "v_mfma_f32_16x16x32_bf16 v[80:83], v[240:243], v[112:115], v[80:83]\n\t"
    "v_mfma_f32_16x16x32_bf16 v[84:87], v[244:247], v[112:115], v[84:87]\n\t"
    "v_mfma_f32_16x16x32_bf16 v[88:91], v[240:243], v[116:119], v[88:91]\n\t"
    "v_mfma_f32_16x16x32_bf16 v[92:95], v[244:247], v[116:119], v[92:95]\n\t"
    "s_waitcnt lgkmcnt(0)\n\t"
    "v_mfma_f32_16x16x32_bf16 v[80:83], v[248:251], v[120:123], v[80:83]\n\t"
    "s_memrealtime s[36:37]\n\t"
    "v_mfma_f32_16x16x32_bf16 v[84:87], v[252:255], v[120:123], v[84:87]\n\t"
    "v_mfma_f32_16x16x32_bf16 v[88:91], v[248:251], v[124:127], v[88:91]\n\t"
    "v_mfma_f32_16x16x32_bf16 v[92:95], v[252:255], v[124:127], v[92:95]\n\t"
    "s_mov_b32 %[got], 0\n\t"
    "v_mov_b32 v76, %[fa]\n\t"
    "v_mov_b32 v77, %[fv]\n\t"
    "v_add_u32 v78, 1, v77\n\t"
    "v_mov_b32 v79, %[ga]\n\t"
    "s_waitcnt lgkmcnt(0)\n\t"
    "ds_write_b128 %[rw], v[80:83] offset:0\n\t"
    "ds_write_b128 %[rw], v[84:87] offset:1024\n\t"
    "ds_write_b128 %[rw], v[88:91] offset:2048\n\t"
    "ds_write_b128 %[rw], v[92:95] offset:3072\n\t"
    "ds_write_b32 v76, v77\n\t"
    "s_cmp_eq_u32 %[ps], 0\n\t"
    "s_cbranch_scc1 9f\n\t"
    "s_waitcnt vmcnt(0)\n\t"
    "v_mad_u32_u24 v76, %[sv], %[mu], %[ad]\n\t"
    "v_cmp_ge_i32 vcc, %[th], v76\n\t"
    "s_cbranch_vccz 11f\n\t"
    "s_sub_u32 s36, s36, 20\n\t"
    "v_sub_u32 v76, s36, %[su]\n\t"
    "v_lshrrev_b32 v77, 24, %[su]\n\t"
    "v_lshlrev_b32 v76, 8, v76\n\t"
    "v_ashrrev_i32 v76, 31, v76\n\t"
    "v_add_u32 v77, v77, v76\n\t"
    "v_cmp_lt_i32 s[36:37], %[tb], v77\n\t"
    "s_andn2_b64 vcc, vcc, s[36:37]\n\t"
    "s_cbranch_scc1 9f\n\t"
    "11:\n\t"
    "ds_write2_b32 v79, v78, v78 offset1:1\n\t"
    "s_add_u32 m0, %[lb], 0\n\t"
    "s_nop 0\n\t"
    "global_load_lds_dwordx4 %[av0], %[s0] sc1\n\t"
    "s_add_u32 m0, %[lb], 32768\n\t"
    "s_nop 0\n\t"
    "global_load_lds_dwordx4 %[av1], %[s0] sc1\n\t"
    "s_add_u32 m0, %[lb], 4096\n\t"
    "s_nop 0\n\t"
    "global_load_lds_dwordx4 %[av2], %[s0] sc1\n\t"
    "s_add_u32 m0, %[lb], 36864\n\t"
    "s_nop 0\n\t"
    "global_load_lds_dwordx4 %[av3], %[s0] sc1\n\t"
    "s_add_u32 m0, %[lb], 8192\n\t"
    "s_nop 0\n\t"
    "global_load_lds_dwordx4 %[av4], %[s0] sc1\n\t"
    "s_add_u32 m0, %[lb], 40960\n\t"
    "s_nop 0\n\t"
    "global_load_lds_dwordx4 %[av5], %[s0] sc1\n\t"
    "s_add_u32 m0, %[lb], 12288\n\t"
    "s_nop 0\n\t"
    "global_load_lds_dwordx4 %[av6], %[s0] sc1\n\t"
    "s_add_u32 m0, %[lb], 45056\n\t"
    "s_nop 0\n\t"
    "global_load_lds_dwordx4 %[av7], %[s0] sc1\n\t"
    "s_add_u32 m0, %[lb], 16384\n\t"
    "s_nop 0\n\t"
    "global_load_lds_dwordx4 %[av0], %[s1] sc1\n\t"
    "s_add_u32 m0, %[lb], 49152\n\t"
    "s_nop 0\n\t"
    "global_load_lds_dwordx4 %[av1], %[s1] sc1\n\t"
    "s_add_u32 m0, %[lb], 20480\n\t"
    "s_nop 0\n\t"
    "global_load_lds_dwordx4 %[av2], %[s1] sc1\n\t"
    "s_add_u32 m0, %[lb], 53248\n\t"
    "s_nop 0\n\t"
    "global_load_lds_dwordx4 %[av3], %[s1] sc1\n\t"
    "s_add_u32 m0, %[lb], 24576\n\t"
    "s_nop 0\n\t"
    "global_load_lds_dwordx4 %[av4], %[s1] sc1\n\t"
    "s_add_u32 m0, %[lb], 57344\n\t"
    "s_nop 0\n\t"
    "global_load_lds_dwordx4 %[av5], %[s1] sc1\n\t"
    "s_add_u32 m0, %[lb], 28672\n\t"
    "s_nop 0\n\t"
    "global_load_lds_dwordx4 %[av6], %[s1] sc1\n\t"
    "s_add_u32 m0, %[lb], 61440\n\t"
    "s_nop 0\n\t"
    "global_load_lds_dwordx4 %[av7], %[s1] sc1\n\t"
    "s_mov_b32 %[got], 1\n\t"
    "s_branch 9f\n\t"
    "12:\n\t"
    "s_waitcnt lgkmcnt(0)\n\t"
    "9:"
    : [sv] "+&v"(sv), [su] "+&v"(su), [got] "=&s"(got)
    : [ea] "v"(ea), [da] "v"(da), [x] "s"(x), "{v[128:159]}"(W[0]), "{v[160:191]}"(W[1]), "{v[192:223]}"(W[2]), "{v[224:255]}"(W[3]), [lb] "s"(lb), [s0] "s"(s0), [s1] "s"(s1), [av0] "v"(av[0]), [av1] "v"(av[1]), [av2] "v"(av[2]), [av3] "v"(av[3]), [av4] "v"(av[4]), [av5] "v"(av[5]), [av6] "v"(av[6]), [av7] "v"(av[7]), [ps] "s"(ps), [dv] "v"(dv), [db] "s"(db), [rw] "v"(rw), [fa] "s"(fa), [fv] "s"(fv), [ga] "s"(ga), [mu] "s"(mu), [ad] "v"(ad), [th] "s"(th), [tb] "s"(tb)
    : "v96", "v97", "v98", "v99", "v100", "v101", "v102", "v103", "v104", "v105", "v106", "v107", "v108", "v109", "v110", "v111", "v112", "v113", "v114", "v115", "v116", "v117", "v118", "v119", "v120", "v121", "v122", "v123", "v124", "v125", "v126", "v127", "v76", "v77", "v78", "v79", "v80", "v81", "v82", "v83", "v84", "v85", "v86", "v87", "v88", "v89", "v90", "v91", "v92", "v93", "v94", "v95", "vcc", "scc", "m0", "memory", "s36", "s37", "s38", "s39");
}
__device__ __forceinline__ void lean_dm0(const u32x32 (&W)[4], unsigned ea, unsigned da, unsigned x, unsigned lb, const char* s0, const char* s1, const unsigned (&av)[8], unsigned ps, unsigned dv, const char* db, unsigned rw, unsigned fa, unsigned fv,
    unsigned ga, unsigned mu, unsigned ad, int th, int tb, unsigned& sv, unsigned& su, unsigned& got) {
  asm volatile("s_setprio 2\n\t"
    "v_mov_b32 v76, %[ga]\n\t"
    "ds_read_b32 v76, v76 offset:24\n\t"
    "ds_read_b128 v[96:99], %[ea] offset:0\n\t"
    "ds_read_b128 v[100:103], %[ea] offset:2048\n\t"
    "ds_read_b128 v[104:107], %[da] offset:0\n\t"
    "ds_read_b128 v[108:111], %[da] offset:2048\n\t"
    "ds_read_b128 v[112:115], %[ea] offset:4096\n\t"
    "ds_read_b128 v[116:119], %[ea] offset:6144\n\t"
    "s_waitcnt lgkmcnt(6)\n\t"
    "v_cmp_ne_u32 vcc, 0, v76\n\t"
    "s_mov_b32 %[got], -1\n\t"
    "s_cbranch_vccnz 12f\n\t"
    "s_waitcnt lgkmcnt(4)\n\t"
    "v_mfma_f32_16x16x32_bf16 v[80:83], v[128:131], v[96:99], 0\n\t"
    "ds_read_b128 v[120:123], %[da] offset:4096\n\t"
    "s_add_u32 m0, %[lb], 0\n\t"
    "v_mfma_f32_16x16x32_bf16 v[84:87], v[132:135], v[96:99], 0\n\t"
    "ds_read_b128 v[124:127], %[da] offset:6144\n\t"
    "global_load_lds_dwordx4 %[av0], %[s0] sc1\n\t"
    "v_mfma_f32_16x16x32_bf16 v[88:91], v[128:131], v[100:103], 0\n\t"
    "s_add_u32 m0, %[lb], 32768\n\t"
    "v_mfma_f32_16x16x32_bf16 v[92:95], v[132:135], v[100:103], 0\n\t"
    "global_load_lds_dwordx4 %[av1], %[s0] sc1\n\t"
    "s_waitcnt vmcnt(10)\n\t"
    "s_barrier\n\t"
    "s_waitcnt lgkmcnt(4)\n\t"
    "v_mfma_f32_16x16x32_bf16 v[80:83], v[136:139], v[104:107], v[80:83]\n\t"
    "ds_read_b128 v[96:99], %[ea] offset:8192\n\t"
    "s_add_u32 m0, %[lb], 4096\n\t"
    "v_mfma_f32_16x16x32_bf16 v[84:87], v[140:143], v[104:107], v[84:87]\n\t"
    "ds_read_b128 v[100:103], %[ea] offset:10240\n\t"
    "global_load_lds_dwordx4 %[av2], %[s0] sc1\n\t"
    "v_mfma_f32_16x16x32_bf16 v[88:91], v[136:139], v[108:111], v[88:91]\n\t"
    "s_add_u32 m0, %[lb], 36864\n\t"
    "v_mfma_f32_16x16x32_bf16 v[92:95], v[140:143], v[108:111], v[92:95]\n\t"
    "global_load_lds_dwordx4 %[av3], %[s0] sc1\n\t"
    "s_waitcnt lgkmcnt(4)\n\t"
    "v_mfma_f32_16x16x32_bf16 v[80:83], v[144:147], v[112:115], v[80:83]\n\t"
    "ds_read_b128 v[104:107], %[da] offset:8192\n\t"
    "s_add_u32 m0, %[lb], 8192\n\t"
    "v_mfma_f32_16x16x32_bf16 v[84:87], v[148:151], v[112:115], v[84:87]\n\t"
    "ds_read_b128 v[108:111], %[da] offset:10240\n\t"
    "global_load_lds_dwordx4 %[av4], %[s0] sc1\n\t"
    "v_mfma_f32_16x16x32_bf16 v[88:91], v[144:147], v[116:119], v[88:91]\n\t"
    "s_add_u32 m0, %[lb], 40960\n\t"
    "v_mfma_f32_16x16x32_bf16 v[92:95], v[148:151], v[116:119], v[92:95]\n\t"
    "global_load_lds_dwordx4 %[av5], %[s0] sc1\n\t"
    "s_waitcnt lgkmcnt(4)\n\t"
    "v_mfma_f32_16x16x32_bf16 v[80:83], v[152:155], v[120:123], v[80:83]\n\t"
    "ds_read_b128 v[112:115], %[ea] offset:12288\n\t"
    "s_add_u32 m0, %[lb], 12288\n\t"
    "v_mfma_f32_16x16x32_bf16 v[84:87], v[156:159], v[120:123], v[84:87]\n\t"
    "ds_read_b128 v[116:119], %[ea] offset:14336\n\t"
    "global_load_lds_dwordx4 %[av6], %[s0] sc1\n\t"
    "v_mfma_f32_16x16x32_bf16 v[88:91], v[152:155], v[124:127], v[88:91]\n\t"
    "s_add_u32 m0, %[lb], 45056\n\t"
    "v_mfma_f32_16x16x32_bf16 v[92:95], v[156:159], v[124:127], v[92:95]\n\t"
    "global_load_lds_dwordx4 %[av7], %[s0] sc1\n\t"
    "s_waitcnt lgkmcnt(4)\n\t"
    "v_mfma_f32_16x16x32_bf16 v[80:83], v[160:163], v[96:99], v[80:83]\n\t"
    "ds_read_b128 v[120:123], %[da] offset:12288\n\t"
    "s_add_u32 m0, %[lb], 16384\n\t"
    "v_mfma_f32_16x16x32_bf16 v[84:87], v[164:167], v[96:99], v[84:87]\n\t"
    "ds_read_b128 v[124:127], %[da] offset:14336\n\t"
    "global_load_lds_dwordx4 %[av0], %[s1] sc1\n\t"
    "v_mfma_f32_16x16x32_bf16 v[88:91], v[160:163], v[100:103], v[88:91]\n\t"
    "s_add_u32 m0, %[lb], 49152\n\t"
    "v_mfma_f32_16x16x32_bf16 v[92:95], v[164:167], v[100:103], v[92:95]\n\t"
    "global_load_lds_dwordx4 %[av1], %[s1] sc1\n\t"
    "s_waitcnt vmcnt(14)\n\t"
    "s_barrier\n\t"
    "s_waitcnt lgkmcnt(4)\n\t"
    "v_mfma_f32_16x16x32_bf16 v[80:83], v[168:171], v[104:107], v[80:83]\n\t"
    "ds_read_b128 v[96:99], %[ea] offset:16384\n\t"
    "s_add_u32 m0, %[lb], 20480\n\t"
    "v_mfma_f32_16x16x32_bf16 v[84:87], v[172:175], v[104:107], v[84:87]\n\t"
    "ds_read_b128 v[100:103], %[ea] offset:18432\n\t"
    "global_load_lds_dwordx4 %[av2], %[s1] sc1\n\t"
    "v_mfma_f32_16x16x32_bf16 v[88:91], v[168:171], v[108:111], v[88:91]\n\t"
    "s_add_u32 m0, %[lb], 53248\n\t"
    "v_mfma_f32_16x16x32_bf16 v[92:95], v[172:175], v[108:111], v[92:95]\n\t"
    "global_load_lds_dwordx4 %[av3], %[s1] sc1\n\t"
    "s_waitcnt lgkmcnt(4)\n\t"
    "v_mfma_f32_16x16x32_bf16 v[80:83], v[176:179], v[112:115], v[80:83]\n\t"
    "ds_read_b128 v[104:107], %[da] offset:16384\n\t"
    "s_add_u32 m0, %[lb], 24576\n\t"
    "v_mfma_f32_16x16x32_bf16 v[84:87], v[180:183], v[112:115], v[84:87]\n\t"
    "ds_read_b128 v[108:111], %[da] offset:18432\n\t"
    "global_load_lds_dwordx4 %[av4], %[s1] sc1\n\t"
    "v_mfma_f32_16x16x32_bf16 v[88:91], v[176:179], v[116:119], v[88:91]\n\t"
    "s_add_u32 m0, %[lb], 57344\n\t"
    "v_mfma_f32_16x16x32_bf16 v[92:95], v[180:183], v[116:119], v[92:95]\n\t"
    "global_load_lds_dwordx4 %[av5], %[s1] sc1\n\t"
    "s_waitcnt lgkmcnt(4)\n\t"
    "v_mfma_f32_16x16x32_bf16 v[80:83], v[184:187], v[120:123], v[80:83]\n\t"
    "ds_read_b128 v[112:115], %[ea] offset:20480\n\t"
    "s_add_u32 m0, %[lb], 28672\n\t"
    "v_mfma_f32_16x16x32_bf16 v[84:87], v[188:191], v[120:123], v[84:87]\n\t"
    "ds_read_b128 v[116:119], %[ea] offset:22528\n\t"
    "global_load_lds_dwordx4 %[av6], %[s1] sc1\n\t"
    "v_mfma_f32_16x16x32_bf16 v[88:91], v[184:187], v[124:127], v[88:91]\n\t"
    "s_add_u32 m0, %[lb], 61440\n\t"
    "v_mfma_f32_16x16x32_bf16 v[92:95], v[188:191], v[124:127], v[92:95]\n\t"
    "global_load_lds_dwordx4 %[av7], %[s1] sc1\n\t"
    "s_waitcnt lgkmcnt(4)\n\t"
    "v_mfma_f32_16x16x32_bf16 v[80:83], v[192:195], v[96:99], v[80:83]\n\t"
    "ds_read_b128 v[120:123], %[da] offset:20480\n\t"
    "v_mfma_f32_16x16x32_bf16 v[84:87], v[196:199], v[96:99], v[84:87]\n\t"
    "ds_read_b128 v[124:127], %[da] offset:22528\n\t"
    "v_mfma_f32_16x16x32_bf16 v[88:91], v[192:195], v[100:103], v[88:91]\n\t"
    "v_mfma_f32_16x16x32_bf16 v[92:95], v[196:199], v[100:103], v[92:95]\n\t"
    "s_waitcnt vmcnt(16)\n\t"
    "s_barrier\n\t"
    "s_waitcnt lgkmcnt(4)\n\t"
    "v_mfma_f32_16x16x32_bf16 v[80:83], v[200:203], v[104:107], v[80:83]\n\t"
    "ds_read_b128 v[96:99], %[ea] offset:24576\n\t"
    "v_mfma_f32_16x16x32_bf16 v[84:87], v[204:207], v[104:107], v[84:87]\n\t"
    "ds_read_b128 v[100:103], %[ea] offset:26624\n\t"
    "v_mfma_f32_16x16x32_bf16 v[88:91], v[200:203], v[108:111], v[88:91]\n\t"
    "v_mfma_f32_16x16x32_bf16 v[92:95], v[204:207], v[108:111], v[92:95]\n\t"
    "s_waitcnt lgkmcnt(4)\n\t"
    "v_mfma_f32_16x16x32_bf16 v[80:83], v[208:211], v[112:115], v[80:83]\n\t"
    "ds_read_b128 v[104:107], %[da] offset:24576\n\t"
    "v_mfma_f32_16x16x32_bf16 v[84:87], v[212:215], v[112:115], v[84:87]\n\t"
    "ds_read_b128 v[108:111], %[da] offset:26624\n\t"
    "v_mfma_f32_16x16x32_bf16 v[88:91], v[208:211], v[116:119], v[88:91]\n\t"
    "v_mfma_f32_16x16x32_bf16 v[92:95], v[212:215], v[116:119], v[92:95]\n\t"
    "s_waitcnt lgkmcnt(4)\n\t"
    "v_mfma_f32_16x16x32_bf16 v[80:83], v[216:219], v[120:123], v[80:83]\n\t"
    "ds_read_b128 v[112:115], %[ea] offset:28672\n\t"
    "v_mfma_f32_16x16x32_bf16 v[84:87], v[220:223], v[120:123], v[84:87]\n\t"
    "ds_read_b128 v[116:119], %[ea] offset:30720\n\t"
    "v_mfma_f32_16x16x32_bf16 v[88:91], v[216:219], v[124:127], v[88:91]\n\t"
    "v_mfma_f32_16x16x32_bf16 v[92:95], v[220:223], v[124:127], v[92:95]\n\t"
    "s_waitcnt lgkmcnt(4)\n\t"
    "v_mfma_f32_16x16x32_bf16 v[80:83], v[224:227], v[96:99], v[80:83]\n\t"
    "ds_read_b128 v[120:123], %[da] offset:28672\n\t"
    "v_mfma_f32_16x16x32_bf16 v[84:87], v[228:231], v[96:99], v[84:87]\n\t"
    "ds_read_b128 v[124:127], %[da] offset:30720\n\t"
    "v_mfma_f32_16x16x32_bf16 v[88:91], v[224:227], v[100:103], v[88:91]\n\t"
    "v_mfma_f32_16x16x32_bf16 v[92:95], v[228:231], v[100:103], v[92:95]\n\t"
    "s_waitcnt lgkmcnt(4)\n\t"
    "v_mfma_f32_16x16x32_bf16 v[80:83], v[232:235], v[104:107], v[80:83]\n\t"
    "v_mfma_f32_16x16x32_bf16 v[84:87], v[236:239], v[104:107], v[84:87]\n\t"
    "v_mfma_f32_16x16x32_bf16 v[88:91], v[232:235], v[108:111], v[88:91]\n\t"
    "v_mfma_f32_16x16x32_bf16 v[92:95], v[236:239], v[108:111], v[92:95]\n\t"
    "s_waitcnt lgkmcnt(2)\n\t"
    "v_mfma_f32_16x16x32_bf16 v[80:83], v[240:243], v[112:115], v[80:83]\n\t"
    "v_mfma_f32_16x16x32_bf16 v[84:87], v[244:247], v[112:115], v[84:87]\n\t"
    "v_mfma_f32_16x16x32_bf16 v[88:91], v[240:243], v[116:119], v[88:91]\n\t"
    "v_mfma_f32_16x16x32_bf16 v[92:95], v[244:247], v[116:119], v[92:95]\n\t"
    "s_waitcnt lgkmcnt(0)\n\t"
    "v_mfma_f32_16x16x32_bf16 v[80:83], v[248:251], v[120:123], v[80:83]\n\t"
    "v_mfma_f32_16x16x32_bf16 v[84:87], v[252:255], v[120:123], v[84:87]\n\t"
    "v_mfma_f32_16x16x32_bf16 v[88:91], v[248:251], v[124:127], v[88:91]\n\t"
    "v_mfma_f32_16x16x32_bf16 v[92:95], v[252:255], v[124:127], v[92:95]\n\t"
    "s_cmp_eq_u32 %[ps], 0\n\t"
    "s_cbranch_scc1 10f\n\t"
    "global_load_dword %[sv], %[dv], %[db] sc1\n\t"
    "global_load_dword %[su], %[dv], %[db] offset:64 sc1\n\t"
    "10:\n\t"
    "s_mov_b32 %[got], 0\n\t"
    "v_mov_b32 v76, %[fa]\n\t"
    "v_mov_b32 v77, %[fv]\n\t"
    "v_add_u32 v78, 1, v77\n\t"
    "v_mov_b32 v79, %[ga]\n\t"
    "ds_write_b128 %[rw], v[80:83] offset:0\n\t"
    "ds_write_b128 %[rw], v[84:87] offset:1024\n\t"
    "ds_write_b128 %[rw], v[88:91] offset:2048\n\t"
    "ds_write_b128 %[rw], v[92:95] offset:3072\n\t"
    "ds_write_b32 v76, v77\n\t"
    "s_cmp_eq_u32 %[ps], 0\n\t"
    "s_cbranch_scc1 9f\n\t"
    "s_waitcnt vmcnt(0)\n\t"
    "s_branch 9f\n\t"
    "12:\n\t"
    "s_waitcnt lgkmcnt(0)\n\t"
    "9:"
    : [sv] "+&v"(sv), [su] "+&v"(su), [got] "=&s"(got)
    : [ea] "v"(ea), [da] "v"(da), [x] "s"(x), "{v[128:159]}"(W[0]), "{v[160:191]}"(W[1]), "{v[192:223]}"(W[2]), "{v[224:255]}"(W[3]), [lb] "s"(lb), [s0] "s"(s0), [s1] "s"(s1), [av0] "v"(av[0]), [av1] "v"(av[1]), [av2] "v"(av[2]), [av3] "v"(av[3]), [av4] "v"(av[4]), [av5] "v"(av[5]), [av6] "v"(av[6]), [av7] "v"(av[7]), [ps] "s"(ps), [dv] "v"(dv), [db] "s"(db), [rw] "v"(rw), [fa] "s"(fa), [fv] "s"(fv), [ga] "s"(ga), [mu] "s"(mu), [ad] "v"(ad), [th] "s"(th), [tb] "s"(tb)
    : "v96", "v97", "v98", "v99", "v100", "v101", "v102", "v103", "v104", "v105", "v106", "v107", "v108", "v109", "v110", "v111", "v112", "v113", "v114", "v115", "v116", "v117", "v118", "v119", "v120", "v121", "v122", "v123", "v124", "v125", "v126", "v127", "v76", "v77", "v78", "v79", "v80", "v81", "v82", "v83", "v84", "v85", "v86", "v87", "v88", "v89", "v90", "v91", "v92", "v93", "v94", "v95", "vcc", "scc", "m0", "memory");
}
__device__ __forceinline__ void lean_ld1(const u32x32 (&W)[4], unsigned ea, unsigned da, unsigned x, unsigned lb, const char* s0, const char* s1, const unsigned (&av)[8], unsigned ps, unsigned dv, const char* db, unsigned rw, unsigned fa, unsigned fv,
    unsigned ga, unsigned mu, unsigned ad, int th, int tb, unsigned& sv, unsigned& su, unsigned& got) {
  asm volatile("s_setprio 2\n\t"
    "v_mov_b32 v76, %[ga]\n\t"
    "ds_read_b32 v76, v76 offset:24\n\t"
    "ds_read_b128 v[96:99], %[ea] offset:0\n\t"
    "ds_read_b128 v[100:103], %[ea] offset:2048\n\t"
    "ds_read_b128 v[104:107], %[da] offset:0\n\t"
    "ds_read_b128 v[108:111], %[da] offset:2048\n\t"
    "ds_read_b128 v[112:115], %[ea] offset:4096\n\t"
    "ds_read_b128 v[116:119], %[ea] offset:6144\n\t"
    "s_waitcnt lgkmcnt(6)\n\t"
    "v_cmp_ne_u32 vcc, 0, v76\n\t"
    "s_mov_b32 %[got], -1\n\t"
    "s_cbranch_vccnz 12f\n\t"
    "s_waitcnt lgkmcnt(4)\n\t"
    "v_mfma_f32_16x16x32_bf16 v[80:83], v[128:131], v[96:99], 0\n\t"
    "ds_read_b128 v[120:123], %[da] offset:4096\n\t"
    "v_mfma_f32_16x16x32_bf16 v[84:87], v[132:135], v[96:99], 0\n\t"
    "ds_read_b128 v[124:127], %[da] offset:6144\n\t"
    "v_mfma_f32_16x16x32_bf16 v[88:91], v[128:131], v[100:103], 0\n\t"
    "v_mfma_f32_16x16x32_bf16 v[92:95], v[132:135], v[100:103], 0\n\t"
    "s_waitcnt vmcnt(12)\n\t"
    "s_cmp_lt_u32 %[x], 2\n\t"
    "s_cbranch_scc0 41f\n\t"
    "s_waitcnt vmcnt(4)\n\t"
    "41:\n\t"
    "s_barrier\n\t"
    "s_waitcnt lgkmcnt(4)\n\t"
    "v_mfma_f32_16x16x32_bf16 v[80:83], v[136:139], v[104:107], v[80:83]\n\t"
    "ds_read_b128 v[96:99], %[ea] offset:8192\n\t"
    "v_mfma_f32_16x16x32_bf16 v[84:87], v[140:143], v[104:107], v[84:87]\n\t"
    "ds_read_b128 v[100:103], %[ea] offset:10240\n\t"
    "v_mfma_f32_16x16x32_bf16 v[88:91], v[136:139], v[108:111], v[88:91]\n\t"
    "v_mfma_f32_16x16x32_bf16 v[92:95], v[140:143], v[108:111], v[92:95]\n\t"
    "s_waitcnt lgkmcnt(4)\n\t"
    "v_mfma_f32_16x16x32_bf16 v[80:83], v[144:147], v[112:115], v[80:83]\n\t"
    "ds_read_b128 v[104:107], %[da] offset:8192\n\t"
    "v_mfma_f32_16x16x32_bf16 v[84:87], v[148:151], v[112:115], v[84:87]\n\t"
    "ds_read_b128 v[108:111], %[da] offset:10240\n\t"
    "v_mfma_f32_16x16x32_bf16 v[88:91], v[144:147], v[116:119], v[88:91]\n\t"
    "v_mfma_f32_16x16x32_bf16 v[92:95], v[148:151], v[116:119], v[92:95]\n\t"
    "s_waitcnt lgkmcnt(4)\n\t"
    "v_mfma_f32_16x16x32_bf16 v[80:83], v[152:155], v[120:123], v[80:83]\n\t"
    "ds_read_b128 v[112:115], %[ea] offset:12288\n\t"
    "v_mfma_f32_16x16x32_bf16 v[84:87], v[156:159], v[120:123], v[84:87]\n\t"
    "ds_read_b128 v[116:119], %[ea] offset:14336\n\t"
    "v_mfma_f32_16x16x32_bf16 v[88:91], v[152:155], v[124:127], v[88:91]\n\t"
    "v_mfma_f32_16x16x32_bf16 v[92:95], v[156:159], v[124:127], v[92:95]\n\t"
    "s_waitcnt lgkmcnt(4)\n\t"
    "v_mfma_f32_16x16x32_bf16 v[80:83], v[160:163], v[96:99], v[80:83]\n\t"
    "ds_read_b128 v[120:123], %[da] offset:12288\n\t"
    "v_mfma_f32_16x16x32_bf16 v[84:87], v[164:167], v[96:99], v[84:87]\n\t"
    "ds_read_b128 v[124:127], %[da] offset:14336\n\t"
    "v_mfma_f32_16x16x32_bf16 v[88:91], v[160:163], v[100:103], v[88:91]\n\t"
    "v_mfma_f32_16x16x32_bf16 v[92:95], v[164:167], v[100:103], v[92:95]\n\t"
    "s_waitcnt vmcnt(10)\n\t"
    "s_cmp_lt_u32 %[x], 2\n\t"
    "s_cbranch_scc0 45f\n\t"
    "s_waitcnt vmcnt(2)\n\t"
    "45:\n\t"
    "s_barrier\n\t"
    "s_waitcnt lgkmcnt(4)\n\t"
    "v_mfma_f32_16x16x32_bf16 v[80:83], v[168:171], v[104:107], v[80:83]\n\t"
    "ds_read_b128 v[96:99], %[ea] offset:16384\n\t"
    "v_mfma_f32_16x16x32_bf16 v[84:87], v[172:175], v[104:107], v[84:87]\n\t"
    "ds_read_b128 v[100:103], %[ea] offset:18432\n\t"
    "v_mfma_f32_16x16x32_bf16 v[88:91], v[168:171], v[108:111], v[88:91]\n\t"
    "v_mfma_f32_16x16x32_bf16 v[92:95], v[172:175], v[108:111], v[92:95]\n\t"
    "s_waitcnt lgkmcnt(4)\n\t"
    "v_mfma_f32_16x16x32_bf16 v[80:83], v[176:179], v[112:115], v[80:83]\n\t"
    "ds_read_b128 v[104:107], %[da] offset:16384\n\t"
    "v_mfma_f32_16x16x32_bf16 v[84:87], v[180:183], v[112:115], v[84:87]\n\t"
    "ds_read_b128 v[108:111], %[da] offset:18432\n\t"
    "v_mfma_f32_16x16x32_bf16 v[88:91], v[176:179], v[116:119], v[88:91]\n\t"
    "v_mfma_f32_16x16x32_bf16 v[92:95], v[180:183], v[116:119], v[92:95]\n\t"
    "s_waitcnt lgkmcnt(4)\n\t"
    "v_mfma_f32_16x16x32_bf16 v[80:83], v[184:187], v[120:123], v[80:83]\n\t"
    "ds_read_b128 v[112:115], %[ea] offset:20480\n\t"
    "v_mfma_f32_16x16x32_bf16 v[84:87], v[188:191], v[120:123], v[84:87]\n\t"
    "ds_read_b128 v[116:119], %[ea] offset:22528\n\t"
    "v_mfma_f32_16x16x32_bf16 v[88:91], v[184:187], v[124:127], v[88:91]\n\t"
    "v_mfma_f32_16x16x32_bf16 v[92:95], v[188:191], v[124:127], v[92:95]\n\t"
    "s_waitcnt lgkmcnt(4)\n\t"
    "v_mfma_f32_16x16x32_bf16 v[80:83], v[192:195], v[96:99], v[80:83]\n\t"
    "ds_read_b128 v[120:123], %[da] offset:20480\n\t"
    "v_mfma_f32_16x16x32_bf16 v[84:87], v[196:199], v[96:99], v[84:87]\n\t"
    "ds_read_b128 v[124:127], %[da] offset:22528\n\t"
    "v_mfma_f32_16x16x32_bf16 v[88:91], v[192:195], v[100:103], v[88:91]\n\t"
    "v_mfma_f32_16x16x32_bf16 v[92:95], v[196:199], v[100:103], v[92:95]\n\t"
    "s_waitcnt vmcnt(8)\n\t"
    "s_cmp_lt_u32 %[x], 2\n\t"
    "s_cbranch_scc0 49f\n\t"
    "s_waitcnt vmcnt(0)\n\t"
    "49:\n\t"
    "s_barrier\n\t"
    "s_waitcnt lgkmcnt(4)\n\t"
    "v_mfma_f32_16x16x32_bf16 v[80:83], v[200:203], v[104:107], v[80:83]\n\t"
    "ds_read_b128 v[96:99], %[ea] offset:24576\n\t"
    "v_mfma_f32_16x16x32_bf16 v[84:87], v[204:207], v[104:107], v[84:87]\n\t"
    "ds_read_b128 v[100:103], %[ea] offset:26624\n\t"
    "v_mfma_f32_16x16x32_bf16 v[88:91], v[200:203], v[108:111], v[88:91]\n\t"
    "v_mfma_f32_16x16x32_bf16 v[92:95], v[204:207], v[108:111], v[92:95]\n\t"
    "s_waitcnt lgkmcnt(4)\n\t"
    "v_mfma_f32_16x16x32_bf16 v[80:83], v[208:211], v[112:115], v[80:83]\n\t"
    "ds_read_b128 v[104:107], %[da] offset:24576\n\t"
    "v_mfma_f32_16x16x32_bf16 v[84:87], v[212:215], v[112:115], v[84:87]\n\t"
    "ds_read_b128 v[108:111], %[da] offset:26624\n\t"
    "v_mfma_f32_16x16x32_bf16 v[88:91], v[208:211], v[116:119], v[88:91]\n\t"
    "v_mfma_f32_16x16x32_bf16 v[92:95], v[212:215], v[116:119], v[92:95]\n\t"
    "s_cmp_eq_u32 %[ps], 0\n\t"
    "s_cbranch_scc1 10f\n\t"
    "global_load_dword %[sv], %[dv], %[db] sc1\n\t"
    "global_load_dword %[su], %[dv], %[db] offset:64 sc1\n\t"
    "10:\n\t"
    "s_waitcnt lgkmcnt(4)\n\t"
    "v_mfma_f32_16x16x32_bf16 v[80:83], v[216:219], v[120:123], v[80:83]\n\t"
    "ds_read_b128 v[112:115], %[ea] offset:28672\n\t"
    "v_mfma_f32_16x16x32_bf16 v[84:87], v[220:223], v[120:123], v[84:87]\n\t"
    "ds_read_b128 v[116:119], %[ea] offset:30720\n\t"
    "v_mfma_f32_16x16x32_bf16 v[88:91], v[216:219], v[124:127], v[88:91]\n\t"
    "v_mfma_f32_16x16x32_bf16 v[92:95], v[220:223], v[124:127], v[92:95]\n\t"
    "s_waitcnt lgkmcnt(4)\n\t"
    "v_mfma_f32_16x16x32_bf16 v[80:83], v[224:227], v[96:99], v[80:83]\n\t"
    "ds_read_b128 v[120:123], %[da] offset:28672\n\t"
    "v_mfma_f32_16x16x32_bf16 v[84:87], v[228:231], v[96:99], v[84:87]\n\t"
    "ds_read_b128 v[124:127], %[da] offset:30720\n\t"
    "v_mfma_f32_16x16x32_bf16 v[88:91], v[224:227], v[100:103], v[88:91]\n\t"
    "v_mfma_f32_16x16x32_bf16 v[92:95], v[228:231], v[100:103], v[92:95]\n\t"
    "s_waitcnt lgkmcnt(4)\n\t"
    "v_mfma_f32_16x16x32_bf16 v[80:83], v[232:235], v[104:107], v[80:83]\n\t"
    "v_mfma_f32_16x16x32_bf16 v[84:87], v[236:239], v[104:107], v[84:87]\n\t"
    "v_mfma_f32_16x16x32_bf16 v[88:91], v[232:235], v[108:111], v[88:91]\n\t"
    "v_mfma_f32_16x16x32_bf16 v[92:95], v[236:239], v[108:111], v[92:95]\n\t"
    "s_waitcnt lgkmcnt(2)\n\t"
    "v_mfma_f32_16x16x32_bf16 v[80:83], v[240:243], v[112:115], v[80:83]\n\t"
    "v_mfma_f32_16x16x32_bf16 v[84:87], v[244:247], v[112:115], v[84:87]\n\t"
    "v_mfma_f32_16x16x32_bf16 v[88:91], v[240:243], v[116:119], v[88:91]\n\t"
    "v_mfma_f32_16x16x32_bf16 v[92:95], v[244:247], v[116:119], v[92:95]\n\t"
    "s_waitcnt lgkmcnt(0)\n\t"
    "v_mfma_f32_16x16x32_bf16 v[80:83], v[248:251], v[120:123], v[80:83]\n\t"
    "s_memrealtime s[36:37]\n\t"
    "v_mfma_f32_16x16x32_bf16 v[84:87], v[252:255], v[120:123], v[84:87]\n\t"
    "v_mfma_f32_16x16x32_bf16 v[88:91], v[248:251], v[124:127], v[88:91]\n\t"
    "v_mfma_f32_16x16x32_bf16 v[92:95], v[252:255], v[124:127], v[92:95]\n\t"
    "s_mov_b32 %[got], 0\n\t"
    "v_mov_b32 v76, %[fa]\n\t"
    "v_mov_b32 v77, %[fv]\n\t"
    "v_add_u32 v78, 1, v77\n\t"
    "v_mov_b32 v79, %[ga]\n\t"
    "s_waitcnt lgkmcnt(0)\n\t"
    "ds_write_b128 %[rw], v[80:83] offset:0\n\t"
    "ds_write_b128 %[rw], v[84:87] offset:1024\n\t"
    "ds_write_b128 %[rw], v[88:91] offset:2048\n\t"
    "ds_write_b128 %[rw], v[92:95] offset:3072\n\t"
    "ds_write_b32 v76, v77\n\t"
    "s_cmp_eq_u32 %[ps], 0\n\t"
    "s_cbranch_scc1 9f\n\t"
    "s_waitcnt vmcnt(0)\n\t"
    "v_mad_u32_u24 v76, %[sv], %[mu], %[ad]\n\t"
    "v_cmp_ge_i32 vcc, %[th], v76\n\t"
    "s_cbranch_vccz 11f\n\t"
    "s_sub_u32 s36, s36, 20\n\t"
    "v_sub_u32 v76, s36, %[su]\n\t"
    "v_lshrrev_b32 v77, 24, %[su]\n\t"
    "v_lshlrev_b32 v76, 8, v76\n\t"
    "v_ashrrev_i32 v76, 31, v76\n\t"
    "v_add_u32 v77, v77, v76\n\t"
    "v_cmp_lt_i32 s[36:37], %[tb], v77\n\t"
    "s_andn2_b64 vcc, vcc, s[36:37]\n\t"
    "s_cbranch_scc1 9f\n\t"
    "11:\n\t"
    "ds_write2_b32 v79, v78, v78 offset1:1\n\t"
    "s_add_u32 m0, %[lb], 0\n\t"
    "s_nop 0\n\t"
    "global_load_lds_dwordx4 %[av0], %[s0] sc1\n\t"
    "s_add_u32 m0, %[lb], 4096\n\t"
    "s_nop 0\n\t"
    "global_load_lds_dwordx4 %[av2], %[s0] sc1\n\t"
    "s_add_u32 m0, %[lb], 8192\n\t"
    "s_nop 0\n\t"
    "global_load_lds_dwordx4 %[av4], %[s0] sc1\n\t"
    "s_add_u32 m0, %[lb], 12288\n\t"
    "s_nop 0\n\t"
    "global_load_lds_dwordx4 %[av6], %[s0] sc1\n\t"
    "s_add_u32 m0, %[lb], 16384\n\t"
    "s_nop 0\n\t"
    "global_load_lds_dwordx4 %[av0], %[s1] sc1\n\t"
    "s_add_u32 m0, %[lb], 20480\n\t"
    "s_nop 0\n\t"
    "global_load_lds_dwordx4 %[av2], %[s1] sc1\n\t"
    "s_add_u32 m0, %[lb], 24576\n\t"
    "s_nop 0\n\t"
    "global_load_lds_dwordx4 %[av4], %[s1] sc1\n\t"
    "s_add_u32 m0, %[lb], 28672\n\t"
    "s_nop 0\n\t"
    "global_load_lds_dwordx4 %[av6], %[s1] sc1\n\t"
    "s_mov_b32 %[got], 1\n\t"
    "s_branch 9f\n\t"
    "12:\n\t"
    "s_waitcnt lgkmcnt(0)\n\t"
    "9:"
    : [sv] "+&v"(sv), [su] "+&v"(su), [got] "=&s"(got)
    : [ea] "v"(ea), [da] "v"(da), [x] "s"(x), "{v[128:159]}"(W[0]), "{v[160:191]}"(W[1]), "{v[192:223]}"(W[2]), "{v[224:255]}"(W[3]), [lb] "s"(lb), [s0] "s"(s0), [s1] "s"(s1), [av0] "v"(av[0]), [av1] "v"(av[1]), [av2] "v"(av[2]), [av3] "v"(av[3]), [av4] "v"(av[4]), [av5] "v"(av[5]), [av6] "v"(av[6]), [av7] "v"(av[7]), [ps] "s"(ps), [dv] "v"(dv), [db] "s"(db), [rw] "v"(rw), [fa] "s"(fa), [fv] "s"(fv), [ga] "s"(ga), [mu] "s"(mu), [ad] "v"(ad), [th] "s"(th), [tb] "s"(tb)
    : "v96", "v97", "v98", "v99", "v100", "v101", "v102", "v103", "v104", "v105", "v106", "v107", "v108", "v109", "v110", "v111", "v112", "v113", "v114", "v115", "v116", "v117", "v118", "v119", "v120", "v121", "v122", "v123", "v124", "v125", "v126", "v127", "v76", "v77", "v78", "v79", "v80", "v81", "v82", "v83", "v84", "v85", "v86", "v87", "v88", "v89", "v90", "v91", "v92", "v93", "v94", "v95", "vcc", "scc", "m0", "memory", "s36", "s37", "s38", "s39");
}
__device__ __forceinline__ void lean_dm1(const u32x32 (&W)[4], unsigned ea, unsigned da, unsigned x, unsigned lb, const char* s0, const char* s1, const unsigned (&av)[8], unsigned ps, unsigned dv, const char* db, unsigned rw, unsigned fa, unsigned fv,
    unsigned ga, unsigned mu, unsigned ad, int th, int tb, unsigned& sv, unsigned& su, unsigned& got) {
  asm volatile("s_setprio 2\n\t"
    "v_mov_b32 v76, %[ga]\n\t"
    "ds_read_b32 v76, v76 offset:24\n\t"
    "ds_read_b128 v[96:99], %[ea] offset:0\n\t"
    "ds_read_b128 v[100:103], %[ea] offset:2048\n\t"
    "ds_read_b128 v[104:107], %[da] offset:0\n\t"
    "ds_read_b128 v[108:111], %[da] offset:2048\n\t"
    "ds_read_b128 v[112:115], %[ea] offset:4096\n\t"
    "ds_read_b128 v[116:119], %[ea] offset:6144\n\t"
    "s_waitcnt lgkmcnt(6)\n\t"
    "v_cmp_ne_u32 vcc, 0, v76\n\t"
    "s_mov_b32 %[got], -1\n\t"
    "s_cbranch_vccnz 12f\n\t"
    "s_waitcnt lgkmcnt(4)\n\t"
    "v_mfma_f32_16x16x32_bf16 v[80:83], v[128:131], v[96:99], 0\n\t"
    "ds_read_b128 v[120:123], %[da] offset:4096\n\t"
    "s_add_u32 m0, %[lb], 0\n\t"
    "v_mfma_f32_16x16x32_bf16 v[84:87], v[132:135], v[96:99], 0\n\t"
    "ds_read_b128 v[124:127], %[da] offset:6144\n\t"
    "global_load_lds_dwordx4 %[av0], %[s0] sc1\n\t"
    "v_mfma_f32_16x16x32_bf16 v[88:91], v[128:131], v[100:103], 0\n\t"
    "v_mfma_f32_16x16x32_bf16 v[92:95], v[132:135], v[100:103], 0\n\t"
    "s_waitcnt vmcnt(5)\n\t"
    "s_barrier\n\t"
    "s_waitcnt lgkmcnt(4)\n\t"
    "v_mfma_f32_16x16x32_bf16 v[80:83], v[136:139], v[104:107], v[80:83]\n\t"
    "ds_read_b128 v[96:99], %[ea] offset:8192\n\t"
    "s_add_u32 m0, %[lb], 4096\n\t"
    "v_mfma_f32_16x16x32_bf16 v[84:87], v[140:143], v[104:107], v[84:87]\n\t"
    "ds_read_b128 v[100:103], %[ea] offset:10240\n\t"
    "global_load_lds_dwordx4 %[av2], %[s0] sc1\n\t"
    "v_mfma_f32_16x16x32_bf16 v[88:91], v[136:139], v[108:111], v[88:91]\n\t"
    "v_mfma_f32_16x16x32_bf16 v[92:95], v[140:143], v[108:111], v[92:95]\n\t"
    "s_waitcnt lgkmcnt(4)\n\t"
    "v_mfma_f32_16x16x32_bf16 v[80:83], v[144:147], v[112:115], v[80:83]\n\t"
    "ds_read_b128 v[104:107], %[da] offset:8192\n\t"
    "s_add_u32 m0, %[lb], 8192\n\t"
    "v_mfma_f32_16x16x32_bf16 v[84:87], v[148:151], v[112:115], v[84:87]\n\t"
    "ds_read_b128 v[108:111], %[da] offset:10240\n\t"
    "global_load_lds_dwordx4 %[av4], %[s0] sc1\n\t"
    "v_mfma_f32_16x16x32_bf16 v[88:91], v[144:147], v[116:119], v[88:91]\n\t"
    "v_mfma_f32_16x16x32_bf16 v[92:95], v[148:151], v[116:119], v[92:95]\n\t"
    "s_waitcnt lgkmcnt(4)\n\t"
    "v_mfma_f32_16x16x32_bf16 v[80:83], v[152:155], v[120:123], v[80:83]\n\t"
    "ds_read_b128 v[112:115], %[ea] offset:12288\n\t"
    "s_add_u32 m0, %[lb], 12288\n\t"
    "v_mfma_f32_16x16x32_bf16 v[84:87], v[156:159], v[120:123], v[84:87]\n\t"
    "ds_read_b128 v[116:119], %[ea] offset:14336\n\t"
    "global_load_lds_dwordx4 %[av6], %[s0] sc1\n\t"
    "v_mfma_f32_16x16x32_bf16 v[88:91], v[152:155], v[124:127], v[88:91]\n\t"
    "v_mfma_f32_16x16x32_bf16 v[92:95], v[156:159], v[124:127], v[92:95]\n\t"
    "s_waitcnt lgkmcnt(4)\n\t"
    "v_mfma_f32_16x16x32_bf16 v[80:83], v[160:163], v[96:99], v[80:83]\n\t"
    "ds_read_b128 v[120:123], %[da] offset:12288\n\t"
    "s_add_u32 m0, %[lb], 16384\n\t"
    "v_mfma_f32_16x16x32_bf16 v[84:87], v[164:167], v[96:99], v[84:87]\n\t"
    "ds_read_b128 v[124:127], %[da] offset:14336\n\t"
    "global_load_lds_dwordx4 %[av0], %[s1] sc1\n\t"
    "v_mfma_f32_16x16x32_bf16 v[88:91], v[160:163], v[100:103], v[88:91]\n\t"
    "v_mfma_f32_16x16x32_bf16 v[92:95], v[164:167], v[100:103], v[92:95]\n\t"
    "s_waitcnt vmcnt(7)\n\t"
    "s_barrier\n\t"
    "s_waitcnt lgkmcnt(4)\n\t"
    "v_mfma_f32_16x16x32_bf16 v[80:83], v[168:171], v[104:107], v[80:83]\n\t"
    "ds_read_b128 v[96:99], %[ea] offset:16384\n\t"
    "s_add_u32 m0, %[lb], 20480\n\t"
    "v_mfma_f32_16x16x32_bf16 v[84:87], v[172:175], v[104:107], v[84:87]\n\t"
    "ds_read_b128 v[100:103], %[ea] offset:18432\n\t"
    "global_load_lds_dwordx4 %[av2], %[s1] sc1\n\t"
    "v_mfma_f32_16x16x32_bf16 v[88:91], v[168:171], v[108:111], v[88:91]\n\t"
    "v_mfma_f32_16x16x32_bf16 v[92:95], v[172:175], v[108:111], v[92:95]\n\t"
    "s_waitcnt lgkmcnt(4)\n\t"
    "v_mfma_f32_16x16x32_bf16 v[80:83], v[176:179], v[112:115], v[80:83]\n\t"
    "ds_read_b128 v[104:107], %[da] offset:16384\n\t"
    "s_add_u32 m0, %[lb], 24576\n\t"
    "v_mfma_f32_16x16x32_bf16 v[84:87], v[180:183], v[112:115], v[84:87]\n\t"
    "ds_read_b128 v[108:111], %[da] offset:18432\n\t"
    "global_load_lds_dwordx4 %[av4], %[s1] sc1\n\t"
    "v_mfma_f32_16x16x32_bf16 v[88:91], v[176:179], v[116:119], v[88:91]\n\t"
    "v_mfma_f32_16x16x32_bf16 v[92:95], v[180:183], v[116:119], v[92:95]\n\t"
    "s_waitcnt lgkmcnt(4)\n\t"
    "v_mfma_f32_16x16x32_bf16 v[80:83], v[184:187], v[120:123], v[80:83]\n\t"
    "ds_read_b128 v[112:115], %[ea] offset:20480\n\t"
    "s_add_u32 m0, %[lb], 28672\n\t"
    "v_mfma_f32_16x16x32_bf16 v[84:87], v[188:191], v[120:123], v[84:87]\n\t"
    "ds_read_b128 v[116:119], %[ea] offset:22528\n\t"
    "global_load_lds_dwordx4 %[av6], %[s1] sc1\n\t"
    "v_mfma_f32_16x16x32_bf16 v[88:91], v[184:187], v[124:127], v[88:91]\n\t"
    "v_mfma_f32_16x16x32_bf16 v[92:95], v[188:191], v[124:127], v[92:95]\n\t"
    "s_waitcnt lgkmcnt(4)\n\t"
    "v_mfma_f32_16x16x32_bf16 v[80:83], v[192:195], v[96:99], v[80:83]\n\t"
    "ds_read_b128 v[120:123], %[da] offset:20480\n\t"
    "v_mfma_f32_16x16x32_bf16 v[84:87], v[196:199], v[96:99], v[84:87]\n\t"
    "ds_read_b128 v[124:127], %[da] offset:22528\n\t"
    "v_mfma_f32_16x16x32_bf16 v[88:91], v[192:195], v[100:103], v[88:91]\n\t"
    "v_mfma_f32_16x16x32_bf16 v[92:95], v[196:199], v[100:103], v[92:95]\n\t"
    "s_waitcnt vmcnt(8)\n\t"
    "s_barrier\n\t"
    "s_waitcnt lgkmcnt(4)\n\t"
    "v_mfma_f32_16x16x32_bf16 v[80:83], v[200:203], v[104:107], v[80:83]\n\t"
    "ds_read_b128 v[96:99], %[ea] offset:24576\n\t"
    "v_mfma_f32_16x16x32_bf16 v[84:87], v[204:207], v[104:107], v[84:87]\n\t"
    "ds_read_b128 v[100:103], %[ea] offset:26624\n\t"
    "v_mfma_f32_16x16x32_bf16 v[88:91], v[200:203], v[108:111], v[88:91]\n\t"
    "v_mfma_f32_16x16x32_bf16 v[92:95], v[204:207], v[108:111], v[92:95]\n\t"
    "s_waitcnt lgkmcnt(4)\n\t"
    "v_mfma_f32_16x16x32_bf16 v[80:83], v[208:211], v[112:115], v[80:83]\n\t"
    "ds_read_b128 v[104:107], %[da] offset:24576\n\t"
    "v_mfma_f32_16x16x32_bf16 v[84:87], v[212:215], v[112:115], v[84:87]\n\t"
    "ds_read_b128 v[108:111], %[da] offset:26624\n\t"
    "v_mfma_f32_16x16x32_bf16 v[88:91], v[208:211], v[116:119], v[88:91]\n\t"
    "v_mfma_f32_16x16x32_bf16 v[92:95], v[212:215], v[116:119], v[92:95]\n\t"
    "s_waitcnt lgkmcnt(4)\n\t"
    "v_mfma_f32_16x16x32_bf16 v[80:83], v[216:219], v[120:123], v[80:83]\n\t"
    "ds_read_b128 v[112:115], %[ea] offset:28672\n\t"
    "v_mfma_f32_16x16x32_bf16 v[84:87], v[220:223], v[120:123], v[84:87]\n\t"
    "ds_read_b128 v[116:119], %[ea] offset:30720\n\t"
    "v_mfma_f32_16x16x32_bf16 v[88:91], v[216:219], v[124:127], v[88:91]\n\t"
    "v_mfma_f32_16x16x32_bf16 v[92:95], v[220:223], v[124:127], v[92:95]\n\t"
    "s_waitcnt lgkmcnt(4)\n\t"
    "v_mfma_f32_16x16x32_bf16 v[80:83], v[224:227], v[96:99], v[80:83]\n\t"
    "ds_read_b128 v[120:123], %[da] offset:28672\n\t"
    "v_mfma_f32_16x16x32_bf16 v[84:87], v[228:231], v[96:99], v[84:87]\n\t"
    "ds_read_b128 v[124:127], %[da] offset:30720\n\t"
    "v_mfma_f32_16x16x32_bf16 v[88:91], v[224:227], v[100:103], v[88:91]\n\t"
    "v_mfma_f32_16x16x32_bf16 v[92:95], v[228:231], v[100:103], v[92:95]\n\t"
    "s_waitcnt lgkmcnt(4)\n\t"
    "v_mfma_f32_16x16x32_bf16 v[80:83], v[232:235], v[104:107], v[80:83]\n\t"
    "v_mfma_f32_16x16x32_bf16 v[84:87], v[236:239], v[104:107], v[84:87]\n\t"
    "v_mfma_f32_16x16x32_bf16 v[88:91], v[232:235], v[108:111], v[88:91]\n\t"
    "v_mfma_f32_16x16x32_bf16 v[92:95], v[236:239], v[108:111], v[92:95]\n\t"
    "s_waitcnt lgkmcnt(2)\n\t"
    "v_mfma_f32_16x16x32_bf16 v[80:83], v[240:243], v[112:115], v[80:83]\n\t"
    "v_mfma_f32_16x16x32_bf16 v[84:87], v[244:247], v[112:115], v[84:87]\n\t"
    "v_mfma_f32_16x16x32_bf16 v[88:91], v[240:243], v[116:119], v[88:91]\n\t"
    "v_mfma_f32_16x16x32_bf16 v[92:95], v[244:247], v[116:119], v[92:95]\n\t"
    "s_waitcnt lgkmcnt(0)\n\t"
    "v_mfma_f32_16x16x32_bf16 v[80:83], v[248:251], v[120:123], v[80:83]\n\t"
    "v_mfma_f32_16x16x32_bf16 v[84:87], v[252:255], v[120:123], v[84:87]\n\t"
    "v_mfma_f32_16x16x32_bf16 v[88:91], v[248:251], v[124:127], v[88:91]\n\t"
    "v_mfma_f32_16x16x32_bf16 v[92:95], v[252:255], v[124:127], v[92:95]\n\t"
    "s_cmp_eq_u32 %[ps], 0\n\t"
    "s_cbranch_scc1 10f\n\t"
    "global_load_dword %[sv], %[dv], %[db] sc1\n\t"
    "global_load_dword %[su], %[dv], %[db] offset:64 sc1\n\t"
    "10:\n\t"
    "s_mov_b32 %[got], 0\n\t"
    "v_mov_b32 v76, %[fa]\n\t"
    "v_mov_b32 v77, %[fv]\n\t"
    "v_add_u32 v78, 1, v77\n\t"
    "v_mov_b32 v79, %[ga]\n\t"
    "ds_write_b128 %[rw], v[80:83] offset:0\n\t"
    "ds_write_b128 %[rw], v[84:87] offset:1024\n\t"
    "ds_write_b128 %[rw], v[88:91] offset:2048\n\t"
    "ds_write_b128 %[rw], v[92:95] offset:3072\n\t"
    "ds_write_b32 v76, v77\n\t"
    "s_cmp_eq_u32 %[ps], 0\n\t"
    "s_cbranch_scc1 9f\n\t"
    "s_waitcnt vmcnt(0)\n\t"
    "s_branch 9f\n\t"
    "12:\n\t"
    "s_waitcnt lgkmcnt(0)\n\t"
    "9:"
    : [sv] "+&v"(sv), [su] "+&v"(su), [got] "=&s"(got)
    : [ea] "v"(ea), [da] "v"(da), [x] "s"(x), "{v[128:159]}"(W[0]), "{v[160:191]}"(W[1]), "{v[192:223]}"(W[2]), "{v[224:255]}"(W[3]), [lb] "s"(lb), [s0] "s"(s0), [s1] "s"(s1), [av0] "v"(av[0]), [av1] "v"(av[1]), [av2] "v"(av[2]), [av3] "v"(av[3]), [av4] "v"(av[4]), [av5] "v"(av[5]), [av6] "v"(av[6]), [av7] "v"(av[7]), [ps] "s"(ps), [dv] "v"(dv), [db] "s"(db), [rw] "v"(rw), [fa] "s"(fa), [fv] "s"(fv), [ga] "s"(ga), [mu] "s"(mu), [ad] "v"(ad), [th] "s"(th), [tb] "s"(tb)
    : "v96", "v97", "v98", "v99", "v100", "v101", "v102", "v103", "v104", "v105", "v106", "v107", "v108", "v109", "v110", "v111", "v112", "v113", "v114", "v115", "v116", "v117", "v118", "v119", "v120", "v121", "v122", "v123", "v124", "v125", "v126", "v127", "v76", "v77", "v78", "v79", "v80", "v81", "v82", "v83", "v84", "v85", "v86", "v87", "v88", "v89", "v90", "v91", "v92", "v93", "v94", "v95", "vcc", "scc", "m0", "memory");
}
__device__ __forceinline__ void lean_st0(const u32x32 (&W)[4], unsigned ea, unsigned da, unsigned x, u32x16& pd, unsigned pv, unsigned cn, const char* pg, unsigned bl, unsigned& tk, unsigned& st) {
  asm volatile("v_mov_b32 v76, %[cn]\n\t"
    "ds_read_b32 v76, v76 offset:28\n\t"
    "ds_read_b128 v[96:99], %[ea] offset:0\n\t"
    "ds_read_b128 v[100:103], %[ea] offset:2048\n\t"
    "ds_read_b128 v[104:107], %[da] offset:0\n\t"
    "ds_read_b128 v[108:111], %[da] offset:2048\n\t"
    "ds_read_b128 v[112:115], %[ea] offset:4096\n\t"
    "ds_read_b128 v[116:119], %[ea] offset:6144\n\t"
    "s_waitcnt lgkmcnt(6)\n\t"
    "v_cmp_ne_u32 vcc, 0, v76\n\t"
    "s_mov_b32 %[st], -1\n\t"
    "s_cbranch_vccnz 12f\n\t"
    "s_waitcnt lgkmcnt(4)\n\t"
    "v_mfma_f32_16x16x32_bf16 v[80:83], v[128:131], v[96:99], 0\n\t"
    "ds_read_b128 v[120:123], %[da] offset:4096\n\t"
    "v_mfma_f32_16x16x32_bf16 v[84:87], v[132:135], v[96:99], 0\n\t"
    "ds_read_b128 v[124:127], %[da] offset:6144\n\t"
    "v_mfma_f32_16x16x32_bf16 v[88:91], v[128:131], v[100:103], 0\n\t"
    "v_mfma_f32_16x16x32_bf16 v[92:95], v[132:135], v[100:103], 0\n\t"
    "s_barrier\n\t"
    "s_bitcmp1_b32 %[x], 2\n\t"
    "s_cbranch_scc1 7f\n\t"
    "8:\n\t"
    "s_bitcmp1_b32 %[x], 3\n\t"
    "s_cbranch_scc1 13f\n\t"
    "14:\n\t"
    "s_waitcnt lgkmcnt(4)\n\t"
    "v_mfma_f32_16x16x32_bf16 v[80:83], v[136:139], v[104:107], v[80:83]\n\t"
    "ds_read_b128 v[96:99], %[ea] offset:8192\n\t"
    "v_mfma_f32_16x16x32_bf16 v[84:87], v[140:143], v[104:107], v[84:87]\n\t"
    "ds_read_b128 v[100:103], %[ea] offset:10240\n\t"
    "v_mfma_f32_16x16x32_bf16 v[88:91], v[136:139], v[108:111], v[88:91]\n\t"
    "v_mfma_f32_16x16x32_bf16 v[92:95], v[140:143], v[108:111], v[92:95]\n\t"
    "s_waitcnt lgkmcnt(4)\n\t"
    "v_mfma_f32_16x16x32_bf16 v[80:83], v[144:147], v[112:115], v[80:83]\n\t"
    "ds_read_b128 v[104:107], %[da] offset:8192\n\t"
    "v_mfma_f32_16x16x32_bf16 v[84:87], v[148:151], v[112:115], v[84:87]\n\t"
    "ds_read_b128 v[108:111], %[da] offset:10240\n\t"
    "v_mfma_f32_16x16x32_bf16 v[88:91], v[144:147], v[116:119], v[88:91]\n\t"
    "v_mfma_f32_16x16x32_bf16 v[92:95], v[148:151], v[116:119], v[92:95]\n\t"
    "s_waitcnt lgkmcnt(4)\n\t"
    "v_mfma_f32_16x16x32_bf16 v[80:83], v[152:155], v[120:123], v[80:83]\n\t"
    "ds_read_b128 v[112:115], %[ea] offset:12288\n\t"
    "v_mfma_f32_16x16x32_bf16 v[84:87], v[156:159], v[120:123], v[84:87]\n\t"
    "ds_read_b128 v[116:119], %[ea] offset:14336\n\t"
    "v_mfma_f32_16x16x32_bf16 v[88:91], v[152:155], v[124:127], v[88:91]\n\t"
    "v_mfma_f32_16x16x32_bf16 v[92:95], v[156:159], v[124:127], v[92:95]\n\t"
    "s_waitcnt lgkmcnt(4)\n\t"
    "v_mfma_f32_16x16x32_bf16 v[80:83], v[160:163], v[96:99], v[80:83]\n\t"
    "ds_read_b128 v[120:123], %[da] offset:12288\n\t"
    "v_mfma_f32_16x16x32_bf16 v[84:87], v[164:167], v[96:99], v[84:87]\n\t"
    "ds_read_b128 v[124:127], %[da] offset:14336\n\t"
    "v_mfma_f32_16x16x32_bf16 v[88:91], v[160:163], v[100:103], v[88:91]\n\t"
    "v_mfma_f32_16x16x32_bf16 v[92:95], v[164:167], v[100:103], v[92:95]\n\t"
    "s_barrier\n\t"
    "s_waitcnt lgkmcnt(4)\n\t"
    "v_mfma_f32_16x16x32_bf16 v[80:83], v[168:171], v[104:107], v[80:83]\n\t"
    "ds_read_b128 v[96:99], %[ea] offset:16384\n\t"
    "v_mfma_f32_16x16x32_bf16 v[84:87], v[172:175], v[104:107], v[84:87]\n\t"
    "ds_read_b128 v[100:103], %[ea] offset:18432\n\t"
    "v_mfma_f32_16x16x32_bf16 v[88:91], v[168:171], v[108:111], v[88:91]\n\t"
    "v_mfma_f32_16x16x32_bf16 v[92:95], v[172:175], v[108:111], v[92:95]\n\t"
    "s_waitcnt lgkmcnt(4)\n\t"
    "v_mfma_f32_16x16x32_bf16 v[80:83], v[176:179], v[112:115], v[80:83]\n\t"
    "ds_read_b128 v[104:107], %[da] offset:16384\n\t"
    "v_mfma_f32_16x16x32_bf16 v[84:87], v[180:183], v[112:115], v[84:87]\n\t"
    "ds_read_b128 v[108:111], %[da] offset:18432\n\t"
    "v_mfma_f32_16x16x32_bf16 v[88:91], v[176:179], v[116:119], v[88:91]\n\t"
    "v_mfma_f32_16x16x32_bf16 v[92:95], v[180:183], v[116:119], v[92:95]\n\t"
    "s_waitcnt lgkmcnt(4)\n\t"
    "v_mfma_f32_16x16x32_bf16 v[80:83], v[184:187], v[120:123], v[80:83]\n\t"
    "ds_read_b128 v[112:115], %[ea] offset:20480\n\t"
    "v_mfma_f32_16x16x32_bf16 v[84:87], v[188:191], v[120:123], v[84:87]\n\t"
    "ds_read_b128 v[116:119], %[ea] offset:22528\n\t"
    "v_mfma_f32_16x16x32_bf16 v[88:91], v[184:187], v[124:127], v[88:91]\n\t"
    "v_mfma_f32_16x16x32_bf16 v[92:95], v[188:191], v[124:127], v[92:95]\n\t"
    "s_waitcnt lgkmcnt(4)\n\t"
    "v_mfma_f32_16x16x32_bf16 v[80:83], v[192:195], v[96:99], v[80:83]\n\t"
    "ds_read_b128 v[120:123], %[da] offset:20480\n\t"
    "v_mfma_f32_16x16x32_bf16 v[84:87], v[196:199], v[96:99], v[84:87]\n\t"
    "ds_read_b128 v[124:127], %[da] offset:22528\n\t"
    "v_mfma_f32_16x16x32_bf16 v[88:91], v[192:195], v[100:103], v[88:91]\n\t"
    "v_mfma_f32_16x16x32_bf16 v[92:95], v[196:199], v[100:103], v[92:95]\n\t"
    "s_barrier\n\t"
    "s_and_b32 %[st], %[x], 3\n\t"
    "s_cbranch_scc1 3f\n\t"
    "4:\n\t"
    "s_waitcnt lgkmcnt(4)\n\t"
    "v_mfma_f32_16x16x32_bf16 v[80:83], v[200:203], v[104:107], v[80:83]\n\t"
    "ds_read_b128 v[96:99], %[ea] offset:24576\n\t"
    "v_mfma_f32_16x16x32_bf16 v[84:87], v[204:207], v[104:107], v[84:87]\n\t"
    "ds_read_b128 v[100:103], %[ea] offset:26624\n\t"
    "v_mfma_f32_16x16x32_bf16 v[88:91], v[200:203], v[108:111], v[88:91]\n\t"
    "v_mfma_f32_16x16x32_bf16 v[92:95], v[204:207], v[108:111], v[92:95]\n\t"
    "s_waitcnt lgkmcnt(4)\n\t"
    "v_mfma_f32_16x16x32_bf16 v[80:83], v[208:211], v[112:115], v[80:83]\n\t"
    "ds_read_b128 v[104:107], %[da] offset:24576\n\t"
    "v_mfma_f32_16x16x32_bf16 v[84:87], v[212:215], v[112:115], v[84:87]\n\t"
    "ds_read_b128 v[108:111], %[da] offset:26624\n\t"
    "v_mfma_f32_16x16x32_bf16 v[88:91], v[208:211], v[116:119], v[88:91]\n\t"
    "v_mfma_f32_16x16x32_bf16 v[92:95], v[212:215], v[116:119], v[92:95]\n\t"
    "s_waitcnt lgkmcnt(4)\n\t"
    "v_mfma_f32_16x16x32_bf16 v[80:83], v[216:219], v[120:123], v[80:83]\n\t"
    "ds_read_b128 v[112:115], %[ea] offset:28672\n\t"
    "v_mfma_f32_16x16x32_bf16 v[84:87], v[220:223], v[120:123], v[84:87]\n\t"
    "ds_read_b128 v[116:119], %[ea] offset:30720\n\t"
    "v_mfma_f32_16x16x32_bf16 v[88:91], v[216:219], v[124:127], v[88:91]\n\t"
    "v_mfma_f32_16x16x32_bf16 v[92:95], v[220:223], v[124:127], v[92:95]\n\t"
    "s_waitcnt lgkmcnt(4)\n\t"
    "v_mfma_f32_16x16x32_bf16 v[80:83], v[224:227], v[96:99], v[80:83]\n\t"
    "ds_read_b128 v[120:123], %[da] offset:28672\n\t"
    "v_mfma_f32_16x16x32_bf16 v[84:87], v[228:231], v[96:99], v[84:87]\n\t"
    "ds_read_b128 v[124:127], %[da] offset:30720\n\t"
    "v_mfma_f32_16x16x32_bf16 v[88:91], v[224:227], v[100:103], v[88:91]\n\t"
    "v_mfma_f32_16x16x32_bf16 v[92:95], v[228:231], v[100:103], v[92:95]\n\t"
    "s_waitcnt lgkmcnt(4)\n\t"
    "v_mfma_f32_16x16x32_bf16 v[80:83], v[232:235], v[104:107], v[80:83]\n\t"
    "v_mfma_f32_16x16x32_bf16 v[84:87], v[236:239], v[104:107], v[84:87]\n\t"
    "v_mfma_f32_16x16x32_bf16 v[88:91], v[232:235], v[108:111], v[88:91]\n\t"
    "v_mfma_f32_16x16x32_bf16 v[92:95], v[236:239], v[108:111], v[92:95]\n\t"
    "s_waitcnt lgkmcnt(2)\n\t"
    "v_mfma_f32_16x16x32_bf16 v[80:83], v[240:243], v[112:115], v[80:83]\n\t"
    "v_mfma_f32_16x16x32_bf16 v[84:87], v[244:247], v[112:115], v[84:87]\n\t"
    "v_mfma_f32_16x16x32_bf16 v[88:91], v[240:243], v[116:119], v[88:91]\n\t"
    "v_mfma_f32_16x16x32_bf16 v[92:95], v[244:247], v[116:119], v[92:95]\n\t"
    "s_waitcnt lgkmcnt(0)\n\t"
    "v_mfma_f32_16x16x32_bf16 v[80:83], v[248:251], v[120:123], v[80:83]\n\t"
    "v_mfma_f32_16x16x32_bf16 v[84:87], v[252:255], v[120:123], v[84:87]\n\t"
    "v_mfma_f32_16x16x32_bf16 v[88:91], v[248:251], v[124:127], v[88:91]\n\t"
    "v_mfma_f32_16x16x32_bf16 v[92:95], v[252:255], v[124:127], v[92:95]\n\t"
    "s_mov_b32 %[st], 0\n\t"
    "s_nop 5\n\t"
    "s_branch 9f\n\t"
    "13:\n\t"
    "s_memrealtime s[36:37]\n\t"
    "v_mov_b32 v79, 0\n\t"
    "s_waitcnt lgkmcnt(0)\n\t"
    "s_mov_b32 %[tk], s36\n\t"
    "s_add_u32 s36, s36, %[bl]\n\t"
    "s_and_b32 s36, s36, 0xffffff\n\t"
    "s_and_b32 s37, %[bl], 0xff000000\n\t"
    "s_or_b32 s36, s36, s37\n\t"
    "v_mov_b32 v78, s36\n\t"
    "s_mov_b64 exec, 1\n\t"
    "global_store_dword v79, v78, %[pg] offset:64 sc1\n\t"
    "s_mov_b64 exec, -1\n\t"
    "s_branch 14b\n\t"
    "3:\n\t"
    "7:\n\t"
    "s_and_b32 %[st], %[pv], 1\n\t"
    "s_mul_i32 %[st], %[st], 36\n\t"
    "s_add_u32 %[st], %[st], %[cn]\n\t"
    "v_mov_b32 v76, %[st]\n\t"
    "v_mov_b32 v77, 1\n\t"
    "v_mov_b32 v78, %[pv]\n\t"
    "v_mov_b32 v79, 0\n\t"
    "s_waitcnt vmcnt(0)\n\t"
    "s_mov_b64 exec, 1\n\t"
    "ds_add_rtn_u32 v77, v76, v77\n\t"
    "s_waitcnt lgkmcnt(0)\n\t"
    "v_readfirstlane_b32 %[st], v77\n\t"
    "s_and_b32 %[st], %[st], 3\n\t"
    "s_cmp_lg_u32 %[st], 3\n\t"
    "s_cbranch_scc1 6f\n\t"
    "global_store_dword v79, v78, %[pg] sc1\n\t"
    "6:\n\t"
    "s_mov_b64 exec, -1\n\t"
    "s_bitcmp1_b32 %[x], 2\n\t"
    "s_cbranch_scc1 8b\n\t"
    "s_branch 4b\n\t"
    "12:\n\t"
    "s_waitcnt lgkmcnt(0)\n\t"
    "9:"
    : "=&{v[80:95]}"(pd), [st] "=&s"(st), [tk] "+&s"(tk)
    : [ea] "v"(ea), [da] "v"(da), [x] "s"(x), "{v[128:159]}"(W[0]), "{v[160:191]}"(W[1]), "{v[192:223]}"(W[2]), "{v[224:255]}"(W[3]), [pv] "s"(pv), [cn] "s"(cn), [pg] "s"(pg), [bl] "s"(bl)
    : "v96", "v97", "v98", "v99", "v100", "v101", "v102", "v103", "v104", "v105", "v106", "v107", "v108", "v109", "v110", "v111", "v112", "v113", "v114", "v115", "v116", "v117", "v118", "v119", "v120", "v121", "v122", "v123", "v124", "v125", "v126", "v127", "v76", "v77", "v78", "v79", "vcc", "scc", "m0", "memory", "s36", "s37", "s38", "s39");
}
#define LEAN_QB 1
#define PREK 10
#define NEARLY 16
__device__ __forceinline__ void fin_ld(const u32x16& pd, unsigned rw, unsigned fa, unsigned fv) {
  asm volatile("ds_write_b128 %[rw], v[80:83] offset:0\n\t"
    "ds_write_b128 %[rw], v[84:87] offset:1024\n\t"
    "ds_write_b128 %[rw], v[88:91] offset:2048\n\t"
    "ds_write_b128 %[rw], v[92:95] offset:3072\n\t"
    "ds_write_b32 %[fa], %[fv]"
    :
    : [rw] "v"(rw), "{v[80:95]}"(pd), [fa] "v"(fa), [fv] "v"(fv)
    : "memory");
}
__device__ __forceinline__ void fin_st0(const u32x16& pd, unsigned rw, unsigned fa, unsigned fv, const char* cb, unsigned co0, unsigned co1, unsigned pa, unsigned& bad, unsigned& pk) {
  asm volatile("1:\n\t"
    "ds_read_b32 v39, %[fa]\n\t"
    "ds_read_b128 v[48:51], %[rw] offset:0\n\t"
    "ds_read_b128 v[52:55], %[rw] offset:1024\n\t"
    "ds_read_b128 v[56:59], %[rw] offset:2048\n\t"
    "ds_read_b128 v[60:63], %[rw] offset:3072\n\t"
    "s_waitcnt lgkmcnt(2)\n\t"
    "v_cmp_ne_u32 vcc, v39, %[fv]\n\t"
    "s_cbranch_vccz 2f\n\t"
    "s_waitcnt lgkmcnt(0)\n\t"
    "s_branch 1b\n\t"
    "2:\n\t"
    "v_add_f32 v48, v48, v80\n\t"
    "v_add_f32 v49, v49, v81\n\t"
    "v_add_f32 v50, v50, v82\n\t"
    "v_add_f32 v51, v51, v83\n\t"
    "v_add_f32 v52, v52, v84\n\t"
    "v_add_f32 v53, v53, v85\n\t"
    "v_add_f32 v54, v54, v86\n\t"
    "v_add_f32 v55, v55, v87\n\t"
    "v_cvt_pk_bf16_f32 v40, v48, v49\n\t"
    "v_cvt_pk_bf16_f32 v41, v50, v51\n\t"
    "v_cvt_pk_bf16_f32 v42, v52, v53\n\t"
    "v_cvt_pk_bf16_f32 v43, v54, v55\n\t"
    "global_store_dwordx4 %[co0], v[40:43], %[cb] sc1\n\t"
    "s_waitcnt lgkmcnt(0)\n\t"
    "v_mov_b32 v39, %[pa]\n\t"
    "ds_read_b32 v39, v39\n\t"
    "v_add_f32 v56, v56, v88\n\t"
    "v_add_f32 v57, v57, v89\n\t"
    "v_add_f32 v58, v58, v90\n\t"
    "v_add_f32 v59, v59, v91\n\t"
    "v_add_f32 v60, v60, v92\n\t"
    "v_add_f32 v61, v61, v93\n\t"
    "v_add_f32 v62, v62, v94\n\t"
    "v_add_f32 v63, v63, v95\n\t"
    "v_cvt_pk_bf16_f32 v44, v56, v57\n\t"
    "v_cvt_pk_bf16_f32 v45, v58, v59\n\t"
    "v_cvt_pk_bf16_f32 v46, v60, v61\n\t"
    "v_cvt_pk_bf16_f32 v47, v62, v63\n\t"
    "global_store_dwordx4 %[co1], v[44:47], %[cb] sc1\n\t"
    "v_cmp_u_f32 vcc, v48, v56\n\t"
    "s_waitcnt lgkmcnt(0)\n\t"
    "s_or_b32 %[bad], vcc_lo, vcc_hi\n\t"
    "v_readfirstlane_b32 %[pk], v39"
    : [bad] "=&s"(bad), [pk] "=&s"(pk)
    : [rw] "v"(rw), "{v[80:95]}"(pd), [fa] "v"(fa), [fv] "v"(fv), [cb] "s"(cb), [co0] "v"(co0), [co1] "v"(co1), [pa] "s"(pa)
    : "v39", "v40", "v41", "v42", "v43", "v44", "v45", "v46", "v47", "v48", "v49", "v50", "v51", "v52", "v53", "v54", "v55", "v56", "v57", "v58", "v59", "v60", "v61", "v62", "v63", "vcc", "scc", "memory");
}
__device__ __forceinline__ void fin_st1(const u32x16& pd, unsigned rw, unsigned fa, unsigned fv, const char* cb, unsigned co0, unsigned co1, unsigned pa, unsigned& bad, unsigned& pk) {
  asm volatile("1:\n\t"
    "ds_read_b32 v39, %[fa]\n\t"
    "ds_read_b128 v[48:51], %[rw] offset:0\n\t"
    "ds_read_b128 v[52:55], %[rw] offset:1024\n\t"
    "ds_read_b128 v[56:59], %[rw] offset:2048\n\t"
    "ds_read_b128 v[60:63], %[rw] offset:3072\n\t"
    "s_waitcnt lgkmcnt(2)\n\t"
    "v_cmp_ne_u32 vcc, v39, %[fv]\n\t"
    "s_cbranch_vccz 2f\n\t"
    "s_waitcnt lgkmcnt(0)\n\t"
    "s_branch 1b\n\t"
    "2:\n\t"
    "v_add_f32 v48, v48, v80\n\t"
    "v_add_f32 v49, v49, v81\n\t"
    "v_add_f32 v50, v50, v82\n\t"
    "v_add_f32 v51, v51, v83\n\t"
    "v_add_f32 v52, v52, v84\n\t"
    "v_add_f32 v53, v53, v85\n\t"
    "v_add_f32 v54, v54, v86\n\t"
    "v_add_f32 v55, v55, v87\n\t"
    "s_waitcnt lgkmcnt(0)\n\t"
    "v_mov_b32 v39, %[pa]\n\t"
    "ds_read_b32 v39, v39\n\t"
    "v_add_f32 v56, v56, v88\n\t"
    "v_add_f32 v57, v57, v89\n\t"
    "v_add_f32 v58, v58, v90\n\t"
    "v_add_f32 v59, v59, v91\n\t"
    "v_add_f32 v60, v60, v92\n\t"
    "v_add_f32 v61, v61, v93\n\t"
    "v_add_f32 v62, v62, v94\n\t"
    "v_add_f32 v63, v63, v95\n\t"
    "v_mul_f32 v40, %[nl], v48\n\t"
    "v_mul_f32 v41, %[nl], v49\n\t"
    "v_mul_f32 v42, %[nl], v50\n\t"
    "v_mul_f32 v43, %[nl], v51\n\t"
    "v_mul_f32 v44, %[nl], v56\n\t"
    "v_mul_f32 v45, %[nl], v57\n\t"
    "v_mul_f32 v46, %[nl], v58\n\t"
    "v_mul_f32 v47, %[nl], v59\n\t"
    "v_exp_f32 v40, v40\n\t"
    "v_exp_f32 v41, v41\n\t"
    "v_exp_f32 v42, v42\n\t"
    "v_exp_f32 v43, v43\n\t"
    "v_exp_f32 v44, v44\n\t"
    "v_exp_f32 v45, v45\n\t"
    "v_exp_f32 v46, v46\n\t"
    "v_exp_f32 v47, v47\n\t"
    "v_add_f32 v40, 1.0, v40\n\t"
    "v_add_f32 v41, 1.0, v41\n\t"
    "v_add_f32 v42, 1.0, v42\n\t"
    "v_add_f32 v43, 1.0, v43\n\t"
    "v_add_f32 v44, 1.0, v44\n\t"
    "v_add_f32 v45, 1.0, v45\n\t"
    "v_add_f32 v46, 1.0, v46\n\t"
    "v_add_f32 v47, 1.0, v47\n\t"
    "v_rcp_f32 v40, v40\n\t"
    "v_rcp_f32 v41, v41\n\t"
    "v_rcp_f32 v42, v42\n\t"
    "v_rcp_f32 v43, v43\n\t"
    "v_rcp_f32 v44, v44\n\t"
    "v_rcp_f32 v45, v45\n\t"
    "v_rcp_f32 v46, v46\n\t"
    "v_rcp_f32 v47, v47\n\t"
    "v_mul_f32 v40, v48, v40\n\t"
    "v_mul_f32 v41, v49, v41\n\t"
    "v_mul_f32 v42, v50, v42\n\t"
    "v_mul_f32 v43, v51, v43\n\t"
    "v_mul_f32 v44, v56, v44\n\t"
    "v_mul_f32 v45, v57, v45\n\t"
    "v_mul_f32 v46, v58, v46\n\t"
    "v_mul_f32 v47, v59, v47\n\t"
    "v_mul_f32 v40, v40, v52\n\t"
    "v_mul_f32 v41, v41, v53\n\t"
    "v_mul_f32 v42, v42, v54\n\t"
    "v_mul_f32 v43, v43, v55\n\t"
    "v_mul_f32 v44, v44, v60\n\t"
    "v_mul_f32 v45, v45, v61\n\t"
    "v_mul_f32 v46, v46, v62\n\t"
    "v_mul_f32 v47, v47, v63\n\t"
    "v_cvt_pk_bf16_f32 v48, v40, v41\n\t"
    "v_cvt_pk_bf16_f32 v49, v42, v43\n\t"
    "v_cvt_pk_bf16_f32 v50, v44, v45\n\t"
    "v_cvt_pk_bf16_f32 v51, v46, v47\n\t"
    "s_nop 1\n\t"
    "v_permlane16_swap_b32 v48, v50\n\t"
    "v_permlane16_swap_b32 v49, v51\n\t"
    "global_store_dwordx4 %[co0], v[48:51], %[cb] sc1\n\t"
    "v_cmp_u_f32 vcc, v52, v60\n\t"
    "s_waitcnt lgkmcnt(0)\n\t"
    "s_or_b32 %[bad], vcc_lo, vcc_hi\n\t"
    "v_readfirstlane_b32 %[pk], v39"
    : [bad] "=&s"(bad), [pk] "=&s"(pk)
    : [rw] "v"(rw), "{v[80:95]}"(pd), [fa] "v"(fa), [fv] "v"(fv), [cb] "s"(cb), [co0] "v"(co0), [nl] "s"(-LOG2E), [pa] "s"(pa)
    : "v39", "v40", "v41", "v42", "v43", "v44", "v45", "v46", "v47", "v48", "v49", "v50", "v51", "v52", "v53", "v54", "v55", "v56", "v57", "v58", "v59", "v60", "v61", "v62", "v63", "vcc", "scc", "memory");
}
__device__ __forceinline__ void fin_st2(const u32x16& pd, unsigned rw, unsigned fa, unsigned fv, const char* cb, unsigned co0, unsigned co1, unsigned pa, unsigned& bad, unsigned& pk) {
  asm volatile("s_nop 1\n\t"
    "v_cvt_pk_bf16_f32 v40, v80, v81\n\t"
    "v_cvt_pk_bf16_f32 v41, v82, v83\n\t"
    "v_cvt_pk_bf16_f32 v42, v84, v85\n\t"
    "v_cvt_pk_bf16_f32 v43, v86, v87\n\t"
    "global_store_dwordx4 %[co0], v[40:43], %[cb] sc1\n\t"
    "v_cvt_pk_bf16_f32 v44, v88, v89\n\t"
    "v_cvt_pk_bf16_f32 v45, v90, v91\n\t"
    "v_cvt_pk_bf16_f32 v46, v92, v93\n\t"
    "v_cvt_pk_bf16_f32 v47, v94, v95\n\t"
    "global_store_dwordx4 %[co1], v[44:47], %[cb] sc1\n\t"
    "1:\n\t"
    "ds_read_b32 v39, %[fa]\n\t"
    "ds_read_b128 v[48:51], %[rw] offset:0\n\t"
    "ds_read_b128 v[52:55], %[rw] offset:1024\n\t"
    "ds_read_b128 v[56:59], %[rw] offset:2048\n\t"
    "ds_read_b128 v[60:63], %[rw] offset:3072\n\t"
    "s_waitcnt lgkmcnt(2)\n\t"
    "v_cmp_ne_u32 vcc, v39, %[fv]\n\t"
    "s_cbranch_vccz 2f\n\t"
    "s_waitcnt lgkmcnt(0)\n\t"
    "s_branch 1b\n\t"
    "2:\n\t"
    "v_cvt_pk_bf16_f32 v48, v48, v49\n\t"
    "v_cvt_pk_bf16_f32 v49, v50, v51\n\t"
    "v_cvt_pk_bf16_f32 v50, v52, v53\n\t"
    "v_cvt_pk_bf16_f32 v51, v54, v55\n\t"
    "global_store_dwordx4 %[co0], v[48:51], %[cb] offset:2048 sc1\n\t"
    "s_waitcnt lgkmcnt(0)\n\t"
    "v_mov_b32 v39, %[pa]\n\t"
    "ds_read_b32 v39, v39\n\t"
    "v_cvt_pk_bf16_f32 v56, v56, v57\n\t"
    "v_cvt_pk_bf16_f32 v57, v58, v59\n\t"
    "v_cvt_pk_bf16_f32 v58, v60, v61\n\t"
    "v_cvt_pk_bf16_f32 v59, v62, v63\n\t"
    "global_store_dwordx4 %[co1], v[56:59], %[cb] offset:2048 sc1\n\t"
    "v_cmp_u_f32 vcc, v80, v88\n\t"
    "s_waitcnt lgkmcnt(0)\n\t"
    "s_or_b32 %[bad], vcc_lo, vcc_hi\n\t"
    "v_readfirstlane_b32 %[pk], v39"
    : [bad] "=&s"(bad), [pk] "=&s"(pk)
    : [rw] "v"(rw), "{v[80:95]}"(pd), [fa] "v"(fa), [fv] "v"(fv), [cb] "s"(cb), [co0] "v"(co0), [co1] "v"(co1), [pa] "s"(pa)
    : "v39", "v40", "v41", "v42", "v43", "v44", "v45", "v46", "v47", "v48", "v49", "v50", "v51", "v52", "v53", "v54", "v55", "v56", "v57", "v58", "v59", "v60", "v61", "v62", "v63", "vcc", "scc", "memory");
}
template <int R> __device__ __forceinline__ void gw_load(u32x32& w, unsigned a0, unsigned a1);
template <> __device__ __forceinline__ void gw_load<0>(u32x32& w, unsigned a0, unsigned a1) {
  asm volatile("ds_read_b64_tr_b16 v[128:129], %1 offset:0\n\t"
    "ds_read_b64_tr_b16 v[130:131], %1 offset:512\n\t"
    "ds_read_b64_tr_b16 v[132:133], %2 offset:0\n\t"
    "ds_read_b64_tr_b16 v[134:135], %2 offset:512\n\t"
    "ds_read_b64_tr_b16 v[136:137], %1 offset:4096\n\t"
    "ds_read_b64_tr_b16 v[138:139], %1 offset:4608\n\t"
    "ds_read_b64_tr_b16 v[140:141], %2 offset:4096\n\t"
    "ds_read_b64_tr_b16 v[142:143], %2 offset:4608\n\t"
    "s_waitcnt lgkmcnt(0)\n\t"
    "ds_read_b64_tr_b16 v[144:145], %1 offset:16384\n\t"
    "ds_read_b64_tr_b16 v[146:147], %1 offset:16896\n\t"
    "ds_read_b64_tr_b16 v[148:149], %2 offset:16384\n\t"
    "ds_read_b64_tr_b16 v[150:151], %2 offset:16896\n\t"
    "ds_read_b64_tr_b16 v[152:153], %1 offset:20480\n\t"
    "ds_read_b64_tr_b16 v[154:155], %1 offset:20992\n\t"
    "ds_read_b64_tr_b16 v[156:157], %2 offset:20480\n\t"
    "ds_read_b64_tr_b16 v[158:159], %2 offset:20992\n\t"
    "s_waitcnt lgkmcnt(0)"
    : "=&{v[128:159]}"(w) : "v"(a0), "v"(a1) : "memory");
}
template <> __device__ __forceinline__ void gw_load<1>(u32x32& w, unsigned a0, unsigned a1) {
  asm volatile("ds_read_b64_tr_b16 v[160:161], %1 offset:0\n\t"
    "ds_read_b64_tr_b16 v[162:163], %1 offset:512\n\t"
    "ds_read_b64_tr_b16 v[164:165], %2 offset:0\n\t"
    "ds_read_b64_tr_b16 v[166:167], %2 offset:512\n\t"
    "ds_read_b64_tr_b16 v[168:169], %1 offset:4096\n\t"
    "ds_read_b64_tr_b16 v[170:171], %1 offset:4608\n\t"
    "ds_read_b64_tr_b16 v[172:173], %2 offset:4096\n\t"
    "ds_read_b64_tr_b16 v[174:175], %2 offset:4608\n\t"
    "s_waitcnt lgkmcnt(0)\n\t"
    "ds_read_b64_tr_b16 v[176:177], %1 offset:16384\n\t"
    "ds_read_b64_tr_b16 v[178:179], %1 offset:16896\n\t"
    "ds_read_b64_tr_b16 v[180:181], %2 offset:16384\n\t"
    "ds_read_b64_tr_b16 v[182:183], %2 offset:16896\n\t"
    "ds_read_b64_tr_b16 v[184:185], %1 offset:20480\n\t"
    "ds_read_b64_tr_b16 v[186:187], %1 offset:20992\n\t"
    "ds_read_b64_tr_b16 v[188:189], %2 offset:20480\n\t"
    "ds_read_b64_tr_b16 v[190:191], %2 offset:20992\n\t"
    "s_waitcnt lgkmcnt(0)"
    : "=&{v[160:191]}"(w) : "v"(a0), "v"(a1) : "memory");
}
template <> __device__ __forceinline__ void gw_load<2>(u32x32& w, unsigned a0, unsigned a1) {
  asm volatile("ds_read_b64_tr_b16 v[192:193], %1 offset:0\n\t"
    "ds_read_b64_tr_b16 v[194:195], %1 offset:512\n\t"
    "ds_read_b64_tr_b16 v[196:197], %2 offset:0\n\t"
    "ds_read_b64_tr_b16 v[198:199], %2 offset:512\n\t"
    "ds_read_b64_tr_b16 v[200:201], %1 offset:4096\n\t"
    "ds_read_b64_tr_b16 v[202:203], %1 offset:4608\n\t"
    "ds_read_b64_tr_b16 v[204:205], %2 offset:4096\n\t"
    "ds_read_b64_tr_b16 v[206:207], %2 offset:4608\n\t"
    "s_waitcnt lgkmcnt(0)\n\t"
    "ds_read_b64_tr_b16 v[208:209], %1 offset:16384\n\t"
    "ds_read_b64_tr_b16 v[210:211], %1 offset:16896\n\t"
    "ds_read_b64_tr_b16 v[212:213], %2 offset:16384\n\t"
    "ds_read_b64_tr_b16 v[214:215], %2 offset:16896\n\t"
    "ds_read_b64_tr_b16 v[216:217], %1 offset:20480\n\t"
    "ds_read_b64_tr_b16 v[218:219], %1 offset:20992\n\t"
    "ds_read_b64_tr_b16 v[220:221], %2 offset:20480\n\t"
    "ds_read_b64_tr_b16 v[222:223], %2 offset:20992\n\t"
    "s_waitcnt lgkmcnt(0)"
    : "=&{v[192:223]}"(w) : "v"(a0), "v"(a1) : "memory");
}
template <> __device__ __forceinline__ void gw_load<3>(u32x32& w, unsigned a0, unsigned a1) {
  asm volatile("ds_read_b64_tr_b16 v[224:225], %1 offset:0\n\t"
    "ds_read_b64_tr_b16 v[226:227], %1 offset:512\n\t"
    "ds_read_b64_tr_b16 v[228:229], %2 offset:0\n\t"
    "ds_read_b64_tr_b16 v[230:231], %2 offset:512\n\t"
    "ds_read_b64_tr_b16 v[232:233], %1 offset:4096\n\t"
    "ds_read_b64_tr_b16 v[234:235], %1 offset:4608\n\t"
    "ds_read_b64_tr_b16 v[236:237], %2 offset:4096\n\t"
    "ds_read_b64_tr_b16 v[238:239], %2 offset:4608\n\t"
    "s_waitcnt lgkmcnt(0)\n\t"
    "ds_read_b64_tr_b16 v[240:241], %1 offset:16384\n\t"
    "ds_read_b64_tr_b16 v[242:243], %1 offset:16896\n\t"
    "ds_read_b64_tr_b16 v[244:245], %2 offset:16384\n\t"
    "ds_read_b64_tr_b16 v[246:247], %2 offset:16896\n\t"
    "ds_read_b64_tr_b16 v[248:249], %1 offset:20480\n\t"
    "ds_read_b64_tr_b16 v[250:251], %1 offset:20992\n\t"
    "ds_read_b64_tr_b16 v[252:253], %2 offset:20480\n\t"
    "ds_read_b64_tr_b16 v[254:255], %2 offset:20992\n\t"
    "s_waitcnt lgkmcnt(0)"
    : "=&{v[224:255]}"(w) : "v"(a0), "v"(a1) : "memory");
}
template <int I, int E, class F> __device__ __forceinline__ void sfor(F&& f) { if constexpr (I < E) { f(std::integral_constant<int, I>{}); sfor<I + 1, E>(f); } }
template <int ROLE, int KQ, class F>
__device__ __forceinline__ void wload(char* smem, int wave_id_, const u16* W0, const u16* W1, int ldb, u32x32 (&bw)[4], int rot, F&& hook) {
  constexpr int ABUF = 65536;
  const int tid = opaque_tid(), lane = tid & 63, wave = WAVE_ID;
  const int kq = KQ == 2 ? wave >> 2 : 0, nq = KQ == 2 ? wave & 3 : wave;
  const int g = lane >> 4, r16 = lane & 15, q4 = r16 >> 2, p4 = lane & 3;
  const unsigned ldsb = __builtin_amdgcn_readfirstlane((unsigned)(size_t)(LAS char*)smem);
  {
    const int srow = 8 * wave + (lane >> 3), ch = (lane & 7) ^ bswz(srow);
    const unsigned voff = (unsigned)(srow * ldb + ch * 8) * 2;
    const char* w0 = uni((const char*)W0); const char* w1 = uni((const char*)(ROLE == R_F1 ? W1 : W0 + 64));
    const size_t kb = (size_t)64 * ldb * 2;
    auto wstage = [&](int r) {
      const unsigned ld = ldsb + (r & 1) * ABUF + wave * 1024;
#pragma unroll
      for (int i = 0; i < 8; ++i) {
        const int kbi = KQ == 2 ? (ROLE == R_PART2 ? 0 : (i >> 2) * 8) + ((2 * r + ((i >> 1) & 1)) ^ rot) : 2 * r + (i >> 2);
        const char* src = KQ == 2 ? ((i & 1) ? w1 : w0) + kbi * kb + (ROLE == R_PART2 ? (i >> 2) * 256 : 0) : w0 + kbi * kb + (i & 3) * 128;
        unsigned keep;
        asm volatile("s_mov_b32 %0, m0\n\ts_mov_b32 m0, %3\n\ts_nop 2\n\tglobal_load_lds_dwordx4 %1, %2" WNT_ "\n\ts_mov_b32 m0, %0"
                     : "=&s"(keep) : "v"(voff), "s"(src), "s"(ld + i * 8192) : "memory");
      }
    };
    const int brow = 8 * g + q4, bs = bswz(brow);
    unsigned la[2];
#pragma unroll
    for (int j = 0; j < 2; ++j) {
      int blk, c16, sub;
      if (ROLE == R_F1) { blk = j; c16 = nq * 2 + (p4 >> 1); sub = (p4 & 1) * 8; }
      else { blk = nq >> 1; c16 = (nq & 1) * 4 + p4; sub = j * 8; }
      int set = kq;
      if (ROLE == R_PART2 && TLP) { set = nq >> 1; blk = nq & 1; c16 = kq * 4 + p4; }
      la[j] = (unsigned)((KQ == 2 ? set * 4 + blk : blk) * 8192 + brow * 128 + ((c16 ^ bs) << 4) + sub);
    }
    hook(4);
    wstage(0); wstage(1);
    sfor<0, 4>([&](auto r_c) __attribute__((always_inline)) {
      constexpr int r = decltype(r_c)::value;
      if (r < 3) WAIT_V(8); else WAIT_V(0);
      __builtin_amdgcn_s_barrier(); asm volatile("" ::: "memory");
      gw_load<r>(bw[r], ldsb + (r & 1) * ABUF + la[0], ldsb + (r & 1) * ABUF + la[1]);
      BAR();
      if (r + 2 < 4) wstage(r + 2);
      hook(r);
    });
  }
}
#define DSR2(d0, d1, a0_, a1_, off) asm volatile("ds_read_b128 %0, %2 offset:%4\n\tds_read_b128 %1, %3 offset:%4" : "=&v"(d0), "=&v"(d1) : "v"(a0_), "v"(a1_), "i"(off))
template <int N> __device__ __forceinline__ void waitl(u32x4& a, u32x4& b) { asm volatile("s_waitcnt lgkmcnt(%2)" : "+v"(a), "+v"(b) : "i"(N)); }
__device__ __forceinline__ void glds1(const char* sbase, unsigned voff, unsigned lds) {
  unsigned keep;
  asm volatile("s_mov_b32 %0, m0\n\ts_mov_b32 m0, %3\n\ts_nop 2\n\tglobal_load_lds_dwordx4 %1, %2 sc1\n\ts_mov_b32 m0, %0"
               : "=&s"(keep) : "v"(voff), "s"(sbase), "s"(lds) : "memory");
}
#define XS(i) do { } while (0)
#define XFL(b_) do { } while (0)
#define XA_ 0
#define XF_ 0
#define XE_ 0
#define XP_ 0
#define PUBK 9
#define LATE 0
#define NOWP 0
#define TB 48
#define TDM 1
#define TDA 15
#define TPUB 1
#define DUR0 160
#define PUP 4
#define PDQ 5
#define PDM 2
#define PDEEP 5
template <int ROLE>
__device__ __forceinline__ void gemm7(char* smem, int wave_id_, const u16* W0, const u16* W1, int ldb, const char* A_, size_t a_blk, Dep dep,
                                      char* C_, size_t c_blk, unsigned*& cnt, int myw, unsigned* tmo, int slot, unsigned long long* mts,
                                      bool gl, int nimm = 0, int nseg = NSEG, int wm0 = 0, int pofs = 0, u64 tag = 0, int b0 = 0) {
  dep.base = (const char*)cnt;
  unsigned* myprog = cnt + myw;
  volatile LAS unsigned* wsl = (volatile LAS unsigned*)(smem + L_SLOT + 512);
  if (opaque_tid() < 144) *(volatile LAS unsigned*)(smem + L_SLOT + opaque_tid() * 4) = 0u;
  const int tid = opaque_tid(), lane = tid & 63, wave = WAVE_ID;
  const int kq = wave >> 2, nq = wave & 3;
  const unsigned ldsb = __builtin_amdgcn_readfirstlane((unsigned)(size_t)(LAS char*)smem);
  constexpr int rot = 0;
  const char* A = uni(A_) + nq * 1024;
  const char* Cu = uni(C_);
  constexpr int PD = PDM;
  constexpr bool P2C = ROLE == R_PART2;
  int pw = 0;
  int wm = wm0, nh = 2 * b0;
  constexpr bool SPEC = ROLE != R_QKV;
  auto lgo = [&](int b, unsigned& v, unsigned& u, bool pre, bool force) {
    if (wave == 4) {
      for (int spins = 0; __builtin_amdgcn_readfirstlane(wsl[2]) != (unsigned)(b + 1); ++spins) {
        if (!pre) ld_fb(v, u, dep.base, dep.voff);
        const bool ok = (int)(v * dep.mul + dep.add) > b;
        if (__all(ok)) break;
        if (SPEC && !force && bells(ok, u, b, b0 ? MARH : MARG)) break;
        if (!pre) __builtin_amdgcn_s_sleep(2);
        pre = false;
        if (give_up(spins, tmo, 9)) { v = 255u; break; }
      }
      wsl[2] = (unsigned)(b + 1);
    } else {
      while (__builtin_amdgcn_readfirstlane(wsl[2]) != (unsigned)(b + 1)) __builtin_amdgcn_s_sleep(1);
    }
  };
  auto lwm = [&](int b, unsigned v) {
    unsigned w;
    if (wave == 4) {
      const int w2 = (int)dep_eval(v, dep);
      w = (unsigned)min(max(max(w2, wm), b + 1), 255);
      wsl[0] = ((unsigned)(b + 1) << 8) | w;
    } else {
      for (;;) {
        w = __builtin_amdgcn_readfirstlane(wsl[0]);
        if ((w >> 8) == (unsigned)(b + 1)) break;
        __builtin_amdgcn_s_sleep(1);
      }
      w &= 255u;
    }
    return (int)w;
  };
  auto pubw = [&](int n) {
    WAIT_V(0);
    if (lane == 0) {
      const unsigned o = __hip_atomic_fetch_add((LAS unsigned*)(smem + L_SLOT + 516 + ((n + pofs) & 1) * 36), 1u, __ATOMIC_RELAXED, __HIP_MEMORY_SCOPE_WORKGROUP);
      if ((o & 3u) == 3u) {
        st4_sc1(myprog, (unsigned)(n + pofs));
      }
    }
    asm volatile("s_waitcnt lgkmcnt(0)" ::: "memory");
  };
  auto hsrc = [&](int hh) { return A + (size_t)(hh >> 1) * a_blk + ((hh ^ (rot >> 2)) & 1) * 16384; };
  auto hlds = [&](int hh) { return ldsb + (unsigned)(((hh >> 1) & 1) * 65536 + (hh & 1) * 16384 + nq * 1024); };
#define POFF(i_) (((i_) & 1) * 32768 + ((i_) >> 1) * 4096)
#define PIECE(s_, l_, i_) asm volatile("s_add_u32 m0, %2, %3\n\ts_nop 0\n\tglobal_load_lds_dwordx4 %0, %1 sc1" :: "v"(av[i_]), "s"(s_), "s"(l_), "i"(POFF(i_)) : "memory", "scc", "m0")
  int na = max(SB, b0);
  auto arm = [&](int bb) {
    unsigned z = SENT; asm volatile("" : "+v"(z));
    constexpr int N = ROLE == R_F1 ? 1 : (P2C ? 4 : 2);
    const unsigned o = nq * N * 1024 + lane_id() * 16;
    const char* p = Cu + (size_t)bb * c_blk;
#pragma unroll
    for (int i = 0; i < N; ++i) st16s(p, o + i * 1024, u32x4{z, z, z, z});
  };
  u32x32 BW[4];
  wload<ROLE, 2>(smem, wave_id_, W0, W1, ldb, BW, rot, [&](int r) {
    if (ROLE == R_QKV && nimm > 0 && wave == 0) STS((char*)tmo - OFF_TMO, 128 + slot, 3 + r);
    if (SPEC && r == 2 && (ROLE != R_PART || gl)) { for (na += kq; na < nseg; na += 2) arm(na); na = nseg; }
    if (ROLE == R_QKV && nimm > 0 && r == 0 && wave == 4) init_issue((char*)tmo - OFF_TMO, ldsb + L_RED);
    if (ROLE == R_QKV && nimm > 0 && r == 2) {
      cnt = cnt_of((char*)tmo - OFF_TMO, init_get((char*)tmo - OFF_TMO, tag, smem + L_RED));
      dep.base = (const char*)cnt; myprog = cnt + myw;
      if (kq) {
        unsigned v0 = 0, u0;
        lgo(0, v0, u0, false, true);
        wm = lwm(0, v0);
        const unsigned l16 = lane_id() * 16;
#pragma unroll
        for (int i = 0; i < 16; ++i) glds1(hsrc(i >> 3) + (i & 1) * 32768 + ((((i & 7) >> 1) ^ rot) & 3) * 4096, l16, hlds(i >> 3) + POFF(i & 7));
      }
      nh = 2;
    }
  });
  if (ROLE == R_QKV && nimm > 0 && !kq) wm = (int)(__builtin_amdgcn_readfirstlane(wsl[0]) & 255u);
  if (ROLE == R_QKV && nimm > 0 && opaque_tid() == 0) STS((char*)tmo - OFF_TMO, 128 + slot, 0);
  if (wave == 0) WFB();
  auto body = [&](auto ldr_c) __attribute__((always_inline)) {
    constexpr bool LDR = decltype(ldr_c)::value;
    if (XP_ == 0 ? !LDR : (XP_ == 1 ? LDR : false)) __builtin_amdgcn_s_setprio(1);
    unsigned av[8];
#pragma unroll
    for (int i = 0; i < 8; ++i) av[i] = lane * 16 + (P2C ? 0 : (i & 1) * 32768) + (((i >> 1) ^ rot) & 3) * 4096;
    auto issue_all = [&](int hh) {
      if (LDR) {
        const char* s_ = hsrc(hh); const unsigned l_ = hlds(hh);
#pragma unroll
        for (int i = 0; i < 8; ++i) if (!(P2C && (i & 1))) PIECE(s_, l_, i);
      }
    };
    int b = b0;
    bool pre = false;
    unsigned smu = 0, tk = 0;
    unsigned smp = 0, got = 0;
    while (b < nseg) {
      const bool fast = (wm < nseg ? wm : nseg) >= b + PD && b >= nimm && nh <= 2 * b + 2;
      if (SPEC && b >= TB && !fast) break;
      const bool pl = wm <= b || pre, cold = nh < 2 * b + 2, nh3 = nh == 2 * b + 3;
      if (!fast) {
        XS(0);
        if (LDR && wm <= b && !got) lgo(b, smp, smu, false, true);
        XS(1);
        if (got) nh += NEARLY / 8;
        { const int upto = nh3 ? 2 * b + 4 : 2 * b + 2; for (; nh < upto; ++nh) issue_all(nh); }
        if (LDR && pl) wm = lwm(b, smp);
        XS(2);
      }
      const int ln = lane_id(), g = ln >> 4, r16 = ln & 15;
      const unsigned e0 = ldsb + (unsigned)((P2C ? 0 : kq * 32768) + (b & 1) * 65536 + (gl ? (g >> 1) * 512 + r16 * 32 + (g & 1) * 16 : r16 * 128 + ((g ^ (r16 & 7)) << 4)));
      const unsigned d0 = e0 ^ (gl ? 1024u : 64u);
      const unsigned rw = ldsb + L_RED + nq * 4096 + ln * 16;
      const unsigned co0 = ROLE == R_F1 ? (unsigned)((g & 1) * 2048 + nq * 512 + r16 * 32 + (g >> 1) * 16)
                         : ((ROLE == R_QKV ? TLQ : TLP) ? (unsigned)(nq * (P2C ? 4096 : 2048) + r16 * 64 + g * 16)
                         : (ROLE == R_QKV ? (unsigned)(r16 * 256 + (((4 * nq + g) ^ r16) << 4)) : (unsigned)(r16 * PROW + (32 * nq + 8 * g) * 2)));
      const unsigned co1 = co0 + ((ROLE == R_QKV ? TLQ : TLP) ? 1024u : (ROLE == R_QKV ? 4096u : 16u * PROW));
      u32x16 pv_;
      bool pr2 = false;

      if (fast) {
        __builtin_amdgcn_s_barrier(); asm volatile("" ::: "memory");
        const int LA = (wm < nseg ? wm : nseg) >= b + (ROLE == R_QKV ? PDQ : PDEEP) ? 3 : 2;
        for (; nh < 2 * b + LA; ++nh) issue_all(nh);
        int lf = (wm < nseg ? wm : nseg) - (LA - 1), cp = nseg - (LA - 1);
        const unsigned sa = ldsb + L_SLOT + ln * 4, ad = dep.add - (unsigned)(LA - 1);
        const unsigned up = LA == 3 ? 1000u : (unsigned)PUP;
#define FARGS BW, pv_, e0, d0, b, lf, cp, up, sa, dep.mul, ad, rw, (unsigned)a_blk, hsrc(2 * b + LA), hsrc(2 * b + LA + 1), hlds(2 * b + LA), hlds(2 * b + LA + 1), av
        if constexpr (LDR) (P2C ? fast_ld2 : (ROLE == R_F1 ? fast_ld1 : fast_ld0))(FARGS, LA);
#undef FARGS
        else {
          const char* c0_ = uni((const char*)tmo - OFF_TMO + OFF_DUMP); const char* c1_ = Cu + (size_t)b * c_blk;
          const int sf = b + pofs;
          const char* pg = uni((const char*)myprog);
          (ROLE == R_F1 ? fast_st1 : (P2C ? fast_st2 : fast_st0))(BW, pv_, e0, d0, b, lf, cp, up, sa, dep.mul, ad, rw, (unsigned)c_blk, c0_, c1_, co0, co1, pofs, sf, (unsigned)wave,
                                                                   ldsb + L_SLOT, dep.voff, dep.base, pg);
        }
        wm = lf + LA - 1 > wm ? lf + LA - 1 : wm;
        nh = 2 * b + 2;
        --b;
        __builtin_amdgcn_s_barrier(); asm volatile("" ::: "memory");
      } else {
        if (LDR) { if (nh >= 2 * b + 4) { if (P2C) WAIT_V(14); else WAIT_V(28); } else { if (P2C) WAIT_V(6); else WAIT_V(12); } }
        BAR();
        XS(3);
        if (ROLE == R_QKV && nimm > 0 && b < 2 && opaque_tid() == 0) STS((char*)tmo - OFF_TMO, 128 + slot, 1 + b);
        if constexpr (!LDR) {
          const unsigned wv = wsl[0];
          unsigned st;
          lean_st0(BW, e0, d0, __builtin_amdgcn_readfirstlane(pw > 0 ? (cold ? 4u : 1u) : 0u), pv_, (unsigned)(pw + pofs), ldsb + L_SLOT + 516, uni((const char*)myprog), 0u, tk, st);
          if (pl) wm = (int)(__builtin_amdgcn_readfirstlane(wv) & 255u);
        }
        const bool dm = nh == 2 * b + 2 && wm > b + 1 && b + 1 < nseg;
        pr2 = b + 1 < nseg && wm < nseg;
        if constexpr (LDR) {
          const bool er = nh == 2 * b + 2 && !dm;
          const unsigned ps = pr2 && (wave == 4 || er) ? (unsigned)(b + 1) : 0u;
#define LARGS BW, e0, d0, (unsigned)(nh - 2 * b - 2), hlds(nh), hsrc(nh), hsrc(nh + 1), av, ps, dep.voff, dep.base, \
              rw, ldsb + L_SLOT + 528 + nq * 4, (unsigned)(b + 1), ldsb + L_SLOT + 520, dep.mul, dep.add, er ? b + 1 : 0x7FFFFFFF, 0x7FFFFFFF, smp, smu, got
          if (dm) (P2C ? lean_dm1 : lean_dm0)(LARGS); else (P2C ? lean_ld1 : lean_ld0)(LARGS);
#undef LARGS
          got = __builtin_amdgcn_readfirstlane(got);
        }
        if (dm) nh += 2;
        XS(5);
      }
      unsigned bad = 0, pk = 0;
      if constexpr (LDR) { if (fast) fin_ld(pv_, rw, ldsb + L_SLOT + 528 + nq * 4, (unsigned)(b + 1)); }
      else (ROLE == R_F1 ? fin_st1 : (P2C ? fin_st2 : fin_st0))(pv_, rw, ldsb + L_SLOT + 528 + nq * 4, (unsigned)(b + 1), Cu + (size_t)b * c_blk, co0, co1, ldsb + L_SLOT + 524, bad, pk);
      XS(6);
      pre = pr2;
      bool now = (nh < 2 * b + 4 && wm < b + 1 + PD) || b + 1 >= nseg || b < nimm;
      if (!LDR && now) {
        if (NOWP == 0 && b >= nimm && pk == (unsigned)(b + 2)) now = false;
        else pubw(b + 1);
      }
      XS(7);
      XFL(b);
      pw = now ? 0 : b + 1;
      if (SPEC && !LDR && !fast && b >= 16 && na < nseg && na > b + 1) { arm(na); arm(na + 1); na += 2; }
      ++b;
    }
    if (SPEC && b < nseg) {
      static_assert(!SPEC || (NEARLY == 16 && TLP), "");
      const int ln = lane_id(), g = ln >> 4, r16 = ln & 15;
      const unsigned e0 = ldsb + (unsigned)((P2C ? 0 : kq * 32768) + (gl ? (g >> 1) * 512 + r16 * 32 + (g & 1) * 16 : r16 * 128 + ((g ^ (r16 & 7)) << 4)));
      const unsigned rw = ldsb + L_RED + nq * 4096 + ln * 16;
      const unsigned co0 = ROLE == R_F1 ? (unsigned)((g & 1) * 2048 + nq * 512 + r16 * 32 + (g >> 1) * 16) : (unsigned)(nq * (P2C ? 4096 : 2048) + r16 * 64 + g * 16);
      const char* pg = uni((const char*)myprog);
      bool force = false, dmx = false;
      unsigned dur = DUR0, dp = 0, x8 = __builtin_amdgcn_readfirstlane(wave == 0 ? 8u : 0u);
      if (nh >= 2 * b + 2) got = 1;
      pre = false;
      for (;;) {
        unsigned rd;
        XS(0);
        if (LDR && b < nseg) {
          if (!got) { lgo(b, smp, smu, pre, force); issue_all(2 * b); issue_all(2 * b + 1); }
          XS(2);
          if (P2C) WAIT_V(6); else WAIT_V(12);
        }
        if (!LDR && b >= SB && na <= nseg) { for (na = max(na, b); na < nseg; ++na) arm(na); WAIT_V(0); ++na; }
        const unsigned ea = e0 + (b & 1) * 65536;
        BAR();
        XS(3);
        if (b >= nseg) rd = __builtin_amdgcn_readfirstlane(wsl[8]);
        else if constexpr (LDR) {
          const int th = b + 1 < nseg ? b + 1 : 0;
#define LARGS BW, ea, ea ^ (gl ? 1024u : 64u), 0u, hlds(2 * b + 2), hsrc(2 * b + 2), hsrc(2 * b + 3), av, (unsigned)th, dep.voff, dep.base, \
              rw, ldsb + L_SLOT + 528 + nq * 4, (unsigned)(b + 1), ldsb + L_SLOT + 520, dep.mul, dep.add, th, th >= SB ? th : 0x7FFFFFFF, smp, smu, got
          if (dmx) {
            *(volatile LAS u32x2*)(wsl + 2) = u32x2{(unsigned)(b + 2), (unsigned)(b + 2)};
            (P2C ? lean_dm1 : lean_dm0)(LARGS);
          } else (P2C ? lean_ld1 : lean_ld0)(LARGS);
#undef LARGS
          got = __builtin_amdgcn_readfirstlane(got) | (dmx ? 1u : 0u);
          rd = got >> 1;
          dmx = TDM && got == 1u && b + 2 < nseg
             && __all((int)(smp * dep.mul + dep.add) > b + 2 || (b + 2 >= SB && (int)(smu >> 24) + ((int)((rt_now() + (dmx ? TDA : 0) - MARG - smu) << 8) >> 31) > b + 2));
          pre = true;
        } else {
          u32x16 pv_;
          lean_st0(BW, ea, ea ^ (gl ? 1024u : 64u), b >= SB - 2 ? (unsigned)min(pw, 1) | x8 : (unsigned)min(pw, 1) * TPUB, pv_, (unsigned)pw, ldsb + L_SLOT + 516, pg, (unsigned)(b + 1) << 24 | dur, tk, rd);
          rd = __builtin_amdgcn_readfirstlane(rd);
          XS(5);
          if (!rd) {
            unsigned bad, pk;
            (ROLE == R_F1 ? fin_st1 : (P2C ? fin_st2 : fin_st0))(pv_, rw, ldsb + L_SLOT + 528 + nq * 4, (unsigned)(b + 1), Cu + (size_t)b * c_blk, co0, co0 + 1024u,
                                                                 ldsb + L_SLOT + 524, bad, pk);
            XS(6);
            pw = b + 1;
            if (x8 && b >= SB - 2) { const unsigned d = min(rt_now() - __builtin_amdgcn_readfirstlane(tk), 300u); dur = max(d, dp); dp = d; }
            if (bad && !force && b >= SB) wsl[8] = 1u;
            else if (b == SB - 1 || (pk != (unsigned)(b + 2) && __builtin_amdgcn_readfirstlane(wsl[2]) != (unsigned)(b + 2))) {
              pubw(b + 1);
              pw = 0;
            }
            if (na < nseg && na > b + 1) { arm(na); arm(na + 1); na += 2; }
          }
        }
        XS(7);
        if (rd) {
          WAIT_V(0);
          BAR();
          if (wave == 4 && ln < 11) wsl[ln] = 0u;
          BAR();
          --b; pw = 0; pre = false; got = 0; force = true;
          continue;
        }
        if (b >= nseg) break;
        force = false;
        ++b;
      }
    }
    __builtin_amdgcn_s_setprio(0);
  };
  if (kq) body(std::true_type{}); else body(std::false_type{});
#undef PIECE
#undef POFF
  WAIT_V(0);
  BAR();
}
__device__ __forceinline__ void fin_role(char* smem, int wave_id_, int fidx, const float* mod, const char* P2, const u16* x1, u16* out, unsigned* cnt, unsigned* tmo,
                                         unsigned long long* mts) {
  const int tid = opaque_tid(), lane = tid & 63, wave = WAVE_ID;
  row_table(smem + L_GB, mod, cnt, tmo, tid);
  volatile LAS unsigned* wsl = (volatile LAS unsigned*)(smem + L_SLOT + 512);
  int wf = 0, par = 0;
  const unsigned pf2 = (unsigned)(PF2 + ((lane & 31) < 28 ? (lane & 31) : 0)) * 128, pln = (unsigned)((PLN + (lane >> 3)) * 128 + (lane & 7) * 4);
  for (int nf = fidx >> 3; nf < NSEG; nf += 2) {
    if (wf <= nf) {
      if (wave == 0) {
        unsigned w;
        for (int spins = 0;; ++spins) {
          unsigned v, u;
          ld_fb(v, u, cnt, pf2 + (nf >= HB ? 4 : 0));
          if (!__all(v > (unsigned)nf) && bells(v > (unsigned)nf, u, nf, nf >= HB ? MARL : MARF)) { w = 0; break; }
          v = rowmin16(v);
          w = min(rdl(v, 0), rdl(v, 16));
          if ((int)w > nf) break;
          __builtin_amdgcn_s_sleep(2);
          if (give_up(spins, tmo, 9)) { w = NSEG; break; }
        }
        if (lane == 0) wsl[par] = w;
      }
      BAR();
      wf = (int)__builtin_amdgcn_readfirstlane(wsl[par]);
      par ^= 1;
    }
    int ln = lane; asm volatile("" : "+v"(ln));
    if (wave == 0) WTS(256 + 8 + fidx, nf / 2, 0);
    bool fin = nf < SB;
    for (int spins = 0; !fin_half(fin, P2, x1, smem + L_GB + 12288, out, nf * 32 + 4 * (fidx & 7) + (wave >> 1), wave & 1, ln); ++spins)
      fin = __all(ld4_sc1((const char*)cnt + pf2 + (nf >= HB ? 4 : 0)) > (unsigned)nf && ld4_sc1((const char*)cnt + pln) > (unsigned)nf) || give_up(spins, tmo, 9);
    if (wave == 0) WTS(256 + 8 + fidx, nf / 2, 2);
  }
}
#define LD8(r, vo, sb) asm volatile("global_load_dwordx4 %0, %8, %12 sc1\n\tglobal_load_dwordx4 %1, %9, %12 sc1\n\t" \
    "global_load_dwordx4 %2, %10, %12 sc1\n\tglobal_load_dwordx4 %3, %11, %12 sc1\n\t" \
    "global_load_dwordx4 %4, %8, %13 sc1\n\tglobal_load_dwordx4 %5, %9, %13 sc1\n\t" \
    "global_load_dwordx4 %6, %10, %13 sc1\n\tglobal_load_dwordx4 %7, %11, %13 sc1" \
    : "=&v"(r[0][0]), "=&v"(r[0][1]), "=&v"(r[0][2]), "=&v"(r[0][3]), "=&v"(r[1][0]), "=&v"(r[1][1]), "=&v"(r[1][2]), "=&v"(r[1][3]) \
    : "v"(vo), "v"(vo ^ (TLQ ? 2048u : 64u)), "v"(vo ^ (TLQ ? 4096u : 128u)), "v"(vo ^ (TLQ ? 6144u : 192u)), "s"(sb), "s"((sb) + (TLQ ? 1024 : 4096)) : "memory")
#define TIE8(r) "+v"(r[0][0]), "+v"(r[0][1]), "+v"(r[0][2]), "+v"(r[0][3]), "+v"(r[1][0]), "+v"(r[1][1]), "+v"(r[1][2]), "+v"(r[1][3])
#define SADL 255
#define DSA 20
#define SASL 8
__device__ __forceinline__ void sa_role(char* smem, int wave_id_, int hbase, const char* __restrict__ Q, const char* __restrict__ Kx, const char* __restrict__ V,
                                        const char* kvm, char* __restrict__ O, unsigned* cnt, unsigned* tmo, int slot, unsigned long long* mts) {
  constexpr float LS = LOG2E * SCALE;
  const int tid = opaque_tid(), wave = WAVE_ID;
  const int h = hbase + (wave & 1), s0 = wave >> 1;
  char* vl = smem + wave * 17408;
  const unsigned ldsw = __builtin_amdgcn_readfirstlane((unsigned)(size_t)(LAS char*)vl);
  unsigned avail = 0;
  {
    static_assert(SB % 4 == 0, "");
    unsigned z = SENT; asm volatile("" : "+v"(z));
    for (int s = SB + s0; s < NSEG; s += 4)
#pragma unroll
      for (int i = 0; i < 8; ++i) st16s(uni(O + ((size_t)s * 64 + 2 * h) * 4096), (unsigned)(i * 1024 + (tid & 63) * 16), u32x4{z, z, z, z});
  }
  for (int s = s0, it = 1; s < NSEG; s += 4, ++it) {
    u32x4 qv[2][4], kv[2][4];
    int lane = tid & 63; asm volatile("" : "+v"(lane));
    const int g = lane >> 4, r16 = lane & 15, q4 = r16 >> 2, p4 = lane & 3;
    auto request = [&](int sr) {
      const size_t o = ((size_t)sr * NH + h) * 8192;
      const char* qs = uni(Q + o); const char* ks = uni(Kx + o); const char* vs = uni(V + o);
      const unsigned fo = TLQ ? (unsigned)(r16 * 64 + g * 16) : (unsigned)(r16 * 256 + ((g ^ r16) << 4));
      LD8(qv, fo, qs); LD8(kv, fo, ks);
      head_to_lds(vs, lane, ldsw);
    };
    {
      for (int spins = 0; avail <= (unsigned)s; ++spins) {
        const unsigned p = ld4_sc1(cnt + (PQKV + h) * 32 + (lane & 15));
        avail = min(min(qkv_avail(p, 0), qkv_avail(p, 1)), qkv_avail(p, 2));
        if (avail > (unsigned)s) break;
        __builtin_amdgcn_s_sleep(32);
        if (give_up(spins, tmo, 10)) break;
      }
      request(s);
      asm volatile("s_waitcnt vmcnt(0)" : TIE8(qv), TIE8(kv) :: "memory");
    }
    WTM(slot, s, 0);
    f32x4 s00 = f32x4{0, 0, 0, 0}, s01 = s00, s11 = s00;
#pragma unroll
    for (int ks = 0; ks < 4; ++ks) {
      s00 = MFMA32(asbf8(kv[0][ks]), asbf8(qv[0][ks]), s00);
      s01 = MFMA32(asbf8(kv[0][ks]), asbf8(qv[1][ks]), s01);
      s11 = MFMA32(asbf8(kv[1][ks]), asbf8(qv[1][ks]), s11);
    }
    float mx[2], sum[2];
    u32x4 pseg[2];
    {
#pragma unroll
      for (int r = 0; r < 4; ++r) { const bool ok = 4 * g + r <= r16; s00[r] = ok ? s00[r] : -3.0e38f; s11[r] = ok ? s11[r] : -3.0e38f; }
      mx[0] = red16_32_max(max3(max3(s00[0], s00[1], s00[2]), s00[3], -1.0e38f));
      mx[1] = red16_32_max(max3(max3(fmaxf(fmaxf(s01[0], s01[1]), fmaxf(s01[2], s01[3])), s11[0], -1.0e38f), max3(s11[1], s11[2], s11[3]), -1.0e38f));
      const float n0 = -mx[0] * LS, n1 = -mx[1] * LS;
#pragma unroll
      for (int r = 0; r < 4; ++r) {
        s00[r] = __builtin_amdgcn_exp2f(s00[r] * LS + n0); s01[r] = __builtin_amdgcn_exp2f(s01[r] * LS + n1); s11[r] = __builtin_amdgcn_exp2f(s11[r] * LS + n1);
      }
      sum[0] = (s00[0] + s00[1]) + (s00[2] + s00[3]);
      sum[1] = ((s01[0] + s01[1]) + (s01[2] + s01[3])) + ((s11[0] + s11[1]) + (s11[2] + s11[3]));
      pseg[0] = u32x4{pack2(s00[0], s00[1]), pack2(s00[2], s00[3]), 0u, 0u};
      pseg[1] = u32x4{pack2(s01[0], s01[1]), pack2(s01[2], s01[3]), pack2(s11[0], s11[1]), pack2(s11[2], s11[3])};
    }
    f32x4 o[8][2];
#pragma unroll
    for (int dt = 0; dt < 8; ++dt) {
      const int row0 = 4 * g + q4;
      const unsigned xo = (unsigned)(row0 * 256 + (((4 * (dt >> 1) + p4) ^ row0) << 4) + (dt & 1) * 8);
      const u32x2 lo = trread(vl, xo), hi = trread(vl, xo + 4096);
      o[dt][0] = MFMA32(asbf8(u32x4{lo.x, lo.y, 0u, 0u}), asbf8(pseg[0]), (f32x4{0, 0, 0, 0}));
      o[dt][1] = MFMA32(asbf8(u32x4{lo.x, lo.y, hi.x, hi.y}), asbf8(pseg[1]), (f32x4{0, 0, 0, 0}));
    }
    float n0[2]; f32x4 sm[2];
#pragma unroll
    for (int b = 0; b < 2; ++b) { const float t = red16_32_add(sum[b]); sm[b] = f32x4{t, t, t, t}; n0[b] = -mx[b] * LS; }
    unsigned oa = (unsigned)(r16 * 128 + ((g ^ (r16 & 7)) << 4)), ka = (unsigned)(g * 256 + (r16 & 7) * 16), va = (unsigned)lane * 16;
    unsigned one = g < 2 ? 0x3F803F80u : 0u;
    u32x4 kmv[4], vmv[4];
#pragma unroll
    for (int i = 0; i < 4; ++i) vmv[i] = u32x4{0u, 0u, 0u, 0u};
#pragma unroll
    for (int dt = 0; dt < 8; ++dt) asm volatile("" : "+v"(o[dt][0]), "+v"(o[dt][1]), "+v"(vmv[dt >> 1]));
    asm volatile("s_waitcnt lgkmcnt(0)" : "+v"(sm[0]), "+v"(sm[1]), "+v"(n0[0]), "+v"(n0[1]), "+v"(oa), "+v"(ka), "+v"(va), "+v"(one) :: "memory");
    unsigned pg;
    for (int spins = 0;; ++spins) {
      pg = ld4_sc1(cnt + (PROG + h) * 32 + ((lane & 4) ? 5 : (lane & 3)));
      if (__all((pg >> 24) + (s >= SDIR ? 1u : 0u) > (unsigned)s)) break;
      __builtin_amdgcn_s_sleep(SASL);
      if (give_up(spins, tmo, 6)) break;
    }
    if (__all((pg >> 24) == (unsigned)s)) wait_rt(latest(pg), SADL);
    const char* mk = uni(kvm + ((size_t)s * NH + h) * 4096);
    for (int spins = 0;; ++spins) {
      asm volatile("global_load_dwordx4 %0, %8, %10 sc1\n\tglobal_load_dwordx4 %1, %8, %10 offset:128 sc1\n\t"
                   "global_load_dwordx4 %2, %8, %10 offset:1024 sc1\n\tglobal_load_dwordx4 %3, %8, %10 offset:1152 sc1\n\ts_mov_b32 exec_hi, 0\n\t"
                   "global_load_dwordx4 %4, %9, %10 offset:2048 sc1\n\tglobal_load_dwordx4 %5, %9, %10 offset:2560 sc1\n\t"
                   "global_load_dwordx4 %6, %9, %10 offset:3072 sc1\n\tglobal_load_dwordx4 %7, %9, %10 offset:3584 sc1\n\ts_mov_b32 exec_hi, -1\n\ts_waitcnt vmcnt(0)"
                   : "=&v"(kmv[0]), "=&v"(kmv[1]), "=&v"(kmv[2]), "=&v"(kmv[3]), "+v"(vmv[0]), "+v"(vmv[1]), "+v"(vmv[2]), "+v"(vmv[3])
                   : "v"(ka), "v"(va), "s"(mk) : "memory");
      unsigned mx_ = 0;
#pragma unroll
      for (int i = 0; i < 4; ++i) mx_ = max(max(max(max(mx_, kmv[i].x), kmv[i].y), max(kmv[i].z, kmv[i].w)), max(max(vmv[i].x, vmv[i].y), max(vmv[i].z, vmv[i].w)));
      if (__all(mx_ != SENT)) break;
      __builtin_amdgcn_s_sleep(2);
      if (give_up(spins, tmo, 6)) break;
    }
    WTM(slot, s, 1);
    f32x4 tt[2] = {f32x4{0, 0, 0, 0}, f32x4{0, 0, 0, 0}};
#pragma unroll
    for (int ks = 0; ks < 4; ++ks) { tt[0] = MFMA32(asbf8(kmv[ks]), asbf8(qv[0][ks]), tt[0]); tt[1] = MFMA32(asbf8(kmv[ks]), asbf8(qv[1][ks]), tt[1]); }
#pragma unroll
    for (int b = 0; b < 2; ++b)
#pragma unroll
      for (int r = 0; r < 4; ++r) tt[b][r] = tt[b][r] * LS + n0[b];
    if (!__all(max3(max3(tt[0][0], tt[0][1], tt[0][2]), max3(tt[0][3], tt[1][0], tt[1][1]), max2(tt[1][2], tt[1][3])) <= 40.0f)) {
#pragma unroll
      for (int b = 0; b < 2; ++b) {
        float m = max3(max3(tt[b][0], tt[b][1], tt[b][2]), tt[b][3], 0.0f);
        { float x, y; sw16(m, x, y); m = max2(x, y); }
        const float f = __builtin_amdgcn_exp2f(-m);
        tt[b] -= m; sm[b] *= f;
#pragma unroll
        for (int dt = 0; dt < 8; ++dt) o[dt][b] *= f;
      }
    }
    u32x2 pm[2]; float inv[2];
#pragma unroll
    for (int b = 0; b < 2; ++b) {
      pm[b] = u32x2{pack2(__builtin_amdgcn_exp2f(tt[b][0]), __builtin_amdgcn_exp2f(tt[b][1])), pack2(__builtin_amdgcn_exp2f(tt[b][2]), __builtin_amdgcn_exp2f(tt[b][3]))};
      sm[b] = __builtin_amdgcn_mfma_f32_16x16x16bf16_1k(ass4(u32x2{one, one}), ass4(pm[b]), sm[b], 0, 0, 0);
    }
#pragma unroll
    for (int dt = 0; dt < 8; ++dt)
#pragma unroll
      for (int b = 0; b < 2; ++b) o[dt][b] = __builtin_amdgcn_mfma_f32_16x16x16bf16_1k(ass4((dt & 1) ? u32x2{vmv[dt >> 1].z, vmv[dt >> 1].w} : u32x2{vmv[dt >> 1].x, vmv[dt >> 1].y}), ass4(pm[b]), o[dt][b], 0, 0, 0);
    inv[0] = __builtin_amdgcn_rcpf(sm[0][0]); inv[1] = __builtin_amdgcn_rcpf(sm[1][0]);
    const char* od = uni(O + ((size_t)s * 64 + 2 * h) * 4096);
#pragma unroll
    for (int j = 0; j < 4; ++j) {
#pragma unroll
      for (int b = 0; b < 2; ++b) {
        const f32x4 r0 = o[2 * j][b] * inv[b], r1 = o[2 * j + 1][b] * inv[b];
        st16s(od + (j >> 1) * 4096 + b * 2048, oa ^ ((j & 1) * 64u), u32x4{pack2(r0[0], r0[1]), pack2(r0[2], r0[3]), pack2(r1[0], r1[1]), pack2(r1[2], r1[3])});
      }
    }
    if (s >= SB && lane == 0) st4_sc1(cnt + (PSA + (hbase >> 1)) * 32 + 16 + wave, bell(s, rt_now() + DSA));
    WTM(slot, s, 3);
    WAIT_V(0);
    if (lane == 0) st4_sc1(cnt + (PSA + (hbase >> 1)) * 32 + wave, (unsigned)it);
    WTM(slot, s, 2);
  }
}
#undef LD8
#undef TIE8

constexpr int Q_MEM = 0;
constexpr int Q_K   = 16384;
constexpr int Q_V   = 24576;
constexpr int Q_QK  = 32768;
constexpr int Q_TM  = 36864;
constexpr int Q_CB  = 40960;
constexpr int Q_RING = 32768;
constexpr int V_P1  = 0;
constexpr int V_P2  = 16384;
constexpr int NCHAIN = 96;
#define VHN 2
#define ROTH 0
#define NQQ 6
#define NQK 4
#define E_AG 9
#define RDYI -2
#define ROTW 2
#define DQU 6
#define DQM 18
#define DQD 1
#define ARSEL (xg >> 1)
#define ORSEL (xg >> 1)
#define D_Q 105
#define D_O 4
#define E_B 6
#define E_O 1900
#define KDEL 5
#define VPOS 0

typedef __bf16 bf16x16 __attribute__((ext_vector_type(16)));
typedef unsigned u32x8 __attribute__((ext_vector_type(8)));
__device__ __forceinline__ f32x4 SMF(u32x4 a, u32x8 b, f32x4 c, int idx) {
  return __builtin_amdgcn_smfmac_f32_16x16x64_bf16(__builtin_bit_cast(bf16x8, a), __builtin_bit_cast(bf16x16, b), c, idx, 0, 0);
}
__device__ __forceinline__ float fold32(float a, float b) {
  const auto p = __builtin_amdgcn_permlane32_swap(__float_as_uint(a), __float_as_uint(b), false, false);
  return __uint_as_float(p[0]) + __uint_as_float(p[1]);
}
__device__ __forceinline__ f32x2 rowsum(f32x4 x) { return f32x2{fold32(x[0], x[2]), fold32(x[1], x[3])}; }
__device__ __forceinline__ unsigned nosent1(unsigned v) { return v < 0xFFFFFFFEu ? v : 0xFFFFFFFEu; }
__device__ __forceinline__ void st4s(const char* sb, unsigned vo, unsigned v) { asm volatile("global_store_dword %0, %1, %2 sc1\n\ts_nop 0" :: "v"(vo), "v"(v), "s"(sb) : "memory"); }
__device__ __forceinline__ void st8s(const char* sb, unsigned vo, u32x2 v) { asm volatile("global_store_dwordx2 %0, %1, %2 sc1\n\ts_nop 1" :: "v"(vo), "v"(v), "s"(sb) : "memory"); }
__device__ __forceinline__ void gldsw(const char* sbase, unsigned voff, unsigned lds) {
  unsigned keep;
  asm volatile("s_mov_b32 %0, m0\n\ts_mov_b32 m0, %3\n\ts_nop 2\n\tglobal_load_lds_dwordx4 %1, %2" CNT_ "\n\ts_mov_b32 m0, %0"
               : "=&s"(keep) : "v"(voff), "s"(sbase), "s"(lds) : "memory");
}
__device__ __forceinline__ void wait_clk(unsigned t0, unsigned d) {
  while ((unsigned)__builtin_amdgcn_s_memtime() - t0 < d) __builtin_amdgcn_s_sleep(1);
}
__device__ __forceinline__ unsigned bell_time(const char* a, unsigned vo, unsigned tag, unsigned cap) {
  unsigned v;
  for (unsigned spins = 0;; ++spins) {
    asm volatile("global_load_dword %0, %1, %2 sc1\n\ts_waitcnt vmcnt(0)" : "=&v"(v) : "v"(vo), "s"(a) : "memory");
    if (__all((v >> 24) >= tag) || spins >= cap) break;
  }
  const unsigned now = (unsigned)__builtin_amdgcn_s_memrealtime() + 250u;
  const unsigned age = rowmin16((now - v) & 0xFFFFFFu);
  return now - min(min(rdl(age, 0), rdl(age, 16)), 500u);
}
#define WBAR() do { __builtin_amdgcn_s_barrier(); asm volatile("" ::: "memory"); } while (0)
#define TN(k, v) do { } while (0)
__device__ __forceinline__ void stage_proj(const char* src, unsigned lds, int tid, int wave) {
  const int row = tid >> 4, f = 2 * ((row & 1) | (((row >> 2) & 1) << 1));
#pragma unroll
  for (int j = 0; j < 4; ++j)
    gldsw(src + (size_t)j * 32 * HD * 2, (unsigned)(row * HD * 2 + (((tid & 15) ^ f ^ ((j & 1) << 3)) << 4)), lds + (wave * 64 + 512 * j) * 16);
}
__device__ __forceinline__ unsigned proj_base(int T, int lane) {
  const int g = lane >> 4, q4 = (lane & 15) >> 2, p4 = lane & 3, fs = 2 * ((q4 & 1) | ((g & 1) << 1) | ((q4 >> 1) << 2));
  return (unsigned)((4 * g + (q4 & 1) + 32 * (q4 >> 1)) * 256 + (((2 * T) ^ fs) << 4) + p4 * 8);
}
__device__ __forceinline__ u32x8 proj_frag(const char* smem, unsigned a) {
  const u32x2 t0 = trread(smem, a), t1 = trread(smem, a + 512), t2 = trread(smem, a + 4096), t3 = trread(smem, a + 4608);
  return u32x8{t0.x, t0.y, t1.x, t1.y, t2.x, t2.y, t3.x, t3.y};
}
__device__ __forceinline__ void flag_frags(unsigned fa, unsigned a, unsigned want, bf16x8 (&f)[4]) {
  u32x2 t[8];
  for (;;) {
    u32x4 fl;
    asm volatile("ds_read_b128 %0, %9\n\t"
                 "ds_read_b64_tr_b16 %1, %10\n\tds_read_b64_tr_b16 %2, %10 offset:64\n\t"
                 "ds_read_b64_tr_b16 %3, %10 offset:512\n\tds_read_b64_tr_b16 %4, %10 offset:576\n\t"
                 "ds_read_b64_tr_b16 %5, %10 offset:1024\n\tds_read_b64_tr_b16 %6, %10 offset:1088\n\t"
                 "ds_read_b64_tr_b16 %7, %10 offset:1536\n\tds_read_b64_tr_b16 %8, %10 offset:1600\n\ts_waitcnt lgkmcnt(0)"
                 : "=&v"(fl), "=&v"(t[0]), "=&v"(t[1]), "=&v"(t[2]), "=&v"(t[3]), "=&v"(t[4]), "=&v"(t[5]), "=&v"(t[6]), "=&v"(t[7])
                 : "v"(fa), "v"(a) : "memory");
    if (__builtin_amdgcn_readfirstlane(min(min(fl.x, fl.y), min(fl.z, fl.w))) >= want) break;
  }
#pragma unroll
  for (int ks = 0; ks < 4; ++ks) f[ks] = asbf8(u32x4{t[2 * ks].x, t[2 * ks].y, t[2 * ks + 1].x, t[2 * ks + 1].y});
}
__device__ __forceinline__ void wait_flags(const char* p, unsigned want) {
  for (;;) {
    const u32x4 fl = *(volatile LAS u32x4*)p;
    if (__builtin_amdgcn_readfirstlane(min(min(fl.x, fl.y), min(fl.z, fl.w))) >= want) break;
  }
}
__device__ __forceinline__ f32x4 zero4() { float z = 0.0f; asm volatile("" : "+v"(z)); return f32x4{z, z, z, z}; }
__device__ __forceinline__ unsigned lds_word(const char* p) { return __builtin_amdgcn_readfirstlane(*(volatile LAS unsigned*)p); }

#define MFMA16(a, b, c) __builtin_amdgcn_mfma_f32_16x16x16bf16_1k((a), (b), (c), 0, 0, 0)
#define UF(x) __uint_as_float(x)
__device__ __forceinline__ unsigned umax3(unsigned a, unsigned b, unsigned c) { return max(max(a, b), c); }
__device__ __forceinline__ f32x2 logit2(float a, float b) {
  return f32x2{a, b};
}
constexpr float LSC = LOG2E * SCALE;

struct QKW { u32x32 w0, w1, w2, w3, w4, w5; u32x8 x; };
template <int W_> __device__ __forceinline__ void qk_step(const QKW& W, const char* ag, const char* vs, const char* od, const char* kv, unsigned& cap, unsigned& sp,
                                                          unsigned long long& tc, unsigned long long* ts, unsigned& d0, unsigned& d1);
template <> __device__ __forceinline__ void qk_step<0>(const QKW& W, const char* ag, const char* vs, const char* od, const char* kv, unsigned& cap, unsigned& sp,
                                                        unsigned long long& tc, unsigned long long* ts, unsigned& d0, unsigned& d1) {
  unsigned st, bg;
  asm volatile("v_mbcnt_lo_u32_b32 v60, -1, 0\n\t"
    "v_mbcnt_hi_u32_b32 v60, -1, v60\n\t"
    "v_lshlrev_b32 v6, 4, v60\n\t"
    "s_mov_b32 %[sp], 0\n\t"
    "s_setprio 3\n\t"
    "global_load_dwordx4 v[40:43], v6, %[ag] sc1\n\t"
    "global_load_dwordx4 v[44:47], v6, %[ag] offset:1024 sc1\n\t"
    "s_memtime %[tc]\n\t"
    "v_add_u32 v7, 40960, v6\n\t"
    "v_add_u32 v8, 98304, v6\n\t"
    "ds_read_b128 v[24:27], v7 offset:0\n\t"
    "ds_read_b128 v[28:31], v7 offset:1024\n\t"
    "ds_read_b128 v[32:35], v7 offset:8192\n\t"
    "ds_read_b128 v[36:39], v7 offset:9216\n\t"
    "v_lshrrev_b32 v48, 4, v60\n\t"
    "v_and_b32 v49, 15, v60\n\t"
    "v_and_b32 v61, 4, v60\n\t"
    "v_mul_u32_u24 v61, 0x2aaa, v61\n\t"
    "v_lshrrev_b32 v9, 1, v60\n\t"
    "v_and_b32 v9, 2, v9\n\t"
    "v_or3_b32 v9, v9, v61, %[c44]\n\t"
    "v_add_u32 v14, 0, v6\n\t"
    "v_lshlrev_b32 v61, 2, v48\n\t"
    "v_lshl_or_b32 v10, v49, 4, v61\n\t"
    "v_lshrrev_b32 v50, 2, v49\n\t"
    "v_and_b32 v51, 3, v60\n\t"
    "v_and_b32 v61, 1, v60\n\t"
    "v_lshlrev_b32 v61, 3, v61\n\t"
    "v_lshl_or_b32 v11, v50, 4, v61\n\t"
    "v_lshl_or_b32 v11, v48, 7, v11\n\t"
    "v_xor_b32 v12, v48, v49\n\t"
    "v_lshlrev_b32 v12, 4, v12\n\t"
    "v_lshl_or_b32 v12, v49, 8, v12\n\t"
    "v_lshl_or_b32 v50, v48, 2, v50\n\t"
    "v_or_b32 v51, 0, v51\n\t"
    "v_xor_b32 v51, v51, v50\n\t"
    "v_lshlrev_b32 v13, 4, v51\n\t"
    "v_lshl_or_b32 v13, v50, 8, v13\n\t"
    "v_and_b32 v61, 3, v60\n\t"
    "v_lshlrev_b32 v61, 4, v61\n\t"
    "v_lshl_or_b32 v15, v48, 6, v61\n\t"
    "v_and_b32 v61, 8, v60\n\t"
    "v_lshl_or_b32 v15, v61, 14, v15\n\t"
    "v_and_b32 v61, 4, v60\n\t"
    "v_lshl_or_b32 v15, v61, 8, v15\n\t"
    "v_add_u32 v15, 0, v15\n\t"
    "3:\n\t"
    "v_mov_b32 v16, 0\n\t"
    "v_mov_b32 v17, 0\n\t"
    "v_mov_b32 v18, 0\n\t"
    "v_mov_b32 v19, 0\n\t"
    "v_mov_b32 v20, 0\n\t"
    "v_mov_b32 v21, 0\n\t"
    "v_mov_b32 v22, 0\n\t"
    "v_mov_b32 v23, 0\n\t"
    "s_waitcnt vmcnt(0) lgkmcnt(0)\n\t"
    "14:\n\t"
    "ds_write_b128 v6, v[40:43] offset:0\n\t"
    "ds_write_b128 v6, v[44:47] offset:1024\n\t"
    "v_smfmac_f32_16x16x64_bf16 v[16:19], v[40:43], v[64:71], v9\n\t"
    "v_max3_u32 v60, v40, v41, v42\n\t"
    "v_max3_u32 v61, v43, v44, v45\n\t"
    "v_smfmac_f32_16x16x64_bf16 v[20:23], v[40:43], v[72:79], v9\n\t"
    "v_max3_u32 v60, v60, v46, v47\n\t"
    "v_max_u32 v60, v60, v61\n\t"
    "v_cmp_eq_u32 vcc, -1, v60\n\t"
    "s_cbranch_vccnz 1f\n\t"
    "2:\n\t"
    "s_waitcnt lgkmcnt(0)\n\t"
    "s_barrier\n\t"
    "ds_read_b128 v[40:43], v6 offset:12288\n\t"
    "ds_read_b128 v[48:51], v6 offset:2048\n\t"
    "v_smfmac_f32_16x16x64_bf16 v[16:19], v[44:47], v[80:87], v9\n\t"
    "ds_read_b128 v[52:55], v6 offset:3072\n\t"
    "ds_read_b128 v[56:59], v6 offset:4096\n\t"
    "v_smfmac_f32_16x16x64_bf16 v[20:23], v[44:47], v[88:95], v9\n\t"
    "ds_read_b128 v[60:63], v6 offset:13312\n\t"
    "ds_read_b128 v[44:47], v6 offset:5120\n\t"
    "s_waitcnt lgkmcnt(5)\n\t"
    "v_smfmac_f32_16x16x64_bf16 v[16:19], v[40:43], v[24:31], v9\n\t"
    "ds_read_b128 v[24:27], v7 offset:16384\n\t"
    "ds_read_b128 v[28:31], v7 offset:17408\n\t"
    "v_smfmac_f32_16x16x64_bf16 v[20:23], v[40:43], v[32:39], v9\n\t"
    "ds_read_b128 v[32:35], v7 offset:24576\n\t"
    "ds_read_b128 v[36:39], v7 offset:25600\n\t"
    "ds_read_b128 v[40:43], v6 offset:6144\n\t"
    "s_waitcnt lgkmcnt(9)\n\t"
    "v_smfmac_f32_16x16x64_bf16 v[16:19], v[48:51], v[96:103], v9\n\t"
    "v_smfmac_f32_16x16x64_bf16 v[20:23], v[48:51], v[104:111], v9\n\t"
    "ds_read_b128 v[48:51], v6 offset:7168\n\t"
    "s_waitcnt lgkmcnt(9)\n\t"
    "v_smfmac_f32_16x16x64_bf16 v[16:19], v[52:55], v[112:119], v9\n\t"
    "v_smfmac_f32_16x16x64_bf16 v[20:23], v[52:55], v[120:127], v9\n\t"
    "ds_read_b128 v[52:55], v6 offset:14336\n\t"
    "s_waitcnt lgkmcnt(9)\n\t"
    "v_smfmac_f32_16x16x64_bf16 v[16:19], v[56:59], v[128:135], v9\n\t"
    "v_smfmac_f32_16x16x64_bf16 v[20:23], v[56:59], v[136:143], v9\n\t"
    "ds_read_b128 v[56:59], v6 offset:8192\n\t"
    "s_waitcnt lgkmcnt(6)\n\t"
    "v_smfmac_f32_16x16x64_bf16 v[16:19], v[60:63], v[24:31], v9\n\t"
    "ds_read_b128 v[24:27], v8 offset:0\n\t"
    "ds_read_b128 v[28:31], v8 offset:1024\n\t"
    "s_waitcnt lgkmcnt(6)\n\t"
    "v_smfmac_f32_16x16x64_bf16 v[20:23], v[60:63], v[32:39], v9\n\t"
    "ds_read_b128 v[32:35], v8 offset:8192\n\t"
    "ds_read_b128 v[36:39], v8 offset:9216\n\t"
    "ds_read_b128 v[60:63], v6 offset:9216\n\t"
    "v_smfmac_f32_16x16x64_bf16 v[16:19], v[44:47], v[144:151], v9\n\t"
    "v_smfmac_f32_16x16x64_bf16 v[20:23], v[44:47], v[152:159], v9\n\t"
    "ds_read_b128 v[44:47], v6 offset:10240\n\t"
    "s_waitcnt lgkmcnt(9)\n\t"
    "v_smfmac_f32_16x16x64_bf16 v[16:19], v[40:43], v[160:167], v9\n\t"
    "v_smfmac_f32_16x16x64_bf16 v[20:23], v[40:43], v[168:175], v9\n\t"
    "ds_read_b128 v[40:43], v6 offset:15360\n\t"
    "s_waitcnt lgkmcnt(9)\n\t"
    "v_smfmac_f32_16x16x64_bf16 v[16:19], v[48:51], v[176:183], v9\n\t"
    "v_smfmac_f32_16x16x64_bf16 v[20:23], v[48:51], v[184:191], v9\n\t"
    "ds_read_b128 v[48:51], v6 offset:11264\n\t"
    "s_waitcnt lgkmcnt(6)\n\t"
    "v_smfmac_f32_16x16x64_bf16 v[16:19], v[52:55], v[24:31], v9\n\t"
    "ds_read_b128 v[24:27], v8 offset:16384\n\t"
    "ds_read_b128 v[28:31], v8 offset:17408\n\t"
    "s_waitcnt lgkmcnt(6)\n\t"
    "v_smfmac_f32_16x16x64_bf16 v[20:23], v[52:55], v[32:39], v9\n\t"
    "ds_read_b128 v[32:35], v8 offset:24576\n\t"
    "ds_read_b128 v[36:39], v8 offset:25600\n\t"
    "v_smfmac_f32_16x16x64_bf16 v[16:19], v[56:59], v[192:199], v9\n\t"
    "v_smfmac_f32_16x16x64_bf16 v[20:23], v[56:59], v[200:207], v9\n\t"
    "s_waitcnt lgkmcnt(7)\n\t"
    "v_smfmac_f32_16x16x64_bf16 v[16:19], v[60:63], v[208:215], v9\n\t"
    "v_smfmac_f32_16x16x64_bf16 v[20:23], v[60:63], v[216:223], v9\n\t"
    "s_waitcnt lgkmcnt(6)\n\t"
    "v_smfmac_f32_16x16x64_bf16 v[16:19], v[44:47], v[224:231], v9\n\t"
    "v_smfmac_f32_16x16x64_bf16 v[20:23], v[44:47], v[232:239], v9\n\t"
    "s_waitcnt lgkmcnt(2)\n\t"
    "v_smfmac_f32_16x16x64_bf16 v[16:19], v[40:43], v[24:31], v9\n\t"
    "s_waitcnt lgkmcnt(0)\n\t"
    "v_smfmac_f32_16x16x64_bf16 v[20:23], v[40:43], v[32:39], v9\n\t"
    "v_smfmac_f32_16x16x64_bf16 v[16:19], v[48:51], v[240:247], v9\n\t"
    "v_smfmac_f32_16x16x64_bf16 v[20:23], v[48:51], v[248:255], v9\n\t"
    "v_xor_b32 v7, 64, v12\n\t"
    "v_xor_b32 v8, 0x80, v12\n\t"
    "v_xor_b32 v6, 0xc0, v12\n\t"
    "ds_read_b128 v[24:27], v12 offset:16384\n\t"
    "ds_read_b128 v[28:31], v12 offset:20480\n\t"
    "ds_read_b128 v[32:35], v7 offset:16384\n\t"
    "ds_read_b128 v[36:39], v7 offset:20480\n\t"
    "v_permlane16_swap_b32 v16, v18\n\t"
    "v_permlane16_swap_b32 v17, v19\n\t"
    "v_permlane16_swap_b32 v20, v22\n\t"
    "v_permlane16_swap_b32 v21, v23\n\t"
    "v_add_f32 v16, v16, v18\n\t"
    "v_add_f32 v17, v17, v19\n\t"
    "v_add_f32 v20, v20, v22\n\t"
    "v_mul_f32 v16, %[lsc], v16\n\t"
    "v_mul_f32 v17, %[lsc], v17\n\t"
    "v_cvt_pk_bf16_f32 v16, v16, v17\n\t"
    "v_add_f32 v21, v21, v23\n\t"
    "ds_write_b32 v10, v16 offset:32768\n\t"
    "v_mul_f32 v20, %[lsc], v20\n\t"
    "v_mul_f32 v21, %[lsc], v21\n\t"
    "v_cvt_pk_bf16_f32 v20, v20, v21\n\t"
    "ds_write_b32 v10, v20 offset:33024\n\t"
    "ds_read_b128 v[40:43], v8 offset:16384\n\t"
    "ds_read_b128 v[44:47], v8 offset:20480\n\t"
    "ds_read_b128 v[48:51], v6 offset:16384\n\t"
    "ds_read_b128 v[52:55], v6 offset:20480\n\t"
    "s_waitcnt lgkmcnt(4)\n\t"
    "s_barrier\n\t"
    "ds_read_b64_tr_b16 v[16:17], v11 offset:32768\n\t"
    "ds_read_b64_tr_b16 v[18:19], v11 offset:32832\n\t"
    "ds_read_b64_tr_b16 v[20:21], v11 offset:33280\n\t"
    "ds_read_b64_tr_b16 v[22:23], v11 offset:33344\n\t"
    "ds_read_b64_tr_b16 v[56:57], v11 offset:33792\n\t"
    "ds_read_b64_tr_b16 v[58:59], v11 offset:33856\n\t"
    "ds_read_b64_tr_b16 v[60:61], v11 offset:34304\n\t"
    "ds_read_b64_tr_b16 v[62:63], v11 offset:34368\n\t"
    "v_mov_b32 v6, 0\n\t"
    "v_mov_b32 v7, 0\n\t"
    "v_mov_b32 v8, 0\n\t"
    "v_mov_b32 v9, 0\n\t"
    "s_mov_b32 exec_hi, 0\n\t"
    "global_load_dwordx4 v[6:9], v14, %[vs] sc1\n\t"
    "s_mov_b32 exec_hi, -1\n\t"
    "s_waitcnt lgkmcnt(6)\n\t"
    "v_mfma_f32_16x16x32_bf16 v[24:27], v[24:27], v[16:19], 0\n\t"
    "v_mfma_f32_16x16x32_bf16 v[28:31], v[28:31], v[16:19], 0\n\t"
    "s_waitcnt lgkmcnt(4)\n\t"
    "v_mfma_f32_16x16x32_bf16 v[24:27], v[32:35], v[20:23], v[24:27]\n\t"
    "v_mfma_f32_16x16x32_bf16 v[28:31], v[36:39], v[20:23], v[28:31]\n\t"
    "ds_read_b64_tr_b16 v[32:33], v13 offset:24576\n\t"
    "ds_read_b64_tr_b16 v[34:35], v13 offset:28672\n\t"
    "ds_read_b64_tr_b16 v[36:37], v13 offset:24584\n\t"
    "ds_read_b64_tr_b16 v[38:39], v13 offset:28680\n\t"
    "s_waitcnt lgkmcnt(6)\n\t"
    "v_mfma_f32_16x16x32_bf16 v[24:27], v[40:43], v[56:59], v[24:27]\n\t"
    "v_mfma_f32_16x16x32_bf16 v[28:31], v[44:47], v[56:59], v[28:31]\n\t"
    "s_waitcnt lgkmcnt(4)\n\t"
    "v_mfma_f32_16x16x32_bf16 v[24:27], v[48:51], v[60:63], v[24:27]\n\t"
    "v_mfma_f32_16x16x32_bf16 v[28:31], v[52:55], v[60:63], v[28:31]\n\t"
    "s_setprio 1\n\t"
    "v_mov_b32 v52, %[one]\n\t"
    "v_mov_b32 v53, %[one]\n\t"
    "v_mov_b32 v54, %[one]\n\t"
    "v_mov_b32 v55, %[one]\n\t"
    "s_nop 2\n\t"
    "v_max3_f32 v10, |v24|, |v25|, |v26|\n\t"
    "v_max3_f32 v40, |v27|, |v28|, |v29|\n\t"
    "v_max3_f32 v10, v10, |v30|, |v31|\n\t"
    "v_max_f32 v10, v10, v40\n\t"
    "v_cmp_nge_f32 vcc, %[big], v10\n\t"
    "s_mov_b32 %[bg], %[big]\n\t"
    "v_mov_b32 v10, 0\n\t"
    "s_cbranch_vccnz 12f\n\t"
    "13:\n\t"
    "v_exp_f32 v24, v24\n\t"
    "v_exp_f32 v25, v25\n\t"
    "v_exp_f32 v26, v26\n\t"
    "v_exp_f32 v27, v27\n\t"
    "v_exp_f32 v28, v28\n\t"
    "v_exp_f32 v29, v29\n\t"
    "v_exp_f32 v30, v30\n\t"
    "v_exp_f32 v31, v31\n\t"
    "v_cvt_pk_bf16_f32 v40, v24, v25\n\t"
    "v_cvt_pk_bf16_f32 v41, v26, v27\n\t"
    "v_cvt_pk_bf16_f32 v42, v28, v29\n\t"
    "v_cvt_pk_bf16_f32 v43, v30, v31\n\t"
    "s_setprio 3\n\t"
    "s_waitcnt lgkmcnt(0)\n\t"
    "s_nop 0\n\t"
    "v_mfma_f32_16x16x32_bf16 v[44:47], v[32:35], v[40:43], 0\n\t"
    "v_mfma_f32_16x16x32_bf16 v[48:51], v[36:39], v[40:43], 0\n\t"
    "v_mfma_f32_16x16x32_bf16 v[52:55], v[52:55], v[40:43], 0\n\t"
    "v_mov_b32 v12, 0\n\t"
    "v_mov_b32 v13, 0\n\t"
    "s_mov_b32 exec_hi, 0\n\t"
    "v_mov_b32 v12, %[one]\n\t"
    "v_mov_b32 v13, %[one]\n\t"
    "s_mov_b32 exec_hi, -1\n\t"
    "s_barrier\n\t"
    "ds_read_b64_tr_b16 v[24:25], v11 offset:34816\n\t"
    "ds_read_b64_tr_b16 v[26:27], v11 offset:34880\n\t"
    "ds_read_b64_tr_b16 v[28:29], v11 offset:35328\n\t"
    "ds_read_b64_tr_b16 v[30:31], v11 offset:35392\n\t"
    "ds_read_b64_tr_b16 v[32:33], v11 offset:35840\n\t"
    "ds_read_b64_tr_b16 v[34:35], v11 offset:35904\n\t"
    "ds_read_b64_tr_b16 v[36:37], v11 offset:36352\n\t"
    "ds_read_b64_tr_b16 v[38:39], v11 offset:36416\n\t"
    "s_waitcnt lgkmcnt(6)\n\t"
    "v_mfma_f32_16x16x32_bf16 v[40:43], v[24:27], v[16:19], 0\n\t"
    "s_waitcnt lgkmcnt(4)\n\t"
    "v_mfma_f32_16x16x32_bf16 v[40:43], v[28:31], v[20:23], v[40:43]\n\t"
    "s_waitcnt lgkmcnt(2)\n\t"
    "v_mfma_f32_16x16x32_bf16 v[40:43], v[32:35], v[56:59], v[40:43]\n\t"
    "s_waitcnt lgkmcnt(0)\n\t"
    "v_mfma_f32_16x16x32_bf16 v[40:43], v[36:39], v[60:63], v[40:43]\n\t"
    "5:\n\t"
    "s_waitcnt vmcnt(0)\n\t"
    "v_max3_u32 v11, v6, v7, v8\n\t"
    "v_max_u32 v11, v11, v9\n\t"
    "v_cmp_eq_u32 vcc, -1, v11\n\t"
    "s_cbranch_vccnz 4f\n\t"
    "6:\n\t"
    "s_nop 2\n\t"
    "v_exp_f32 v28, v40\n\t"
    "v_max3_f32 v11, v40, v41, v42\n\t"
    "v_exp_f32 v29, v41\n\t"
    "v_max_f32 v11, v11, v43\n\t"
    "v_exp_f32 v30, v42\n\t"
    "v_cmp_nge_f32 vcc, %[bg], v11\n\t"
    "v_exp_f32 v31, v43\n\t"
    "s_cbranch_vccnz 10f\n\t"
    "11:\n\t"
    "v_cvt_pk_bf16_f32 v24, v28, v29\n\t"
    "v_cvt_pk_bf16_f32 v25, v30, v31\n\t"
    "s_nop 1\n\t"
    "v_mfma_f32_16x16x16_bf16 v[52:55], v[12:13], v[24:25], v[52:55]\n\t"
    "v_mfma_f32_16x16x16_bf16 v[44:47], v[6:7], v[24:25], v[44:47]\n\t"
    "v_mfma_f32_16x16x16_bf16 v[48:51], v[8:9], v[24:25], v[48:51]\n\t"
    "s_nop 5\n\t"
    "v_rcp_f32 v26, v52\n\t"
    "s_nop 0\n\t"
    "v_pk_mul_f32 v[44:45], v[44:45], v[26:27] op_sel_hi:[1,0]\n\t"
    "v_pk_mul_f32 v[46:47], v[46:47], v[26:27] op_sel_hi:[1,0]\n\t"
    "v_pk_mul_f32 v[48:49], v[48:49], v[26:27] op_sel_hi:[1,0]\n\t"
    "v_pk_mul_f32 v[50:51], v[50:51], v[26:27] op_sel_hi:[1,0]\n\t"
    "v_cvt_pk_bf16_f32 v36, v44, v45\n\t"
    "v_cvt_pk_bf16_f32 v37, v46, v47\n\t"
    "v_cvt_pk_bf16_f32 v38, v48, v49\n\t"
    "v_cvt_pk_bf16_f32 v39, v50, v51\n\t"
    "v_min_u32 v36, -2, v36\n\t"
    "v_min_u32 v37, -2, v37\n\t"
    "v_min_u32 v38, -2, v38\n\t"
    "v_min_u32 v39, -2, v39\n\t"
    "global_store_dwordx4 v15, v[36:39], %[od] sc1\n\t"
    "s_barrier\n\t"
    "s_mov_b32 exec_hi, 0\n\t"
    "global_store_dwordx4 v14, v[6:9], %[kv] sc1\n\t"
    "s_mov_b32 exec_hi, -1\n\t"
    "s_nop 0\n\t"
    "s_waitcnt lgkmcnt(0)\n\t"
    "s_branch 9f\n\t"
    "1:\n\t"
    "s_add_u32 %[sp], %[sp], 1\n\t"
    "s_cmp_ge_u32 %[sp], %[cap]\n\t"
    "s_cbranch_scc1 7f\n\t"
    "global_load_dwordx4 v[40:43], v6, %[ag] sc1\n\t"
    "global_load_dwordx4 v[44:47], v6, %[ag] offset:1024 sc1\n\t"
    "s_waitcnt lgkmcnt(0)\n\t"
    "s_memtime %[tc]\n\t"
    "s_nop 7\n\t"
    "s_branch 3b\n\t"
    "7:\n\t"
    "s_mov_b32 %[cap], 0\n\t"
    "s_branch 2b\n\t"
    "4:\n\t"
    "s_add_u32 %[sp], %[sp], 0x10000\n\t"
    "s_lshr_b32 %[st], %[sp], 16\n\t"
    "s_cmp_ge_u32 %[st], %[cap]\n\t"
    "s_cbranch_scc1 8f\n\t"
    "s_mov_b32 exec_hi, 0\n\t"
    "global_load_dwordx4 v[6:9], v14, %[vs] sc1\n\t"
    "s_mov_b32 exec_hi, -1\n\t"
    "s_branch 5b\n\t"
    "8:\n\t"
    "s_mov_b32 %[cap], 0\n\t"
    "s_branch 6b\n\t"
    "10:\n\t"
    "v_max_f32 v11, v11, v10\n\t"
    "v_mov_b32 v26, v11\n\t"
    "s_nop 1\n\t"
    "v_permlane16_swap_b32 v11, v26\n\t"
    "v_max_f32 v11, v11, v26\n\t"
    "v_sub_f32 v28, v40, v11\n\t"
    "v_sub_f32 v29, v41, v11\n\t"
    "v_sub_f32 v30, v42, v11\n\t"
    "v_sub_f32 v31, v43, v11\n\t"
    "v_sub_f32 v11, v10, v11\n\t"
    "v_exp_f32 v11, v11\n\t"
    "v_exp_f32 v28, v28\n\t"
    "v_exp_f32 v29, v29\n\t"
    "v_exp_f32 v30, v30\n\t"
    "v_exp_f32 v31, v31\n\t"
    "v_mul_f32 v44, v44, v11\n\t"
    "v_mul_f32 v45, v45, v11\n\t"
    "v_mul_f32 v46, v46, v11\n\t"
    "v_mul_f32 v47, v47, v11\n\t"
    "v_mul_f32 v48, v48, v11\n\t"
    "v_mul_f32 v49, v49, v11\n\t"
    "v_mul_f32 v50, v50, v11\n\t"
    "v_mul_f32 v51, v51, v11\n\t"
    "v_mul_f32 v52, v52, v11\n\t"
    "v_mul_f32 v53, v53, v11\n\t"
    "v_mul_f32 v54, v54, v11\n\t"
    "v_mul_f32 v55, v55, v11\n\t"
    "s_branch 11b\n\t"
    "12:\n\t"
    "v_max3_f32 v10, v24, v25, v26\n\t"
    "v_max3_f32 v40, v27, v28, v29\n\t"
    "v_max3_f32 v10, v10, v30, v31\n\t"
    "v_max3_f32 v10, v10, v40, %[neg]\n\t"
    "v_mov_b32 v40, v10\n\t"
    "s_nop 1\n\t"
    "v_permlane16_swap_b32 v10, v40\n\t"
    "v_max_f32 v10, v10, v40\n\t"
    "v_mov_b32 v40, v10\n\t"
    "s_nop 1\n\t"
    "v_permlane32_swap_b32 v10, v40\n\t"
    "v_max_f32 v10, v10, v40\n\t"
    "v_sub_f32 v24, v24, v10\n\t"
    "v_sub_f32 v25, v25, v10\n\t"
    "v_sub_f32 v26, v26, v10\n\t"
    "v_sub_f32 v27, v27, v10\n\t"
    "v_sub_f32 v28, v28, v10\n\t"
    "v_sub_f32 v29, v29, v10\n\t"
    "v_sub_f32 v30, v30, v10\n\t"
    "v_sub_f32 v31, v31, v10\n\t"
    "s_mov_b32 %[bg], 0x7fc00000\n\t"
    "s_branch 13b\n\t"
    "9:"
    : [tc] "=&s"(tc), [sp] "=&s"(sp), [cap] "+s"(cap), [st] "=&s"(st), [bg] "=&s"(bg)
    : [ag] "s"(ag), [c44] "s"(0x4444u), [kv] "s"(kv), [vs] "s"(vs), [od] "s"(od), [od2] "s"(od + 262144), [neg] "s"(-1.0e38f), [lsc] "s"(LSC), [one] "s"(0x3F803F80u), [big] "s"(24.0f), "{v[64:95]}"(W.w0), "{v[96:127]}"(W.w1), "{v[128:159]}"(W.w2), "{v[160:191]}"(W.w3), "{v[192:223]}"(W.w4), "{v[224:255]}"(W.w5)
    : "v6", "v7", "v8", "v9", "v10", "v11", "v12", "v13", "v14", "v15", "v16", "v17", "v18", "v19", "v20", "v21", "v22", "v23", "v24", "v25", "v26", "v27", "v28", "v29", "v30", "v31", "v32", "v33", "v34", "v35", "v36", "v37", "v38", "v39", "v40", "v41", "v42", "v43", "v44", "v45", "v46", "v47", "v48", "v49", "v50", "v51", "v52", "v53", "v54", "v55", "v56", "v57", "v58", "v59", "v60", "v61", "v62", "v63", "vcc", "scc", "memory");
}
template <> __device__ __forceinline__ void qk_step<1>(const QKW& W, const char* ag, const char* vs, const char* od, const char* kv, unsigned& cap, unsigned& sp,
                                                        unsigned long long& tc, unsigned long long* ts, unsigned& d0, unsigned& d1) {
  unsigned st, bg;
  asm volatile("v_mbcnt_lo_u32_b32 v60, -1, 0\n\t"
    "v_mbcnt_hi_u32_b32 v60, -1, v60\n\t"
    "v_lshlrev_b32 v6, 4, v60\n\t"
    "s_mov_b32 %[sp], 0\n\t"
    "s_setprio 3\n\t"
    "global_load_dwordx4 v[40:43], v6, %[ag] sc1\n\t"
    "global_load_dwordx4 v[44:47], v6, %[ag] offset:1024 sc1\n\t"
    "s_memtime %[tc]\n\t"
    "v_add_u32 v7, 43008, v6\n\t"
    "v_add_u32 v8, 100352, v6\n\t"
    "ds_read_b128 v[24:27], v7 offset:0\n\t"
    "ds_read_b128 v[28:31], v7 offset:1024\n\t"
    "ds_read_b128 v[32:35], v7 offset:8192\n\t"
    "ds_read_b128 v[36:39], v7 offset:9216\n\t"
    "v_lshrrev_b32 v48, 4, v60\n\t"
    "v_and_b32 v49, 15, v60\n\t"
    "v_and_b32 v61, 4, v60\n\t"
    "v_mul_u32_u24 v61, 0x2aaa, v61\n\t"
    "v_lshrrev_b32 v9, 1, v60\n\t"
    "v_and_b32 v9, 2, v9\n\t"
    "v_or3_b32 v9, v9, v61, %[c44]\n\t"
    "v_add_u32 v14, 512, v6\n\t"
    "v_lshlrev_b32 v61, 2, v48\n\t"
    "v_lshl_or_b32 v10, v49, 4, v61\n\t"
    "v_lshrrev_b32 v50, 2, v49\n\t"
    "v_and_b32 v51, 3, v60\n\t"
    "v_and_b32 v61, 1, v60\n\t"
    "v_lshlrev_b32 v61, 3, v61\n\t"
    "v_lshl_or_b32 v11, v50, 4, v61\n\t"
    "v_lshl_or_b32 v11, v48, 7, v11\n\t"
    "v_xor_b32 v12, v48, v49\n\t"
    "v_lshlrev_b32 v12, 4, v12\n\t"
    "v_lshl_or_b32 v12, v49, 8, v12\n\t"
    "v_lshl_or_b32 v50, v48, 2, v50\n\t"
    "v_or_b32 v51, 4, v51\n\t"
    "v_xor_b32 v51, v51, v50\n\t"
    "v_lshlrev_b32 v13, 4, v51\n\t"
    "v_lshl_or_b32 v13, v50, 8, v13\n\t"
    "v_and_b32 v61, 3, v60\n\t"
    "v_lshlrev_b32 v61, 4, v61\n\t"
    "v_lshl_or_b32 v15, v48, 6, v61\n\t"
    "v_and_b32 v61, 8, v60\n\t"
    "v_lshl_or_b32 v15, v61, 14, v15\n\t"
    "v_and_b32 v61, 4, v60\n\t"
    "v_lshl_or_b32 v15, v61, 8, v15\n\t"
    "v_add_u32 v15, 256, v15\n\t"
    "3:\n\t"
    "v_mov_b32 v16, 0\n\t"
    "v_mov_b32 v17, 0\n\t"
    "v_mov_b32 v18, 0\n\t"
    "v_mov_b32 v19, 0\n\t"
    "v_mov_b32 v20, 0\n\t"
    "v_mov_b32 v21, 0\n\t"
    "v_mov_b32 v22, 0\n\t"
    "v_mov_b32 v23, 0\n\t"
    "s_waitcnt vmcnt(0) lgkmcnt(0)\n\t"
    "14:\n\t"
    "ds_write_b128 v6, v[40:43] offset:2048\n\t"
    "ds_write_b128 v6, v[44:47] offset:3072\n\t"
    "v_smfmac_f32_16x16x64_bf16 v[16:19], v[40:43], v[96:103], v9\n\t"
    "v_max3_u32 v60, v40, v41, v42\n\t"
    "v_max3_u32 v61, v43, v44, v45\n\t"
    "v_smfmac_f32_16x16x64_bf16 v[20:23], v[40:43], v[104:111], v9\n\t"
    "v_max3_u32 v60, v60, v46, v47\n\t"
    "v_max_u32 v60, v60, v61\n\t"
    "v_cmp_eq_u32 vcc, -1, v60\n\t"
    "s_cbranch_vccnz 1f\n\t"
    "2:\n\t"
    "s_waitcnt lgkmcnt(0)\n\t"
    "s_barrier\n\t"
    "ds_read_b128 v[40:43], v6 offset:12288\n\t"
    "ds_read_b128 v[48:51], v6 offset:0\n\t"
    "v_smfmac_f32_16x16x64_bf16 v[16:19], v[44:47], v[112:119], v9\n\t"
    "ds_read_b128 v[52:55], v6 offset:1024\n\t"
    "ds_read_b128 v[56:59], v6 offset:4096\n\t"
    "v_smfmac_f32_16x16x64_bf16 v[20:23], v[44:47], v[120:127], v9\n\t"
    "ds_read_b128 v[60:63], v6 offset:13312\n\t"
    "ds_read_b128 v[44:47], v6 offset:5120\n\t"
    "s_waitcnt lgkmcnt(5)\n\t"
    "v_smfmac_f32_16x16x64_bf16 v[16:19], v[40:43], v[24:31], v9\n\t"
    "ds_read_b128 v[24:27], v7 offset:16384\n\t"
    "ds_read_b128 v[28:31], v7 offset:17408\n\t"
    "v_smfmac_f32_16x16x64_bf16 v[20:23], v[40:43], v[32:39], v9\n\t"
    "ds_read_b128 v[32:35], v7 offset:24576\n\t"
    "ds_read_b128 v[36:39], v7 offset:25600\n\t"
    "ds_read_b128 v[40:43], v6 offset:6144\n\t"
    "s_waitcnt lgkmcnt(9)\n\t"
    "v_smfmac_f32_16x16x64_bf16 v[16:19], v[48:51], v[64:71], v9\n\t"
    "v_smfmac_f32_16x16x64_bf16 v[20:23], v[48:51], v[72:79], v9\n\t"
    "ds_read_b128 v[48:51], v6 offset:7168\n\t"
    "s_waitcnt lgkmcnt(9)\n\t"
    "v_smfmac_f32_16x16x64_bf16 v[16:19], v[52:55], v[80:87], v9\n\t"
    "v_smfmac_f32_16x16x64_bf16 v[20:23], v[52:55], v[88:95], v9\n\t"
    "ds_read_b128 v[52:55], v6 offset:14336\n\t"
    "s_waitcnt lgkmcnt(9)\n\t"
    "v_smfmac_f32_16x16x64_bf16 v[16:19], v[56:59], v[128:135], v9\n\t"
    "v_smfmac_f32_16x16x64_bf16 v[20:23], v[56:59], v[136:143], v9\n\t"
    "ds_read_b128 v[56:59], v6 offset:8192\n\t"
    "s_waitcnt lgkmcnt(6)\n\t"
    "v_smfmac_f32_16x16x64_bf16 v[16:19], v[60:63], v[24:31], v9\n\t"
    "ds_read_b128 v[24:27], v8 offset:0\n\t"
    "ds_read_b128 v[28:31], v8 offset:1024\n\t"
    "s_waitcnt lgkmcnt(6)\n\t"
    "v_smfmac_f32_16x16x64_bf16 v[20:23], v[60:63], v[32:39], v9\n\t"
    "ds_read_b128 v[32:35], v8 offset:8192\n\t"
    "ds_read_b128 v[36:39], v8 offset:9216\n\t"
    "ds_read_b128 v[60:63], v6 offset:9216\n\t"
    "v_smfmac_f32_16x16x64_bf16 v[16:19], v[44:47], v[144:151], v9\n\t"
    "v_smfmac_f32_16x16x64_bf16 v[20:23], v[44:47], v[152:159], v9\n\t"
    "ds_read_b128 v[44:47], v6 offset:10240\n\t"
    "s_waitcnt lgkmcnt(9)\n\t"
    "v_smfmac_f32_16x16x64_bf16 v[16:19], v[40:43], v[160:167], v9\n\t"
    "v_smfmac_f32_16x16x64_bf16 v[20:23], v[40:43], v[168:175], v9\n\t"
    "ds_read_b128 v[40:43], v6 offset:15360\n\t"
    "s_waitcnt lgkmcnt(9)\n\t"
    "v_smfmac_f32_16x16x64_bf16 v[16:19], v[48:51], v[176:183], v9\n\t"
    "v_smfmac_f32_16x16x64_bf16 v[20:23], v[48:51], v[184:191], v9\n\t"
    "ds_read_b128 v[48:51], v6 offset:11264\n\t"
    "s_waitcnt lgkmcnt(6)\n\t"
    "v_smfmac_f32_16x16x64_bf16 v[16:19], v[52:55], v[24:31], v9\n\t"
    "ds_read_b128 v[24:27], v8 offset:16384\n\t"
    "ds_read_b128 v[28:31], v8 offset:17408\n\t"
    "s_waitcnt lgkmcnt(6)\n\t"
    "v_smfmac_f32_16x16x64_bf16 v[20:23], v[52:55], v[32:39], v9\n\t"
    "ds_read_b128 v[32:35], v8 offset:24576\n\t"
    "ds_read_b128 v[36:39], v8 offset:25600\n\t"
    "v_smfmac_f32_16x16x64_bf16 v[16:19], v[56:59], v[192:199], v9\n\t"
    "v_smfmac_f32_16x16x64_bf16 v[20:23], v[56:59], v[200:207], v9\n\t"
    "s_waitcnt lgkmcnt(7)\n\t"
    "v_smfmac_f32_16x16x64_bf16 v[16:19], v[60:63], v[208:215], v9\n\t"
    "v_smfmac_f32_16x16x64_bf16 v[20:23], v[60:63], v[216:223], v9\n\t"
    "s_waitcnt lgkmcnt(6)\n\t"
    "v_smfmac_f32_16x16x64_bf16 v[16:19], v[44:47], v[224:231], v9\n\t"
    "v_smfmac_f32_16x16x64_bf16 v[20:23], v[44:47], v[232:239], v9\n\t"
    "s_waitcnt lgkmcnt(2)\n\t"
    "v_smfmac_f32_16x16x64_bf16 v[16:19], v[40:43], v[24:31], v9\n\t"
    "s_waitcnt lgkmcnt(0)\n\t"
    "v_smfmac_f32_16x16x64_bf16 v[20:23], v[40:43], v[32:39], v9\n\t"
    "v_smfmac_f32_16x16x64_bf16 v[16:19], v[48:51], v[240:247], v9\n\t"
    "v_smfmac_f32_16x16x64_bf16 v[20:23], v[48:51], v[248:255], v9\n\t"
    "v_xor_b32 v7, 64, v12\n\t"
    "v_xor_b32 v8, 0x80, v12\n\t"
    "v_xor_b32 v6, 0xc0, v12\n\t"
    "ds_read_b128 v[24:27], v12 offset:16384\n\t"
    "ds_read_b128 v[28:31], v12 offset:20480\n\t"
    "ds_read_b128 v[32:35], v7 offset:16384\n\t"
    "ds_read_b128 v[36:39], v7 offset:20480\n\t"
    "v_permlane16_swap_b32 v16, v18\n\t"
    "v_permlane16_swap_b32 v17, v19\n\t"
    "v_permlane16_swap_b32 v20, v22\n\t"
    "v_permlane16_swap_b32 v21, v23\n\t"
    "v_add_f32 v16, v16, v18\n\t"
    "v_add_f32 v17, v17, v19\n\t"
    "v_add_f32 v20, v20, v22\n\t"
    "v_mul_f32 v16, %[lsc], v16\n\t"
    "v_mul_f32 v17, %[lsc], v17\n\t"
    "v_cvt_pk_bf16_f32 v16, v16, v17\n\t"
    "v_add_f32 v21, v21, v23\n\t"
    "ds_write_b32 v10, v16 offset:33280\n\t"
    "v_mul_f32 v20, %[lsc], v20\n\t"
    "v_mul_f32 v21, %[lsc], v21\n\t"
    "v_cvt_pk_bf16_f32 v20, v20, v21\n\t"
    "ds_write_b32 v10, v20 offset:33536\n\t"
    "ds_read_b128 v[40:43], v8 offset:16384\n\t"
    "ds_read_b128 v[44:47], v8 offset:20480\n\t"
    "ds_read_b128 v[48:51], v6 offset:16384\n\t"
    "ds_read_b128 v[52:55], v6 offset:20480\n\t"
    "s_waitcnt lgkmcnt(4)\n\t"
    "s_barrier\n\t"
    "ds_read_b64_tr_b16 v[16:17], v11 offset:32768\n\t"
    "ds_read_b64_tr_b16 v[18:19], v11 offset:32832\n\t"
    "ds_read_b64_tr_b16 v[20:21], v11 offset:33280\n\t"
    "ds_read_b64_tr_b16 v[22:23], v11 offset:33344\n\t"
    "ds_read_b64_tr_b16 v[56:57], v11 offset:33792\n\t"
    "ds_read_b64_tr_b16 v[58:59], v11 offset:33856\n\t"
    "ds_read_b64_tr_b16 v[60:61], v11 offset:34304\n\t"
    "ds_read_b64_tr_b16 v[62:63], v11 offset:34368\n\t"
    "v_mov_b32 v6, 0\n\t"
    "v_mov_b32 v7, 0\n\t"
    "v_mov_b32 v8, 0\n\t"
    "v_mov_b32 v9, 0\n\t"
    "s_mov_b32 exec_hi, 0\n\t"
    "global_load_dwordx4 v[6:9], v14, %[vs] sc1\n\t"
    "s_mov_b32 exec_hi, -1\n\t"
    "s_waitcnt lgkmcnt(6)\n\t"
    "v_mfma_f32_16x16x32_bf16 v[24:27], v[24:27], v[16:19], 0\n\t"
    "v_mfma_f32_16x16x32_bf16 v[28:31], v[28:31], v[16:19], 0\n\t"
    "s_waitcnt lgkmcnt(4)\n\t"
    "v_mfma_f32_16x16x32_bf16 v[24:27], v[32:35], v[20:23], v[24:27]\n\t"
    "v_mfma_f32_16x16x32_bf16 v[28:31], v[36:39], v[20:23], v[28:31]\n\t"
    "ds_read_b64_tr_b16 v[32:33], v13 offset:24576\n\t"
    "ds_read_b64_tr_b16 v[34:35], v13 offset:28672\n\t"
    "ds_read_b64_tr_b16 v[36:37], v13 offset:24584\n\t"
    "ds_read_b64_tr_b16 v[38:39], v13 offset:28680\n\t"
    "s_waitcnt lgkmcnt(6)\n\t"
    "v_mfma_f32_16x16x32_bf16 v[24:27], v[40:43], v[56:59], v[24:27]\n\t"
    "v_mfma_f32_16x16x32_bf16 v[28:31], v[44:47], v[56:59], v[28:31]\n\t"
    "s_waitcnt lgkmcnt(4)\n\t"
    "v_mfma_f32_16x16x32_bf16 v[24:27], v[48:51], v[60:63], v[24:27]\n\t"
    "v_mfma_f32_16x16x32_bf16 v[28:31], v[52:55], v[60:63], v[28:31]\n\t"
    "s_setprio 1\n\t"
    "v_mov_b32 v52, %[one]\n\t"
    "v_mov_b32 v53, %[one]\n\t"
    "v_mov_b32 v54, %[one]\n\t"
    "v_mov_b32 v55, %[one]\n\t"
    "s_nop 2\n\t"
    "v_max3_f32 v10, |v24|, |v25|, |v26|\n\t"
    "v_max3_f32 v40, |v27|, |v28|, |v29|\n\t"
    "v_max3_f32 v10, v10, |v30|, |v31|\n\t"
    "v_max_f32 v10, v10, v40\n\t"
    "v_cmp_nge_f32 vcc, %[big], v10\n\t"
    "s_mov_b32 %[bg], %[big]\n\t"
    "v_mov_b32 v10, 0\n\t"
    "s_cbranch_vccnz 12f\n\t"
    "13:\n\t"
    "v_exp_f32 v24, v24\n\t"
    "v_exp_f32 v25, v25\n\t"
    "v_exp_f32 v26, v26\n\t"
    "v_exp_f32 v27, v27\n\t"
    "v_exp_f32 v28, v28\n\t"
    "v_exp_f32 v29, v29\n\t"
    "v_exp_f32 v30, v30\n\t"
    "v_exp_f32 v31, v31\n\t"
    "v_cvt_pk_bf16_f32 v40, v24, v25\n\t"
    "v_cvt_pk_bf16_f32 v41, v26, v27\n\t"
    "v_cvt_pk_bf16_f32 v42, v28, v29\n\t"
    "v_cvt_pk_bf16_f32 v43, v30, v31\n\t"
    "s_setprio 3\n\t"
    "s_waitcnt lgkmcnt(0)\n\t"
    "s_nop 0\n\t"
    "v_mfma_f32_16x16x32_bf16 v[44:47], v[32:35], v[40:43], 0\n\t"
    "v_mfma_f32_16x16x32_bf16 v[48:51], v[36:39], v[40:43], 0\n\t"
    "v_mfma_f32_16x16x32_bf16 v[52:55], v[52:55], v[40:43], 0\n\t"
    "v_mov_b32 v12, 0\n\t"
    "v_mov_b32 v13, 0\n\t"
    "s_mov_b32 exec_hi, 0\n\t"
    "v_mov_b32 v12, %[one]\n\t"
    "v_mov_b32 v13, %[one]\n\t"
    "s_mov_b32 exec_hi, -1\n\t"
    "s_barrier\n\t"
    "ds_read_b64_tr_b16 v[24:25], v11 offset:34816\n\t"
    "ds_read_b64_tr_b16 v[26:27], v11 offset:34880\n\t"
    "ds_read_b64_tr_b16 v[28:29], v11 offset:35328\n\t"
    "ds_read_b64_tr_b16 v[30:31], v11 offset:35392\n\t"
    "ds_read_b64_tr_b16 v[32:33], v11 offset:35840\n\t"
    "ds_read_b64_tr_b16 v[34:35], v11 offset:35904\n\t"
    "ds_read_b64_tr_b16 v[36:37], v11 offset:36352\n\t"
    "ds_read_b64_tr_b16 v[38:39], v11 offset:36416\n\t"
    "s_waitcnt lgkmcnt(6)\n\t"
    "v_mfma_f32_16x16x32_bf16 v[40:43], v[24:27], v[16:19], 0\n\t"
    "s_waitcnt lgkmcnt(4)\n\t"
    "v_mfma_f32_16x16x32_bf16 v[40:43], v[28:31], v[20:23], v[40:43]\n\t"
    "s_waitcnt lgkmcnt(2)\n\t"
    "v_mfma_f32_16x16x32_bf16 v[40:43], v[32:35], v[56:59], v[40:43]\n\t"
    "s_waitcnt lgkmcnt(0)\n\t"
    "v_mfma_f32_16x16x32_bf16 v[40:43], v[36:39], v[60:63], v[40:43]\n\t"
    "5:\n\t"
    "s_waitcnt vmcnt(0)\n\t"
    "v_max3_u32 v11, v6, v7, v8\n\t"
    "v_max_u32 v11, v11, v9\n\t"
    "v_cmp_eq_u32 vcc, -1, v11\n\t"
    "s_cbranch_vccnz 4f\n\t"
    "6:\n\t"
    "s_nop 2\n\t"
    "v_exp_f32 v28, v40\n\t"
    "v_max3_f32 v11, v40, v41, v42\n\t"
    "v_exp_f32 v29, v41\n\t"
    "v_max_f32 v11, v11, v43\n\t"
    "v_exp_f32 v30, v42\n\t"
    "v_cmp_nge_f32 vcc, %[bg], v11\n\t"
    "v_exp_f32 v31, v43\n\t"
    "s_cbranch_vccnz 10f\n\t"
    "11:\n\t"
    "v_cvt_pk_bf16_f32 v24, v28, v29\n\t"
    "v_cvt_pk_bf16_f32 v25, v30, v31\n\t"
    "s_nop 1\n\t"
    "v_mfma_f32_16x16x16_bf16 v[52:55], v[12:13], v[24:25], v[52:55]\n\t"
    "v_mfma_f32_16x16x16_bf16 v[44:47], v[6:7], v[24:25], v[44:47]\n\t"
    "v_mfma_f32_16x16x16_bf16 v[48:51], v[8:9], v[24:25], v[48:51]\n\t"
    "s_nop 5\n\t"
    "v_rcp_f32 v26, v52\n\t"
    "s_nop 0\n\t"
    "v_pk_mul_f32 v[44:45], v[44:45], v[26:27] op_sel_hi:[1,0]\n\t"
    "v_pk_mul_f32 v[46:47], v[46:47], v[26:27] op_sel_hi:[1,0]\n\t"
    "v_pk_mul_f32 v[48:49], v[48:49], v[26:27] op_sel_hi:[1,0]\n\t"
    "v_pk_mul_f32 v[50:51], v[50:51], v[26:27] op_sel_hi:[1,0]\n\t"
    "v_cvt_pk_bf16_f32 v36, v44, v45\n\t"
    "v_cvt_pk_bf16_f32 v37, v46, v47\n\t"
    "v_cvt_pk_bf16_f32 v38, v48, v49\n\t"
    "v_cvt_pk_bf16_f32 v39, v50, v51\n\t"
    "v_min_u32 v36, -2, v36\n\t"
    "v_min_u32 v37, -2, v37\n\t"
    "v_min_u32 v38, -2, v38\n\t"
    "v_min_u32 v39, -2, v39\n\t"
    "global_store_dwordx4 v15, v[36:39], %[od] sc1\n\t"
    "s_barrier\n\t"
    "s_mov_b32 exec_hi, 0\n\t"
    "global_store_dwordx4 v14, v[6:9], %[kv] sc1\n\t"
    "s_mov_b32 exec_hi, -1\n\t"
    "s_nop 0\n\t"
    "s_waitcnt lgkmcnt(0)\n\t"
    "s_branch 9f\n\t"
    "1:\n\t"
    "s_add_u32 %[sp], %[sp], 1\n\t"
    "s_cmp_ge_u32 %[sp], %[cap]\n\t"
    "s_cbranch_scc1 7f\n\t"
    "global_load_dwordx4 v[40:43], v6, %[ag] sc1\n\t"
    "global_load_dwordx4 v[44:47], v6, %[ag] offset:1024 sc1\n\t"
    "s_waitcnt lgkmcnt(0)\n\t"
    "s_memtime %[tc]\n\t"
    "s_nop 7\n\t"
    "s_branch 3b\n\t"
    "7:\n\t"
    "s_mov_b32 %[cap], 0\n\t"
    "s_branch 2b\n\t"
    "4:\n\t"
    "s_add_u32 %[sp], %[sp], 0x10000\n\t"
    "s_lshr_b32 %[st], %[sp], 16\n\t"
    "s_cmp_ge_u32 %[st], %[cap]\n\t"
    "s_cbranch_scc1 8f\n\t"
    "s_mov_b32 exec_hi, 0\n\t"
    "global_load_dwordx4 v[6:9], v14, %[vs] sc1\n\t"
    "s_mov_b32 exec_hi, -1\n\t"
    "s_branch 5b\n\t"
    "8:\n\t"
    "s_mov_b32 %[cap], 0\n\t"
    "s_branch 6b\n\t"
    "10:\n\t"
    "v_max_f32 v11, v11, v10\n\t"
    "v_mov_b32 v26, v11\n\t"
    "s_nop 1\n\t"
    "v_permlane16_swap_b32 v11, v26\n\t"
    "v_max_f32 v11, v11, v26\n\t"
    "v_sub_f32 v28, v40, v11\n\t"
    "v_sub_f32 v29, v41, v11\n\t"
    "v_sub_f32 v30, v42, v11\n\t"
    "v_sub_f32 v31, v43, v11\n\t"
    "v_sub_f32 v11, v10, v11\n\t"
    "v_exp_f32 v11, v11\n\t"
    "v_exp_f32 v28, v28\n\t"
    "v_exp_f32 v29, v29\n\t"
    "v_exp_f32 v30, v30\n\t"
    "v_exp_f32 v31, v31\n\t"
    "v_mul_f32 v44, v44, v11\n\t"
    "v_mul_f32 v45, v45, v11\n\t"
    "v_mul_f32 v46, v46, v11\n\t"
    "v_mul_f32 v47, v47, v11\n\t"
    "v_mul_f32 v48, v48, v11\n\t"
    "v_mul_f32 v49, v49, v11\n\t"
    "v_mul_f32 v50, v50, v11\n\t"
    "v_mul_f32 v51, v51, v11\n\t"
    "v_mul_f32 v52, v52, v11\n\t"
    "v_mul_f32 v53, v53, v11\n\t"
    "v_mul_f32 v54, v54, v11\n\t"
    "v_mul_f32 v55, v55, v11\n\t"
    "s_branch 11b\n\t"
    "12:\n\t"
    "v_max3_f32 v10, v24, v25, v26\n\t"
    "v_max3_f32 v40, v27, v28, v29\n\t"
    "v_max3_f32 v10, v10, v30, v31\n\t"
    "v_max3_f32 v10, v10, v40, %[neg]\n\t"
    "v_mov_b32 v40, v10\n\t"
    "s_nop 1\n\t"
    "v_permlane16_swap_b32 v10, v40\n\t"
    "v_max_f32 v10, v10, v40\n\t"
    "v_mov_b32 v40, v10\n\t"
    "s_nop 1\n\t"
    "v_permlane32_swap_b32 v10, v40\n\t"
    "v_max_f32 v10, v10, v40\n\t"
    "v_sub_f32 v24, v24, v10\n\t"
    "v_sub_f32 v25, v25, v10\n\t"
    "v_sub_f32 v26, v26, v10\n\t"
    "v_sub_f32 v27, v27, v10\n\t"
    "v_sub_f32 v28, v28, v10\n\t"
    "v_sub_f32 v29, v29, v10\n\t"
    "v_sub_f32 v30, v30, v10\n\t"
    "v_sub_f32 v31, v31, v10\n\t"
    "s_mov_b32 %[bg], 0x7fc00000\n\t"
    "s_branch 13b\n\t"
    "9:"
    : [tc] "=&s"(tc), [sp] "=&s"(sp), [cap] "+s"(cap), [st] "=&s"(st), [bg] "=&s"(bg)
    : [ag] "s"(ag), [c44] "s"(0x4444u), [kv] "s"(kv), [vs] "s"(vs), [od] "s"(od), [od2] "s"(od + 262144), [neg] "s"(-1.0e38f), [lsc] "s"(LSC), [one] "s"(0x3F803F80u), [big] "s"(24.0f), "{v[64:95]}"(W.w0), "{v[96:127]}"(W.w1), "{v[128:159]}"(W.w2), "{v[160:191]}"(W.w3), "{v[192:223]}"(W.w4), "{v[224:255]}"(W.w5)
    : "v6", "v7", "v8", "v9", "v10", "v11", "v12", "v13", "v14", "v15", "v16", "v17", "v18", "v19", "v20", "v21", "v22", "v23", "v24", "v25", "v26", "v27", "v28", "v29", "v30", "v31", "v32", "v33", "v34", "v35", "v36", "v37", "v38", "v39", "v40", "v41", "v42", "v43", "v44", "v45", "v46", "v47", "v48", "v49", "v50", "v51", "v52", "v53", "v54", "v55", "v56", "v57", "v58", "v59", "v60", "v61", "v62", "v63", "vcc", "scc", "memory");
}
template <> __device__ __forceinline__ void qk_step<2>(const QKW& W, const char* ag, const char* vs, const char* od, const char* kv, unsigned& cap, unsigned& sp,
                                                        unsigned long long& tc, unsigned long long* ts, unsigned& d0, unsigned& d1) {
  unsigned st, bg;
  asm volatile("v_mbcnt_lo_u32_b32 v60, -1, 0\n\t"
    "v_mbcnt_hi_u32_b32 v60, -1, v60\n\t"
    "v_lshlrev_b32 v6, 4, v60\n\t"
    "s_mov_b32 %[sp], 0\n\t"
    "s_setprio 3\n\t"
    "global_load_dwordx4 v[40:43], v6, %[ag] sc1\n\t"
    "global_load_dwordx4 v[44:47], v6, %[ag] offset:1024 sc1\n\t"
    "s_memtime %[tc]\n\t"
    "v_add_u32 v7, 45056, v6\n\t"
    "v_add_u32 v8, 102400, v6\n\t"
    "ds_read_b128 v[24:27], v7 offset:0\n\t"
    "ds_read_b128 v[28:31], v7 offset:1024\n\t"
    "ds_read_b128 v[32:35], v7 offset:8192\n\t"
    "ds_read_b128 v[36:39], v7 offset:9216\n\t"
    "v_lshrrev_b32 v48, 4, v60\n\t"
    "v_and_b32 v49, 15, v60\n\t"
    "v_and_b32 v61, 4, v60\n\t"
    "v_mul_u32_u24 v61, 0x2aaa, v61\n\t"
    "v_lshrrev_b32 v9, 1, v60\n\t"
    "v_and_b32 v9, 2, v9\n\t"
    "v_or3_b32 v9, v9, v61, %[c44]\n\t"
    "v_add_u32 v14, 1024, v6\n\t"
    "v_lshlrev_b32 v61, 2, v48\n\t"
    "v_lshl_or_b32 v10, v49, 4, v61\n\t"
    "v_lshrrev_b32 v50, 2, v49\n\t"
    "v_and_b32 v51, 3, v60\n\t"
    "v_and_b32 v61, 1, v60\n\t"
    "v_lshlrev_b32 v61, 3, v61\n\t"
    "v_lshl_or_b32 v11, v50, 4, v61\n\t"
    "v_lshl_or_b32 v11, v48, 7, v11\n\t"
    "v_xor_b32 v12, v48, v49\n\t"
    "v_lshlrev_b32 v12, 4, v12\n\t"
    "v_lshl_or_b32 v12, v49, 8, v12\n\t"
    "v_lshl_or_b32 v50, v48, 2, v50\n\t"
    "v_or_b32 v51, 8, v51\n\t"
    "v_xor_b32 v51, v51, v50\n\t"
    "v_lshlrev_b32 v13, 4, v51\n\t"
    "v_lshl_or_b32 v13, v50, 8, v13\n\t"
    "v_and_b32 v61, 3, v60\n\t"
    "v_lshlrev_b32 v61, 4, v61\n\t"
    "v_lshl_or_b32 v15, v48, 6, v61\n\t"
    "v_and_b32 v61, 8, v60\n\t"
    "v_lshl_or_b32 v15, v61, 14, v15\n\t"
    "v_and_b32 v61, 4, v60\n\t"
    "v_lshl_or_b32 v15, v61, 8, v15\n\t"
    "v_add_u32 v15, 512, v15\n\t"
    "3:\n\t"
    "v_mov_b32 v16, 0\n\t"
    "v_mov_b32 v17, 0\n\t"
    "v_mov_b32 v18, 0\n\t"
    "v_mov_b32 v19, 0\n\t"
    "v_mov_b32 v20, 0\n\t"
    "v_mov_b32 v21, 0\n\t"
    "v_mov_b32 v22, 0\n\t"
    "v_mov_b32 v23, 0\n\t"
    "s_waitcnt vmcnt(0) lgkmcnt(0)\n\t"
    "14:\n\t"
    "ds_write_b128 v6, v[40:43] offset:4096\n\t"
    "ds_write_b128 v6, v[44:47] offset:5120\n\t"
    "v_smfmac_f32_16x16x64_bf16 v[16:19], v[40:43], v[128:135], v9\n\t"
    "v_max3_u32 v60, v40, v41, v42\n\t"
    "v_max3_u32 v61, v43, v44, v45\n\t"
    "v_smfmac_f32_16x16x64_bf16 v[20:23], v[40:43], v[136:143], v9\n\t"
    "v_max3_u32 v60, v60, v46, v47\n\t"
    "v_max_u32 v60, v60, v61\n\t"
    "v_cmp_eq_u32 vcc, -1, v60\n\t"
    "s_cbranch_vccnz 1f\n\t"
    "2:\n\t"
    "s_waitcnt lgkmcnt(0)\n\t"
    "s_barrier\n\t"
    "ds_read_b128 v[40:43], v6 offset:12288\n\t"
    "ds_read_b128 v[48:51], v6 offset:0\n\t"
    "v_smfmac_f32_16x16x64_bf16 v[16:19], v[44:47], v[144:151], v9\n\t"
    "ds_read_b128 v[52:55], v6 offset:1024\n\t"
    "ds_read_b128 v[56:59], v6 offset:2048\n\t"
    "v_smfmac_f32_16x16x64_bf16 v[20:23], v[44:47], v[152:159], v9\n\t"
    "ds_read_b128 v[60:63], v6 offset:13312\n\t"
    "ds_read_b128 v[44:47], v6 offset:3072\n\t"
    "s_waitcnt lgkmcnt(5)\n\t"
    "v_smfmac_f32_16x16x64_bf16 v[16:19], v[40:43], v[24:31], v9\n\t"
    "ds_read_b128 v[24:27], v7 offset:16384\n\t"
    "ds_read_b128 v[28:31], v7 offset:17408\n\t"
    "v_smfmac_f32_16x16x64_bf16 v[20:23], v[40:43], v[32:39], v9\n\t"
    "ds_read_b128 v[32:35], v7 offset:24576\n\t"
    "ds_read_b128 v[36:39], v7 offset:25600\n\t"
    "ds_read_b128 v[40:43], v6 offset:6144\n\t"
    "s_waitcnt lgkmcnt(9)\n\t"
    "v_smfmac_f32_16x16x64_bf16 v[16:19], v[48:51], v[64:71], v9\n\t"
    "v_smfmac_f32_16x16x64_bf16 v[20:23], v[48:51], v[72:79], v9\n\t"
    "ds_read_b128 v[48:51], v6 offset:7168\n\t"
    "s_waitcnt lgkmcnt(9)\n\t"
    "v_smfmac_f32_16x16x64_bf16 v[16:19], v[52:55], v[80:87], v9\n\t"
    "v_smfmac_f32_16x16x64_bf16 v[20:23], v[52:55], v[88:95], v9\n\t"
    "ds_read_b128 v[52:55], v6 offset:14336\n\t"
    "s_waitcnt lgkmcnt(9)\n\t"
    "v_smfmac_f32_16x16x64_bf16 v[16:19], v[56:59], v[96:103], v9\n\t"
    "v_smfmac_f32_16x16x64_bf16 v[20:23], v[56:59], v[104:111], v9\n\t"
    "ds_read_b128 v[56:59], v6 offset:8192\n\t"
    "s_waitcnt lgkmcnt(6)\n\t"
    "v_smfmac_f32_16x16x64_bf16 v[16:19], v[60:63], v[24:31], v9\n\t"
    "ds_read_b128 v[24:27], v8 offset:0\n\t"
    "ds_read_b128 v[28:31], v8 offset:1024\n\t"
    "s_waitcnt lgkmcnt(6)\n\t"
    "v_smfmac_f32_16x16x64_bf16 v[20:23], v[60:63], v[32:39], v9\n\t"
    "ds_read_b128 v[32:35], v8 offset:8192\n\t"
    "ds_read_b128 v[36:39], v8 offset:9216\n\t"
    "ds_read_b128 v[60:63], v6 offset:9216\n\t"
    "v_smfmac_f32_16x16x64_bf16 v[16:19], v[44:47], v[112:119], v9\n\t"
    "v_smfmac_f32_16x16x64_bf16 v[20:23], v[44:47], v[120:127], v9\n\t"
    "ds_read_b128 v[44:47], v6 offset:10240\n\t"
    "s_waitcnt lgkmcnt(9)\n\t"
    "v_smfmac_f32_16x16x64_bf16 v[16:19], v[40:43], v[160:167], v9\n\t"
    "v_smfmac_f32_16x16x64_bf16 v[20:23], v[40:43], v[168:175], v9\n\t"
    "ds_read_b128 v[40:43], v6 offset:15360\n\t"
    "s_waitcnt lgkmcnt(9)\n\t"
    "v_smfmac_f32_16x16x64_bf16 v[16:19], v[48:51], v[176:183], v9\n\t"
    "v_smfmac_f32_16x16x64_bf16 v[20:23], v[48:51], v[184:191], v9\n\t"
    "ds_read_b128 v[48:51], v6 offset:11264\n\t"
    "s_waitcnt lgkmcnt(6)\n\t"
    "v_smfmac_f32_16x16x64_bf16 v[16:19], v[52:55], v[24:31], v9\n\t"
    "ds_read_b128 v[24:27], v8 offset:16384\n\t"
    "ds_read_b128 v[28:31], v8 offset:17408\n\t"
    "s_waitcnt lgkmcnt(6)\n\t"
    "v_smfmac_f32_16x16x64_bf16 v[20:23], v[52:55], v[32:39], v9\n\t"
    "ds_read_b128 v[32:35], v8 offset:24576\n\t"
    "ds_read_b128 v[36:39], v8 offset:25600\n\t"
    "v_smfmac_f32_16x16x64_bf16 v[16:19], v[56:59], v[192:199], v9\n\t"
    "v_smfmac_f32_16x16x64_bf16 v[20:23], v[56:59], v[200:207], v9\n\t"
    "s_waitcnt lgkmcnt(7)\n\t"
    "v_smfmac_f32_16x16x64_bf16 v[16:19], v[60:63], v[208:215], v9\n\t"
    "v_smfmac_f32_16x16x64_bf16 v[20:23], v[60:63], v[216:223], v9\n\t"
    "s_waitcnt lgkmcnt(6)\n\t"
    "v_smfmac_f32_16x16x64_bf16 v[16:19], v[44:47], v[224:231], v9\n\t"
    "v_smfmac_f32_16x16x64_bf16 v[20:23], v[44:47], v[232:239], v9\n\t"
    "s_waitcnt lgkmcnt(2)\n\t"
    "v_smfmac_f32_16x16x64_bf16 v[16:19], v[40:43], v[24:31], v9\n\t"
    "s_waitcnt lgkmcnt(0)\n\t"
    "v_smfmac_f32_16x16x64_bf16 v[20:23], v[40:43], v[32:39], v9\n\t"
    "v_smfmac_f32_16x16x64_bf16 v[16:19], v[48:51], v[240:247], v9\n\t"
    "v_smfmac_f32_16x16x64_bf16 v[20:23], v[48:51], v[248:255], v9\n\t"
    "v_xor_b32 v7, 64, v12\n\t"
    "v_xor_b32 v8, 0x80, v12\n\t"
    "v_xor_b32 v6, 0xc0, v12\n\t"
    "ds_read_b128 v[24:27], v12 offset:16384\n\t"
    "ds_read_b128 v[28:31], v12 offset:20480\n\t"
    "ds_read_b128 v[32:35], v7 offset:16384\n\t"
    "ds_read_b128 v[36:39], v7 offset:20480\n\t"
    "v_permlane16_swap_b32 v16, v18\n\t"
    "v_permlane16_swap_b32 v17, v19\n\t"
    "v_permlane16_swap_b32 v20, v22\n\t"
    "v_permlane16_swap_b32 v21, v23\n\t"
    "v_add_f32 v16, v16, v18\n\t"
    "v_add_f32 v17, v17, v19\n\t"
    "v_add_f32 v20, v20, v22\n\t"
    "v_mul_f32 v16, %[lsc], v16\n\t"
    "v_mul_f32 v17, %[lsc], v17\n\t"
    "v_cvt_pk_bf16_f32 v16, v16, v17\n\t"
    "v_add_f32 v21, v21, v23\n\t"
    "ds_write_b32 v10, v16 offset:33792\n\t"
    "v_mul_f32 v20, %[lsc], v20\n\t"
    "v_mul_f32 v21, %[lsc], v21\n\t"
    "v_cvt_pk_bf16_f32 v20, v20, v21\n\t"
    "ds_write_b32 v10, v20 offset:34048\n\t"
    "ds_read_b128 v[40:43], v8 offset:16384\n\t"
    "ds_read_b128 v[44:47], v8 offset:20480\n\t"
    "ds_read_b128 v[48:51], v6 offset:16384\n\t"
    "ds_read_b128 v[52:55], v6 offset:20480\n\t"
    "s_waitcnt lgkmcnt(4)\n\t"
    "s_barrier\n\t"
    "ds_read_b64_tr_b16 v[16:17], v11 offset:32768\n\t"
    "ds_read_b64_tr_b16 v[18:19], v11 offset:32832\n\t"
    "ds_read_b64_tr_b16 v[20:21], v11 offset:33280\n\t"
    "ds_read_b64_tr_b16 v[22:23], v11 offset:33344\n\t"
    "ds_read_b64_tr_b16 v[56:57], v11 offset:33792\n\t"
    "ds_read_b64_tr_b16 v[58:59], v11 offset:33856\n\t"
    "ds_read_b64_tr_b16 v[60:61], v11 offset:34304\n\t"
    "ds_read_b64_tr_b16 v[62:63], v11 offset:34368\n\t"
    "v_mov_b32 v6, 0\n\t"
    "v_mov_b32 v7, 0\n\t"
    "v_mov_b32 v8, 0\n\t"
    "v_mov_b32 v9, 0\n\t"
    "s_mov_b32 exec_hi, 0\n\t"
    "global_load_dwordx4 v[6:9], v14, %[vs] sc1\n\t"
    "s_mov_b32 exec_hi, -1\n\t"
    "s_waitcnt lgkmcnt(6)\n\t"
    "v_mfma_f32_16x16x32_bf16 v[24:27], v[24:27], v[16:19], 0\n\t"
    "v_mfma_f32_16x16x32_bf16 v[28:31], v[28:31], v[16:19], 0\n\t"
    "s_waitcnt lgkmcnt(4)\n\t"
    "v_mfma_f32_16x16x32_bf16 v[24:27], v[32:35], v[20:23], v[24:27]\n\t"
    "v_mfma_f32_16x16x32_bf16 v[28:31], v[36:39], v[20:23], v[28:31]\n\t"
    "ds_read_b64_tr_b16 v[32:33], v13 offset:24576\n\t"
    "ds_read_b64_tr_b16 v[34:35], v13 offset:28672\n\t"
    "ds_read_b64_tr_b16 v[36:37], v13 offset:24584\n\t"
    "ds_read_b64_tr_b16 v[38:39], v13 offset:28680\n\t"
    "s_waitcnt lgkmcnt(6)\n\t"
    "v_mfma_f32_16x16x32_bf16 v[24:27], v[40:43], v[56:59], v[24:27]\n\t"
    "v_mfma_f32_16x16x32_bf16 v[28:31], v[44:47], v[56:59], v[28:31]\n\t"
    "s_waitcnt lgkmcnt(4)\n\t"
    "v_mfma_f32_16x16x32_bf16 v[24:27], v[48:51], v[60:63], v[24:27]\n\t"
    "v_mfma_f32_16x16x32_bf16 v[28:31], v[52:55], v[60:63], v[28:31]\n\t"
    "s_setprio 1\n\t"
    "v_mov_b32 v52, %[one]\n\t"
    "v_mov_b32 v53, %[one]\n\t"
    "v_mov_b32 v54, %[one]\n\t"
    "v_mov_b32 v55, %[one]\n\t"
    "s_nop 2\n\t"
    "v_max3_f32 v10, |v24|, |v25|, |v26|\n\t"
    "v_max3_f32 v40, |v27|, |v28|, |v29|\n\t"
    "v_max3_f32 v10, v10, |v30|, |v31|\n\t"
    "v_max_f32 v10, v10, v40\n\t"
    "v_cmp_nge_f32 vcc, %[big], v10\n\t"
    "s_mov_b32 %[bg], %[big]\n\t"
    "v_mov_b32 v10, 0\n\t"
    "s_cbranch_vccnz 12f\n\t"
    "13:\n\t"
    "v_exp_f32 v24, v24\n\t"
    "v_exp_f32 v25, v25\n\t"
    "v_exp_f32 v26, v26\n\t"
    "v_exp_f32 v27, v27\n\t"
    "v_exp_f32 v28, v28\n\t"
    "v_exp_f32 v29, v29\n\t"
    "v_exp_f32 v30, v30\n\t"
    "v_exp_f32 v31, v31\n\t"
    "v_cvt_pk_bf16_f32 v40, v24, v25\n\t"
    "v_cvt_pk_bf16_f32 v41, v26, v27\n\t"
    "v_cvt_pk_bf16_f32 v42, v28, v29\n\t"
    "v_cvt_pk_bf16_f32 v43, v30, v31\n\t"
    "s_setprio 3\n\t"
    "s_waitcnt lgkmcnt(0)\n\t"
    "s_nop 0\n\t"
    "v_mfma_f32_16x16x32_bf16 v[44:47], v[32:35], v[40:43], 0\n\t"
    "v_mfma_f32_16x16x32_bf16 v[48:51], v[36:39], v[40:43], 0\n\t"
    "v_mfma_f32_16x16x32_bf16 v[52:55], v[52:55], v[40:43], 0\n\t"
    "v_mov_b32 v12, 0\n\t"
    "v_mov_b32 v13, 0\n\t"
    "s_mov_b32 exec_hi, 0\n\t"
    "v_mov_b32 v12, %[one]\n\t"
    "v_mov_b32 v13, %[one]\n\t"
    "s_mov_b32 exec_hi, -1\n\t"
    "s_barrier\n\t"
    "ds_read_b64_tr_b16 v[24:25], v11 offset:34816\n\t"
    "ds_read_b64_tr_b16 v[26:27], v11 offset:34880\n\t"
    "ds_read_b64_tr_b16 v[28:29], v11 offset:35328\n\t"
    "ds_read_b64_tr_b16 v[30:31], v11 offset:35392\n\t"
    "ds_read_b64_tr_b16 v[32:33], v11 offset:35840\n\t"
    "ds_read_b64_tr_b16 v[34:35], v11 offset:35904\n\t"
    "ds_read_b64_tr_b16 v[36:37], v11 offset:36352\n\t"
    "ds_read_b64_tr_b16 v[38:39], v11 offset:36416\n\t"
    "s_waitcnt lgkmcnt(6)\n\t"
    "v_mfma_f32_16x16x32_bf16 v[40:43], v[24:27], v[16:19], 0\n\t"
    "s_waitcnt lgkmcnt(4)\n\t"
    "v_mfma_f32_16x16x32_bf16 v[40:43], v[28:31], v[20:23], v[40:43]\n\t"
    "s_waitcnt lgkmcnt(2)\n\t"
    "v_mfma_f32_16x16x32_bf16 v[40:43], v[32:35], v[56:59], v[40:43]\n\t"
    "s_waitcnt lgkmcnt(0)\n\t"
    "v_mfma_f32_16x16x32_bf16 v[40:43], v[36:39], v[60:63], v[40:43]\n\t"
    "5:\n\t"
    "s_waitcnt vmcnt(0)\n\t"
    "v_max3_u32 v11, v6, v7, v8\n\t"
    "v_max_u32 v11, v11, v9\n\t"
    "v_cmp_eq_u32 vcc, -1, v11\n\t"
    "s_cbranch_vccnz 4f\n\t"
    "6:\n\t"
    "s_nop 2\n\t"
    "v_exp_f32 v28, v40\n\t"
    "v_max3_f32 v11, v40, v41, v42\n\t"
    "v_exp_f32 v29, v41\n\t"
    "v_max_f32 v11, v11, v43\n\t"
    "v_exp_f32 v30, v42\n\t"
    "v_cmp_nge_f32 vcc, %[bg], v11\n\t"
    "v_exp_f32 v31, v43\n\t"
    "s_cbranch_vccnz 10f\n\t"
    "11:\n\t"
    "v_cvt_pk_bf16_f32 v24, v28, v29\n\t"
    "v_cvt_pk_bf16_f32 v25, v30, v31\n\t"
    "s_nop 1\n\t"
    "v_mfma_f32_16x16x16_bf16 v[52:55], v[12:13], v[24:25], v[52:55]\n\t"
    "v_mfma_f32_16x16x16_bf16 v[44:47], v[6:7], v[24:25], v[44:47]\n\t"
    "v_mfma_f32_16x16x16_bf16 v[48:51], v[8:9], v[24:25], v[48:51]\n\t"
    "s_nop 5\n\t"
    "v_rcp_f32 v26, v52\n\t"
    "s_nop 0\n\t"
    "v_pk_mul_f32 v[44:45], v[44:45], v[26:27] op_sel_hi:[1,0]\n\t"
    "v_pk_mul_f32 v[46:47], v[46:47], v[26:27] op_sel_hi:[1,0]\n\t"
    "v_pk_mul_f32 v[48:49], v[48:49], v[26:27] op_sel_hi:[1,0]\n\t"
    "v_pk_mul_f32 v[50:51], v[50:51], v[26:27] op_sel_hi:[1,0]\n\t"
    "v_cvt_pk_bf16_f32 v36, v44, v45\n\t"
    "v_cvt_pk_bf16_f32 v37, v46, v47\n\t"
    "v_cvt_pk_bf16_f32 v38, v48, v49\n\t"
    "v_cvt_pk_bf16_f32 v39, v50, v51\n\t"
    "v_min_u32 v36, -2, v36\n\t"
    "v_min_u32 v37, -2, v37\n\t"
    "v_min_u32 v38, -2, v38\n\t"
    "v_min_u32 v39, -2, v39\n\t"
    "global_store_dwordx4 v15, v[36:39], %[od] sc1\n\t"
    "s_barrier\n\t"
    "s_mov_b32 exec_hi, 0\n\t"
    "global_store_dwordx4 v14, v[6:9], %[kv] sc1\n\t"
    "s_mov_b32 exec_hi, -1\n\t"
    "s_nop 0\n\t"
    "s_waitcnt lgkmcnt(0)\n\t"
    "s_branch 9f\n\t"
    "1:\n\t"
    "s_add_u32 %[sp], %[sp], 1\n\t"
    "s_cmp_ge_u32 %[sp], %[cap]\n\t"
    "s_cbranch_scc1 7f\n\t"
    "global_load_dwordx4 v[40:43], v6, %[ag] sc1\n\t"
    "global_load_dwordx4 v[44:47], v6, %[ag] offset:1024 sc1\n\t"
    "s_waitcnt lgkmcnt(0)\n\t"
    "s_memtime %[tc]\n\t"
    "s_nop 7\n\t"
    "s_branch 3b\n\t"
    "7:\n\t"
    "s_mov_b32 %[cap], 0\n\t"
    "s_branch 2b\n\t"
    "4:\n\t"
    "s_add_u32 %[sp], %[sp], 0x10000\n\t"
    "s_lshr_b32 %[st], %[sp], 16\n\t"
    "s_cmp_ge_u32 %[st], %[cap]\n\t"
    "s_cbranch_scc1 8f\n\t"
    "s_mov_b32 exec_hi, 0\n\t"
    "global_load_dwordx4 v[6:9], v14, %[vs] sc1\n\t"
    "s_mov_b32 exec_hi, -1\n\t"
    "s_branch 5b\n\t"
    "8:\n\t"
    "s_mov_b32 %[cap], 0\n\t"
    "s_branch 6b\n\t"
    "10:\n\t"
    "v_max_f32 v11, v11, v10\n\t"
    "v_mov_b32 v26, v11\n\t"
    "s_nop 1\n\t"
    "v_permlane16_swap_b32 v11, v26\n\t"
    "v_max_f32 v11, v11, v26\n\t"
    "v_sub_f32 v28, v40, v11\n\t"
    "v_sub_f32 v29, v41, v11\n\t"
    "v_sub_f32 v30, v42, v11\n\t"
    "v_sub_f32 v31, v43, v11\n\t"
    "v_sub_f32 v11, v10, v11\n\t"
    "v_exp_f32 v11, v11\n\t"
    "v_exp_f32 v28, v28\n\t"
    "v_exp_f32 v29, v29\n\t"
    "v_exp_f32 v30, v30\n\t"
    "v_exp_f32 v31, v31\n\t"
    "v_mul_f32 v44, v44, v11\n\t"
    "v_mul_f32 v45, v45, v11\n\t"
    "v_mul_f32 v46, v46, v11\n\t"
    "v_mul_f32 v47, v47, v11\n\t"
    "v_mul_f32 v48, v48, v11\n\t"
    "v_mul_f32 v49, v49, v11\n\t"
    "v_mul_f32 v50, v50, v11\n\t"
    "v_mul_f32 v51, v51, v11\n\t"
    "v_mul_f32 v52, v52, v11\n\t"
    "v_mul_f32 v53, v53, v11\n\t"
    "v_mul_f32 v54, v54, v11\n\t"
    "v_mul_f32 v55, v55, v11\n\t"
    "s_branch 11b\n\t"
    "12:\n\t"
    "v_max3_f32 v10, v24, v25, v26\n\t"
    "v_max3_f32 v40, v27, v28, v29\n\t"
    "v_max3_f32 v10, v10, v30, v31\n\t"
    "v_max3_f32 v10, v10, v40, %[neg]\n\t"
    "v_mov_b32 v40, v10\n\t"
    "s_nop 1\n\t"
    "v_permlane16_swap_b32 v10, v40\n\t"
    "v_max_f32 v10, v10, v40\n\t"
    "v_mov_b32 v40, v10\n\t"
    "s_nop 1\n\t"
    "v_permlane32_swap_b32 v10, v40\n\t"
    "v_max_f32 v10, v10, v40\n\t"
    "v_sub_f32 v24, v24, v10\n\t"
    "v_sub_f32 v25, v25, v10\n\t"
    "v_sub_f32 v26, v26, v10\n\t"
    "v_sub_f32 v27, v27, v10\n\t"
    "v_sub_f32 v28, v28, v10\n\t"
    "v_sub_f32 v29, v29, v10\n\t"
    "v_sub_f32 v30, v30, v10\n\t"
    "v_sub_f32 v31, v31, v10\n\t"
    "s_mov_b32 %[bg], 0x7fc00000\n\t"
    "s_branch 13b\n\t"
    "9:"
    : [tc] "=&s"(tc), [sp] "=&s"(sp), [cap] "+s"(cap), [st] "=&s"(st), [bg] "=&s"(bg)
    : [ag] "s"(ag), [c44] "s"(0x4444u), [kv] "s"(kv), [vs] "s"(vs), [od] "s"(od), [od2] "s"(od + 262144), [neg] "s"(-1.0e38f), [lsc] "s"(LSC), [one] "s"(0x3F803F80u), [big] "s"(24.0f), "{v[64:95]}"(W.w0), "{v[96:127]}"(W.w1), "{v[128:159]}"(W.w2), "{v[160:191]}"(W.w3), "{v[192:223]}"(W.w4), "{v[224:255]}"(W.w5)
    : "v6", "v7", "v8", "v9", "v10", "v11", "v12", "v13", "v14", "v15", "v16", "v17", "v18", "v19", "v20", "v21", "v22", "v23", "v24", "v25", "v26", "v27", "v28", "v29", "v30", "v31", "v32", "v33", "v34", "v35", "v36", "v37", "v38", "v39", "v40", "v41", "v42", "v43", "v44", "v45", "v46", "v47", "v48", "v49", "v50", "v51", "v52", "v53", "v54", "v55", "v56", "v57", "v58", "v59", "v60", "v61", "v62", "v63", "vcc", "scc", "memory");
}
template <> __device__ __forceinline__ void qk_step<3>(const QKW& W, const char* ag, const char* vs, const char* od, const char* kv, unsigned& cap, unsigned& sp,
                                                        unsigned long long& tc, unsigned long long* ts, unsigned& d0, unsigned& d1) {
  unsigned st, bg;
  asm volatile("v_mbcnt_lo_u32_b32 v60, -1, 0\n\t"
    "v_mbcnt_hi_u32_b32 v60, -1, v60\n\t"
    "v_lshlrev_b32 v6, 4, v60\n\t"
    "s_mov_b32 %[sp], 0\n\t"
    "s_setprio 3\n\t"
    "global_load_dwordx4 v[40:43], v6, %[ag] sc1\n\t"
    "global_load_dwordx4 v[44:47], v6, %[ag] offset:1024 sc1\n\t"
    "s_memtime %[tc]\n\t"
    "v_add_u32 v7, 47104, v6\n\t"
    "v_add_u32 v8, 104448, v6\n\t"
    "ds_read_b128 v[24:27], v7 offset:0\n\t"
    "ds_read_b128 v[28:31], v7 offset:1024\n\t"
    "ds_read_b128 v[32:35], v7 offset:8192\n\t"
    "ds_read_b128 v[36:39], v7 offset:9216\n\t"
    "v_lshrrev_b32 v48, 4, v60\n\t"
    "v_and_b32 v49, 15, v60\n\t"
    "v_and_b32 v61, 4, v60\n\t"
    "v_mul_u32_u24 v61, 0x2aaa, v61\n\t"
    "v_lshrrev_b32 v9, 1, v60\n\t"
    "v_and_b32 v9, 2, v9\n\t"
    "v_or3_b32 v9, v9, v61, %[c44]\n\t"
    "v_add_u32 v14, 1536, v6\n\t"
    "v_lshlrev_b32 v61, 2, v48\n\t"
    "v_lshl_or_b32 v10, v49, 4, v61\n\t"
    "v_lshrrev_b32 v50, 2, v49\n\t"
    "v_and_b32 v51, 3, v60\n\t"
    "v_and_b32 v61, 1, v60\n\t"
    "v_lshlrev_b32 v61, 3, v61\n\t"
    "v_lshl_or_b32 v11, v50, 4, v61\n\t"
    "v_lshl_or_b32 v11, v48, 7, v11\n\t"
    "v_xor_b32 v12, v48, v49\n\t"
    "v_lshlrev_b32 v12, 4, v12\n\t"
    "v_lshl_or_b32 v12, v49, 8, v12\n\t"
    "v_lshl_or_b32 v50, v48, 2, v50\n\t"
    "v_or_b32 v51, 12, v51\n\t"
    "v_xor_b32 v51, v51, v50\n\t"
    "v_lshlrev_b32 v13, 4, v51\n\t"
    "v_lshl_or_b32 v13, v50, 8, v13\n\t"
    "v_and_b32 v61, 3, v60\n\t"
    "v_lshlrev_b32 v61, 4, v61\n\t"
    "v_lshl_or_b32 v15, v48, 6, v61\n\t"
    "v_and_b32 v61, 8, v60\n\t"
    "v_lshl_or_b32 v15, v61, 14, v15\n\t"
    "v_and_b32 v61, 4, v60\n\t"
    "v_lshl_or_b32 v15, v61, 8, v15\n\t"
    "v_add_u32 v15, 768, v15\n\t"
    "3:\n\t"
    "v_mov_b32 v16, 0\n\t"
    "v_mov_b32 v17, 0\n\t"
    "v_mov_b32 v18, 0\n\t"
    "v_mov_b32 v19, 0\n\t"
    "v_mov_b32 v20, 0\n\t"
    "v_mov_b32 v21, 0\n\t"
    "v_mov_b32 v22, 0\n\t"
    "v_mov_b32 v23, 0\n\t"
    "s_waitcnt vmcnt(0) lgkmcnt(0)\n\t"
    "14:\n\t"
    "ds_write_b128 v6, v[40:43] offset:6144\n\t"
    "ds_write_b128 v6, v[44:47] offset:7168\n\t"
    "v_smfmac_f32_16x16x64_bf16 v[16:19], v[40:43], v[160:167], v9\n\t"
    "v_max3_u32 v60, v40, v41, v42\n\t"
    "v_max3_u32 v61, v43, v44, v45\n\t"
    "v_smfmac_f32_16x16x64_bf16 v[20:23], v[40:43], v[168:175], v9\n\t"
    "v_max3_u32 v60, v60, v46, v47\n\t"
    "v_max_u32 v60, v60, v61\n\t"
    "v_cmp_eq_u32 vcc, -1, v60\n\t"
    "s_cbranch_vccnz 1f\n\t"
    "2:\n\t"
    "s_waitcnt lgkmcnt(0)\n\t"
    "s_barrier\n\t"
    "ds_read_b128 v[40:43], v6 offset:12288\n\t"
    "ds_read_b128 v[48:51], v6 offset:0\n\t"
    "v_smfmac_f32_16x16x64_bf16 v[16:19], v[44:47], v[176:183], v9\n\t"
    "ds_read_b128 v[52:55], v6 offset:1024\n\t"
    "ds_read_b128 v[56:59], v6 offset:2048\n\t"
    "v_smfmac_f32_16x16x64_bf16 v[20:23], v[44:47], v[184:191], v9\n\t"
    "ds_read_b128 v[60:63], v6 offset:13312\n\t"
    "ds_read_b128 v[44:47], v6 offset:3072\n\t"
    "s_waitcnt lgkmcnt(5)\n\t"
    "v_smfmac_f32_16x16x64_bf16 v[16:19], v[40:43], v[24:31], v9\n\t"
    "ds_read_b128 v[24:27], v7 offset:16384\n\t"
    "ds_read_b128 v[28:31], v7 offset:17408\n\t"
    "v_smfmac_f32_16x16x64_bf16 v[20:23], v[40:43], v[32:39], v9\n\t"
    "ds_read_b128 v[32:35], v7 offset:24576\n\t"
    "ds_read_b128 v[36:39], v7 offset:25600\n\t"
    "ds_read_b128 v[40:43], v6 offset:4096\n\t"
    "s_waitcnt lgkmcnt(9)\n\t"
    "v_smfmac_f32_16x16x64_bf16 v[16:19], v[48:51], v[64:71], v9\n\t"
    "v_smfmac_f32_16x16x64_bf16 v[20:23], v[48:51], v[72:79], v9\n\t"
    "ds_read_b128 v[48:51], v6 offset:5120\n\t"
    "s_waitcnt lgkmcnt(9)\n\t"
    "v_smfmac_f32_16x16x64_bf16 v[16:19], v[52:55], v[80:87], v9\n\t"
    "v_smfmac_f32_16x16x64_bf16 v[20:23], v[52:55], v[88:95], v9\n\t"
    "ds_read_b128 v[52:55], v6 offset:14336\n\t"
    "s_waitcnt lgkmcnt(9)\n\t"
    "v_smfmac_f32_16x16x64_bf16 v[16:19], v[56:59], v[96:103], v9\n\t"
    "v_smfmac_f32_16x16x64_bf16 v[20:23], v[56:59], v[104:111], v9\n\t"
    "ds_read_b128 v[56:59], v6 offset:8192\n\t"
    "s_waitcnt lgkmcnt(6)\n\t"
    "v_smfmac_f32_16x16x64_bf16 v[16:19], v[60:63], v[24:31], v9\n\t"
    "ds_read_b128 v[24:27], v8 offset:0\n\t"
    "ds_read_b128 v[28:31], v8 offset:1024\n\t"
    "s_waitcnt lgkmcnt(6)\n\t"
    "v_smfmac_f32_16x16x64_bf16 v[20:23], v[60:63], v[32:39], v9\n\t"
    "ds_read_b128 v[32:35], v8 offset:8192\n\t"
    "ds_read_b128 v[36:39], v8 offset:9216\n\t"
    "ds_read_b128 v[60:63], v6 offset:9216\n\t"
    "v_smfmac_f32_16x16x64_bf16 v[16:19], v[44:47], v[112:119], v9\n\t"
    "v_smfmac_f32_16x16x64_bf16 v[20:23], v[44:47], v[120:127], v9\n\t"
    "ds_read_b128 v[44:47], v6 offset:10240\n\t"
    "s_waitcnt lgkmcnt(9)\n\t"
    "v_smfmac_f32_16x16x64_bf16 v[16:19], v[40:43], v[128:135], v9\n\t"
    "v_smfmac_f32_16x16x64_bf16 v[20:23], v[40:43], v[136:143], v9\n\t"
    "ds_read_b128 v[40:43], v6 offset:15360\n\t"
    "s_waitcnt lgkmcnt(9)\n\t"
    "v_smfmac_f32_16x16x64_bf16 v[16:19], v[48:51], v[144:151], v9\n\t"
    "v_smfmac_f32_16x16x64_bf16 v[20:23], v[48:51], v[152:159], v9\n\t"
    "ds_read_b128 v[48:51], v6 offset:11264\n\t"
    "s_waitcnt lgkmcnt(6)\n\t"
    "v_smfmac_f32_16x16x64_bf16 v[16:19], v[52:55], v[24:31], v9\n\t"
    "ds_read_b128 v[24:27], v8 offset:16384\n\t"
    "ds_read_b128 v[28:31], v8 offset:17408\n\t"
    "s_waitcnt lgkmcnt(6)\n\t"
    "v_smfmac_f32_16x16x64_bf16 v[20:23], v[52:55], v[32:39], v9\n\t"
    "ds_read_b128 v[32:35], v8 offset:24576\n\t"
    "ds_read_b128 v[36:39], v8 offset:25600\n\t"
    "v_smfmac_f32_16x16x64_bf16 v[16:19], v[56:59], v[192:199], v9\n\t"
    "v_smfmac_f32_16x16x64_bf16 v[20:23], v[56:59], v[200:207], v9\n\t"
    "s_waitcnt lgkmcnt(7)\n\t"
    "v_smfmac_f32_16x16x64_bf16 v[16:19], v[60:63], v[208:215], v9\n\t"
    "v_smfmac_f32_16x16x64_bf16 v[20:23], v[60:63], v[216:223], v9\n\t"
    "s_waitcnt lgkmcnt(6)\n\t"
    "v_smfmac_f32_16x16x64_bf16 v[16:19], v[44:47], v[224:231], v9\n\t"
    "v_smfmac_f32_16x16x64_bf16 v[20:23], v[44:47], v[232:239], v9\n\t"
    "s_waitcnt lgkmcnt(2)\n\t"
    "v_smfmac_f32_16x16x64_bf16 v[16:19], v[40:43], v[24:31], v9\n\t"
    "s_waitcnt lgkmcnt(0)\n\t"
    "v_smfmac_f32_16x16x64_bf16 v[20:23], v[40:43], v[32:39], v9\n\t"
    "v_smfmac_f32_16x16x64_bf16 v[16:19], v[48:51], v[240:247], v9\n\t"
    "v_smfmac_f32_16x16x64_bf16 v[20:23], v[48:51], v[248:255], v9\n\t"
    "v_xor_b32 v7, 64, v12\n\t"
    "v_xor_b32 v8, 0x80, v12\n\t"
    "v_xor_b32 v6, 0xc0, v12\n\t"
    "ds_read_b128 v[24:27], v12 offset:16384\n\t"
    "ds_read_b128 v[28:31], v12 offset:20480\n\t"
    "ds_read_b128 v[32:35], v7 offset:16384\n\t"
    "ds_read_b128 v[36:39], v7 offset:20480\n\t"
    "v_permlane16_swap_b32 v16, v18\n\t"
    "v_permlane16_swap_b32 v17, v19\n\t"
    "v_permlane16_swap_b32 v20, v22\n\t"
    "v_permlane16_swap_b32 v21, v23\n\t"
    "v_add_f32 v16, v16, v18\n\t"
    "v_add_f32 v17, v17, v19\n\t"
    "v_add_f32 v20, v20, v22\n\t"
    "v_mul_f32 v16, %[lsc], v16\n\t"
    "v_mul_f32 v17, %[lsc], v17\n\t"
    "v_cvt_pk_bf16_f32 v16, v16, v17\n\t"
    "v_add_f32 v21, v21, v23\n\t"
    "ds_write_b32 v10, v16 offset:34304\n\t"
    "v_mul_f32 v20, %[lsc], v20\n\t"
    "v_mul_f32 v21, %[lsc], v21\n\t"
    "v_cvt_pk_bf16_f32 v20, v20, v21\n\t"
    "ds_write_b32 v10, v20 offset:34560\n\t"
    "ds_read_b128 v[40:43], v8 offset:16384\n\t"
    "ds_read_b128 v[44:47], v8 offset:20480\n\t"
    "ds_read_b128 v[48:51], v6 offset:16384\n\t"
    "ds_read_b128 v[52:55], v6 offset:20480\n\t"
    "s_waitcnt lgkmcnt(4)\n\t"
    "s_barrier\n\t"
    "ds_read_b64_tr_b16 v[16:17], v11 offset:32768\n\t"
    "ds_read_b64_tr_b16 v[18:19], v11 offset:32832\n\t"
    "ds_read_b64_tr_b16 v[20:21], v11 offset:33280\n\t"
    "ds_read_b64_tr_b16 v[22:23], v11 offset:33344\n\t"
    "ds_read_b64_tr_b16 v[56:57], v11 offset:33792\n\t"
    "ds_read_b64_tr_b16 v[58:59], v11 offset:33856\n\t"
    "ds_read_b64_tr_b16 v[60:61], v11 offset:34304\n\t"
    "ds_read_b64_tr_b16 v[62:63], v11 offset:34368\n\t"
    "v_mov_b32 v6, 0\n\t"
    "v_mov_b32 v7, 0\n\t"
    "v_mov_b32 v8, 0\n\t"
    "v_mov_b32 v9, 0\n\t"
    "s_mov_b32 exec_hi, 0\n\t"
    "global_load_dwordx4 v[6:9], v14, %[vs] sc1\n\t"
    "s_mov_b32 exec_hi, -1\n\t"
    "s_waitcnt lgkmcnt(6)\n\t"
    "v_mfma_f32_16x16x32_bf16 v[24:27], v[24:27], v[16:19], 0\n\t"
    "v_mfma_f32_16x16x32_bf16 v[28:31], v[28:31], v[16:19], 0\n\t"
    "s_waitcnt lgkmcnt(4)\n\t"
    "v_mfma_f32_16x16x32_bf16 v[24:27], v[32:35], v[20:23], v[24:27]\n\t"
    "v_mfma_f32_16x16x32_bf16 v[28:31], v[36:39], v[20:23], v[28:31]\n\t"
    "ds_read_b64_tr_b16 v[32:33], v13 offset:24576\n\t"
    "ds_read_b64_tr_b16 v[34:35], v13 offset:28672\n\t"
    "ds_read_b64_tr_b16 v[36:37], v13 offset:24584\n\t"
    "ds_read_b64_tr_b16 v[38:39], v13 offset:28680\n\t"
    "s_waitcnt lgkmcnt(6)\n\t"
    "v_mfma_f32_16x16x32_bf16 v[24:27], v[40:43], v[56:59], v[24:27]\n\t"
    "v_mfma_f32_16x16x32_bf16 v[28:31], v[44:47], v[56:59], v[28:31]\n\t"
    "s_waitcnt lgkmcnt(4)\n\t"
    "v_mfma_f32_16x16x32_bf16 v[24:27], v[48:51], v[60:63], v[24:27]\n\t"
    "v_mfma_f32_16x16x32_bf16 v[28:31], v[52:55], v[60:63], v[28:31]\n\t"
    "s_setprio 1\n\t"
    "v_mov_b32 v52, %[one]\n\t"
    "v_mov_b32 v53, %[one]\n\t"
    "v_mov_b32 v54, %[one]\n\t"
    "v_mov_b32 v55, %[one]\n\t"
    "s_nop 2\n\t"
    "v_max3_f32 v10, |v24|, |v25|, |v26|\n\t"
    "v_max3_f32 v40, |v27|, |v28|, |v29|\n\t"
    "v_max3_f32 v10, v10, |v30|, |v31|\n\t"
    "v_max_f32 v10, v10, v40\n\t"
    "v_cmp_nge_f32 vcc, %[big], v10\n\t"
    "s_mov_b32 %[bg], %[big]\n\t"
    "v_mov_b32 v10, 0\n\t"
    "s_cbranch_vccnz 12f\n\t"
    "13:\n\t"
    "v_exp_f32 v24, v24\n\t"
    "v_exp_f32 v25, v25\n\t"
    "v_exp_f32 v26, v26\n\t"
    "v_exp_f32 v27, v27\n\t"
    "v_exp_f32 v28, v28\n\t"
    "v_exp_f32 v29, v29\n\t"
    "v_exp_f32 v30, v30\n\t"
    "v_exp_f32 v31, v31\n\t"
    "v_cvt_pk_bf16_f32 v40, v24, v25\n\t"
    "v_cvt_pk_bf16_f32 v41, v26, v27\n\t"
    "v_cvt_pk_bf16_f32 v42, v28, v29\n\t"
    "v_cvt_pk_bf16_f32 v43, v30, v31\n\t"
    "s_setprio 3\n\t"
    "s_waitcnt lgkmcnt(0)\n\t"
    "s_nop 0\n\t"
    "v_mfma_f32_16x16x32_bf16 v[44:47], v[32:35], v[40:43], 0\n\t"
    "v_mfma_f32_16x16x32_bf16 v[48:51], v[36:39], v[40:43], 0\n\t"
    "v_mfma_f32_16x16x32_bf16 v[52:55], v[52:55], v[40:43], 0\n\t"
    "v_mov_b32 v12, 0\n\t"
    "v_mov_b32 v13, 0\n\t"
    "s_mov_b32 exec_hi, 0\n\t"
    "v_mov_b32 v12, %[one]\n\t"
    "v_mov_b32 v13, %[one]\n\t"
    "s_mov_b32 exec_hi, -1\n\t"
    "s_barrier\n\t"
    "ds_read_b64_tr_b16 v[24:25], v11 offset:34816\n\t"
    "ds_read_b64_tr_b16 v[26:27], v11 offset:34880\n\t"
    "ds_read_b64_tr_b16 v[28:29], v11 offset:35328\n\t"
    "ds_read_b64_tr_b16 v[30:31], v11 offset:35392\n\t"
    "ds_read_b64_tr_b16 v[32:33], v11 offset:35840\n\t"
    "ds_read_b64_tr_b16 v[34:35], v11 offset:35904\n\t"
    "ds_read_b64_tr_b16 v[36:37], v11 offset:36352\n\t"
    "ds_read_b64_tr_b16 v[38:39], v11 offset:36416\n\t"
    "s_waitcnt lgkmcnt(6)\n\t"
    "v_mfma_f32_16x16x32_bf16 v[40:43], v[24:27], v[16:19], 0\n\t"
    "s_waitcnt lgkmcnt(4)\n\t"
    "v_mfma_f32_16x16x32_bf16 v[40:43], v[28:31], v[20:23], v[40:43]\n\t"
    "s_waitcnt lgkmcnt(2)\n\t"
    "v_mfma_f32_16x16x32_bf16 v[40:43], v[32:35], v[56:59], v[40:43]\n\t"
    "s_waitcnt lgkmcnt(0)\n\t"
    "v_mfma_f32_16x16x32_bf16 v[40:43], v[36:39], v[60:63], v[40:43]\n\t"
    "5:\n\t"
    "s_waitcnt vmcnt(0)\n\t"
    "v_max3_u32 v11, v6, v7, v8\n\t"
    "v_max_u32 v11, v11, v9\n\t"
    "v_cmp_eq_u32 vcc, -1, v11\n\t"
    "s_cbranch_vccnz 4f\n\t"
    "6:\n\t"
    "s_nop 2\n\t"
    "v_exp_f32 v28, v40\n\t"
    "v_max3_f32 v11, v40, v41, v42\n\t"
    "v_exp_f32 v29, v41\n\t"
    "v_max_f32 v11, v11, v43\n\t"
    "v_exp_f32 v30, v42\n\t"
    "v_cmp_nge_f32 vcc, %[bg], v11\n\t"
    "v_exp_f32 v31, v43\n\t"
    "s_cbranch_vccnz 10f\n\t"
    "11:\n\t"
    "v_cvt_pk_bf16_f32 v24, v28, v29\n\t"
    "v_cvt_pk_bf16_f32 v25, v30, v31\n\t"
    "s_nop 1\n\t"
    "v_mfma_f32_16x16x16_bf16 v[52:55], v[12:13], v[24:25], v[52:55]\n\t"
    "v_mfma_f32_16x16x16_bf16 v[44:47], v[6:7], v[24:25], v[44:47]\n\t"
    "v_mfma_f32_16x16x16_bf16 v[48:51], v[8:9], v[24:25], v[48:51]\n\t"
    "s_nop 5\n\t"
    "v_rcp_f32 v26, v52\n\t"
    "s_nop 0\n\t"
    "v_pk_mul_f32 v[44:45], v[44:45], v[26:27] op_sel_hi:[1,0]\n\t"
    "v_pk_mul_f32 v[46:47], v[46:47], v[26:27] op_sel_hi:[1,0]\n\t"
    "v_pk_mul_f32 v[48:49], v[48:49], v[26:27] op_sel_hi:[1,0]\n\t"
    "v_pk_mul_f32 v[50:51], v[50:51], v[26:27] op_sel_hi:[1,0]\n\t"
    "v_cvt_pk_bf16_f32 v36, v44, v45\n\t"
    "v_cvt_pk_bf16_f32 v37, v46, v47\n\t"
    "v_cvt_pk_bf16_f32 v38, v48, v49\n\t"
    "v_cvt_pk_bf16_f32 v39, v50, v51\n\t"
    "v_min_u32 v36, -2, v36\n\t"
    "v_min_u32 v37, -2, v37\n\t"
    "v_min_u32 v38, -2, v38\n\t"
    "v_min_u32 v39, -2, v39\n\t"
    "global_store_dwordx4 v15, v[36:39], %[od] sc1\n\t"
    "s_barrier\n\t"
    "s_mov_b32 exec_hi, 0\n\t"
    "global_store_dwordx4 v14, v[6:9], %[kv] sc1\n\t"
    "s_mov_b32 exec_hi, -1\n\t"
    "s_nop 0\n\t"
    "s_waitcnt lgkmcnt(0)\n\t"
    "s_branch 9f\n\t"
    "1:\n\t"
    "s_add_u32 %[sp], %[sp], 1\n\t"
    "s_cmp_ge_u32 %[sp], %[cap]\n\t"
    "s_cbranch_scc1 7f\n\t"
    "global_load_dwordx4 v[40:43], v6, %[ag] sc1\n\t"
    "global_load_dwordx4 v[44:47], v6, %[ag] offset:1024 sc1\n\t"
    "s_waitcnt lgkmcnt(0)\n\t"
    "s_memtime %[tc]\n\t"
    "s_nop 7\n\t"
    "s_branch 3b\n\t"
    "7:\n\t"
    "s_mov_b32 %[cap], 0\n\t"
    "s_branch 2b\n\t"
    "4:\n\t"
    "s_add_u32 %[sp], %[sp], 0x10000\n\t"
    "s_lshr_b32 %[st], %[sp], 16\n\t"
    "s_cmp_ge_u32 %[st], %[cap]\n\t"
    "s_cbranch_scc1 8f\n\t"
    "s_mov_b32 exec_hi, 0\n\t"
    "global_load_dwordx4 v[6:9], v14, %[vs] sc1\n\t"
    "s_mov_b32 exec_hi, -1\n\t"
    "s_branch 5b\n\t"
    "8:\n\t"
    "s_mov_b32 %[cap], 0\n\t"
    "s_branch 6b\n\t"
    "10:\n\t"
    "v_max_f32 v11, v11, v10\n\t"
    "v_mov_b32 v26, v11\n\t"
    "s_nop 1\n\t"
    "v_permlane16_swap_b32 v11, v26\n\t"
    "v_max_f32 v11, v11, v26\n\t"
    "v_sub_f32 v28, v40, v11\n\t"
    "v_sub_f32 v29, v41, v11\n\t"
    "v_sub_f32 v30, v42, v11\n\t"
    "v_sub_f32 v31, v43, v11\n\t"
    "v_sub_f32 v11, v10, v11\n\t"
    "v_exp_f32 v11, v11\n\t"
    "v_exp_f32 v28, v28\n\t"
    "v_exp_f32 v29, v29\n\t"
    "v_exp_f32 v30, v30\n\t"
    "v_exp_f32 v31, v31\n\t"
    "v_mul_f32 v44, v44, v11\n\t"
    "v_mul_f32 v45, v45, v11\n\t"
    "v_mul_f32 v46, v46, v11\n\t"
    "v_mul_f32 v47, v47, v11\n\t"
    "v_mul_f32 v48, v48, v11\n\t"
    "v_mul_f32 v49, v49, v11\n\t"
    "v_mul_f32 v50, v50, v11\n\t"
    "v_mul_f32 v51, v51, v11\n\t"
    "v_mul_f32 v52, v52, v11\n\t"
    "v_mul_f32 v53, v53, v11\n\t"
    "v_mul_f32 v54, v54, v11\n\t"
    "v_mul_f32 v55, v55, v11\n\t"
    "s_branch 11b\n\t"
    "12:\n\t"
    "v_max3_f32 v10, v24, v25, v26\n\t"
    "v_max3_f32 v40, v27, v28, v29\n\t"
    "v_max3_f32 v10, v10, v30, v31\n\t"
    "v_max3_f32 v10, v10, v40, %[neg]\n\t"
    "v_mov_b32 v40, v10\n\t"
    "s_nop 1\n\t"
    "v_permlane16_swap_b32 v10, v40\n\t"
    "v_max_f32 v10, v10, v40\n\t"
    "v_mov_b32 v40, v10\n\t"
    "s_nop 1\n\t"
    "v_permlane32_swap_b32 v10, v40\n\t"
    "v_max_f32 v10, v10, v40\n\t"
    "v_sub_f32 v24, v24, v10\n\t"
    "v_sub_f32 v25, v25, v10\n\t"
    "v_sub_f32 v26, v26, v10\n\t"
    "v_sub_f32 v27, v27, v10\n\t"
    "v_sub_f32 v28, v28, v10\n\t"
    "v_sub_f32 v29, v29, v10\n\t"
    "v_sub_f32 v30, v30, v10\n\t"
    "v_sub_f32 v31, v31, v10\n\t"
    "s_mov_b32 %[bg], 0x7fc00000\n\t"
    "s_branch 13b\n\t"
    "9:"
    : [tc] "=&s"(tc), [sp] "=&s"(sp), [cap] "+s"(cap), [st] "=&s"(st), [bg] "=&s"(bg)
    : [ag] "s"(ag), [c44] "s"(0x4444u), [kv] "s"(kv), [vs] "s"(vs), [od] "s"(od), [od2] "s"(od + 262144), [neg] "s"(-1.0e38f), [lsc] "s"(LSC), [one] "s"(0x3F803F80u), [big] "s"(24.0f), "{v[64:95]}"(W.w0), "{v[96:127]}"(W.w1), "{v[128:159]}"(W.w2), "{v[160:191]}"(W.w3), "{v[192:223]}"(W.w4), "{v[224:255]}"(W.w5)
    : "v6", "v7", "v8", "v9", "v10", "v11", "v12", "v13", "v14", "v15", "v16", "v17", "v18", "v19", "v20", "v21", "v22", "v23", "v24", "v25", "v26", "v27", "v28", "v29", "v30", "v31", "v32", "v33", "v34", "v35", "v36", "v37", "v38", "v39", "v40", "v41", "v42", "v43", "v44", "v45", "v46", "v47", "v48", "v49", "v50", "v51", "v52", "v53", "v54", "v55", "v56", "v57", "v58", "v59", "v60", "v61", "v62", "v63", "vcc", "scc", "memory");
}
template <> __device__ __forceinline__ void qk_step<4>(const QKW& W, const char* ag, const char* vs, const char* od, const char* kv, unsigned& cap, unsigned& sp,
                                                        unsigned long long& tc, unsigned long long* ts, unsigned& d0, unsigned& d1) {
  unsigned st, bg;
  asm volatile("v_mbcnt_lo_u32_b32 v52, -1, 0\n\t"
    "v_mbcnt_hi_u32_b32 v52, -1, v52\n\t"
    "v_lshlrev_b32 v6, 4, v52\n\t"
    "s_mov_b32 %[sp], 0\n\t"
    "global_load_dwordx4 v[36:39], v6, %[ag] sc1\n\t"
    "global_load_dwordx4 v[40:43], v6, %[ag] offset:1024 sc1\n\t"
    "s_memtime %[tc]\n\t"
    "v_add_u32 v7, 73728, v6\n\t"
    "v_add_u32 v8, 131072, v6\n\t"
    "ds_read_b128 v[20:23], v7 offset:8192\n\t"
    "ds_read_b128 v[24:27], v7 offset:9216\n\t"
    "ds_read_b128 v[28:31], v7 offset:0\n\t"
    "ds_read_b128 v[32:35], v7 offset:1024\n\t"
    "v_lshrrev_b32 v44, 4, v52\n\t"
    "v_and_b32 v45, 15, v52\n\t"
    "v_and_b32 v53, 4, v52\n\t"
    "v_mul_u32_u24 v53, 0x2aaa, v53\n\t"
    "v_lshrrev_b32 v9, 1, v52\n\t"
    "v_and_b32 v9, 2, v9\n\t"
    "v_or3_b32 v9, v9, v53, %[c44]\n\t"
    "v_lshlrev_b32 v53, 2, v44\n\t"
    "v_lshl_or_b32 v10, v45, 4, v53\n\t"
    "3:\n\t"
    "v_mov_b32 v12, 0\n\t"
    "v_mov_b32 v13, 0\n\t"
    "v_mov_b32 v14, 0\n\t"
    "v_mov_b32 v15, 0\n\t"
    "v_mov_b32 v16, 0\n\t"
    "v_mov_b32 v17, 0\n\t"
    "v_mov_b32 v18, 0\n\t"
    "v_mov_b32 v19, 0\n\t"
    "s_waitcnt vmcnt(0) lgkmcnt(0)\n\t"
    "14:\n\t"
    "ds_write_b128 v6, v[36:39] offset:8192\n\t"
    "ds_write_b128 v6, v[40:43] offset:9216\n\t"
    "v_smfmac_f32_16x16x64_bf16 v[12:15], v[36:39], v[128:135], v9\n\t"
    "v_max3_u32 v52, v36, v37, v38\n\t"
    "v_max3_u32 v53, v39, v40, v41\n\t"
    "v_smfmac_f32_16x16x64_bf16 v[16:19], v[36:39], v[136:143], v9\n\t"
    "v_max3_u32 v52, v52, v42, v43\n\t"
    "v_max_u32 v52, v52, v53\n\t"
    "v_cmp_eq_u32 vcc, -1, v52\n\t"
    "s_cbranch_vccnz 1f\n\t"
    "2:\n\t"
    "s_waitcnt lgkmcnt(0)\n\t"
    "s_barrier\n\t"
    "s_memrealtime s[36:37]\n\t"
    "ds_read_b128 v[36:39], v6 offset:0\n\t"
    "ds_read_b128 v[44:47], v6 offset:4096\n\t"
    "v_smfmac_f32_16x16x64_bf16 v[12:15], v[40:43], v[144:151], v9\n\t"
    "ds_read_b128 v[48:51], v6 offset:5120\n\t"
    "ds_read_b128 v[52:55], v6 offset:6144\n\t"
    "v_smfmac_f32_16x16x64_bf16 v[16:19], v[40:43], v[152:159], v9\n\t"
    "ds_read_b128 v[40:43], v6 offset:1024\n\t"
    "s_waitcnt lgkmcnt(0)\n\t"
    "s_mov_b32 s38, s36\n\t"
    "s_min_u32 s37, %[d0], %[d1]\n\t"
    "s_add_u32 s36, s36, s37\n\t"
    "s_and_b32 s36, s36, 0xffffff\n\t"
    "s_or_b32 s36, s36, %[sn]\n\t"
    "v_lshlrev_b32 v11, 10, v6\n\t"
    "v_mov_b32 v5, s36\n\t"
    "s_mov_b64 exec, 15\n\t"
    "global_store_dword v11, v5, %[vs] offset:-4096 sc1\n\t"
    "s_mov_b64 exec, -1\n\t"
    "s_sleep 0\n\t"
    "v_smfmac_f32_16x16x64_bf16 v[12:15], v[36:39], v[56:63], v9\n\t"
    "v_smfmac_f32_16x16x64_bf16 v[16:19], v[36:39], v[28:35], v9\n\t"
    "ds_read_b128 v[28:31], v7 offset:16384\n\t"
    "ds_read_b128 v[32:35], v7 offset:17408\n\t"
    "ds_read_b128 v[36:39], v6 offset:7168\n\t"
    "v_smfmac_f32_16x16x64_bf16 v[12:15], v[44:47], v[64:71], v9\n\t"
    "v_smfmac_f32_16x16x64_bf16 v[16:19], v[44:47], v[72:79], v9\n\t"
    "ds_read_b128 v[44:47], v6 offset:10240\n\t"
    "v_smfmac_f32_16x16x64_bf16 v[12:15], v[48:51], v[80:87], v9\n\t"
    "v_smfmac_f32_16x16x64_bf16 v[16:19], v[48:51], v[88:95], v9\n\t"
    "ds_read_b128 v[48:51], v6 offset:11264\n\t"
    "v_smfmac_f32_16x16x64_bf16 v[12:15], v[52:55], v[96:103], v9\n\t"
    "v_smfmac_f32_16x16x64_bf16 v[16:19], v[52:55], v[104:111], v9\n\t"
    "ds_read_b128 v[52:55], v6 offset:2048\n\t"
    "v_smfmac_f32_16x16x64_bf16 v[12:15], v[40:43], v[20:27], v9\n\t"
    "ds_read_b128 v[20:23], v8 offset:0\n\t"
    "ds_read_b128 v[24:27], v8 offset:1024\n\t"
    "s_waitcnt lgkmcnt(6)\n\t"
    "v_smfmac_f32_16x16x64_bf16 v[16:19], v[40:43], v[28:35], v9\n\t"
    "ds_read_b128 v[28:31], v8 offset:8192\n\t"
    "ds_read_b128 v[32:35], v8 offset:9216\n\t"
    "ds_read_b128 v[40:43], v6 offset:12288\n\t"
    "s_waitcnt lgkmcnt(8)\n\t"
    "v_smfmac_f32_16x16x64_bf16 v[12:15], v[36:39], v[112:119], v9\n\t"
    "v_smfmac_f32_16x16x64_bf16 v[16:19], v[36:39], v[120:127], v9\n\t"
    "ds_read_b128 v[36:39], v6 offset:13312\n\t"
    "s_waitcnt lgkmcnt(8)\n\t"
    "v_smfmac_f32_16x16x64_bf16 v[12:15], v[44:47], v[160:167], v9\n\t"
    "v_smfmac_f32_16x16x64_bf16 v[16:19], v[44:47], v[168:175], v9\n\t"
    "ds_read_b128 v[44:47], v6 offset:14336\n\t"
    "s_barrier\n\t"
    "s_setprio 2\n\t"
    "s_waitcnt lgkmcnt(8)\n\t"
    "v_smfmac_f32_16x16x64_bf16 v[12:15], v[48:51], v[176:183], v9\n\t"
    "v_smfmac_f32_16x16x64_bf16 v[16:19], v[48:51], v[184:191], v9\n\t"
    "ds_read_b128 v[48:51], v6 offset:3072\n\t"
    "s_waitcnt lgkmcnt(6)\n\t"
    "v_smfmac_f32_16x16x64_bf16 v[12:15], v[52:55], v[20:27], v9\n\t"
    "ds_read_b128 v[20:23], v8 offset:16384\n\t"
    "ds_read_b128 v[24:27], v8 offset:17408\n\t"
    "s_waitcnt lgkmcnt(6)\n\t"
    "v_smfmac_f32_16x16x64_bf16 v[16:19], v[52:55], v[28:35], v9\n\t"
    "ds_read_b128 v[28:31], v8 offset:24576\n\t"
    "ds_read_b128 v[32:35], v8 offset:25600\n\t"
    "ds_read_b128 v[52:55], v6 offset:15360\n\t"
    "s_waitcnt lgkmcnt(8)\n\t"
    "v_smfmac_f32_16x16x64_bf16 v[12:15], v[40:43], v[192:199], v9\n\t"
    "v_smfmac_f32_16x16x64_bf16 v[16:19], v[40:43], v[200:207], v9\n\t"
    "s_waitcnt lgkmcnt(7)\n\t"
    "v_smfmac_f32_16x16x64_bf16 v[12:15], v[36:39], v[208:215], v9\n\t"
    "v_smfmac_f32_16x16x64_bf16 v[16:19], v[36:39], v[216:223], v9\n\t"
    "s_waitcnt lgkmcnt(6)\n\t"
    "v_smfmac_f32_16x16x64_bf16 v[12:15], v[44:47], v[224:231], v9\n\t"
    "v_smfmac_f32_16x16x64_bf16 v[16:19], v[44:47], v[232:239], v9\n\t"
    "s_waitcnt lgkmcnt(3)\n\t"
    "v_smfmac_f32_16x16x64_bf16 v[12:15], v[48:51], v[20:27], v9\n\t"
    "s_waitcnt lgkmcnt(1)\n\t"
    "v_smfmac_f32_16x16x64_bf16 v[16:19], v[48:51], v[28:35], v9\n\t"
    "s_waitcnt lgkmcnt(0)\n\t"
    "v_smfmac_f32_16x16x64_bf16 v[12:15], v[52:55], v[240:247], v9\n\t"
    "v_smfmac_f32_16x16x64_bf16 v[16:19], v[52:55], v[248:255], v9\n\t"
    "s_nop 6\n\t"
    "v_permlane16_swap_b32 v12, v14\n\t"
    "v_permlane16_swap_b32 v13, v15\n\t"
    "v_permlane16_swap_b32 v16, v18\n\t"
    "v_permlane16_swap_b32 v17, v19\n\t"
    "v_add_f32 v12, v12, v14\n\t"
    "v_add_f32 v13, v13, v15\n\t"
    "v_add_f32 v16, v16, v18\n\t"
    "v_cvt_pk_bf16_f32 v12, v12, v13\n\t"
    "v_add_f32 v17, v17, v19\n\t"
    "ds_write_b32 v10, v12 offset:34816\n\t"
    "v_cvt_pk_bf16_f32 v16, v16, v17\n\t"
    "ds_write_b32 v10, v16 offset:35072\n\t"
    "s_waitcnt lgkmcnt(0)\n\t"
    "s_setprio 0\n\t"
    "s_barrier\n\t"
    "s_memrealtime s[36:37]\n\t"
    "s_waitcnt lgkmcnt(0)\n\t"
    "s_and_b32 s36, s36, 0xffffff\n\t"
    "s_or_b32 s36, s36, %[sn]\n\t"
    "v_lshlrev_b32 v7, 10, v6\n\t"
    "v_mov_b32 v8, s36\n\t"
    "s_mov_b64 exec, 15\n\t"
    "global_store_dword v7, v8, %[vs] offset:0 sc1\n\t"
    "s_mov_b64 exec, -1\n\t"
    "s_barrier\n\t"
    "s_memrealtime s[36:37]\n\t"
    "s_mov_b32 %[d1], %[d0]\n\t"
    "s_waitcnt lgkmcnt(0)\n\t"
    "s_sub_u32 %[d0], s36, s38\n\t"
    "s_min_u32 %[d0], %[d0], 200\n\t"
    "s_waitcnt lgkmcnt(0)\n\t"
    "s_branch 9f\n\t"
    "1:\n\t"
    "s_add_u32 %[sp], %[sp], 1\n\t"
    "s_cmp_ge_u32 %[sp], %[cap]\n\t"
    "s_cbranch_scc1 7f\n\t"
    "global_load_dwordx4 v[36:39], v6, %[ag] sc1\n\t"
    "global_load_dwordx4 v[40:43], v6, %[ag] offset:1024 sc1\n\t"
    "s_waitcnt lgkmcnt(0)\n\t"
    "s_memtime %[tc]\n\t"
    "s_nop 7\n\t"
    "s_branch 3b\n\t"
    "7:\n\t"
    "s_mov_b32 %[cap], 0\n\t"
    "s_branch 2b\n\t"
    "9:"
    : [tc] "=&s"(tc), [sp] "=&s"(sp), [cap] "+s"(cap), [d0] "+s"(d0), [d1] "+s"(d1)
    : [ag] "s"(ag), [c44] "s"(0x4444u), [vs] "s"(vs), [sn] "s"((unsigned)(size_t)od), "{v[64:95]}"(W.w0), "{v[96:127]}"(W.w1), "{v[128:159]}"(W.w2), "{v[160:191]}"(W.w3), "{v[192:223]}"(W.w4), "{v[224:255]}"(W.w5), "{v[56:63]}"(W.x)
    : "v5", "v6", "v7", "v8", "v9", "v10", "v11", "v12", "v13", "v14", "v15", "v16", "v17", "v18", "v19", "v20", "v21", "v22", "v23", "v24", "v25", "v26", "v27", "v28", "v29", "v30", "v31", "v32", "v33", "v34", "v35", "v36", "v37", "v38", "v39", "v40", "v41", "v42", "v43", "v44", "v45", "v46", "v47", "v48", "v49", "v50", "v51", "v52", "v53", "v54", "v55", "vcc", "scc", "memory", "s36", "s37", "s38");
}
template <> __device__ __forceinline__ void qk_step<5>(const QKW& W, const char* ag, const char* vs, const char* od, const char* kv, unsigned& cap, unsigned& sp,
                                                        unsigned long long& tc, unsigned long long* ts, unsigned& d0, unsigned& d1) {
  unsigned st, bg;
  asm volatile("v_mbcnt_lo_u32_b32 v52, -1, 0\n\t"
    "v_mbcnt_hi_u32_b32 v52, -1, v52\n\t"
    "v_lshlrev_b32 v6, 4, v52\n\t"
    "s_mov_b32 %[sp], 0\n\t"
    "global_load_dwordx4 v[36:39], v6, %[ag] sc1\n\t"
    "global_load_dwordx4 v[40:43], v6, %[ag] offset:1024 sc1\n\t"
    "s_memtime %[tc]\n\t"
    "v_add_u32 v7, 75776, v6\n\t"
    "v_add_u32 v8, 133120, v6\n\t"
    "ds_read_b128 v[20:23], v7 offset:8192\n\t"
    "ds_read_b128 v[24:27], v7 offset:9216\n\t"
    "ds_read_b128 v[28:31], v7 offset:0\n\t"
    "ds_read_b128 v[32:35], v7 offset:1024\n\t"
    "v_lshrrev_b32 v44, 4, v52\n\t"
    "v_and_b32 v45, 15, v52\n\t"
    "v_and_b32 v53, 4, v52\n\t"
    "v_mul_u32_u24 v53, 0x2aaa, v53\n\t"
    "v_lshrrev_b32 v9, 1, v52\n\t"
    "v_and_b32 v9, 2, v9\n\t"
    "v_or3_b32 v9, v9, v53, %[c44]\n\t"
    "v_lshlrev_b32 v53, 2, v44\n\t"
    "v_lshl_or_b32 v10, v45, 4, v53\n\t"
    "v_and_b32 v53, 1, v52\n\t"
    "v_lshlrev_b32 v53, 3, v53\n\t"
    "v_lshl_or_b32 v11, v44, 7, v53\n\t"
    "v_and_b32 v53, 12, v52\n\t"
    "v_lshl_or_b32 v11, v53, 2, v11\n\t"
    "v_and_b32 v53, 2, v52\n\t"
    "v_lshl_or_b32 v11, v53, 8, v11\n\t"
    "3:\n\t"
    "v_mov_b32 v12, 0\n\t"
    "v_mov_b32 v13, 0\n\t"
    "v_mov_b32 v14, 0\n\t"
    "v_mov_b32 v15, 0\n\t"
    "v_mov_b32 v16, 0\n\t"
    "v_mov_b32 v17, 0\n\t"
    "v_mov_b32 v18, 0\n\t"
    "v_mov_b32 v19, 0\n\t"
    "s_waitcnt vmcnt(0) lgkmcnt(0)\n\t"
    "14:\n\t"
    "ds_write_b128 v6, v[36:39] offset:10240\n\t"
    "ds_write_b128 v6, v[40:43] offset:11264\n\t"
    "v_smfmac_f32_16x16x64_bf16 v[12:15], v[36:39], v[160:167], v9\n\t"
    "v_max3_u32 v52, v36, v37, v38\n\t"
    "v_max3_u32 v53, v39, v40, v41\n\t"
    "v_smfmac_f32_16x16x64_bf16 v[16:19], v[36:39], v[168:175], v9\n\t"
    "v_max3_u32 v52, v52, v42, v43\n\t"
    "v_max_u32 v52, v52, v53\n\t"
    "v_cmp_eq_u32 vcc, -1, v52\n\t"
    "s_cbranch_vccnz 1f\n\t"
    "2:\n\t"
    "s_waitcnt lgkmcnt(0)\n\t"
    "s_barrier\n\t"
    "ds_read_b128 v[36:39], v6 offset:0\n\t"
    "ds_read_b128 v[44:47], v6 offset:4096\n\t"
    "v_smfmac_f32_16x16x64_bf16 v[12:15], v[40:43], v[176:183], v9\n\t"
    "ds_read_b128 v[48:51], v6 offset:5120\n\t"
    "ds_read_b128 v[52:55], v6 offset:6144\n\t"
    "v_smfmac_f32_16x16x64_bf16 v[16:19], v[40:43], v[184:191], v9\n\t"
    "ds_read_b128 v[40:43], v6 offset:1024\n\t"
    "s_sleep 0\n\t"
    "s_waitcnt lgkmcnt(4)\n\t"
    "v_smfmac_f32_16x16x64_bf16 v[12:15], v[36:39], v[56:63], v9\n\t"
    "v_smfmac_f32_16x16x64_bf16 v[16:19], v[36:39], v[28:35], v9\n\t"
    "ds_read_b128 v[28:31], v7 offset:16384\n\t"
    "ds_read_b128 v[32:35], v7 offset:17408\n\t"
    "ds_read_b128 v[36:39], v6 offset:7168\n\t"
    "s_waitcnt lgkmcnt(6)\n\t"
    "v_smfmac_f32_16x16x64_bf16 v[12:15], v[44:47], v[64:71], v9\n\t"
    "v_smfmac_f32_16x16x64_bf16 v[16:19], v[44:47], v[72:79], v9\n\t"
    "ds_read_b128 v[44:47], v6 offset:8192\n\t"
    "s_waitcnt lgkmcnt(6)\n\t"
    "v_smfmac_f32_16x16x64_bf16 v[12:15], v[48:51], v[80:87], v9\n\t"
    "v_smfmac_f32_16x16x64_bf16 v[16:19], v[48:51], v[88:95], v9\n\t"
    "ds_read_b128 v[48:51], v6 offset:9216\n\t"
    "s_waitcnt lgkmcnt(6)\n\t"
    "v_smfmac_f32_16x16x64_bf16 v[12:15], v[52:55], v[96:103], v9\n\t"
    "v_smfmac_f32_16x16x64_bf16 v[16:19], v[52:55], v[104:111], v9\n\t"
    "ds_read_b128 v[52:55], v6 offset:2048\n\t"
    "s_waitcnt lgkmcnt(6)\n\t"
    "v_smfmac_f32_16x16x64_bf16 v[12:15], v[40:43], v[20:27], v9\n\t"
    "ds_read_b128 v[20:23], v8 offset:0\n\t"
    "ds_read_b128 v[24:27], v8 offset:1024\n\t"
    "s_waitcnt lgkmcnt(6)\n\t"
    "v_smfmac_f32_16x16x64_bf16 v[16:19], v[40:43], v[28:35], v9\n\t"
    "ds_read_b128 v[28:31], v8 offset:8192\n\t"
    "ds_read_b128 v[32:35], v8 offset:9216\n\t"
    "ds_read_b128 v[40:43], v6 offset:12288\n\t"
    "s_waitcnt lgkmcnt(8)\n\t"
    "v_smfmac_f32_16x16x64_bf16 v[12:15], v[36:39], v[112:119], v9\n\t"
    "v_smfmac_f32_16x16x64_bf16 v[16:19], v[36:39], v[120:127], v9\n\t"
    "ds_read_b128 v[36:39], v6 offset:13312\n\t"
    "s_waitcnt lgkmcnt(8)\n\t"
    "v_smfmac_f32_16x16x64_bf16 v[12:15], v[44:47], v[128:135], v9\n\t"
    "v_smfmac_f32_16x16x64_bf16 v[16:19], v[44:47], v[136:143], v9\n\t"
    "ds_read_b128 v[44:47], v6 offset:14336\n\t"
    "s_barrier\n\t"
    "s_setprio 2\n\t"
    "s_waitcnt lgkmcnt(8)\n\t"
    "v_smfmac_f32_16x16x64_bf16 v[12:15], v[48:51], v[144:151], v9\n\t"
    "v_smfmac_f32_16x16x64_bf16 v[16:19], v[48:51], v[152:159], v9\n\t"
    "ds_read_b128 v[48:51], v6 offset:3072\n\t"
    "s_waitcnt lgkmcnt(6)\n\t"
    "v_smfmac_f32_16x16x64_bf16 v[12:15], v[52:55], v[20:27], v9\n\t"
    "ds_read_b128 v[20:23], v8 offset:16384\n\t"
    "ds_read_b128 v[24:27], v8 offset:17408\n\t"
    "s_waitcnt lgkmcnt(6)\n\t"
    "v_smfmac_f32_16x16x64_bf16 v[16:19], v[52:55], v[28:35], v9\n\t"
    "ds_read_b128 v[28:31], v8 offset:24576\n\t"
    "ds_read_b128 v[32:35], v8 offset:25600\n\t"
    "ds_read_b128 v[52:55], v6 offset:15360\n\t"
    "s_waitcnt lgkmcnt(8)\n\t"
    "v_smfmac_f32_16x16x64_bf16 v[12:15], v[40:43], v[192:199], v9\n\t"
    "v_smfmac_f32_16x16x64_bf16 v[16:19], v[40:43], v[200:207], v9\n\t"
    "s_waitcnt lgkmcnt(7)\n\t"
    "v_smfmac_f32_16x16x64_bf16 v[12:15], v[36:39], v[208:215], v9\n\t"
    "v_smfmac_f32_16x16x64_bf16 v[16:19], v[36:39], v[216:223], v9\n\t"
    "s_waitcnt lgkmcnt(6)\n\t"
    "v_smfmac_f32_16x16x64_bf16 v[12:15], v[44:47], v[224:231], v9\n\t"
    "v_smfmac_f32_16x16x64_bf16 v[16:19], v[44:47], v[232:239], v9\n\t"
    "s_waitcnt lgkmcnt(3)\n\t"
    "v_smfmac_f32_16x16x64_bf16 v[12:15], v[48:51], v[20:27], v9\n\t"
    "s_waitcnt lgkmcnt(1)\n\t"
    "v_smfmac_f32_16x16x64_bf16 v[16:19], v[48:51], v[28:35], v9\n\t"
    "s_waitcnt lgkmcnt(0)\n\t"
    "v_smfmac_f32_16x16x64_bf16 v[12:15], v[52:55], v[240:247], v9\n\t"
    "v_smfmac_f32_16x16x64_bf16 v[16:19], v[52:55], v[248:255], v9\n\t"
    "s_nop 6\n\t"
    "v_permlane16_swap_b32 v12, v14\n\t"
    "v_permlane16_swap_b32 v13, v15\n\t"
    "v_permlane16_swap_b32 v16, v18\n\t"
    "v_permlane16_swap_b32 v17, v19\n\t"
    "v_add_f32 v12, v12, v14\n\t"
    "v_add_f32 v13, v13, v15\n\t"
    "v_add_f32 v16, v16, v18\n\t"
    "v_cvt_pk_bf16_f32 v12, v12, v13\n\t"
    "v_add_f32 v17, v17, v19\n\t"
    "ds_write_b32 v10, v12 offset:35328\n\t"
    "v_cvt_pk_bf16_f32 v16, v16, v17\n\t"
    "ds_write_b32 v10, v16 offset:35584\n\t"
    "s_waitcnt lgkmcnt(0)\n\t"
    "s_setprio 0\n\t"
    "s_barrier\n\t"
    "ds_read_b64_tr_b16 v[36:37], v11 offset:34816\n\t"
    "ds_read_b64_tr_b16 v[38:39], v11 offset:34880\n\t"
    "ds_read_b64_tr_b16 v[40:41], v11 offset:35840\n\t"
    "ds_read_b64_tr_b16 v[42:43], v11 offset:35904\n\t"
    "s_waitcnt lgkmcnt(0)\n\t"
    "v_min_u32 v36, -2, v36\n\t"
    "v_min_u32 v37, -2, v37\n\t"
    "v_min_u32 v38, -2, v38\n\t"
    "v_min_u32 v39, -2, v39\n\t"
    "v_min_u32 v40, -2, v40\n\t"
    "v_min_u32 v41, -2, v41\n\t"
    "v_min_u32 v42, -2, v42\n\t"
    "v_min_u32 v43, -2, v43\n\t"
    "global_store_dwordx4 v6, v[36:39], %[vs] sc1\n\t"
    "global_store_dwordx4 v6, v[40:43], %[vs] offset:1024 sc1\n\t"
    "s_nop 1\n\t"
    "s_barrier\n\t"
    "s_waitcnt lgkmcnt(0)\n\t"
    "s_branch 9f\n\t"
    "1:\n\t"
    "s_add_u32 %[sp], %[sp], 1\n\t"
    "s_cmp_ge_u32 %[sp], %[cap]\n\t"
    "s_cbranch_scc1 7f\n\t"
    "global_load_dwordx4 v[36:39], v6, %[ag] sc1\n\t"
    "global_load_dwordx4 v[40:43], v6, %[ag] offset:1024 sc1\n\t"
    "s_waitcnt lgkmcnt(0)\n\t"
    "s_memtime %[tc]\n\t"
    "s_nop 7\n\t"
    "s_branch 3b\n\t"
    "7:\n\t"
    "s_mov_b32 %[cap], 0\n\t"
    "s_branch 2b\n\t"
    "9:"
    : [tc] "=&s"(tc), [sp] "=&s"(sp), [cap] "+s"(cap)
    : [ag] "s"(ag), [c44] "s"(0x4444u), [vs] "s"(vs), "{v[64:95]}"(W.w0), "{v[96:127]}"(W.w1), "{v[128:159]}"(W.w2), "{v[160:191]}"(W.w3), "{v[192:223]}"(W.w4), "{v[224:255]}"(W.w5), "{v[56:63]}"(W.x)
    : "v6", "v7", "v8", "v9", "v10", "v11", "v12", "v13", "v14", "v15", "v16", "v17", "v18", "v19", "v20", "v21", "v22", "v23", "v24", "v25", "v26", "v27", "v28", "v29", "v30", "v31", "v32", "v33", "v34", "v35", "v36", "v37", "v38", "v39", "v40", "v41", "v42", "v43", "v44", "v45", "v46", "v47", "v48", "v49", "v50", "v51", "v52", "v53", "v54", "v55", "vcc", "scc", "memory");
}
template <> __device__ __forceinline__ void qk_step<6>(const QKW& W, const char* ag, const char* vs, const char* od, const char* kv, unsigned& cap, unsigned& sp,
                                                        unsigned long long& tc, unsigned long long* ts, unsigned& d0, unsigned& d1) {
  unsigned st, bg;
  asm volatile("v_mbcnt_lo_u32_b32 v52, -1, 0\n\t"
    "v_mbcnt_hi_u32_b32 v52, -1, v52\n\t"
    "v_lshlrev_b32 v6, 4, v52\n\t"
    "s_mov_b32 %[sp], 0\n\t"
    "global_load_dwordx4 v[36:39], v6, %[ag] sc1\n\t"
    "global_load_dwordx4 v[40:43], v6, %[ag] offset:1024 sc1\n\t"
    "s_memtime %[tc]\n\t"
    "v_add_u32 v7, 77824, v6\n\t"
    "v_add_u32 v8, 135168, v6\n\t"
    "ds_read_b128 v[20:23], v7 offset:8192\n\t"
    "ds_read_b128 v[24:27], v7 offset:9216\n\t"
    "ds_read_b128 v[28:31], v7 offset:0\n\t"
    "ds_read_b128 v[32:35], v7 offset:1024\n\t"
    "v_lshrrev_b32 v44, 4, v52\n\t"
    "v_and_b32 v45, 15, v52\n\t"
    "v_and_b32 v53, 4, v52\n\t"
    "v_mul_u32_u24 v53, 0x2aaa, v53\n\t"
    "v_lshrrev_b32 v9, 1, v52\n\t"
    "v_and_b32 v9, 2, v9\n\t"
    "v_or3_b32 v9, v9, v53, %[c44]\n\t"
    "v_lshlrev_b32 v53, 2, v44\n\t"
    "v_lshl_or_b32 v10, v45, 4, v53\n\t"
    "3:\n\t"
    "v_mov_b32 v12, 0\n\t"
    "v_mov_b32 v13, 0\n\t"
    "v_mov_b32 v14, 0\n\t"
    "v_mov_b32 v15, 0\n\t"
    "v_mov_b32 v16, 0\n\t"
    "v_mov_b32 v17, 0\n\t"
    "v_mov_b32 v18, 0\n\t"
    "v_mov_b32 v19, 0\n\t"
    "s_waitcnt vmcnt(0) lgkmcnt(0)\n\t"
    "14:\n\t"
    "ds_write_b128 v6, v[36:39] offset:12288\n\t"
    "ds_write_b128 v6, v[40:43] offset:13312\n\t"
    "v_smfmac_f32_16x16x64_bf16 v[12:15], v[36:39], v[192:199], v9\n\t"
    "v_max3_u32 v52, v36, v37, v38\n\t"
    "v_max3_u32 v53, v39, v40, v41\n\t"
    "v_smfmac_f32_16x16x64_bf16 v[16:19], v[36:39], v[200:207], v9\n\t"
    "v_max3_u32 v52, v52, v42, v43\n\t"
    "v_max_u32 v52, v52, v53\n\t"
    "v_cmp_eq_u32 vcc, -1, v52\n\t"
    "s_cbranch_vccnz 1f\n\t"
    "2:\n\t"
    "s_waitcnt lgkmcnt(0)\n\t"
    "s_barrier\n\t"
    "ds_read_b128 v[36:39], v6 offset:0\n\t"
    "ds_read_b128 v[44:47], v6 offset:4096\n\t"
    "v_smfmac_f32_16x16x64_bf16 v[12:15], v[40:43], v[208:215], v9\n\t"
    "ds_read_b128 v[48:51], v6 offset:5120\n\t"
    "ds_read_b128 v[52:55], v6 offset:6144\n\t"
    "v_smfmac_f32_16x16x64_bf16 v[16:19], v[40:43], v[216:223], v9\n\t"
    "ds_read_b128 v[40:43], v6 offset:1024\n\t"
    "s_sleep 0\n\t"
    "s_waitcnt lgkmcnt(4)\n\t"
    "v_smfmac_f32_16x16x64_bf16 v[12:15], v[36:39], v[56:63], v9\n\t"
    "v_smfmac_f32_16x16x64_bf16 v[16:19], v[36:39], v[28:35], v9\n\t"
    "ds_read_b128 v[28:31], v7 offset:16384\n\t"
    "ds_read_b128 v[32:35], v7 offset:17408\n\t"
    "ds_read_b128 v[36:39], v6 offset:7168\n\t"
    "s_waitcnt lgkmcnt(6)\n\t"
    "v_smfmac_f32_16x16x64_bf16 v[12:15], v[44:47], v[64:71], v9\n\t"
    "v_smfmac_f32_16x16x64_bf16 v[16:19], v[44:47], v[72:79], v9\n\t"
    "ds_read_b128 v[44:47], v6 offset:8192\n\t"
    "s_waitcnt lgkmcnt(6)\n\t"
    "v_smfmac_f32_16x16x64_bf16 v[12:15], v[48:51], v[80:87], v9\n\t"
    "v_smfmac_f32_16x16x64_bf16 v[16:19], v[48:51], v[88:95], v9\n\t"
    "ds_read_b128 v[48:51], v6 offset:9216\n\t"
    "s_waitcnt lgkmcnt(6)\n\t"
    "v_smfmac_f32_16x16x64_bf16 v[12:15], v[52:55], v[96:103], v9\n\t"
    "v_smfmac_f32_16x16x64_bf16 v[16:19], v[52:55], v[104:111], v9\n\t"
    "ds_read_b128 v[52:55], v6 offset:2048\n\t"
    "s_waitcnt lgkmcnt(6)\n\t"
    "v_smfmac_f32_16x16x64_bf16 v[12:15], v[40:43], v[20:27], v9\n\t"
    "ds_read_b128 v[20:23], v8 offset:0\n\t"
    "ds_read_b128 v[24:27], v8 offset:1024\n\t"
    "s_waitcnt lgkmcnt(6)\n\t"
    "v_smfmac_f32_16x16x64_bf16 v[16:19], v[40:43], v[28:35], v9\n\t"
    "ds_read_b128 v[28:31], v8 offset:8192\n\t"
    "ds_read_b128 v[32:35], v8 offset:9216\n\t"
    "ds_read_b128 v[40:43], v6 offset:10240\n\t"
    "s_waitcnt lgkmcnt(8)\n\t"
    "v_smfmac_f32_16x16x64_bf16 v[12:15], v[36:39], v[112:119], v9\n\t"
    "v_smfmac_f32_16x16x64_bf16 v[16:19], v[36:39], v[120:127], v9\n\t"
    "ds_read_b128 v[36:39], v6 offset:11264\n\t"
    "s_waitcnt lgkmcnt(8)\n\t"
    "v_smfmac_f32_16x16x64_bf16 v[12:15], v[44:47], v[128:135], v9\n\t"
    "v_smfmac_f32_16x16x64_bf16 v[16:19], v[44:47], v[136:143], v9\n\t"
    "ds_read_b128 v[44:47], v6 offset:14336\n\t"
    "s_barrier\n\t"
    "s_setprio 2\n\t"
    "s_waitcnt lgkmcnt(8)\n\t"
    "v_smfmac_f32_16x16x64_bf16 v[12:15], v[48:51], v[144:151], v9\n\t"
    "v_smfmac_f32_16x16x64_bf16 v[16:19], v[48:51], v[152:159], v9\n\t"
    "ds_read_b128 v[48:51], v6 offset:3072\n\t"
    "s_waitcnt lgkmcnt(6)\n\t"
    "v_smfmac_f32_16x16x64_bf16 v[12:15], v[52:55], v[20:27], v9\n\t"
    "ds_read_b128 v[20:23], v8 offset:16384\n\t"
    "ds_read_b128 v[24:27], v8 offset:17408\n\t"
    "s_waitcnt lgkmcnt(6)\n\t"
    "v_smfmac_f32_16x16x64_bf16 v[16:19], v[52:55], v[28:35], v9\n\t"
    "ds_read_b128 v[28:31], v8 offset:24576\n\t"
    "ds_read_b128 v[32:35], v8 offset:25600\n\t"
    "ds_read_b128 v[52:55], v6 offset:15360\n\t"
    "s_waitcnt lgkmcnt(8)\n\t"
    "v_smfmac_f32_16x16x64_bf16 v[12:15], v[40:43], v[160:167], v9\n\t"
    "v_smfmac_f32_16x16x64_bf16 v[16:19], v[40:43], v[168:175], v9\n\t"
    "s_waitcnt lgkmcnt(7)\n\t"
    "v_smfmac_f32_16x16x64_bf16 v[12:15], v[36:39], v[176:183], v9\n\t"
    "v_smfmac_f32_16x16x64_bf16 v[16:19], v[36:39], v[184:191], v9\n\t"
    "s_waitcnt lgkmcnt(6)\n\t"
    "v_smfmac_f32_16x16x64_bf16 v[12:15], v[44:47], v[224:231], v9\n\t"
    "v_smfmac_f32_16x16x64_bf16 v[16:19], v[44:47], v[232:239], v9\n\t"
    "s_waitcnt lgkmcnt(3)\n\t"
    "v_smfmac_f32_16x16x64_bf16 v[12:15], v[48:51], v[20:27], v9\n\t"
    "s_waitcnt lgkmcnt(1)\n\t"
    "v_smfmac_f32_16x16x64_bf16 v[16:19], v[48:51], v[28:35], v9\n\t"
    "s_waitcnt lgkmcnt(0)\n\t"
    "v_smfmac_f32_16x16x64_bf16 v[12:15], v[52:55], v[240:247], v9\n\t"
    "v_smfmac_f32_16x16x64_bf16 v[16:19], v[52:55], v[248:255], v9\n\t"
    "s_nop 6\n\t"
    "v_permlane16_swap_b32 v12, v14\n\t"
    "v_permlane16_swap_b32 v13, v15\n\t"
    "v_permlane16_swap_b32 v16, v18\n\t"
    "v_permlane16_swap_b32 v17, v19\n\t"
    "v_add_f32 v12, v12, v14\n\t"
    "v_add_f32 v13, v13, v15\n\t"
    "v_add_f32 v16, v16, v18\n\t"
    "v_cvt_pk_bf16_f32 v12, v12, v13\n\t"
    "v_add_f32 v17, v17, v19\n\t"
    "ds_write_b32 v10, v12 offset:35840\n\t"
    "v_cvt_pk_bf16_f32 v16, v16, v17\n\t"
    "ds_write_b32 v10, v16 offset:36096\n\t"
    "s_waitcnt lgkmcnt(0)\n\t"
    "s_setprio 0\n\t"
    "s_barrier\n\t"
    "s_barrier\n\t"
    "s_waitcnt lgkmcnt(0)\n\t"
    "s_branch 9f\n\t"
    "1:\n\t"
    "s_add_u32 %[sp], %[sp], 1\n\t"
    "s_cmp_ge_u32 %[sp], %[cap]\n\t"
    "s_cbranch_scc1 7f\n\t"
    "global_load_dwordx4 v[36:39], v6, %[ag] sc1\n\t"
    "global_load_dwordx4 v[40:43], v6, %[ag] offset:1024 sc1\n\t"
    "s_waitcnt lgkmcnt(0)\n\t"
    "s_memtime %[tc]\n\t"
    "s_nop 7\n\t"
    "s_branch 3b\n\t"
    "7:\n\t"
    "s_mov_b32 %[cap], 0\n\t"
    "s_branch 2b\n\t"
    "9:"
    : [tc] "=&s"(tc), [sp] "=&s"(sp), [cap] "+s"(cap)
    : [ag] "s"(ag), [c44] "s"(0x4444u), "{v[64:95]}"(W.w0), "{v[96:127]}"(W.w1), "{v[128:159]}"(W.w2), "{v[160:191]}"(W.w3), "{v[192:223]}"(W.w4), "{v[224:255]}"(W.w5), "{v[56:63]}"(W.x)
    : "v6", "v7", "v8", "v9", "v10", "v11", "v12", "v13", "v14", "v15", "v16", "v17", "v18", "v19", "v20", "v21", "v22", "v23", "v24", "v25", "v26", "v27", "v28", "v29", "v30", "v31", "v32", "v33", "v34", "v35", "v36", "v37", "v38", "v39", "v40", "v41", "v42", "v43", "v44", "v45", "v46", "v47", "v48", "v49", "v50", "v51", "v52", "v53", "v54", "v55", "vcc", "scc", "memory");
}
template <> __device__ __forceinline__ void qk_step<7>(const QKW& W, const char* ag, const char* vs, const char* od, const char* kv, unsigned& cap, unsigned& sp,
                                                        unsigned long long& tc, unsigned long long* ts, unsigned& d0, unsigned& d1) {
  unsigned st, bg;
  asm volatile("v_mbcnt_lo_u32_b32 v52, -1, 0\n\t"
    "v_mbcnt_hi_u32_b32 v52, -1, v52\n\t"
    "v_lshlrev_b32 v6, 4, v52\n\t"
    "s_mov_b32 %[sp], 0\n\t"
    "global_load_dwordx4 v[36:39], v6, %[ag] sc1\n\t"
    "global_load_dwordx4 v[40:43], v6, %[ag] offset:1024 sc1\n\t"
    "s_memtime %[tc]\n\t"
    "v_add_u32 v7, 79872, v6\n\t"
    "v_add_u32 v8, 137216, v6\n\t"
    "ds_read_b128 v[20:23], v7 offset:8192\n\t"
    "ds_read_b128 v[24:27], v7 offset:9216\n\t"
    "ds_read_b128 v[28:31], v7 offset:0\n\t"
    "ds_read_b128 v[32:35], v7 offset:1024\n\t"
    "v_lshrrev_b32 v44, 4, v52\n\t"
    "v_and_b32 v45, 15, v52\n\t"
    "v_and_b32 v53, 4, v52\n\t"
    "v_mul_u32_u24 v53, 0x2aaa, v53\n\t"
    "v_lshrrev_b32 v9, 1, v52\n\t"
    "v_and_b32 v9, 2, v9\n\t"
    "v_or3_b32 v9, v9, v53, %[c44]\n\t"
    "v_lshlrev_b32 v53, 2, v44\n\t"
    "v_lshl_or_b32 v10, v45, 4, v53\n\t"
    "3:\n\t"
    "v_mov_b32 v12, 0\n\t"
    "v_mov_b32 v13, 0\n\t"
    "v_mov_b32 v14, 0\n\t"
    "v_mov_b32 v15, 0\n\t"
    "v_mov_b32 v16, 0\n\t"
    "v_mov_b32 v17, 0\n\t"
    "v_mov_b32 v18, 0\n\t"
    "v_mov_b32 v19, 0\n\t"
    "s_waitcnt vmcnt(0) lgkmcnt(0)\n\t"
    "14:\n\t"
    "ds_write_b128 v6, v[36:39] offset:14336\n\t"
    "ds_write_b128 v6, v[40:43] offset:15360\n\t"
    "v_smfmac_f32_16x16x64_bf16 v[12:15], v[36:39], v[224:231], v9\n\t"
    "v_max3_u32 v52, v36, v37, v38\n\t"
    "v_max3_u32 v53, v39, v40, v41\n\t"
    "v_smfmac_f32_16x16x64_bf16 v[16:19], v[36:39], v[232:239], v9\n\t"
    "v_max3_u32 v52, v52, v42, v43\n\t"
    "v_max_u32 v52, v52, v53\n\t"
    "v_cmp_eq_u32 vcc, -1, v52\n\t"
    "s_cbranch_vccnz 1f\n\t"
    "2:\n\t"
    "s_waitcnt lgkmcnt(0)\n\t"
    "s_barrier\n\t"
    "ds_read_b128 v[36:39], v6 offset:0\n\t"
    "ds_read_b128 v[44:47], v6 offset:4096\n\t"
    "v_smfmac_f32_16x16x64_bf16 v[12:15], v[40:43], v[240:247], v9\n\t"
    "ds_read_b128 v[48:51], v6 offset:5120\n\t"
    "ds_read_b128 v[52:55], v6 offset:6144\n\t"
    "v_smfmac_f32_16x16x64_bf16 v[16:19], v[40:43], v[248:255], v9\n\t"
    "ds_read_b128 v[40:43], v6 offset:1024\n\t"
    "s_sleep 0\n\t"
    "s_waitcnt lgkmcnt(4)\n\t"
    "v_smfmac_f32_16x16x64_bf16 v[12:15], v[36:39], v[56:63], v9\n\t"
    "v_smfmac_f32_16x16x64_bf16 v[16:19], v[36:39], v[28:35], v9\n\t"
    "ds_read_b128 v[28:31], v7 offset:16384\n\t"
    "ds_read_b128 v[32:35], v7 offset:17408\n\t"
    "ds_read_b128 v[36:39], v6 offset:7168\n\t"
    "s_waitcnt lgkmcnt(6)\n\t"
    "v_smfmac_f32_16x16x64_bf16 v[12:15], v[44:47], v[64:71], v9\n\t"
    "v_smfmac_f32_16x16x64_bf16 v[16:19], v[44:47], v[72:79], v9\n\t"
    "ds_read_b128 v[44:47], v6 offset:8192\n\t"
    "s_waitcnt lgkmcnt(6)\n\t"
    "v_smfmac_f32_16x16x64_bf16 v[12:15], v[48:51], v[80:87], v9\n\t"
    "v_smfmac_f32_16x16x64_bf16 v[16:19], v[48:51], v[88:95], v9\n\t"
    "ds_read_b128 v[48:51], v6 offset:9216\n\t"
    "s_waitcnt lgkmcnt(6)\n\t"
    "v_smfmac_f32_16x16x64_bf16 v[12:15], v[52:55], v[96:103], v9\n\t"
    "v_smfmac_f32_16x16x64_bf16 v[16:19], v[52:55], v[104:111], v9\n\t"
    "ds_read_b128 v[52:55], v6 offset:2048\n\t"
    "s_waitcnt lgkmcnt(6)\n\t"
    "v_smfmac_f32_16x16x64_bf16 v[12:15], v[40:43], v[20:27], v9\n\t"
    "ds_read_b128 v[20:23], v8 offset:0\n\t"
    "ds_read_b128 v[24:27], v8 offset:1024\n\t"
    "s_waitcnt lgkmcnt(6)\n\t"
    "v_smfmac_f32_16x16x64_bf16 v[16:19], v[40:43], v[28:35], v9\n\t"
    "ds_read_b128 v[28:31], v8 offset:8192\n\t"
    "ds_read_b128 v[32:35], v8 offset:9216\n\t"
    "ds_read_b128 v[40:43], v6 offset:10240\n\t"
    "s_waitcnt lgkmcnt(8)\n\t"
    "v_smfmac_f32_16x16x64_bf16 v[12:15], v[36:39], v[112:119], v9\n\t"
    "v_smfmac_f32_16x16x64_bf16 v[16:19], v[36:39], v[120:127], v9\n\t"
    "ds_read_b128 v[36:39], v6 offset:11264\n\t"
    "s_waitcnt lgkmcnt(8)\n\t"
    "v_smfmac_f32_16x16x64_bf16 v[12:15], v[44:47], v[128:135], v9\n\t"
    "v_smfmac_f32_16x16x64_bf16 v[16:19], v[44:47], v[136:143], v9\n\t"
    "ds_read_b128 v[44:47], v6 offset:12288\n\t"
    "s_barrier\n\t"
    "s_setprio 2\n\t"
    "s_waitcnt lgkmcnt(8)\n\t"
    "v_smfmac_f32_16x16x64_bf16 v[12:15], v[48:51], v[144:151], v9\n\t"
    "v_smfmac_f32_16x16x64_bf16 v[16:19], v[48:51], v[152:159], v9\n\t"
    "ds_read_b128 v[48:51], v6 offset:3072\n\t"
    "s_waitcnt lgkmcnt(6)\n\t"
    "v_smfmac_f32_16x16x64_bf16 v[12:15], v[52:55], v[20:27], v9\n\t"
    "ds_read_b128 v[20:23], v8 offset:16384\n\t"
    "ds_read_b128 v[24:27], v8 offset:17408\n\t"
    "s_waitcnt lgkmcnt(6)\n\t"
    "v_smfmac_f32_16x16x64_bf16 v[16:19], v[52:55], v[28:35], v9\n\t"
    "ds_read_b128 v[28:31], v8 offset:24576\n\t"
    "ds_read_b128 v[32:35], v8 offset:25600\n\t"
    "ds_read_b128 v[52:55], v6 offset:13312\n\t"
    "s_waitcnt lgkmcnt(8)\n\t"
    "v_smfmac_f32_16x16x64_bf16 v[12:15], v[40:43], v[160:167], v9\n\t"
    "v_smfmac_f32_16x16x64_bf16 v[16:19], v[40:43], v[168:175], v9\n\t"
    "s_waitcnt lgkmcnt(7)\n\t"
    "v_smfmac_f32_16x16x64_bf16 v[12:15], v[36:39], v[176:183], v9\n\t"
    "v_smfmac_f32_16x16x64_bf16 v[16:19], v[36:39], v[184:191], v9\n\t"
    "s_waitcnt lgkmcnt(6)\n\t"
    "v_smfmac_f32_16x16x64_bf16 v[12:15], v[44:47], v[192:199], v9\n\t"
    "v_smfmac_f32_16x16x64_bf16 v[16:19], v[44:47], v[200:207], v9\n\t"
    "s_waitcnt lgkmcnt(3)\n\t"
    "v_smfmac_f32_16x16x64_bf16 v[12:15], v[48:51], v[20:27], v9\n\t"
    "s_waitcnt lgkmcnt(1)\n\t"
    "v_smfmac_f32_16x16x64_bf16 v[16:19], v[48:51], v[28:35], v9\n\t"
    "s_waitcnt lgkmcnt(0)\n\t"
    "v_smfmac_f32_16x16x64_bf16 v[12:15], v[52:55], v[208:215], v9\n\t"
    "v_smfmac_f32_16x16x64_bf16 v[16:19], v[52:55], v[216:223], v9\n\t"
    "s_nop 6\n\t"
    "v_permlane16_swap_b32 v12, v14\n\t"
    "v_permlane16_swap_b32 v13, v15\n\t"
    "v_permlane16_swap_b32 v16, v18\n\t"
    "v_permlane16_swap_b32 v17, v19\n\t"
    "v_add_f32 v12, v12, v14\n\t"
    "v_add_f32 v13, v13, v15\n\t"
    "v_add_f32 v16, v16, v18\n\t"
    "v_cvt_pk_bf16_f32 v12, v12, v13\n\t"
    "v_add_f32 v17, v17, v19\n\t"
    "ds_write_b32 v10, v12 offset:36352\n\t"
    "v_cvt_pk_bf16_f32 v16, v16, v17\n\t"
    "ds_write_b32 v10, v16 offset:36608\n\t"
    "s_waitcnt lgkmcnt(0)\n\t"
    "s_setprio 0\n\t"
    "s_barrier\n\t"
    "s_barrier\n\t"
    "s_waitcnt lgkmcnt(0)\n\t"
    "s_branch 9f\n\t"
    "1:\n\t"
    "s_add_u32 %[sp], %[sp], 1\n\t"
    "s_cmp_ge_u32 %[sp], %[cap]\n\t"
    "s_cbranch_scc1 7f\n\t"
    "global_load_dwordx4 v[36:39], v6, %[ag] sc1\n\t"
    "global_load_dwordx4 v[40:43], v6, %[ag] offset:1024 sc1\n\t"
    "s_waitcnt lgkmcnt(0)\n\t"
    "s_memtime %[tc]\n\t"
    "s_nop 7\n\t"
    "s_branch 3b\n\t"
    "7:\n\t"
    "s_mov_b32 %[cap], 0\n\t"
    "s_branch 2b\n\t"
    "9:"
    : [tc] "=&s"(tc), [sp] "=&s"(sp), [cap] "+s"(cap)
    : [ag] "s"(ag), [c44] "s"(0x4444u), "{v[64:95]}"(W.w0), "{v[96:127]}"(W.w1), "{v[128:159]}"(W.w2), "{v[160:191]}"(W.w3), "{v[192:223]}"(W.w4), "{v[224:255]}"(W.w5), "{v[56:63]}"(W.x)
    : "v6", "v7", "v8", "v9", "v10", "v11", "v12", "v13", "v14", "v15", "v16", "v17", "v18", "v19", "v20", "v21", "v22", "v23", "v24", "v25", "v26", "v27", "v28", "v29", "v30", "v31", "v32", "v33", "v34", "v35", "v36", "v37", "v38", "v39", "v40", "v41", "v42", "v43", "v44", "v45", "v46", "v47", "v48", "v49", "v50", "v51", "v52", "v53", "v54", "v55", "vcc", "scc", "memory");
}
template <int I> __device__ __forceinline__ void qk_wload(u32x32& w, unsigned a0, unsigned a1);
template <> __device__ __forceinline__ void qk_wload<0>(u32x32& w, unsigned a0, unsigned a1) {
  asm volatile("ds_read_b64_tr_b16 v[64:65], %1 offset:0\n\t"
    "ds_read_b64_tr_b16 v[66:67], %1 offset:512\n\t"
    "ds_read_b64_tr_b16 v[68:69], %1 offset:4096\n\t"
    "ds_read_b64_tr_b16 v[70:71], %1 offset:4608\n\t"
    "ds_read_b64_tr_b16 v[72:73], %2 offset:0\n\t"
    "ds_read_b64_tr_b16 v[74:75], %2 offset:512\n\t"
    "ds_read_b64_tr_b16 v[76:77], %2 offset:4096\n\t"
    "ds_read_b64_tr_b16 v[78:79], %2 offset:4608\n\t"
    "s_waitcnt lgkmcnt(0)\n\t"
    "ds_read_b64_tr_b16 v[80:81], %1 offset:16384\n\t"
    "ds_read_b64_tr_b16 v[82:83], %1 offset:16896\n\t"
    "ds_read_b64_tr_b16 v[84:85], %1 offset:20480\n\t"
    "ds_read_b64_tr_b16 v[86:87], %1 offset:20992\n\t"
    "ds_read_b64_tr_b16 v[88:89], %2 offset:16384\n\t"
    "ds_read_b64_tr_b16 v[90:91], %2 offset:16896\n\t"
    "ds_read_b64_tr_b16 v[92:93], %2 offset:20480\n\t"
    "ds_read_b64_tr_b16 v[94:95], %2 offset:20992\n\t"
    "s_waitcnt lgkmcnt(0)"
    : "=&{v[64:95]}"(w) : "v"(a0), "v"(a1) : "memory");
}
template <> __device__ __forceinline__ void qk_wload<1>(u32x32& w, unsigned a0, unsigned a1) {
  asm volatile("ds_read_b64_tr_b16 v[96:97], %1 offset:0\n\t"
    "ds_read_b64_tr_b16 v[98:99], %1 offset:512\n\t"
    "ds_read_b64_tr_b16 v[100:101], %1 offset:4096\n\t"
    "ds_read_b64_tr_b16 v[102:103], %1 offset:4608\n\t"
    "ds_read_b64_tr_b16 v[104:105], %2 offset:0\n\t"
    "ds_read_b64_tr_b16 v[106:107], %2 offset:512\n\t"
    "ds_read_b64_tr_b16 v[108:109], %2 offset:4096\n\t"
    "ds_read_b64_tr_b16 v[110:111], %2 offset:4608\n\t"
    "s_waitcnt lgkmcnt(0)\n\t"
    "ds_read_b64_tr_b16 v[112:113], %1 offset:16384\n\t"
    "ds_read_b64_tr_b16 v[114:115], %1 offset:16896\n\t"
    "ds_read_b64_tr_b16 v[116:117], %1 offset:20480\n\t"
    "ds_read_b64_tr_b16 v[118:119], %1 offset:20992\n\t"
    "ds_read_b64_tr_b16 v[120:121], %2 offset:16384\n\t"
    "ds_read_b64_tr_b16 v[122:123], %2 offset:16896\n\t"
    "ds_read_b64_tr_b16 v[124:125], %2 offset:20480\n\t"
    "ds_read_b64_tr_b16 v[126:127], %2 offset:20992\n\t"
    "s_waitcnt lgkmcnt(0)"
    : "=&{v[96:127]}"(w) : "v"(a0), "v"(a1) : "memory");
}
template <> __device__ __forceinline__ void qk_wload<2>(u32x32& w, unsigned a0, unsigned a1) {
  asm volatile("ds_read_b64_tr_b16 v[128:129], %1 offset:0\n\t"
    "ds_read_b64_tr_b16 v[130:131], %1 offset:512\n\t"
    "ds_read_b64_tr_b16 v[132:133], %1 offset:4096\n\t"
    "ds_read_b64_tr_b16 v[134:135], %1 offset:4608\n\t"
    "ds_read_b64_tr_b16 v[136:137], %2 offset:0\n\t"
    "ds_read_b64_tr_b16 v[138:139], %2 offset:512\n\t"
    "ds_read_b64_tr_b16 v[140:141], %2 offset:4096\n\t"
    "ds_read_b64_tr_b16 v[142:143], %2 offset:4608\n\t"
    "s_waitcnt lgkmcnt(0)\n\t"
    "ds_read_b64_tr_b16 v[144:145], %1 offset:16384\n\t"
    "ds_read_b64_tr_b16 v[146:147], %1 offset:16896\n\t"
    "ds_read_b64_tr_b16 v[148:149], %1 offset:20480\n\t"
    "ds_read_b64_tr_b16 v[150:151], %1 offset:20992\n\t"
    "ds_read_b64_tr_b16 v[152:153], %2 offset:16384\n\t"
    "ds_read_b64_tr_b16 v[154:155], %2 offset:16896\n\t"
    "ds_read_b64_tr_b16 v[156:157], %2 offset:20480\n\t"
    "ds_read_b64_tr_b16 v[158:159], %2 offset:20992\n\t"
    "s_waitcnt lgkmcnt(0)"
    : "=&{v[128:159]}"(w) : "v"(a0), "v"(a1) : "memory");
}
template <> __device__ __forceinline__ void qk_wload<3>(u32x32& w, unsigned a0, unsigned a1) {
  asm volatile("ds_read_b64_tr_b16 v[160:161], %1 offset:0\n\t"
    "ds_read_b64_tr_b16 v[162:163], %1 offset:512\n\t"
    "ds_read_b64_tr_b16 v[164:165], %1 offset:4096\n\t"
    "ds_read_b64_tr_b16 v[166:167], %1 offset:4608\n\t"
    "ds_read_b64_tr_b16 v[168:169], %2 offset:0\n\t"
    "ds_read_b64_tr_b16 v[170:171], %2 offset:512\n\t"
    "ds_read_b64_tr_b16 v[172:173], %2 offset:4096\n\t"
    "ds_read_b64_tr_b16 v[174:175], %2 offset:4608\n\t"
    "s_waitcnt lgkmcnt(0)\n\t"
    "ds_read_b64_tr_b16 v[176:177], %1 offset:16384\n\t"
    "ds_read_b64_tr_b16 v[178:179], %1 offset:16896\n\t"
    "ds_read_b64_tr_b16 v[180:181], %1 offset:20480\n\t"
    "ds_read_b64_tr_b16 v[182:183], %1 offset:20992\n\t"
    "ds_read_b64_tr_b16 v[184:185], %2 offset:16384\n\t"
    "ds_read_b64_tr_b16 v[186:187], %2 offset:16896\n\t"
    "ds_read_b64_tr_b16 v[188:189], %2 offset:20480\n\t"
    "ds_read_b64_tr_b16 v[190:191], %2 offset:20992\n\t"
    "s_waitcnt lgkmcnt(0)"
    : "=&{v[160:191]}"(w) : "v"(a0), "v"(a1) : "memory");
}
template <> __device__ __forceinline__ void qk_wload<4>(u32x32& w, unsigned a0, unsigned a1) {
  asm volatile("ds_read_b64_tr_b16 v[192:193], %1 offset:0\n\t"
    "ds_read_b64_tr_b16 v[194:195], %1 offset:512\n\t"
    "ds_read_b64_tr_b16 v[196:197], %1 offset:4096\n\t"
    "ds_read_b64_tr_b16 v[198:199], %1 offset:4608\n\t"
    "ds_read_b64_tr_b16 v[200:201], %2 offset:0\n\t"
    "ds_read_b64_tr_b16 v[202:203], %2 offset:512\n\t"
    "ds_read_b64_tr_b16 v[204:205], %2 offset:4096\n\t"
    "ds_read_b64_tr_b16 v[206:207], %2 offset:4608\n\t"
    "s_waitcnt lgkmcnt(0)\n\t"
    "ds_read_b64_tr_b16 v[208:209], %1 offset:16384\n\t"
    "ds_read_b64_tr_b16 v[210:211], %1 offset:16896\n\t"
    "ds_read_b64_tr_b16 v[212:213], %1 offset:20480\n\t"
    "ds_read_b64_tr_b16 v[214:215], %1 offset:20992\n\t"
    "ds_read_b64_tr_b16 v[216:217], %2 offset:16384\n\t"
    "ds_read_b64_tr_b16 v[218:219], %2 offset:16896\n\t"
    "ds_read_b64_tr_b16 v[220:221], %2 offset:20480\n\t"
    "ds_read_b64_tr_b16 v[222:223], %2 offset:20992\n\t"
    "s_waitcnt lgkmcnt(0)"
    : "=&{v[192:223]}"(w) : "v"(a0), "v"(a1) : "memory");
}
template <> __device__ __forceinline__ void qk_wload<5>(u32x32& w, unsigned a0, unsigned a1) {
  asm volatile("ds_read_b64_tr_b16 v[224:225], %1 offset:0\n\t"
    "ds_read_b64_tr_b16 v[226:227], %1 offset:512\n\t"
    "ds_read_b64_tr_b16 v[228:229], %1 offset:4096\n\t"
    "ds_read_b64_tr_b16 v[230:231], %1 offset:4608\n\t"
    "ds_read_b64_tr_b16 v[232:233], %2 offset:0\n\t"
    "ds_read_b64_tr_b16 v[234:235], %2 offset:512\n\t"
    "ds_read_b64_tr_b16 v[236:237], %2 offset:4096\n\t"
    "ds_read_b64_tr_b16 v[238:239], %2 offset:4608\n\t"
    "s_waitcnt lgkmcnt(0)\n\t"
    "ds_read_b64_tr_b16 v[240:241], %1 offset:16384\n\t"
    "ds_read_b64_tr_b16 v[242:243], %1 offset:16896\n\t"
    "ds_read_b64_tr_b16 v[244:245], %1 offset:20480\n\t"
    "ds_read_b64_tr_b16 v[246:247], %1 offset:20992\n\t"
    "ds_read_b64_tr_b16 v[248:249], %2 offset:16384\n\t"
    "ds_read_b64_tr_b16 v[250:251], %2 offset:16896\n\t"
    "ds_read_b64_tr_b16 v[252:253], %2 offset:20480\n\t"
    "ds_read_b64_tr_b16 v[254:255], %2 offset:20992\n\t"
    "s_waitcnt lgkmcnt(0)"
    : "=&{v[224:255]}"(w) : "v"(a0), "v"(a1) : "memory");
}
#define QK_OWN 0
template <int I> __device__ __forceinline__ void qk_swap(u32x32& w, unsigned a);
template <> __device__ __forceinline__ void qk_swap<0>(u32x32& w, unsigned a) {
  asm volatile("ds_read_b128 v[8:11], %1 offset:0\n\t"
    "ds_read_b128 v[12:15], %1 offset:1024\n\t"
    "ds_read_b128 v[16:19], %1 offset:8192\n\t"
    "ds_read_b128 v[20:23], %1 offset:9216\n\t"
    "ds_read_b128 v[24:27], %1 offset:16384\n\t"
    "ds_read_b128 v[28:31], %1 offset:17408\n\t"
    "ds_read_b128 v[32:35], %1 offset:24576\n\t"
    "ds_read_b128 v[36:39], %1 offset:25600\n\t"
    "ds_write_b128 %1, v[64:67] offset:0\n\t"
    "ds_write_b128 %1, v[68:71] offset:1024\n\t"
    "ds_write_b128 %1, v[72:75] offset:8192\n\t"
    "ds_write_b128 %1, v[76:79] offset:9216\n\t"
    "ds_write_b128 %1, v[80:83] offset:16384\n\t"
    "ds_write_b128 %1, v[84:87] offset:17408\n\t"
    "ds_write_b128 %1, v[88:91] offset:24576\n\t"
    "ds_write_b128 %1, v[92:95] offset:25600\n\t"
    "s_waitcnt lgkmcnt(0)\n\t"
    "v_mov_b32 v64, v8\n\t"
    "v_mov_b32 v65, v9\n\t"
    "v_mov_b32 v66, v10\n\t"
    "v_mov_b32 v67, v11\n\t"
    "v_mov_b32 v68, v12\n\t"
    "v_mov_b32 v69, v13\n\t"
    "v_mov_b32 v70, v14\n\t"
    "v_mov_b32 v71, v15\n\t"
    "v_mov_b32 v72, v16\n\t"
    "v_mov_b32 v73, v17\n\t"
    "v_mov_b32 v74, v18\n\t"
    "v_mov_b32 v75, v19\n\t"
    "v_mov_b32 v76, v20\n\t"
    "v_mov_b32 v77, v21\n\t"
    "v_mov_b32 v78, v22\n\t"
    "v_mov_b32 v79, v23\n\t"
    "v_mov_b32 v80, v24\n\t"
    "v_mov_b32 v81, v25\n\t"
    "v_mov_b32 v82, v26\n\t"
    "v_mov_b32 v83, v27\n\t"
    "v_mov_b32 v84, v28\n\t"
    "v_mov_b32 v85, v29\n\t"
    "v_mov_b32 v86, v30\n\t"
    "v_mov_b32 v87, v31\n\t"
    "v_mov_b32 v88, v32\n\t"
    "v_mov_b32 v89, v33\n\t"
    "v_mov_b32 v90, v34\n\t"
    "v_mov_b32 v91, v35\n\t"
    "v_mov_b32 v92, v36\n\t"
    "v_mov_b32 v93, v37\n\t"
    "v_mov_b32 v94, v38\n\t"
    "v_mov_b32 v95, v39"
    :: "{v[64:95]}"(w), "v"(a) : "v8", "v9", "v10", "v11", "v12", "v13", "v14", "v15", "v16", "v17", "v18", "v19", "v20", "v21", "v22", "v23", "v24", "v25", "v26", "v27", "v28", "v29", "v30", "v31", "v32", "v33", "v34", "v35", "v36", "v37", "v38", "v39", "memory");
}
template <> __device__ __forceinline__ void qk_swap<1>(u32x32& w, unsigned a) {
  asm volatile("ds_read_b128 v[8:11], %1 offset:0\n\t"
    "ds_read_b128 v[12:15], %1 offset:1024\n\t"
    "ds_read_b128 v[16:19], %1 offset:8192\n\t"
    "ds_read_b128 v[20:23], %1 offset:9216\n\t"
    "ds_read_b128 v[24:27], %1 offset:16384\n\t"
    "ds_read_b128 v[28:31], %1 offset:17408\n\t"
    "ds_read_b128 v[32:35], %1 offset:24576\n\t"
    "ds_read_b128 v[36:39], %1 offset:25600\n\t"
    "ds_write_b128 %1, v[96:99] offset:0\n\t"
    "ds_write_b128 %1, v[100:103] offset:1024\n\t"
    "ds_write_b128 %1, v[104:107] offset:8192\n\t"
    "ds_write_b128 %1, v[108:111] offset:9216\n\t"
    "ds_write_b128 %1, v[112:115] offset:16384\n\t"
    "ds_write_b128 %1, v[116:119] offset:17408\n\t"
    "ds_write_b128 %1, v[120:123] offset:24576\n\t"
    "ds_write_b128 %1, v[124:127] offset:25600\n\t"
    "s_waitcnt lgkmcnt(0)\n\t"
    "v_mov_b32 v96, v8\n\t"
    "v_mov_b32 v97, v9\n\t"
    "v_mov_b32 v98, v10\n\t"
    "v_mov_b32 v99, v11\n\t"
    "v_mov_b32 v100, v12\n\t"
    "v_mov_b32 v101, v13\n\t"
    "v_mov_b32 v102, v14\n\t"
    "v_mov_b32 v103, v15\n\t"
    "v_mov_b32 v104, v16\n\t"
    "v_mov_b32 v105, v17\n\t"
    "v_mov_b32 v106, v18\n\t"
    "v_mov_b32 v107, v19\n\t"
    "v_mov_b32 v108, v20\n\t"
    "v_mov_b32 v109, v21\n\t"
    "v_mov_b32 v110, v22\n\t"
    "v_mov_b32 v111, v23\n\t"
    "v_mov_b32 v112, v24\n\t"
    "v_mov_b32 v113, v25\n\t"
    "v_mov_b32 v114, v26\n\t"
    "v_mov_b32 v115, v27\n\t"
    "v_mov_b32 v116, v28\n\t"
    "v_mov_b32 v117, v29\n\t"
    "v_mov_b32 v118, v30\n\t"
    "v_mov_b32 v119, v31\n\t"
    "v_mov_b32 v120, v32\n\t"
    "v_mov_b32 v121, v33\n\t"
    "v_mov_b32 v122, v34\n\t"
    "v_mov_b32 v123, v35\n\t"
    "v_mov_b32 v124, v36\n\t"
    "v_mov_b32 v125, v37\n\t"
    "v_mov_b32 v126, v38\n\t"
    "v_mov_b32 v127, v39"
    :: "{v[96:127]}"(w), "v"(a) : "v8", "v9", "v10", "v11", "v12", "v13", "v14", "v15", "v16", "v17", "v18", "v19", "v20", "v21", "v22", "v23", "v24", "v25", "v26", "v27", "v28", "v29", "v30", "v31", "v32", "v33", "v34", "v35", "v36", "v37", "v38", "v39", "memory");
}
template <> __device__ __forceinline__ void qk_swap<2>(u32x32& w, unsigned a) {
  asm volatile("ds_read_b128 v[8:11], %1 offset:0\n\t"
    "ds_read_b128 v[12:15], %1 offset:1024\n\t"
    "ds_read_b128 v[16:19], %1 offset:8192\n\t"
    "ds_read_b128 v[20:23], %1 offset:9216\n\t"
    "ds_read_b128 v[24:27], %1 offset:16384\n\t"
    "ds_read_b128 v[28:31], %1 offset:17408\n\t"
    "ds_read_b128 v[32:35], %1 offset:24576\n\t"
    "ds_read_b128 v[36:39], %1 offset:25600\n\t"
    "ds_write_b128 %1, v[128:131] offset:0\n\t"
    "ds_write_b128 %1, v[132:135] offset:1024\n\t"
    "ds_write_b128 %1, v[136:139] offset:8192\n\t"
    "ds_write_b128 %1, v[140:143] offset:9216\n\t"
    "ds_write_b128 %1, v[144:147] offset:16384\n\t"
    "ds_write_b128 %1, v[148:151] offset:17408\n\t"
    "ds_write_b128 %1, v[152:155] offset:24576\n\t"
    "ds_write_b128 %1, v[156:159] offset:25600\n\t"
    "s_waitcnt lgkmcnt(0)\n\t"
    "v_mov_b32 v128, v8\n\t"
    "v_mov_b32 v129, v9\n\t"
    "v_mov_b32 v130, v10\n\t"
    "v_mov_b32 v131, v11\n\t"
    "v_mov_b32 v132, v12\n\t"
    "v_mov_b32 v133, v13\n\t"
    "v_mov_b32 v134, v14\n\t"
    "v_mov_b32 v135, v15\n\t"
    "v_mov_b32 v136, v16\n\t"
    "v_mov_b32 v137, v17\n\t"
    "v_mov_b32 v138, v18\n\t"
    "v_mov_b32 v139, v19\n\t"
    "v_mov_b32 v140, v20\n\t"
    "v_mov_b32 v141, v21\n\t"
    "v_mov_b32 v142, v22\n\t"
    "v_mov_b32 v143, v23\n\t"
    "v_mov_b32 v144, v24\n\t"
    "v_mov_b32 v145, v25\n\t"
    "v_mov_b32 v146, v26\n\t"
    "v_mov_b32 v147, v27\n\t"
    "v_mov_b32 v148, v28\n\t"
    "v_mov_b32 v149, v29\n\t"
    "v_mov_b32 v150, v30\n\t"
    "v_mov_b32 v151, v31\n\t"
    "v_mov_b32 v152, v32\n\t"
    "v_mov_b32 v153, v33\n\t"
    "v_mov_b32 v154, v34\n\t"
    "v_mov_b32 v155, v35\n\t"
    "v_mov_b32 v156, v36\n\t"
    "v_mov_b32 v157, v37\n\t"
    "v_mov_b32 v158, v38\n\t"
    "v_mov_b32 v159, v39"
    :: "{v[128:159]}"(w), "v"(a) : "v8", "v9", "v10", "v11", "v12", "v13", "v14", "v15", "v16", "v17", "v18", "v19", "v20", "v21", "v22", "v23", "v24", "v25", "v26", "v27", "v28", "v29", "v30", "v31", "v32", "v33", "v34", "v35", "v36", "v37", "v38", "v39", "memory");
}
template <> __device__ __forceinline__ void qk_swap<3>(u32x32& w, unsigned a) {
  asm volatile("ds_read_b128 v[8:11], %1 offset:0\n\t"
    "ds_read_b128 v[12:15], %1 offset:1024\n\t"
    "ds_read_b128 v[16:19], %1 offset:8192\n\t"
    "ds_read_b128 v[20:23], %1 offset:9216\n\t"
    "ds_read_b128 v[24:27], %1 offset:16384\n\t"
    "ds_read_b128 v[28:31], %1 offset:17408\n\t"
    "ds_read_b128 v[32:35], %1 offset:24576\n\t"
    "ds_read_b128 v[36:39], %1 offset:25600\n\t"
    "ds_write_b128 %1, v[160:163] offset:0\n\t"
    "ds_write_b128 %1, v[164:167] offset:1024\n\t"
    "ds_write_b128 %1, v[168:171] offset:8192\n\t"
    "ds_write_b128 %1, v[172:175] offset:9216\n\t"
    "ds_write_b128 %1, v[176:179] offset:16384\n\t"
    "ds_write_b128 %1, v[180:183] offset:17408\n\t"
    "ds_write_b128 %1, v[184:187] offset:24576\n\t"
    "ds_write_b128 %1, v[188:191] offset:25600\n\t"
    "s_waitcnt lgkmcnt(0)\n\t"
    "v_mov_b32 v160, v8\n\t"
    "v_mov_b32 v161, v9\n\t"
    "v_mov_b32 v162, v10\n\t"
    "v_mov_b32 v163, v11\n\t"
    "v_mov_b32 v164, v12\n\t"
    "v_mov_b32 v165, v13\n\t"
    "v_mov_b32 v166, v14\n\t"
    "v_mov_b32 v167, v15\n\t"
    "v_mov_b32 v168, v16\n\t"
    "v_mov_b32 v169, v17\n\t"
    "v_mov_b32 v170, v18\n\t"
    "v_mov_b32 v171, v19\n\t"
    "v_mov_b32 v172, v20\n\t"
    "v_mov_b32 v173, v21\n\t"
    "v_mov_b32 v174, v22\n\t"
    "v_mov_b32 v175, v23\n\t"
    "v_mov_b32 v176, v24\n\t"
    "v_mov_b32 v177, v25\n\t"
    "v_mov_b32 v178, v26\n\t"
    "v_mov_b32 v179, v27\n\t"
    "v_mov_b32 v180, v28\n\t"
    "v_mov_b32 v181, v29\n\t"
    "v_mov_b32 v182, v30\n\t"
    "v_mov_b32 v183, v31\n\t"
    "v_mov_b32 v184, v32\n\t"
    "v_mov_b32 v185, v33\n\t"
    "v_mov_b32 v186, v34\n\t"
    "v_mov_b32 v187, v35\n\t"
    "v_mov_b32 v188, v36\n\t"
    "v_mov_b32 v189, v37\n\t"
    "v_mov_b32 v190, v38\n\t"
    "v_mov_b32 v191, v39"
    :: "{v[160:191]}"(w), "v"(a) : "v8", "v9", "v10", "v11", "v12", "v13", "v14", "v15", "v16", "v17", "v18", "v19", "v20", "v21", "v22", "v23", "v24", "v25", "v26", "v27", "v28", "v29", "v30", "v31", "v32", "v33", "v34", "v35", "v36", "v37", "v38", "v39", "memory");
}
template <> __device__ __forceinline__ void qk_swap<4>(u32x32& w, unsigned a) {
  asm volatile("ds_read_b128 v[8:11], %1 offset:0\n\t"
    "ds_read_b128 v[12:15], %1 offset:1024\n\t"
    "ds_read_b128 v[16:19], %1 offset:8192\n\t"
    "ds_read_b128 v[20:23], %1 offset:9216\n\t"
    "ds_read_b128 v[24:27], %1 offset:16384\n\t"
    "ds_read_b128 v[28:31], %1 offset:17408\n\t"
    "ds_read_b128 v[32:35], %1 offset:24576\n\t"
    "ds_read_b128 v[36:39], %1 offset:25600\n\t"
    "ds_write_b128 %1, v[192:195] offset:0\n\t"
    "ds_write_b128 %1, v[196:199] offset:1024\n\t"
    "ds_write_b128 %1, v[200:203] offset:8192\n\t"
    "ds_write_b128 %1, v[204:207] offset:9216\n\t"
    "ds_write_b128 %1, v[208:211] offset:16384\n\t"
    "ds_write_b128 %1, v[212:215] offset:17408\n\t"
    "ds_write_b128 %1, v[216:219] offset:24576\n\t"
    "ds_write_b128 %1, v[220:223] offset:25600\n\t"
    "s_waitcnt lgkmcnt(0)\n\t"
    "v_mov_b32 v192, v8\n\t"
    "v_mov_b32 v193, v9\n\t"
    "v_mov_b32 v194, v10\n\t"
    "v_mov_b32 v195, v11\n\t"
    "v_mov_b32 v196, v12\n\t"
    "v_mov_b32 v197, v13\n\t"
    "v_mov_b32 v198, v14\n\t"
    "v_mov_b32 v199, v15\n\t"
    "v_mov_b32 v200, v16\n\t"
    "v_mov_b32 v201, v17\n\t"
    "v_mov_b32 v202, v18\n\t"
    "v_mov_b32 v203, v19\n\t"
    "v_mov_b32 v204, v20\n\t"
    "v_mov_b32 v205, v21\n\t"
    "v_mov_b32 v206, v22\n\t"
    "v_mov_b32 v207, v23\n\t"
    "v_mov_b32 v208, v24\n\t"
    "v_mov_b32 v209, v25\n\t"
    "v_mov_b32 v210, v26\n\t"
    "v_mov_b32 v211, v27\n\t"
    "v_mov_b32 v212, v28\n\t"
    "v_mov_b32 v213, v29\n\t"
    "v_mov_b32 v214, v30\n\t"
    "v_mov_b32 v215, v31\n\t"
    "v_mov_b32 v216, v32\n\t"
    "v_mov_b32 v217, v33\n\t"
    "v_mov_b32 v218, v34\n\t"
    "v_mov_b32 v219, v35\n\t"
    "v_mov_b32 v220, v36\n\t"
    "v_mov_b32 v221, v37\n\t"
    "v_mov_b32 v222, v38\n\t"
    "v_mov_b32 v223, v39"
    :: "{v[192:223]}"(w), "v"(a) : "v8", "v9", "v10", "v11", "v12", "v13", "v14", "v15", "v16", "v17", "v18", "v19", "v20", "v21", "v22", "v23", "v24", "v25", "v26", "v27", "v28", "v29", "v30", "v31", "v32", "v33", "v34", "v35", "v36", "v37", "v38", "v39", "memory");
}
template <> __device__ __forceinline__ void qk_swap<5>(u32x32& w, unsigned a) {
  asm volatile("ds_read_b128 v[8:11], %1 offset:0\n\t"
    "ds_read_b128 v[12:15], %1 offset:1024\n\t"
    "ds_read_b128 v[16:19], %1 offset:8192\n\t"
    "ds_read_b128 v[20:23], %1 offset:9216\n\t"
    "ds_read_b128 v[24:27], %1 offset:16384\n\t"
    "ds_read_b128 v[28:31], %1 offset:17408\n\t"
    "ds_read_b128 v[32:35], %1 offset:24576\n\t"
    "ds_read_b128 v[36:39], %1 offset:25600\n\t"
    "ds_write_b128 %1, v[224:227] offset:0\n\t"
    "ds_write_b128 %1, v[228:231] offset:1024\n\t"
    "ds_write_b128 %1, v[232:235] offset:8192\n\t"
    "ds_write_b128 %1, v[236:239] offset:9216\n\t"
    "ds_write_b128 %1, v[240:243] offset:16384\n\t"
    "ds_write_b128 %1, v[244:247] offset:17408\n\t"
    "ds_write_b128 %1, v[248:251] offset:24576\n\t"
    "ds_write_b128 %1, v[252:255] offset:25600\n\t"
    "s_waitcnt lgkmcnt(0)\n\t"
    "v_mov_b32 v224, v8\n\t"
    "v_mov_b32 v225, v9\n\t"
    "v_mov_b32 v226, v10\n\t"
    "v_mov_b32 v227, v11\n\t"
    "v_mov_b32 v228, v12\n\t"
    "v_mov_b32 v229, v13\n\t"
    "v_mov_b32 v230, v14\n\t"
    "v_mov_b32 v231, v15\n\t"
    "v_mov_b32 v232, v16\n\t"
    "v_mov_b32 v233, v17\n\t"
    "v_mov_b32 v234, v18\n\t"
    "v_mov_b32 v235, v19\n\t"
    "v_mov_b32 v236, v20\n\t"
    "v_mov_b32 v237, v21\n\t"
    "v_mov_b32 v238, v22\n\t"
    "v_mov_b32 v239, v23\n\t"
    "v_mov_b32 v240, v24\n\t"
    "v_mov_b32 v241, v25\n\t"
    "v_mov_b32 v242, v26\n\t"
    "v_mov_b32 v243, v27\n\t"
    "v_mov_b32 v244, v28\n\t"
    "v_mov_b32 v245, v29\n\t"
    "v_mov_b32 v246, v30\n\t"
    "v_mov_b32 v247, v31\n\t"
    "v_mov_b32 v248, v32\n\t"
    "v_mov_b32 v249, v33\n\t"
    "v_mov_b32 v250, v34\n\t"
    "v_mov_b32 v251, v35\n\t"
    "v_mov_b32 v252, v36\n\t"
    "v_mov_b32 v253, v37\n\t"
    "v_mov_b32 v254, v38\n\t"
    "v_mov_b32 v255, v39"
    :: "{v[224:255]}"(w), "v"(a) : "v8", "v9", "v10", "v11", "v12", "v13", "v14", "v15", "v16", "v17", "v18", "v19", "v20", "v21", "v22", "v23", "v24", "v25", "v26", "v27", "v28", "v29", "v30", "v31", "v32", "v33", "v34", "v35", "v36", "v37", "v38", "v39", "memory");
}
template <int I> __device__ __forceinline__ void vr_wload(u32x32& w, unsigned a);
template <> __device__ __forceinline__ void vr_wload<0>(u32x32& w, unsigned a) {
  asm volatile("ds_read_b64_tr_b16 v[64:65], %1 offset:0\n\t"
    "ds_read_b64_tr_b16 v[66:67], %1 offset:256\n\t"
    "ds_read_b64_tr_b16 v[68:69], %1 offset:2048\n\t"
    "ds_read_b64_tr_b16 v[70:71], %1 offset:2304\n\t"
    "ds_read_b64_tr_b16 v[72:73], %1 offset:32\n\t"
    "ds_read_b64_tr_b16 v[74:75], %1 offset:288\n\t"
    "ds_read_b64_tr_b16 v[76:77], %1 offset:2080\n\t"
    "ds_read_b64_tr_b16 v[78:79], %1 offset:2336\n\t"
    "s_waitcnt lgkmcnt(0)\n\t"
    "ds_read_b64_tr_b16 v[80:81], %1 offset:64\n\t"
    "ds_read_b64_tr_b16 v[82:83], %1 offset:320\n\t"
    "ds_read_b64_tr_b16 v[84:85], %1 offset:2112\n\t"
    "ds_read_b64_tr_b16 v[86:87], %1 offset:2368\n\t"
    "ds_read_b64_tr_b16 v[88:89], %1 offset:96\n\t"
    "ds_read_b64_tr_b16 v[90:91], %1 offset:352\n\t"
    "ds_read_b64_tr_b16 v[92:93], %1 offset:2144\n\t"
    "ds_read_b64_tr_b16 v[94:95], %1 offset:2400\n\t"
    "s_waitcnt lgkmcnt(0)"
    : "=&{v[64:95]}"(w) : "v"(a) : "memory");
}
template <> __device__ __forceinline__ void vr_wload<1>(u32x32& w, unsigned a) {
  asm volatile("ds_read_b64_tr_b16 v[96:97], %1 offset:8192\n\t"
    "ds_read_b64_tr_b16 v[98:99], %1 offset:8448\n\t"
    "ds_read_b64_tr_b16 v[100:101], %1 offset:10240\n\t"
    "ds_read_b64_tr_b16 v[102:103], %1 offset:10496\n\t"
    "ds_read_b64_tr_b16 v[104:105], %1 offset:8224\n\t"
    "ds_read_b64_tr_b16 v[106:107], %1 offset:8480\n\t"
    "ds_read_b64_tr_b16 v[108:109], %1 offset:10272\n\t"
    "ds_read_b64_tr_b16 v[110:111], %1 offset:10528\n\t"
    "s_waitcnt lgkmcnt(0)\n\t"
    "ds_read_b64_tr_b16 v[112:113], %1 offset:8256\n\t"
    "ds_read_b64_tr_b16 v[114:115], %1 offset:8512\n\t"
    "ds_read_b64_tr_b16 v[116:117], %1 offset:10304\n\t"
    "ds_read_b64_tr_b16 v[118:119], %1 offset:10560\n\t"
    "ds_read_b64_tr_b16 v[120:121], %1 offset:8288\n\t"
    "ds_read_b64_tr_b16 v[122:123], %1 offset:8544\n\t"
    "ds_read_b64_tr_b16 v[124:125], %1 offset:10336\n\t"
    "ds_read_b64_tr_b16 v[126:127], %1 offset:10592\n\t"
    "s_waitcnt lgkmcnt(0)"
    : "=&{v[96:127]}"(w) : "v"(a) : "memory");
}
template <> __device__ __forceinline__ void vr_wload<2>(u32x32& w, unsigned a) {
  asm volatile("ds_read_b64_tr_b16 v[128:129], %1 offset:0\n\t"
    "ds_read_b64_tr_b16 v[130:131], %1 offset:128\n\t"
    "ds_read_b64_tr_b16 v[132:133], %1 offset:1024\n\t"
    "ds_read_b64_tr_b16 v[134:135], %1 offset:1152\n\t"
    "ds_read_b64_tr_b16 v[136:137], %1 offset:32\n\t"
    "ds_read_b64_tr_b16 v[138:139], %1 offset:160\n\t"
    "ds_read_b64_tr_b16 v[140:141], %1 offset:1056\n\t"
    "ds_read_b64_tr_b16 v[142:143], %1 offset:1184\n\t"
    "s_waitcnt lgkmcnt(0)\n\t"
    "ds_read_b64_tr_b16 v[144:145], %1 offset:4096\n\t"
    "ds_read_b64_tr_b16 v[146:147], %1 offset:4224\n\t"
    "ds_read_b64_tr_b16 v[148:149], %1 offset:5120\n\t"
    "ds_read_b64_tr_b16 v[150:151], %1 offset:5248\n\t"
    "ds_read_b64_tr_b16 v[152:153], %1 offset:4128\n\t"
    "ds_read_b64_tr_b16 v[154:155], %1 offset:4256\n\t"
    "ds_read_b64_tr_b16 v[156:157], %1 offset:5152\n\t"
    "ds_read_b64_tr_b16 v[158:159], %1 offset:5280\n\t"
    "s_waitcnt lgkmcnt(0)"
    : "=&{v[128:159]}"(w) : "v"(a) : "memory");
}
template <> __device__ __forceinline__ void vr_wload<3>(u32x32& w, unsigned a) {
  asm volatile("ds_read_b64_tr_b16 v[160:161], %1 offset:8192\n\t"
    "ds_read_b64_tr_b16 v[162:163], %1 offset:8320\n\t"
    "ds_read_b64_tr_b16 v[164:165], %1 offset:9216\n\t"
    "ds_read_b64_tr_b16 v[166:167], %1 offset:9344\n\t"
    "ds_read_b64_tr_b16 v[168:169], %1 offset:8224\n\t"
    "ds_read_b64_tr_b16 v[170:171], %1 offset:8352\n\t"
    "ds_read_b64_tr_b16 v[172:173], %1 offset:9248\n\t"
    "ds_read_b64_tr_b16 v[174:175], %1 offset:9376\n\t"
    "s_waitcnt lgkmcnt(0)\n\t"
    "ds_read_b64_tr_b16 v[176:177], %1 offset:12288\n\t"
    "ds_read_b64_tr_b16 v[178:179], %1 offset:12416\n\t"
    "ds_read_b64_tr_b16 v[180:181], %1 offset:13312\n\t"
    "ds_read_b64_tr_b16 v[182:183], %1 offset:13440\n\t"
    "ds_read_b64_tr_b16 v[184:185], %1 offset:12320\n\t"
    "ds_read_b64_tr_b16 v[186:187], %1 offset:12448\n\t"
    "ds_read_b64_tr_b16 v[188:189], %1 offset:13344\n\t"
    "ds_read_b64_tr_b16 v[190:191], %1 offset:13472\n\t"
    "s_waitcnt lgkmcnt(0)"
    : "=&{v[160:191]}"(w) : "v"(a) : "memory");
}
template <> __device__ __forceinline__ void vr_wload<4>(u32x32& w, unsigned a) {
  asm volatile("ds_read_b64_tr_b16 v[192:193], %1 offset:0\n\t"
    "ds_read_b64_tr_b16 v[194:195], %1 offset:128\n\t"
    "ds_read_b64_tr_b16 v[196:197], %1 offset:1024\n\t"
    "ds_read_b64_tr_b16 v[198:199], %1 offset:1152\n\t"
    "ds_read_b64_tr_b16 v[200:201], %1 offset:32\n\t"
    "ds_read_b64_tr_b16 v[202:203], %1 offset:160\n\t"
    "ds_read_b64_tr_b16 v[204:205], %1 offset:1056\n\t"
    "ds_read_b64_tr_b16 v[206:207], %1 offset:1184\n\t"
    "s_waitcnt lgkmcnt(0)\n\t"
    "ds_read_b64_tr_b16 v[208:209], %1 offset:4096\n\t"
    "ds_read_b64_tr_b16 v[210:211], %1 offset:4224\n\t"
    "ds_read_b64_tr_b16 v[212:213], %1 offset:5120\n\t"
    "ds_read_b64_tr_b16 v[214:215], %1 offset:5248\n\t"
    "ds_read_b64_tr_b16 v[216:217], %1 offset:4128\n\t"
    "ds_read_b64_tr_b16 v[218:219], %1 offset:4256\n\t"
    "ds_read_b64_tr_b16 v[220:221], %1 offset:5152\n\t"
    "ds_read_b64_tr_b16 v[222:223], %1 offset:5280\n\t"
    "s_waitcnt lgkmcnt(0)"
    : "=&{v[192:223]}"(w) : "v"(a) : "memory");
}
template <> __device__ __forceinline__ void vr_wload<5>(u32x32& w, unsigned a) {
  asm volatile("ds_read_b64_tr_b16 v[224:225], %1 offset:8192\n\t"
    "ds_read_b64_tr_b16 v[226:227], %1 offset:8320\n\t"
    "ds_read_b64_tr_b16 v[228:229], %1 offset:9216\n\t"
    "ds_read_b64_tr_b16 v[230:231], %1 offset:9344\n\t"
    "ds_read_b64_tr_b16 v[232:233], %1 offset:8224\n\t"
    "ds_read_b64_tr_b16 v[234:235], %1 offset:8352\n\t"
    "ds_read_b64_tr_b16 v[236:237], %1 offset:9248\n\t"
    "ds_read_b64_tr_b16 v[238:239], %1 offset:9376\n\t"
    "s_waitcnt lgkmcnt(0)\n\t"
    "ds_read_b64_tr_b16 v[240:241], %1 offset:12288\n\t"
    "ds_read_b64_tr_b16 v[242:243], %1 offset:12416\n\t"
    "ds_read_b64_tr_b16 v[244:245], %1 offset:13312\n\t"
    "ds_read_b64_tr_b16 v[246:247], %1 offset:13440\n\t"
    "ds_read_b64_tr_b16 v[248:249], %1 offset:12320\n\t"
    "ds_read_b64_tr_b16 v[250:251], %1 offset:12448\n\t"
    "ds_read_b64_tr_b16 v[252:253], %1 offset:13344\n\t"
    "ds_read_b64_tr_b16 v[254:255], %1 offset:13472\n\t"
    "s_waitcnt lgkmcnt(0)"
    : "=&{v[224:255]}"(w) : "v"(a) : "memory");
}
#define OREP 2
#define AREP 2
struct VRW { u32x32 v0, v1, o0, o1, o2, o3; };
__device__ __forceinline__ void vr_v(const VRW& W, const char* ag, const char* vd, unsigned wa, unsigned ra, unsigned& cap, unsigned& sp, unsigned long long& tc, unsigned long long* ts) {
  asm volatile("v_mbcnt_lo_u32_b32 v16, -1, 0\n\t"
    "v_mbcnt_hi_u32_b32 v16, -1, v16\n\t"
    "v_lshlrev_b32 v17, 4, v16\n\t"
    "s_mov_b32 %[sp], 0\n\t"
    "global_load_dwordx4 v[24:27], v17, %[ag] sc1\n\t"
    "global_load_dwordx4 v[28:31], v17, %[ag] offset:1024 sc1\n\t"
    "s_memtime %[tc]\n\t"
    "v_and_b32 v22, 4, v16\n\t"
    "v_mul_u32_u24 v22, 0x2aaa, v22\n\t"
    "v_lshrrev_b32 v18, 1, v16\n\t"
    "v_and_b32 v18, 2, v18\n\t"
    "v_or3_b32 v18, v18, v22, %[c44]\n\t"
    "v_lshl_add_u32 v19, v16, 3, %[wa]\n\t"
    "v_lshl_add_u32 v20, v16, 3, %[ra]\n\t"
    "v_and_b32 v22, 40, v16\n\t"
    "v_lshlrev_b32 v21, 3, v22\n\t"
    "v_and_b32 v22, 3, v16\n\t"
    "v_lshl_or_b32 v21, v22, 4, v21\n\t"
    "v_and_b32 v22, 4, v16\n\t"
    "v_lshl_or_b32 v21, v22, 1, v21\n\t"
    "v_lshrrev_b32 v22, 2, v16\n\t"
    "v_and_or_b32 v21, v22, 4, v21\n\t"
    "3:\n\t"
    "v_mov_b32 v32, 0\n\t"
    "v_mov_b32 v33, 0\n\t"
    "v_mov_b32 v34, 0\n\t"
    "v_mov_b32 v35, 0\n\t"
    "v_mov_b32 v36, 0\n\t"
    "v_mov_b32 v37, 0\n\t"
    "v_mov_b32 v38, 0\n\t"
    "v_mov_b32 v39, 0\n\t"
    "v_mov_b32 v40, 0\n\t"
    "v_mov_b32 v41, 0\n\t"
    "v_mov_b32 v42, 0\n\t"
    "v_mov_b32 v43, 0\n\t"
    "v_mov_b32 v44, 0\n\t"
    "v_mov_b32 v45, 0\n\t"
    "v_mov_b32 v46, 0\n\t"
    "v_mov_b32 v47, 0\n\t"
    "s_waitcnt vmcnt(0) lgkmcnt(0)\n\t"
    "v_smfmac_f32_16x16x64_bf16 v[32:35], v[24:27], v[64:71], v18\n\t"
    "v_smfmac_f32_16x16x64_bf16 v[32:35], v[28:31], v[96:103], v18\n\t"
    "v_max3_u32 v22, v24, v25, v26\n\t"
    "v_max3_u32 v23, v27, v28, v29\n\t"
    "v_smfmac_f32_16x16x64_bf16 v[36:39], v[24:27], v[72:79], v18\n\t"
    "v_max3_u32 v22, v22, v30, v31\n\t"
    "v_max_u32 v22, v22, v23\n\t"
    "v_smfmac_f32_16x16x64_bf16 v[36:39], v[28:31], v[104:111], v18\n\t"
    "v_cmp_eq_u32 vcc, -1, v22\n\t"
    "s_cbranch_vccnz 1f\n\t"
    "2:\n\t"
    "v_smfmac_f32_16x16x64_bf16 v[40:43], v[24:27], v[80:87], v18\n\t"
    "v_permlane16_swap_b32 v32, v34\n\t"
    "v_permlane16_swap_b32 v33, v35\n\t"
    "v_smfmac_f32_16x16x64_bf16 v[40:43], v[28:31], v[112:119], v18\n\t"
    "v_pk_add_f32 v[32:33], v[32:33], v[34:35]\n\t"
    "ds_write_b64 v19, v[32:33] offset:0\n\t"
    "v_smfmac_f32_16x16x64_bf16 v[44:47], v[24:27], v[88:95], v18\n\t"
    "s_nop 0\n\t"
    "v_permlane16_swap_b32 v36, v38\n\t"
    "v_permlane16_swap_b32 v37, v39\n\t"
    "v_smfmac_f32_16x16x64_bf16 v[44:47], v[28:31], v[120:127], v18\n\t"
    "v_pk_add_f32 v[36:37], v[36:37], v[38:39]\n\t"
    "ds_write_b64 v19, v[36:37] offset:512\n\t"
    "v_permlane16_swap_b32 v40, v42\n\t"
    "v_permlane16_swap_b32 v41, v43\n\t"
    "v_pk_add_f32 v[40:41], v[40:41], v[42:43]\n\t"
    "ds_write_b64 v19, v[40:41] offset:1024\n\t"
    "s_nop 1\n\t"
    "v_permlane16_swap_b32 v44, v46\n\t"
    "v_permlane16_swap_b32 v45, v47\n\t"
    "v_pk_add_f32 v[44:45], v[44:45], v[46:47]\n\t"
    "ds_write_b64 v19, v[44:45] offset:1536\n\t"
    "s_cmp_ge_u32 %[wa], 8192\n\t"
    "s_waitcnt lgkmcnt(0)\n\t"
    "s_barrier\n\t"
    "s_cbranch_scc1 9f\n\t"
    "ds_read_b64 v[48:49], v20 offset:0\n\t"
    "ds_read_b64 v[50:51], v20 offset:2048\n\t"
    "ds_read_b64 v[52:53], v20 offset:4096\n\t"
    "ds_read_b64 v[54:55], v20 offset:6144\n\t"
    "ds_read_b64 v[56:57], v20 offset:8192\n\t"
    "ds_read_b64 v[58:59], v20 offset:10240\n\t"
    "ds_read_b64 v[60:61], v20 offset:12288\n\t"
    "ds_read_b64 v[62:63], v20 offset:14336\n\t"
    "s_waitcnt lgkmcnt(6)\n\t"
    "v_pk_add_f32 v[48:49], v[48:49], v[50:51]\n\t"
    "s_waitcnt lgkmcnt(4)\n\t"
    "v_pk_add_f32 v[52:53], v[52:53], v[54:55]\n\t"
    "v_pk_add_f32 v[48:49], v[48:49], v[52:53]\n\t"
    "s_waitcnt lgkmcnt(2)\n\t"
    "v_pk_add_f32 v[56:57], v[56:57], v[58:59]\n\t"
    "s_waitcnt lgkmcnt(0)\n\t"
    "v_pk_add_f32 v[60:61], v[60:61], v[62:63]\n\t"
    "v_pk_add_f32 v[56:57], v[56:57], v[60:61]\n\t"
    "v_pk_add_f32 v[48:49], v[48:49], v[56:57]\n\t"
    "v_cvt_pk_bf16_f32 v22, v48, v49\n\t"
    "v_min_u32 v22, -2, v22\n\t"
    "global_store_dword v21, v22, %[vd] sc1\n\t"
    "s_waitcnt lgkmcnt(0)\n\t"
    "s_branch 9f\n\t"
    "1:\n\t"
    "s_add_u32 %[sp], %[sp], 1\n\t"
    "s_cmp_ge_u32 %[sp], %[cap]\n\t"
    "s_cbranch_scc1 7f\n\t"
    "global_load_dwordx4 v[24:27], v17, %[ag] sc1\n\t"
    "global_load_dwordx4 v[28:31], v17, %[ag] offset:1024 sc1\n\t"
    "s_waitcnt lgkmcnt(0)\n\t"
    "s_memtime %[tc]\n\t"
    "s_nop 7\n\t"
    "s_branch 3b\n\t"
    "7:\n\t"
    "s_mov_b32 %[cap], 0\n\t"
    "s_branch 2b\n\t"
    "9:"
    : [tc] "=&s"(tc), [sp] "=&s"(sp), [cap] "+s"(cap)
    : [ag] "s"(ag), [vd] "s"(vd), [wa] "s"(wa), [ra] "s"(ra), [c44] "s"(0x4444u), "{v[64:95]}"(W.v0), "{v[96:127]}"(W.v1), "{v[128:159]}"(W.o0), "{v[160:191]}"(W.o1), "{v[192:223]}"(W.o2), "{v[224:255]}"(W.o3)
    : "v16", "v17", "v18", "v19", "v20", "v21", "v22", "v23", "v24", "v25", "v26", "v27", "v28", "v29", "v30", "v31", "v32", "v33", "v34", "v35", "v36", "v37", "v38", "v39", "v40", "v41", "v42", "v43", "v44", "v45", "v46", "v47", "v48", "v49", "v50", "v51", "v52", "v53", "v54", "v55", "v56", "v57", "v58", "v59", "v60", "v61", "v62", "v63", "vcc", "scc", "memory");
}
__device__ __forceinline__ void vr_r(const VRW& W, const char* os, const char* ad, unsigned wa, unsigned jx, unsigned& cap, unsigned& sp, unsigned long long* ts) {
  asm volatile("v_mbcnt_lo_u32_b32 v16, -1, 0\n\t"
    "v_mbcnt_hi_u32_b32 v16, -1, v16\n\t"
    "v_lshl_or_b32 v17, v16, 4, %[jx]\n\t"
    "global_load_dwordx4 v[24:27], v17, %[os] sc1\n\t"
    "v_xor_b32 v18, 2048, v17\n\t"
    "global_load_dwordx4 v[28:31], v18, %[os] sc1\n\t"
    "v_xor_b32 v57, 4096, v17\n\t"
    "global_load_dwordx4 v[32:35], v57, %[os] sc1\n\t"
    "v_xor_b32 v58, 6144, v17\n\t"
    "global_load_dwordx4 v[36:39], v58, %[os] sc1\n\t"
    "s_mov_b32 %[sp], 0\n\t"
    "v_and_b32 v23, 4, v16\n\t"
    "v_mul_u32_u24 v23, 0x2aaa, v23\n\t"
    "v_lshrrev_b32 v19, 1, v16\n\t"
    "v_and_b32 v19, 2, v19\n\t"
    "v_or3_b32 v19, v19, v23, %[c44]\n\t"
    "v_and_b32 v23, 15, v16\n\t"
    "v_lshl_add_u32 v20, v23, 2, %[wa]\n\t"
    "v_and_b32 v23, 48, v16\n\t"
    "v_lshl_add_u32 v20, v23, 3, v20\n\t"
    "v_and_b32 v23, 32, v16\n\t"
    "v_and_b32 v21, 28, v16\n\t"
    "v_lshlrev_b32 v21, 4, v21\n\t"
    "v_or_b32 v21, v21, v23\n\t"
    "v_lshlrev_b32 v22, 3, v23\n\t"
    "v_and_b32 v23, 3, v16\n\t"
    "v_lshl_or_b32 v21, v23, 3, v21\n\t"
    "v_and_b32 v23, 31, v16\n\t"
    "v_lshl_or_b32 v22, v23, 2, v22\n\t"
    "3:\n\t"
    "v_mov_b32 v40, 0\n\t"
    "v_mov_b32 v41, 0\n\t"
    "v_mov_b32 v42, 0\n\t"
    "v_mov_b32 v43, 0\n\t"
    "v_mov_b32 v44, 0\n\t"
    "v_mov_b32 v45, 0\n\t"
    "v_mov_b32 v46, 0\n\t"
    "v_mov_b32 v47, 0\n\t"
    "v_mov_b32 v48, 0\n\t"
    "v_mov_b32 v49, 0\n\t"
    "v_mov_b32 v50, 0\n\t"
    "v_mov_b32 v51, 0\n\t"
    "v_mov_b32 v52, 0\n\t"
    "v_mov_b32 v53, 0\n\t"
    "v_mov_b32 v54, 0\n\t"
    "v_mov_b32 v55, 0\n\t"
    "v_mov_b32 v56, 0\n\t"
    "s_waitcnt vmcnt(3)\n\t"
    "v_smfmac_f32_16x16x64_bf16 v[44:47], v[24:27], v[144:151], v19\n\t"
    "v_max3_u32 v56, v56, v24, v25\n\t"
    "v_smfmac_f32_16x16x64_bf16 v[48:51], v[24:27], v[136:143], v19\n\t"
    "v_max3_u32 v56, v56, v26, v27\n\t"
    "v_smfmac_f32_16x16x64_bf16 v[40:43], v[24:27], v[128:135], v19\n\t"
    "v_smfmac_f32_16x16x64_bf16 v[52:55], v[24:27], v[152:159], v19\n\t"
    "s_waitcnt vmcnt(2)\n\t"
    "v_smfmac_f32_16x16x64_bf16 v[44:47], v[28:31], v[176:183], v19\n\t"
    "v_max3_u32 v56, v56, v28, v29\n\t"
    "v_smfmac_f32_16x16x64_bf16 v[48:51], v[28:31], v[168:175], v19\n\t"
    "v_max3_u32 v56, v56, v30, v31\n\t"
    "v_smfmac_f32_16x16x64_bf16 v[40:43], v[28:31], v[160:167], v19\n\t"
    "v_smfmac_f32_16x16x64_bf16 v[52:55], v[28:31], v[184:191], v19\n\t"
    "s_waitcnt vmcnt(1)\n\t"
    "v_smfmac_f32_16x16x64_bf16 v[44:47], v[32:35], v[208:215], v19\n\t"
    "v_max3_u32 v56, v56, v32, v33\n\t"
    "v_smfmac_f32_16x16x64_bf16 v[48:51], v[32:35], v[200:207], v19\n\t"
    "v_max3_u32 v56, v56, v34, v35\n\t"
    "v_smfmac_f32_16x16x64_bf16 v[40:43], v[32:35], v[192:199], v19\n\t"
    "v_smfmac_f32_16x16x64_bf16 v[52:55], v[32:35], v[216:223], v19\n\t"
    "s_waitcnt vmcnt(0)\n\t"
    "v_smfmac_f32_16x16x64_bf16 v[44:47], v[36:39], v[240:247], v19\n\t"
    "v_max3_u32 v56, v56, v36, v37\n\t"
    "v_smfmac_f32_16x16x64_bf16 v[48:51], v[36:39], v[232:239], v19\n\t"
    "v_max3_u32 v56, v56, v38, v39\n\t"
    "v_smfmac_f32_16x16x64_bf16 v[40:43], v[36:39], v[224:231], v19\n\t"
    "v_cmp_eq_u32 vcc, -1, v56\n\t"
    "v_smfmac_f32_16x16x64_bf16 v[52:55], v[36:39], v[248:255], v19\n\t"
    "s_cbranch_vccnz 4f\n\t"
    "5:\n\t"
    "s_nop 2\n\t"
    "v_permlane32_swap_b32 v44, v48\n\t"
    "v_permlane32_swap_b32 v45, v49\n\t"
    "v_permlane32_swap_b32 v46, v50\n\t"
    "v_permlane32_swap_b32 v47, v51\n\t"
    "s_mov_b32 exec_hi, 0\n\t"
    "v_pk_add_f32 v[40:41], v[40:41], v[48:49]\n\t"
    "v_pk_add_f32 v[42:43], v[42:43], v[50:51]\n\t"
    "s_mov_b64 exec, %[hi]\n\t"
    "v_pk_add_f32 v[40:41], v[44:45], v[52:53]\n\t"
    "v_pk_add_f32 v[42:43], v[46:47], v[54:55]\n\t"
    "s_mov_b32 exec_lo, -1\n\t"
    "s_nop 0\n\t"
    "v_permlane16_swap_b32 v40, v42\n\t"
    "v_permlane16_swap_b32 v41, v43\n\t"
    "v_add_f32 v40, v40, v42\n\t"
    "v_add_f32 v41, v41, v43\n\t"
    "ds_write_b32 v20, v40 offset:16384\n\t"
    "ds_write_b32 v20, v41 offset:16448\n\t"
    "s_cmp_ge_u32 %[wa], 1024\n\t"
    "s_waitcnt lgkmcnt(0)\n\t"
    "s_barrier\n\t"
    "s_cbranch_scc1 9f\n\t"
    "ds_read_b64 v[24:25], v21 offset:16384\n\t"
    "ds_read_b64 v[26:27], v21 offset:16896\n\t"
    "ds_read_b64 v[28:29], v21 offset:17408\n\t"
    "ds_read_b64 v[30:31], v21 offset:17920\n\t"
    "ds_read_b64 v[32:33], v21 offset:18432\n\t"
    "ds_read_b64 v[34:35], v21 offset:18944\n\t"
    "ds_read_b64 v[36:37], v21 offset:19456\n\t"
    "ds_read_b64 v[38:39], v21 offset:19968\n\t"
    "s_waitcnt lgkmcnt(6)\n\t"
    "v_pk_add_f32 v[24:25], v[24:25], v[26:27]\n\t"
    "s_waitcnt lgkmcnt(4)\n\t"
    "v_pk_add_f32 v[28:29], v[28:29], v[30:31]\n\t"
    "v_pk_add_f32 v[24:25], v[24:25], v[28:29]\n\t"
    "s_waitcnt lgkmcnt(2)\n\t"
    "v_pk_add_f32 v[32:33], v[32:33], v[34:35]\n\t"
    "s_waitcnt lgkmcnt(0)\n\t"
    "v_pk_add_f32 v[36:37], v[36:37], v[38:39]\n\t"
    "v_pk_add_f32 v[32:33], v[32:33], v[36:37]\n\t"
    "v_pk_add_f32 v[24:25], v[24:25], v[32:33]\n\t"
    "v_cvt_pk_bf16_f32 v23, v24, v25\n\t"
    "v_min_u32 v23, -2, v23\n\t"
    "global_store_dword v22, v23, %[ad] sc1\n\t"
    "s_waitcnt lgkmcnt(0)\n\t"
    "s_branch 9f\n\t"
    "4:\n\t"
    "s_add_u32 %[sp], %[sp], 1\n\t"
    "s_cmp_ge_u32 %[sp], %[cap]\n\t"
    "s_cbranch_scc1 7f\n\t"
    "global_load_dwordx4 v[24:27], v17, %[os] sc1\n\t"
    "global_load_dwordx4 v[28:31], v18, %[os] sc1\n\t"
    "global_load_dwordx4 v[32:35], v57, %[os] sc1\n\t"
    "global_load_dwordx4 v[36:39], v58, %[os] sc1\n\t"
    "s_nop 7\n\t"
    "s_branch 3b\n\t"
    "7:\n\t"
    "s_mov_b32 %[cap], 0\n\t"
    "s_branch 5b\n\t"
    "9:"
    : [sp] "=&s"(sp), [cap] "+s"(cap)
    : [os] "s"(os), [ad] "s"(ad), [wa] "s"(wa), [jx] "s"(jx), [c44] "s"(0x4444u), [hi] "s"(0xFFFFFFFF00000000ull), "{v[64:95]}"(W.v0), "{v[96:127]}"(W.v1), "{v[128:159]}"(W.o0), "{v[160:191]}"(W.o1), "{v[192:223]}"(W.o2), "{v[224:255]}"(W.o3)
    : "v16", "v17", "v18", "v19", "v20", "v21", "v22", "v23", "v24", "v25", "v26", "v27", "v28", "v29", "v30", "v31", "v32", "v33", "v34", "v35", "v36", "v37", "v38", "v39", "v40", "v41", "v42", "v43", "v44", "v45", "v46", "v47", "v48", "v49", "v50", "v51", "v52", "v53", "v54", "v55", "v56", "v57", "v58", "v59", "v60", "v61", "v62", "v63", "vcc", "scc", "memory");
}
__device__ __forceinline__ void qk_body(char* smem, int wave_id_, int xg, int h, int wg, const u16* __restrict__ Wq, const u16* __restrict__ Wk,
                                        const char* __restrict__ Kx, const char* __restrict__ V, char* ws, unsigned* tmo, u64 tag) {
  const int wave = WAVE_ID;
  const int ty = wave >> 2, w4 = wave & 3;
  const char* wq = uni((const char*)(Wq + h * DH)); const char* wk = uni((const char*)(Wk + h * DH));
  auto stage = [&](int u) {
    stage_proj((u & 1) ? wk + (size_t)(((u >> 1) + 2) & 7) * 128 * HD * 2 : wq + (size_t)(u >> 1) * 128 * HD * 2, ((u + 4) % 5) * 32768, wave * 64 + lane_id(), wave);
  };
  QKW W;
  if (wave == 0) STS(ws, wg, 7);
  stage(0); stage(1); stage(2); stage(3);
#define WLD(i, w) do { WAIT_V(12); WBAR(); stage(2 * (i) + 4); if (ty) { WAIT_V(12); WBAR(); stage(2 * (i) + 5); } \
    { const int l_ = lane_id(); const unsigned o_ = ty ? ((2 * (i) + 5) % 5) * 32768 : ((2 * (i) + 4) % 5) * 32768; qk_wload<i>(w, proj_base(2 * w4, l_) + o_, proj_base(2 * w4 + 1, l_) + o_); } \
    if (!ty) { WAIT_V(12); WBAR(); stage(2 * (i) + 5); } if (wave == 0) STS(ws, wg, 1 + (i)); } while (0)
  WLD(0, W.w0); WLD(1, W.w1); WLD(2, W.w2); WLD(3, W.w3); WLD(4, W.w4); WLD(5, W.w5);
#undef WLD
  unsigned in = 0;
#pragma unroll
  for (int i = 0; i < 2; ++i) {
    if (i == 0) WAIT_V(8); else WAIT_V(0);
    WBAR();
    if (i == 0 && wave == 1) init_issue(ws, Q_V);
    if (i == 1) in = init_get(ws, tag, smem + Q_V);
    const int lane = lane_id();
    const unsigned o = Q_RING + ty * 32768 + i * 65536, f0 = proj_base(2 * w4, lane) + o, f1 = proj_base(2 * w4 + 1, lane) + o;
    u32x8 x[4] = {proj_frag(smem, f0), proj_frag(smem, f1), proj_frag(smem, f0 + 16384), proj_frag(smem, f1 + 16384)};
    asm volatile("" : "+v"(x[0]), "+v"(x[1]), "+v"(x[2]), "+v"(x[3]));
    BAR();
    char* d = smem + Q_CB + i * 57344 + ty * (i ? 32768 : 24576) + w4 * 2048 + lane * 16;
#pragma unroll
    for (int j = 0; j < 4; ++j)
      if (i || j || !ty) { *(u32x4*)(d + j * 8192) = u32x4{x[j][0], x[j][1], x[j][2], x[j][3]}; *(u32x4*)(d + j * 8192 + 1024) = u32x4{x[j][4], x[j][5], x[j][6], x[j][7]}; }
    if (i == 0 && ty) { *(u32x4*)(smem + Q_MEM + wave * 2048 + lane * 16) = u32x4{x[0][0], x[0][1], x[0][2], x[0][3]}; *(u32x4*)(smem + Q_MEM + wave * 2048 + 1024 + lane * 16) = u32x4{x[0][4], x[0][5], x[0][6], x[0][7]}; }
  }
  if (wave == 4) *(volatile LAS u32x2*)(smem + Q_TM) = u32x2{0u, 0u};
  BAR();
  if (wave == 0) STS(ws, wg, 0);
  unsigned* cnt = cnt_of(ws, in);
  const char* xb = uni(xch_base(ws) + ((size_t)3 << 21) + (size_t)((in & 2u) << 12));
  const char* oh = uni(xch_base(ws) + (size_t)((in & 1u) << 13) + (size_t)(h >> 2) * 16384 + (h & 3) * 2048);
  auto rearm = [&](int ps) {
    unsigned z = SENT; asm volatile("" : "+v"(z));
    const unsigned l16 = lane_id() * 16;
#pragma unroll
    for (int r = 0; r < 2 * OREP; ++r) st16s(oh + (size_t)(ps * 4 + (r >> 1)) * 131072, (r & 1) * 1024u + l16, u32x4{z, z, z, z});
  };
  const char* dbw = xb + (size_t)48 * 16384 + 4096 + h * 128;
  unsigned avail = 0;
  auto stage_kv = [&](int s) {
    if (avail <= (unsigned)s) {
      for (int spins = 0;; ++spins) {
        const unsigned p = ld4_sc1(cnt + (PQKV + h) * 32 + (lane_id() & 15));
        avail = min(qkv_avail(p, 1), qkv_avail(p, 2));
        if (avail > (unsigned)s) break;
        __builtin_amdgcn_s_sleep(8);
        if (give_up(spins, tmo, 5)) break;
      }
    }
    const char* ks = uni(Kx + ((size_t)s * NH + h) * 8192); const char* vs = uni(V + ((size_t)s * NH + h) * 8192);
    const int lane = lane_id();
    head_to_lds(ks, lane, Q_K); head_to_lds(vs, lane, Q_V);
  };
  auto loop = [&](auto w_c) __attribute__((always_inline)) {
    constexpr int WV = decltype(w_c)::value;
    if (WV >= 4) asm volatile("ds_read_b128 v[56:59], %1\n\tds_read_b128 v[60:63], %1 offset:1024\n\ts_waitcnt lgkmcnt(0)" : "=&{v[56:63]}"(W.x) : "v"(Q_MEM + WV * 2048 + lane_id() * 16) : "memory");
    if (WV == 6) stage_kv(0);
    if (QK_OWN) {
      constexpr int PI = WV < 4 ? WV : WV - 2;
      qk_swap<PI>(PI == 0 ? W.w0 : PI == 1 ? W.w1 : PI == 2 ? W.w2 : PI == 3 ? W.w3 : PI == 4 ? W.w4 : W.w5, Q_CB + 57344 + (WV >> 2) * 32768 + (WV & 3) * 2048 + lane_id() * 16);
    }
    if (WV < 4) __builtin_amdgcn_s_setprio(2);
    unsigned t0 = 0, cap = 60000, d0 = 105, d1 = 200;
    int dq = D_Q;
    int sl = 0;
    for (int s = 0; s < NSEG; ++s) {
      TSDECL;
      const char* ag = xb + (size_t)((sl * 4 + (AREP == 4 ? xg : (AREP == 2 ? ARSEL : 0))) * 2 + (WV >> 2)) * 16384 + (WV & 3) * 2048;
      const char* vs = WV == 4 ? dbw : WV == 5 ? ws + OFF_KVM + ((size_t)s * NH + h) * 4096 : xb + (size_t)(24 + sl * 8 + (h >> 2)) * 16384 + (h & 3) * 2048;
      const char* od = WV == 4 ? (const char*)(size_t)((unsigned)(s + 1) << 24) : oh + (size_t)sl * 524288;
      if (s > 0) wait_rt(t0, dq);
      TS(0);
      unsigned long long tc; unsigned sp;
      qk_step<WV>(W, ag, vs, od, ws + OFF_KVM + ((size_t)s * NH + h) * 4096 + 2048, cap, sp, tc, nullptr, d0, d1);
      dq = (sp & 0xFFFFu) ? min(dq + DQU, D_Q + DQM) : max(dq - DQD, D_Q);
      if (WV < 4 || WV == 5) {
        const unsigned pv = (unsigned)(s + 1) << 24 | (rt_now() & 0xFFFFFFu);
        WAIT_V(0);
        if (lane_id() == 0) st4_sc1(cnt + (PROG + h) * 32 + WV, pv);
      }
      if (WV == 6 && s + 1 < NSEG - 1) stage_kv(s + 1);
      if (WV == 7 && s > 0 && s < NSEG - 1) rearm(sl == 0 ? 2 : sl - 1);
      if (WV == 7 && s >= 8 && s < 8 + NSEG - SDIR) {
        unsigned z = SENT; asm volatile("" : "+v"(z));
        const char* kd = ws + OFF_KVM + ((size_t)(s - 8 + SDIR) * NH + h) * 4096;
#pragma unroll
        for (int r = 0; r < 4; ++r) st16s(kd, r * 1024u + lane_id() * 16, u32x4{z, z, z, z});
      }
      if (s < NSEG - 1) {
        if (WV == 4) {
          delay64<E_B>();
          t0 = bell_time(xb + (size_t)(48 + xg) * 16384 + 4096, (lane_id() & 31) * 128, (unsigned)(s + 1), cap);
          *(volatile LAS u32x2*)(smem + Q_TM) = u32x2{(unsigned)(s + 1), t0};
        } else {
          for (;;) {
            const u32x2 v = *(volatile LAS u32x2*)(smem + Q_TM);
            t0 = __builtin_amdgcn_readfirstlane(v.y);
            if (__builtin_amdgcn_readfirstlane(v.x) == (unsigned)(s + 1)) break;
            __builtin_amdgcn_s_sleep(1);
          }
        }
      }
      sl = sl == 2 ? 0 : sl + 1;
    }
    if (cap == 0 && lane_id() == 0) st4_sc1(tmo, 1u);
  };
  switch (wave) {
    case 0: loop(std::integral_constant<int, 0>{}); break;
    case 1: loop(std::integral_constant<int, 1>{}); break;
    case 2: loop(std::integral_constant<int, 2>{}); break;
    case 3: loop(std::integral_constant<int, 3>{}); break;
    case 4: loop(std::integral_constant<int, 4>{}); break;
    case 5: loop(std::integral_constant<int, 5>{}); break;
    case 6: loop(std::integral_constant<int, 6>{}); break;
    default: loop(std::integral_constant<int, 7>{}); break;
  }
  __builtin_amdgcn_s_setprio(0);
}

__device__ __forceinline__ unsigned vr_body(char* smem, int wave_id_, int xg, int h, int c, int r, int wg, const u16* __restrict__ Wv, const u16* __restrict__ Wo,
                                        const u16* __restrict__ Wq, const u16* __restrict__ Wk, char* ws, unsigned* tmo, u64 tag, const char* code) {
  const int wave = WAVE_ID;
  const unsigned po = (unsigned)(r >> 5) * 16384u + ((r >> 2) & 7) * 1024 + (r & 1) * 512 + ((r >> 1) & 1) * 128;
  const unsigned vo_ = (unsigned)(24 + (h >> 2)) * 16384u + (h & 3) * 2048 + c * 1024;
  const int hg = (wave + ROTW * (r & 3)) & 7;
  const int jr = ROTH ? r & 3 : 0;
  VRW W;
  {
    const int lane = lane_id();
    const int g = lane >> 4, q4 = (lane & 15) >> 2, p4 = lane & 3, fr = 4 * g + (q4 & 1) + 32 * (q4 >> 1);
    const unsigned lw = wave * 16384;
    const char* sv_ = uni((const char*)(Wv + (size_t)(128 * wave) * HD + h * DH + 64 * c));
    const char* so = uni((const char*)(Wo + (size_t)(512 * hg) * DM + 64 * (r >> 2) + 16 * (r & 1)));
    const unsigned vo = (unsigned)((32 * (lane >> 5) + ((lane >> 2) & 7)) * DM * 2 + ((lane >> 1) & 1) * 64 + (lane & 1) * 16);
    auto issue = [&](int p) {
#pragma unroll
      for (int i = 0; i < 8; ++i) {
        if (p >= 2 && (NTM & 8)) gldsw(so + (size_t)(128 * ((p - 2) ^ jr) + 64 * (i & 1) + 8 * (i >> 1)) * DM * 2, vo, lw + (p & 1) * 8192 + i * 1024);
        else if (p >= 2) glds16s<0>(so + (size_t)(128 * ((p - 2) ^ jr) + 64 * (i & 1) + 8 * (i >> 1)) * DM * 2, vo, lw + (p & 1) * 8192 + i * 1024);
        else gldsw(sv_ + (size_t)(8 * p + i) * 8 * HD * 2, (unsigned)((lane >> 3) * HD * 2 + (lane & 7) * 16), lw + (p & 1) * 8192 + i * 1024);
      }
    };
#define VTS(p) do { } while (0)
    if (wave == 0) STS(ws, wg, 7);
    issue(0); issue(1);
    const unsigned av = lw + (unsigned)(fr * 128 + p4 * 8), ao = lw + (unsigned)(fr * 64 + p4 * 8);
    WAIT_V(8); VTS(0); vr_wload<0>(W.v0, av); issue(2);
    WAIT_V(8); VTS(1); vr_wload<1>(W.v1, av); issue(3);
    WAIT_V(8); VTS(2); vr_wload<2>(W.o0, ao); issue(4);
    WAIT_V(8); vr_wload<3>(W.o1, ao); issue(5);
    if (wave == 0) init_issue(ws, 131072);
    WAIT_V(8); vr_wload<4>(W.o2, ao);
    WAIT_V(0); VTS(3);
    if (VHN) {
      const int rl = r - 20 * xg, hn = ((xg == 1 ? rl : rl - (rl >= (xg ? 8 : 16) ? 4 : 0)) * 8 + wave) * (6 * VHN) + (lane >> 1);
      const int pm = hn / 384, hr = hn - pm * 384, p = 8 - VHN + (pm >> 1), hh = 12 * xg + 1 + 4 * (hr >> 7);
      const u16* hp = ((pm & 1) ? Wk : Wq) + (size_t)(((pm & 1) ? (p + 2) & 7 : p) * 128 + (hr & 127)) * HD + min(hh, 31) * DH + (lane & 1) * 64;
      if (((r >> 2) & 7) != 3 && lane < 12 * VHN && pm < 2 * VHN) asm volatile("s_mov_b32 m0, %0\n\ts_nop 0\n\tglobal_load_lds_dword %1, off" :: "s"(155648 + wave * 256), "v"(hp) : "memory", "m0");
    }
    vr_wload<5>(W.o3, ao);
#undef VTS
  }
  if (wave == 0) STS(ws, wg, 5);
  if (wave == 0) STS(ws, wg, 6);
  BAR();
  if (wave == 0) STS(ws, wg, 0);
  const unsigned in = init_get(ws, tag, smem + 131072);
  const char* xb = uni(xch_base(ws) + ((size_t)3 << 21) + (size_t)((in & 2u) << 12));
  const char* ob = uni(xch_base(ws) + (size_t)((in & 1u) << 13));
  auto rearm = [&](int ps, bool ag) {
    const int lane = lane_id();
    unsigned z = SENT; asm volatile("" : "+v"(z));
    const u32x4 sv = u32x4{z, z, z, z};
    if (ag && lane < 16 * AREP) st16s(xb, (unsigned)(ps * 8 + (lane >> 4) * 2) * 16384u + po + ((lane >> 3) & 1) * 256 + (lane & 7) * 16, sv);
    st16s(xb, (unsigned)ps * 131072u + vo_ + lane * 16, sv);
  };
  __builtin_amdgcn_s_setprio(2);
  unsigned cap = 60000;
  int sl = 0;
  for (int s = 0; s < NSEG; ++s) {
    TSDECL;
    const int sn = sl == 2 ? 0 : sl + 1;
    const char* ag = xb + (size_t)((sl * 4 + (AREP == 4 ? xg : (AREP == 2 ? ARSEL : 0))) * 2 + (wave >> 2)) * 16384 + (wave & 3) * 2048;
    const char* vd = xb + (size_t)sl * 131072 + vo_ + (wave & 2) * 256 + (wave & 1) * 128;
    const char* os = ob + (size_t)((sl * 4 + (OREP == 4 ? xg : ORSEL)) * 8 + hg) * 16384 + ((r >> 1) & 1) * 1024;
    const char* ad = xb + (size_t)((sn * 4 + (wave & 3)) * 2) * 16384 + po;
    if (s > 0) delay64<E_AG>();
    TS(0);
    unsigned long long tc; unsigned sp;
    unsigned long long* tsp = nullptr;
    vr_v(W, ag, vd, wave * 2048, (wave & 3) * 512, cap, sp, tc, tsp);
    TN(12, sp + 1);
    if (s == NSEG - 1) break;
    if (wave == 7 && s > 0) rearm(sl == 0 ? 2 : sl - 1, true);
    if (wave == 6 && s == NSEG - 4 && code) touch(code, (unsigned)lane_id() * 128, 155648);
    wait_clk((unsigned)tc, E_O);
    TS(3);
    const unsigned tb = bell_time(xb + (size_t)(48 + xg) * 16384 + hg * 512, (lane_id() & 3) * 128, (unsigned)(s + 1), cap);
    wait_rt(tb, D_O);
    TS(4);
    vr_r(W, os, ad, wave * 512, jr * 2048, cap, sp, tsp);
    TN(14, (sp & 0xFFFF) + 1);
    if (wave == 1) TSFLUSH(0x6078);
    sl = sn;
  }
  __builtin_amdgcn_s_setprio(0);
  if (cap == 0 && lane_id() == 0) st4_sc1(tmo, 2u);
  return in;
}
struct ModIn { u32x4 w[8]; unsigned cv; };
__device__ __forceinline__ void mod_issue(ModIn& I, int wave_id_, int j, const u16* __restrict__ c, const u16* __restrict__ wmod) {
  const int tid = opaque_tid(), k0 = (j >> 5) * 512 + (tid >> 3);
  I.cv = c[k0 + 64 * (tid & 7)];
#pragma unroll
  for (int i = 0; i < 8; ++i) I.w[i] = ldnt<1>(wmod + (size_t)(k0 + 64 * i) * (6 * DM) + (j & 31) * 64 + (tid & 7) * 8);
}
__device__ __forceinline__ void mod_job(char* smem, int wave_id_, int j, u64 tag, const ModIn& I, char* ws) {
  const int tid = opaque_tid(), lane = tid & 63, wave = WAVE_ID, cg = tid & 7;
  const float cv = bflo(I.cv), sl = bfr(cv / (1.0f + __expf(-cv)));
  float acc[8] = {0, 0, 0, 0, 0, 0, 0, 0};
#pragma unroll
  for (int i = 0; i < 8; ++i) {
    const float sv = __shfl(sl, (lane & 56) + i);
#pragma unroll
    for (int e = 0; e < 4; ++e) { acc[2 * e] += sv * bflo(I.w[i][e]); acc[2 * e + 1] += sv * bfhi(I.w[i][e]); }
  }
#pragma unroll
  for (int e = 0; e < 8; ++e) acc[e] = red16_32_add(add_ror8(acc[e]));
  float* red = (float*)(smem + L_GB);
  if (lane < 8) { *(f32x4*)(red + wave * 64 + cg * 8) = f32x4{acc[0], acc[1], acc[2], acc[3]}; *(f32x4*)(red + wave * 64 + cg * 8 + 4) = f32x4{acc[4], acc[5], acc[6], acc[7]}; }
  __syncthreads();
  if (tid < 32) {
    f32x2 s = f32x2{0, 0};
#pragma unroll
    for (int r = 0; r < 8; ++r) s += *(const f32x2*)(red + r * 64 + tid * 2);
    st16_sc1(ws + OFF_MOD + (j * 32 + tid) * 16, u32x4{__float_as_uint(s[0]), (unsigned)tag, __float_as_uint(s[1]), tag2(tag)});
  }
  BAR();
}
#define NIMM 3
constexpr unsigned TK(int m, int hl, int part, int bo, int be) { return (unsigned)(m | hl << 2 | part << 5 | bo << 8 | be << 16); }
#define QX 30
#define CA 36
#define CB 42
#define C2 46
constexpr unsigned task_tab(int t, int it) {
  return t >= 8 && t < 13 ? (it == 0 ? TK(1, t - 8, 0, 0, 64) : it == 1 ? (t < 12 ? TK(2, t - 4, 1, C2, 64) : TK(1, 5, 1, C2, 64)) : 0)
       : it > 0 ? 0
       : t < 8 ? TK(0, t, 1, QX, 64)
       : t < 16 ? TK(1, t - 8, 0, 0, C2)
       : t < 24 ? TK(2, t - 16, 0, 0, t < 18 ? CA : (t < 20 ? CB : C2))
       : t < 26 ? TK(2, t - 24, 1, CA, 64)
       : t == 26 ? TK(1, 6, 1, C2, (C2 + 64) / 2) : t == 27 ? TK(1, 6, 2, (C2 + 64) / 2, 64)
       : t < 30 ? TK(2, t - 26, 1, CB, 64)
       : t == 30 ? TK(1, 7, 1, C2, (C2 + 64) / 2) : t == 31 ? TK(1, 7, 2, (C2 + 64) / 2, 64)
       : TK(0, t - 32, 0, 0, QX);
}
template <int IT, int I = 0> __device__ __forceinline__ unsigned task_sel(int t) {
  if constexpr (I == 40) return 0u;
  else { unsigned r = task_sel<IT, I + 1>(t); constexpr unsigned c = task_tab(I, IT); if (c) asm volatile("s_cmp_eq_u32 %1, %2\n\ts_cselect_b32 %0, %3, %0" : "+s"(r) : "s"(t), "i"(I), "i"(c) : "scc"); return r; }
}
__global__ void __launch_bounds__(512, 2) k_mega(const u16* __restrict__ Wq, const u16* __restrict__ Wk, const u16* __restrict__ Wv,
                                                 const u16* __restrict__ Wo, const u16* __restrict__ w1, const u16* __restrict__ w3,
                                                 const u16* __restrict__ w2, const u16* __restrict__ x, const u16* __restrict__ cvec,
                                                 const u16* __restrict__ wmod, const u16* __restrict__ mem0, char* ws, u16* out) {
  extern __shared__ __attribute__((aligned(16))) char smem[];
  int wave_id_ = __builtin_amdgcn_readfirstlane((int)threadIdx.x >> 6);
  unsigned bid = blockIdx.x;
  asm volatile("" : "+s"(wave_id_), "+s"(bid));
  unsigned* tmo = (unsigned*)(ws + OFF_TMO);
  const unsigned tst = (unsigned)__builtin_amdgcn_s_memrealtime();
  unsigned long long* mts = nullptr;
  const float* mod = (const float*)(ws + OFF_MOD);
  volatile LAS unsigned* tslot = (volatile LAS unsigned*)(smem + L_SLOT + 768);
#define CPF 131072
  u64 pc; asm volatile("s_getpc_b64 %0" : "=s"(pc));
  const u64 pc0 = pc & ~(u64)127;
  if (CPF && wave_id_ == 1) {
    const unsigned o = (((bid >> 3) & 31) * 64 + lane_id()) * 64;
    if (o < CPF) asm volatile("s_mov_b32 m0, %0\n\ts_nop 0\n\tglobal_load_lds_dword %1, %2" :: "s"(155648 + 1024), "v"(o), "s"(pc & ~(u64)127) : "memory", "m0");
  }
  unsigned xcc; asm volatile("s_getreg_b32 %0, hwreg(HW_REG_XCC_ID)" : "=s"(xcc));
  xcc &= 7;
  const unsigned pred = xcc * 32 + ((bid >> 3) & 31);
  ModIn mi;
  auto modj = [](unsigned sl) { return sl >= 128 && ((sl + 8) & 31) < 16 ? (int)(((sl >> 5) & 3) * 16 + (sl & 15)) : -1; };
  if (modj(pred) >= 0) mod_issue(mi, wave_id_, modj(pred), cvec, wmod);
  u64 tag;
  asm volatile("s_load_dwordx2 %0, %1, 0x0\n\ts_waitcnt lgkmcnt(0)" : "=s"(tag) : "s"(ws + OFF_TAG) : "memory");
  tag += 1;
  if (opaque_tid() == 0) {
    unsigned sl = pred;
    const unsigned s0 = sl;
    for (unsigned i = 1; i <= 256; ++i) {
      if (__hip_atomic_exchange((u64*)(ws + OFF_CLAIM + sl * 128), tag, __ATOMIC_RELAXED, __HIP_MEMORY_SCOPE_AGENT) != tag) break;
      sl = i == 256 ? 0xFFFFu : (((s0 + (i & ~31u)) & 0xE0u) | ((s0 + i) & 31u));
    }
    *tslot = sl;
  }
  BAR();
  const int slot = (int)__builtin_amdgcn_readfirstlane(*tslot);
  BAR();
  if (slot >= 256) return;
  if (modj(slot) >= 0) {
    if (slot != (int)pred) mod_issue(mi, wave_id_, modj(slot), cvec, wmod);
    mod_job(smem, wave_id_, modj(slot), tag, mi, ws);
  }
  unsigned* cnt = nullptr;
  const int xg = slot >> 5;
  int t = xg == 3 ? 24 + (slot & 7) : slot & 31;
  int hx = -1;
  if (xg < 3) {
    const int nq = xg == 2 ? 8 : 12;
    if (t < nq) { const int h = xg * 12 + t; qk_body(smem, wave_id_, xg, h, (h >> 3) * 32 + (h & 7), Wq, Wk, ws + OFF_K, ws + OFF_V, ws, tmo, tag); }
    else {
      const int u = t - nq, r = xg * 20 + u;
      cnt = cnt_of(ws, vr_body(smem, wave_id_, xg, r >> 1, r & 1, r, (r >> 4) * 32 + 8 + (r & 15), Wv, Wo, Wq, Wk, ws, tmo, tag, CPF && HB < NSEG && u < 16 ? (const char*)pc0 + u * 8192 : nullptr));
      if (HB < NSEG) hx = u < 8 ? xg * 8 + u : (u == 8 ? 24 + xg : (u == 9 && xg == 2 ? 27 : -1));
#define HPF 1
      if (HPF && HB < NSEG && u >= 10 && u < 22) {
        const int lane = lane_id(), wv = (u - 10) * 8 + wave_id_;
        if (wv >= 64) touch(w2 + (size_t)(3072 + 256 + (wv & 15) * 16) * DM + (wv >= 80 ? 3 : xg) * 256, (unsigned)((lane >> 2) * DM * 2 + (lane & 3) * 128), wave_id_ * 256);
        else {
#pragma unroll
          for (int j = 0; j < 2; ++j) touch(w2 + (size_t)(xg * 1024 + 256 + j * 128 + (wv & 32) * 16 + (wv & 31) * 4) * DM, (unsigned)lane * 128, wave_id_ * 256);
        }
        WAIT_V(0);
      }
    }
    if (hx < 0) return;
    t = 8;
  }
  const bool cx = xg == 3;
  const int xq = cx ? (slot >> 3) & 3 : xg & 3, wsl = xq * 32 + t;
#define GDEL 1200
#define GDX 1200
  if (cx || t < 8) l1_role(smem, wave_id_, cx, xq * 8 + (t & 7), x, mod, mem0, ws + OFF_H1, cnt, tag, tmo, mts);
#define KH 0
  if (KH && !cx && t >= 24) {
    const int it = (t & 7) * 8 + wave_id_, lane = lane_id();
    touch(((it & 16) ? Wk : Wv) + (size_t)(256 + (it & 8) * 64 + (it & 7) * 32) * HD + (8 * xq + 1 + 4 * (it >> 5)) * DH,
          (unsigned)((lane >> 1) * HD * 2 + (lane & 1) * 128), 155648 + wave_id_ * 256);
  }
  if (t < 8 || t >= 24) {
    while ((unsigned)__builtin_amdgcn_s_memrealtime() - tst < (cx ? GDX : GDEL)) __builtin_amdgcn_s_sleep(8);
    if (!cx && t >= 24) {
      cnt = cnt_of(ws, init_wait(ws, tag));
      mod2_job(smem, wave_id_, xq * 8 + t - 24, mod, cvec, wmod, cnt);
    }
  }
  auto u2_of = [&]() { return cx ? -1 : (t >= 13 && t < 16 ? t - 13 : (t >= 20 && t < 24 ? t - 17 : -1)); };
  auto f_of = [&]() {
    int fl = t < 8 ? t : (t < 18 ? t - 4 : (t < 20 ? t - 10 : t - 14));
    const int sa = (3 + 2 * xq) & 7, sb = sa + 8 < 14 ? sa + 8 : 13;
    if (fl == 12) fl = sa; else if (fl == sa) fl = 12;
    else if (fl == 13) fl = sb; else if (fl == sb) fl = 13;
    return (int)__builtin_amdgcn_readfirstlane(14 * xq + fl);
  };
  unsigned tk = task_sel<0>(hx >= 0 ? 99 : (cx ? t + 8 : t)), t2 = task_sel<1>(cx ? t + 8 : t), t3 = task_sel<2>(cx ? t + 8 : t);
  for (int it = 0; tk != 0; ++it, tk = t2, t2 = t3, t3 = 0) {
    const int m = tk & 3, hl = (tk >> 2) & 7, part = (tk >> 5) & 3, bo = (tk >> 8) & 255, ns = (int)(tk >> 16) - bo, h = 8 * xq + hl;
    const bool first = m != 0 && bo == 0, late = it > 0;
    const int lane = lane_id();
    const Dep d{nullptr, (unsigned)((lane < 32 ? PL1 : PL1B - 32) + lane) * 128, 8u, (unsigned)((lane < 32 ? 0 : 8) - bo)};
    if (wave_id_ == 0 && late) wait_l1(cnt + (PL1B - PL1) * 32, 7u, tmo);
    BAR();
    gemm7<R_QKV>(smem, wave_id_, (m == 0 ? Wq : (m == 1 ? Wk : Wv)) + h * DH, nullptr, HD, ws + OFF_H1 + (size_t)bo * 65536, 65536, d,
                    ws + (m == 0 ? OFF_Q : (m == 1 ? OFF_K : OFF_V)) + h * 8192 + (size_t)bo * 32 * 8192, 32 * 8192,
                    cnt, (PQKV + h) * 32 + 4 * part + m, tmo, wsl, mts, false, first ? NIMM : 0, ns, late ? ns : 0, bo * 257, tag);
  }
  __syncthreads();
  const int u2 = u2_of();
  const int f2 = hx >= 0 ? hx : (u2 < 0 ? -1 : (xq < 3 ? xq * 8 + u2 : (u2 < 3 ? u2 * 8 + 7 : 21 + u2)));
  const int fb = hx >= 0 ? HB : 0, fe = hx >= 0 ? NSEG : HB;
  if (cx || (f2 >= 0 && f2 < 24)) {
    const int ks = cx ? xq : f2 >> 3, cn = cx ? (t + 1) & 7 : f2 & 7;
    const int lane = lane_id();
    const Dep d{nullptr, (unsigned)(cx ? (PSA + 4 * xq + ((lane >> 3) & 3)) * 128 + (lane & 7) * 4 : (PF1 + 16 * ks + (lane & 15)) * 128), cx ? 4u : 1u, cx ? (unsigned)((lane >> 1) & 3) : 0u};
    gemm7<R_PART>(smem, wave_id_, (cx ? Wo : w2) + (size_t)ks * 1024 * DM + cn * 128, nullptr, DM, ws + (cx ? OFF_O : OFF_G) + ks * 16 * 4096, (cx ? 64 : 56) * 4096, d,
                  ws + (cx ? OFF_P : OFF_P2) + (ks * DM + cn * 128) * (TLP ? 64 : 2), (size_t)32 * PROW, cnt, ((cx ? PWO : PF2) + ks * 8 + cn) * 32 + (hx >= 0 ? 1 : 0), tmo,
                  hx >= 0 ? 280 + hx : 128 + xq * 32 + (cx ? cn : 22 + u2), mts, !cx, 0, cx ? NSEG : fe, 0, 0, 0, fb);
  } else if (f2 >= 0) {
    const int c4 = f2 - 24;
    const int lane = lane_id();
    const Dep d{nullptr, (unsigned)(PF1 + 48 + (lane & 7)) * 128, 1u, 0u};
    gemm7<R_PART2>(smem, wave_id_, w2 + (size_t)3072 * DM + c4 * 256, nullptr, DM, ws + OFF_G + 48 * 4096, 56 * 4096, d,
                   ws + OFF_P2 + (3 * DM + c4 * 256) * (TLP ? 64 : 2), (size_t)32 * PROW, cnt, (PF2 + 24 + c4) * 32 + (hx >= 0 ? 1 : 0), tmo, hx >= 0 ? 280 + hx : 128 + xq * 32 + 22 + u2, mts, true, 0, fe, 0, 0, 0, fb);
  } else if (t < 8 || (t >= 16 && t < 20) || t == 24 || t == 25) {
    int f = f_of();
    asm volatile("" : "+s"(f));
    const int lane = lane_id();
    const Dep d{nullptr, (unsigned)((PLN + (lane >> 3)) * 128 + (lane & 7) * 4), 1u, 0u};
    gemm7<R_F1>(smem, wave_id_, w1 + 64 * f, w3 + 64 * f, FF, ws + OFF_H2, 65536, d, ws + OFF_G + f * 4096, 56 * 4096, cnt, (PF1 + f) * 32, tmo, 128 + xq * 18 + 8 + f, mts, false);
  } else if (t >= 9 && t < 13) {
    fin_role(smem, wave_id_, xq * 4 + t - 9, mod, ws + OFF_P2, (const u16*)(ws + OFF_X1), out, cnt, tmo, mts);
    if (xq == 3 && t == 12 && opaque_tid() == 0) {
      const u32x4 v = ld16_sc1(ws + OFF_TAG);
      const u64 tn = (((u64)v.y << 32) | v.x) + 1;
      st8_sc1(ws + OFF_TAG, u32x2{(unsigned)tn, (unsigned)(tn >> 32)});
    }
  } else if (t == 28 || t == 29) {
    row_role(smem, wave_id_, 8 * xq, t - 28, mod, ws + OFF_P, x, (u16*)(ws + OFF_X1), ws + OFF_H2, cnt, tmo, 256 + xq, mts);
  } else if (t >= 26) {
    sa_role(smem, wave_id_, 8 * xq + 2 * (t >= 30 ? t - 30 : t - 24), ws + OFF_Q, ws + OFF_K, ws + OFF_V, ws + OFF_KVM, ws + OFF_O, cnt, tmo, 128 + xq * 32 + 30 + (t & 1), mts);
  }
}

extern "C" void kernel_launch(void* const* d_in, const int* in_sizes, int n_in, void* d_out, int out_size, void* d_ws, size_t ws_size,
                              hipStream_t stream) {
  const u16* x    = (const u16*)d_in[0];
  const u16* mem0 = (const u16*)d_in[1];
  const u16* Wq   = (const u16*)d_in[2];
  const u16* Wk   = (const u16*)d_in[3];
  const u16* Wv   = (const u16*)d_in[4];
  const u16* Wo   = (const u16*)d_in[5];
  const u16* w1   = (const u16*)d_in[8];
  const u16* w3   = (const u16*)d_in[9];
  const u16* w2   = (const u16*)d_in[10];
  const u16* c    = (const u16*)d_in[11];
  const u16* wmod = (const u16*)d_in[12];
#define WSHIFT 0
  char* ws = (char*)d_ws + ((size_t)WSHIFT << 20);
  hipFuncSetAttribute((const void*)k_mega, hipFuncAttributeMaxDynamicSharedMemorySize, MEGA_LDS);
  k_mega<<<256, 512, MEGA_LDS, stream>>>(Wq, Wk, Wv, Wo, w1, w3, w2, x, c, wmod, mem0, ws, (u16*)d_out);
}
```

```cpp
#include <hip/hip_runtime.h>
#include <hip/hip_bf16.h>
#include <stdint.h>
#include <type_traits>
#pragma clang diagnostic ignored "-Winline-asm"
#pragma clang diagnostic ignored "-Wunused-value"

typedef unsigned short u16;
typedef short s16x4 __attribute__((ext_vector_type(4)));
typedef __bf16 bf16x8 __attribute__((ext_vector_type(8)));
typedef float f32x4 __attribute__((ext_vector_type(4)));
typedef float f32x2 __attribute__((ext_vector_type(2)));
typedef unsigned u32x4 __attribute__((ext_vector_type(4)));
typedef unsigned u32x2 __attribute__((ext_vector_type(2)));
#define LAS __attribute__((address_space(3)))

constexpr int SEQ = 2048, DM = 1024, NH = 32, DH = 128, HD = 4096, NMEM = 8, SEG = 32, NSEG = 64, FF = 3584;
constexpr unsigned SENT = 0xFFFFFFFFu;

__device__ __forceinline__ int lane_id() { int l; asm volatile("v_mbcnt_lo_u32_b32 %0, -1, 0\n\tv_mbcnt_hi_u32_b32 %0, -1, %0" : "=v"(l)); return l; }
#define WAVE_ID wave_id_
#define opaque_tid() (WAVE_ID * 64 + lane_id())
__device__ __forceinline__ float bf2f(u16 v) { return __builtin_bit_cast(float, (unsigned)v << 16); }
__device__ __forceinline__ float bflo(unsigned v) { return __builtin_bit_cast(float, v << 16); }
__device__ __forceinline__ float bfhi(unsigned v) { return __builtin_bit_cast(float, v & 0xffff0000u); }
__device__ __forceinline__ float bfr(float x) { return (float)(__bf16)x; }
__device__ __forceinline__ unsigned pack2(float a, float b) {
  typedef __bf16 bf16x2 __attribute__((ext_vector_type(2)));
  bf16x2 v; v[0] = (__bf16)a; v[1] = (__bf16)b; return __builtin_bit_cast(unsigned, v);
}
__device__ __forceinline__ unsigned nosent(unsigned v) { return v == SENT ? 0x7FC07FC0u : v; }
__device__ __forceinline__ bf16x8 asbf8(u32x4 v) { return __builtin_bit_cast(bf16x8, v); }
__device__ __forceinline__ s16x4 ass4(u32x2 v) { return __builtin_bit_cast(s16x4, v); }
__device__ __forceinline__ u32x2 trread(const char* lds_base, unsigned off) {
  s16x4 v = __builtin_amdgcn_ds_read_tr16_b64_v4i16((LAS s16x4*)(lds_base + off));
  return __builtin_bit_cast(u32x2, v);
}
__device__ __forceinline__ void glds16(const void* gsrc, unsigned lds_dst) {
  unsigned keep;
  asm volatile("s_mov_b32 %0, m0\n\ts_mov_b32 m0, %2\n\ts_nop 0\n\tglobal_load_lds_dwordx4 %1, off\n\ts_mov_b32 m0, %0"
               : "=&s"(keep) : "v"(gsrc), "s"(lds_dst) : "memory");
}
#define CNT_ " nt"
#define WNT_ " nt"
__device__ __forceinline__ void glds16_w(const void* gsrc, unsigned lds_dst) {
  unsigned keep;
  asm volatile("s_mov_b32 %0, m0\n\ts_mov_b32 m0, %2\n\ts_nop 0\n\tglobal_load_lds_dwordx4 %1, off" CNT_ "\n\ts_mov_b32 m0, %0"
               : "=&s"(keep) : "v"(gsrc), "s"(lds_dst) : "memory");
}
__device__ __forceinline__ void glds16_sc1(const void* gsrc, unsigned lds_dst) {
  unsigned keep;
  asm volatile("s_mov_b32 %0, m0\n\ts_mov_b32 m0, %2\n\ts_nop 0\n\tglobal_load_lds_dwordx4 %1, off sc1\n\ts_mov_b32 m0, %0"
               : "=&s"(keep) : "v"(gsrc), "s"(lds_dst) : "memory");
}
#define NTM 1
template <int B> __device__ __forceinline__ u32x4 ldnt(const void* p) { if (NTM & B) return __builtin_nontemporal_load((const u32x4*)p); else return *(const u32x4*)p; }
#define MFMA32(a, b, c) __builtin_amdgcn_mfma_f32_16x16x32_bf16((a), (b), (c), 0, 0, 0)

__device__ __forceinline__ void st16_sc1(void* p, u32x4 v) { asm volatile("global_store_dwordx4 %0, %1, off sc1\n\ts_nop 1" :: "v"(p), "v"(v) : "memory"); }
__device__ __forceinline__ void st8_sc1(void* p, u32x2 v) { asm volatile("global_store_dwordx2 %0, %1, off sc1\n\ts_nop 1" :: "v"(p), "v"(v) : "memory"); }
template <bool WT> __device__ __forceinline__ void st8(void* p, u32x2 v) { if (WT) st8_sc1(p, v); else *(u32x2*)p = v; }
template <bool WT> __device__ __forceinline__ void st16(void* p, u32x4 v) { if (WT) st16_sc1(p, v); else *(u32x4*)p = v; }
__device__ __forceinline__ u32x4 ld16_sc1(const void* p) {
  u32x4 r; asm volatile("global_load_dwordx4 %0, %1, off sc1\n\ts_waitcnt vmcnt(0)" : "=&v"(r) : "v"(p) : "memory"); return r;
}
__device__ __forceinline__ void ld16x4_sc1(const void* p, u32x4& a, u32x4& b, u32x4& c, u32x4& d) {
  asm volatile("global_load_dwordx4 %0, %4, off sc1\n\tglobal_load_dwordx4 %1, %4, off offset:1024 sc1\n\t"
               "global_load_dwordx4 %2, %4, off offset:2048 sc1\n\tglobal_load_dwordx4 %3, %4, off offset:3072 sc1\n\ts_waitcnt vmcnt(0)"
               : "=&v"(a), "=&v"(b), "=&v"(c), "=&v"(d) : "v"(p) : "memory");
}
typedef __amdgpu_buffer_rsrc_t rsrc_t;
__device__ __forceinline__ rsrc_t mkrs(const void* p) { return __builtin_amdgcn_make_buffer_rsrc((void*)p, 0, 0x7FFFFFFF, 0x00020000); }
__device__ __forceinline__ u32x4 bld16(rsrc_t r, unsigned off) { return __builtin_amdgcn_raw_buffer_load_b128(r, off, 0, 16); }
__device__ __forceinline__ u32x2 bld8(rsrc_t r, unsigned off) { return __builtin_amdgcn_raw_buffer_load_b64(r, off, 0, 16); }
__device__ __forceinline__ bool ok4(u32x4 v) { return v.x != SENT && v.y != SENT && v.z != SENT && v.w != SENT; }
constexpr int SPIN_CAP = 2000000;
__device__ __forceinline__ bool give_up(int spins, unsigned* tmo, unsigned code) {
  if ((spins & 4095) != 4095) return false;
  if (spins > SPIN_CAP) { __hip_atomic_store(tmo, code, __ATOMIC_RELAXED, __HIP_MEMORY_SCOPE_AGENT); return true; }
  return __hip_atomic_load(tmo, __ATOMIC_RELAXED, __HIP_MEMORY_SCOPE_AGENT) != 0u;
}
template <bool ACQ = true>
__device__ __forceinline__ void wait_cnt(unsigned* c, unsigned n, unsigned* tmo) {
  for (int spins = 0;; ++spins) {
    const unsigned v = __hip_atomic_load(c, __ATOMIC_RELAXED, __HIP_MEMORY_SCOPE_AGENT);
    if (v >= n) break;
    __builtin_amdgcn_s_sleep(8);
    if (give_up(spins, tmo, 5)) break;
  }
  if (ACQ) {
    __builtin_amdgcn_fence(__ATOMIC_ACQUIRE, "agent");
    asm volatile("s_waitcnt vmcnt(0)" ::: "memory");
  }
}

__device__ __forceinline__ const char* uni(const char* p) {
  const unsigned long long v = (unsigned long long)p;
  const unsigned lo = __builtin_amdgcn_readfirstlane((unsigned)v), hi = __builtin_amdgcn_readfirstlane((unsigned)(v >> 32));
  return (const char*)(((unsigned long long)hi << 32) | lo);
}
#define TSCLK "s_memrealtime"
#define TC(k) do { } while (0)
#define TS(k) do { } while (0)
#define TSDECL do { } while (0)
#define TSFLUSH(mask) do { } while (0)

constexpr size_t OFF_MOD  = 0;
constexpr size_t OFF_MOD2 = 65536;
constexpr size_t OFF_TMO  = 98304;
constexpr size_t OFF_TAG  = 131072;
constexpr size_t OFF_CLAIM = 135168;
constexpr size_t OFF_INIT = 196608;
constexpr size_t OFF_H1   = 262144;
constexpr size_t OFF_Q    = OFF_H1 + (size_t)SEQ * DM * 2;
constexpr size_t OFF_K    = OFF_Q + (size_t)SEQ * HD * 2;
constexpr size_t OFF_V    = OFF_K + (size_t)SEQ * HD * 2;
constexpr size_t OFF_O    = OFF_V + (size_t)SEQ * HD * 2;
constexpr size_t OFF_X1   = OFF_O + (size_t)SEQ * HD * 2;
constexpr size_t OFF_H2   = OFF_X1 + (size_t)SEQ * DM * 2;
constexpr size_t OFF_G    = OFF_H2 + (size_t)SEQ * DM * 2;
constexpr size_t OFF_KVM  = (size_t)92 << 20;
constexpr size_t OFF_XCH  = (size_t)100 << 20;
constexpr size_t OFF_P    = (size_t)112 << 20;
constexpr size_t OFF_P2   = (size_t)128 << 20;
constexpr size_t OFF_DUMP = (size_t)144 << 20;
constexpr int PROW = 4 * DM * 2;
#define TLQ 1
#define TLP 1
static_assert(OFF_G + (size_t)SEQ * FF * 2 <= OFF_KVM, "layout");
__device__ __forceinline__ char* xch_base(char* ws) { return (char*)(((size_t)(ws + OFF_XCH) + ((2u << 20) - 1)) & ~(size_t)((2u << 20) - 1)); }
#define XPAR 0
constexpr int PROG = 16;
constexpr int PQKV = 64;
constexpr int PL1 = 96;
constexpr int PL1B = 350;
constexpr int PSA = 128;
constexpr int PWO = 192;
constexpr int PLN = 224;
constexpr int PF2 = 320;
constexpr int PF1 = 448;
#define SDIR 40
#define SB 52
#define SC 60
#define MARG 20
#define MARL 15
#define MARF 8
#define LAST 52
#define HB 61
#define MARH 35
#define SAW 0
constexpr int L_RED = 131072;
constexpr int L_SLOT = L_RED + 16384;
constexpr int L_GB = L_SLOT + 1024;
constexpr int MEGA_LDS = 163840;
static_assert(L_GB + 12288 + 2048 <= MEGA_LDS, "lds");
#define XPARC 0

__device__ __forceinline__ int bswz(int row) { return (((row >> 1) & 1) | (((row >> 3) & 1) << 1)) << 1; }
template <int OFF, bool SC1 = false>
__device__ __forceinline__ void glds16s(const char* sbase, unsigned voff, unsigned lds_base) {
  unsigned keep;
  if (SC1)
    asm volatile("s_mov_b32 %0, m0\n\ts_add_u32 m0, %3, %4\n\ts_nop 2\n\tglobal_load_lds_dwordx4 %1, %2 sc1\n\ts_mov_b32 m0, %0"
                 : "=&s"(keep) : "v"(voff), "s"(sbase), "s"(lds_base), "i"(OFF) : "memory", "scc");
  else
    asm volatile("s_mov_b32 %0, m0\n\ts_add_u32 m0, %3, %4\n\ts_nop 2\n\tglobal_load_lds_dwordx4 %1, %2\n\ts_mov_b32 m0, %0"
                 : "=&s"(keep) : "v"(voff), "s"(sbase), "s"(lds_base), "i"(OFF) : "memory", "scc");
}
template <int OFF>
__device__ __forceinline__ void glds16x2s(const char* sbase, unsigned voff, unsigned lds_base) {
  unsigned keep;
  asm volatile("s_mov_b32 %0, m0\n\ts_add_u32 m0, %3, %4\n\ts_nop 2\n\tglobal_load_lds_dwordx4 %1, %2 sc1\n\tglobal_load_lds_dwordx4 %1, %2 offset:1024 sc1\n\ts_mov_b32 m0, %0"
               : "=&s"(keep) : "v"(voff), "s"(sbase), "s"(lds_base), "i"(OFF) : "memory", "scc");
}
__device__ __forceinline__ void st16s(const char* sb, unsigned vo, u32x4 v) { asm volatile("global_store_dwordx4 %0, %1, %2 sc1\n\ts_nop 1" :: "v"(vo), "v"(v), "s"(sb) : "memory"); }
__device__ __forceinline__ void head_to_lds(const char* src, int lane, unsigned lds) {
  if (TLQ) {
    const int c = (lane & 15) ^ (lane >> 4);
    const unsigned v0 = (unsigned)((c >> 2) * 2048 + (lane >> 4) * 64 + (c & 3) * 16);
#pragma unroll
    for (int i = 0; i < 8; ++i) glds16s<0, true>(src + i * 256, v0 ^ ((i & 3) * 2048), lds + i * 1024);
  } else {
#pragma unroll
    for (int i = 0; i < 4; ++i) glds16x2s<0>(src + i * 2048, (unsigned)lane * 16, lds + i * 2048);
  }
}
#define TCHP ""
__device__ __forceinline__ void touch(const void* sb, unsigned vo, unsigned lds) {
  asm volatile("s_mov_b32 m0, %0\n\ts_nop 0\n\tglobal_load_lds_dword %1, %2" TCHP :: "s"(lds), "v"(vo), "s"(sb) : "memory", "m0");
}
#define WAIT_V(n) asm volatile("s_waitcnt vmcnt(" #n ")" ::: "memory")
#define BAR() do { asm volatile("s_waitcnt lgkmcnt(0)" ::: "memory"); __builtin_amdgcn_s_barrier(); asm volatile("" ::: "memory"); } while (0)
__device__ __forceinline__ void st4_sc1(void* p, unsigned v) { asm volatile("global_store_dword %0, %1, off sc1" :: "v"(p), "v"(v) : "memory"); }
__device__ __forceinline__ unsigned ld4_sc1(const void* p) {
  unsigned r; asm volatile("global_load_dword %0, %1, off sc1\n\ts_waitcnt vmcnt(0)" : "=&v"(r) : "v"(p) : "memory"); return r;
}
template <int CTRL> __device__ __forceinline__ unsigned dppu(unsigned x) { return (unsigned)__builtin_amdgcn_update_dpp(0, (int)x, CTRL, 0xf, 0xf, false); }
__device__ __forceinline__ unsigned rowmin16(unsigned v) {
  v = min(v, dppu<0x128>(v)); v = min(v, dppu<0x124>(v)); v = min(v, dppu<0x122>(v)); v = min(v, dppu<0x121>(v));
  return v;
}
__device__ __forceinline__ unsigned rdl(unsigned v, int l) { return (unsigned)__builtin_amdgcn_readlane((int)v, l); }
__device__ __forceinline__ unsigned qkv_avail(unsigned v, int m) {
  unsigned a = rdl(v, m);
#pragma unroll
  for (int j = 1; j < 4; ++j) { const unsigned p = rdl(v, 4 * j + m); if ((p >> 8) <= a) a = max(a, p & 255u); }
  return a;
}
constexpr float SCALE = 0.08838834764831845f;
constexpr float LOG2E = 1.4426950408889634f;
template <int N> __device__ __forceinline__ void delay64() {
  if (N >= 16) { __builtin_amdgcn_s_sleep(15); delay64<(N >= 16 ? N - 15 : 0)>(); } else if (N > 0) __builtin_amdgcn_s_sleep(N);
}
__device__ __forceinline__ void sw16(float x, float& a, float& b) {
  a = x; b = x; asm volatile("s_nop 1\n\tv_permlane16_swap_b32 %0, %1" : "+v"(a), "+v"(b));
}
__device__ __forceinline__ void sw32(float x, float& a, float& b) {
  a = x; b = x; asm volatile("s_nop 1\n\tv_permlane32_swap_b32 %0, %1" : "+v"(a), "+v"(b));
}
__device__ __forceinline__ float red16_32_max(float x) {
  float a, b; sw16(x, a, b); x = fmaxf(a, b); sw32(x, a, b); return fmaxf(a, b);
}
__device__ __forceinline__ float red16_32_add(float x) {
  float a, b; sw16(x, a, b); x = a + b; sw32(x, a, b); return a + b;
}
__device__ __forceinline__ unsigned cvtpk(float a, float b) { unsigned r; asm("v_cvt_pk_bf16_f32 %0, %1, %2" : "=v"(r) : "v"(a), "v"(b)); return r; }
__device__ __forceinline__ float max3(float a, float b, float c) { float r; asm("v_max3_f32 %0, %1, %2, %3" : "=v"(r) : "v"(a), "v"(b), "v"(c)); return r; }
__device__ __forceinline__ float max2(float a, float b) { float r; asm("v_max_f32 %0, %1, %2" : "=v"(r) : "v"(a), "v"(b)); return r; }
__device__ __forceinline__ unsigned ror8u(unsigned x) { return (unsigned)__builtin_amdgcn_update_dpp(0, (int)x, 0x128, 0xf, 0xf, false); }
__device__ __forceinline__ float ror8(float x) { return __builtin_bit_cast(float, ror8u(__builtin_bit_cast(unsigned, x))); }
__device__ __forceinline__ float add_ror8(float x) {
  return x + __builtin_bit_cast(float, __builtin_amdgcn_update_dpp(0, __builtin_bit_cast(int, x), 0x128, 0xf, 0xf, false));
}

__device__ __forceinline__ unsigned rt_now() { return (unsigned)__builtin_amdgcn_s_memrealtime(); }
__device__ __forceinline__ unsigned latest(unsigned v) {
  const unsigned now = rt_now() + 400u, age = rowmin16((now - v) & 0xFFFFFFu);
  return now - min(min(min(rdl(age, 0), rdl(age, 16)), min(rdl(age, 32), rdl(age, 48))), 900u);
}
__device__ __forceinline__ void wait_rt(unsigned t0, int d) {
  while ((int)(rt_now() - t0) < d) { }
}
__device__ __forceinline__ bool bells(bool ok, unsigned u, int b, int marg, int sb = SB) {
  if (b < sb || !__all(ok || (int)(u >> 24) > b)) return false;
  wait_rt(latest(!ok && (int)(u >> 24) == b + 1 ? u : rt_now() - 400u), marg);
  return true;
}
__device__ __forceinline__ void ld_fb(unsigned& v, unsigned& u, const void* sb, unsigned vo) {
  asm volatile("global_load_dword %0, %2, %3 sc1\n\tglobal_load_dword %1, %2, %3 offset:64 sc1\n\ts_waitcnt vmcnt(0)" : "=&v"(v), "=&v"(u) : "v"(vo), "s"(sb) : "memory");
}
__device__ __forceinline__ unsigned bell(int b, unsigned t) { return (unsigned)(b + 1) << 24 | (t & 0xFFFFFFu); }
typedef unsigned long long u64;
__device__ __forceinline__ unsigned tag2(u64 tag) { return (unsigned)(tag >> 32) ^ ((unsigned)tag * 0x9E3779B1u); }
__device__ __forceinline__ unsigned init_eval(u32x4 v, u64 tag) {
  if (!__all((v.x & ~63u) == (unsigned)tag << 6 && v.y == (unsigned)(tag >> 26))) return 0u;
  unsigned r = 8u;
#pragma unroll
  for (int c = 0; c < 3; ++c) r |= __popcll(__ballot(v.x & (1u << c))) > __popcll(__ballot(v.x & (8u << c))) ? 1u << c : 0u;
  return r ^ (XPAR * 3u + XPARC * 4u);
}
__device__ __forceinline__ unsigned init_wait(const char* ws, u64 tag) {
  for (int spins = 0; spins < SPIN_CAP; ++spins) {
    const unsigned r = init_eval(ld16_sc1(ws + OFF_INIT + (lane_id() & 31) * 128), tag);
    if (r) return r;
    __builtin_amdgcn_s_sleep(4);
  }
  return 8u;
}
__device__ __forceinline__ void init_issue(const char* ws, unsigned lds) { glds16s<0, true>(uni(ws + OFF_INIT), (unsigned)(lane_id() & 31) * 128u, lds); }
__device__ __forceinline__ unsigned init_get(const char* ws, u64 tag, const char* copy) {
  const unsigned r = init_eval(*(volatile LAS u32x4*)(copy + lane_id() * 16), tag);
  return r ? r : init_wait(ws, tag);
}
__device__ __forceinline__ unsigned* cnt_of(char* ws, unsigned in) { return (unsigned*)uni(xch_base(ws) + ((size_t)4 << 21) + (size_t)((in & 4u) << 11)); }
__device__ __forceinline__ void wait_l1(const unsigned* cnt, unsigned n, unsigned* tmo) {
  const int lane = lane_id();
  for (int spins = 0;; ++spins) {
    const unsigned v = ld4_sc1(cnt + (PL1 + (lane & 31)) * 32);
    if (__all(v >= n)) break;
    __builtin_amdgcn_s_sleep(8);
    if (give_up(spins, tmo, 12)) break;
  }
}
#define GKV 1
__device__ __forceinline__ void wait_k0(const unsigned* cnt, int h, unsigned* tmo) {
  for (int spins = 0;; ++spins) {
    const int l = lane_id();
    const unsigned v = ld4_sc1(cnt + (PQKV + h) * 32 + 1 + (l & 1));
    if (__all(v >= (unsigned)GKV)) break;
    __builtin_amdgcn_s_sleep(32);
    if (give_up(spins, tmo, 13)) break;
  }
}
#define WCLK(slot, b) do { } while (0)
#define WTM(slot, b, k) do { } while (0)
#define WTS(slot, b, k) do { } while (0)
#define WFB() do { } while (0)
#define STS(ws_, slot_, k_) do { } while (0)
typedef __bf16 bf16x2 __attribute__((ext_vector_type(2)));
__device__ __forceinline__ f32x2 pkfma(f32x2 a, f32x2 b, f32x2 c) { return __builtin_elementwise_fma(a, b, c); }
__device__ __forceinline__ float wavesum(float v) {
  v += __builtin_bit_cast(float, dppu<0x128>(__builtin_bit_cast(unsigned, v))); v += __builtin_bit_cast(float, dppu<0x124>(__builtin_bit_cast(unsigned, v)));
  v += __builtin_bit_cast(float, dppu<0x122>(__builtin_bit_cast(unsigned, v))); v += __builtin_bit_cast(float, dppu<0x121>(__builtin_bit_cast(unsigned, v)));
  return red16_32_add(v);
}
__device__ __forceinline__ float adds(unsigned v, unsigned sel, float c) { return __builtin_amdgcn_fdot2_f32_bf16(__builtin_bit_cast(bf16x2, v), __builtin_bit_cast(bf16x2, sel), c, false); }
__device__ __forceinline__ void sum4(const u32x4 (&p)[4], f32x2 (&a)[4]) {
  unsigned slo = 0x00003F80u, shi = 0x3F800000u;
  asm("" : "+s"(slo), "+s"(shi));
#pragma unroll
  for (int e = 0; e < 4; ++e) {
    float lo = bflo(p[0][e]), hi = bfhi(p[0][e]);
#pragma unroll
    for (int q = 1; q < 4; ++q) { lo = adds(p[q][e], slo, lo); hi = adds(p[q][e], shi, hi); }
    a[e] = f32x2{lo, hi};
  }
}
__device__ __forceinline__ u32x4 norm8(const f32x2 (&v)[4], const char* mt, float mb, float rden) {
  const f32x4 h0 = *(const f32x4*)(mt + 4096), h1 = *(const f32x4*)(mt + 4096 + 1024), c0 = *(const f32x4*)(mt + 8192), c1 = *(const f32x4*)(mt + 8192 + 1024);
  const f32x2 m2 = f32x2{mb, mb}, d2 = f32x2{rden, rden};
  const f32x2 r0 = pkfma(v[0] - m2, f32x2{c0[0], c0[1]} * d2, f32x2{h0[0], h0[1]}), r1 = pkfma(v[1] - m2, f32x2{c0[2], c0[3]} * d2, f32x2{h0[2], h0[3]});
  const f32x2 r2 = pkfma(v[2] - m2, f32x2{c1[0], c1[1]} * d2, f32x2{h1[0], h1[1]}), r3 = pkfma(v[3] - m2, f32x2{c1[2], c1[3]} * d2, f32x2{h1[2], h1[3]});
  return u32x4{pack2(r0[0], r0[1]), pack2(r1[0], r1[1]), pack2(r2[0], r2[1]), pack2(r3[0], r3[1])};
}
__device__ __forceinline__ void half_load(const char* P, const u16* res, int row, int hf, int lane, f32x2 (&a)[4], u32x4& rv) {
  const rsrc_t rp = mkrs(P), rr = mkrs(res);
  const unsigned po = TLP ? (unsigned)((row >> 5) * 32 * PROW + (hf * 16 + (lane >> 2)) * 2048 + (row & 31) * 64 + (lane & 3) * 16) : (unsigned)(row * PROW + hf * 1024 + lane * 16), ro = (unsigned)(row * DM * 2 + hf * 1024 + lane * 16);
  u32x4 p[4];
#pragma unroll
  for (int q = 0; q < 4; ++q) p[q] = bld16(rp, po + q * (TLP ? 65536 : 2048));
  if (res) rv = bld16(rr, ro);
  sum4(p, a);
}
__device__ __forceinline__ bool fin_half(bool fin, const char* P, const u16* res, const char* gbt, u16* __restrict__ o1, int row, int hf, int lane) {
  f32x2 a[4]; u32x4 rv;
  half_load(P, res, row, hf, lane, a, rv);
  const u32x4 gv = *(const u32x4*)(gbt + hf * 1024 + lane * 16);
  u32x4 o; f32x2 t = f32x2{0, 0};
#pragma unroll
  for (int e = 0; e < 4; ++e) { const f32x2 s = pkfma(f32x2{bflo(gv[e]), bfhi(gv[e])}, a[e], f32x2{bflo(rv[e]), bfhi(rv[e])}); o[e] = pack2(s[0], s[1]); t += s; }
  if (!fin && !__all(t[0] == t[0] && t[1] == t[1])) return false;
  st16_sc1(o1 + (size_t)row * DM + hf * 512 + lane * 8, o);
  return true;
}
#define DLN 10
#define LEB 64
#define MARE 25
#define LNE 1
template <int CTRL> __device__ __forceinline__ float dppf(float x) { return __builtin_bit_cast(float, dppu<CTRL>(__builtin_bit_cast(unsigned, x))); }
__device__ __forceinline__ bool ln_row(bool fin, int bb, unsigned* bl, const f32x2 (&a)[2][4], const f32x2 (&xf)[8], const f32x2 (&tg)[8], const f32x2 (&th)[8], const f32x2 (&tc)[8],
                                       const char* x1, const char* h2, unsigned xo, unsigned ho) {
  f32x2 v[8]; u32x4 o[2];
#pragma unroll
  for (int e = 0; e < 8; ++e) { const f32x2 t = pkfma(tg[e], a[e >> 2][e & 3], xf[e]); const unsigned u = pack2(t[0], t[1]); o[e >> 2][e & 3] = u; v[e] = f32x2{bflo(u), bfhi(u)}; }
  const float pv = __builtin_bit_cast(float, __builtin_amdgcn_readfirstlane(__float_as_int(v[0][0])));
  f32x2 d[8];
#pragma unroll
  for (int e = 0; e < 8; ++e) d[e] = v[e] - f32x2{pv, pv};
  const f32x2 s2 = ((d[0] + d[1]) + (d[2] + d[3])) + ((d[4] + d[5]) + (d[6] + d[7]));
  const f32x2 q2 = pkfma(d[3], d[3], pkfma(d[2], d[2], pkfma(d[1], d[1], d[0] * d[0]))) + pkfma(d[7], d[7], pkfma(d[6], d[6], pkfma(d[5], d[5], d[4] * d[4])));
  float s = s2[0] + s2[1], q = q2[0] + q2[1];
  if (!fin && !__all(s == s)) return false;
  if (LNE && bb >= SB && bl && lane_id() == 0) st4_sc1(bl, bell(bb, rt_now() + DLN + 30));
  s += dppf<0x128>(s); q += dppf<0x128>(q); s += dppf<0x124>(s); q += dppf<0x124>(q);
  s += dppf<0x122>(s); q += dppf<0x122>(q); s += dppf<0x121>(s); q += dppf<0x121>(q);
  asm volatile("s_nop 1\n\tv_permlane16_swap_b32 %0, %1" : "+v"(s), "+v"(q));
  s += q;
  { float x, y; sw32(s, x, y); s = x + y; }
  const float sh = __builtin_bit_cast(float, rdl(__builtin_bit_cast(unsigned, s), 0)) * (1.0f / DM), sq = __builtin_bit_cast(float, rdl(__builtin_bit_cast(unsigned, s), 16));
  const float mb = bfr(pv + sh), vb = bfr(fmaxf(sq * (1.0f / DM) - sh * sh, 0.0f));
  const float rden = __builtin_amdgcn_rcpf(bfr(__builtin_amdgcn_sqrtf(bfr(vb + 1e-5f))));
#pragma unroll
  for (int i = 0; i < 2; ++i) {
    u32x4 r;
#pragma unroll
    for (int e = 0; e < 4; ++e) { const f32x2 t = pkfma(v[4 * i + e] - f32x2{mb, mb}, tc[4 * i + e] * f32x2{rden, rden}, th[4 * i + e]); r[e] = pack2(t[0], t[1]); }
    st16s(h2, ho + i * 32768, r);
  }
  if (!LNE && bb >= SB && lane_id() == 0) st4_sc1(bl, bell(bb, rt_now() + DLN));
  st16s(x1, xo, o[0]); st16s(x1, xo + 1024, o[1]);
  return true;
}
__device__ __forceinline__ void row_table(char* tab, const float* mod, const unsigned* cnt, unsigned* tmo, int tid) {
  wait_l1(cnt + 1, 1u, tmo);
  const rsrc_t rm = mkrs((const char*)mod + OFF_MOD2);
#pragma unroll
  for (int i = 0; i < 2; ++i) {
    const int u = tid + 512 * i, a = u >> 8, c = u & 255;
    f32x4 v = __builtin_bit_cast(f32x4, bld16(rm, (unsigned)u * 16));
    v = f32x4{bfr(v[0]), bfr(v[1]), bfr(v[2]), bfr(v[3])};
    if (a == 2) v = f32x4{bfr(1.0f + v[0]), bfr(1.0f + v[1]), bfr(1.0f + v[2]), bfr(1.0f + v[3])};
    if (a < 3) *(f32x4*)(tab + a * 4096 + ((c >> 7) * 2 + (c & 1)) * 1024 + ((c >> 1) & 63) * 16) = v;
    else *(u32x2*)(tab + 12288 + c * 8) = u32x2{pack2(v[0], v[1]), pack2(v[2], v[3])};
  }
  __syncthreads();
}
__device__ __forceinline__ void row_role(char* smem, int wave_id_, int rbase, int par, const float* mod, const char* P, const u16* x, u16* x1, char* h2c,
                                         unsigned* cnt, unsigned* tmo, int slot, unsigned long long* mts) {
  const int tid = opaque_tid(), lane = tid & 63, wave = WAVE_ID;
  if (tid < 80) *(volatile LAS unsigned*)(smem + 16384 + tid * 4) = 0u;
  {
    static_assert(TLP && SB % 2 == 0, "");
    unsigned z = SENT; asm volatile("" : "+v"(z));
    const int r4 = rbase + 4 * par + (wave & 3);
    for (int bb = SB + (wave >> 2); bb < NSEG; bb += 2)
#pragma unroll
      for (int i = 0; i < 2; ++i) {
        st16s(uni((const char*)x1 + (size_t)bb * 65536), (unsigned)(r4 * DM * 2 + i * 1024 + lane * 16), u32x4{z, z, z, z});
        st16s(uni(h2c + (size_t)bb * 65536), (unsigned)(r4 * 128 + (lane & 7) * 16 + (lane >> 3) * 4096 + i * 32768), u32x4{z, z, z, z});
      }
    WAIT_V(0);
  }
  row_table(smem, mod, cnt, tmo, tid);
  const int rb = rbase + wave + 32 * par;
  const rsrc_t rp = mkrs(P), rr = mkrs(x);
  const unsigned l16 = lane * 16;
  const unsigned pwo = (unsigned)(PWO + (lane & 31)) * 128;
  unsigned* myp = cnt + (PLN + (rb >> 3)) * 32 + wave;
  f32x2 a[2][4];
  const int gp = wave >> 2, r4 = rbase + 4 * par + (wave & 3);
  volatile LAS unsigned* lwb = (volatile LAS unsigned*)(smem + 16384 + gp * 8);
  if (gp && lane == 0) st4_sc1(myp, 1u);
  f32x2 tg[8], th[8], tc[8];
#pragma unroll
  for (int i = 0; i < 4; ++i) {
    const char* mt = smem + lane * 16 + i * 1024;
    const f32x4 g_ = *(const f32x4*)mt, h_ = *(const f32x4*)(mt + 4096), c_ = *(const f32x4*)(mt + 8192);
    tg[2 * i] = f32x2{g_[0], g_[1]}; tg[2 * i + 1] = f32x2{g_[2], g_[3]}; th[2 * i] = f32x2{h_[0], h_[1]}; th[2 * i + 1] = f32x2{h_[2], h_[3]};
    tc[2 * i] = f32x2{c_[0], c_[1]}; tc[2 * i + 1] = f32x2{c_[2], c_[3]};
  }
  const unsigned xo = (unsigned)(r4 * DM * 2 + lane * 16), ho = (unsigned)(r4 * 128 + (((lane & 7) ^ (r4 & 7)) << 4) + (lane >> 3) * 4096);
  const unsigned po = (l16 >> 6) * 2048 + (l16 & 48);
  u32x4 rv[2];
#pragma unroll
  for (int i = 0; i < 2; ++i) rv[i] = __builtin_amdgcn_raw_buffer_load_b128(rr, xo + i * 1024, (unsigned)gp * 65536u, 0);
  unsigned wb = 0, d0 = 150, d1 = 150;
  for (int bb = gp; bb < NSEG; bb += 2) {
    f32x2 xf[8];
#pragma unroll
    for (int e = 0; e < 8; ++e) xf[e] = f32x2{bflo(rv[e >> 2][e & 3]), bfhi(rv[e >> 2][e & 3])};
    asm volatile("" : "+v"(xf[0]), "+v"(xf[1]), "+v"(xf[2]), "+v"(xf[3]), "+v"(xf[4]), "+v"(xf[5]), "+v"(xf[6]), "+v"(xf[7]));
    if (wb <= (unsigned)bb) {
      if ((wave & 3) == 0) {
        wb = (unsigned)(bb + 1);
        for (int spins = 0;; ++spins) {
          unsigned v, u;
          ld_fb(v, u, cnt, pwo);
          if (__all(v > (unsigned)bb + 2u)) { v = rowmin16(v); wb = min(rdl(v, 0), rdl(v, 16)); break; }
          if (__all(v > (unsigned)bb)) break;
          if (bb >= LEB && __all(v > (unsigned)bb || (u >> 24) > (unsigned)bb)) {
            const unsigned now = rt_now(), g0 = latest(v <= (unsigned)bb && (u >> 24) == (unsigned)bb + 1u ? u : now - 400u) + MARE, go = (int)(g0 - now) > 0 ? g0 : now;
            if (lane < 4) st4_sc1(myp + 16 + lane, bell(bb, go + max(d0, d1)));
            wait_rt(go, 0);
            wb |= 256u;
            break;
          }
          if (bells(v > (unsigned)bb, u, bb, MARL)) break;
          __builtin_amdgcn_s_sleep(2);
          if (give_up(spins, tmo, 11)) break;
        }
        lwb[0] = wb;
      } else {
        while (((wb = __builtin_amdgcn_readfirstlane(lwb[0])) & 255u) <= (unsigned)bb) __builtin_amdgcn_s_sleep(1);
      }
    }
    const bool rung = wb == (unsigned)bb + 257u;
    wb &= 255u;
    const unsigned t0 = rt_now();
    WTM(slot, bb, 0);
    bool fin = bb < SB;
    for (int spins = 0;; ++spins) {
      u32x4 pp[2][4];
#pragma unroll
      for (int i = 0; i < 2; ++i)
#pragma unroll
        for (int q = 0; q < 4; ++q) pp[i][q] = __builtin_amdgcn_raw_buffer_load_b128(rp, po + i * 32768, (unsigned)(bb * 32 * PROW + q * 65536 + r4 * 64), 16);
      sum4(pp[0], a[0]); sum4(pp[1], a[1]);
      if (ln_row(fin, bb, rung && spins == 0 ? nullptr : myp + 16, a, xf, tg, th, tc, uni((const char*)x1 + (size_t)bb * 65536), uni(h2c + (size_t)bb * 65536), xo, ho)) break;
      fin = __all(ld4_sc1((const char*)cnt + pwo) > (unsigned)bb) || give_up(spins, tmo, 11);
    }
    d1 = d0; d0 = min(rt_now() - t0 + 12u, 200u);
    WTM(slot, bb, 3);
#pragma unroll
    for (int i = 0; i < 2; ++i) rv[i] = __builtin_amdgcn_raw_buffer_load_b128(rr, xo + i * 1024, (unsigned)min(bb + 2, NSEG - 1) * 65536u, 0);
    WAIT_V(2);
    unsigned o = 0;
    if (lane == 0) o = __hip_atomic_fetch_add((LAS unsigned*)(smem + 16448 + bb * 4), 1u, __ATOMIC_RELAXED, __HIP_MEMORY_SCOPE_WORKGROUP);
    if (__builtin_amdgcn_readfirstlane(o) == 3u) {
      if (lane < 4) st4_sc1(myp - (wave & 3) + lane, (unsigned)(bb + 2));
      WTM(slot, bb, 2);
    }
  }
}
#define CALF 1
#define NCS 6
__device__ __forceinline__ void l1_role(char* smem, int wave_id_, bool fst, int c, const u16* __restrict__ x, const float* __restrict__ mod,
                                        const u16* __restrict__ mem0, char* __restrict__ h1c, unsigned*& cnt, u64 tag, unsigned* tmo, unsigned long long* mts) {
  const int tid = opaque_tid(), lane = tid & 63, wave = WAVE_ID;
  char* ws = (char*)mod - OFF_MOD;
  u32x4 xr[7][2];
  const int row0 = (fst ? 0 : 256) + 8 * c + wave;
  const u16* src = x + (size_t)row0 * DM + lane * 8;
  xr[0][0] = ldnt<2>(src); xr[0][1] = ldnt<2>(src + 512);
  volatile LAS unsigned* sg = (volatile LAS unsigned*)(smem + L_SLOT + 784);
  if (!fst) {
#pragma unroll
    for (int pp = 1; pp < 7; ++pp) { xr[pp][0] = ldnt<2>(src + (size_t)pp * 256 * DM); xr[pp][1] = ldnt<2>(src + (size_t)pp * 256 * DM + 512); }
  } else if (wave >= 5) {
    char* cal = xch_base(ws) + ((size_t)(wave == 5 ? 0 : wave - 3) << 21) + (126 + (c >> 4)) * 16384 + (c & 15) * 512;
    st16_sc1(cal + (lane >> 5) * 8192 + (lane & 31) * 16, u32x4{SENT, SENT, SENT, SENT});
    WAIT_V(0);
    unsigned best[2] = {1u << 30, 1u << 30};
    const char* sb = uni(cal);
#pragma unroll
    for (int r = 1; r < NCS; ++r) {
      unsigned long long t0, t1; unsigned v;
      asm volatile("s_memtime %0\n\ts_waitcnt lgkmcnt(0)\n\ts_load_dword %2, %3, %4 glc\n\ts_waitcnt lgkmcnt(0)\n\ts_memtime %1\n\ts_waitcnt lgkmcnt(0)"
                   : "=&s"(t0), "=&s"(t1), "=&s"(v) : "s"(sb), "i"((r & 1) * 8192 + (r >> 1) * 128) : "memory");
      if (r > 1) best[r & 1] = min(best[r & 1], (unsigned)(t1 - t0));
    }
    unsigned xcc; asm volatile("s_getreg_b32 %0, hwreg(HW_REG_XCC_ID)" : "=s"(xcc));
    const unsigned df = max(best[0], best[1]) - min(best[0], best[1]);
    sg[wave - 5] = df < 110u || df > 400u ? 0u : (((best[1] < best[0]) != ((xcc & 4) != 0)) ? 1u : 8u) << (wave - 5);
  }
  if (CALF) BAR();
  if (!fst || wave >= 5) {
  } else if (wave < 3) {
    const int b = c + 32 * wave;
    if (b < 89) {
      const char* p = uni(xch_base(ws) + (b < 48 ? (size_t)((b >> 4) * 32 + (b & 15)) * 16384
                                        : b < 56 ? ((size_t)3 << 21) + (size_t)(8 + ((b - 48) >> 2) * 8 + (b & 3)) * 16384
                                        : b < 84 ? ((size_t)3 << 21) + (size_t)(b - 56 + 24) * 16384 : ((size_t)4 << 21) + (size_t)(b - 84) * 16384));
      const unsigned v = b < 80 ? SENT : 0u;
#pragma unroll
      for (int i = 0; i < 16; ++i) st16s(p, (unsigned)(i * 1024 + lane * 16), u32x4{v, v, v, v});
    } else if (b == 89 && lane == 0) st4_sc1(tmo, 0u);
    WAIT_V(0);
  } else {
    const int pc = (c & 7) * 2 + (wave & 1), g = lane >> 4, hf = (lane >> 2) & 1, row = ((lane >> 3) & 1) * 4 + (lane & 3);
    u32x4 v = *(const u32x4*)(mem0 + row * DM + 32 * (2 * pc + hf) + 8 * g);
    v.x = nosent(v.x); v.y = nosent(v.y); v.z = nosent(v.z); v.w = nosent(v.w);
    st16_sc1(xch_base(ws) + ((size_t)3 << 21) + (size_t)(((c >> 3) & 1) * 2 + (pc >> 3)) * 16384 + (c >> 4) * 8192 + (pc & 7) * 1024 + lane * 16, v);
    WAIT_V(0);
  }
  BAR();
  if (fst && tid == 0) st8_sc1(ws + OFF_INIT + c * 128, u32x2{(unsigned)tag << 6 | sg[0] | sg[1] | sg[2], (unsigned)(tag >> 26)});
  if (fst && wave == 0) STS(ws, 128 + (c >> 3) * 32 + 24 + (c & 7), 2);
  {
    f32x4 v;
    for (int spins = 0; spins < SPIN_CAP; ++spins) {
      u32x4 m[4];
      asm volatile("s_nop 4\n\tglobal_load_dwordx4 %0, %4, %6 sc1\n\tglobal_load_dwordx4 %1, %4, %6 offset:16 sc1\n\t"
                   "global_load_dwordx4 %2, %5, %6 sc1\n\tglobal_load_dwordx4 %3, %5, %6 offset:16 sc1\n\ts_waitcnt vmcnt(0)"
                   : "=&v"(m[0]), "=&v"(m[1]), "=&v"(m[2]), "=&v"(m[3]) : "v"((unsigned)tid * 32), "v"((unsigned)tid * 32 + 16384), "s"(mod) : "memory");
      bool ok = true;
#pragma unroll
      for (int i = 0; i < 4; ++i) ok = ok && m[i].y == (unsigned)tag && m[i].w == tag2(tag);
      v = f32x4{__uint_as_float(m[0].x) + __uint_as_float(m[2].x), __uint_as_float(m[0].z) + __uint_as_float(m[2].z),
                __uint_as_float(m[1].x) + __uint_as_float(m[3].x), __uint_as_float(m[1].z) + __uint_as_float(m[3].z)};
      if (__all(ok)) break;
      __builtin_amdgcn_s_sleep(4);
    }
    v = f32x4{bfr(v[0]), bfr(v[1]), bfr(v[2]), bfr(v[3])};
    const int a = tid >> 8, cc = tid & 255;
    if (a) v = f32x4{bfr(1.0f + v[0]), bfr(1.0f + v[1]), bfr(1.0f + v[2]), bfr(1.0f + v[3])};
    *(f32x4*)(smem + 4096 + a * 4096 + ((cc >> 7) * 2 + (cc & 1)) * 1024 + ((cc >> 1) & 63) * 16) = v;
  }
  BAR();
  if (fst && wave == 0) STS(ws, 128 + (c >> 3) * 32 + 24 + (c & 7), 3);
#pragma unroll
  for (int p = 0; p < 7; ++p) {
    if (p > 0 && fst) break;
    f32x2 v[2][4];
#pragma unroll
    for (int i = 0; i < 2; ++i)
#pragma unroll
      for (int e = 0; e < 4; ++e) v[i][e] = f32x2{bflo(xr[p][i][e]), bfhi(xr[p][i][e])};
    const f32x2 s2 = ((v[0][0] + v[0][1]) + (v[0][2] + v[0][3])) + ((v[1][0] + v[1][1]) + (v[1][2] + v[1][3]));
    const float mean = wavesum(s2[0] + s2[1]) * (1.0f / DM);
    f32x2 q2 = f32x2{0, 0};
#pragma unroll
    for (int i = 0; i < 8; ++i) { const f32x2 d = v[i >> 2][i & 3] - f32x2{mean, mean}; q2 = pkfma(d, d, q2); }
    const float q = wavesum(q2[0] + q2[1]);
    if (p == 0 && fst && wave == 0) STS((char*)tmo - OFF_TMO, 128 + (c >> 3) * 32 + 24 + (c & 7), 5);
    const float mb = bfr(mean), vb = bfr(q * (1.0f / DM));
    const float rden = 1.0f / bfr(sqrtf(bfr(vb + 1e-5f)));
    const int row = 256 * p + row0, rb = row & 31;
    char* dst = h1c + (size_t)(row >> 5) * 65536 + (lane >> 3) * 4096 + rb * 128 + (((lane & 7) ^ (rb & 7)) << 4);
    const char* ml = smem + lane * 16;
#pragma unroll
    for (int i = 0; i < 2; ++i) st16_sc1(dst + i * 32768, norm8(v[i], ml + i * 2048, mb, rden));
  }
  cnt = cnt_of(ws, init_wait(ws, tag));
  WAIT_V(0);
  __builtin_amdgcn_s_barrier();
  if (tid == 0) st4_sc1(cnt + ((fst ? PL1 : PL1B) + c) * 32, fst ? 8u : 7u);
  if (wave == 0) STS(ws, 128 + (c >> 3) * 32 + 24 + (c & 7), fst ? 6 : 7);
}
__device__ __forceinline__ void mod2_job(char* smem, int wave_id_, int c, const float* __restrict__ mod, const u16* __restrict__ cvec, const u16* __restrict__ wmod, unsigned* cnt) {
  const int tid = opaque_tid();
  {
    const float sva = bf2f(cvec[tid]), svb = bf2f(cvec[tid + 512]);
    float* sv = (float*)(smem + 12288);
    sv[tid] = bfr(sva / (1.0f + __expf(-sva))); sv[tid + 512] = bfr(svb / (1.0f + __expf(-svb)));
  }
  __syncthreads();
  {
    const int cg = tid & 15, r0 = tid >> 4;
    const u16* wp = wmod + (size_t)r0 * (6 * DM) + 2 * DM + 128 * c + cg * 8;
    const float* sv = (const float*)(smem + 12288) + r0;
    f32x2 acc[4] = {f32x2{0, 0}, f32x2{0, 0}, f32x2{0, 0}, f32x2{0, 0}};
#pragma unroll 1
    for (int o = 0; o < 4; ++o) {
      u32x4 w[8];
#pragma unroll
      for (int i = 0; i < 8; ++i) w[i] = ldnt<1>(wp + (size_t)(o * 8 + i) * 32 * (6 * DM));
#pragma unroll
      for (int i = 0; i < 8; ++i) {
        const float s = sv[(o * 8 + i) * 32];
#pragma unroll
        for (int e = 0; e < 4; ++e) acc[e] = pkfma(f32x2{s, s}, f32x2{bflo(w[i][e]), bfhi(w[i][e])}, acc[e]);
      }
    }
    float* red = (float*)(smem + 16384);
    *(f32x4*)(red + r0 * 128 + cg * 8) = f32x4{acc[0][0], acc[0][1], acc[1][0], acc[1][1]};
    *(f32x4*)(red + r0 * 128 + cg * 8 + 4) = f32x4{acc[2][0], acc[2][1], acc[3][0], acc[3][1]};
    __syncthreads();
    if (tid < 128) {
      float s = 0;
#pragma unroll
      for (int r = 0; r < 32; ++r) s += red[r * 128 + tid];
      st4_sc1((char*)mod + OFF_MOD2 + (128 * c + tid) * 4, __builtin_bit_cast(unsigned, s));
    }
    WAIT_V(0);
    __builtin_amdgcn_s_barrier();
    if (tid == 0) st4_sc1(cnt + (PL1 + c) * 32 + 1, 1u);
  }
}
enum { R_QKV = 0, R_PART = 1, R_F1 = 2, R_PART2 = 3 };
struct Dep {
  const char* base; unsigned voff; unsigned mul, add;
};
__device__ __forceinline__ unsigned dep_eval(unsigned v, const Dep& d) {
  v = rowmin16(v * d.mul + (d.add + 64u));
  return min(min(rdl(v, 0), rdl(v, 16)), min(rdl(v, 32), rdl(v, 48))) - 64u;
}
typedef unsigned u32x32 __attribute__((ext_vector_type(32)));
typedef unsigned u32x16 __attribute__((ext_vector_type(16)));
__device__ __forceinline__ void fast_ld0(const u32x32 (&W)[4], u32x16& pd, unsigned ea, unsigned da, int& b, int& lf, int cp, unsigned up, unsigned sa, unsigned mu, unsigned ad,
    unsigned rw, unsigned ab, const char* p1, const char* p0, unsigned l1, unsigned l0, const unsigned (&av)[8], int la) {
  asm volatile("s_setprio 0\n\t"
    "s_cmp_eq_u32 %[la], 3\n\t"
    "s_waitcnt vmcnt(12)\n\t"
    "s_cbranch_scc1 20f\n\t"
    "s_waitcnt vmcnt(4)\n\t"
    "20:\n\t"
    "s_barrier\n\t"
    "ds_read_b128 v[96:99], %[ea] offset:0\n\t"
    "ds_read_b128 v[100:103], %[ea] offset:2048\n\t"
    "ds_read_b128 v[104:107], %[da] offset:0\n\t"
    "ds_read_b128 v[108:111], %[da] offset:2048\n\t"
    "ds_read_b128 v[112:115], %[ea] offset:4096\n\t"
    "ds_read_b128 v[116:119], %[ea] offset:6144\n\t"
    "s_branch 2f\n\t"
    ".p2align 6\n\t"
    "1:\n\t"
    "s_cmp_eq_u32 %[la], 3\n\t"
    "s_waitcnt vmcnt(12)\n\t"
    "s_cbranch_scc1 21f\n\t"
    "s_waitcnt vmcnt(4)\n\t"
    "21:\n\t"
    "s_barrier\n\t"
    "2:\n\t"
    "s_waitcnt lgkmcnt(4)\n\t"
    "v_mfma_f32_16x16x32_bf16 v[64:67], v[128:131], v[96:99], 0\n\t"
    "ds_read_b128 v[120:123], %[da] offset:4096\n\t"
    "s_add_u32 m0, %[l1], 0\n\t"
    "ds_write_b128 %[rw], v[80:83] offset:0\n\t"
    "v_mfma_f32_16x16x32_bf16 v[68:71], v[132:135], v[96:99], 0\n\t"
    "ds_read_b128 v[124:127], %[da] offset:6144\n\t"
    "global_load_lds_dwordx4 %[av0], s[36:37] sc1\n\t"
    "ds_write_b128 %[rw], v[84:87] offset:1024\n\t"
    "v_mfma_f32_16x16x32_bf16 v[72:75], v[128:131], v[100:103], 0\n\t"
    "s_add_u32 m0, %[l1], 32768\n\t"
    "ds_write_b128 %[rw], v[88:91] offset:2048\n\t"
    "v_mfma_f32_16x16x32_bf16 v[76:79], v[132:135], v[100:103], 0\n\t"
    "global_load_lds_dwordx4 %[av1], s[36:37] sc1\n\t"
    "ds_write_b128 %[rw], v[92:95] offset:3072\n\t"
    "s_waitcnt lgkmcnt(8)\n\t"
    "v_mfma_f32_16x16x32_bf16 v[64:67], v[136:139], v[104:107], v[64:67]\n\t"
    "ds_read_b128 v[96:99], %[ea] offset:8192\n\t"
    "s_add_u32 m0, %[l1], 4096\n\t"
    "v_mfma_f32_16x16x32_bf16 v[68:71], v[140:143], v[104:107], v[68:71]\n\t"
    "ds_read_b128 v[100:103], %[ea] offset:10240\n\t"
    "global_load_lds_dwordx4 %[av2], s[36:37] sc1\n\t"
    "v_mfma_f32_16x16x32_bf16 v[72:75], v[136:139], v[108:111], v[72:75]\n\t"
    "s_add_u32 m0, %[l1], 36864\n\t"
    "v_mfma_f32_16x16x32_bf16 v[76:79], v[140:143], v[108:111], v[76:79]\n\t"
    "global_load_lds_dwordx4 %[av3], s[36:37] sc1\n\t"
    "s_waitcnt lgkmcnt(8)\n\t"
    "v_mfma_f32_16x16x32_bf16 v[64:67], v[144:147], v[112:115], v[64:67]\n\t"
    "ds_read_b128 v[104:107], %[da] offset:8192\n\t"
    "s_add_u32 m0, %[l1], 8192\n\t"
    "v_mfma_f32_16x16x32_bf16 v[68:71], v[148:151], v[112:115], v[68:71]\n\t"
    "ds_read_b128 v[108:111], %[da] offset:10240\n\t"
    "global_load_lds_dwordx4 %[av4], s[36:37] sc1\n\t"
    "v_mfma_f32_16x16x32_bf16 v[72:75], v[144:147], v[116:119], v[72:75]\n\t"
    "s_add_u32 m0, %[l1], 40960\n\t"
    "v_mfma_f32_16x16x32_bf16 v[76:79], v[148:151], v[116:119], v[76:79]\n\t"
    "global_load_lds_dwordx4 %[av5], s[36:37] sc1\n\t"
    "s_waitcnt lgkmcnt(7)\n\t"
    "v_mfma_f32_16x16x32_bf16 v[64:67], v[152:155], v[120:123], v[64:67]\n\t"
    "ds_read_b128 v[112:115], %[ea] offset:12288\n\t"
    "s_add_u32 m0, %[l1], 12288\n\t"
    "v_mfma_f32_16x16x32_bf16 v[68:71], v[156:159], v[120:123], v[68:71]\n\t"
    "ds_read_b128 v[116:119], %[ea] offset:14336\n\t"
    "global_load_lds_dwordx4 %[av6], s[36:37] sc1\n\t"
    "v_mfma_f32_16x16x32_bf16 v[72:75], v[152:155], v[124:127], v[72:75]\n\t"
    "s_add_u32 m0, %[l1], 45056\n\t"
    "v_mfma_f32_16x16x32_bf16 v[76:79], v[156:159], v[124:127], v[76:79]\n\t"
    "global_load_lds_dwordx4 %[av7], s[36:37] sc1\n\t"
    "s_waitcnt lgkmcnt(4)\n\t"
    "v_mfma_f32_16x16x32_bf16 v[64:67], v[160:163], v[96:99], v[64:67]\n\t"
    "ds_read_b128 v[120:123], %[da] offset:12288\n\t"
    "v_mfma_f32_16x16x32_bf16 v[68:71], v[164:167], v[96:99], v[68:71]\n\t"
    "ds_read_b128 v[124:127], %[da] offset:14336\n\t"
    "v_mfma_f32_16x16x32_bf16 v[72:75], v[160:163], v[100:103], v[72:75]\n\t"
    "v_mfma_f32_16x16x32_bf16 v[76:79], v[164:167], v[100:103], v[76:79]\n\t"
    "s_waitcnt lgkmcnt(4)\n\t"
    "v_mfma_f32_16x16x32_bf16 v[64:67], v[168:171], v[104:107], v[64:67]\n\t"
    "ds_read_b128 v[96:99], %[ea] offset:16384\n\t"
    "v_mfma_f32_16x16x32_bf16 v[68:71], v[172:175], v[104:107], v[68:71]\n\t"
    "ds_read_b128 v[100:103], %[ea] offset:18432\n\t"
    "v_mfma_f32_16x16x32_bf16 v[72:75], v[168:171], v[108:111], v[72:75]\n\t"
    "v_mfma_f32_16x16x32_bf16 v[76:79], v[172:175], v[108:111], v[76:79]\n\t"
    "s_waitcnt lgkmcnt(4)\n\t"
    "v_mfma_f32_16x16x32_bf16 v[64:67], v[176:179], v[112:115], v[64:67]\n\t"
    "ds_read_b128 v[104:107], %[da] offset:16384\n\t"
    "v_mfma_f32_16x16x32_bf16 v[68:71], v[180:183], v[112:115], v[68:71]\n\t"
    "ds_read_b128 v[108:111], %[da] offset:18432\n\t"
    "v_mfma_f32_16x16x32_bf16 v[72:75], v[176:179], v[116:119], v[72:75]\n\t"
    "v_mfma_f32_16x16x32_bf16 v[76:79], v[180:183], v[116:119], v[76:79]\n\t"
    "s_waitcnt lgkmcnt(4)\n\t"
    "v_mfma_f32_16x16x32_bf16 v[64:67], v[184:187], v[120:123], v[64:67]\n\t"
    "ds_read_b128 v[112:115], %[ea] offset:20480\n\t"
    "v_mfma_f32_16x16x32_bf16 v[68:71], v[188:191], v[120:123], v[68:71]\n\t"
    "ds_read_b128 v[116:119], %[ea] offset:22528\n\t"
    "v_mfma_f32_16x16x32_bf16 v[72:75], v[184:187], v[124:127], v[72:75]\n\t"
    "v_mfma_f32_16x16x32_bf16 v[76:79], v[188:191], v[124:127], v[76:79]\n\t"
    "s_cmp_eq_u32 %[la], 3\n\t"
    "s_waitcnt vmcnt(12)\n\t"
    "s_cbranch_scc1 22f\n\t"
    "s_waitcnt vmcnt(4)\n\t"
    "22:\n\t"
    "s_barrier\n\t"
    "s_waitcnt lgkmcnt(4)\n\t"
    "v_mfma_f32_16x16x32_bf16 v[64:67], v[192:195], v[96:99], v[64:67]\n\t"
    "ds_read_b128 v[120:123], %[da] offset:20480\n\t"
    "s_add_u32 m0, %[l0], 0\n\t"
    "v_mfma_f32_16x16x32_bf16 v[68:71], v[196:199], v[96:99], v[68:71]\n\t"
    "ds_read_b128 v[124:127], %[da] offset:22528\n\t"
    "global_load_lds_dwordx4 %[av0], s[38:39] sc1\n\t"
    "v_mfma_f32_16x16x32_bf16 v[72:75], v[192:195], v[100:103], v[72:75]\n\t"
    "s_add_u32 m0, %[l0], 32768\n\t"
    "v_mfma_f32_16x16x32_bf16 v[76:79], v[196:199], v[100:103], v[76:79]\n\t"
    "global_load_lds_dwordx4 %[av1], s[38:39] sc1\n\t"
    "s_waitcnt lgkmcnt(4)\n\t"
    "v_mfma_f32_16x16x32_bf16 v[64:67], v[200:203], v[104:107], v[64:67]\n\t"
    "ds_read_b128 v[96:99], %[ea] offset:24576\n\t"
    "s_add_u32 m0, %[l0], 4096\n\t"
    "v_mfma_f32_16x16x32_bf16 v[68:71], v[204:207], v[104:107], v[68:71]\n\t"
    "ds_read_b128 v[100:103], %[ea] offset:26624\n\t"
    "global_load_lds_dwordx4 %[av2], s[38:39] sc1\n\t"
    "v_mfma_f32_16x16x32_bf16 v[72:75], v[200:203], v[108:111], v[72:75]\n\t"
    "s_add_u32 m0, %[l0], 36864\n\t"
    "v_mfma_f32_16x16x32_bf16 v[76:79], v[204:207], v[108:111], v[76:79]\n\t"
    "global_load_lds_dwordx4 %[av3], s[38:39] sc1\n\t"
    "s_add_u32 s36, s36, %[ab]\n\t"
    "s_addc_u32 s37, s37, 0\n\t"
    "s_waitcnt lgkmcnt(4)\n\t"
    "v_mfma_f32_16x16x32_bf16 v[64:67], v[208:211], v[112:115], v[64:67]\n\t"
    "ds_read_b128 v[104:107], %[da] offset:24576\n\t"
    "s_add_u32 m0, %[l0], 8192\n\t"
    "v_mfma_f32_16x16x32_bf16 v[68:71], v[212:215], v[112:115], v[68:71]\n\t"
    "ds_read_b128 v[108:111], %[da] offset:26624\n\t"
    "global_load_lds_dwordx4 %[av4], s[38:39] sc1\n\t"
    "v_mfma_f32_16x16x32_bf16 v[72:75], v[208:211], v[116:119], v[72:75]\n\t"
    "ds_read_b32 v39, %[sa]\n\t"
    "s_add_u32 m0, %[l0], 40960\n\t"
    "v_mfma_f32_16x16x32_bf16 v[76:79], v[212:215], v[116:119], v[76:79]\n\t"
    "global_load_lds_dwordx4 %[av5], s[38:39] sc1\n\t"
    "s_xor_b32 %[l1], %[l1], 0x10000\n\t"
    "s_waitcnt lgkmcnt(5)\n\t"
    "v_mfma_f32_16x16x32_bf16 v[64:67], v[216:219], v[120:123], v[64:67]\n\t"
    "ds_read_b128 v[112:115], %[ea] offset:28672\n\t"
    "s_add_u32 m0, %[l0], 12288\n\t"
    "v_mfma_f32_16x16x32_bf16 v[68:71], v[220:223], v[120:123], v[68:71]\n\t"
    "ds_read_b128 v[116:119], %[ea] offset:30720\n\t"
    "global_load_lds_dwordx4 %[av6], s[38:39] sc1\n\t"
    "v_mfma_f32_16x16x32_bf16 v[72:75], v[216:219], v[124:127], v[72:75]\n\t"
    "v_xor_b32 %[ea], 0x10000, %[ea]\n\t"
    "s_add_u32 m0, %[l0], 45056\n\t"
    "v_mfma_f32_16x16x32_bf16 v[76:79], v[220:223], v[124:127], v[76:79]\n\t"
    "global_load_lds_dwordx4 %[av7], s[38:39] sc1\n\t"
    "s_waitcnt lgkmcnt(5)\n\t"
    "v_mfma_f32_16x16x32_bf16 v[64:67], v[224:227], v[96:99], v[64:67]\n\t"
    "ds_read_b128 v[120:123], %[da] offset:28672\n\t"
    "v_mfma_f32_16x16x32_bf16 v[68:71], v[228:231], v[96:99], v[68:71]\n\t"
    "ds_read_b128 v[124:127], %[da] offset:30720\n\t"
    "v_mfma_f32_16x16x32_bf16 v[72:75], v[224:227], v[100:103], v[72:75]\n\t"
    "v_xor_b32 %[da], 0x10000, %[da]\n\t"
    "v_mfma_f32_16x16x32_bf16 v[76:79], v[228:231], v[100:103], v[76:79]\n\t"
    "s_waitcnt lgkmcnt(5)\n\t"
    "v_mfma_f32_16x16x32_bf16 v[64:67], v[232:235], v[104:107], v[64:67]\n\t"
    "ds_read_b128 v[96:99], %[ea] offset:0\n\t"
    "v_mfma_f32_16x16x32_bf16 v[68:71], v[236:239], v[104:107], v[68:71]\n\t"
    "ds_read_b128 v[100:103], %[ea] offset:2048\n\t"
    "v_mfma_f32_16x16x32_bf16 v[72:75], v[232:235], v[108:111], v[72:75]\n\t"
    "v_mfma_f32_16x16x32_bf16 v[76:79], v[236:239], v[108:111], v[76:79]\n\t"
    "s_waitcnt lgkmcnt(4)\n\t"
    "v_mfma_f32_16x16x32_bf16 v[64:67], v[240:243], v[112:115], v[64:67]\n\t"
    "ds_read_b128 v[104:107], %[da] offset:0\n\t"
    "v_mad_u32_u24 v39, v39, %[mu], %[ad]\n\t"
    "v_mfma_f32_16x16x32_bf16 v[68:71], v[244:247], v[112:115], v[68:71]\n\t"
    "ds_read_b128 v[108:111], %[da] offset:2048\n\t"
    "v_cmp_ge_i32 vcc, %[lf], v39\n\t"
    "s_cmp_eq_u64 vcc, 0\n\t"
    "s_addc_u32 %[lf], %[lf], 0\n\t"
    "v_mfma_f32_16x16x32_bf16 v[72:75], v[240:243], v[116:119], v[72:75]\n\t"
    "v_mfma_f32_16x16x32_bf16 v[76:79], v[244:247], v[116:119], v[76:79]\n\t"
    "v_cmp_ge_i32 vcc, %[lf], v39\n\t"
    "s_cmp_eq_u64 vcc, 0\n\t"
    "s_addc_u32 %[lf], %[lf], 0\n\t"
    "s_waitcnt lgkmcnt(4)\n\t"
    "v_mfma_f32_16x16x32_bf16 v[80:83], v[248:251], v[120:123], v[64:67]\n\t"
    "ds_read_b128 v[112:115], %[ea] offset:4096\n\t"
    "s_min_i32 %[lf], %[lf], %[cp]\n\t"
    "s_add_u32 %[b], %[b], 1\n\t"
    "v_mfma_f32_16x16x32_bf16 v[84:87], v[252:255], v[120:123], v[68:71]\n\t"
    "ds_read_b128 v[116:119], %[ea] offset:6144\n\t"
    "v_mfma_f32_16x16x32_bf16 v[88:91], v[248:251], v[124:127], v[72:75]\n\t"
    "v_mfma_f32_16x16x32_bf16 v[92:95], v[252:255], v[124:127], v[76:79]\n\t"
    "s_add_u32 s38, s38, %[ab]\n\t"
    "s_addc_u32 s39, s39, 0\n\t"
    "s_xor_b32 %[l0], %[l0], 0x10000\n\t"
    "s_sub_u32 m0, %[lf], %[b]\n\t"
    "s_sub_u32 m0, m0, 1\n\t"
    "s_cmp_lt_u32 m0, %[up]\n\t"
    "s_cbranch_scc1 1b\n\t"
    "s_waitcnt lgkmcnt(0)\n\t"
    "s_nop 4"
    : "=&{v[80:95]}"(pd), [ea] "+&v"(ea), [da] "+&v"(da), [b] "+&s"(b), [lf] "+&s"(lf), "+{s[36:37]}"(p1), "+{s[38:39]}"(p0), [sa] "+&v"(sa), [l1] "+&s"(l1), [l0] "+&s"(l0)
    : [rw] "v"(rw), [ab] "s"(ab), [cp] "s"(cp), [mu] "s"(mu), [ad] "v"(ad), [up] "s"(up), "{v[128:159]}"(W[0]), "{v[160:191]}"(W[1]), "{v[192:223]}"(W[2]), "{v[224:255]}"(W[3]), [av0] "v"(av[0]), [av1] "v"(av[1]), [av2] "v"(av[2]), [av3] "v"(av[3]), [av4] "v"(av[4]), [av5] "v"(av[5]), [av6] "v"(av[6]), [av7] "v"(av[7]), [la] "s"(la)
    : "v39", "v64", "v65", "v66", "v67", "v68", "v69", "v70", "v71", "v72", "v73", "v74", "v75", "v76", "v77", "v78", "v79", "v96", "v97", "v98", "v99", "v100", "v101", "v102", "v103", "v104", "v105", "v106", "v107", "v108", "v109", "v110", "v111", "v112", "v113", "v114", "v115", "v116", "v117", "v118", "v119", "v120", "v121", "v122", "v123", "v124", "v125", "v126", "v127", "vcc", "scc", "m0", "memory");
}
__device__ __forceinline__ void fast_ld1(const u32x32 (&W)[4], u32x16& pd, unsigned ea, unsigned da, int& b, int& lf, int cp, unsigned up, unsigned sa, unsigned mu, unsigned ad,
    unsigned rw, unsigned ab, const char* p1, const char* p0, unsigned l1, unsigned l0, const unsigned (&av)[8], int la) {
  asm volatile("s_setprio 0\n\t"
    "s_cmp_eq_u32 %[la], 3\n\t"
    "s_waitcnt vmcnt(12)\n\t"
    "s_cbranch_scc1 20f\n\t"
    "s_waitcnt vmcnt(4)\n\t"
    "20:\n\t"
    "s_barrier\n\t"
    "ds_read_b128 v[96:99], %[ea] offset:0\n\t"
    "ds_read_b128 v[100:103], %[ea] offset:2048\n\t"
    "ds_read_b128 v[104:107], %[da] offset:0\n\t"
    "ds_read_b128 v[108:111], %[da] offset:2048\n\t"
    "ds_read_b128 v[112:115], %[ea] offset:4096\n\t"
    "ds_read_b128 v[116:119], %[ea] offset:6144\n\t"
    "s_branch 2f\n\t"
    ".p2align 6\n\t"
    "1:\n\t"
    "s_cmp_eq_u32 %[la], 3\n\t"
    "s_waitcnt vmcnt(12)\n\t"
    "s_cbranch_scc1 21f\n\t"
    "s_waitcnt vmcnt(4)\n\t"
    "21:\n\t"
    "s_barrier\n\t"
    "2:\n\t"
    "s_waitcnt lgkmcnt(4)\n\t"
    "v_mfma_f32_16x16x32_bf16 v[64:67], v[128:131], v[96:99], 0\n\t"
    "ds_read_b128 v[120:123], %[da] offset:4096\n\t"
    "s_add_u32 m0, %[l1], 0\n\t"
    "ds_write_b128 %[rw], v[80:83] offset:0\n\t"
    "v_mfma_f32_16x16x32_bf16 v[68:71], v[132:135], v[96:99], 0\n\t"
    "ds_read_b128 v[124:127], %[da] offset:6144\n\t"
    "global_load_lds_dwordx4 %[av0], s[36:37] sc1\n\t"
    "ds_write_b128 %[rw], v[84:87] offset:1024\n\t"
    "v_mfma_f32_16x16x32_bf16 v[72:75], v[128:131], v[100:103], 0\n\t"
    "s_add_u32 m0, %[l1], 32768\n\t"
    "ds_write_b128 %[rw], v[88:91] offset:2048\n\t"
    "v_mfma_f32_16x16x32_bf16 v[76:79], v[132:135], v[100:103], 0\n\t"
    "global_load_lds_dwordx4 %[av1], s[36:37] sc1\n\t"
    "ds_write_b128 %[rw], v[92:95] offset:3072\n\t"
    "s_waitcnt lgkmcnt(8)\n\t"
    "v_mfma_f32_16x16x32_bf16 v[64:67], v[136:139], v[104:107], v[64:67]\n\t"
    "ds_read_b128 v[96:99], %[ea] offset:8192\n\t"
    "s_add_u32 m0, %[l1], 4096\n\t"
    "v_mfma_f32_16x16x32_bf16 v[68:71], v[140:143], v[104:107], v[68:71]\n\t"
    "ds_read_b128 v[100:103], %[ea] offset:10240\n\t"
    "global_load_lds_dwordx4 %[av2], s[36:37] sc1\n\t"
    "v_mfma_f32_16x16x32_bf16 v[72:75], v[136:139], v[108:111], v[72:75]\n\t"
    "s_add_u32 m0, %[l1], 36864\n\t"
    "v_mfma_f32_16x16x32_bf16 v[76:79], v[140:143], v[108:111], v[76:79]\n\t"
    "global_load_lds_dwordx4 %[av3], s[36:37] sc1\n\t"
    "s_waitcnt lgkmcnt(8)\n\t"
    "v_mfma_f32_16x16x32_bf16 v[64:67], v[144:147], v[112:115], v[64:67]\n\t"
    "ds_read_b128 v[104:107], %[da] offset:8192\n\t"
    "s_add_u32 m0, %[l1], 8192\n\t"
    "v_mfma_f32_16x16x32_bf16 v[68:71], v[148:151], v[112:115], v[68:71]\n\t"
    "ds_read_b128 v[108:111], %[da] offset:10240\n\t"
    "global_load_lds_dwordx4 %[av4], s[36:37] sc1\n\t"
    "v_mfma_f32_16x16x32_bf16 v[72:75], v[144:147], v[116:119], v[72:75]\n\t"
    "s_add_u32 m0, %[l1], 40960\n\t"
    "v_mfma_f32_16x16x32_bf16 v[76:79], v[148:151], v[116:119], v[76:79]\n\t"
    "global_load_lds_dwordx4 %[av5], s[36:37] sc1\n\t"
    "s_waitcnt lgkmcnt(7)\n\t"
    "v_mfma_f32_16x16x32_bf16 v[64:67], v[152:155], v[120:123], v[64:67]\n\t"
    "ds_read_b128 v[112:115], %[ea] offset:12288\n\t"
    "s_add_u32 m0, %[l1], 12288\n\t"
    "v_mfma_f32_16x16x32_bf16 v[68:71], v[156:159], v[120:123], v[68:71]\n\t"
    "ds_read_b128 v[116:119], %[ea] offset:14336\n\t"
    "global_load_lds_dwordx4 %[av6], s[36:37] sc1\n\t"
    "v_mfma_f32_16x16x32_bf16 v[72:75], v[152:155], v[124:127], v[72:75]\n\t"
    "s_add_u32 m0, %[l1], 45056\n\t"
    "v_mfma_f32_16x16x32_bf16 v[76:79], v[156:159], v[124:127], v[76:79]\n\t"
    "global_load_lds_dwordx4 %[av7], s[36:37] sc1\n\t"
    "s_waitcnt lgkmcnt(4)\n\t"
    "v_mfma_f32_16x16x32_bf16 v[64:67], v[160:163], v[96:99], v[64:67]\n\t"
    "ds_read_b128 v[120:123], %[da] offset:12288\n\t"
    "v_mfma_f32_16x16x32_bf16 v[68:71], v[164:167], v[96:99], v[68:71]\n\t"
    "ds_read_b128 v[124:127], %[da] offset:14336\n\t"
    "v_mfma_f32_16x16x32_bf16 v[72:75], v[160:163], v[100:103], v[72:75]\n\t"
    "v_mfma_f32_16x16x32_bf16 v[76:79], v[164:167], v[100:103], v[76:79]\n\t"
    "s_waitcnt lgkmcnt(4)\n\t"
    "v_mfma_f32_16x16x32_bf16 v[64:67], v[168:171], v[104:107], v[64:67]\n\t"
    "ds_read_b128 v[96:99], %[ea] offset:16384\n\t"
    "v_mfma_f32_16x16x32_bf16 v[68:71], v[172:175], v[104:107], v[68:71]\n\t"
    "ds_read_b128 v[100:103], %[ea] offset:18432\n\t"
    "v_mfma_f32_16x16x32_bf16 v[72:75], v[168:171], v[108:111], v[72:75]\n\t"
    "v_mfma_f32_16x16x32_bf16 v[76:79], v[172:175], v[108:111], v[76:79]\n\t"
    "s_waitcnt lgkmcnt(4)\n\t"
    "v_mfma_f32_16x16x32_bf16 v[64:67], v[176:179], v[112:115], v[64:67]\n\t"
    "ds_read_b128 v[104:107], %[da] offset:16384\n\t"
    "v_mfma_f32_16x16x32_bf16 v[68:71], v[180:183], v[112:115], v[68:71]\n\t"
    "ds_read_b128 v[108:111], %[da] offset:18432\n\t"
    "v_mfma_f32_16x16x32_bf16 v[72:75], v[176:179], v[116:119], v[72:75]\n\t"
    "v_mfma_f32_16x16x32_bf16 v[76:79], v[180:183], v[116:119], v[76:79]\n\t"
    "s_waitcnt lgkmcnt(4)\n\t"
    "v_mfma_f32_16x16x32_bf16 v[64:67], v[184:187], v[120:123], v[64:67]\n\t"
    "ds_read_b128 v[112:115], %[ea] offset:20480\n\t"
    "v_mfma_f32_16x16x32_bf16 v[68:71], v[188:191], v[120:123], v[68:71]\n\t"
    "ds_read_b128 v[116:119], %[ea] offset:22528\n\t"
    "v_mfma_f32_16x16x32_bf16 v[72:75], v[184:187], v[124:127], v[72:75]\n\t"
    "v_mfma_f32_16x16x32_bf16 v[76:79], v[188:191], v[124:127], v[76:79]\n\t"
    "s_cmp_eq_u32 %[la], 3\n\t"
    "s_waitcnt vmcnt(12)\n\t"
    "s_cbranch_scc1 22f\n\t"
    "s_waitcnt vmcnt(4)\n\t"
    "22:\n\t"
    "s_barrier\n\t"
    "s_waitcnt lgkmcnt(4)\n\t"
    "v_mfma_f32_16x16x32_bf16 v[64:67], v[192:195], v[96:99], v[64:67]\n\t"
    "ds_read_b128 v[120:123], %[da] offset:20480\n\t"
    "s_add_u32 m0, %[l0], 0\n\t"
    "v_mfma_f32_16x16x32_bf16 v[68:71], v[196:199], v[96:99], v[68:71]\n\t"
    "ds_read_b128 v[124:127], %[da] offset:22528\n\t"
    "global_load_lds_dwordx4 %[av0], s[38:39] sc1\n\t"
    "v_mfma_f32_16x16x32_bf16 v[72:75], v[192:195], v[100:103], v[72:75]\n\t"
    "s_add_u32 m0, %[l0], 32768\n\t"
    "v_mfma_f32_16x16x32_bf16 v[76:79], v[196:199], v[100:103], v[76:79]\n\t"
    "global_load_lds_dwordx4 %[av1], s[38:39] sc1\n\t"
    "s_waitcnt lgkmcnt(4)\n\t"
    "v_mfma_f32_16x16x32_bf16 v[64:67], v[200:203], v[104:107], v[64:67]\n\t"
    "ds_read_b128 v[96:99], %[ea] offset:24576\n\t"
    "s_add_u32 m0, %[l0], 4096\n\t"
    "v_mfma_f32_16x16x32_bf16 v[68:71], v[204:207], v[104:107], v[68:71]\n\t"
    "ds_read_b128 v[100:103], %[ea] offset:26624\n\t"
    "global_load_lds_dwordx4 %[av2], s[38:39] sc1\n\t"
    "v_mfma_f32_16x16x32_bf16 v[72:75], v[200:203], v[108:111], v[72:75]\n\t"
    "s_add_u32 m0, %[l0], 36864\n\t"
    "v_mfma_f32_16x16x32_bf16 v[76:79], v[204:207], v[108:111], v[76:79]\n\t"
    "global_load_lds_dwordx4 %[av3], s[38:39] sc1\n\t"
    "s_add_u32 s36, s36, %[ab]\n\t"
    "s_addc_u32 s37, s37, 0\n\t"
    "s_waitcnt lgkmcnt(4)\n\t"
    "v_mfma_f32_16x16x32_bf16 v[64:67], v[208:211], v[112:115], v[64:67]\n\t"
    "ds_read_b128 v[104:107], %[da] offset:24576\n\t"
    "s_add_u32 m0, %[l0], 8192\n\t"
    "v_mfma_f32_16x16x32_bf16 v[68:71], v[212:215], v[112:115], v[68:71]\n\t"
    "ds_read_b128 v[108:111], %[da] offset:26624\n\t"
    "global_load_lds_dwordx4 %[av4], s[38:39] sc1\n\t"
    "v_mfma_f32_16x16x32_bf16 v[72:75], v[208:211], v[116:119], v[72:75]\n\t"
    "ds_read_b32 v39, %[sa]\n\t"
    "s_add_u32 m0, %[l0], 40960\n\t"
    "v_mfma_f32_16x16x32_bf16 v[76:79], v[212:215], v[116:119], v[76:79]\n\t"
    "global_load_lds_dwordx4 %[av5], s[38:39] sc1\n\t"
    "s_xor_b32 %[l1], %[l1], 0x10000\n\t"
    "s_waitcnt lgkmcnt(5)\n\t"
    "v_mfma_f32_16x16x32_bf16 v[64:67], v[216:219], v[120:123], v[64:67]\n\t"
    "ds_read_b128 v[112:115], %[ea] offset:28672\n\t"
    "s_add_u32 m0, %[l0], 12288\n\t"
    "v_mfma_f32_16x16x32_bf16 v[68:71], v[220:223], v[120:123], v[68:71]\n\t"
    "ds_read_b128 v[116:119], %[ea] offset:30720\n\t"
    "global_load_lds_dwordx4 %[av6], s[38:39] sc1\n\t"
    "v_mfma_f32_16x16x32_bf16 v[72:75], v[216:219], v[124:127], v[72:75]\n\t"
    "v_xor_b32 %[ea], 0x10000, %[ea]\n\t"
    "s_add_u32 m0, %[l0], 45056\n\t"
    "v_mfma_f32_16x16x32_bf16 v[76:79], v[220:223], v[124:127], v[76:79]\n\t"
    "global_load_lds_dwordx4 %[av7], s[38:39] sc1\n\t"
    "s_waitcnt lgkmcnt(5)\n\t"
    "v_mfma_f32_16x16x32_bf16 v[64:67], v[224:227], v[96:99], v[64:67]\n\t"
    "ds_read_b128 v[120:123], %[da] offset:28672\n\t"
    "v_mfma_f32_16x16x32_bf16 v[68:71], v[228:231], v[96:99], v[68:71]\n\t"
    "ds_read_b128 v[124:127], %[da] offset:30720\n\t"
    "v_mfma_f32_16x16x32_bf16 v[72:75], v[224:227], v[100:103], v[72:75]\n\t"
    "v_xor_b32 %[da], 0x10000, %[da]\n\t"
    "v_mfma_f32_16x16x32_bf16 v[76:79], v[228:231], v[100:103], v[76:79]\n\t"
    "s_waitcnt lgkmcnt(5)\n\t"
    "v_mfma_f32_16x16x32_bf16 v[64:67], v[232:235], v[104:107], v[64:67]\n\t"
    "ds_read_b128 v[96:99], %[ea] offset:0\n\t"
    "v_mfma_f32_16x16x32_bf16 v[68:71], v[236:239], v[104:107], v[68:71]\n\t"
    "ds_read_b128 v[100:103], %[ea] offset:2048\n\t"
    "v_mfma_f32_16x16x32_bf16 v[72:75], v[232:235], v[108:111], v[72:75]\n\t"
    "v_mfma_f32_16x16x32_bf16 v[76:79], v[236:239], v[108:111], v[76:79]\n\t"
    "s_waitcnt lgkmcnt(4)\n\t"
    "v_mfma_f32_16x16x32_bf16 v[64:67], v[240:243], v[112:115], v[64:67]\n\t"
    "ds_read_b128 v[104:107], %[da] offset:0\n\t"
    "v_mad_u32_u24 v39, v39, %[mu], %[ad]\n\t"
    "v_mfma_f32_16x16x32_bf16 v[68:71], v[244:247], v[112:115], v[68:71]\n\t"
    "ds_read_b128 v[108:111], %[da] offset:2048\n\t"
    "v_cmp_ge_i32 vcc, %[lf], v39\n\t"
    "s_cmp_eq_u64 vcc, 0\n\t"
    "s_addc_u32 %[lf], %[lf], 0\n\t"
    "v_mfma_f32_16x16x32_bf16 v[72:75], v[240:243], v[116:119], v[72:75]\n\t"
    "v_mfma_f32_16x16x32_bf16 v[76:79], v[244:247], v[116:119], v[76:79]\n\t"
    "v_cmp_ge_i32 vcc, %[lf], v39\n\t"
    "s_cmp_eq_u64 vcc, 0\n\t"
    "s_addc_u32 %[lf], %[lf], 0\n\t"
    "s_waitcnt lgkmcnt(4)\n\t"
    "v_mfma_f32_16x16x32_bf16 v[80:83], v[248:251], v[120:123], v[64:67]\n\t"
    "ds_read_b128 v[112:115], %[ea] offset:4096\n\t"
    "s_min_i32 %[lf], %[lf], %[cp]\n\t"
    "s_add_u32 %[b], %[b], 1\n\t"
    "v_mfma_f32_16x16x32_bf16 v[84:87], v[252:255], v[120:123], v[68:71]\n\t"
    "ds_read_b128 v[116:119], %[ea] offset:6144\n\t"
    "v_mfma_f32_16x16x32_bf16 v[88:91], v[248:251], v[124:127], v[72:75]\n\t"
    "v_mfma_f32_16x16x32_bf16 v[92:95], v[252:255], v[124:127], v[76:79]\n\t"
    "s_add_u32 s38, s38, %[ab]\n\t"
    "s_addc_u32 s39, s39, 0\n\t"
    "s_xor_b32 %[l0], %[l0], 0x10000\n\t"
    "s_sub_u32 m0, %[lf], %[b]\n\t"
    "s_sub_u32 m0, m0, 1\n\t"
    "s_cmp_lt_u32 m0, %[up]\n\t"
    "s_cbranch_scc1 1b\n\t"
    "s_waitcnt lgkmcnt(0)\n\t"
    "s_nop 4"
    : "=&{v[80:95]}"(pd), [ea] "+&v"(ea), [da] "+&v"(da), [b] "+&s"(b), [lf] "+&s"(lf), "+{s[36:37]}"(p1), "+{s[38:39]}"(p0), [sa] "+&v"(sa), [l1] "+&s"(l1), [l0] "+&s"(l0)
    : [rw] "v"(rw), [ab] "s"(ab), [cp] "s"(cp), [mu] "s"(mu), [ad] "v"(ad), [up] "s"(up), "{v[128:159]}"(W[0]), "{v[160:191]}"(W[1]), "{v[192:223]}"(W[2]), "{v[224:255]}"(W[3]), [av0] "v"(av[0]), [av1] "v"(av[1]), [av2] "v"(av[2]), [av3] "v"(av[3]), [av4] "v"(av[4]), [av5] "v"(av[5]), [av6] "v"(av[6]), [av7] "v"(av[7]), [la] "s"(la)
    : "v39", "v64", "v65", "v66", "v67", "v68", "v69", "v70", "v71", "v72", "v73", "v74", "v75", "v76", "v77", "v78", "v79", "v96", "v97", "v98", "v99", "v100", "v101", "v102", "v103", "v104", "v105", "v106", "v107", "v108", "v109", "v110", "v111", "v112", "v113", "v114", "v115", "v116", "v117", "v118", "v119", "v120", "v121", "v122", "v123", "v124", "v125", "v126", "v127", "vcc", "scc", "m0", "memory");
}
__device__ __forceinline__ void fast_ld2(const u32x32 (&W)[4], u32x16& pd, unsigned ea, unsigned da, int& b, int& lf, int cp, unsigned up, unsigned sa, unsigned mu, unsigned ad,
    unsigned rw, unsigned ab, const char* p1, const char* p0, unsigned l1, unsigned l0, const unsigned (&av)[8], int la) {
  asm volatile("s_setprio 0\n\t"
    "s_cmp_eq_u32 %[la], 3\n\t"
    "s_waitcnt vmcnt(6)\n\t"
    "s_cbranch_scc1 20f\n\t"
    "s_waitcnt vmcnt(2)\n\t"
    "20:\n\t"
    "s_barrier\n\t"
    "ds_read_b128 v[96:99], %[ea] offset:0\n\t"
    "ds_read_b128 v[100:103], %[ea] offset:2048\n\t"
    "ds_read_b128 v[104:107], %[da] offset:0\n\t"
    "ds_read_b128 v[108:111], %[da] offset:2048\n\t"
    "ds_read_b128 v[112:115], %[ea] offset:4096\n\t"
    "ds_read_b128 v[116:119], %[ea] offset:6144\n\t"
    "s_branch 2f\n\t"
    ".p2align 6\n\t"
    "1:\n\t"
    "s_cmp_eq_u32 %[la], 3\n\t"
    "s_waitcnt vmcnt(6)\n\t"
    "s_cbranch_scc1 21f\n\t"
    "s_waitcnt vmcnt(2)\n\t"
    "21:\n\t"
    "s_barrier\n\t"
    "2:\n\t"
    "s_waitcnt lgkmcnt(4)\n\t"
    "v_mfma_f32_16x16x32_bf16 v[64:67], v[128:131], v[96:99], 0\n\t"
    "ds_read_b128 v[120:123], %[da] offset:4096\n\t"
    "s_add_u32 m0, %[l1], 0\n\t"
    "ds_write_b128 %[rw], v[80:83] offset:0\n\t"
    "v_mfma_f32_16x16x32_bf16 v[68:71], v[132:135], v[96:99], 0\n\t"
    "ds_read_b128 v[124:127], %[da] offset:6144\n\t"
    "global_load_lds_dwordx4 %[av0], s[36:37] sc1\n\t"
    "ds_write_b128 %[rw], v[84:87] offset:1024\n\t"
    "v_mfma_f32_16x16x32_bf16 v[72:75], v[128:131], v[100:103], 0\n\t"
    "ds_write_b128 %[rw], v[88:91] offset:2048\n\t"
    "v_mfma_f32_16x16x32_bf16 v[76:79], v[132:135], v[100:103], 0\n\t"
    "ds_write_b128 %[rw], v[92:95] offset:3072\n\t"
    "s_waitcnt lgkmcnt(8)\n\t"
    "v_mfma_f32_16x16x32_bf16 v[64:67], v[136:139], v[104:107], v[64:67]\n\t"
    "ds_read_b128 v[96:99], %[ea] offset:8192\n\t"
    "s_add_u32 m0, %[l1], 4096\n\t"
    "v_mfma_f32_16x16x32_bf16 v[68:71], v[140:143], v[104:107], v[68:71]\n\t"
    "ds_read_b128 v[100:103], %[ea] offset:10240\n\t"
    "global_load_lds_dwordx4 %[av2], s[36:37] sc1\n\t"
    "v_mfma_f32_16x16x32_bf16 v[72:75], v[136:139], v[108:111], v[72:75]\n\t"
    "v_mfma_f32_16x16x32_bf16 v[76:79], v[140:143], v[108:111], v[76:79]\n\t"
    "s_waitcnt lgkmcnt(8)\n\t"
    "v_mfma_f32_16x16x32_bf16 v[64:67], v[144:147], v[112:115], v[64:67]\n\t"
    "ds_read_b128 v[104:107], %[da] offset:8192\n\t"
    "s_add_u32 m0, %[l1], 8192\n\t"
    "v_mfma_f32_16x16x32_bf16 v[68:71], v[148:151], v[112:115], v[68:71]\n\t"
    "ds_read_b128 v[108:111], %[da] offset:10240\n\t"
    "global_load_lds_dwordx4 %[av4], s[36:37] sc1\n\t"
    "v_mfma_f32_16x16x32_bf16 v[72:75], v[144:147], v[116:119], v[72:75]\n\t"
    "v_mfma_f32_16x16x32_bf16 v[76:79], v[148:151], v[116:119], v[76:79]\n\t"
    "s_waitcnt lgkmcnt(7)\n\t"
    "v_mfma_f32_16x16x32_bf16 v[64:67], v[152:155], v[120:123], v[64:67]\n\t"
    "ds_read_b128 v[112:115], %[ea] offset:12288\n\t"
    "s_add_u32 m0, %[l1], 12288\n\t"
    "v_mfma_f32_16x16x32_bf16 v[68:71], v[156:159], v[120:123], v[68:71]\n\t"
    "ds_read_b128 v[116:119], %[ea] offset:14336\n\t"
    "global_load_lds_dwordx4 %[av6], s[36:37] sc1\n\t"
    "v_mfma_f32_16x16x32_bf16 v[72:75], v[152:155], v[124:127], v[72:75]\n\t"
    "v_mfma_f32_16x16x32_bf16 v[76:79], v[156:159], v[124:127], v[76:79]\n\t"
    "s_waitcnt lgkmcnt(4)\n\t"
    "v_mfma_f32_16x16x32_bf16 v[64:67], v[160:163], v[96:99], v[64:67]\n\t"
    "ds_read_b128 v[120:123], %[da] offset:12288\n\t"
    "v_mfma_f32_16x16x32_bf16 v[68:71], v[164:167], v[96:99], v[68:71]\n\t"
    "ds_read_b128 v[124:127], %[da] offset:14336\n\t"
    "v_mfma_f32_16x16x32_bf16 v[72:75], v[160:163], v[100:103], v[72:75]\n\t"
    "v_mfma_f32_16x16x32_bf16 v[76:79], v[164:167], v[100:103], v[76:79]\n\t"
    "s_waitcnt lgkmcnt(4)\n\t"
    "v_mfma_f32_16x16x32_bf16 v[64:67], v[168:171], v[104:107], v[64:67]\n\t"
    "ds_read_b128 v[96:99], %[ea] offset:16384\n\t"
    "v_mfma_f32_16x16x32_bf16 v[68:71], v[172:175], v[104:107], v[68:71]\n\t"
    "ds_read_b128 v[100:103], %[ea] offset:18432\n\t"
    "v_mfma_f32_16x16x32_bf16 v[72:75], v[168:171], v[108:111], v[72:75]\n\t"
    "v_mfma_f32_16x16x32_bf16 v[76:79], v[172:175], v[108:111], v[76:79]\n\t"
    "s_waitcnt lgkmcnt(4)\n\t"
    "v_mfma_f32_16x16x32_bf16 v[64:67], v[176:179], v[112:115], v[64:67]\n\t"
    "ds_read_b128 v[104:107], %[da] offset:16384\n\t"
    "v_mfma_f32_16x16x32_bf16 v[68:71], v[180:183], v[112:115], v[68:71]\n\t"
    "ds_read_b128 v[108:111], %[da] offset:18432\n\t"
    "v_mfma_f32_16x16x32_bf16 v[72:75], v[176:179], v[116:119], v[72:75]\n\t"
    "v_mfma_f32_16x16x32_bf16 v[76:79], v[180:183], v[116:119], v[76:79]\n\t"
    "s_waitcnt lgkmcnt(4)\n\t"
    "v_mfma_f32_16x16x32_bf16 v[64:67], v[184:187], v[120:123], v[64:67]\n\t"
    "ds_read_b128 v[112:115], %[ea] offset:20480\n\t"
    "v_mfma_f32_16x16x32_bf16 v[68:71], v[188:191], v[120:123], v[68:71]\n\t"
    "ds_read_b128 v[116:119], %[ea] offset:22528\n\t"
    "v_mfma_f32_16x16x32_bf16 v[72:75], v[184:187], v[124:127], v[72:75]\n\t"
    "v_mfma_f32_16x16x32_bf16 v[76:79], v[188:191], v[124:127], v[76:79]\n\t"
    "s_cmp_eq_u32 %[la], 3\n\t"
    "s_waitcnt vmcnt(6)\n\t"
    "s_cbranch_scc1 22f\n\t"
    "s_waitcnt vmcnt(2)\n\t"
    "22:\n\t"
    "s_barrier\n\t"
    "s_waitcnt lgkmcnt(4)\n\t"
    "v_mfma_f32_16x16x32_bf16 v[64:67], v[192:195], v[96:99], v[64:67]\n\t"
    "ds_read_b128 v[120:123], %[da] offset:20480\n\t"
    "s_add_u32 m0, %[l0], 0\n\t"
    "v_mfma_f32_16x16x32_bf16 v[68:71], v[196:199], v[96:99], v[68:71]\n\t"
    "ds_read_b128 v[124:127], %[da] offset:22528\n\t"
    "global_load_lds_dwordx4 %[av0], s[38:39] sc1\n\t"
    "v_mfma_f32_16x16x32_bf16 v[72:75], v[192:195], v[100:103], v[72:75]\n\t"
    "v_mfma_f32_16x16x32_bf16 v[76:79], v[196:199], v[100:103], v[76:79]\n\t"
    "s_waitcnt lgkmcnt(4)\n\t"
    "v_mfma_f32_16x16x32_bf16 v[64:67], v[200:203], v[104:107], v[64:67]\n\t"
    "ds_read_b128 v[96:99], %[ea] offset:24576\n\t"
    "s_add_u32 m0, %[l0], 4096\n\t"
    "v_mfma_f32_16x16x32_bf16 v[68:71], v[204:207], v[104:107], v[68:71]\n\t"
    "ds_read_b128 v[100:103], %[ea] offset:26624\n\t"
    "global_load_lds_dwordx4 %[av2], s[38:39] sc1\n\t"
    "v_mfma_f32_16x16x32_bf16 v[72:75], v[200:203], v[108:111], v[72:75]\n\t"
    "v_mfma_f32_16x16x32_bf16 v[76:79], v[204:207], v[108:111], v[76:79]\n\t"
    "s_add_u32 s36, s36, %[ab]\n\t"
    "s_addc_u32 s37, s37, 0\n\t"
    "s_waitcnt lgkmcnt(4)\n\t"
    "v_mfma_f32_16x16x32_bf16 v[64:67], v[208:211], v[112:115], v[64:67]\n\t"
    "ds_read_b128 v[104:107], %[da] offset:24576\n\t"
    "s_add_u32 m0, %[l0], 8192\n\t"
    "v_mfma_f32_16x16x32_bf16 v[68:71], v[212:215], v[112:115], v[68:71]\n\t"
    "ds_read_b128 v[108:111], %[da] offset:26624\n\t"
    "global_load_lds_dwordx4 %[av4], s[38:39] sc1\n\t"
    "v_mfma_f32_16x16x32_bf16 v[72:75], v[208:211], v[116:119], v[72:75]\n\t"
    "ds_read_b32 v39, %[sa]\n\t"
    "v_mfma_f32_16x16x32_bf16 v[76:79], v[212:215], v[116:119], v[76:79]\n\t"
    "s_xor_b32 %[l1], %[l1], 0x10000\n\t"
    "s_waitcnt lgkmcnt(5)\n\t"
    "v_mfma_f32_16x16x32_bf16 v[64:67], v[216:219], v[120:123], v[64:67]\n\t"
    "ds_read_b128 v[112:115], %[ea] offset:28672\n\t"
    "s_add_u32 m0, %[l0], 12288\n\t"
    "v_mfma_f32_16x16x32_bf16 v[68:71], v[220:223], v[120:123], v[68:71]\n\t"
    "ds_read_b128 v[116:119], %[ea] offset:30720\n\t"
    "global_load_lds_dwordx4 %[av6], s[38:39] sc1\n\t"
    "v_mfma_f32_16x16x32_bf16 v[72:75], v[216:219], v[124:127], v[72:75]\n\t"
    "v_xor_b32 %[ea], 0x10000, %[ea]\n\t"
    "v_mfma_f32_16x16x32_bf16 v[76:79], v[220:223], v[124:127], v[76:79]\n\t"
    "s_waitcnt lgkmcnt(5)\n\t"
    "v_mfma_f32_16x16x32_bf16 v[64:67], v[224:227], v[96:99], v[64:67]\n\t"
    "ds_read_b128 v[120:123], %[da] offset:28672\n\t"
    "v_mfma_f32_16x16x32_bf16 v[68:71], v[228:231], v[96:99], v[68:71]\n\t"
    "ds_read_b128 v[124:127], %[da] offset:30720\n\t"
    "v_mfma_f32_16x16x32_bf16 v[72:75], v[224:227], v[100:103], v[72:75]\n\t"
    "v_xor_b32 %[da], 0x10000, %[da]\n\t"
    "v_mfma_f32_16x16x32_bf16 v[76:79], v[228:231], v[100:103], v[76:79]\n\t"
    "s_waitcnt lgkmcnt(5)\n\t"
    "v_mfma_f32_16x16x32_bf16 v[64:67], v[232:235], v[104:107], v[64:67]\n\t"
    "ds_read_b128 v[96:99], %[ea] offset:0\n\t"
    "v_mfma_f32_16x16x32_bf16 v[68:71], v[236:239], v[104:107], v[68:71]\n\t"
    "ds_read_b128 v[100:103], %[ea] offset:2048\n\t"
    "v_mfma_f32_16x16x32_bf16 v[72:75], v[232:235], v[108:111], v[72:75]\n\t"
    "v_mfma_f32_16x16x32_bf16 v[76:79], v[236:239], v[108:111], v[76:79]\n\t"
    "s_waitcnt lgkmcnt(4)\n\t"
    "v_mfma_f32_16x16x32_bf16 v[64:67], v[240:243], v[112:115], v[64:67]\n\t"
    "ds_read_b128 v[104:107], %[da] offset:0\n\t"
    "v_mad_u32_u24 v39, v39, %[mu], %[ad]\n\t"
    "v_mfma_f32_16x16x32_bf16 v[68:71], v[244:247], v[112:115], v[68:71]\n\t"
    "ds_read_b128 v[108:111], %[da] offset:2048\n\t"
    "v_cmp_ge_i32 vcc, %[lf], v39\n\t"
    "s_cmp_eq_u64 vcc, 0\n\t"
    "s_addc_u32 %[lf], %[lf], 0\n\t"
    "v_mfma_f32_16x16x32_bf16 v[72:75], v[240:243], v[116:119], v[72:75]\n\t"
    "v_mfma_f32_16x16x32_bf16 v[76:79], v[244:247], v[116:119], v[76:79]\n\t"
    "v_cmp_ge_i32 vcc, %[lf], v39\n\t"
    "s_cmp_eq_u64 vcc, 0\n\t"
    "s_addc_u32 %[lf], %[lf], 0\n\t"
    "s_waitcnt lgkmcnt(4)\n\t"
    "v_mfma_f32_16x16x32_bf16 v[80:83], v[248:251], v[120:123], v[64:67]\n\t"
    "ds_read_b128 v[112:115], %[ea] offset:4096\n\t"
    "s_min_i32 %[lf], %[lf], %[cp]\n\t"
    "s_add_u32 %[b], %[b], 1\n\t"
    "v_mfma_f32_16x16x32_bf16 v[84:87], v[252:255], v[120:123], v[68:71]\n\t"
    "ds_read_b128 v[116:119], %[ea] offset:6144\n\t"
    "v_mfma_f32_16x16x32_bf16 v[88:91], v[248:251], v[124:127], v[72:75]\n\t"
    "v_mfma_f32_16x16x32_bf16 v[92:95], v[252:255], v[124:127], v[76:79]\n\t"
    "s_add_u32 s38, s38, %[ab]\n\t"
    "s_addc_u32 s39, s39, 0\n\t"
    "s_xor_b32 %[l0], %[l0], 0x10000\n\t"
    "s_sub_u32 m0, %[lf], %[b]\n\t"
    "s_sub_u32 m0, m0, 1\n\t"
    "s_cmp_lt_u32 m0, %[up]\n\t"
    "s_cbranch_scc1 1b\n\t"
    "s_waitcnt lgkmcnt(0)\n\t"
    "s_nop 4"
    : "=&{v[80:95]}"(pd), [ea] "+&v"(ea), [da] "+&v"(da), [b] "+&s"(b), [lf] "+&s"(lf), "+{s[36:37]}"(p1), "+{s[38:39]}"(p0), [sa] "+&v"(sa), [l1] "+&s"(l1), [l0] "+&s"(l0)
    : [rw] "v"(rw), [ab] "s"(ab), [cp] "s"(cp), [mu] "s"(mu), [ad] "v"(ad), [up] "s"(up), "{v[128:159]}"(W[0]), "{v[160:191]}"(W[1]), "{v[192:223]}"(W[2]), "{v[224:255]}"(W[3]), [av0] "v"(av[0]), [av1] "v"(av[1]), [av2] "v"(av[2]), [av3] "v"(av[3]), [av4] "v"(av[4]), [av5] "v"(av[5]), [av6] "v"(av[6]), [av7] "v"(av[7]), [la] "s"(la)
    : "v39", "v64", "v65", "v66", "v67", "v68", "v69", "v70", "v71", "v72", "v73", "v74", "v75", "v76", "v77", "v78", "v79", "v96", "v97", "v98", "v99", "v100", "v101", "v102", "v103", "v104", "v105", "v106", "v107", "v108", "v109", "v110", "v111", "v112", "v113", "v114", "v115", "v116", "v117", "v118", "v119", "v120", "v121", "v122", "v123", "v124", "v125", "v126", "v127", "vcc", "scc", "m0", "memory");
}
__device__ __forceinline__ void fast_st0(const u32x32 (&W)[4], u32x16& pd, unsigned ea, unsigned da, int& b, int& lf, int cp, unsigned up, unsigned sa, unsigned mu, unsigned ad,
    unsigned rw, unsigned ab, const char* p1, const char* p0, unsigned co0, unsigned co1, int po, int pf, unsigned w0, unsigned sl, unsigned dv, const char* db, const char* pg) {
  unsigned st;
  asm volatile("s_barrier\n\t"
    "ds_read_b128 v[96:99], %[ea] offset:0\n\t"
    "ds_read_b128 v[100:103], %[ea] offset:2048\n\t"
    "ds_read_b128 v[104:107], %[da] offset:0\n\t"
    "ds_read_b128 v[108:111], %[da] offset:2048\n\t"
    "ds_read_b128 v[112:115], %[ea] offset:4096\n\t"
    "ds_read_b128 v[116:119], %[ea] offset:6144\n\t"
    "s_branch 2f\n\t"
    ".p2align 6\n\t"
    "1:\n\t"
    "s_barrier\n\t"
    "2:\n\t"
    "s_waitcnt lgkmcnt(4)\n\t"
    "v_mfma_f32_16x16x32_bf16 v[64:67], v[128:131], v[96:99], 0\n\t"
    "ds_read_b128 v[120:123], %[da] offset:4096\n\t"
    "v_mfma_f32_16x16x32_bf16 v[68:71], v[132:135], v[96:99], 0\n\t"
    "ds_read_b128 v[124:127], %[da] offset:6144\n\t"
    "v_mfma_f32_16x16x32_bf16 v[72:75], v[128:131], v[100:103], 0\n\t"
    "v_mfma_f32_16x16x32_bf16 v[76:79], v[132:135], v[100:103], 0\n\t"
    "s_waitcnt lgkmcnt(4)\n\t"
    "v_mfma_f32_16x16x32_bf16 v[64:67], v[136:139], v[104:107], v[64:67]\n\t"
    "ds_read_b128 v[96:99], %[ea] offset:8192\n\t"
    "v_mfma_f32_16x16x32_bf16 v[68:71], v[140:143], v[104:107], v[68:71]\n\t"
    "ds_read_b128 v[100:103], %[ea] offset:10240\n\t"
    "v_mfma_f32_16x16x32_bf16 v[72:75], v[136:139], v[108:111], v[72:75]\n\t"
    "v_mfma_f32_16x16x32_bf16 v[76:79], v[140:143], v[108:111], v[76:79]\n\t"
    "s_cmp_eq_u32 %[w0], 1\n\t"
    "s_cbranch_scc1 7f\n\t"
    "8:\n\t"
    "s_waitcnt lgkmcnt(4)\n\t"
    "v_mfma_f32_16x16x32_bf16 v[64:67], v[144:147], v[112:115], v[64:67]\n\t"
    "ds_read_b128 v[104:107], %[da] offset:8192\n\t"
    "v_mfma_f32_16x16x32_bf16 v[68:71], v[148:151], v[112:115], v[68:71]\n\t"
    "ds_read_b128 v[108:111], %[da] offset:10240\n\t"
    "v_mfma_f32_16x16x32_bf16 v[72:75], v[144:147], v[116:119], v[72:75]\n\t"
    "v_mfma_f32_16x16x32_bf16 v[76:79], v[148:151], v[116:119], v[76:79]\n\t"
    "s_waitcnt lgkmcnt(4)\n\t"
    "v_mfma_f32_16x16x32_bf16 v[64:67], v[152:155], v[120:123], v[64:67]\n\t"
    "ds_read_b128 v[112:115], %[ea] offset:12288\n\t"
    "v_mfma_f32_16x16x32_bf16 v[68:71], v[156:159], v[120:123], v[68:71]\n\t"
    "ds_read_b128 v[116:119], %[ea] offset:14336\n\t"
    "v_mfma_f32_16x16x32_bf16 v[72:75], v[152:155], v[124:127], v[72:75]\n\t"
    "v_mfma_f32_16x16x32_bf16 v[76:79], v[156:159], v[124:127], v[76:79]\n\t"
    "s_waitcnt lgkmcnt(4)\n\t"
    "v_mfma_f32_16x16x32_bf16 v[64:67], v[160:163], v[96:99], v[64:67]\n\t"
    "ds_read_b128 v[120:123], %[da] offset:12288\n\t"
    "v_mfma_f32_16x16x32_bf16 v[68:71], v[164:167], v[96:99], v[68:71]\n\t"
    "ds_read_b128 v[124:127], %[da] offset:14336\n\t"
    "v_mfma_f32_16x16x32_bf16 v[72:75], v[160:163], v[100:103], v[72:75]\n\t"
    "v_mfma_f32_16x16x32_bf16 v[76:79], v[164:167], v[100:103], v[76:79]\n\t"
    "s_waitcnt lgkmcnt(4)\n\t"
    "v_mfma_f32_16x16x32_bf16 v[64:67], v[168:171], v[104:107], v[64:67]\n\t"
    "ds_read_b128 v[96:99], %[ea] offset:16384\n\t"
    "v_mfma_f32_16x16x32_bf16 v[68:71], v[172:175], v[104:107], v[68:71]\n\t"
    "ds_read_b128 v[100:103], %[ea] offset:18432\n\t"
    "v_mfma_f32_16x16x32_bf16 v[72:75], v[168:171], v[108:111], v[72:75]\n\t"
    "v_mfma_f32_16x16x32_bf16 v[76:79], v[172:175], v[108:111], v[76:79]\n\t"
    "s_waitcnt lgkmcnt(4)\n\t"
    "v_mfma_f32_16x16x32_bf16 v[64:67], v[176:179], v[112:115], v[64:67]\n\t"
    "ds_read_b128 v[104:107], %[da] offset:16384\n\t"
    "v_mfma_f32_16x16x32_bf16 v[68:71], v[180:183], v[112:115], v[68:71]\n\t"
    "ds_read_b128 v[108:111], %[da] offset:18432\n\t"
    "v_mfma_f32_16x16x32_bf16 v[72:75], v[176:179], v[116:119], v[72:75]\n\t"
    "v_mfma_f32_16x16x32_bf16 v[76:79], v[180:183], v[116:119], v[76:79]\n\t"
    "s_waitcnt lgkmcnt(4)\n\t"
    "v_mfma_f32_16x16x32_bf16 v[64:67], v[184:187], v[120:123], v[64:67]\n\t"
    "ds_read_b128 v[112:115], %[ea] offset:20480\n\t"
    "v_mfma_f32_16x16x32_bf16 v[68:71], v[188:191], v[120:123], v[68:71]\n\t"
    "ds_read_b128 v[116:119], %[ea] offset:22528\n\t"
    "v_mfma_f32_16x16x32_bf16 v[72:75], v[184:187], v[124:127], v[72:75]\n\t"
    "v_mfma_f32_16x16x32_bf16 v[76:79], v[188:191], v[124:127], v[76:79]\n\t"
    "s_waitcnt vmcnt(0)\n\t"
    "s_barrier\n\t"
    "s_waitcnt lgkmcnt(4)\n\t"
    "v_mfma_f32_16x16x32_bf16 v[64:67], v[192:195], v[96:99], v[64:67]\n\t"
    "ds_read_b128 v[120:123], %[da] offset:20480\n\t"
    "v_mfma_f32_16x16x32_bf16 v[68:71], v[196:199], v[96:99], v[68:71]\n\t"
    "ds_read_b128 v[124:127], %[da] offset:22528\n\t"
    "v_mfma_f32_16x16x32_bf16 v[72:75], v[192:195], v[100:103], v[72:75]\n\t"
    "ds_read_b128 v[48:51], %[rw] offset:0\n\t"
    "v_mfma_f32_16x16x32_bf16 v[76:79], v[196:199], v[100:103], v[76:79]\n\t"
    "ds_read_b128 v[52:55], %[rw] offset:1024\n\t"
    "s_cmp_eq_u32 %[w0], 0\n\t"
    "s_cbranch_scc1 5f\n\t"
    "6:\n\t"
    "s_waitcnt lgkmcnt(6)\n\t"
    "v_mfma_f32_16x16x32_bf16 v[64:67], v[200:203], v[104:107], v[64:67]\n\t"
    "ds_read_b128 v[96:99], %[ea] offset:24576\n\t"
    "v_mfma_f32_16x16x32_bf16 v[68:71], v[204:207], v[104:107], v[68:71]\n\t"
    "ds_read_b128 v[100:103], %[ea] offset:26624\n\t"
    "v_mfma_f32_16x16x32_bf16 v[72:75], v[200:203], v[108:111], v[72:75]\n\t"
    "ds_read_b128 v[56:59], %[rw] offset:2048\n\t"
    "v_mfma_f32_16x16x32_bf16 v[76:79], v[204:207], v[108:111], v[76:79]\n\t"
    "ds_read_b128 v[60:63], %[rw] offset:3072\n\t"
    "s_waitcnt lgkmcnt(0)\n\t"
    "v_mfma_f32_16x16x32_bf16 v[64:67], v[208:211], v[112:115], v[64:67]\n\t"
    "ds_read_b128 v[104:107], %[da] offset:24576\n\t"
    "v_add_f32 v48, v48, v80\n\t"
    "v_add_f32 v49, v49, v81\n\t"
    "v_mfma_f32_16x16x32_bf16 v[68:71], v[212:215], v[112:115], v[68:71]\n\t"
    "ds_read_b128 v[108:111], %[da] offset:26624\n\t"
    "v_add_f32 v50, v50, v82\n\t"
    "v_add_f32 v51, v51, v83\n\t"
    "v_mfma_f32_16x16x32_bf16 v[72:75], v[208:211], v[116:119], v[72:75]\n\t"
    "ds_read_b32 v39, %[sa]\n\t"
    "v_add_f32 v52, v52, v84\n\t"
    "v_mfma_f32_16x16x32_bf16 v[76:79], v[212:215], v[116:119], v[76:79]\n\t"
    "v_add_f32 v53, v53, v85\n\t"
    "v_add_f32 v54, v54, v86\n\t"
    "v_mfma_f32_16x16x32_bf16 v[64:67], v[216:219], v[120:123], v[64:67]\n\t"
    "ds_read_b128 v[112:115], %[ea] offset:28672\n\t"
    "v_add_f32 v55, v55, v87\n\t"
    "v_add_f32 v56, v56, v88\n\t"
    "v_mfma_f32_16x16x32_bf16 v[68:71], v[220:223], v[120:123], v[68:71]\n\t"
    "ds_read_b128 v[116:119], %[ea] offset:30720\n\t"
    "v_add_f32 v57, v57, v89\n\t"
    "v_mfma_f32_16x16x32_bf16 v[72:75], v[216:219], v[124:127], v[72:75]\n\t"
    "v_xor_b32 %[ea], 0x10000, %[ea]\n\t"
    "v_add_f32 v58, v58, v90\n\t"
    "v_add_f32 v59, v59, v91\n\t"
    "v_mfma_f32_16x16x32_bf16 v[76:79], v[220:223], v[124:127], v[76:79]\n\t"
    "v_add_f32 v60, v60, v92\n\t"
    "v_mfma_f32_16x16x32_bf16 v[64:67], v[224:227], v[96:99], v[64:67]\n\t"
    "ds_read_b128 v[120:123], %[da] offset:28672\n\t"
    "v_add_f32 v61, v61, v93\n\t"
    "v_add_f32 v62, v62, v94\n\t"
    "v_mfma_f32_16x16x32_bf16 v[68:71], v[228:231], v[96:99], v[68:71]\n\t"
    "ds_read_b128 v[124:127], %[da] offset:30720\n\t"
    "v_add_f32 v63, v63, v95\n\t"
    "v_cvt_pk_bf16_f32 v40, v48, v49\n\t"
    "v_mfma_f32_16x16x32_bf16 v[72:75], v[224:227], v[100:103], v[72:75]\n\t"
    "v_xor_b32 %[da], 0x10000, %[da]\n\t"
    "v_cvt_pk_bf16_f32 v41, v50, v51\n\t"
    "v_mfma_f32_16x16x32_bf16 v[76:79], v[228:231], v[100:103], v[76:79]\n\t"
    "v_cvt_pk_bf16_f32 v42, v52, v53\n\t"
    "v_cvt_pk_bf16_f32 v43, v54, v55\n\t"
    "s_waitcnt lgkmcnt(5)\n\t"
    "v_mfma_f32_16x16x32_bf16 v[64:67], v[232:235], v[104:107], v[64:67]\n\t"
    "ds_read_b128 v[96:99], %[ea] offset:0\n\t"
    "global_store_dwordx4 %[co0], v[40:43], s[36:37] sc1\n\t"
    "v_cvt_pk_bf16_f32 v44, v56, v57\n\t"
    "v_mfma_f32_16x16x32_bf16 v[68:71], v[236:239], v[104:107], v[68:71]\n\t"
    "ds_read_b128 v[100:103], %[ea] offset:2048\n\t"
    "v_cvt_pk_bf16_f32 v45, v58, v59\n\t"
    "v_mfma_f32_16x16x32_bf16 v[72:75], v[232:235], v[108:111], v[72:75]\n\t"
    "v_cvt_pk_bf16_f32 v46, v60, v61\n\t"
    "v_cvt_pk_bf16_f32 v47, v62, v63\n\t"
    "v_mfma_f32_16x16x32_bf16 v[76:79], v[236:239], v[108:111], v[76:79]\n\t"
    "global_store_dwordx4 %[co1], v[44:47], s[36:37] sc1\n\t"
    "s_waitcnt lgkmcnt(4)\n\t"
    "v_mfma_f32_16x16x32_bf16 v[64:67], v[240:243], v[112:115], v[64:67]\n\t"
    "ds_read_b128 v[104:107], %[da] offset:0\n\t"
    "v_mad_u32_u24 v39, v39, %[mu], %[ad]\n\t"
    "v_mfma_f32_16x16x32_bf16 v[68:71], v[244:247], v[112:115], v[68:71]\n\t"
    "ds_read_b128 v[108:111], %[da] offset:2048\n\t"
    "v_cmp_ge_i32 vcc, %[lf], v39\n\t"
    "s_cmp_eq_u64 vcc, 0\n\t"
    "s_addc_u32 %[lf], %[lf], 0\n\t"
    "v_mfma_f32_16x16x32_bf16 v[72:75], v[240:243], v[116:119], v[72:75]\n\t"
    "v_mfma_f32_16x16x32_bf16 v[76:79], v[244:247], v[116:119], v[76:79]\n\t"
    "v_cmp_ge_i32 vcc, %[lf], v39\n\t"
    "s_cmp_eq_u64 vcc, 0\n\t"
    "s_addc_u32 %[lf], %[lf], 0\n\t"
    "s_waitcnt lgkmcnt(4)\n\t"
    "v_mfma_f32_16x16x32_bf16 v[80:83], v[248:251], v[120:123], v[64:67]\n\t"
    "ds_read_b128 v[112:115], %[ea] offset:4096\n\t"
    "s_min_i32 %[lf], %[lf], %[cp]\n\t"
    "s_add_u32 %[b], %[b], 1\n\t"
    "v_mfma_f32_16x16x32_bf16 v[84:87], v[252:255], v[120:123], v[68:71]\n\t"
    "ds_read_b128 v[116:119], %[ea] offset:6144\n\t"
    "v_mfma_f32_16x16x32_bf16 v[88:91], v[248:251], v[124:127], v[72:75]\n\t"
    "v_mfma_f32_16x16x32_bf16 v[92:95], v[252:255], v[124:127], v[76:79]\n\t"
    "s_mov_b64 s[36:37], s[38:39]\n\t"
    "s_add_u32 s38, s38, %[ab]\n\t"
    "s_addc_u32 s39, s39, 0\n\t"
    "s_sub_u32 m0, %[lf], %[b]\n\t"
    "s_sub_u32 m0, m0, 1\n\t"
    "s_cmp_lt_u32 m0, %[up]\n\t"
    "s_cbranch_scc1 1b\n\t"
    "s_waitcnt lgkmcnt(0)\n\t"
    "s_nop 4\n\t"
    "s_branch 9f\n\t"
    "5:\n\t"
    "s_add_u32 %[st], %[b], %[po]\n\t"
    "s_max_i32 %[st], %[st], %[p0]\n\t"
    "v_mov_b32 v39, %[st]\n\t"
    "v_mov_b32 v38, 0\n\t"
    "s_mov_b64 exec, 1\n\t"
    "global_store_dword v38, v39, %[pg] sc1\n\t"
    "s_mov_b64 exec, -1\n\t"
    "s_mov_b32 %[p0], %[p1]\n\t"
    "s_branch 6b\n\t"
    "7:\n\t"
    "s_cmp_ge_i32 %[lf], %[cp]\n\t"
    "s_cbranch_scc1 8b\n\t"
    "s_mov_b32 m0, %[sl]\n\t"
    "s_nop 0\n\t"
    "global_load_lds_dword %[dv], %[db] sc1\n\t"
    "s_branch 8b\n\t"
    "9:"
    : "=&{v[80:95]}"(pd), [ea] "+&v"(ea), [da] "+&v"(da), [b] "+&s"(b), [lf] "+&s"(lf), "+{s[36:37]}"(p1), "+{s[38:39]}"(p0), [sa] "+&v"(sa), [st] "=&s"(st), [sl] "+&s"(sl), [p0] "+&s"(pf)
    : [rw] "v"(rw), [ab] "s"(ab), [cp] "s"(cp), [mu] "s"(mu), [ad] "v"(ad), [up] "s"(up), "{v[128:159]}"(W[0]), "{v[160:191]}"(W[1]), "{v[192:223]}"(W[2]), "{v[224:255]}"(W[3]), [co0] "v"(co0), [w0] "s"(w0), [dv] "v"(dv), [db] "s"(db), [pg] "s"(pg), [po] "s"(po - 1), [p1] "s"(b + po), [co1] "v"(co1)
    : "v38", "v39", "v40", "v41", "v42", "v43", "v44", "v45", "v46", "v47", "v48", "v49", "v50", "v51", "v52", "v53", "v54", "v55", "v56", "v57", "v58", "v59", "v60", "v61", "v62", "v63", "v64", "v65", "v66", "v67", "v68", "v69", "v70", "v71", "v72", "v73", "v74", "v75", "v76", "v77", "v78", "v79", "v96", "v97", "v98", "v99", "v100", "v101", "v102", "v103", "v104", "v105", "v106", "v107", "v108", "v109", "v110", "v111", "v112", "v113", "v114", "v115", "v116", "v117", "v118", "v119", "v120", "v121", "v122", "v123", "v124", "v125", "v126", "v127", "vcc", "scc", "m0", "memory");
}
__device__ __forceinline__ void fast_st1(const u32x32 (&W)[4], u32x16& pd, unsigned ea, unsigned da, int& b, int& lf, int cp, unsigned up, unsigned sa, unsigned mu, unsigned ad,
    unsigned rw, unsigned ab, const char* p1, const char* p0, unsigned co0, unsigned co1, int po, int pf, unsigned w0, unsigned sl, unsigned dv, const char* db, const char* pg) {
  unsigned st;
  asm volatile("s_barrier\n\t"
    "ds_read_b128 v[96:99], %[ea] offset:0\n\t"
    "ds_read_b128 v[100:103], %[ea] offset:2048\n\t"
    "ds_read_b128 v[104:107], %[da] offset:0\n\t"
    "ds_read_b128 v[108:111], %[da] offset:2048\n\t"
    "ds_read_b128 v[112:115], %[ea] offset:4096\n\t"
    "ds_read_b128 v[116:119], %[ea] offset:6144\n\t"
    "s_branch 2f\n\t"
    ".p2align 6\n\t"
    "1:\n\t"
    "s_barrier\n\t"
    "2:\n\t"
    "s_waitcnt lgkmcnt(4)\n\t"
    "v_mfma_f32_16x16x32_bf16 v[64:67], v[128:131], v[96:99], 0\n\t"
    "ds_read_b128 v[120:123], %[da] offset:4096\n\t"
    "v_mfma_f32_16x16x32_bf16 v[68:71], v[132:135], v[96:99], 0\n\t"
    "ds_read_b128 v[124:127], %[da] offset:6144\n\t"
    "v_mfma_f32_16x16x32_bf16 v[72:75], v[128:131], v[100:103], 0\n\t"
    "v_mfma_f32_16x16x32_bf16 v[76:79], v[132:135], v[100:103], 0\n\t"
    "s_waitcnt lgkmcnt(4)\n\t"
    "v_mfma_f32_16x16x32_bf16 v[64:67], v[136:139], v[104:107], v[64:67]\n\t"
    "ds_read_b128 v[96:99], %[ea] offset:8192\n\t"
    "v_mfma_f32_16x16x32_bf16 v[68:71], v[140:143], v[104:107], v[68:71]\n\t"
    "ds_read_b128 v[100:103], %[ea] offset:10240\n\t"
    "v_mfma_f32_16x16x32_bf16 v[72:75], v[136:139], v[108:111], v[72:75]\n\t"
    "v_mfma_f32_16x16x32_bf16 v[76:79], v[140:143], v[108:111], v[76:79]\n\t"
    "s_cmp_eq_u32 %[w0], 1\n\t"
    "s_cbranch_scc1 7f\n\t"
    "8:\n\t"
    "s_waitcnt lgkmcnt(4)\n\t"
    "v_mfma_f32_16x16x32_bf16 v[64:67], v[144:147], v[112:115], v[64:67]\n\t"
    "ds_read_b128 v[104:107], %[da] offset:8192\n\t"
    "v_mfma_f32_16x16x32_bf16 v[68:71], v[148:151], v[112:115], v[68:71]\n\t"
    "ds_read_b128 v[108:111], %[da] offset:10240\n\t"
    "v_mfma_f32_16x16x32_bf16 v[72:75], v[144:147], v[116:119], v[72:75]\n\t"
    "v_mfma_f32_16x16x32_bf16 v[76:79], v[148:151], v[116:119], v[76:79]\n\t"
    "s_waitcnt lgkmcnt(4)\n\t"
    "v_mfma_f32_16x16x32_bf16 v[64:67], v[152:155], v[120:123], v[64:67]\n\t"
    "ds_read_b128 v[112:115], %[ea] offset:12288\n\t"
    "v_mfma_f32_16x16x32_bf16 v[68:71], v[156:159], v[120:123], v[68:71]\n\t"
    "ds_read_b128 v[116:119], %[ea] offset:14336\n\t"
    "v_mfma_f32_16x16x32_bf16 v[72:75], v[152:155], v[124:127], v[72:75]\n\t"
    "v_mfma_f32_16x16x32_bf16 v[76:79], v[156:159], v[124:127], v[76:79]\n\t"
    "s_waitcnt lgkmcnt(4)\n\t"
    "v_mfma_f32_16x16x32_bf16 v[64:67], v[160:163], v[96:99], v[64:67]\n\t"
    "ds_read_b128 v[120:123], %[da] offset:12288\n\t"
    "v_mfma_f32_16x16x32_bf16 v[68:71], v[164:167], v[96:99], v[68:71]\n\t"
    "ds_read_b128 v[124:127], %[da] offset:14336\n\t"
    "v_mfma_f32_16x16x32_bf16 v[72:75], v[160:163], v[100:103], v[72:75]\n\t"
    "v_mfma_f32_16x16x32_bf16 v[76:79], v[164:167], v[100:103], v[76:79]\n\t"
    "s_waitcnt lgkmcnt(4)\n\t"
    "v_mfma_f32_16x16x32_bf16 v[64:67], v[168:171], v[104:107], v[64:67]\n\t"
    "ds_read_b128 v[96:99], %[ea] offset:16384\n\t"
    "v_mfma_f32_16x16x32_bf16 v[68:71], v[172:175], v[104:107], v[68:71]\n\t"
    "ds_read_b128 v[100:103], %[ea] offset:18432\n\t"
    "v_mfma_f32_16x16x32_bf16 v[72:75], v[168:171], v[108:111], v[72:75]\n\t"
    "v_mfma_f32_16x16x32_bf16 v[76:79], v[172:175], v[108:111], v[76:79]\n\t"
    "s_waitcnt lgkmcnt(4)\n\t"
    "v_mfma_f32_16x16x32_bf16 v[64:67], v[176:179], v[112:115], v[64:67]\n\t"
    "ds_read_b128 v[104:107], %[da] offset:16384\n\t"
    "v_mfma_f32_16x16x32_bf16 v[68:71], v[180:183], v[112:115], v[68:71]\n\t"
    "ds_read_b128 v[108:111], %[da] offset:18432\n\t"
    "v_mfma_f32_16x16x32_bf16 v[72:75], v[176:179], v[116:119], v[72:75]\n\t"
    "v_mfma_f32_16x16x32_bf16 v[76:79], v[180:183], v[116:119], v[76:79]\n\t"
    "s_waitcnt lgkmcnt(4)\n\t"
    "v_mfma_f32_16x16x32_bf16 v[64:67], v[184:187], v[120:123], v[64:67]\n\t"
    "ds_read_b128 v[112:115], %[ea] offset:20480\n\t"
    "v_mfma_f32_16x16x32_bf16 v[68:71], v[188:191], v[120:123], v[68:71]\n\t"
    "ds_read_b128 v[116:119], %[ea] offset:22528\n\t"
    "v_mfma_f32_16x16x32_bf16 v[72:75], v[184:187], v[124:127], v[72:75]\n\t"
    "v_mfma_f32_16x16x32_bf16 v[76:79], v[188:191], v[124:127], v[76:79]\n\t"
    "s_waitcnt vmcnt(0)\n\t"
    "s_barrier\n\t"
    "s_waitcnt lgkmcnt(4)\n\t"
    "v_mfma_f32_16x16x32_bf16 v[64:67], v[192:195], v[96:99], v[64:67]\n\t"
    "ds_read_b128 v[120:123], %[da] offset:20480\n\t"
    "v_mfma_f32_16x16x32_bf16 v[68:71], v[196:199], v[96:99], v[68:71]\n\t"
    "ds_read_b128 v[124:127], %[da] offset:22528\n\t"
    "v_mfma_f32_16x16x32_bf16 v[72:75], v[192:195], v[100:103], v[72:75]\n\t"
    "ds_read_b128 v[48:51], %[rw] offset:0\n\t"
    "v_mfma_f32_16x16x32_bf16 v[76:79], v[196:199], v[100:103], v[76:79]\n\t"
    "ds_read_b128 v[52:55], %[rw] offset:1024\n\t"
    "s_cmp_eq_u32 %[w0], 0\n\t"
    "s_cbranch_scc1 5f\n\t"
    "6:\n\t"
    "s_waitcnt lgkmcnt(6)\n\t"
    "v_mfma_f32_16x16x32_bf16 v[64:67], v[200:203], v[104:107], v[64:67]\n\t"
    "ds_read_b128 v[96:99], %[ea] offset:24576\n\t"
    "v_mfma_f32_16x16x32_bf16 v[68:71], v[204:207], v[104:107], v[68:71]\n\t"
    "ds_read_b128 v[100:103], %[ea] offset:26624\n\t"
    "v_mfma_f32_16x16x32_bf16 v[72:75], v[200:203], v[108:111], v[72:75]\n\t"
    "ds_read_b128 v[56:59], %[rw] offset:2048\n\t"
    "v_mfma_f32_16x16x32_bf16 v[76:79], v[204:207], v[108:111], v[76:79]\n\t"
    "ds_read_b128 v[60:63], %[rw] offset:3072\n\t"
    "s_waitcnt lgkmcnt(0)\n\t"
    "v_mfma_f32_16x16x32_bf16 v[64:67], v[208:211], v[112:115], v[64:67]\n\t"
    "ds_read_b128 v[104:107], %[da] offset:24576\n\t"
    "v_add_f32 v48, v48, v80\n\t"
    "v_add_f32 v49, v49, v81\n\t"
    "v_add_f32 v50, v50, v82\n\t"
    "v_mfma_f32_16x16x32_bf16 v[68:71], v[212:215], v[112:115], v[68:71]\n\t"
    "ds_read_b128 v[108:111], %[da] offset:26624\n\t"
    "v_add_f32 v51, v51, v83\n\t"
    "v_add_f32 v52, v52, v84\n\t"
    "v_add_f32 v53, v53, v85\n\t"
    "v_mfma_f32_16x16x32_bf16 v[72:75], v[208:211], v[116:119], v[72:75]\n\t"
    "ds_read_b32 v39, %[sa]\n\t"
    "v_add_f32 v54, v54, v86\n\t"
    "v_add_f32 v55, v55, v87\n\t"
    "v_add_f32 v56, v56, v88\n\t"
    "v_mfma_f32_16x16x32_bf16 v[76:79], v[212:215], v[116:119], v[76:79]\n\t"
    "v_add_f32 v57, v57, v89\n\t"
    "v_add_f32 v58, v58, v90\n\t"
    "v_add_f32 v59, v59, v91\n\t"
    "v_mfma_f32_16x16x32_bf16 v[64:67], v[216:219], v[120:123], v[64:67]\n\t"
    "ds_read_b128 v[112:115], %[ea] offset:28672\n\t"
    "v_add_f32 v60, v60, v92\n\t"
    "v_add_f32 v61, v61, v93\n\t"
    "v_add_f32 v62, v62, v94\n\t"
    "v_mfma_f32_16x16x32_bf16 v[68:71], v[220:223], v[120:123], v[68:71]\n\t"
    "ds_read_b128 v[116:119], %[ea] offset:30720\n\t"
    "v_add_f32 v63, v63, v95\n\t"
    "v_mul_f32 v40, %[nl], v48\n\t"
    "v_mul_f32 v41, %[nl], v49\n\t"
    "v_mfma_f32_16x16x32_bf16 v[72:75], v[216:219], v[124:127], v[72:75]\n\t"
    "v_xor_b32 %[ea], 0x10000, %[ea]\n\t"
    "v_mul_f32 v42, %[nl], v50\n\t"
    "v_mul_f32 v43, %[nl], v51\n\t"
    "v_mul_f32 v44, %[nl], v56\n\t"
    "v_mfma_f32_16x16x32_bf16 v[76:79], v[220:223], v[124:127], v[76:79]\n\t"
    "v_mul_f32 v45, %[nl], v57\n\t"
    "v_mul_f32 v46, %[nl], v58\n\t"
    "v_mul_f32 v47, %[nl], v59\n\t"
    "v_mfma_f32_16x16x32_bf16 v[64:67], v[224:227], v[96:99], v[64:67]\n\t"
    "ds_read_b128 v[120:123], %[da] offset:28672\n\t"
    "v_exp_f32 v40, v40\n\t"
    "v_exp_f32 v41, v41\n\t"
    "v_exp_f32 v42, v42\n\t"
    "v_mfma_f32_16x16x32_bf16 v[68:71], v[228:231], v[96:99], v[68:71]\n\t"
    "ds_read_b128 v[124:127], %[da] offset:30720\n\t"
    "v_exp_f32 v43, v43\n\t"
    "v_exp_f32 v44, v44\n\t"
    "v_exp_f32 v45, v45\n\t"
    "v_mfma_f32_16x16x32_bf16 v[72:75], v[224:227], v[100:103], v[72:75]\n\t"
    "v_xor_b32 %[da], 0x10000, %[da]\n\t"
    "v_exp_f32 v46, v46\n\t"
    "v_exp_f32 v47, v47\n\t"
    "v_add_f32 v40, 1.0, v40\n\t"
    "v_mfma_f32_16x16x32_bf16 v[76:79], v[228:231], v[100:103], v[76:79]\n\t"
    "v_add_f32 v41, 1.0, v41\n\t"
    "v_add_f32 v42, 1.0, v42\n\t"
    "v_add_f32 v43, 1.0, v43\n\t"
    "s_waitcnt lgkmcnt(5)\n\t"
    "v_mfma_f32_16x16x32_bf16 v[64:67], v[232:235], v[104:107], v[64:67]\n\t"
    "ds_read_b128 v[96:99], %[ea] offset:0\n\t"
    "v_add_f32 v44, 1.0, v44\n\t"
    "v_add_f32 v45, 1.0, v45\n\t"
    "v_add_f32 v46, 1.0, v46\n\t"
    "v_mfma_f32_16x16x32_bf16 v[68:71], v[236:239], v[104:107], v[68:71]\n\t"
    "ds_read_b128 v[100:103], %[ea] offset:2048\n\t"
    "v_add_f32 v47, 1.0, v47\n\t"
    "v_rcp_f32 v40, v40\n\t"
    "v_rcp_f32 v41, v41\n\t"
    "v_mfma_f32_16x16x32_bf16 v[72:75], v[232:235], v[108:111], v[72:75]\n\t"
    "v_rcp_f32 v42, v42\n\t"
    "v_rcp_f32 v43, v43\n\t"
    "v_rcp_f32 v44, v44\n\t"
    "v_mfma_f32_16x16x32_bf16 v[76:79], v[236:239], v[108:111], v[76:79]\n\t"
    "v_rcp_f32 v45, v45\n\t"
    "v_rcp_f32 v46, v46\n\t"
    "v_rcp_f32 v47, v47\n\t"
    "s_waitcnt lgkmcnt(4)\n\t"
    "v_mfma_f32_16x16x32_bf16 v[64:67], v[240:243], v[112:115], v[64:67]\n\t"
    "ds_read_b128 v[104:107], %[da] offset:0\n\t"
    "v_mad_u32_u24 v39, v39, %[mu], %[ad]\n\t"
    "v_mul_f32 v40, v48, v40\n\t"
    "v_mul_f32 v41, v49, v41\n\t"
    "v_mul_f32 v42, v50, v42\n\t"
    "v_mfma_f32_16x16x32_bf16 v[68:71], v[244:247], v[112:115], v[68:71]\n\t"
    "ds_read_b128 v[108:111], %[da] offset:2048\n\t"
    "v_cmp_ge_i32 vcc, %[lf], v39\n\t"
    "s_cmp_eq_u64 vcc, 0\n\t"
    "s_addc_u32 %[lf], %[lf], 0\n\t"
    "v_mul_f32 v43, v51, v43\n\t"
    "v_mul_f32 v44, v56, v44\n\t"
    "v_mul_f32 v45, v57, v45\n\t"
    "v_mfma_f32_16x16x32_bf16 v[72:75], v[240:243], v[116:119], v[72:75]\n\t"
    "v_mul_f32 v46, v58, v46\n\t"
    "v_mul_f32 v47, v59, v47\n\t"
    "v_mul_f32 v40, v40, v52\n\t"
    "v_mfma_f32_16x16x32_bf16 v[76:79], v[244:247], v[116:119], v[76:79]\n\t"
    "v_cmp_ge_i32 vcc, %[lf], v39\n\t"
    "s_cmp_eq_u64 vcc, 0\n\t"
    "s_addc_u32 %[lf], %[lf], 0\n\t"
    "v_mul_f32 v41, v41, v53\n\t"
    "v_mul_f32 v42, v42, v54\n\t"
    "v_mul_f32 v43, v43, v55\n\t"
    "s_waitcnt lgkmcnt(4)\n\t"
    "v_mfma_f32_16x16x32_bf16 v[80:83], v[248:251], v[120:123], v[64:67]\n\t"
    "ds_read_b128 v[112:115], %[ea] offset:4096\n\t"
    "s_min_i32 %[lf], %[lf], %[cp]\n\t"
    "s_add_u32 %[b], %[b], 1\n\t"
    "v_mul_f32 v44, v44, v60\n\t"
    "v_mul_f32 v45, v45, v61\n\t"
    "v_mul_f32 v46, v46, v62\n\t"
    "v_mfma_f32_16x16x32_bf16 v[84:87], v[252:255], v[120:123], v[68:71]\n\t"
    "ds_read_b128 v[116:119], %[ea] offset:6144\n\t"
    "v_mul_f32 v47, v47, v63\n\t"
    "v_cvt_pk_bf16_f32 v48, v40, v41\n\t"
    "v_cvt_pk_bf16_f32 v49, v42, v43\n\t"
    "v_mfma_f32_16x16x32_bf16 v[88:91], v[248:251], v[124:127], v[72:75]\n\t"
    "v_cvt_pk_bf16_f32 v50, v44, v45\n\t"
    "v_cvt_pk_bf16_f32 v51, v46, v47\n\t"
    "s_nop 1\n\t"
    "v_mfma_f32_16x16x32_bf16 v[92:95], v[252:255], v[124:127], v[76:79]\n\t"
    "v_permlane16_swap_b32 v48, v50\n\t"
    "v_permlane16_swap_b32 v49, v51\n\t"
    "global_store_dwordx4 %[co0], v[48:51], s[36:37] sc1\n\t"
    "s_mov_b64 s[36:37], s[38:39]\n\t"
    "s_add_u32 s38, s38, %[ab]\n\t"
    "s_addc_u32 s39, s39, 0\n\t"
    "s_sub_u32 m0, %[lf], %[b]\n\t"
    "s_sub_u32 m0, m0, 1\n\t"
    "s_cmp_lt_u32 m0, %[up]\n\t"
    "s_cbranch_scc1 1b\n\t"
    "s_waitcnt lgkmcnt(0)\n\t"
    "s_nop 4\n\t"
    "s_branch 9f\n\t"
    "5:\n\t"
    "s_add_u32 %[st], %[b], %[po]\n\t"
    "s_max_i32 %[st], %[st], %[p0]\n\t"
    "v_mov_b32 v39, %[st]\n\t"
    "v_mov_b32 v38, 0\n\t"
    "s_mov_b64 exec, 1\n\t"
    "global_store_dword v38, v39, %[pg] sc1\n\t"
    "s_mov_b64 exec, -1\n\t"
    "s_mov_b32 %[p0], %[p1]\n\t"
    "s_branch 6b\n\t"
    "7:\n\t"
    "s_cmp_ge_i32 %[lf], %[cp]\n\t"
    "s_cbranch_scc1 8b\n\t"
    "s_mov_b32 m0, %[sl]\n\t"
    "s_nop 0\n\t"
    "global_load_lds_dword %[dv], %[db] sc1\n\t"
    "s_branch 8b\n\t"
    "9:"
    : "=&{v[80:95]}"(pd), [ea] "+&v"(ea), [da] "+&v"(da), [b] "+&s"(b), [lf] "+&s"(lf), "+{s[36:37]}"(p1), "+{s[38:39]}"(p0), [sa] "+&v"(sa), [st] "=&s"(st), [sl] "+&s"(sl), [p0] "+&s"(pf)
    : [rw] "v"(rw), [ab] "s"(ab), [cp] "s"(cp), [mu] "s"(mu), [ad] "v"(ad), [up] "s"(up), "{v[128:159]}"(W[0]), "{v[160:191]}"(W[1]), "{v[192:223]}"(W[2]), "{v[224:255]}"(W[3]), [co0] "v"(co0), [w0] "s"(w0), [dv] "v"(dv), [db] "s"(db), [pg] "s"(pg), [po] "s"(po - 1), [p1] "s"(b + po), [nl] "s"(-LOG2E)
    : "v38", "v39", "v40", "v41", "v42", "v43", "v44", "v45", "v46", "v47", "v48", "v49", "v50", "v51", "v52", "v53", "v54", "v55", "v56", "v57", "v58", "v59", "v60", "v61", "v62", "v63", "v64", "v65", "v66", "v67", "v68", "v69", "v70", "v71", "v72", "v73", "v74", "v75", "v76", "v77", "v78", "v79", "v96", "v97", "v98", "v99", "v100", "v101", "v102", "v103", "v104", "v105", "v106", "v107", "v108", "v109", "v110", "v111", "v112", "v113", "v114", "v115", "v116", "v117", "v118", "v119", "v120", "v121", "v122", "v123", "v124", "v125", "v126", "v127", "vcc", "scc", "m0", "memory");
}
__device__ __forceinline__ void fast_st2(const u32x32 (&W)[4], u32x16& pd, unsigned ea, unsigned da, int& b, int& lf, int cp, unsigned up, unsigned sa, unsigned mu, unsigned ad,
    unsigned rw, unsigned ab, const char* p1, const char* p0, unsigned co0, unsigned co1, int po, int pf, unsigned w0, unsigned sl, unsigned dv, const char* db, const char* pg) {
  unsigned st;
  asm volatile("s_barrier\n\t"
    "ds_read_b128 v[96:99], %[ea] offset:0\n\t"
    "ds_read_b128 v[100:103], %[ea] offset:2048\n\t"
    "ds_read_b128 v[104:107], %[da] offset:0\n\t"
    "ds_read_b128 v[108:111], %[da] offset:2048\n\t"
    "ds_read_b128 v[112:115], %[ea] offset:4096\n\t"
    "ds_read_b128 v[116:119], %[ea] offset:6144\n\t"
    "s_branch 2f\n\t"
    ".p2align 6\n\t"
    "1:\n\t"
    "s_barrier\n\t"
    "2:\n\t"
    "s_waitcnt lgkmcnt(4)\n\t"
    "v_mfma_f32_16x16x32_bf16 v[64:67], v[128:131], v[96:99], 0\n\t"
    "ds_read_b128 v[120:123], %[da] offset:4096\n\t"
    "v_mfma_f32_16x16x32_bf16 v[68:71], v[132:135], v[96:99], 0\n\t"
    "ds_read_b128 v[124:127], %[da] offset:6144\n\t"
    "v_mfma_f32_16x16x32_bf16 v[72:75], v[128:131], v[100:103], 0\n\t"
    "v_mfma_f32_16x16x32_bf16 v[76:79], v[132:135], v[100:103], 0\n\t"
    "s_waitcnt lgkmcnt(4)\n\t"
    "v_mfma_f32_16x16x32_bf16 v[64:67], v[136:139], v[104:107], v[64:67]\n\t"
    "ds_read_b128 v[96:99], %[ea] offset:8192\n\t"
    "v_mfma_f32_16x16x32_bf16 v[68:71], v[140:143], v[104:107], v[68:71]\n\t"
    "ds_read_b128 v[100:103], %[ea] offset:10240\n\t"
    "v_mfma_f32_16x16x32_bf16 v[72:75], v[136:139], v[108:111], v[72:75]\n\t"
    "v_mfma_f32_16x16x32_bf16 v[76:79], v[140:143], v[108:111], v[76:79]\n\t"
    "s_cmp_eq_u32 %[w0], 1\n\t"
    "s_cbranch_scc1 7f\n\t"
    "8:\n\t"
    "s_waitcnt lgkmcnt(4)\n\t"
    "v_mfma_f32_16x16x32_bf16 v[64:67], v[144:147], v[112:115], v[64:67]\n\t"
    "ds_read_b128 v[104:107], %[da] offset:8192\n\t"
    "v_mfma_f32_16x16x32_bf16 v[68:71], v[148:151], v[112:115], v[68:71]\n\t"
    "ds_read_b128 v[108:111], %[da] offset:10240\n\t"
    "v_mfma_f32_16x16x32_bf16 v[72:75], v[144:147], v[116:119], v[72:75]\n\t"
    "v_mfma_f32_16x16x32_bf16 v[76:79], v[148:151], v[116:119], v[76:79]\n\t"
    "s_waitcnt lgkmcnt(4)\n\t"
    "v_mfma_f32_16x16x32_bf16 v[64:67], v[152:155], v[120:123], v[64:67]\n\t"
    "ds_read_b128 v[112:115], %[ea] offset:12288\n\t"
    "v_mfma_f32_16x16x32_bf16 v[68:71], v[156:159], v[120:123], v[68:71]\n\t"
    "ds_read_b128 v[116:119], %[ea] offset:14336\n\t"
    "v_mfma_f32_16x16x32_bf16 v[72:75], v[152:155], v[124:127], v[72:75]\n\t"
    "v_mfma_f32_16x16x32_bf16 v[76:79], v[156:159], v[124:127], v[76:79]\n\t"
    "s_waitcnt lgkmcnt(4)\n\t"
    "v_mfma_f32_16x16x32_bf16 v[64:67], v[160:163], v[96:99], v[64:67]\n\t"
    "ds_read_b128 v[120:123], %[da] offset:12288\n\t"
    "v_mfma_f32_16x16x32_bf16 v[68:71], v[164:167], v[96:99], v[68:71]\n\t"
    "ds_read_b128 v[124:127], %[da] offset:14336\n\t"
    "v_mfma_f32_16x16x32_bf16 v[72:75], v[160:163], v[100:103], v[72:75]\n\t"
    "v_mfma_f32_16x16x32_bf16 v[76:79], v[164:167], v[100:103], v[76:79]\n\t"
    "s_waitcnt lgkmcnt(4)\n\t"
    "v_mfma_f32_16x16x32_bf16 v[64:67], v[168:171], v[104:107], v[64:67]\n\t"
    "ds_read_b128 v[96:99], %[ea] offset:16384\n\t"
    "v_mfma_f32_16x16x32_bf16 v[68:71], v[172:175], v[104:107], v[68:71]\n\t"
    "ds_read_b128 v[100:103], %[ea] offset:18432\n\t"
    "v_mfma_f32_16x16x32_bf16 v[72:75], v[168:171], v[108:111], v[72:75]\n\t"
    "v_mfma_f32_16x16x32_bf16 v[76:79], v[172:175], v[108:111], v[76:79]\n\t"
    "s_waitcnt lgkmcnt(4)\n\t"
    "v_mfma_f32_16x16x32_bf16 v[64:67], v[176:179], v[112:115], v[64:67]\n\t"
    "ds_read_b128 v[104:107], %[da] offset:16384\n\t"
    "v_mfma_f32_16x16x32_bf16 v[68:71], v[180:183], v[112:115], v[68:71]\n\t"
    "ds_read_b128 v[108:111], %[da] offset:18432\n\t"
    "v_mfma_f32_16x16x32_bf16 v[72:75], v[176:179], v[116:119], v[72:75]\n\t"
    "v_mfma_f32_16x16x32_bf16 v[76:79], v[180:183], v[116:119], v[76:79]\n\t"
    "s_waitcnt lgkmcnt(4)\n\t"
    "v_mfma_f32_16x16x32_bf16 v[64:67], v[184:187], v[120:123], v[64:67]\n\t"
    "ds_read_b128 v[112:115], %[ea] offset:20480\n\t"
    "v_mfma_f32_16x16x32_bf16 v[68:71], v[188:191], v[120:123], v[68:71]\n\t"
    "ds_read_b128 v[116:119], %[ea] offset:22528\n\t"
    "v_mfma_f32_16x16x32_bf16 v[72:75], v[184:187], v[124:127], v[72:75]\n\t"
    "v_mfma_f32_16x16x32_bf16 v[76:79], v[188:191], v[124:127], v[76:79]\n\t"
    "s_waitcnt vmcnt(0)\n\t"
    "s_barrier\n\t"
    "s_waitcnt lgkmcnt(4)\n\t"
    "v_mfma_f32_16x16x32_bf16 v[64:67], v[192:195], v[96:99], v[64:67]\n\t"
    "ds_read_b128 v[120:123], %[da] offset:20480\n\t"
    "v_mfma_f32_16x16x32_bf16 v[68:71], v[196:199], v[96:99], v[68:71]\n\t"
    "ds_read_b128 v[124:127], %[da] offset:22528\n\t"
    "v_mfma_f32_16x16x32_bf16 v[72:75], v[192:195], v[100:103], v[72:75]\n\t"
    "ds_read_b128 v[48:51], %[rw] offset:0\n\t"
    "v_mfma_f32_16x16x32_bf16 v[76:79], v[196:199], v[100:103], v[76:79]\n\t"
    "ds_read_b128 v[52:55], %[rw] offset:1024\n\t"
    "s_cmp_eq_u32 %[w0], 0\n\t"
    "s_cbranch_scc1 5f\n\t"
    "6:\n\t"
    "s_waitcnt lgkmcnt(6)\n\t"
    "v_mfma_f32_16x16x32_bf16 v[64:67], v[200:203], v[104:107], v[64:67]\n\t"
    "ds_read_b128 v[96:99], %[ea] offset:24576\n\t"
    "v_mfma_f32_16x16x32_bf16 v[68:71], v[204:207], v[104:107], v[68:71]\n\t"
    "ds_read_b128 v[100:103], %[ea] offset:26624\n\t"
    "v_mfma_f32_16x16x32_bf16 v[72:75], v[200:203], v[108:111], v[72:75]\n\t"
    "ds_read_b128 v[56:59], %[rw] offset:2048\n\t"
    "v_mfma_f32_16x16x32_bf16 v[76:79], v[204:207], v[108:111], v[76:79]\n\t"
    "ds_read_b128 v[60:63], %[rw] offset:3072\n\t"
    "s_waitcnt lgkmcnt(0)\n\t"
    "v_mfma_f32_16x16x32_bf16 v[64:67], v[208:211], v[112:115], v[64:67]\n\t"
    "ds_read_b128 v[104:107], %[da] offset:24576\n\t"
    "v_cvt_pk_bf16_f32 v40, v80, v81\n\t"
    "v_cvt_pk_bf16_f32 v41, v82, v83\n\t"
    "v_mfma_f32_16x16x32_bf16 v[68:71], v[212:215], v[112:115], v[68:71]\n\t"
    "ds_read_b128 v[108:111], %[da] offset:26624\n\t"
    "v_cvt_pk_bf16_f32 v42, v84, v85\n\t"
    "v_mfma_f32_16x16x32_bf16 v[72:75], v[208:211], v[116:119], v[72:75]\n\t"
    "ds_read_b32 v39, %[sa]\n\t"
    "v_cvt_pk_bf16_f32 v43, v86, v87\n\t"
    "v_mfma_f32_16x16x32_bf16 v[76:79], v[212:215], v[116:119], v[76:79]\n\t"
    "global_store_dwordx4 %[co0], v[40:43], s[36:37] sc1\n\t"
    "v_mfma_f32_16x16x32_bf16 v[64:67], v[216:219], v[120:123], v[64:67]\n\t"
    "ds_read_b128 v[112:115], %[ea] offset:28672\n\t"
    "v_cvt_pk_bf16_f32 v44, v88, v89\n\t"
    "v_cvt_pk_bf16_f32 v45, v90, v91\n\t"
    "v_mfma_f32_16x16x32_bf16 v[68:71], v[220:223], v[120:123], v[68:71]\n\t"
    "ds_read_b128 v[116:119], %[ea] offset:30720\n\t"
    "v_cvt_pk_bf16_f32 v46, v92, v93\n\t"
    "v_mfma_f32_16x16x32_bf16 v[72:75], v[216:219], v[124:127], v[72:75]\n\t"
    "v_xor_b32 %[ea], 0x10000, %[ea]\n\t"
    "v_cvt_pk_bf16_f32 v47, v94, v95\n\t"
    "v_mfma_f32_16x16x32_bf16 v[76:79], v[220:223], v[124:127], v[76:79]\n\t"
    "global_store_dwordx4 %[co1], v[44:47], s[36:37] sc1\n\t"
    "v_mfma_f32_16x16x32_bf16 v[64:67], v[224:227], v[96:99], v[64:67]\n\t"
    "ds_read_b128 v[120:123], %[da] offset:28672\n\t"
    "v_cvt_pk_bf16_f32 v48, v48, v49\n\t"
    "v_cvt_pk_bf16_f32 v49, v50, v51\n\t"
    "v_mfma_f32_16x16x32_bf16 v[68:71], v[228:231], v[96:99], v[68:71]\n\t"
    "ds_read_b128 v[124:127], %[da] offset:30720\n\t"
    "v_cvt_pk_bf16_f32 v50, v52, v53\n\t"
    "v_mfma_f32_16x16x32_bf16 v[72:75], v[224:227], v[100:103], v[72:75]\n\t"
    "v_xor_b32 %[da], 0x10000, %[da]\n\t"
    "v_cvt_pk_bf16_f32 v51, v54, v55\n\t"
    "v_mfma_f32_16x16x32_bf16 v[76:79], v[228:231], v[100:103], v[76:79]\n\t"
    "global_store_dwordx4 %[co0], v[48:51], s[36:37] offset:2048 sc1\n\t"
    "s_waitcnt lgkmcnt(5)\n\t"
    "v_mfma_f32_16x16x32_bf16 v[64:67], v[232:235], v[104:107], v[64:67]\n\t"
    "ds_read_b128 v[96:99], %[ea] offset:0\n\t"
    "v_cvt_pk_bf16_f32 v56, v56, v57\n\t"
    "v_cvt_pk_bf16_f32 v57, v58, v59\n\t"
    "v_mfma_f32_16x16x32_bf16 v[68:71], v[236:239], v[104:107], v[68:71]\n\t"
    "ds_read_b128 v[100:103], %[ea] offset:2048\n\t"
    "v_cvt_pk_bf16_f32 v58, v60, v61\n\t"
    "v_mfma_f32_16x16x32_bf16 v[72:75], v[232:235], v[108:111], v[72:75]\n\t"
    "v_cvt_pk_bf16_f32 v59, v62, v63\n\t"
    "v_mfma_f32_16x16x32_bf16 v[76:79], v[236:239], v[108:111], v[76:79]\n\t"
    "global_store_dwordx4 %[co1], v[56:59], s[36:37] offset:2048 sc1\n\t"
    "s_waitcnt lgkmcnt(4)\n\t"
    "v_mfma_f32_16x16x32_bf16 v[64:67], v[240:243], v[112:115], v[64:67]\n\t"
    "ds_read_b128 v[104:107], %[da] offset:0\n\t"
    "v_mad_u32_u24 v39, v39, %[mu], %[ad]\n\t"
    "v_mfma_f32_16x16x32_bf16 v[68:71], v[244:247], v[112:115], v[68:71]\n\t"
    "ds_read_b128 v[108:111], %[da] offset:2048\n\t"
    "v_cmp_ge_i32 vcc, %[lf], v39\n\t"
    "s_cmp_eq_u64 vcc, 0\n\t"
    "s_addc_u32 %[lf], %[lf], 0\n\t"
    "v_mfma_f32_16x16x32_bf16 v[72:75], v[240:243], v[116:119], v[72:75]\n\t"
    "v_mfma_f32_16x16x32_bf16 v[76:79], v[244:247], v[116:119], v[76:79]\n\t"
    "v_cmp_ge_i32 vcc, %[lf], v39\n\t"
    "s_cmp_eq_u64 vcc, 0\n\t"
    "s_addc_u32 %[lf], %[lf], 0\n\t"
    "s_waitcnt lgkmcnt(4)\n\t"
    "v_mfma_f32_16x16x32_bf16 v[80:83], v[248:251], v[120:123], v[64:67]\n\t"
    "ds_read_b128 v[112:115], %[ea] offset:4096\n\t"
    "s_min_i32 %[lf], %[lf], %[cp]\n\t"
    "s_add_u32 %[b], %[b], 1\n\t"
    "v_mfma_f32_16x16x32_bf16 v[84:87], v[252:255], v[120:123], v[68:71]\n\t"
    "ds_read_b128 v[116:119], %[ea] offset:6144\n\t"
    "v_mfma_f32_16x16x32_bf16 v[88:91], v[248:251], v[124:127], v[72:75]\n\t"
    "v_mfma_f32_16x16x32_bf16 v[92:95], v[252:255], v[124:127], v[76:79]\n\t"
    "s_mov_b64 s[36:37], s[38:39]\n\t"
    "s_add_u32 s38, s38, %[ab]\n\t"
    "s_addc_u32 s39, s39, 0\n\t"
    "s_sub_u32 m0, %[lf], %[b]\n\t"
    "s_sub_u32 m0, m0, 1\n\t"
    "s_cmp_lt_u32 m0, %[up]\n\t"
    "s_cbranch_scc1 1b\n\t"
    "s_waitcnt lgkmcnt(0)\n\t"
    "s_nop 4\n\t"
    "s_branch 9f\n\t"
    "5:\n\t"
    "s_add_u32 %[st], %[b], %[po]\n\t"
    "s_max_i32 %[st], %[st], %[p0]\n\t"
    "v_mov_b32 v39, %[st]\n\t"
    "v_mov_b32 v38, 0\n\t"
    "s_mov_b64 exec, 1\n\t"
    "global_store_dword v38, v39, %[pg] sc1\n\t"
    "s_mov_b64 exec, -1\n\t"
    "s_mov_b32 %[p0], %[p1]\n\t"
    "s_branch 6b\n\t"
    "7:\n\t"
    "s_cmp_ge_i32 %[lf], %[cp]\n\t"
    "s_cbranch_scc1 8b\n\t"
    "s_mov_b32 m0, %[sl]\n\t"
    "s_nop 0\n\t"
    "global_load_lds_dword %[dv], %[db] sc1\n\t"
    "s_branch 8b\n\t"
    "9:"
    : "=&{v[80:95]}"(pd), [ea] "+&v"(ea), [da] "+&v"(da), [b] "+&s"(b), [lf] "+&s"(lf), "+{s[36:37]}"(p1), "+{s[38:39]}"(p0), [sa] "+&v"(sa), [st] "=&s"(st), [sl] "+&s"(sl), [p0] "+&s"(pf)
    : [rw] "v"(rw), [ab] "s"(ab), [cp] "s"(cp), [mu] "s"(mu), [ad] "v"(ad), [up] "s"(up), "{v[128:159]}"(W[0]), "{v[160:191]}"(W[1]), "{v[192:223]}"(W[2]), "{v[224:255]}"(W[3]), [co0] "v"(co0), [w0] "s"(w0), [dv] "v"(dv), [db] "s"(db), [pg] "s"(pg), [po] "s"(po - 1), [p1] "s"(b + po), [co1] "v"(co1)
    : "v38", "v39", "v40", "v41", "v42", "v43", "v44", "v45", "v46", "v47", "v48", "v49", "v50", "v51", "v52", "v53", "v54", "v55", "v56", "v57", "v58", "v59", "v60", "v61", "v62", "v63", "v64", "v65", "v66", "v67", "v68", "v69", "v70", "v71", "v72", "v73", "v74", "v75", "v76", "v77", "v78", "v79", "v96", "v97", "v98", "v99", "v100", "v101", "v102", "v103", "v104", "v105", "v106", "v107", "v108", "v109", "v110", "v111", "v112", "v113", "v114", "v115", "v116", "v117", "v118", "v119", "v120", "v121", "v122", "v123", "v124", "v125", "v126", "v127", "vcc", "scc", "m0", "memory");
}
__device__ __forceinline__ void lean_ld0(const u32x32 (&W)[4], unsigned ea, unsigned da, unsigned x, unsigned lb, const char* s0, const char* s1, const unsigned (&av)[8], unsigned ps, unsigned dv, const char* db, unsigned rw, unsigned fa, unsigned fv,
    unsigned ga, unsigned mu, unsigned ad, int th, int tb, unsigned& sv, unsigned& su, unsigned& got) {
  asm volatile("s_setprio 2\n\t"
    "v_mov_b32 v76, %[ga]\n\t"
    "ds_read_b32 v76, v76 offset:24\n\t"
    "ds_read_b128 v[96:99], %[ea] offset:0\n\t"
    "ds_read_b128 v[100:103], %[ea] offset:2048\n\t"
    "ds_read_b128 v[104:107], %[da] offset:0\n\t"
    "ds_read_b128 v[108:111], %[da] offset:2048\n\t"
    "ds_read_b128 v[112:115], %[ea] offset:4096\n\t"
    "ds_read_b128 v[116:119], %[ea] offset:6144\n\t"
    "s_waitcnt lgkmcnt(6)\n\t"
    "v_cmp_ne_u32 vcc, 0, v76\n\t"
    "s_mov_b32 %[got], -1\n\t"
    "s_cbranch_vccnz 12f\n\t"
    "s_waitcnt lgkmcnt(4)\n\t"
    "v_mfma_f32_16x16x32_bf16 v[80:83], v[128:131], v[96:99], 0\n\t"
    "ds_read_b128 v[120:123], %[da] offset:4096\n\t"
    "v_mfma_f32_16x16x32_bf16 v[84:87], v[132:135], v[96:99], 0\n\t"
    "ds_read_b128 v[124:127], %[da] offset:6144\n\t"
    "v_mfma_f32_16x16x32_bf16 v[88:91], v[128:131], v[100:103], 0\n\t"
    "v_mfma_f32_16x16x32_bf16 v[92:95], v[132:135], v[100:103], 0\n\t"
    "s_waitcnt vmcnt(24)\n\t"
    "s_cmp_lt_u32 %[x], 2\n\t"
    "s_cbranch_scc0 41f\n\t"
    "s_waitcnt vmcnt(8)\n\t"
    "41:\n\t"
    "s_barrier\n\t"
    "s_waitcnt lgkmcnt(4)\n\t"
    "v_mfma_f32_16x16x32_bf16 v[80:83], v[136:139], v[104:107], v[80:83]\n\t"
    "ds_read_b128 v[96:99], %[ea] offset:8192\n\t"
    "v_mfma_f32_16x16x32_bf16 v[84:87], v[140:143], v[104:107], v[84:87]\n\t"
    "ds_read_b128 v[100:103], %[ea] offset:10240\n\t"
    "v_mfma_f32_16x16x32_bf16 v[88:91], v[136:139], v[108:111], v[88:91]\n\t"
    "v_mfma_f32_16x16x32_bf16 v[92:95], v[140:143], v[108:111], v[92:95]\n\t"
    "s_waitcnt lgkmcnt(4)\n\t"
    "v_mfma_f32_16x16x32_bf16 v[80:83], v[144:147], v[112:115], v[80:83]\n\t"
    "ds_read_b128 v[104:107], %[da] offset:8192\n\t"
    "v_mfma_f32_16x16x32_bf16 v[84:87], v[148:151], v[112:115], v[84:87]\n\t"
    "ds_read_b128 v[108:111], %[da] offset:10240\n\t"
    "v_mfma_f32_16x16x32_bf16 v[88:91], v[144:147], v[116:119], v[88:91]\n\t"
    "v_mfma_f32_16x16x32_bf16 v[92:95], v[148:151], v[116:119], v[92:95]\n\t"
    "s_waitcnt lgkmcnt(4)\n\t"
    "v_mfma_f32_16x16x32_bf16 v[80:83], v[152:155], v[120:123], v[80:83]\n\t"
    "ds_read_b128 v[112:115], %[ea] offset:12288\n\t"
    "v_mfma_f32_16x16x32_bf16 v[84:87], v[156:159], v[120:123], v[84:87]\n\t"
    "ds_read_b128 v[116:119], %[ea] offset:14336\n\t"
    "v_mfma_f32_16x16x32_bf16 v[88:91], v[152:155], v[124:127], v[88:91]\n\t"
    "v_mfma_f32_16x16x32_bf16 v[92:95], v[156:159], v[124:127], v[92:95]\n\t"
    "s_waitcnt lgkmcnt(4)\n\t"
    "v_mfma_f32_16x16x32_bf16 v[80:83], v[160:163], v[96:99], v[80:83]\n\t"
    "ds_read_b128 v[120:123], %[da] offset:12288\n\t"
    "v_mfma_f32_16x16x32_bf16 v[84:87], v[164:167], v[96:99], v[84:87]\n\t"
    "ds_read_b128 v[124:127], %[da] offset:14336\n\t"
    "v_mfma_f32_16x16x32_bf16 v[88:91], v[160:163], v[100:103], v[88:91]\n\t"
    "v_mfma_f32_16x16x32_bf16 v[92:95], v[164:167], v[100:103], v[92:95]\n\t"
    "s_waitcnt vmcnt(20)\n\t"
    "s_cmp_lt_u32 %[x], 2\n\t"
    "s_cbranch_scc0 45f\n\t"
    "s_waitcnt vmcnt(4)\n\t"
    "45:\n\t"
    "s_barrier\n\t"
    "s_waitcnt lgkmcnt(4)\n\t"
    "v_mfma_f32_16x16x32_bf16 v[80:83], v[168:171], v[104:107], v[80:83]\n\t"
    "ds_read_b128 v[96:99], %[ea] offset:16384\n\t"
    "v_mfma_f32_16x16x32_bf16 v[84:87], v[172:175], v[104:107], v[84:87]\n\t"
    "ds_read_b128 v[100:103], %[ea] offset:18432\n\t"
    "v_mfma_f32_16x16x32_bf16 v[88:91], v[168:171], v[108:111], v[88:91]\n\t"
    "v_mfma_f32_16x16x32_bf16 v[92:95], v[172:175], v[108:111], v[92:95]\n\t"
    "s_waitcnt lgkmcnt(4)\n\t"
    "v_mfma_f32_16x16x32_bf16 v[80:83], v[176:179], v[112:115], v[80:83]\n\t"
    "ds_read_b128 v[104:107], %[da] offset:16384\n\t"
    "v_mfma_f32_16x16x32_bf16 v[84:87], v[180:183], v[112:115], v[84:87]\n\t"
    "ds_read_b128 v[108:111], %[da] offset:18432\n\t"
    "v_mfma_f32_16x16x32_bf16 v[88:91], v[176:179], v[116:119], v[88:91]\n\t"
    "v_mfma_f32_16x16x32_bf16 v[92:95], v[180:183], v[116:119], v[92:95]\n\t"
    "s_waitcnt lgkmcnt(4)\n\t"
    "v_mfma_f32_16x16x32_bf16 v[80:83], v[184:187], v[120:123], v[80:83]\n\t"
    "ds_read_b128 v[112:115], %[ea] offset:20480\n\t"
    "v_mfma_f32_16x16x32_bf16 v[84:87], v[188:191], v[120:123], v[84:87]\n\t"
    "ds_read_b128 v[116:119], %[ea] offset:22528\n\t"
    "v_mfma_f32_16x16x32_bf16 v[88:91], v[184:187], v[124:127], v[88:91]\n\t"
    "v_mfma_f32_16x16x32_bf16 v[92:95], v[188:191], v[124:127], v[92:95]\n\t"
    "s_waitcnt lgkmcnt(4)\n\t"
    "v_mfma_f32_16x16x32_bf16 v[80:83], v[192:195], v[96:99], v[80:83]\n\t"
    "ds_read_b128 v[120:123], %[da] offset:20480\n\t"
    "v_mfma_f32_16x16x32_bf16 v[84:87], v[196:199], v[96:99], v[84:87]\n\t"
    "ds_read_b128 v[124:127], %[da] offset:22528\n\t"
    "v_mfma_f32_16x16x32_bf16 v[88:91], v[192:195], v[100:103], v[88:91]\n\t"
    "v_mfma_f32_16x16x32_bf16 v[92:95], v[196:199], v[100:103], v[92:95]\n\t"
    "s_waitcnt vmcnt(16)\n\t"
    "s_cmp_lt_u32 %[x], 2\n\t"
    "s_cbranch_scc0 49f\n\t"
    "s_waitcnt vmcnt(0)\n\t"
    "49:\n\t"
    "s_barrier\n\t"
    "s_waitcnt lgkmcnt(4)\n\t"
    "v_mfma_f32_16x16x32_bf16 v[80:83], v[200:203], v[104:107], v[80:83]\n\t"
    "ds_read_b128 v[96:99], %[ea] offset:24576\n\t"
    "v_mfma_f32_16x16x32_bf16 v[84:87], v[204:207], v[104:107], v[84:87]\n\t"
    "ds_read_b128 v[100:103], %[ea] offset:26624\n\t"
    "v_mfma_f32_16x16x32_bf16 v[88:91], v[200:203], v[108:111], v[88:91]\n\t"
    "v_mfma_f32_16x16x32_bf16 v[92:95], v[204:207], v[108:111], v[92:95]\n\t"
    "s_waitcnt lgkmcnt(4)\n\t"
    "v_mfma_f32_16x16x32_bf16 v[80:83], v[208:211], v[112:115], v[80:83]\n\t"
    "ds_read_b128 v[104:107], %[da] offset:24576\n\t"
    "v_mfma_f32_16x16x32_bf16 v[84:87], v[212:215], v[112:115], v[84:87]\n\t"
    "ds_read_b128 v[108:111], %[da] offset:26624\n\t"
    "v_mfma_f32_16x16x32_bf16 v[88:91], v[208:211], v[116:119], v[88:91]\n\t"
    "v_mfma_f32_16x16x32_bf16 v[92:95], v[212:215], v[116:119], v[92:95]\n\t"
    "s_cmp_eq_u32 %[ps], 0\n\t"
    "s_cbranch_scc1 10f\n\t"
    "global_load_dword %[sv], %[dv], %[db] sc1\n\t"
    "global_load_dword %[su], %[dv], %[db] offset:64 sc1\n\t"
    "10:\n\t"
    "s_waitcnt lgkmcnt(4)\n\t"
    "v_mfma_f32_16x16x32_bf16 v[80:83], v[216:219], v[120:123], v[80:83]\n\t"
    "ds_read_b128 v[112:115], %[ea] offset:28672\n\t"
    "v_mfma_f32_16x16x32_bf16 v[84:87], v[220:223], v[120:123], v[84:87]\n\t"
    "ds_read_b128 v[116:119], %[ea] offset:30720\n\t"
    "v_mfma_f32_16x16x32_bf16 v[88:91], v[216:219], v[124:127], v[88:91]\n\t"
    "v_mfma_f32_16x16x32_bf16 v[92:95], v[220:223], v[124:127], v[92:95]\n\t"
    "s_waitcnt lgkmcnt(4)\n\t"
    "v_mfma_f32_16x16x32_bf16 v[80:83], v[224:227], v[96:99], v[80:83]\n\t"
    "ds_read_b128 v[120:123], %[da] offset:28672\n\t"
    "v_mfma_f32_16x16x32_bf16 v[84:87], v[228:231], v[96:99], v[84:87]\n\t"
    "ds_read_b128 v[124:127], %[da] offset:30720\n\t"
    "v_mfma_f32_16x16x32_bf16 v[88:91], v[224:227], v[100:103], v[88:91]\n\t"
    "v_mfma_f32_16x16x32_bf16 v[92:95], v[228:231], v[100:103], v[92:95]\n\t"
    "s_waitcnt lgkmcnt(4)\n\t"
    "v_mfma_f32_16x16x32_bf16 v[80:83], v[232:235], v[104:107], v[80:83]\n\t"
    "v_mfma_f32_16x16x32_bf16 v[84:87], v[236:239], v[104:107], v[84:87]\n\t"
    "v_mfma_f32_16x16x32_bf16 v[88:91], v[232:235], v[108:111], v[88:91]\n\t"
    "v_mfma_f32_16x16x32_bf16 v[92:95], v[236:239], v[108:111], v[92:95]\n\t"
    "s_waitcnt lgkmcnt(2)\n\t"
    "v_mfma_f32_16x16x32_bf16 v[80:83], v[240:243], v[112:115], v[80:83]\n\t"
    "v_mfma_f32_16x16x32_bf16 v[84:87], v[244:247], v[112:115], v[84:87]\n\t"
    "v_mfma_f32_16x16x32_bf16 v[88:91], v[240:243], v[116:119], v[88:91]\n\t"
    "v_mfma_f32_16x16x32_bf16 v[92:95], v[244:247], v[116:119], v[92:95]\n\t"
    "s_waitcnt lgkmcnt(0)\n\t"
    "v_mfma_f32_16x16x32_bf16 v[80:83], v[248:251], v[120:123], v[80:83]\n\t"
    "s_memrealtime s[36:37]\n\t"
    "v_mfma_f32_16x16x32_bf16 v[84:87], v[252:255], v[120:123], v[84:87]\n\t"
    "v_mfma_f32_16x16x32_bf16 v[88:91], v[248:251], v[124:127], v[88:91]\n\t"
    "v_mfma_f32_16x16x32_bf16 v[92:95], v[252:255], v[124:127], v[92:95]\n\t"
    "s_mov_b32 %[got], 0\n\t"
    "v_mov_b32 v76, %[fa]\n\t"
    "v_mov_b32 v77, %[fv]\n\t"
    "v_add_u32 v78, 1, v77\n\t"
    "v_mov_b32 v79, %[ga]\n\t"
    "s_waitcnt lgkmcnt(0)\n\t"
    "ds_write_b128 %[rw], v[80:83] offset:0\n\t"
    "ds_write_b128 %[rw], v[84:87] offset:1024\n\t"
    "ds_write_b128 %[rw], v[88:91] offset:2048\n\t"
    "ds_write_b128 %[rw], v[92:95] offset:3072\n\t"
    "ds_write_b32 v76, v77\n\t"
    "s_cmp_eq_u32 %[ps], 0\n\t"
    "s_cbranch_scc1 9f\n\t"
    "s_waitcnt vmcnt(0)\n\t"
    "v_mad_u32_u24 v76, %[sv], %[mu], %[ad]\n\t"
    "v_cmp_ge_i32 vcc, %[th], v76\n\t"
    "s_cbranch_vccz 11f\n\t"
    "s_sub_u32 s36, s36, 20\n\t"
    "v_sub_u32 v76, s36, %[su]\n\t"
    "v_lshrrev_b32 v77, 24, %[su]\n\t"
    "v_lshlrev_b32 v76, 8, v76\n\t"
    "v_ashrrev_i32 v76, 31, v76\n\t"
    "v_add_u32 v77, v77, v76\n\t"
    "v_cmp_lt_i32 s[36:37], %[tb], v77\n\t"
    "s_andn2_b64 vcc, vcc, s[36:37]\n\t"
    "s_cbranch_scc1 9f\n\t"
    "11:\n\t"
    "ds_write2_b32 v79, v78, v78 offset1:1\n\t"
    "s_add_u32 m0, %[lb], 0\n\t"
    "s_nop 0\n\t"
    "global_load_lds_dwordx4 %[av0], %[s0] sc1\n\t"
    "s_add_u32 m0, %[lb], 32768\n\t"
    "s_nop 0\n\t"
    "global_load_lds_dwordx4 %[av1], %[s0] sc1\n\t"
    "s_add_u32 m0, %[lb], 4096\n\t"
    "s_nop 0\n\t"
    "global_load_lds_dwordx4 %[av2], %[s0] sc1\n\t"
    "s_add_u32 m0, %[lb], 36864\n\t"
    "s_nop 0\n\t"
    "global_load_lds_dwordx4 %[av3], %[s0] sc1\n\t"
    "s_add_u32 m0, %[lb], 8192\n\t"
    "s_nop 0\n\t"
    "global_load_lds_dwordx4 %[av4], %[s0] sc1\n\t"
    "s_add_u32 m0, %[lb], 40960\n\t"
    "s_nop 0\n\t"
    "global_load_lds_dwordx4 %[av5], %[s0] sc1\n\t"
    "s_add_u32 m0, %[lb], 12288\n\t"
    "s_nop 0\n\t"
    "global_load_lds_dwordx4 %[av6], %[s0] sc1\n\t"
    "s_add_u32 m0, %[lb], 45056\n\t"
    "s_nop 0\n\t"
    "global_load_lds_dwordx4 %[av7], %[s0] sc1\n\t"
    "s_add_u32 m0, %[lb], 16384\n\t"
    "s_nop 0\n\t"
    "global_load_lds_dwordx4 %[av0], %[s1] sc1\n\t"
    "s_add_u32 m0, %[lb], 49152\n\t"
    "s_nop 0\n\t"
    "global_load_lds_dwordx4 %[av1], %[s1] sc1\n\t"
    "s_add_u32 m0, %[lb], 20480\n\t"
    "s_nop 0\n\t"
    "global_load_lds_dwordx4 %[av2], %[s1] sc1\n\t"
    "s_add_u32 m0, %[lb], 53248\n\t"
    "s_nop 0\n\t"
    "global_load_lds_dwordx4 %[av3], %[s1] sc1\n\t"
    "s_add_u32 m0, %[lb], 24576\n\t"
    "s_nop 0\n\t"
    "global_load_lds_dwordx4 %[av4], %[s1] sc1\n\t"
    "s_add_u32 m0, %[lb], 57344\n\t"
    "s_nop 0\n\t"
    "global_load_lds_dwordx4 %[av5], %[s1] sc1\n\t"
    "s_add_u32 m0, %[lb], 28672\n\t"
    "s_nop 0\n\t"
    "global_load_lds_dwordx4 %[av6], %[s1] sc1\n\t"
    "s_add_u32 m0, %[lb], 61440\n\t"
    "s_nop 0\n\t"
    "global_load_lds_dwordx4 %[av7], %[s1] sc1\n\t"
    "s_mov_b32 %[got], 1\n\t"
    "s_branch 9f\n\t"
    "12:\n\t"
    "s_waitcnt lgkmcnt(0)\n\t"
    "9:"
    : [sv] "+&v"(sv), [su] "+&v"(su), [got] "=&s"(got)
    : [ea] "v"(ea), [da] "v"(da), [x] "s"(x), "{v[128:159]}"(W[0]), "{v[160:191]}"(W[1]), "{v[192:223]}"(W[2]), "{v[224:255]}"(W[3]), [lb] "s"(lb), [s0] "s"(s0), [s1] "s"(s1), [av0] "v"(av[0]), [av1] "v"(av[1]), [av2] "v"(av[2]), [av3] "v"(av[3]), [av4] "v"(av[4]), [av5] "v"(av[5]), [av6] "v"(av[6]), [av7] "v"(av[7]), [ps] "s"(ps), [dv] "v"(dv), [db] "s"(db), [rw] "v"(rw), [fa] "s"(fa), [fv] "s"(fv), [ga] "s"(ga), [mu] "s"(mu), [ad] "v"(ad), [th] "s"(th), [tb] "s"(tb)
    : "v96", "v97", "v98", "v99", "v100", "v101", "v102", "v103", "v104", "v105", "v106", "v107", "v108", "v109", "v110", "v111", "v112", "v113", "v114", "v115", "v116", "v117", "v118", "v119", "v120", "v121", "v122", "v123", "v124", "v125", "v126", "v127", "v76", "v77", "v78", "v79", "v80", "v81", "v82", "v83", "v84", "v85", "v86", "v87", "v88", "v89", "v90", "v91", "v92", "v93", "v94", "v95", "vcc", "scc", "m0", "memory", "s36", "s37", "s38", "s39");
}
__device__ __forceinline__ void lean_dm0(const u32x32 (&W)[4], unsigned ea, unsigned da, unsigned x, unsigned lb, const char* s0, const char* s1, const unsigned (&av)[8], unsigned ps, unsigned dv, const char* db, unsigned rw, unsigned fa, unsigned fv,
    unsigned ga, unsigned mu, unsigned ad, int th, int tb, unsigned& sv, unsigned& su, unsigned& got) {
  asm volatile("s_setprio 2\n\t"
    "v_mov_b32 v76, %[ga]\n\t"
    "ds_read_b32 v76, v76 offset:24\n\t"
    "ds_read_b128 v[96:99], %[ea] offset:0\n\t"
    "ds_read_b128 v[100:103], %[ea] offset:2048\n\t"
    "ds_read_b128 v[104:107], %[da] offset:0\n\t"
    "ds_read_b128 v[108:111], %[da] offset:2048\n\t"
    "ds_read_b128 v[112:115], %[ea] offset:4096\n\t"
    "ds_read_b128 v[116:119], %[ea] offset:6144\n\t"
    "s_waitcnt lgkmcnt(6)\n\t"
    "v_cmp_ne_u32 vcc, 0, v76\n\t"
    "s_mov_b32 %[got], -1\n\t"
    "s_cbranch_vccnz 12f\n\t"
    "s_waitcnt lgkmcnt(4)\n\t"
    "v_mfma_f32_16x16x32_bf16 v[80:83], v[128:131], v[96:99], 0\n\t"
    "ds_read_b128 v[120:123], %[da] offset:4096\n\t"
    "s_add_u32 m0, %[lb], 0\n\t"
    "v_mfma_f32_16x16x32_bf16 v[84:87], v[132:135], v[96:99], 0\n\t"
    "ds_read_b128 v[124:127], %[da] offset:6144\n\t"
    "global_load_lds_dwordx4 %[av0], %[s0] sc1\n\t"
    "v_mfma_f32_16x16x32_bf16 v[88:91], v[128:131], v[100:103], 0\n\t"
    "s_add_u32 m0, %[lb], 32768\n\t"
    "v_mfma_f32_16x16x32_bf16 v[92:95], v[132:135], v[100:103], 0\n\t"
    "global_load_lds_dwordx4 %[av1], %[s0] sc1\n\t"
    "s_waitcnt vmcnt(10)\n\t"
    "s_barrier\n\t"
    "s_waitcnt lgkmcnt(4)\n\t"
    "v_mfma_f32_16x16x32_bf16 v[80:83], v[136:139], v[104:107], v[80:83]\n\t"
    "ds_read_b128 v[96:99], %[ea] offset:8192\n\t"
    "s_add_u32 m0, %[lb], 4096\n\t"
    "v_mfma_f32_16x16x32_bf16 v[84:87], v[140:143], v[104:107], v[84:87]\n\t"
    "ds_read_b128 v[100:103], %[ea] offset:10240\n\t"
    "global_load_lds_dwordx4 %[av2], %[s0] sc1\n\t"
    "v_mfma_f32_16x16x32_bf16 v[88:91], v[136:139], v[108:111], v[88:91]\n\t"
    "s_add_u32 m0, %[lb], 36864\n\t"
    "v_mfma_f32_16x16x32_bf16 v[92:95], v[140:143], v[108:111], v[92:95]\n\t"
    "global_load_lds_dwordx4 %[av3], %[s0] sc1\n\t"
    "s_waitcnt lgkmcnt(4)\n\t"
    "v_mfma_f32_16x16x32_bf16 v[80:83], v[144:147], v[112:115], v[80:83]\n\t"
    "ds_read_b128 v[104:107], %[da] offset:8192\n\t"
    "s_add_u32 m0, %[lb], 8192\n\t"
    "v_mfma_f32_16x16x32_bf16 v[84:87], v[148:151], v[112:115], v[84:87]\n\t"
    "ds_read_b128 v[108:111], %[da] offset:10240\n\t"
    "global_load_lds_dwordx4 %[av4], %[s0] sc1\n\t"
    "v_mfma_f32_16x16x32_bf16 v[88:91], v[144:147], v[116:119], v[88:91]\n\t"
    "s_add_u32 m0, %[lb], 40960\n\t"
    "v_mfma_f32_16x16x32_bf16 v[92:95], v[148:151], v[116:119], v[92:95]\n\t"
    "global_load_lds_dwordx4 %[av5], %[s0] sc1\n\t"
    "s_waitcnt lgkmcnt(4)\n\t"
    "v_mfma_f32_16x16x32_bf16 v[80:83], v[152:155], v[120:123], v[80:83]\n\t"
    "ds_read_b128 v[112:115], %[ea] offset:12288\n\t"
    "s_add_u32 m0, %[lb], 12288\n\t"
    "v_mfma_f32_16x16x32_bf16 v[84:87], v[156:159], v[120:123], v[84:87]\n\t"
    "ds_read_b128 v[116:119], %[ea] offset:14336\n\t"
    "global_load_lds_dwordx4 %[av6], %[s0] sc1\n\t"
    "v_mfma_f32_16x16x32_bf16 v[88:91], v[152:155], v[124:127], v[88:91]\n\t"
    "s_add_u32 m0, %[lb], 45056\n\t"
    "v_mfma_f32_16x16x32_bf16 v[92:95], v[156:159], v[124:127], v[92:95]\n\t"
    "global_load_lds_dwordx4 %[av7], %[s0] sc1\n\t"
    "s_waitcnt lgkmcnt(4)\n\t"
    "v_mfma_f32_16x16x32_bf16 v[80:83], v[160:163], v[96:99], v[80:83]\n\t"
    "ds_read_b128 v[120:123], %[da] offset:12288\n\t"
    "s_add_u32 m0, %[lb], 16384\n\t"
    "v_mfma_f32_16x16x32_bf16 v[84:87], v[164:167], v[96:99], v[84:87]\n\t"
    "ds_read_b128 v[124:127], %[da] offset:14336\n\t"
    "global_load_lds_dwordx4 %[av0], %[s1] sc1\n\t"
    "v_mfma_f32_16x16x32_bf16 v[88:91], v[160:163], v[100:103], v[88:91]\n\t"
    "s_add_u32 m0, %[lb], 49152\n\t"
    "v_mfma_f32_16x16x32_bf16 v[92:95], v[164:167], v[100:103], v[92:95]\n\t"
    "global_load_lds_dwordx4 %[av1], %[s1] sc1\n\t"
    "s_waitcnt vmcnt(14)\n\t"
    "s_barrier\n\t"
    "s_waitcnt lgkmcnt(4)\n\t"
    "v_mfma_f32_16x16x32_bf16 v[80:83], v[168:171], v[104:107], v[80:83]\n\t"
    "ds_read_b128 v[96:99], %[ea] offset:16384\n\t"
    "s_add_u32 m0, %[lb], 20480\n\t"
    "v_mfma_f32_16x16x32_bf16 v[84:87], v[172:175], v[104:107], v[84:87]\n\t"
    "ds_read_b128 v[100:103], %[ea] offset:18432\n\t"
    "global_load_lds_dwordx4 %[av2], %[s1] sc1\n\t"
    "v_mfma_f32_16x16x32_bf16 v[88:91], v[168:171], v[108:111], v[88:91]\n\t"
    "s_add_u32 m0, %[lb], 53248\n\t"
    "v_mfma_f32_16x16x32_bf16 v[92:95], v[172:175], v[108:111], v[92:95]\n\t"
    "global_load_lds_dwordx4 %[av3], %[s1] sc1\n\t"
    "s_waitcnt lgkmcnt(4)\n\t"
    "v_mfma_f32_16x16x32_bf16 v[80:83], v[176:179], v[112:115], v[80:83]\n\t"
    "ds_read_b128 v[104:107], %[da] offset:16384\n\t"
    "s_add_u32 m0, %[lb], 24576\n\t"
    "v_mfma_f32_16x16x32_bf16 v[84:87], v[180:183], v[112:115], v[84:87]\n\t"
    "ds_read_b128 v[108:111], %[da] offset:18432\n\t"
    "global_load_lds_dwordx4 %[av4], %[s1] sc1\n\t"
    "v_mfma_f32_16x16x32_bf16 v[88:91], v[176:179], v[116:119], v[88:91]\n\t"
    "s_add_u32 m0, %[lb], 57344\n\t"
    "v_mfma_f32_16x16x32_bf16 v[92:95], v[180:183], v[116:119], v[92:95]\n\t"
    "global_load_lds_dwordx4 %[av5], %[s1] sc1\n\t"
    "s_waitcnt lgkmcnt(4)\n\t"
    "v_mfma_f32_16x16x32_bf16 v[80:83], v[184:187], v[120:123], v[80:83]\n\t"
    "ds_read_b128 v[112:115], %[ea] offset:20480\n\t"
    "s_add_u32 m0, %[lb], 28672\n\t"
    "v_mfma_f32_16x16x32_bf16 v[84:87], v[188:191], v[120:123], v[84:87]\n\t"
    "ds_read_b128 v[116:119], %[ea] offset:22528\n\t"
    "global_load_lds_dwordx4 %[av6], %[s1] sc1\n\t"
    "v_mfma_f32_16x16x32_bf16 v[88:91], v[184:187], v[124:127], v[88:91]\n\t"
    "s_add_u32 m0, %[lb], 61440\n\t"
    "v_mfma_f32_16x16x32_bf16 v[92:95], v[188:191], v[124:127], v[92:95]\n\t"
    "global_load_lds_dwordx4 %[av7], %[s1] sc1\n\t"
    "s_waitcnt lgkmcnt(4)\n\t"
    "v_mfma_f32_16x16x32_bf16 v[80:83], v[192:195], v[96:99], v[80:83]\n\t"
    "ds_read_b128 v[120:123], %[da] offset:20480\n\t"
    "v_mfma_f32_16x16x32_bf16 v[84:87], v[196:199], v[96:99], v[84:87]\n\t"
    "ds_read_b128 v[124:127], %[da] offset:22528\n\t"
    "v_mfma_f32_16x16x32_bf16 v[88:91], v[192:195], v[100:103], v[88:91]\n\t"
    "v_mfma_f32_16x16x32_bf16 v[92:95], v[196:199], v[100:103], v[92:95]\n\t"
    "s_waitcnt vmcnt(16)\n\t"
    "s_barrier\n\t"
    "s_waitcnt lgkmcnt(4)\n\t"
    "v_mfma_f32_16x16x32_bf16 v[80:83], v[200:203], v[104:107], v[80:83]\n\t"
    "ds_read_b128 v[96:99], %[ea] offset:24576\n\t"
    "v_mfma_f32_16x16x32_bf16 v[84:87], v[204:207], v[104:107], v[84:87]\n\t"
    "ds_read_b128 v[100:103], %[ea] offset:26624\n\t"
    "v_mfma_f32_16x16x32_bf16 v[88:91], v[200:203], v[108:111], v[88:91]\n\t"
    "v_mfma_f32_16x16x32_bf16 v[92:95], v[204:207], v[108:111], v[92:95]\n\t"
    "s_waitcnt lgkmcnt(4)\n\t"
    "v_mfma_f32_16x16x32_bf16 v[80:83], v[208:211], v[112:115], v[80:83]\n\t"
    "ds_read_b128 v[104:107], %[da] offset:24576\n\t"
    "v_mfma_f32_16x16x32_bf16 v[84:87], v[212:215], v[112:115], v[84:87]\n\t"
    "ds_read_b128 v[108:111], %[da] offset:26624\n\t"
    "v_mfma_f32_16x16x32_bf16 v[88:91], v[208:211], v[116:119], v[88:91]\n\t"
    "v_mfma_f32_16x16x32_bf16 v[92:95], v[212:215], v[116:119], v[92:95]\n\t"
    "s_waitcnt lgkmcnt(4)\n\t"
    "v_mfma_f32_16x16x32_bf16 v[80:83], v[216:219], v[120:123], v[80:83]\n\t"
    "ds_read_b128 v[112:115], %[ea] offset:28672\n\t"
    "v_mfma_f32_16x16x32_bf16 v[84:87], v[220:223], v[120:123], v[84:87]\n\t"
    "ds_read_b128 v[116:119], %[ea] offset:30720\n\t"
    "v_mfma_f32_16x16x32_bf16 v[88:91], v[216:219], v[124:127], v[88:91]\n\t"
    "v_mfma_f32_16x16x32_bf16 v[92:95], v[220:223], v[124:127], v[92:95]\n\t"
    "s_waitcnt lgkmcnt(4)\n\t"
    "v_mfma_f32_16x16x32_bf16 v[80:83], v[224:227], v[96:99], v[80:83]\n\t"
    "ds_read_b128 v[120:123], %[da] offset:28672\n\t"
    "v_mfma_f32_16x16x32_bf16 v[84:87], v[228:231], v[96:99], v[84:87]\n\t"
    "ds_read_b128 v[124:127], %[da] offset:30720\n\t"
    "v_mfma_f32_16x16x32_bf16 v[88:91], v[224:227], v[100:103], v[88:91]\n\t"
    "v_mfma_f32_16x16x32_bf16 v[92:95], v[228:231], v[100:103], v[92:95]\n\t"
    "s_waitcnt lgkmcnt(4)\n\t"
    "v_mfma_f32_16x16x32_bf16 v[80:83], v[232:235], v[104:107], v[80:83]\n\t"
    "v_mfma_f32_16x16x32_bf16 v[84:87], v[236:239], v[104:107], v[84:87]\n\t"
    "v_mfma_f32_16x16x32_bf16 v[88:91], v[232:235], v[108:111], v[88:91]\n\t"
    "v_mfma_f32_16x16x32_bf16 v[92:95], v[236:239], v[108:111], v[92:95]\n\t"
    "s_waitcnt lgkmcnt(2)\n\t"
    "v_mfma_f32_16x16x32_bf16 v[80:83], v[240:243], v[112:115], v[80:83]\n\t"
    "v_mfma_f32_16x16x32_bf16 v[84:87], v[244:247], v[112:115], v[84:87]\n\t"
    "v_mfma_f32_16x16x32_bf16 v[88:91], v[240:243], v[116:119], v[88:91]\n\t"
    "v_mfma_f32_16x16x32_bf16 v[92:95], v[244:247], v[116:119], v[92:95]\n\t"
    "s_waitcnt lgkmcnt(0)\n\t"
    "v_mfma_f32_16x16x32_bf16 v[80:83], v[248:251], v[120:123], v[80:83]\n\t"
    "v_mfma_f32_16x16x32_bf16 v[84:87], v[252:255], v[120:123], v[84:87]\n\t"
    "v_mfma_f32_16x16x32_bf16 v[88:91], v[248:251], v[124:127], v[88:91]\n\t"
    "v_mfma_f32_16x16x32_bf16 v[92:95], v[252:255], v[124:127], v[92:95]\n\t"
    "s_cmp_eq_u32 %[ps], 0\n\t"
    "s_cbranch_scc1 10f\n\t"
    "global_load_dword %[sv], %[dv], %[db] sc1\n\t"
    "global_load_dword %[su], %[dv], %[db] offset:64 sc1\n\t"
    "10:\n\t"
    "s_mov_b32 %[got], 0\n\t"
    "v_mov_b32 v76, %[fa]\n\t"
    "v_mov_b32 v77, %[fv]\n\t"
    "v_add_u32 v78, 1, v77\n\t"
    "v_mov_b32 v79, %[ga]\n\t"
    "ds_write_b128 %[rw], v[80:83] offset:0\n\t"
    "ds_write_b128 %[rw], v[84:87] offset:1024\n\t"
    "ds_write_b128 %[rw], v[88:91] offset:2048\n\t"
    "ds_write_b128 %[rw], v[92:95] offset:3072\n\t"
    "ds_write_b32 v76, v77\n\t"
    "s_cmp_eq_u32 %[ps], 0\n\t"
    "s_cbranch_scc1 9f\n\t"
    "s_waitcnt vmcnt(0)\n\t"
    "s_branch 9f\n\t"
    "12:\n\t"
    "s_waitcnt lgkmcnt(0)\n\t"
    "9:"
    : [sv] "+&v"(sv), [su] "+&v"(su), [got] "=&s"(got)
    : [ea] "v"(ea), [da] "v"(da), [x] "s"(x), "{v[128:159]}"(W[0]), "{v[160:191]}"(W[1]), "{v[192:223]}"(W[2]), "{v[224:255]}"(W[3]), [lb] "s"(lb), [s0] "s"(s0), [s1] "s"(s1), [av0] "v"(av[0]), [av1] "v"(av[1]), [av2] "v"(av[2]), [av3] "v"(av[3]), [av4] "v"(av[4]), [av5] "v"(av[5]), [av6] "v"(av[6]), [av7] "v"(av[7]), [ps] "s"(ps), [dv] "v"(dv), [db] "s"(db), [rw] "v"(rw), [fa] "s"(fa), [fv] "s"(fv), [ga] "s"(ga), [mu] "s"(mu), [ad] "v"(ad), [th] "s"(th), [tb] "s"(tb)
    : "v96", "v97", "v98", "v99", "v100", "v101", "v102", "v103", "v104", "v105", "v106", "v107", "v108", "v109", "v110", "v111", "v112", "v113", "v114", "v115", "v116", "v117", "v118", "v119", "v120", "v121", "v122", "v123", "v124", "v125", "v126", "v127", "v76", "v77", "v78", "v79", "v80", "v81", "v82", "v83", "v84", "v85", "v86", "v87", "v88", "v89", "v90", "v91", "v92", "v93", "v94", "v95", "vcc", "scc", "m0", "memory");
}
__device__ __forceinline__ void lean_ld1(const u32x32 (&W)[4], unsigned ea, unsigned da, unsigned x, unsigned lb, const char* s0, const char* s1, const unsigned (&av)[8], unsigned ps, unsigned dv, const char* db, unsigned rw, unsigned fa, unsigned fv,
    unsigned ga, unsigned mu, unsigned ad, int th, int tb, unsigned& sv, unsigned& su, unsigned& got) {
  asm volatile("s_setprio 2\n\t"
    "v_mov_b32 v76, %[ga]\n\t"
    "ds_read_b32 v76, v76 offset:24\n\t"
    "ds_read_b128 v[96:99], %[ea] offset:0\n\t"
    "ds_read_b128 v[100:103], %[ea] offset:2048\n\t"
    "ds_read_b128 v[104:107], %[da] offset:0\n\t"
    "ds_read_b128 v[108:111], %[da] offset:2048\n\t"
    "ds_read_b128 v[112:115], %[ea] offset:4096\n\t"
    "ds_read_b128 v[116:119], %[ea] offset:6144\n\t"
    "s_waitcnt lgkmcnt(6)\n\t"
    "v_cmp_ne_u32 vcc, 0, v76\n\t"
    "s_mov_b32 %[got], -1\n\t"
    "s_cbranch_vccnz 12f\n\t"
    "s_waitcnt lgkmcnt(4)\n\t"
    "v_mfma_f32_16x16x32_bf16 v[80:83], v[128:131], v[96:99], 0\n\t"
    "ds_read_b128 v[120:123], %[da] offset:4096\n\t"
    "v_mfma_f32_16x16x32_bf16 v[84:87], v[132:135], v[96:99], 0\n\t"
    "ds_read_b128 v[124:127], %[da] offset:6144\n\t"
    "v_mfma_f32_16x16x32_bf16 v[88:91], v[128:131], v[100:103], 0\n\t"
    "v_mfma_f32_16x16x32_bf16 v[92:95], v[132:135], v[100:103], 0\n\t"
    "s_waitcnt vmcnt(12)\n\t"
    "s_cmp_lt_u32 %[x], 2\n\t"
    "s_cbranch_scc0 41f\n\t"
    "s_waitcnt vmcnt(4)\n\t"
    "41:\n\t"
    "s_barrier\n\t"
    "s_waitcnt lgkmcnt(4)\n\t"
    "v_mfma_f32_16x16x32_bf16 v[80:83], v[136:139], v[104:107], v[80:83]\n\t"
    "ds_read_b128 v[96:99], %[ea] offset:8192\n\t"
    "v_mfma_f32_16x16x32_bf16 v[84:87], v[140:143], v[104:107], v[84:87]\n\t"
    "ds_read_b128 v[100:103], %[ea] offset:10240\n\t"
    "v_mfma_f32_16x16x32_bf16 v[88:91], v[136:139], v[108:111], v[88:91]\n\t"
    "v_mfma_f32_16x16x32_bf16 v[92:95], v[140:143], v[108:111], v[92:95]\n\t"
    "s_waitcnt lgkmcnt(4)\n\t"
    "v_mfma_f32_16x16x32_bf16 v[80:83], v[144:147], v[112:115], v[80:83]\n\t"
    "ds_read_b128 v[104:107], %[da] offset:8192\n\t"
    "v_mfma_f32_16x16x32_bf16 v[84:87], v[148:151], v[112:115], v[84:87]\n\t"
    "ds_read_b128 v[108:111], %[da] offset:10240\n\t"
    "v_mfma_f32_16x16x32_bf16 v[88:91], v[144:147], v[116:119], v[88:91]\n\t"
    "v_mfma_f32_16x16x32_bf16 v[92:95], v[148:151], v[116:119], v[92:95]\n\t"
    "s_waitcnt lgkmcnt(4)\n\t"
    "v_mfma_f32_16x16x32_bf16 v[80:83], v[152:155], v[120:123], v[80:83]\n\t"
    "ds_read_b128 v[112:115], %[ea] offset:12288\n\t"
    "v_mfma_f32_16x16x32_bf16 v[84:87], v[156:159], v[120:123], v[84:87]\n\t"
    "ds_read_b128 v[116:119], %[ea] offset:14336\n\t"
    "v_mfma_f32_16x16x32_bf16 v[88:91], v[152:155], v[124:127], v[88:91]\n\t"
    "v_mfma_f32_16x16x32_bf16 v[92:95], v[156:159], v[124:127], v[92:95]\n\t"
    "s_waitcnt lgkmcnt(4)\n\t"
    "v_mfma_f32_16x16x32_bf16 v[80:83], v[160:163], v[96:99], v[80:83]\n\t"
    "ds_read_b128 v[120:123], %[da] offset:12288\n\t"
    "v_mfma_f32_16x16x32_bf16 v[84:87], v[164:167], v[96:99], v[84:87]\n\t"
    "ds_read_b128 v[124:127], %[da] offset:14336\n\t"
    "v_mfma_f32_16x16x32_bf16 v[88:91], v[160:163], v[100:103], v[88:91]\n\t"
    "v_mfma_f32_16x16x32_bf16 v[92:95], v[164:167], v[100:103], v[92:95]\n\t"
    "s_waitcnt vmcnt(10)\n\t"
    "s_cmp_lt_u32 %[x], 2\n\t"
    "s_cbranch_scc0 45f\n\t"
    "s_waitcnt vmcnt(2)\n\t"
    "45:\n\t"
    "s_barrier\n\t"
    "s_waitcnt lgkmcnt(4)\n\t"
    "v_mfma_f32_16x16x32_bf16 v[80:83], v[168:171], v[104:107], v[80:83]\n\t"
    "ds_read_b128 v[96:99], %[ea] offset:16384\n\t"
    "v_mfma_f32_16x16x32_bf16 v[84:87], v[172:175], v[104:107], v[84:87]\n\t"
    "ds_read_b128 v[100:103], %[ea] offset:18432\n\t"
    "v_mfma_f32_16x16x32_bf16 v[88:91], v[168:171], v[108:111], v[88:91]\n\t"
    "v_mfma_f32_16x16x32_bf16 v[92:95], v[172:175], v[108:111], v[92:95]\n\t"
    "s_waitcnt lgkmcnt(4)\n\t"
    "v_mfma_f32_16x16x32_bf16 v[80:83], v[176:179], v[112:115], v[80:83]\n\t"
    "ds_read_b128 v[104:107], %[da] offset:16384\n\t"
    "v_mfma_f32_16x16x32_bf16 v[84:87], v[180:183], v[112:115], v[84:87]\n\t"
    "ds_read_b128 v[108:111], %[da] offset:18432\n\t"
    "v_mfma_f32_16x16x32_bf16 v[88:91], v[176:179], v[116:119], v[88:91]\n\t"
    "v_mfma_f32_16x16x32_bf16 v[92:95], v[180:183], v[116:119], v[92:95]\n\t"
    "s_waitcnt lgkmcnt(4)\n\t"
    "v_mfma_f32_16x16x32_bf16 v[80:83], v[184:187], v[120:123], v[80:83]\n\t"
    "ds_read_b128 v[112:115], %[ea] offset:20480\n\t"
    "v_mfma_f32_16x16x32_bf16 v[84:87], v[188:191], v[120:123], v[84:87]\n\t"
    "ds_read_b128 v[116:119], %[ea] offset:22528\n\t"
    "v_mfma_f32_16x16x32_bf16 v[88:91], v[184:187], v[124:127], v[88:91]\n\t"
    "v_mfma_f32_16x16x32_bf16 v[92:95], v[188:191], v[124:127], v[92:95]\n\t"
    "s_waitcnt lgkmcnt(4)\n\t"
    "v_mfma_f32_16x16x32_bf16 v[80:83], v[192:195], v[96:99], v[80:83]\n\t"
    "ds_read_b128 v[120:123], %[da] offset:20480\n\t"
    "v_mfma_f32_16x16x32_bf16 v[84:87], v[196:199], v[96:99], v[84:87]\n\t"
    "ds_read_b128 v[124:127], %[da] offset:22528\n\t"
    "v_mfma_f32_16x16x32_bf16 v[88:91], v[192:195], v[100:103], v[88:91]\n\t"
    "v_mfma_f32_16x16x32_bf16 v[92:95], v[196:199], v[100:103], v[92:95]\n\t"
    "s_waitcnt vmcnt(8)\n\t"
    "s_cmp_lt_u32 %[x], 2\n\t"
    "s_cbranch_scc0 49f\n\t"
    "s_waitcnt vmcnt(0)\n\t"
    "49:\n\t"
    "s_barrier\n\t"
    "s_waitcnt lgkmcnt(4)\n\t"
    "v_mfma_f32_16x16x32_bf16 v[80:83], v[200:203], v[104:107], v[80:83]\n\t"
    "ds_read_b128 v[96:99], %[ea] offset:24576\n\t"
    "v_mfma_f32_16x16x32_bf16 v[84:87], v[204:207], v[104:107], v[84:87]\n\t"
    "ds_read_b128 v[100:103], %[ea] offset:26624\n\t"
    "v_mfma_f32_16x16x32_bf16 v[88:91], v[200:203], v[108:111], v[88:91]\n\t"
    "v_mfma_f32_16x16x32_bf16 v[92:95], v[204:207], v[108:111], v[92:95]\n\t"
    "s_waitcnt lgkmcnt(4)\n\t"
    "v_mfma_f32_16x16x32_bf16 v[80:83], v[208:211], v[112:115], v[80:83]\n\t"
    "ds_read_b128 v[104:107], %[da] offset:24576\n\t"
    "v_mfma_f32_16x16x32_bf16 v[84:87], v[212:215], v[112:115], v[84:87]\n\t"
    "ds_read_b128 v[108:111], %[da] offset:26624\n\t"
    "v_mfma_f32_16x16x32_bf16 v[88:91], v[208:211], v[116:119], v[88:91]\n\t"
    "v_mfma_f32_16x16x32_bf16 v[92:95], v[212:215], v[116:119], v[92:95]\n\t"
    "s_cmp_eq_u32 %[ps], 0\n\t"
    "s_cbranch_scc1 10f\n\t"
    "global_load_dword %[sv], %[dv], %[db] sc1\n\t"
    "global_load_dword %[su], %[dv], %[db] offset:64 sc1\n\t"
    "10:\n\t"
    "s_waitcnt lgkmcnt(4)\n\t"
    "v_mfma_f32_16x16x32_bf16 v[80:83], v[216:219], v[120:123], v[80:83]\n\t"
    "ds_read_b128 v[112:115], %[ea] offset:28672\n\t"
    "v_mfma_f32_16x16x32_bf16 v[84:87], v[220:223], v[120:123], v[84:87]\n\t"
    "ds_read_b128 v[116:119], %[ea] offset:30720\n\t"
    "v_mfma_f32_16x16x32_bf16 v[88:91], v[216:219], v[124:127], v[88:91]\n\t"
    "v_mfma_f32_16x16x32_bf16 v[92:95], v[220:223], v[124:127], v[92:95]\n\t"
    "s_waitcnt lgkmcnt(4)\n\t"
    "v_mfma_f32_16x16x32_bf16 v[80:83], v[224:227], v[96:99], v[80:83]\n\t"
    "ds_read_b128 v[120:123], %[da] offset:28672\n\t"
    "v_mfma_f32_16x16x32_bf16 v[84:87], v[228:231], v[96:99], v[84:87]\n\t"
    "ds_read_b128 v[124:127], %[da] offset:30720\n\t"
    "v_mfma_f32_16x16x32_bf16 v[88:91], v[224:227], v[100:103], v[88:91]\n\t"
    "v_mfma_f32_16x16x32_bf16 v[92:95], v[228:231], v[100:103], v[92:95]\n\t"
    "s_waitcnt lgkmcnt(4)\n\t"
    "v_mfma_f32_16x16x32_bf16 v[80:83], v[232:235], v[104:107], v[80:83]\n\t"
    "v_mfma_f32_16x16x32_bf16 v[84:87], v[236:239], v[104:107], v[84:87]\n\t"
    "v_mfma_f32_16x16x32_bf16 v[88:91], v[232:235], v[108:111], v[88:91]\n\t"
    "v_mfma_f32_16x16x32_bf16 v[92:95], v[236:239], v[108:111], v[92:95]\n\t"
    "s_waitcnt lgkmcnt(2)\n\t"
    "v_mfma_f32_16x16x32_bf16 v[80:83], v[240:243], v[112:115], v[80:83]\n\t"
    "v_mfma_f32_16x16x32_bf16 v[84:87], v[244:247], v[112:115], v[84:87]\n\t"
    "v_mfma_f32_16x16x32_bf16 v[88:91], v[240:243], v[116:119], v[88:91]\n\t"
    "v_mfma_f32_16x16x32_bf16 v[92:95], v[244:247], v[116:119], v[92:95]\n\t"
    "s_waitcnt lgkmcnt(0)\n\t"
    "v_mfma_f32_16x16x32_bf16 v[80:83], v[248:251], v[120:123], v[80:83]\n\t"
    "s_memrealtime s[36:37]\n\t"
    "v_mfma_f32_16x16x32_bf16 v[84:87], v[252:255], v[120:123], v[84:87]\n\t"
    "v_mfma_f32_16x16x32_bf16 v[88:91], v[248:251], v[124:127], v[88:91]\n\t"
    "v_mfma_f32_16x16x32_bf16 v[92:95], v[252:255], v[124:127], v[92:95]\n\t"
    "s_mov_b32 %[got], 0\n\t"
    "v_mov_b32 v76, %[fa]\n\t"
    "v_mov_b32 v77, %[fv]\n\t"
    "v_add_u32 v78, 1, v77\n\t"
    "v_mov_b32 v79, %[ga]\n\t"
    "s_waitcnt lgkmcnt(0)\n\t"
    "ds_write_b128 %[rw], v[80:83] offset:0\n\t"
    "ds_write_b128 %[rw], v[84:87] offset:1024\n\t"
    "ds_write_b128 %[rw], v[88:91] offset:2048\n\t"
    "ds_write_b128 %[rw], v[92:95] offset:3072\n\t"
    "ds_write_b32 v76, v77\n\t"
    "s_cmp_eq_u32 %[ps], 0\n\t"
    "s_cbranch_scc1 9f\n\t"
    "s_waitcnt vmcnt(0)\n\t"
    "v_mad_u32_u24 v76, %[sv], %[mu], %[ad]\n\t"
    "v_cmp_ge_i32 vcc, %[th], v76\n\t"
    "s_cbranch_vccz 11f\n\t"
    "s_sub_u32 s36, s36, 20\n\t"
    "v_sub_u32 v76, s36, %[su]\n\t"
    "v_lshrrev_b32 v77, 24, %[su]\n\t"
    "v_lshlrev_b32 v76, 8, v76\n\t"
    "v_ashrrev_i32 v76, 31, v76\n\t"
    "v_add_u32 v77, v77, v76\n\t"
    "v_cmp_lt_i32 s[36:37], %[tb], v77\n\t"
    "s_andn2_b64 vcc, vcc, s[36:37]\n\t"
    "s_cbranch_scc1 9f\n\t"
    "11:\n\t"
    "ds_write2_b32 v79, v78, v78 offset1:1\n\t"
    "s_add_u32 m0, %[lb], 0\n\t"
    "s_nop 0\n\t"
    "global_load_lds_dwordx4 %[av0], %[s0] sc1\n\t"
    "s_add_u32 m0, %[lb], 4096\n\t"
    "s_nop 0\n\t"
    "global_load_lds_dwordx4 %[av2], %[s0] sc1\n\t"
    "s_add_u32 m0, %[lb], 8192\n\t"
    "s_nop 0\n\t"
    "global_load_lds_dwordx4 %[av4], %[s0] sc1\n\t"
    "s_add_u32 m0, %[lb], 12288\n\t"
    "s_nop 0\n\t"
    "global_load_lds_dwordx4 %[av6], %[s0] sc1\n\t"
    "s_add_u32 m0, %[lb], 16384\n\t"
    "s_nop 0\n\t"
    "global_load_lds_dwordx4 %[av0], %[s1] sc1\n\t"
    "s_add_u32 m0, %[lb], 20480\n\t"
    "s_nop 0\n\t"
    "global_load_lds_dwordx4 %[av2], %[s1] sc1\n\t"
    "s_add_u32 m0, %[lb], 24576\n\t"
    "s_nop 0\n\t"
    "global_load_lds_dwordx4 %[av4], %[s1] sc1\n\t"
    "s_add_u32 m0, %[lb], 28672\n\t"
    "s_nop 0\n\t"
    "global_load_lds_dwordx4 %[av6], %[s1] sc1\n\t"
    "s_mov_b32 %[got], 1\n\t"
    "s_branch 9f\n\t"
    "12:\n\t"
    "s_waitcnt lgkmcnt(0)\n\t"
    "9:"
    : [sv] "+&v"(sv), [su] "+&v"(su), [got] "=&s"(got)
    : [ea] "v"(ea), [da] "v"(da), [x] "s"(x), "{v[128:159]}"(W[0]), "{v[160:191]}"(W[1]), "{v[192:223]}"(W[2]), "{v[224:255]}"(W[3]), [lb] "s"(lb), [s0] "s"(s0), [s1] "s"(s1), [av0] "v"(av[0]), [av1] "v"(av[1]), [av2] "v"(av[2]), [av3] "v"(av[3]), [av4] "v"(av[4]), [av5] "v"(av[5]), [av6] "v"(av[6]), [av7] "v"(av[7]), [ps] "s"(ps), [dv] "v"(dv), [db] "s"(db), [rw] "v"(rw), [fa] "s"(fa), [fv] "s"(fv), [ga] "s"(ga), [mu] "s"(mu), [ad] "v"(ad), [th] "s"(th), [tb] "s"(tb)
    : "v96", "v97", "v98", "v99", "v100", "v101", "v102", "v103", "v104", "v105", "v106", "v107", "v108", "v109", "v110", "v111", "v112", "v113", "v114", "v115", "v116", "v117", "v118", "v119", "v120", "v121", "v122", "v123", "v124", "v125", "v126", "v127", "v76", "v77", "v78", "v79", "v80", "v81", "v82", "v83", "v84", "v85", "v86", "v87", "v88", "v89", "v90", "v91", "v92", "v93", "v94", "v95", "vcc", "scc", "m0", "memory", "s36", "s37", "s38", "s39");
}
__device__ __forceinline__ void lean_dm1(const u32x32 (&W)[4], unsigned ea, unsigned da, unsigned x, unsigned lb, const char* s0, const char* s1, const unsigned (&av)[8], unsigned ps, unsigned dv, const char* db, unsigned rw, unsigned fa, unsigned fv,
    unsigned ga, unsigned mu, unsigned ad, int th, int tb, unsigned& sv, unsigned& su, unsigned& got) {
  asm volatile("s_setprio 2\n\t"
    "v_mov_b32 v76, %[ga]\n\t"
    "ds_read_b32 v76, v76 offset:24\n\t"
    "ds_read_b128 v[96:99], %[ea] offset:0\n\t"
    "ds_read_b128 v[100:103], %[ea] offset:2048\n\t"
    "ds_read_b128 v[104:107], %[da] offset:0\n\t"
    "ds_read_b128 v[108:111], %[da] offset:2048\n\t"
    "ds_read_b128 v[112:115], %[ea] offset:4096\n\t"
    "ds_read_b128 v[116:119], %[ea] offset:6144\n\t"
    "s_waitcnt lgkmcnt(6)\n\t"
    "v_cmp_ne_u32 vcc, 0, v76\n\t"
    "s_mov_b32 %[got], -1\n\t"
    "s_cbranch_vccnz 12f\n\t"
    "s_waitcnt lgkmcnt(4)\n\t"
    "v_mfma_f32_16x16x32_bf16 v[80:83], v[128:131], v[96:99], 0\n\t"
    "ds_read_b128 v[120:123], %[da] offset:4096\n\t"
    "s_add_u32 m0, %[lb], 0\n\t"
    "v_mfma_f32_16x16x32_bf16 v[84:87], v[132:135], v[96:99], 0\n\t"
    "ds_read_b128 v[124:127], %[da] offset:6144\n\t"
    "global_load_lds_dwordx4 %[av0], %[s0] sc1\n\t"
    "v_mfma_f32_16x16x32_bf16 v[88:91], v[128:131], v[100:103], 0\n\t"
    "v_mfma_f32_16x16x32_bf16 v[92:95], v[132:135], v[100:103], 0\n\t"
    "s_waitcnt vmcnt(5)\n\t"
    "s_barrier\n\t"
    "s_waitcnt lgkmcnt(4)\n\t"
    "v_mfma_f32_16x16x32_bf16 v[80:83], v[136:139], v[104:107], v[80:83]\n\t"
    "ds_read_b128 v[96:99], %[ea] offset:8192\n\t"
    "s_add_u32 m0, %[lb], 4096\n\t"
    "v_mfma_f32_16x16x32_bf16 v[84:87], v[140:143], v[104:107], v[84:87]\n\t"
    "ds_read_b128 v[100:103], %[ea] offset:10240\n\t"
    "global_load_lds_dwordx4 %[av2], %[s0] sc1\n\t"
    "v_mfma_f32_16x16x32_bf16 v[88:91], v[136:139], v[108:111], v[88:91]\n\t"
    "v_mfma_f32_16x16x32_bf16 v[92:95], v[140:143], v[108:111], v[92:95]\n\t"
    "s_waitcnt lgkmcnt(4)\n\t"
    "v_mfma_f32_16x16x32_bf16 v[80:83], v[144:147], v[112:115], v[80:83]\n\t"
    "ds_read_b128 v[104:107], %[da] offset:8192\n\t"
    "s_add_u32 m0, %[lb], 8192\n\t"
    "v_mfma_f32_16x16x32_bf16 v[84:87], v[148:151], v[112:115], v[84:87]\n\t"
    "ds_read_b128 v[108:111], %[da] offset:10240\n\t"
    "global_load_lds_dwordx4 %[av4], %[s0] sc1\n\t"
    "v_mfma_f32_16x16x32_bf16 v[88:91], v[144:147], v[116:119], v[88:91]\n\t"
    "v_mfma_f32_16x16x32_bf16 v[92:95], v[148:151], v[116:119], v[92:95]\n\t"
    "s_waitcnt lgkmcnt(4)\n\t"
    "v_mfma_f32_16x16x32_bf16 v[80:83], v[152:155], v[120:123], v[80:83]\n\t"
    "ds_read_b128 v[112:115], %[ea] offset:12288\n\t"
    "s_add_u32 m0, %[lb], 12288\n\t"
    "v_mfma_f32_16x16x32_bf16 v[84:87], v[156:159], v[120:123], v[84:87]\n\t"
    "ds_read_b128 v[116:119], %[ea] offset:14336\n\t"
    "global_load_lds_dwordx4 %[av6], %[s0] sc1\n\t"
    "v_mfma_f32_16x16x32_bf16 v[88:91], v[152:155], v[124:127], v[88:91]\n\t"
    "v_mfma_f32_16x16x32_bf16 v[92:95], v[156:159], v[124:127], v[92:95]\n\t"
    "s_waitcnt lgkmcnt(4)\n\t"
    "v_mfma_f32_16x16x32_bf16 v[80:83], v[160:163], v[96:99], v[80:83]\n\t"
    "ds_read_b128 v[120:123], %[da] offset:12288\n\t"
    "s_add_u32 m0, %[lb], 16384\n\t"
    "v_mfma_f32_16x16x32_bf16 v[84:87], v[164:167], v[96:99], v[84:87]\n\t"
    "ds_read_b128 v[124:127], %[da] offset:14336\n\t"
    "global_load_lds_dwordx4 %[av0], %[s1] sc1\n\t"
    "v_mfma_f32_16x16x32_bf16 v[88:91], v[160:163], v[100:103], v[88:91]\n\t"
    "v_mfma_f32_16x16x32_bf16 v[92:95], v[164:167], v[100:103], v[92:95]\n\t"
    "s_waitcnt vmcnt(7)\n\t"
    "s_barrier\n\t"
    "s_waitcnt lgkmcnt(4)\n\t"
    "v_mfma_f32_16x16x32_bf16 v[80:83], v[168:171], v[104:107], v[80:83]\n\t"
    "ds_read_b128 v[96:99], %[ea] offset:16384\n\t"
    "s_add_u32 m0, %[lb], 20480\n\t"
    "v_mfma_f32_16x16x32_bf16 v[84:87], v[172:175], v[104:107], v[84:87]\n\t"
    "ds_read_b128 v[100:103], %[ea] offset:18432\n\t"
    "global_load_lds_dwordx4 %[av2], %[s1] sc1\n\t"
    "v_mfma_f32_16x16x32_bf16 v[88:91], v[168:171], v[108:111], v[88:91]\n\t"
    "v_mfma_f32_16x16x32_bf16 v[92:95], v[172:175], v[108:111], v[92:95]\n\t"
    "s_waitcnt lgkmcnt(4)\n\t"
    "v_mfma_f32_16x16x32_bf16 v[80:83], v[176:179], v[112:115], v[80:83]\n\t"
    "ds_read_b128 v[104:107], %[da] offset:16384\n\t"
    "s_add_u32 m0, %[lb], 24576\n\t"
    "v_mfma_f32_16x16x32_bf16 v[84:87], v[180:183], v[112:115], v[84:87]\n\t"
    "ds_read_b128 v[108:111], %[da] offset:18432\n\t"
    "global_load_lds_dwordx4 %[av4], %[s1] sc1\n\t"
    "v_mfma_f32_16x16x32_bf16 v[88:91], v[176:179], v[116:119], v[88:91]\n\t"
    "v_mfma_f32_16x16x32_bf16 v[92:95], v[180:183], v[116:119], v[92:95]\n\t"
    "s_waitcnt lgkmcnt(4)\n\t"
    "v_mfma_f32_16x16x32_bf16 v[80:83], v[184:187], v[120:123], v[80:83]\n\t"
    "ds_read_b128 v[112:115], %[ea] offset:20480\n\t"
    "s_add_u32 m0, %[lb], 28672\n\t"
    "v_mfma_f32_16x16x32_bf16 v[84:87], v[188:191], v[120:123], v[84:87]\n\t"
    "ds_read_b128 v[116:119], %[ea] offset:22528\n\t"
    "global_load_lds_dwordx4 %[av6], %[s1] sc1\n\t"
    "v_mfma_f32_16x16x32_bf16 v[88:91], v[184:187], v[124:127], v[88:91]\n\t"
    "v_mfma_f32_16x16x32_bf16 v[92:95], v[188:191], v[124:127], v[92:95]\n\t"
    "s_waitcnt lgkmcnt(4)\n\t"
    "v_mfma_f32_16x16x32_bf16 v[80:83], v[192:195], v[96:99], v[80:83]\n\t"
    "ds_read_b128 v[120:123], %[da] offset:20480\n\t"
    "v_mfma_f32_16x16x32_bf16 v[84:87], v[196:199], v[96:99], v[84:87]\n\t"
    "ds_read_b128 v[124:127], %[da] offset:22528\n\t"
    "v_mfma_f32_16x16x32_bf16 v[88:91], v[192:195], v[100:103], v[88:91]\n\t"
    "v_mfma_f32_16x16x32_bf16 v[92:95], v[196:199], v[100:103], v[92:95]\n\t"
    "s_waitcnt vmcnt(8)\n\t"
    "s_barrier\n\t"
    "s_waitcnt lgkmcnt(4)\n\t"
    "v_mfma_f32_16x16x32_bf16 v[80:83], v[200:203], v[104:107], v[80:83]\n\t"
    "ds_read_b128 v[96:99], %[ea] offset:24576\n\t"
    "v_mfma_f32_16x16x32_bf16 v[84:87], v[204:207], v[104:107], v[84:87]\n\t"
    "ds_read_b128 v[100:103], %[ea] offset:26624\n\t"
    "v_mfma_f32_16x16x32_bf16 v[88:91], v[200:203], v[108:111], v[88:91]\n\t"
    "v_mfma_f32_16x16x32_bf16 v[92:95], v[204:207], v[108:111], v[92:95]\n\t"
    "s_waitcnt lgkmcnt(4)\n\t"
    "v_mfma_f32_16x16x32_bf16 v[80:83], v[208:211], v[112:115], v[80:83]\n\t"
    "ds_read_b128 v[104:107], %[da] offset:24576\n\t"
    "v_mfma_f32_16x16x32_bf16 v[84:87], v[212:215], v[112:115], v[84:87]\n\t"
    "ds_read_b128 v[108:111], %[da] offset:26624\n\t"
    "v_mfma_f32_16x16x32_bf16 v[88:91], v[208:211], v[116:119], v[88:91]\n\t"
    "v_mfma_f32_16x16x32_bf16 v[92:95], v[212:215], v[116:119], v[92:95]\n\t"
    "s_waitcnt lgkmcnt(4)\n\t"
    "v_mfma_f32_16x16x32_bf16 v[80:83], v[216:219], v[120:123], v[80:83]\n\t"
    "ds_read_b128 v[112:115], %[ea] offset:28672\n\t"
    "v_mfma_f32_16x16x32_bf16 v[84:87], v[220:223], v[120:123], v[84:87]\n\t"
    "ds_read_b128 v[116:119], %[ea] offset:30720\n\t"
    "v_mfma_f32_16x16x32_bf16 v[88:91], v[216:219], v[124:127], v[88:91]\n\t"
    "v_mfma_f32_16x16x32_bf16 v[92:95], v[220:223], v[124:127], v[92:95]\n\t"
    "s_waitcnt lgkmcnt(4)\n\t"
    "v_mfma_f32_16x16x32_bf16 v[80:83], v[224:227], v[96:99], v[80:83]\n\t"
    "ds_read_b128 v[120:123], %[da] offset:28672\n\t"
    "v_mfma_f32_16x16x32_bf16 v[84:87], v[228:231], v[96:99], v[84:87]\n\t"
    "ds_read_b128 v[124:127], %[da] offset:30720\n\t"
    "v_mfma_f32_16x16x32_bf16 v[88:91], v[224:227], v[100:103], v[88:91]\n\t"
    "v_mfma_f32_16x16x32_bf16 v[92:95], v[228:231], v[100:103], v[92:95]\n\t"
    "s_waitcnt lgkmcnt(4)\n\t"
    "v_mfma_f32_16x16x32_bf16 v[80:83], v[232:235], v[104:107], v[80:83]\n\t"
    "v_mfma_f32_16x16x32_bf16 v[84:87], v[236:239], v[104:107], v[84:87]\n\t"
    "v_mfma_f32_16x16x32_bf16 v[88:91], v[232:235], v[108:111], v[88:91]\n\t"
    "v_mfma_f32_16x16x32_bf16 v[92:95], v[236:239], v[108:111], v[92:95]\n\t"
    "s_waitcnt lgkmcnt(2)\n\t"
    "v_mfma_f32_16x16x32_bf16 v[80:83], v[240:243], v[112:115], v[80:83]\n\t"
    "v_mfma_f32_16x16x32_bf16 v[84:87], v[244:247], v[112:115], v[84:87]\n\t"
    "v_mfma_f32_16x16x32_bf16 v[88:91], v[240:243], v[116:119], v[88:91]\n\t"
    "v_mfma_f32_16x16x32_bf16 v[92:95], v[244:247], v[116:119], v[92:95]\n\t"
    "s_waitcnt lgkmcnt(0)\n\t"
    "v_mfma_f32_16x16x32_bf16 v[80:83], v[248:251], v[120:123], v[80:83]\n\t"
    "v_mfma_f32_16x16x32_bf16 v[84:87], v[252:255], v[120:123], v[84:87]\n\t"
    "v_mfma_f32_16x16x32_bf16 v[88:91], v[248:251], v[124:127], v[88:91]\n\t"
    "v_mfma_f32_16x16x32_bf16 v[92:95], v[252:255], v[124:127], v[92:95]\n\t"
    "s_cmp_eq_u32 %[ps], 0\n\t"
    "s_cbranch_scc1 10f\n\t"
    "global_load_dword %[sv], %[dv], %[db] sc1\n\t"
    "global_load_dword %[su], %[dv], %[db] offset:64 sc1\n\t"
    "10:\n\t"
    "s_mov_b32 %[got], 0\n\t"
    "v_mov_b32 v76, %[fa]\n\t"
    "v_mov_b32 v77, %[fv]\n\t"
    "v_add_u32 v78, 1, v77\n\t"
    "v_mov_b32 v79, %[ga]\n\t"
    "ds_write_b128 %[rw], v[80:83] offset:0\n\t"
    "ds_write_b128 %[rw], v[84:87] offset:1024\n\t"
    "ds_write_b128 %[rw], v[88:91] offset:2048\n\t"
    "ds_write_b128 %[rw], v[92:95] offset:3072\n\t"
    "ds_write_b32 v76, v77\n\t"
    "s_cmp_eq_u32 %[ps], 0\n\t"
    "s_cbranch_scc1 9f\n\t"
    "s_waitcnt vmcnt(0)\n\t"
    "s_branch 9f\n\t"
    "12:\n\t"
    "s_waitcnt lgkmcnt(0)\n\t"
    "9:"
    : [sv] "+&v"(sv), [su] "+&v"(su), [got] "=&s"(got)
    : [ea] "v"(ea), [da] "v"(da), [x] "s"(x), "{v[128:159]}"(W[0]), "{v[160:191]}"(W[1]), "{v[192:223]}"(W[2]), "{v[224:255]}"(W[3]), [lb] "s"(lb), [s0] "s"(s0), [s1] "s"(s1), [av0] "v"(av[0]), [av1] "v"(av[1]), [av2] "v"(av[2]), [av3] "v"(av[3]), [av4] "v"(av[4]), [av5] "v"(av[5]), [av6] "v"(av[6]), [av7] "v"(av[7]), [ps] "s"(ps), [dv] "v"(dv), [db] "s"(db), [rw] "v"(rw), [fa] "s"(fa), [fv] "s"(fv), [ga] "s"(ga), [mu] "s"(mu), [ad] "v"(ad), [th] "s"(th), [tb] "s"(tb)
    : "v96", "v97", "v98", "v99", "v100", "v101", "v102", "v103", "v104", "v105", "v106", "v107", "v108", "v109", "v110", "v111", "v112", "v113", "v114", "v115", "v116", "v117", "v118", "v119", "v120", "v121", "v122", "v123", "v124", "v125", "v126", "v127", "v76", "v77", "v78", "v79", "v80", "v81", "v82", "v83", "v84", "v85", "v86", "v87", "v88", "v89", "v90", "v91", "v92", "v93", "v94", "v95", "vcc", "scc", "m0", "memory");
}
__device__ __forceinline__ void lean_st0(const u32x32 (&W)[4], unsigned ea, unsigned da, unsigned x, u32x16& pd, unsigned pv, unsigned cn, const char* pg, unsigned bl, unsigned& tk, unsigned& st) {
  asm volatile("v_mov_b32 v76, %[cn]\n\t"
    "ds_read_b32 v76, v76 offset:28\n\t"
    "ds_read_b128 v[96:99], %[ea] offset:0\n\t"
    "ds_read_b128 v[100:103], %[ea] offset:2048\n\t"
    "ds_read_b128 v[104:107], %[da] offset:0\n\t"
    "ds_read_b128 v[108:111], %[da] offset:2048\n\t"
    "ds_read_b128 v[112:115], %[ea] offset:4096\n\t"
    "ds_read_b128 v[116:119], %[ea] offset:6144\n\t"
    "s_waitcnt lgkmcnt(6)\n\t"
    "v_cmp_ne_u32 vcc, 0, v76\n\t"
    "s_mov_b32 %[st], -1\n\t"
    "s_cbranch_vccnz 12f\n\t"
    "s_waitcnt lgkmcnt(4)\n\t"
    "v_mfma_f32_16x16x32_bf16 v[80:83], v[128:131], v[96:99], 0\n\t"
    "ds_read_b128 v[120:123], %[da] offset:4096\n\t"
    "v_mfma_f32_16x16x32_bf16 v[84:87], v[132:135], v[96:99], 0\n\t"
    "ds_read_b128 v[124:127], %[da] offset:6144\n\t"
    "v_mfma_f32_16x16x32_bf16 v[88:91], v[128:131], v[100:103], 0\n\t"
    "v_mfma_f32_16x16x32_bf16 v[92:95], v[132:135], v[100:103], 0\n\t"
    "s_barrier\n\t"
    "s_bitcmp1_b32 %[x], 2\n\t"
    "s_cbranch_scc1 7f\n\t"
    "8:\n\t"
    "s_bitcmp1_b32 %[x], 3\n\t"
    "s_cbranch_scc1 13f\n\t"
    "14:\n\t"
    "s_waitcnt lgkmcnt(4)\n\t"
    "v_mfma_f32_16x16x32_bf16 v[80:83], v[136:139], v[104:107], v[80:83]\n\t"
    "ds_read_b128 v[96:99], %[ea] offset:8192\n\t"
    "v_mfma_f32_16x16x32_bf16 v[84:87], v[140:143], v[104:107], v[84:87]\n\t"
    "ds_read_b128 v[100:103], %[ea] offset:10240\n\t"
    "v_mfma_f32_16x16x32_bf16 v[88:91], v[136:139], v[108:111], v[88:91]\n\t"
    "v_mfma_f32_16x16x32_bf16 v[92:95], v[140:143], v[108:111], v[92:95]\n\t"
    "s_waitcnt lgkmcnt(4)\n\t"
    "v_mfma_f32_16x16x32_bf16 v[80:83], v[144:147], v[112:115], v[80:83]\n\t"
    "ds_read_b128 v[104:107], %[da] offset:8192\n\t"
    "v_mfma_f32_16x16x32_bf16 v[84:87], v[148:151], v[112:115], v[84:87]\n\t"
    "ds_read_b128 v[108:111], %[da] offset:10240\n\t"
    "v_mfma_f32_16x16x32_bf16 v[88:91], v[144:147], v[116:119], v[88:91]\n\t"
    "v_mfma_f32_16x16x32_bf16 v[92:95], v[148:151], v[116:119], v[92:95]\n\t"
    "s_waitcnt lgkmcnt(4)\n\t"
    "v_mfma_f32_16x16x32_bf16 v[80:83], v[152:155], v[120:123], v[80:83]\n\t"
    "ds_read_b128 v[112:115], %[ea] offset:12288\n\t"
    "v_mfma_f32_16x16x32_bf16 v[84:87], v[156:159], v[120:123], v[84:87]\n\t"
    "ds_read_b128 v[116:119], %[ea] offset:14336\n\t"
    "v_mfma_f32_16x16x32_bf16 v[88:91], v[152:155], v[124:127], v[88:91]\n\t"
    "v_mfma_f32_16x16x32_bf16 v[92:95], v[156:159], v[124:127], v[92:95]\n\t"
    "s_waitcnt lgkmcnt(4)\n\t"
    "v_mfma_f32_16x16x32_bf16 v[80:83], v[160:163], v[96:99], v[80:83]\n\t"
    "ds_read_b128 v[120:123], %[da] offset:12288\n\t"
    "v_mfma_f32_16x16x32_bf16 v[84:87], v[164:167], v[96:99], v[84:87]\n\t"
    "ds_read_b128 v[124:127], %[da] offset:14336\n\t"
    "v_mfma_f32_16x16x32_bf16 v[88:91], v[160:163], v[100:103], v[88:91]\n\t"
    "v_mfma_f32_16x16x32_bf16 v[92:95], v[164:167], v[100:103], v[92:95]\n\t"
    "s_barrier\n\t"
    "s_waitcnt lgkmcnt(4)\n\t"
    "v_mfma_f32_16x16x32_bf16 v[80:83], v[168:171], v[104:107], v[80:83]\n\t"
    "ds_read_b128 v[96:99], %[ea] offset:16384\n\t"
    "v_mfma_f32_16x16x32_bf16 v[84:87], v[172:175], v[104:107], v[84:87]\n\t"
    "ds_read_b128 v[100:103], %[ea] offset:18432\n\t"
    "v_mfma_f32_16x16x32_bf16 v[88:91], v[168:171], v[108:111], v[88:91]\n\t"
    "v_mfma_f32_16x16x32_bf16 v[92:95], v[172:175], v[108:111], v[92:95]\n\t"
    "s_waitcnt lgkmcnt(4)\n\t"
    "v_mfma_f32_16x16x32_bf16 v[80:83], v[176:179], v[112:115], v[80:83]\n\t"
    "ds_read_b128 v[104:107], %[da] offset:16384\n\t"
    "v_mfma_f32_16x16x32_bf16 v[84:87], v[180:183], v[112:115], v[84:87]\n\t"
    "ds_read_b128 v[108:111], %[da] offset:18432\n\t"
    "v_mfma_f32_16x16x32_bf16 v[88:91], v[176:179], v[116:119], v[88:91]\n\t"
    "v_mfma_f32_16x16x32_bf16 v[92:95], v[180:183], v[116:119], v[92:95]\n\t"
    "s_waitcnt lgkmcnt(4)\n\t"
    "v_mfma_f32_16x16x32_bf16 v[80:83], v[184:187], v[120:123], v[80:83]\n\t"
    "ds_read_b128 v[112:115], %[ea] offset:20480\n\t"
    "v_mfma_f32_16x16x32_bf16 v[84:87], v[188:191], v[120:123], v[84:87]\n\t"
    "ds_read_b128 v[116:119], %[ea] offset:22528\n\t"
    "v_mfma_f32_16x16x32_bf16 v[88:91], v[184:187], v[124:127], v[88:91]\n\t"
    "v_mfma_f32_16x16x32_bf16 v[92:95], v[188:191], v[124:127], v[92:95]\n\t"
    "s_waitcnt lgkmcnt(4)\n\t"
    "v_mfma_f32_16x16x32_bf16 v[80:83], v[192:195], v[96:99], v[80:83]\n\t"
    "ds_read_b128 v[120:123], %[da] offset:20480\n\t"
    "v_mfma_f32_16x16x32_bf16 v[84:87], v[196:199], v[96:99], v[84:87]\n\t"
    "ds_read_b128 v[124:127], %[da] offset:22528\n\t"
    "v_mfma_f32_16x16x32_bf16 v[88:91], v[192:195], v[100:103], v[88:91]\n\t"
    "v_mfma_f32_16x16x32_bf16 v[92:95], v[196:199], v[100:103], v[92:95]\n\t"
    "s_barrier\n\t"
    "s_and_b32 %[st], %[x], 3\n\t"
    "s_cbranch_scc1 3f\n\t"
    "4:\n\t"
    "s_waitcnt lgkmcnt(4)\n\t"
    "v_mfma_f32_16x16x32_bf16 v[80:83], v[200:203], v[104:107], v[80:83]\n\t"
    "ds_read_b128 v[96:99], %[ea] offset:24576\n\t"
    "v_mfma_f32_16x16x32_bf16 v[84:87], v[204:207], v[104:107], v[84:87]\n\t"
    "ds_read_b128 v[100:103], %[ea] offset:26624\n\t"
    "v_mfma_f32_16x16x32_bf16 v[88:91], v[200:203], v[108:111], v[88:91]\n\t"
    "v_mfma_f32_16x16x32_bf16 v[92:95], v[204:207], v[108:111], v[92:95]\n\t"
    "s_waitcnt lgkmcnt(4)\n\t"
    "v_mfma_f32_16x16x32_bf16 v[80:83], v[208:211], v[112:115], v[80:83]\n\t"
    "ds_read_b128 v[104:107], %[da] offset:24576\n\t"
    "v_mfma_f32_16x16x32_bf16 v[84:87], v[212:215], v[112:115], v[84:87]\n\t"
    "ds_read_b128 v[108:111], %[da] offset:26624\n\t"
    "v_mfma_f32_16x16x32_bf16 v[88:91], v[208:211], v[116:119], v[88:91]\n\t"
    "v_mfma_f32_16x16x32_bf16 v[92:95], v[212:215], v[116:119], v[92:95]\n\t"
    "s_waitcnt lgkmcnt(4)\n\t"
    "v_mfma_f32_16x16x32_bf16 v[80:83], v[216:219], v[120:123], v[80:83]\n\t"
    "ds_read_b128 v[112:115], %[ea] offset:28672\n\t"
    "v_mfma_f32_16x16x32_bf16 v[84:87], v[220:223], v[120:123], v[84:87]\n\t"
    "ds_read_b128 v[116:119], %[ea] offset:30720\n\t"
    "v_mfma_f32_16x16x32_bf16 v[88:91], v[216:219], v[124:127], v[88:91]\n\t"
    "v_mfma_f32_16x16x32_bf16 v[92:95], v[220:223], v[124:127], v[92:95]\n\t"
    "s_waitcnt lgkmcnt(4)\n\t"
    "v_mfma_f32_16x16x32_bf16 v[80:83], v[224:227], v[96:99], v[80:83]\n\t"
    "ds_read_b128 v[120:123], %[da] offset:28672\n\t"
    "v_mfma_f32_16x16x32_bf16 v[84:87], v[228:231], v[96:99], v[84:87]\n\t"
    "ds_read_b128 v[124:127], %[da] offset:30720\n\t"
    "v_mfma_f32_16x16x32_bf16 v[88:91], v[224:227], v[100:103], v[88:91]\n\t"
    "v_mfma_f32_16x16x32_bf16 v[92:95], v[228:231], v[100:103], v[92:95]\n\t"
    "s_waitcnt lgkmcnt(4)\n\t"
    "v_mfma_f32_16x16x32_bf16 v[80:83], v[232:235], v[104:107], v[80:83]\n\t"
    "v_mfma_f32_16x16x32_bf16 v[84:87], v[236:239], v[104:107], v[84:87]\n\t"
    "v_mfma_f32_16x16x32_bf16 v[88:91], v[232:235], v[108:111], v[88:91]\n\t"
    "v_mfma_f32_16x16x32_bf16 v[92:95], v[236:239], v[108:111], v[92:95]\n\t"
    "s_waitcnt lgkmcnt(2)\n\t"
    "v_mfma_f32_16x16x32_bf16 v[80:83], v[240:243], v[112:115], v[80:83]\n\t"
    "v_mfma_f32_16x16x32_bf16 v[84:87], v[244:247], v[112:115], v[84:87]\n\t"
    "v_mfma_f32_16x16x32_bf16 v[88:91], v[240:243], v[116:119], v[88:91]\n\t"
    "v_mfma_f32_16x16x32_bf16 v[92:95], v[244:247], v[116:119], v[92:95]\n\t"
    "s_waitcnt lgkmcnt(0)\n\t"
    "v_mfma_f32_16x16x32_bf16 v[80:83], v[248:251], v[120:123], v[80:83]\n\t"
    "v_mfma_f32_16x16x32_bf16 v[84:87], v[252:255], v[120:123], v[84:87]\n\t"
    "v_mfma_f32_16x16x32_bf16 v[88:91], v[248:251], v[124:127], v[88:91]\n\t"
    "v_mfma_f32_16x16x32_bf16 v[92:95], v[252:255], v[124:127], v[92:95]\n\t"
    "s_mov_b32 %[st], 0\n\t"
    "s_nop 5\n\t"
    "s_branch 9f\n\t"
    "13:\n\t"
    "s_memrealtime s[36:37]\n\t"
    "v_mov_b32 v79, 0\n\t"
    "s_waitcnt lgkmcnt(0)\n\t"
    "s_mov_b32 %[tk], s36\n\t"
    "s_add_u32 s36, s36, %[bl]\n\t"
    "s_and_b32 s36, s36, 0xffffff\n\t"
    "s_and_b32 s37, %[bl], 0xff000000\n\t"
    "s_or_b32 s36, s36, s37\n\t"
    "v_mov_b32 v78, s36\n\t"
    "s_mov_b64 exec, 1\n\t"
    "global_store_dword v79, v78, %[pg] offset:64 sc1\n\t"
    "s_mov_b64 exec, -1\n\t"
    "s_branch 14b\n\t"
    "3:\n\t"
    "7:\n\t"
    "s_and_b32 %[st], %[pv], 1\n\t"
    "s_mul_i32 %[st], %[st], 36\n\t"
    "s_add_u32 %[st], %[st], %[cn]\n\t"
    "v_mov_b32 v76, %[st]\n\t"
    "v_mov_b32 v77, 1\n\t"
    "v_mov_b32 v78, %[pv]\n\t"
    "v_mov_b32 v79, 0\n\t"
    "s_waitcnt vmcnt(0)\n\t"
    "s_mov_b64 exec, 1\n\t"
    "ds_add_rtn_u32 v77, v76, v77\n\t"
    "s_waitcnt lgkmcnt(0)\n\t"
    "v_readfirstlane_b32 %[st], v77\n\t"
    "s_and_b32 %[st], %[st], 3\n\t"
    "s_cmp_lg_u32 %[st], 3\n\t"
    "s_cbranch_scc1 6f\n\t"
    "global_store_dword v79, v78, %[pg] sc1\n\t"
    "6:\n\t"
    "s_mov_b64 exec, -1\n\t"
    "s_bitcmp1_b32 %[x], 2\n\t"
    "s_cbranch_scc1 8b\n\t"
    "s_branch 4b\n\t"
    "12:\n\t"
    "s_waitcnt lgkmcnt(0)\n\t"
    "9:"
    : "=&{v[80:95]}"(pd), [st] "=&s"(st), [tk] "+&s"(tk)
    : [ea] "v"(ea), [da] "v"(da), [x] "s"(x), "{v[128:159]}"(W[0]), "{v[160:191]}"(W[1]), "{v[192:223]}"(W[2]), "{v[224:255]}"(W[3]), [pv] "s"(pv), [cn] "s"(cn), [pg] "s"(pg), [bl] "s"(bl)
    : "v96", "v97", "v98", "v99", "v100", "v101", "v102", "v103", "v104", "v105", "v106", "v107", "v108", "v109", "v110", "v111", "v112", "v113", "v114", "v115", "v116", "v117", "v118", "v119", "v120", "v121", "v122", "v123", "v124", "v125", "v126", "v127", "v76", "v77", "v78", "v79", "vcc", "scc", "m0", "memory", "s36", "s37", "s38", "s39");
}
#define LEAN_QB 1
#define PREK 10
#define NEARLY 16
__device__ __forceinline__ void fin_ld(const u32x16& pd, unsigned rw, unsigned fa, unsigned fv) {
  asm volatile("ds_write_b128 %[rw], v[80:83] offset:0\n\t"
    "ds_write_b128 %[rw], v[84:87] offset:1024\n\t"
    "ds_write_b128 %[rw], v[88:91] offset:2048\n\t"
    "ds_write_b128 %[rw], v[92:95] offset:3072\n\t"
    "ds_write_b32 %[fa], %[fv]"
    :
    : [rw] "v"(rw), "{v[80:95]}"(pd), [fa] "v"(fa), [fv] "v"(fv)
    : "memory");
}
__device__ __forceinline__ void fin_st0(const u32x16& pd, unsigned rw, unsigned fa, unsigned fv, const char* cb, unsigned co0, unsigned co1, unsigned pa, unsigned& bad, unsigned& pk) {
  asm volatile("1:\n\t"
    "ds_read_b32 v39, %[fa]\n\t"
    "ds_read_b128 v[48:51], %[rw] offset:0\n\t"
    "ds_read_b128 v[52:55], %[rw] offset:1024\n\t"
    "ds_read_b128 v[56:59], %[rw] offset:2048\n\t"
    "ds_read_b128 v[60:63], %[rw] offset:3072\n\t"
    "s_waitcnt lgkmcnt(2)\n\t"
    "v_cmp_ne_u32 vcc, v39, %[fv]\n\t"
    "s_cbranch_vccz 2f\n\t"
    "s_waitcnt lgkmcnt(0)\n\t"
    "s_branch 1b\n\t"
    "2:\n\t"
    "v_add_f32 v48, v48, v80\n\t"
    "v_add_f32 v49, v49, v81\n\t"
    "v_add_f32 v50, v50, v82\n\t"
    "v_add_f32 v51, v51, v83\n\t"
    "v_add_f32 v52, v52, v84\n\t"
    "v_add_f32 v53, v53, v85\n\t"
    "v_add_f32 v54, v54, v86\n\t"
    "v_add_f32 v55, v55, v87\n\t"
    "v_cvt_pk_bf16_f32 v40, v48, v49\n\t"
    "v_cvt_pk_bf16_f32 v41, v50, v51\n\t"
    "v_cvt_pk_bf16_f32 v42, v52, v53\n\t"
    "v_cvt_pk_bf16_f32 v43, v54, v55\n\t"
    "global_store_dwordx4 %[co0], v[40:43], %[cb] sc1\n\t"
    "s_waitcnt lgkmcnt(0)\n\t"
    "v_mov_b32 v39, %[pa]\n\t"
    "ds_read_b32 v39, v39\n\t"
    "v_add_f32 v56, v56, v88\n\t"
    "v_add_f32 v57, v57, v89\n\t"
    "v_add_f32 v58, v58, v90\n\t"
    "v_add_f32 v59, v59, v91\n\t"
    "v_add_f32 v60, v60, v92\n\t"
    "v_add_f32 v61, v61, v93\n\t"
    "v_add_f32 v62, v62, v94\n\t"
    "v_add_f32 v63, v63, v95\n\t"
    "v_cvt_pk_bf16_f32 v44, v56, v57\n\t"
    "v_cvt_pk_bf16_f32 v45, v58, v59\n\t"
    "v_cvt_pk_bf16_f32 v46, v60, v61\n\t"
    "v_cvt_pk_bf16_f32 v47, v62, v63\n\t"
    "global_store_dwordx4 %[co1], v[44:47], %[cb] sc1\n\t"
    "v_cmp_u_f32 vcc, v48, v56\n\t"
    "s_waitcnt lgkmcnt(0)\n\t"
    "s_or_b32 %[bad], vcc_lo, vcc_hi\n\t"
    "v_readfirstlane_b32 %[pk], v39"
    : [bad] "=&s"(bad), [pk] "=&s"(pk)
    : [rw] "v"(rw), "{v[80:95]}"(pd), [fa] "v"(fa), [fv] "v"(fv), [cb] "s"(cb), [co0] "v"(co0), [co1] "v"(co1), [pa] "s"(pa)
    : "v39", "v40", "v41", "v42", "v43", "v44", "v45", "v46", "v47", "v48", "v49", "v50", "v51", "v52", "v53", "v54", "v55", "v56", "v57", "v58", "v59", "v60", "v61", "v62", "v63", "vcc", "scc", "memory");
}
__device__ __forceinline__ void fin_st1(const u32x16& pd, unsigned rw, unsigned fa, unsigned fv, const char* cb, unsigned co0, unsigned co1, unsigned pa, unsigned& bad, unsigned& pk) {
  asm volatile("1:\n\t"
    "ds_read_b32 v39, %[fa]\n\t"
    "ds_read_b128 v[48:51], %[rw] offset:0\n\t"
    "ds_read_b128 v[52:55], %[rw] offset:1024\n\t"
    "ds_read_b128 v[56:59], %[rw] offset:2048\n\t"
    "ds_read_b128 v[60:63], %[rw] offset:3072\n\t"
    "s_waitcnt lgkmcnt(2)\n\t"
    "v_cmp_ne_u32 vcc, v39, %[fv]\n\t"
    "s_cbranch_vccz 2f\n\t"
    "s_waitcnt lgkmcnt(0)\n\t"
    "s_branch 1b\n\t"
    "2:\n\t"
    "v_add_f32 v48, v48, v80\n\t"
    "v_add_f32 v49, v49, v81\n\t"
    "v_add_f32 v50, v50, v82\n\t"
    "v_add_f32 v51, v51, v83\n\t"
    "v_add_f32 v52, v52, v84\n\t"
    "v_add_f32 v53, v53, v85\n\t"
    "v_add_f32 v54, v54, v86\n\t"
    "v_add_f32 v55, v55, v87\n\t"
    "s_waitcnt lgkmcnt(0)\n\t"
    "v_mov_b32 v39, %[pa]\n\t"
    "ds_read_b32 v39, v39\n\t"
    "v_add_f32 v56, v56, v88\n\t"
    "v_add_f32 v57, v57, v89\n\t"
    "v_add_f32 v58, v58, v90\n\t"
    "v_add_f32 v59, v59, v91\n\t"
    "v_add_f32 v60, v60, v92\n\t"
    "v_add_f32 v61, v61, v93\n\t"
    "v_add_f32 v62, v62, v94\n\t"
    "v_add_f32 v63, v63, v95\n\t"
    "v_mul_f32 v40, %[nl], v48\n\t"
    "v_mul_f32 v41, %[nl], v49\n\t"
    "v_mul_f32 v42, %[nl], v50\n\t"
    "v_mul_f32 v43, %[nl], v51\n\t"
    "v_mul_f32 v44, %[nl], v56\n\t"
    "v_mul_f32 v45, %[nl], v57\n\t"
    "v_mul_f32 v46, %[nl], v58\n\t"
    "v_mul_f32 v47, %[nl], v59\n\t"
    "v_exp_f32 v40, v40\n\t"
    "v_exp_f32 v41, v41\n\t"
    "v_exp_f32 v42, v42\n\t"
    "v_exp_f32 v43, v43\n\t"
    "v_exp_f32 v44, v44\n\t"
    "v_exp_f32 v45, v45\n\t"
    "v_exp_f32 v46, v46\n\t"
    "v_exp_f32 v47, v47\n\t"
    "v_add_f32 v40, 1.0, v40\n\t"
    "v_add_f32 v41, 1.0, v41\n\t"
    "v_add_f32 v42, 1.0, v42\n\t"
    "v_add_f32 v43, 1.0, v43\n\t"
    "v_add_f32 v44, 1.0, v44\n\t"
    "v_add_f32 v45, 1.0, v45\n\t"
    "v_add_f32 v46, 1.0, v46\n\t"
    "v_add_f32 v47, 1.0, v47\n\t"
    "v_rcp_f32 v40, v40\n\t"
    "v_rcp_f32 v41, v41\n\t"
    "v_rcp_f32 v42, v42\n\t"
    "v_rcp_f32 v43, v43\n\t"
    "v_rcp_f32 v44, v44\n\t"
    "v_rcp_f32 v45, v45\n\t"
    "v_rcp_f32 v46, v46\n\t"
    "v_rcp_f32 v47, v47\n\t"
    "v_mul_f32 v40, v48, v40\n\t"
    "v_mul_f32 v41, v49, v41\n\t"
    "v_mul_f32 v42, v50, v42\n\t"
    "v_mul_f32 v43, v51, v43\n\t"
    "v_mul_f32 v44, v56, v44\n\t"
    "v_mul_f32 v45, v57, v45\n\t"
    "v_mul_f32 v46, v58, v46\n\t"
    "v_mul_f32 v47, v59, v47\n\t"
    "v_mul_f32 v40, v40, v52\n\t"
    "v_mul_f32 v41, v41, v53\n\t"
    "v_mul_f32 v42, v42, v54\n\t"
    "v_mul_f32 v43, v43, v55\n\t"
    "v_mul_f32 v44, v44, v60\n\t"
    "v_mul_f32 v45, v45, v61\n\t"
    "v_mul_f32 v46, v46, v62\n\t"
    "v_mul_f32 v47, v47, v63\n\t"
    "v_cvt_pk_bf16_f32 v48, v40, v41\n\t"
    "v_cvt_pk_bf16_f32 v49, v42, v43\n\t"
    "v_cvt_pk_bf16_f32 v50, v44, v45\n\t"
    "v_cvt_pk_bf16_f32 v51, v46, v47\n\t"
    "s_nop 1\n\t"
    "v_permlane16_swap_b32 v48, v50\n\t"
    "v_permlane16_swap_b32 v49, v51\n\t"
    "global_store_dwordx4 %[co0], v[48:51], %[cb] sc1\n\t"
    "v_cmp_u_f32 vcc, v52, v60\n\t"
    "s_waitcnt lgkmcnt(0)\n\t"
    "s_or_b32 %[bad], vcc_lo, vcc_hi\n\t"
    "v_readfirstlane_b32 %[pk], v39"
    : [bad] "=&s"(bad), [pk] "=&s"(pk)
    : [rw] "v"(rw), "{v[80:95]}"(pd), [fa] "v"(fa), [fv] "v"(fv), [cb] "s"(cb), [co0] "v"(co0), [nl] "s"(-LOG2E), [pa] "s"(pa)
    : "v39", "v40", "v41", "v42", "v43", "v44", "v45", "v46", "v47", "v48", "v49", "v50", "v51", "v52", "v53", "v54", "v55", "v56", "v57", "v58", "v59", "v60", "v61", "v62", "v63", "vcc", "scc", "memory");
}
__device__ __forceinline__ void fin_st2(const u32x16& pd, unsigned rw, unsigned fa, unsigned fv, const char* cb, unsigned co0, unsigned co1, unsigned pa, unsigned& bad, unsigned& pk) {
  asm volatile("s_nop 1\n\t"
    "v_cvt_pk_bf16_f32 v40, v80, v81\n\t"
    "v_cvt_pk_bf16_f32 v41, v82, v83\n\t"
    "v_cvt_pk_bf16_f32 v42, v84, v85\n\t"
    "v_cvt_pk_bf16_f32 v43, v86, v87\n\t"
    "global_store_dwordx4 %[co0], v[40:43], %[cb] sc1\n\t"
    "v_cvt_pk_bf16_f32 v44, v88, v89\n\t"
    "v_cvt_pk_bf16_f32 v45, v90, v91\n\t"
    "v_cvt_pk_bf16_f32 v46, v92, v93\n\t"
    "v_cvt_pk_bf16_f32 v47, v94, v95\n\t"
    "global_store_dwordx4 %[co1], v[44:47], %[cb] sc1\n\t"
    "1:\n\t"
    "ds_read_b32 v39, %[fa]\n\t"
    "ds_read_b128 v[48:51], %[rw] offset:0\n\t"
    "ds_read_b128 v[52:55], %[rw] offset:1024\n\t"
    "ds_read_b128 v[56:59], %[rw] offset:2048\n\t"
    "ds_read_b128 v[60:63], %[rw] offset:3072\n\t"
    "s_waitcnt lgkmcnt(2)\n\t"
    "v_cmp_ne_u32 vcc, v39, %[fv]\n\t"
    "s_cbranch_vccz 2f\n\t"
    "s_waitcnt lgkmcnt(0)\n\t"
    "s_branch 1b\n\t"
    "2:\n\t"
    "v_cvt_pk_bf16_f32 v48, v48, v49\n\t"
    "v_cvt_pk_bf16_f32 v49, v50, v51\n\t"
    "v_cvt_pk_bf16_f32 v50, v52, v53\n\t"
    "v_cvt_pk_bf16_f32 v51, v54, v55\n\t"
    "global_store_dwordx4 %[co0], v[48:51], %[cb] offset:2048 sc1\n\t"
    "s_waitcnt lgkmcnt(0)\n\t"
    "v_mov_b32 v39, %[pa]\n\t"
    "ds_read_b32 v39, v39\n\t"
    "v_cvt_pk_bf16_f32 v56, v56, v57\n\t"
    "v_cvt_pk_bf16_f32 v57, v58, v59\n\t"
    "v_cvt_pk_bf16_f32 v58, v60, v61\n\t"
    "v_cvt_pk_bf16_f32 v59, v62, v63\n\t"
    "global_store_dwordx4 %[co1], v[56:59], %[cb] offset:2048 sc1\n\t"
    "v_cmp_u_f32 vcc, v80, v88\n\t"
    "s_waitcnt lgkmcnt(0)\n\t"
    "s_or_b32 %[bad], vcc_lo, vcc_hi\n\t"
    "v_readfirstlane_b32 %[pk], v39"
    : [bad] "=&s"(bad), [pk] "=&s"(pk)
    : [rw] "v"(rw), "{v[80:95]}"(pd), [fa] "v"(fa), [fv] "v"(fv), [cb] "s"(cb), [co0] "v"(co0), [co1] "v"(co1), [pa] "s"(pa)
    : "v39", "v40", "v41", "v42", "v43", "v44", "v45", "v46", "v47", "v48", "v49", "v50", "v51", "v52", "v53", "v54", "v55", "v56", "v57", "v58", "v59", "v60", "v61", "v62", "v63", "vcc", "scc", "memory");
}
template <int R> __device__ __forceinline__ void gw_load(u32x32& w, unsigned a0, unsigned a1);
template <> __device__ __forceinline__ void gw_load<0>(u32x32& w, unsigned a0, unsigned a1) {
  asm volatile("ds_read_b64_tr_b16 v[128:129], %1 offset:0\n\t"
    "ds_read_b64_tr_b16 v[130:131], %1 offset:512\n\t"
    "ds_read_b64_tr_b16 v[132:133], %2 offset:0\n\t"
    "ds_read_b64_tr_b16 v[134:135], %2 offset:512\n\t"
    "ds_read_b64_tr_b16 v[136:137], %1 offset:4096\n\t"
    "ds_read_b64_tr_b16 v[138:139], %1 offset:4608\n\t"
    "ds_read_b64_tr_b16 v[140:141], %2 offset:4096\n\t"
    "ds_read_b64_tr_b16 v[142:143], %2 offset:4608\n\t"
    "s_waitcnt lgkmcnt(0)\n\t"
    "ds_read_b64_tr_b16 v[144:145], %1 offset:16384\n\t"
    "ds_read_b64_tr_b16 v[146:147], %1 offset:16896\n\t"
    "ds_read_b64_tr_b16 v[148:149], %2 offset:16384\n\t"
    "ds_read_b64_tr_b16 v[150:151], %2 offset:16896\n\t"
    "ds_read_b64_tr_b16 v[152:153], %1 offset:20480\n\t"
    "ds_read_b64_tr_b16 v[154:155], %1 offset:20992\n\t"
    "ds_read_b64_tr_b16 v[156:157], %2 offset:20480\n\t"
    "ds_read_b64_tr_b16 v[158:159], %2 offset:20992\n\t"
    "s_waitcnt lgkmcnt(0)"
    : "=&{v[128:159]}"(w) : "v"(a0), "v"(a1) : "memory");
}
template <> __device__ __forceinline__ void gw_load<1>(u32x32& w, unsigned a0, unsigned a1) {
  asm volatile("ds_read_b64_tr_b16 v[160:161], %1 offset:0\n\t"
    "ds_read_b64_tr_b16 v[162:163], %1 offset:512\n\t"
    "ds_read_b64_tr_b16 v[164:165], %2 offset:0\n\t"
    "ds_read_b64_tr_b16 v[166:167], %2 offset:512\n\t"
    "ds_read_b64_tr_b16 v[168:169], %1 offset:4096\n\t"
    "ds_read_b64_tr_b16 v[170:171], %1 offset:4608\n\t"
    "ds_read_b64_tr_b16 v[172:173], %2 offset:4096\n\t"
    "ds_read_b64_tr_b16 v[174:175], %2 offset:4608\n\t"
    "s_waitcnt lgkmcnt(0)\n\t"
    "ds_read_b64_tr_b16 v[176:177], %1 offset:16384\n\t"
    "ds_read_b64_tr_b16 v[178:179], %1 offset:16896\n\t"
    "ds_read_b64_tr_b16 v[180:181], %2 offset:16384\n\t"
    "ds_read_b64_tr_b16 v[182:183], %2 offset:16896\n\t"
    "ds_read_b64_tr_b16 v[184:185], %1 offset:20480\n\t"
    "ds_read_b64_tr_b16 v[186:187], %1 offset:20992\n\t"
    "ds_read_b64_tr_b16 v[188:189], %2 offset:20480\n\t"
    "ds_read_b64_tr_b16 v[190:191], %2 offset:20992\n\t"
    "s_waitcnt lgkmcnt(0)"
    : "=&{v[160:191]}"(w) : "v"(a0), "v"(a1) : "memory");
}
template <> __device__ __forceinline__ void gw_load<2>(u32x32& w, unsigned a0, unsigned a1) {
  asm volatile("ds_read_b64_tr_b16 v[192:193], %1 offset:0\n\t"
    "ds_read_b64_tr_b16 v[194:195], %1 offset:512\n\t"
    "ds_read_b64_tr_b16 v[196:197], %2 offset:0\n\t"
    "ds_read_b64_tr_b16 v[198:199], %2 offset:512\n\t"
    "ds_read_b64_tr_b16 v[200:201], %1 offset:4096\n\t"
    "ds_read_b64_tr_b16 v[202:203], %1 offset:4608\n\t"
    "ds_read_b64_tr_b16 v[204:205], %2 offset:4096\n\t"
    "ds_read_b64_tr_b16 v[206:207], %2 offset:4608\n\t"
    "s_waitcnt lgkmcnt(0)\n\t"
    "ds_read_b64_tr_b16 v[208:209], %1 offset:16384\n\t"
    "ds_read_b64_tr_b16 v[210:211], %1 offset:16896\n\t"
    "ds_read_b64_tr_b16 v[212:213], %2 offset:16384\n\t"
    "ds_read_b64_tr_b16 v[214:215], %2 offset:16896\n\t"
    "ds_read_b64_tr_b16 v[216:217], %1 offset:20480\n\t"
    "ds_read_b64_tr_b16 v[218:219], %1 offset:20992\n\t"
    "ds_read_b64_tr_b16 v[220:221], %2 offset:20480\n\t"
    "ds_read_b64_tr_b16 v[222:223], %2 offset:20992\n\t"
    "s_waitcnt lgkmcnt(0)"
    : "=&{v[192:223]}"(w) : "v"(a0), "v"(a1) : "memory");
}
template <> __device__ __forceinline__ void gw_load<3>(u32x32& w, unsigned a0, unsigned a1) {
  asm volatile("ds_read_b64_tr_b16 v[224:225], %1 offset:0\n\t"
    "ds_read_b64_tr_b16 v[226:227], %1 offset:512\n\t"
    "ds_read_b64_tr_b16 v[228:229], %2 offset:0\n\t"
    "ds_read_b64_tr_b16 v[230:231], %2 offset:512\n\t"
    "ds_read_b64_tr_b16 v[232:233], %1 offset:4096\n\t"
    "ds_read_b64_tr_b16 v[234:235], %1 offset:4608\n\t"
    "ds_read_b64_tr_b16 v[236:237], %2 offset:4096\n\t"
    "ds_read_b64_tr_b16 v[238:239], %2 offset:4608\n\t"
    "s_waitcnt lgkmcnt(0)\n\t"
    "ds_read_b64_tr_b16 v[240:241], %1 offset:16384\n\t"
    "ds_read_b64_tr_b16 v[242:243], %1 offset:16896\n\t"
    "ds_read_b64_tr_b16 v[244:245], %2 offset:16384\n\t"
    "ds_read_b64_tr_b16 v[246:247], %2 offset:16896\n\t"
    "ds_read_b64_tr_b16 v[248:249], %1 offset:20480\n\t"
    "ds_read_b64_tr_b16 v[250:251], %1 offset:20992\n\t"
    "ds_read_b64_tr_b16 v[252:253], %2 offset:20480\n\t"
    "ds_read_b64_tr_b16 v[254:255], %2 offset:20992\n\t"
    "s_waitcnt lgkmcnt(0)"
    : "=&{v[224:255]}"(w) : "v"(a0), "v"(a1) : "memory");
}
template <int I, int E, class F> __device__ __forceinline__ void sfor(F&& f) { if constexpr (I < E) { f(std::integral_constant<int, I>{}); sfor<I + 1, E>(f); } }
template <int ROLE, int KQ, class F>
__device__ __forceinline__ void wload(char* smem, int wave_id_, const u16* W0, const u16* W1, int ldb, u32x32 (&bw)[4], int rot, F&& hook) {
  constexpr int ABUF = 65536;
  const int tid = opaque_tid(), lane = tid & 63, wave = WAVE_ID;
  const int kq = KQ == 2 ? wave >> 2 : 0, nq = KQ == 2 ? wave & 3 : wave;
  const int g = lane >> 4, r16 = lane & 15, q4 = r16 >> 2, p4 = lane & 3;
  const unsigned ldsb = __builtin_amdgcn_readfirstlane((unsigned)(size_t)(LAS char*)smem);
  {
    const int srow = 8 * wave + (lane >> 3), ch = (lane & 7) ^ bswz(srow);
    const unsigned voff = (unsigned)(srow * ldb + ch * 8) * 2;
    const char* w0 = uni((const char*)W0); const char* w1 = uni((const char*)(ROLE == R_F1 ? W1 : W0 + 64));
    const size_t kb = (size_t)64 * ldb * 2;
    auto wstage = [&](int r) {
      const unsigned ld = ldsb + (r & 1) * ABUF + wave * 1024;
#pragma unroll
      for (int i = 0; i < 8; ++i) {
        const int kbi = KQ == 2 ? (ROLE == R_PART2 ? 0 : (i >> 2) * 8) + ((2 * r + ((i >> 1) & 1)) ^ rot) : 2 * r + (i >> 2);
        const char* src = KQ == 2 ? ((i & 1) ? w1 : w0) + kbi * kb + (ROLE == R_PART2 ? (i >> 2) * 256 : 0) : w0 + kbi * kb + (i & 3) * 128;
        unsigned keep;
        asm volatile("s_mov_b32 %0, m0\n\ts_mov_b32 m0, %3\n\ts_nop 2\n\tglobal_load_lds_dwordx4 %1, %2" WNT_ "\n\ts_mov_b32 m0, %0"
                     : "=&s"(keep) : "v"(voff), "s"(src), "s"(ld + i * 8192) : "memory");
      }
    };
    const int brow = 8 * g + q4, bs = bswz(brow);
    unsigned la[2];
#pragma unroll
    for (int j = 0; j < 2; ++j) {
      int blk, c16, sub;
      if (ROLE == R_F1) { blk = j; c16 = nq * 2 + (p4 >> 1); sub = (p4 & 1) * 8; }
      else { blk = nq >> 1; c16 = (nq & 1) * 4 + p4; sub = j * 8; }
      int set = kq;
      if (ROLE == R_PART2 && TLP) { set = nq >> 1; blk = nq & 1; c16 = kq * 4 + p4; }
      la[j] = (unsigned)((KQ == 2 ? set * 4 + blk : blk) * 8192 + brow * 128 + ((c16 ^ bs) << 4) + sub);
    }
    hook(4);
    wstage(0); wstage(1);
    sfor<0, 4>([&](auto r_c) __attribute__((always_inline)) {
      constexpr int r = decltype(r_c)::value;
      if (r < 3) WAIT_V(8); else WAIT_V(0);
      __builtin_amdgcn_s_barrier(); asm volatile("" ::: "memory");
      gw_load<r>(bw[r], ldsb + (r & 1) * ABUF + la[0], ldsb + (r & 1) * ABUF + la[1]);
      BAR();
      if (r + 2 < 4) wstage(r + 2);
      hook(r);
    });
  }
}
#define DSR2(d0, d1, a0_, a1_, off) asm volatile("ds_read_b128 %0, %2 offset:%4\n\tds_read_b128 %1, %3 offset:%4" : "=&v"(d0), "=&v"(d1) : "v"(a0_), "v"(a1_), "i"(off))
template <int N> __device__ __forceinline__ void waitl(u32x4& a, u32x4& b) { asm volatile("s_waitcnt lgkmcnt(%2)" : "+v"(a), "+v"(b) : "i"(N)); }
__device__ __forceinline__ void glds1(const char* sbase, unsigned voff, unsigned lds) {
  unsigned keep;
  asm volatile("s_mov_b32 %0, m0\n\ts_mov_b32 m0, %3\n\ts_nop 2\n\tglobal_load_lds_dwordx4 %1, %2 sc1\n\ts_mov_b32 m0, %0"
               : "=&s"(keep) : "v"(voff), "s"(sbase), "s"(lds) : "memory");
}
#define XS(i) do { } while (0)
#define XFL(b_) do { } while (0)
#define XA_ 0
#define XF_ 0
#define XE_ 0
#define XP_ 0
#define PUBK 9
#define LATE 0
#define NOWP 0
#define TB 48
#define TDM 1
#define TDA 15
#define TPUB 1
#define DUR0 160
#define PUP 4
#define PDQ 5
#define PDM 2
#define PDEEP 5
template <int ROLE>
__device__ __forceinline__ void gemm7(char* smem, int wave_id_, const u16* W0, const u16* W1, int ldb, const char* A_, size_t a_blk, Dep dep,
                                      char* C_, size_t c_blk, unsigned*& cnt, int myw, unsigned* tmo, int slot, unsigned long long* mts,
                                      bool gl, int nimm = 0, int nseg = NSEG, int wm0 = 0, int pofs = 0, u64 tag = 0, int b0 = 0) {
  dep.base = (const char*)cnt;
  unsigned* myprog = cnt + myw;
  volatile LAS unsigned* wsl = (volatile LAS unsigned*)(smem + L_SLOT + 512);
  if (opaque_tid() < 144) *(volatile LAS unsigned*)(smem + L_SLOT + opaque_tid() * 4) = 0u;
  const int tid = opaque_tid(), lane = tid & 63, wave = WAVE_ID;
  const int kq = wave >> 2, nq = wave & 3;
  const unsigned ldsb = __builtin_amdgcn_readfirstlane((unsigned)(size_t)(LAS char*)smem);
  constexpr int rot = 0;
  const char* A = uni(A_) + nq * 1024;
  const char* Cu = uni(C_);
  constexpr int PD = PDM;
  constexpr bool P2C = ROLE == R_PART2;
  int pw = 0;
  int wm = wm0, nh = 2 * b0;
  constexpr bool SPEC = ROLE != R_QKV;
  const int sbi = gl ? SC : SB, sbo = ROLE == R_F1 || gl ? SC : SB;
  auto lgo = [&](int b, unsigned& v, unsigned& u, bool pre, bool force) {
    if (wave == 4) {
      for (int spins = 0; __builtin_amdgcn_readfirstlane(wsl[2]) != (unsigned)(b + 1); ++spins) {
        if (!pre) ld_fb(v, u, dep.base, dep.voff);
        const bool ok = (int)(v * dep.mul + dep.add) > b;
        if (__all(ok)) break;
        if (SPEC && !force && bells(ok, u, b, b0 ? MARH : MARG, sbi)) break;
        if (!pre) __builtin_amdgcn_s_sleep(2);
        pre = false;
        if (give_up(spins, tmo, 9)) { v = 255u; break; }
      }
      wsl[2] = (unsigned)(b + 1);
    } else {
      while (__builtin_amdgcn_readfirstlane(wsl[2]) != (unsigned)(b + 1)) __builtin_amdgcn_s_sleep(1);
    }
  };
  auto lwm = [&](int b, unsigned v) {
    unsigned w;
    if (wave == 4) {
      const int w2 = (int)dep_eval(v, dep);
      w = (unsigned)min(max(max(w2, wm), b + 1), 255);
      wsl[0] = ((unsigned)(b + 1) << 8) | w;
    } else {
      for (;;) {
        w = __builtin_amdgcn_readfirstlane(wsl[0]);
        if ((w >> 8) == (unsigned)(b + 1)) break;
        __builtin_amdgcn_s_sleep(1);
      }
      w &= 255u;
    }
    return (int)w;
  };
  auto pubw = [&](int n) {
    WAIT_V(0);
    if (lane == 0) {
      const unsigned o = __hip_atomic_fetch_add((LAS unsigned*)(smem + L_SLOT + 516 + ((n + pofs) & 1) * 36), 1u, __ATOMIC_RELAXED, __HIP_MEMORY_SCOPE_WORKGROUP);
      if ((o & 3u) == 3u) {
        st4_sc1(myprog, (unsigned)(n + pofs));
      }
    }
    asm volatile("s_waitcnt lgkmcnt(0)" ::: "memory");
  };
  auto hsrc = [&](int hh) { return A + (size_t)(hh >> 1) * a_blk + ((hh ^ (rot >> 2)) & 1) * 16384; };
  auto hlds = [&](int hh) { return ldsb + (unsigned)(((hh >> 1) & 1) * 65536 + (hh & 1) * 16384 + nq * 1024); };
#define POFF(i_) (((i_) & 1) * 32768 + ((i_) >> 1) * 4096)
#define PIECE(s_, l_, i_) asm volatile("s_add_u32 m0, %2, %3\n\ts_nop 0\n\tglobal_load_lds_dwordx4 %0, %1 sc1" :: "v"(av[i_]), "s"(s_), "s"(l_), "i"(POFF(i_)) : "memory", "scc", "m0")
  int na = max(sbo, b0);
  auto arm = [&](int bb) {
    unsigned z = SENT; asm volatile("" : "+v"(z));
    constexpr int N = ROLE == R_F1 ? 1 : (P2C ? 4 : 2);
    const unsigned o = nq * N * 1024 + lane_id() * 16;
    const char* p = Cu + (size_t)bb * c_blk;
#pragma unroll
    for (int i = 0; i < N; ++i) st16s(p, o + i * 1024, u32x4{z, z, z, z});
  };
  u32x32 BW[4];
  wload<ROLE, 2>(smem, wave_id_, W0, W1, ldb, BW, rot, [&](int r) {
    if (ROLE == R_QKV && nimm > 0 && wave == 0) STS((char*)tmo - OFF_TMO, 128 + slot, 3 + r);
    if (SPEC && r == 2 && (ROLE != R_PART || gl)) { for (na += kq; na < nseg; na += 2) arm(na); na = nseg; }
    if (ROLE == R_QKV && nimm > 0 && r == 0 && wave == 4) init_issue((char*)tmo - OFF_TMO, ldsb + L_RED);
    if (ROLE == R_QKV && nimm > 0 && r == 2) {
      cnt = cnt_of((char*)tmo - OFF_TMO, init_get((char*)tmo - OFF_TMO, tag, smem + L_RED));
      dep.base = (const char*)cnt; myprog = cnt + myw;
      if (kq) {
        unsigned v0 = 0, u0;
        lgo(0, v0, u0, false, true);
        wm = lwm(0, v0);
        const unsigned l16 = lane_id() * 16;
#pragma unroll
        for (int i = 0; i < 16; ++i) glds1(hsrc(i >> 3) + (i & 1) * 32768 + ((((i & 7) >> 1) ^ rot) & 3) * 4096, l16, hlds(i >> 3) + POFF(i & 7));
      }
      nh = 2;
    }
  });
  if (ROLE == R_QKV && nimm > 0 && !kq) wm = (int)(__builtin_amdgcn_readfirstlane(wsl[0]) & 255u);
  if (ROLE == R_QKV && nimm > 0 && opaque_tid() == 0) STS((char*)tmo - OFF_TMO, 128 + slot, 0);
  if (wave == 0) WFB();
  auto body = [&](auto ldr_c) __attribute__((always_inline)) {
    constexpr bool LDR = decltype(ldr_c)::value;
    if (XP_ == 0 ? !LDR : (XP_ == 1 ? LDR : false)) __builtin_amdgcn_s_setprio(1);
    unsigned av[8];
#pragma unroll
    for (int i = 0; i < 8; ++i) av[i] = lane * 16 + (P2C ? 0 : (i & 1) * 32768) + (((i >> 1) ^ rot) & 3) * 4096;
    auto issue_all = [&](int hh) {
      if (LDR) {
        const char* s_ = hsrc(hh); const unsigned l_ = hlds(hh);
#pragma unroll
        for (int i = 0; i < 8; ++i) if (!(P2C && (i & 1))) PIECE(s_, l_, i);
      }
    };
    int b = b0;
    bool pre = false;
    unsigned smu = 0, tk = 0;
    unsigned smp = 0, got = 0;
    while (b < nseg) {
      const bool fast = (wm < nseg ? wm : nseg) >= b + PD && b >= nimm && nh <= 2 * b + 2;
      if (SPEC && b >= TB && !fast) break;
      const bool pl = wm <= b || pre, cold = nh < 2 * b + 2, nh3 = nh == 2 * b + 3;
      if (!fast) {
        XS(0);
        if (LDR && wm <= b && !got) lgo(b, smp, smu, false, true);
        XS(1);
        if (got) nh += NEARLY / 8;
        { const int upto = nh3 ? 2 * b + 4 : 2 * b + 2; for (; nh < upto; ++nh) issue_all(nh); }
        if (LDR && pl) wm = lwm(b, smp);
        XS(2);
      }
      const int ln = lane_id(), g = ln >> 4, r16 = ln & 15;
      const unsigned e0 = ldsb + (unsigned)((P2C ? 0 : kq * 32768) + (b & 1) * 65536 + (gl ? (g >> 1) * 512 + r16 * 32 + (g & 1) * 16 : r16 * 128 + ((g ^ (r16 & 7)) << 4)));
      const unsigned d0 = e0 ^ (gl ? 1024u : 64u);
      const unsigned rw = ldsb + L_RED + nq * 4096 + ln * 16;
      const unsigned co0 = ROLE == R_F1 ? (unsigned)((g & 1) * 2048 + nq * 512 + r16 * 32 + (g >> 1) * 16)
                         : ((ROLE == R_QKV ? TLQ : TLP) ? (unsigned)(nq * (P2C ? 4096 : 2048) + r16 * 64 + g * 16)
                         : (ROLE == R_QKV ? (unsigned)(r16 * 256 + (((4 * nq + g) ^ r16) << 4)) : (unsigned)(r16 * PROW + (32 * nq + 8 * g) * 2)));
      const unsigned co1 = co0 + ((ROLE == R_QKV ? TLQ : TLP) ? 1024u : (ROLE == R_QKV ? 4096u : 16u * PROW));
      u32x16 pv_;
      bool pr2 = false;

      if (fast) {
        __builtin_amdgcn_s_barrier(); asm volatile("" ::: "memory");
        const int LA = (wm < nseg ? wm : nseg) >= b + (ROLE == R_QKV ? PDQ : PDEEP) ? 3 : 2;
        for (; nh < 2 * b + LA; ++nh) issue_all(nh);
        int lf = (wm < nseg ? wm : nseg) - (LA - 1), cp = nseg - (LA - 1);
        const unsigned sa = ldsb + L_SLOT + ln * 4, ad = dep.add - (unsigned)(LA - 1);
        const unsigned up = LA == 3 ? 1000u : (unsigned)PUP;
#define FARGS BW, pv_, e0, d0, b, lf, cp, up, sa, dep.mul, ad, rw, (unsigned)a_blk, hsrc(2 * b + LA), hsrc(2 * b + LA + 1), hlds(2 * b + LA), hlds(2 * b + LA + 1), av
        if constexpr (LDR) (P2C ? fast_ld2 : (ROLE == R_F1 ? fast_ld1 : fast_ld0))(FARGS, LA);
#undef FARGS
        else {
          const char* c0_ = uni((const char*)tmo - OFF_TMO + OFF_DUMP); const char* c1_ = Cu + (size_t)b * c_blk;
          const int sf = b + pofs;
          const char* pg = uni((const char*)myprog);
          (ROLE == R_F1 ? fast_st1 : (P2C ? fast_st2 : fast_st0))(BW, pv_, e0, d0, b, lf, cp, up, sa, dep.mul, ad, rw, (unsigned)c_blk, c0_, c1_, co0, co1, pofs, sf, (unsigned)wave,
                                                                   ldsb + L_SLOT, dep.voff, dep.base, pg);
        }
        wm = lf + LA - 1 > wm ? lf + LA - 1 : wm;
        nh = 2 * b + 2;
        --b;
        __builtin_amdgcn_s_barrier(); asm volatile("" ::: "memory");
      } else {
        if (LDR) { if (nh >= 2 * b + 4) { if (P2C) WAIT_V(14); else WAIT_V(28); } else { if (P2C) WAIT_V(6); else WAIT_V(12); } }
        BAR();
        XS(3);
        if (ROLE == R_QKV && nimm > 0 && b < 2 && opaque_tid() == 0) STS((char*)tmo - OFF_TMO, 128 + slot, 1 + b);
        if constexpr (!LDR) {
          const unsigned wv = wsl[0];
          unsigned st;
          lean_st0(BW, e0, d0, __builtin_amdgcn_readfirstlane(pw > 0 ? (cold ? 4u : 1u) : 0u), pv_, (unsigned)(pw + pofs), ldsb + L_SLOT + 516, uni((const char*)myprog), 0u, tk, st);
          if (pl) wm = (int)(__builtin_amdgcn_readfirstlane(wv) & 255u);
        }
        const bool dm = nh == 2 * b + 2 && wm > b + 1 && b + 1 < nseg;
        pr2 = b + 1 < nseg && wm < nseg;
        if constexpr (LDR) {
          const bool er = nh == 2 * b + 2 && !dm;
          const unsigned ps = pr2 && (wave == 4 || er) ? (unsigned)(b + 1) : 0u;
#define LARGS BW, e0, d0, (unsigned)(nh - 2 * b - 2), hlds(nh), hsrc(nh), hsrc(nh + 1), av, ps, dep.voff, dep.base, \
              rw, ldsb + L_SLOT + 528 + nq * 4, (unsigned)(b + 1), ldsb + L_SLOT + 520, dep.mul, dep.add, er ? b + 1 : 0x7FFFFFFF, 0x7FFFFFFF, smp, smu, got
          if (dm) (P2C ? lean_dm1 : lean_dm0)(LARGS); else (P2C ? lean_ld1 : lean_ld0)(LARGS);
#undef LARGS
          got = __builtin_amdgcn_readfirstlane(got);
        }
        if (dm) nh += 2;
        XS(5);
      }
      unsigned bad = 0, pk = 0;
      if constexpr (LDR) { if (fast) fin_ld(pv_, rw, ldsb + L_SLOT + 528 + nq * 4, (unsigned)(b + 1)); }
      else (ROLE == R_F1 ? fin_st1 : (P2C ? fin_st2 : fin_st0))(pv_, rw, ldsb + L_SLOT + 528 + nq * 4, (unsigned)(b + 1), Cu + (size_t)b * c_blk, co0, co1, ldsb + L_SLOT + 524, bad, pk);
      XS(6);
      pre = pr2;
      bool now = (nh < 2 * b + 4 && wm < b + 1 + PD) || b + 1 >= nseg || b < nimm;
      if (!LDR && now) {
        if (NOWP == 0 && b >= nimm && pk == (unsigned)(b + 2)) now = false;
        else pubw(b + 1);
      }
      XS(7);
      XFL(b);
      pw = now ? 0 : b + 1;
      if (SPEC && !LDR && !fast && b >= 16 && na < nseg && na > b + 1) { arm(na); arm(na + 1); na += 2; }
      ++b;
    }
    if (SPEC && b < nseg) {
      static_assert(!SPEC || (NEARLY == 16 && TLP), "");
      const int ln = lane_id(), g = ln >> 4, r16 = ln & 15;
      const unsigned e0 = ldsb + (unsigned)((P2C ? 0 : kq * 32768) + (gl ? (g >> 1) * 512 + r16 * 32 + (g & 1) * 16 : r16 * 128 + ((g ^ (r16 & 7)) << 4)));
      const unsigned rw = ldsb + L_RED + nq * 4096 + ln * 16;
      const unsigned co0 = ROLE == R_F1 ? (unsigned)((g & 1) * 2048 + nq * 512 + r16 * 32 + (g >> 1) * 16) : (unsigned)(nq * (P2C ? 4096 : 2048) + r16 * 64 + g * 16);
      const char* pg = uni((const char*)myprog);
      bool force = false, dmx = false;
      unsigned dur = DUR0, dp = 0, x8 = __builtin_amdgcn_readfirstlane(wave == 0 ? 8u : 0u);
      if (nh >= 2 * b + 2) got = 1;
      pre = false;
      for (;;) {
        unsigned rd;
        XS(0);
        if (LDR && b < nseg) {
          if (!got) { lgo(b, smp, smu, pre, force); issue_all(2 * b); issue_all(2 * b + 1); }
          XS(2);
          if (P2C) WAIT_V(6); else WAIT_V(12);
        }
        if (!LDR && b >= sbo && na <= nseg) { for (na = max(na, b); na < nseg; ++na) arm(na); WAIT_V(0); ++na; }
        const unsigned ea = e0 + (b & 1) * 65536;
        BAR();
        XS(3);
        if (b >= nseg) rd = __builtin_amdgcn_readfirstlane(wsl[8]);
        else if constexpr (LDR) {
          const int th = b + 1 < nseg ? b + 1 : 0;
#define LARGS BW, ea, ea ^ (gl ? 1024u : 64u), 0u, hlds(2 * b + 2), hsrc(2 * b + 2), hsrc(2 * b + 3), av, (unsigned)th, dep.voff, dep.base, \
              rw, ldsb + L_SLOT + 528 + nq * 4, (unsigned)(b + 1), ldsb + L_SLOT + 520, dep.mul, dep.add, th, th >= sbi ? th : 0x7FFFFFFF, smp, smu, got
          if (dmx) {
            *(volatile LAS u32x2*)(wsl + 2) = u32x2{(unsigned)(b + 2), (unsigned)(b + 2)};
            (P2C ? lean_dm1 : lean_dm0)(LARGS);
          } else (P2C ? lean_ld1 : lean_ld0)(LARGS);
#undef LARGS
          got = __builtin_amdgcn_readfirstlane(got) | (dmx ? 1u : 0u);
          rd = got >> 1;
          dmx = TDM && got == 1u && b + 2 < nseg
             && __all((int)(smp * dep.mul + dep.add) > b + 2 || (b + 2 >= sbi && (int)(smu >> 24) + ((int)((rt_now() + (dmx ? TDA : 0) - MARG - smu) << 8) >> 31) > b + 2));
          pre = true;
        } else {
          u32x16 pv_;
          lean_st0(BW, ea, ea ^ (gl ? 1024u : 64u), b >= sbo - 2 ? (unsigned)min(pw, 1) | x8 : (unsigned)min(pw, 1) * TPUB, pv_, (unsigned)pw, ldsb + L_SLOT + 516, pg, (unsigned)(b + 1) << 24 | dur, tk, rd);
          rd = __builtin_amdgcn_readfirstlane(rd);
          XS(5);
          if (!rd) {
            unsigned bad, pk;
            (ROLE == R_F1 ? fin_st1 : (P2C ? fin_st2 : fin_st0))(pv_, rw, ldsb + L_SLOT + 528 + nq * 4, (unsigned)(b + 1), Cu + (size_t)b * c_blk, co0, co0 + 1024u,
                                                                 ldsb + L_SLOT + 524, bad, pk);
            XS(6);
            pw = b + 1;
            if (x8 && b >= sbo - 2) { const unsigned d = min(rt_now() - __builtin_amdgcn_readfirstlane(tk), 300u); dur = max(d, dp); dp = d; }
            if (bad && !force && b >= sbi) wsl[8] = 1u;
            else if (b == sbo - 1 || (pk != (unsigned)(b + 2) && __builtin_amdgcn_readfirstlane(wsl[2]) != (unsigned)(b + 2))) {
              pubw(b + 1);
              pw = 0;
            }
            if (na < nseg && na > b + 1) { arm(na); arm(na + 1); na += 2; }
          }
        }
        XS(7);
        if (rd) {
          WAIT_V(0);
          BAR();
          if (wave == 4 && ln < 11) wsl[ln] = 0u;
          BAR();
          --b; pw = 0; pre = false; got = 0; force = true;
          continue;
        }
        if (b >= nseg) break;
        force = false;
        ++b;
      }
    }
    __builtin_amdgcn_s_setprio(0);
  };
  if (kq) body(std::true_type{}); else body(std::false_type{});
#undef PIECE
#undef POFF
  WAIT_V(0);
  BAR();
}
__device__ __forceinline__ void fin_role(char* smem, int wave_id_, int fidx, const float* mod, const char* P2, const u16* x1, u16* out, unsigned* cnt, unsigned* tmo,
                                         unsigned long long* mts) {
  const int tid = opaque_tid(), lane = tid & 63, wave = WAVE_ID;
  row_table(smem + L_GB, mod, cnt, tmo, tid);
  volatile LAS unsigned* wsl = (volatile LAS unsigned*)(smem + L_SLOT + 512);
  int wf = 0, par = 0;
  const unsigned pf2 = (unsigned)(PF2 + ((lane & 31) < 28 ? (lane & 31) : 0)) * 128, pln = (unsigned)((PLN + (lane >> 3)) * 128 + (lane & 7) * 4);
  for (int nf = fidx >> 3; nf < NSEG; nf += 2) {
    if (wf <= nf) {
      if (wave == 0) {
        unsigned w;
        for (int spins = 0;; ++spins) {
          unsigned v, u;
          ld_fb(v, u, cnt, pf2 + (nf >= HB ? 4 : 0));
          if (!__all(v > (unsigned)nf) && bells(v > (unsigned)nf, u, nf, nf >= HB ? MARL : MARF, SC)) { w = 0; break; }
          v = rowmin16(v);
          w = min(rdl(v, 0), rdl(v, 16));
          if ((int)w > nf) break;
          __builtin_amdgcn_s_sleep(2);
          if (give_up(spins, tmo, 9)) { w = NSEG; break; }
        }
        if (lane == 0) wsl[par] = w;
      }
      BAR();
      wf = (int)__builtin_amdgcn_readfirstlane(wsl[par]);
      par ^= 1;
    }
    int ln = lane; asm volatile("" : "+v"(ln));
    if (wave == 0) WTS(256 + 8 + fidx, nf / 2, 0);
    bool fin = nf < SB;
    for (int spins = 0; !fin_half(fin, P2, x1, smem + L_GB + 12288, out, nf * 32 + 4 * (fidx & 7) + (wave >> 1), wave & 1, ln); ++spins)
      fin = __all(ld4_sc1((const char*)cnt + pf2 + (nf >= HB ? 4 : 0)) > (unsigned)nf && ld4_sc1((const char*)cnt + pln) > (unsigned)nf) || give_up(spins, tmo, 9);
    if (wave == 0) WTS(256 + 8 + fidx, nf / 2, 2);
  }
}
#define LD8(r, vo, sb) asm volatile("global_load_dwordx4 %0, %8, %12 sc1\n\tglobal_load_dwordx4 %1, %9, %12 sc1\n\t" \
    "global_load_dwordx4 %2, %10, %12 sc1\n\tglobal_load_dwordx4 %3, %11, %12 sc1\n\t" \
    "global_load_dwordx4 %4, %8, %13 sc1\n\tglobal_load_dwordx4 %5, %9, %13 sc1\n\t" \
    "global_load_dwordx4 %6, %10, %13 sc1\n\tglobal_load_dwordx4 %7, %11, %13 sc1" \
    : "=&v"(r[0][0]), "=&v"(r[0][1]), "=&v"(r[0][2]), "=&v"(r[0][3]), "=&v"(r[1][0]), "=&v"(r[1][1]), "=&v"(r[1][2]), "=&v"(r[1][3]) \
    : "v"(vo), "v"(vo ^ (TLQ ? 2048u : 64u)), "v"(vo ^ (TLQ ? 4096u : 128u)), "v"(vo ^ (TLQ ? 6144u : 192u)), "s"(sb), "s"((sb) + (TLQ ? 1024 : 4096)) : "memory")
#define TIE8(r) "+v"(r[0][0]), "+v"(r[0][1]), "+v"(r[0][2]), "+v"(r[0][3]), "+v"(r[1][0]), "+v"(r[1][1]), "+v"(r[1][2]), "+v"(r[1][3])
#define SADL 255
#define SAE 110
#define DSA 20
#define SASL 8
__device__ __forceinline__ void sa_role(char* smem, int wave_id_, int hbase, const char* __restrict__ Q, const char* __restrict__ Kx, const char* __restrict__ V,
                                        const char* kvm, char* __restrict__ O, unsigned* cnt, unsigned* tmo, int slot, unsigned long long* mts) {
  constexpr float LS = LOG2E * SCALE;
  const int tid = opaque_tid(), wave = WAVE_ID;
  const int h = hbase + (wave & 1), s0 = wave >> 1;
  char* vl = smem + wave * 17408;
  const unsigned ldsw = __builtin_amdgcn_readfirstlane((unsigned)(size_t)(LAS char*)vl);
  unsigned avail = 0;
  {
    static_assert(SB % 4 == 0, "");
    unsigned z = SENT; asm volatile("" : "+v"(z));
    for (int s = SB + s0; s < NSEG; s += 4)
#pragma unroll
      for (int i = 0; i < 8; ++i) st16s(uni(O + ((size_t)s * 64 + 2 * h) * 4096), (unsigned)(i * 1024 + (tid & 63) * 16), u32x4{z, z, z, z});
  }
  for (int s = s0, it = 1; s < NSEG; s += 4, ++it) {
    u32x4 qv[2][4], kv[2][4];
    int lane = tid & 63; asm volatile("" : "+v"(lane));
    const int g = lane >> 4, r16 = lane & 15, q4 = r16 >> 2, p4 = lane & 3;
    auto request = [&](int sr) {
      const size_t o = ((size_t)sr * NH + h) * 8192;
      const char* qs = uni(Q + o); const char* ks = uni(Kx + o); const char* vs = uni(V + o);
      const unsigned fo = TLQ ? (unsigned)(r16 * 64 + g * 16) : (unsigned)(r16 * 256 + ((g ^ r16) << 4));
      LD8(qv, fo, qs); LD8(kv, fo, ks);
      head_to_lds(vs, lane, ldsw);
    };
    {
      for (int spins = 0; avail <= (unsigned)s; ++spins) {
        const unsigned p = ld4_sc1(cnt + (PQKV + h) * 32 + (lane & 15));
        avail = min(min(qkv_avail(p, 0), qkv_avail(p, 1)), qkv_avail(p, 2));
        if (avail > (unsigned)s) break;
        __builtin_amdgcn_s_sleep(32);
        if (give_up(spins, tmo, 10)) break;
      }
      request(s);
      asm volatile("s_waitcnt vmcnt(0)" : TIE8(qv), TIE8(kv) :: "memory");
    }
    WTM(slot, s, 0);
    f32x4 s00 = f32x4{0, 0, 0, 0}, s01 = s00, s11 = s00;
#pragma unroll
    for (int ks = 0; ks < 4; ++ks) {
      s00 = MFMA32(asbf8(kv[0][ks]), asbf8(qv[0][ks]), s00);
      s01 = MFMA32(asbf8(kv[0][ks]), asbf8(qv[1][ks]), s01);
      s11 = MFMA32(asbf8(kv[1][ks]), asbf8(qv[1][ks]), s11);
    }
    float mx[2], sum[2];
    u32x4 pseg[2];
    {
#pragma unroll
      for (int r = 0; r < 4; ++r) { const bool ok = 4 * g + r <= r16; s00[r] = ok ? s00[r] : -3.0e38f; s11[r] = ok ? s11[r] : -3.0e38f; }
      mx[0] = red16_32_max(max3(max3(s00[0], s00[1], s00[2]), s00[3], -1.0e38f));
      mx[1] = red16_32_max(max3(max3(fmaxf(fmaxf(s01[0], s01[1]), fmaxf(s01[2], s01[3])), s11[0], -1.0e38f), max3(s11[1], s11[2], s11[3]), -1.0e38f));
      const float n0 = -mx[0] * LS, n1 = -mx[1] * LS;
#pragma unroll
      for (int r = 0; r < 4; ++r) {
        s00[r] = __builtin_amdgcn_exp2f(s00[r] * LS + n0); s01[r] = __builtin_amdgcn_exp2f(s01[r] * LS + n1); s11[r] = __builtin_amdgcn_exp2f(s11[r] * LS + n1);
      }
      sum[0] = (s00[0] + s00[1]) + (s00[2] + s00[3]);
      sum[1] = ((s01[0] + s01[1]) + (s01[2] + s01[3])) + ((s11[0] + s11[1]) + (s11[2] + s11[3]));
      pseg[0] = u32x4{pack2(s00[0], s00[1]), pack2(s00[2], s00[3]), 0u, 0u};
      pseg[1] = u32x4{pack2(s01[0], s01[1]), pack2(s01[2], s01[3]), pack2(s11[0], s11[1]), pack2(s11[2], s11[3])};
    }
    f32x4 o[8][2];
#pragma unroll
    for (int dt = 0; dt < 8; ++dt) {
      const int row0 = 4 * g + q4;
      const unsigned xo = (unsigned)(row0 * 256 + (((4 * (dt >> 1) + p4) ^ row0) << 4) + (dt & 1) * 8);
      const u32x2 lo = trread(vl, xo), hi = trread(vl, xo + 4096);
      o[dt][0] = MFMA32(asbf8(u32x4{lo.x, lo.y, 0u, 0u}), asbf8(pseg[0]), (f32x4{0, 0, 0, 0}));
      o[dt][1] = MFMA32(asbf8(u32x4{lo.x, lo.y, hi.x, hi.y}), asbf8(pseg[1]), (f32x4{0, 0, 0, 0}));
    }
    float n0[2]; f32x4 sm[2];
#pragma unroll
    for (int b = 0; b < 2; ++b) { const float t = red16_32_add(sum[b]); sm[b] = f32x4{t, t, t, t}; n0[b] = -mx[b] * LS; }
    unsigned oa = (unsigned)(r16 * 128 + ((g ^ (r16 & 7)) << 4)), ka = (unsigned)(g * 256 + (r16 & 7) * 16), va = (unsigned)lane * 16;
    unsigned one = g < 2 ? 0x3F803F80u : 0u;
    u32x4 kmv[4], vmv[4];
#pragma unroll
    for (int i = 0; i < 4; ++i) vmv[i] = u32x4{0u, 0u, 0u, 0u};
#pragma unroll
    for (int dt = 0; dt < 8; ++dt) asm volatile("" : "+v"(o[dt][0]), "+v"(o[dt][1]), "+v"(vmv[dt >> 1]));
    asm volatile("s_waitcnt lgkmcnt(0)" : "+v"(sm[0]), "+v"(sm[1]), "+v"(n0[0]), "+v"(n0[1]), "+v"(oa), "+v"(ka), "+v"(va), "+v"(one) :: "memory");
    unsigned pg;
    for (int spins = 0;; ++spins) {
      pg = ld4_sc1(cnt + (PROG + h) * 32 + ((lane & 4) ? 5 : (lane & 3)));
      if (__all((pg >> 24) + (s >= SDIR ? 1u : 0u) > (unsigned)s)) break;
      __builtin_amdgcn_s_sleep(SASL);
      if (give_up(spins, tmo, 6)) break;
    }
    if (__all((pg >> 24) == (unsigned)s)) wait_rt(latest(pg), s == NSEG - 1 ? SADL - LAST : SADL);
    const char* mk = uni(kvm + ((size_t)s * NH + h) * 4096);
    for (int spins = 0;; ++spins) {
      asm volatile("global_load_dwordx4 %0, %8, %10 sc1\n\tglobal_load_dwordx4 %1, %8, %10 offset:128 sc1\n\t"
                   "global_load_dwordx4 %2, %8, %10 offset:1024 sc1\n\tglobal_load_dwordx4 %3, %8, %10 offset:1152 sc1\n\ts_mov_b32 exec_hi, 0\n\t"
                   "global_load_dwordx4 %4, %9, %10 offset:2048 sc1\n\tglobal_load_dwordx4 %5, %9, %10 offset:2560 sc1\n\t"
                   "global_load_dwordx4 %6, %9, %10 offset:3072 sc1\n\tglobal_load_dwordx4 %7, %9, %10 offset:3584 sc1\n\ts_mov_b32 exec_hi, -1\n\ts_waitcnt vmcnt(0)"
                   : "=&v"(kmv[0]), "=&v"(kmv[1]), "=&v"(kmv[2]), "=&v"(kmv[3]), "+v"(vmv[0]), "+v"(vmv[1]), "+v"(vmv[2]), "+v"(vmv[3])
                   : "v"(ka), "v"(va), "s"(mk) : "memory");
      unsigned mx_ = 0;
#pragma unroll
      for (int i = 0; i < 4; ++i) mx_ = max(max(max(max(mx_, kmv[i].x), kmv[i].y), max(kmv[i].z, kmv[i].w)), max(max(vmv[i].x, vmv[i].y), max(vmv[i].z, vmv[i].w)));
      if (__all(mx_ != SENT)) break;
      __builtin_amdgcn_s_sleep(2);
      if (give_up(spins, tmo, 6)) break;
    }
    if (SAE && s >= SB && lane == 0) st4_sc1(cnt + (PSA + (hbase >> 1)) * 32 + 16 + wave, bell(s, rt_now() + SAE));
    WTM(slot, s, 1);
    f32x4 tt[2] = {f32x4{0, 0, 0, 0}, f32x4{0, 0, 0, 0}};
#pragma unroll
    for (int ks = 0; ks < 4; ++ks) { tt[0] = MFMA32(asbf8(kmv[ks]), asbf8(qv[0][ks]), tt[0]); tt[1] = MFMA32(asbf8(kmv[ks]), asbf8(qv[1][ks]), tt[1]); }
#pragma unroll
    for (int b = 0; b < 2; ++b)
#pragma unroll
      for (int r = 0; r < 4; ++r) tt[b][r] = tt[b][r] * LS + n0[b];
    if (!__all(max3(max3(tt[0][0], tt[0][1], tt[0][2]), max3(tt[0][3], tt[1][0], tt[1][1]), max2(tt[1][2], tt[1][3])) <= 40.0f)) {
#pragma unroll
      for (int b = 0; b < 2; ++b) {
        float m = max3(max3(tt[b][0], tt[b][1], tt[b][2]), tt[b][3], 0.0f);
        { float x, y; sw16(m, x, y); m = max2(x, y); }
        const float f = __builtin_amdgcn_exp2f(-m);
        tt[b] -= m; sm[b] *= f;
#pragma unroll
        for (int dt = 0; dt < 8; ++dt) o[dt][b] *= f;
      }
    }
    u32x2 pm[2]; float inv[2];
#pragma unroll
    for (int b = 0; b < 2; ++b) {
      pm[b] = u32x2{pack2(__builtin_amdgcn_exp2f(tt[b][0]), __builtin_amdgcn_exp2f(tt[b][1])), pack2(__builtin_amdgcn_exp2f(tt[b][2]), __builtin_amdgcn_exp2f(tt[b][3]))};
      sm[b] = __builtin_amdgcn_mfma_f32_16x16x16bf16_1k(ass4(u32x2{one, one}), ass4(pm[b]), sm[b], 0, 0, 0);
    }
#pragma unroll
    for (int dt = 0; dt < 8; ++dt)
#pragma unroll
      for (int b = 0; b < 2; ++b) o[dt][b] = __builtin_amdgcn_mfma_f32_16x16x16bf16_1k(ass4((dt & 1) ? u32x2{vmv[dt >> 1].z, vmv[dt >> 1].w} : u32x2{vmv[dt >> 1].x, vmv[dt >> 1].y}), ass4(pm[b]), o[dt][b], 0, 0, 0);
    inv[0] = __builtin_amdgcn_rcpf(sm[0][0]); inv[1] = __builtin_amdgcn_rcpf(sm[1][0]);
    const char* od = uni(O + ((size_t)s * 64 + 2 * h) * 4096);
#pragma unroll
    for (int j = 0; j < 4; ++j) {
#pragma unroll
      for (int b = 0; b < 2; ++b) {
        const f32x4 r0 = o[2 * j][b] * inv[b], r1 = o[2 * j + 1][b] * inv[b];
        st16s(od + (j >> 1) * 4096 + b * 2048, oa ^ ((j & 1) * 64u), u32x4{pack2(r0[0], r0[1]), pack2(r0[2], r0[3]), pack2(r1[0], r1[1]), pack2(r1[2], r1[3])});
      }
    }
    if (!SAE && s >= SB && lane == 0) st4_sc1(cnt + (PSA + (hbase >> 1)) * 32 + 16 + wave, bell(s, rt_now() + DSA));
    WTM(slot, s, 3);
    WAIT_V(0);
    if (lane == 0) st4_sc1(cnt + (PSA + (hbase >> 1)) * 32 + wave, (unsigned)it);
    WTM(slot, s, 2);
  }
}
#undef LD8
#undef TIE8

constexpr int Q_MEM = 0;
constexpr int Q_K   = 16384;
constexpr int Q_V   = 24576;
constexpr int Q_QK  = 32768;
constexpr int Q_TM  = 36864;
constexpr int Q_CB  = 40960;
constexpr int Q_RING = 32768;
constexpr int V_P1  = 0;
constexpr int V_P2  = 16384;
constexpr int NCHAIN = 96;
#define VHN 2
#define ROTH 0
#define NQQ 6
#define NQK 4
#define E_AG 9
#define RDYI -2
#define ROTW 2
#define DQU 6
#define DQM 18
#define DQD 1
#define ARSEL (xg >> 1)
#define ORSEL (xg >> 1)
#define D_Q 105
#define D_O 4
#define E_B 6
#define E_O 1900
#define KDEL 5
#define VPOS 0

typedef __bf16 bf16x16 __attribute__((ext_vector_type(16)));
typedef unsigned u32x8 __attribute__((ext_vector_type(8)));
__device__ __forceinline__ f32x4 SMF(u32x4 a, u32x8 b, f32x4 c, int idx) {
  return __builtin_amdgcn_smfmac_f32_16x16x64_bf16(__builtin_bit_cast(bf16x8, a), __builtin_bit_cast(bf16x16, b), c, idx, 0, 0);
}
__device__ __forceinline__ float fold32(float a, float b) {
  const auto p = __builtin_amdgcn_permlane32_swap(__float_as_uint(a), __float_as_uint(b), false, false);
  return __uint_as_float(p[0]) + __uint_as_float(p[1]);
}
__device__ __forceinline__ f32x2 rowsum(f32x4 x) { return f32x2{fold32(x[0], x[2]), fold32(x[1], x[3])}; }
__device__ __forceinline__ unsigned nosent1(unsigned v) { return v < 0xFFFFFFFEu ? v : 0xFFFFFFFEu; }
__device__ __forceinline__ void st4s(const char* sb, unsigned vo, unsigned v) { asm volatile("global_store_dword %0, %1, %2 sc1\n\ts_nop 0" :: "v"(vo), "v"(v), "s"(sb) : "memory"); }
__device__ __forceinline__ void st8s(const char* sb, unsigned vo, u32x2 v) { asm volatile("global_store_dwordx2 %0, %1, %2 sc1\n\ts_nop 1" :: "v"(vo), "v"(v), "s"(sb) : "memory"); }
__device__ __forceinline__ void gldsw(const char* sbase, unsigned voff, unsigned lds) {
  unsigned keep;
  asm volatile("s_mov_b32 %0, m0\n\ts_mov_b32 m0, %3\n\ts_nop 2\n\tglobal_load_lds_dwordx4 %1, %2" CNT_ "\n\ts_mov_b32 m0, %0"
               : "=&s"(keep) : "v"(voff), "s"(sbase), "s"(lds) : "memory");
}
__device__ __forceinline__ void wait_clk(unsigned t0, unsigned d) {
  while ((unsigned)__builtin_amdgcn_s_memtime() - t0 < d) __builtin_amdgcn_s_sleep(1);
}
__device__ __forceinline__ unsigned bell_time(const char* a, unsigned vo, unsigned tag, unsigned cap) {
  unsigned v;
  for (unsigned spins = 0;; ++spins) {
    asm volatile("global_load_dword %0, %1, %2 sc1\n\ts_waitcnt vmcnt(0)" : "=&v"(v) : "v"(vo), "s"(a) : "memory");
    if (__all((v >> 24) >= tag) || spins >= cap) break;
  }
  const unsigned now = (unsigned)__builtin_amdgcn_s_memrealtime() + 250u;
  const unsigned age = rowmin16((now - v) & 0xFFFFFFu);
  return now - min(min(rdl(age, 0), rdl(age, 16)), 500u);
}
#define WBAR() do { __builtin_amdgcn_s_barrier(); asm volatile("" ::: "memory"); } while (0)
#define TN(k, v) do { } while (0)
__device__ __forceinline__ void stage_proj(const char* src, unsigned lds, int tid, int wave) {
  const int row = tid >> 4, f = 2 * ((row & 1) | (((row >> 2) & 1) << 1));
#pragma unroll
  for (int j = 0; j < 4; ++j)
    gldsw(src + (size_t)j * 32 * HD * 2, (unsigned)(row * HD * 2 + (((tid & 15) ^ f ^ ((j & 1) << 3)) << 4)), lds + (wave * 64 + 512 * j) * 16);
}
__device__ __forceinline__ unsigned proj_base(int T, int lane) {
  const int g = lane >> 4, q4 = (lane & 15) >> 2, p4 = lane & 3, fs = 2 * ((q4 & 1) | ((g & 1) << 1) | ((q4 >> 1) << 2));
  return (unsigned)((4 * g + (q4 & 1) + 32 * (q4 >> 1)) * 256 + (((2 * T) ^ fs) << 4) + p4 * 8);
}
__device__ __forceinline__ u32x8 proj_frag(const char* smem, unsigned a) {
  const u32x2 t0 = trread(smem, a), t1 = trread(smem, a + 512), t2 = trread(smem, a + 4096), t3 = trread(smem, a + 4608);
  return u32x8{t0.x, t0.y, t1.x, t1.y, t2.x, t2.y, t3.x, t3.y};
}
__device__ __forceinline__ void flag_frags(unsigned fa, unsigned a, unsigned want, bf16x8 (&f)[4]) {
  u32x2 t[8];
  for (;;) {
    u32x4 fl;
    asm volatile("ds_read_b128 %0, %9\n\t"
                 "ds_read_b64_tr_b16 %1, %10\n\tds_read_b64_tr_b16 %2, %10 offset:64\n\t"
                 "ds_read_b64_tr_b16 %3, %10 offset:512\n\tds_read_b64_tr_b16 %4, %10 offset:576\n\t"
                 "ds_read_b64_tr_b16 %5, %10 offset:1024\n\tds_read_b64_tr_b16 %6, %10 offset:1088\n\t"
                 "ds_read_b64_tr_b16 %7, %10 offset:1536\n\tds_read_b64_tr_b16 %8, %10 offset:1600\n\ts_waitcnt lgkmcnt(0)"
                 : "=&v"(fl), "=&v"(t[0]), "=&v"(t[1]), "=&v"(t[2]), "=&v"(t[3]), "=&v"(t[4]), "=&v"(t[5]), "=&v"(t[6]), "=&v"(t[7])
                 : "v"(fa), "v"(a) : "memory");
    if (__builtin_amdgcn_readfirstlane(min(min(fl.x, fl.y), min(fl.z, fl.w))) >= want) break;
  }
#pragma unroll
  for (int ks = 0; ks < 4; ++ks) f[ks] = asbf8(u32x4{t[2 * ks].x, t[2 * ks].y, t[2 * ks + 1].x, t[2 * ks + 1].y});
}
__device__ __forceinline__ void wait_flags(const char* p, unsigned want) {
  for (;;) {
    const u32x4 fl = *(volatile LAS u32x4*)p;
    if (__builtin_amdgcn_readfirstlane(min(min(fl.x, fl.y), min(fl.z, fl.w))) >= want) break;
  }
}
__device__ __forceinline__ f32x4 zero4() { float z = 0.0f; asm volatile("" : "+v"(z)); return f32x4{z, z, z, z}; }
__device__ __forceinline__ unsigned lds_word(const char* p) { return __builtin_amdgcn_readfirstlane(*(volatile LAS unsigned*)p); }

#define MFMA16(a, b, c) __builtin_amdgcn_mfma_f32_16x16x16bf16_1k((a), (b), (c), 0, 0, 0)
#define UF(x) __uint_as_float(x)
__device__ __forceinline__ unsigned umax3(unsigned a, unsigned b, unsigned c) { return max(max(a, b), c); }
__device__ __forceinline__ f32x2 logit2(float a, float b) {
  return f32x2{a, b};
}
constexpr float LSC = LOG2E * SCALE;

struct QKW { u32x32 w0, w1, w2, w3, w4, w5; u32x8 x; };
template <int W_> __device__ __forceinline__ void qk_step(const QKW& W, const char* ag, const char* vs, const char* od, const char* kv, unsigned& cap, unsigned& sp,
                                                          unsigned long long& tc, unsigned long long* ts, unsigned& d0, unsigned& d1);
template <> __device__ __forceinline__ void qk_step<0>(const QKW& W, const char* ag, const char* vs, const char* od, const char* kv, unsigned& cap, unsigned& sp,
                                                        unsigned long long& tc, unsigned long long* ts, unsigned& d0, unsigned& d1) {
  unsigned st, bg;
  asm volatile("v_mbcnt_lo_u32_b32 v60, -1, 0\n\t"
    "v_mbcnt_hi_u32_b32 v60, -1, v60\n\t"
    "v_lshlrev_b32 v6, 4, v60\n\t"
    "s_mov_b32 %[sp], 0\n\t"
    "s_setprio 3\n\t"
    "global_load_dwordx4 v[40:43], v6, %[ag] sc1\n\t"
    "global_load_dwordx4 v[44:47], v6, %[ag] offset:1024 sc1\n\t"
    "s_memtime %[tc]\n\t"
    "v_add_u32 v7, 40960, v6\n\t"
    "v_add_u32 v8, 98304, v6\n\t"
    "ds_read_b128 v[24:27], v7 offset:0\n\t"
    "ds_read_b128 v[28:31], v7 offset:1024\n\t"
    "ds_read_b128 v[32:35], v7 offset:8192\n\t"
    "ds_read_b128 v[36:39], v7 offset:9216\n\t"
    "v_lshrrev_b32 v48, 4, v60\n\t"
    "v_and_b32 v49, 15, v60\n\t"
    "v_and_b32 v61, 4, v60\n\t"
    "v_mul_u32_u24 v61, 0x2aaa, v61\n\t"
    "v_lshrrev_b32 v9, 1, v60\n\t"
    "v_and_b32 v9, 2, v9\n\t"
    "v_or3_b32 v9, v9, v61, %[c44]\n\t"
    "v_add_u32 v14, 0, v6\n\t"
    "v_lshlrev_b32 v61, 2, v48\n\t"
    "v_lshl_or_b32 v10, v49, 4, v61\n\t"
    "v_lshrrev_b32 v50, 2, v49\n\t"
    "v_and_b32 v51, 3, v60\n\t"
    "v_and_b32 v61, 1, v60\n\t"
    "v_lshlrev_b32 v61, 3, v61\n\t"
    "v_lshl_or_b32 v11, v50, 4, v61\n\t"
    "v_lshl_or_b32 v11, v48, 7, v11\n\t"
    "v_xor_b32 v12, v48, v49\n\t"
    "v_lshlrev_b32 v12, 4, v12\n\t"
    "v_lshl_or_b32 v12, v49, 8, v12\n\t"
    "v_lshl_or_b32 v50, v48, 2, v50\n\t"
    "v_or_b32 v51, 0, v51\n\t"
    "v_xor_b32 v51, v51, v50\n\t"
    "v_lshlrev_b32 v13, 4, v51\n\t"
    "v_lshl_or_b32 v13, v50, 8, v13\n\t"
    "v_and_b32 v61, 3, v60\n\t"
    "v_lshlrev_b32 v61, 4, v61\n\t"
    "v_lshl_or_b32 v15, v48, 6, v61\n\t"
    "v_and_b32 v61, 8, v60\n\t"
    "v_lshl_or_b32 v15, v61, 14, v15\n\t"
    "v_and_b32 v61, 4, v60\n\t"
    "v_lshl_or_b32 v15, v61, 8, v15\n\t"
    "v_add_u32 v15, 0, v15\n\t"
    "3:\n\t"
    "v_mov_b32 v16, 0\n\t"
    "v_mov_b32 v17, 0\n\t"
    "v_mov_b32 v18, 0\n\t"
    "v_mov_b32 v19, 0\n\t"
    "v_mov_b32 v20, 0\n\t"
    "v_mov_b32 v21, 0\n\t"
    "v_mov_b32 v22, 0\n\t"
    "v_mov_b32 v23, 0\n\t"
    "s_waitcnt vmcnt(0) lgkmcnt(0)\n\t"
    "14:\n\t"
    "ds_write_b128 v6, v[40:43] offset:0\n\t"
    "ds_write_b128 v6, v[44:47] offset:1024\n\t"
    "v_smfmac_f32_16x16x64_bf16 v[16:19], v[40:43], v[64:71], v9\n\t"
    "v_max3_u32 v60, v40, v41, v42\n\t"
    "v_max3_u32 v61, v43, v44, v45\n\t"
    "v_smfmac_f32_16x16x64_bf16 v[20:23], v[40:43], v[72:79], v9\n\t"
    "v_max3_u32 v60, v60, v46, v47\n\t"
    "v_max_u32 v60, v60, v61\n\t"
    "v_cmp_eq_u32 vcc, -1, v60\n\t"
    "s_cbranch_vccnz 1f\n\t"
    "2:\n\t"
    "s_waitcnt lgkmcnt(0)\n\t"
    "s_cmp_eq_u64 %[kv], 0\n\t"
    "s_barrier\n\t"
    "s_cbranch_scc1 20f\n\t"
    "ds_read_b128 v[40:43], v6 offset:12288\n\t"
    "ds_read_b128 v[48:51], v6 offset:2048\n\t"
    "v_smfmac_f32_16x16x64_bf16 v[16:19], v[44:47], v[80:87], v9\n\t"
    "ds_read_b128 v[52:55], v6 offset:3072\n\t"
    "ds_read_b128 v[56:59], v6 offset:4096\n\t"
    "v_smfmac_f32_16x16x64_bf16 v[20:23], v[44:47], v[88:95], v9\n\t"
    "ds_read_b128 v[60:63], v6 offset:13312\n\t"
    "ds_read_b128 v[44:47], v6 offset:5120\n\t"
    "s_waitcnt lgkmcnt(5)\n\t"
    "v_smfmac_f32_16x16x64_bf16 v[16:19], v[40:43], v[24:31], v9\n\t"
    "ds_read_b128 v[24:27], v7 offset:16384\n\t"
    "ds_read_b128 v[28:31], v7 offset:17408\n\t"
    "v_smfmac_f32_16x16x64_bf16 v[20:23], v[40:43], v[32:39], v9\n\t"
    "ds_read_b128 v[32:35], v7 offset:24576\n\t"
    "ds_read_b128 v[36:39], v7 offset:25600\n\t"
    "ds_read_b128 v[40:43], v6 offset:6144\n\t"
    "s_waitcnt lgkmcnt(9)\n\t"
    "v_smfmac_f32_16x16x64_bf16 v[16:19], v[48:51], v[96:103], v9\n\t"
    "v_smfmac_f32_16x16x64_bf16 v[20:23], v[48:51], v[104:111], v9\n\t"
    "ds_read_b128 v[48:51], v6 offset:7168\n\t"
    "s_waitcnt lgkmcnt(9)\n\t"
    "v_smfmac_f32_16x16x64_bf16 v[16:19], v[52:55], v[112:119], v9\n\t"
    "v_smfmac_f32_16x16x64_bf16 v[20:23], v[52:55], v[120:127], v9\n\t"
    "ds_read_b128 v[52:55], v6 offset:14336\n\t"
    "s_waitcnt lgkmcnt(9)\n\t"
    "v_smfmac_f32_16x16x64_bf16 v[16:19], v[56:59], v[128:135], v9\n\t"
    "v_smfmac_f32_16x16x64_bf16 v[20:23], v[56:59], v[136:143], v9\n\t"
    "ds_read_b128 v[56:59], v6 offset:8192\n\t"
    "s_waitcnt lgkmcnt(6)\n\t"
    "v_smfmac_f32_16x16x64_bf16 v[16:19], v[60:63], v[24:31], v9\n\t"
    "ds_read_b128 v[24:27], v8 offset:0\n\t"
    "ds_read_b128 v[28:31], v8 offset:1024\n\t"
    "s_waitcnt lgkmcnt(6)\n\t"
    "v_smfmac_f32_16x16x64_bf16 v[20:23], v[60:63], v[32:39], v9\n\t"
    "ds_read_b128 v[32:35], v8 offset:8192\n\t"
    "ds_read_b128 v[36:39], v8 offset:9216\n\t"
    "ds_read_b128 v[60:63], v6 offset:9216\n\t"
    "v_smfmac_f32_16x16x64_bf16 v[16:19], v[44:47], v[144:151], v9\n\t"
    "v_smfmac_f32_16x16x64_bf16 v[20:23], v[44:47], v[152:159], v9\n\t"
    "ds_read_b128 v[44:47], v6 offset:10240\n\t"
    "s_waitcnt lgkmcnt(9)\n\t"
    "v_smfmac_f32_16x16x64_bf16 v[16:19], v[40:43], v[160:167], v9\n\t"
    "v_smfmac_f32_16x16x64_bf16 v[20:23], v[40:43], v[168:175], v9\n\t"
    "ds_read_b128 v[40:43], v6 offset:15360\n\t"
    "s_waitcnt lgkmcnt(9)\n\t"
    "v_smfmac_f32_16x16x64_bf16 v[16:19], v[48:51], v[176:183], v9\n\t"
    "v_smfmac_f32_16x16x64_bf16 v[20:23], v[48:51], v[184:191], v9\n\t"
    "ds_read_b128 v[48:51], v6 offset:11264\n\t"
    "s_waitcnt lgkmcnt(6)\n\t"
    "v_smfmac_f32_16x16x64_bf16 v[16:19], v[52:55], v[24:31], v9\n\t"
    "ds_read_b128 v[24:27], v8 offset:16384\n\t"
    "ds_read_b128 v[28:31], v8 offset:17408\n\t"
    "s_waitcnt lgkmcnt(6)\n\t"
    "v_smfmac_f32_16x16x64_bf16 v[20:23], v[52:55], v[32:39], v9\n\t"
    "ds_read_b128 v[32:35], v8 offset:24576\n\t"
    "ds_read_b128 v[36:39], v8 offset:25600\n\t"
    "v_smfmac_f32_16x16x64_bf16 v[16:19], v[56:59], v[192:199], v9\n\t"
    "v_smfmac_f32_16x16x64_bf16 v[20:23], v[56:59], v[200:207], v9\n\t"
    "s_waitcnt lgkmcnt(7)\n\t"
    "v_smfmac_f32_16x16x64_bf16 v[16:19], v[60:63], v[208:215], v9\n\t"
    "v_smfmac_f32_16x16x64_bf16 v[20:23], v[60:63], v[216:223], v9\n\t"
    "s_waitcnt lgkmcnt(6)\n\t"
    "v_smfmac_f32_16x16x64_bf16 v[16:19], v[44:47], v[224:231], v9\n\t"
    "v_smfmac_f32_16x16x64_bf16 v[20:23], v[44:47], v[232:239], v9\n\t"
    "s_waitcnt lgkmcnt(2)\n\t"
    "v_smfmac_f32_16x16x64_bf16 v[16:19], v[40:43], v[24:31], v9\n\t"
    "s_waitcnt lgkmcnt(0)\n\t"
    "v_smfmac_f32_16x16x64_bf16 v[20:23], v[40:43], v[32:39], v9\n\t"
    "v_smfmac_f32_16x16x64_bf16 v[16:19], v[48:51], v[240:247], v9\n\t"
    "v_smfmac_f32_16x16x64_bf16 v[20:23], v[48:51], v[248:255], v9\n\t"
    "v_xor_b32 v7, 64, v12\n\t"
    "v_xor_b32 v8, 0x80, v12\n\t"
    "v_xor_b32 v6, 0xc0, v12\n\t"
    "ds_read_b128 v[24:27], v12 offset:16384\n\t"
    "ds_read_b128 v[28:31], v12 offset:20480\n\t"
    "ds_read_b128 v[32:35], v7 offset:16384\n\t"
    "ds_read_b128 v[36:39], v7 offset:20480\n\t"
    "v_permlane16_swap_b32 v16, v18\n\t"
    "v_permlane16_swap_b32 v17, v19\n\t"
    "v_permlane16_swap_b32 v20, v22\n\t"
    "v_permlane16_swap_b32 v21, v23\n\t"
    "v_add_f32 v16, v16, v18\n\t"
    "v_add_f32 v17, v17, v19\n\t"
    "v_add_f32 v20, v20, v22\n\t"
    "v_mul_f32 v16, %[lsc], v16\n\t"
    "v_mul_f32 v17, %[lsc], v17\n\t"
    "v_cvt_pk_bf16_f32 v16, v16, v17\n\t"
    "v_add_f32 v21, v21, v23\n\t"
    "ds_write_b32 v10, v16 offset:32768\n\t"
    "v_mul_f32 v20, %[lsc], v20\n\t"
    "v_mul_f32 v21, %[lsc], v21\n\t"
    "v_cvt_pk_bf16_f32 v20, v20, v21\n\t"
    "ds_write_b32 v10, v20 offset:33024\n\t"
    "ds_read_b128 v[40:43], v8 offset:16384\n\t"
    "ds_read_b128 v[44:47], v8 offset:20480\n\t"
    "ds_read_b128 v[48:51], v6 offset:16384\n\t"
    "ds_read_b128 v[52:55], v6 offset:20480\n\t"
    "s_waitcnt lgkmcnt(4)\n\t"
    "s_barrier\n\t"
    "ds_read_b64_tr_b16 v[16:17], v11 offset:32768\n\t"
    "ds_read_b64_tr_b16 v[18:19], v11 offset:32832\n\t"
    "ds_read_b64_tr_b16 v[20:21], v11 offset:33280\n\t"
    "ds_read_b64_tr_b16 v[22:23], v11 offset:33344\n\t"
    "ds_read_b64_tr_b16 v[56:57], v11 offset:33792\n\t"
    "ds_read_b64_tr_b16 v[58:59], v11 offset:33856\n\t"
    "ds_read_b64_tr_b16 v[60:61], v11 offset:34304\n\t"
    "ds_read_b64_tr_b16 v[62:63], v11 offset:34368\n\t"
    "v_mov_b32 v6, 0\n\t"
    "v_mov_b32 v7, 0\n\t"
    "v_mov_b32 v8, 0\n\t"
    "v_mov_b32 v9, 0\n\t"
    "s_mov_b32 exec_hi, 0\n\t"
    "global_load_dwordx4 v[6:9], v14, %[vs] sc1\n\t"
    "s_mov_b32 exec_hi, -1\n\t"
    "s_waitcnt lgkmcnt(6)\n\t"
    "v_mfma_f32_16x16x32_bf16 v[24:27], v[24:27], v[16:19], 0\n\t"
    "v_mfma_f32_16x16x32_bf16 v[28:31], v[28:31], v[16:19], 0\n\t"
    "s_waitcnt lgkmcnt(4)\n\t"
    "v_mfma_f32_16x16x32_bf16 v[24:27], v[32:35], v[20:23], v[24:27]\n\t"
    "v_mfma_f32_16x16x32_bf16 v[28:31], v[36:39], v[20:23], v[28:31]\n\t"
    "ds_read_b64_tr_b16 v[32:33], v13 offset:24576\n\t"
    "ds_read_b64_tr_b16 v[34:35], v13 offset:28672\n\t"
    "ds_read_b64_tr_b16 v[36:37], v13 offset:24584\n\t"
    "ds_read_b64_tr_b16 v[38:39], v13 offset:28680\n\t"
    "s_waitcnt lgkmcnt(6)\n\t"
    "v_mfma_f32_16x16x32_bf16 v[24:27], v[40:43], v[56:59], v[24:27]\n\t"
    "v_mfma_f32_16x16x32_bf16 v[28:31], v[44:47], v[56:59], v[28:31]\n\t"
    "s_waitcnt lgkmcnt(4)\n\t"
    "v_mfma_f32_16x16x32_bf16 v[24:27], v[48:51], v[60:63], v[24:27]\n\t"
    "v_mfma_f32_16x16x32_bf16 v[28:31], v[52:55], v[60:63], v[28:31]\n\t"
    "s_setprio 1\n\t"
    "v_mov_b32 v52, %[one]\n\t"
    "v_mov_b32 v53, %[one]\n\t"
    "v_mov_b32 v54, %[one]\n\t"
    "v_mov_b32 v55, %[one]\n\t"
    "s_nop 2\n\t"
    "v_max3_f32 v10, |v24|, |v25|, |v26|\n\t"
    "v_max3_f32 v40, |v27|, |v28|, |v29|\n\t"
    "v_max3_f32 v10, v10, |v30|, |v31|\n\t"
    "v_max_f32 v10, v10, v40\n\t"
    "v_cmp_nge_f32 vcc, %[big], v10\n\t"
    "s_mov_b32 %[bg], %[big]\n\t"
    "v_mov_b32 v10, 0\n\t"
    "s_cbranch_vccnz 12f\n\t"
    "13:\n\t"
    "v_exp_f32 v24, v24\n\t"
    "v_exp_f32 v25, v25\n\t"
    "v_exp_f32 v26, v26\n\t"
    "v_exp_f32 v27, v27\n\t"
    "v_exp_f32 v28, v28\n\t"
    "v_exp_f32 v29, v29\n\t"
    "v_exp_f32 v30, v30\n\t"
    "v_exp_f32 v31, v31\n\t"
    "v_cvt_pk_bf16_f32 v40, v24, v25\n\t"
    "v_cvt_pk_bf16_f32 v41, v26, v27\n\t"
    "v_cvt_pk_bf16_f32 v42, v28, v29\n\t"
    "v_cvt_pk_bf16_f32 v43, v30, v31\n\t"
    "s_setprio 3\n\t"
    "s_waitcnt lgkmcnt(0)\n\t"
    "s_nop 0\n\t"
    "v_mfma_f32_16x16x32_bf16 v[44:47], v[32:35], v[40:43], 0\n\t"
    "v_mfma_f32_16x16x32_bf16 v[48:51], v[36:39], v[40:43], 0\n\t"
    "v_mfma_f32_16x16x32_bf16 v[52:55], v[52:55], v[40:43], 0\n\t"
    "v_mov_b32 v12, 0\n\t"
    "v_mov_b32 v13, 0\n\t"
    "s_mov_b32 exec_hi, 0\n\t"
    "v_mov_b32 v12, %[one]\n\t"
    "v_mov_b32 v13, %[one]\n\t"
    "s_mov_b32 exec_hi, -1\n\t"
    "s_barrier\n\t"
    "ds_read_b64_tr_b16 v[24:25], v11 offset:34816\n\t"
    "ds_read_b64_tr_b16 v[26:27], v11 offset:34880\n\t"
    "ds_read_b64_tr_b16 v[28:29], v11 offset:35328\n\t"
    "ds_read_b64_tr_b16 v[30:31], v11 offset:35392\n\t"
    "ds_read_b64_tr_b16 v[32:33], v11 offset:35840\n\t"
    "ds_read_b64_tr_b16 v[34:35], v11 offset:35904\n\t"
    "ds_read_b64_tr_b16 v[36:37], v11 offset:36352\n\t"
    "ds_read_b64_tr_b16 v[38:39], v11 offset:36416\n\t"
    "s_waitcnt lgkmcnt(6)\n\t"
    "v_mfma_f32_16x16x32_bf16 v[40:43], v[24:27], v[16:19], 0\n\t"
    "s_waitcnt lgkmcnt(4)\n\t"
    "v_mfma_f32_16x16x32_bf16 v[40:43], v[28:31], v[20:23], v[40:43]\n\t"
    "s_waitcnt lgkmcnt(2)\n\t"
    "v_mfma_f32_16x16x32_bf16 v[40:43], v[32:35], v[56:59], v[40:43]\n\t"
    "s_waitcnt lgkmcnt(0)\n\t"
    "v_mfma_f32_16x16x32_bf16 v[40:43], v[36:39], v[60:63], v[40:43]\n\t"
    "5:\n\t"
    "s_waitcnt vmcnt(0)\n\t"
    "v_max3_u32 v11, v6, v7, v8\n\t"
    "v_max_u32 v11, v11, v9\n\t"
    "v_cmp_eq_u32 vcc, -1, v11\n\t"
    "s_cbranch_vccnz 4f\n\t"
    "6:\n\t"
    "s_nop 2\n\t"
    "v_exp_f32 v28, v40\n\t"
    "v_max3_f32 v11, v40, v41, v42\n\t"
    "v_exp_f32 v29, v41\n\t"
    "v_max_f32 v11, v11, v43\n\t"
    "v_exp_f32 v30, v42\n\t"
    "v_cmp_nge_f32 vcc, %[bg], v11\n\t"
    "v_exp_f32 v31, v43\n\t"
    "s_cbranch_vccnz 10f\n\t"
    "11:\n\t"
    "v_cvt_pk_bf16_f32 v24, v28, v29\n\t"
    "v_cvt_pk_bf16_f32 v25, v30, v31\n\t"
    "s_nop 1\n\t"
    "v_mfma_f32_16x16x16_bf16 v[52:55], v[12:13], v[24:25], v[52:55]\n\t"
    "v_mfma_f32_16x16x16_bf16 v[44:47], v[6:7], v[24:25], v[44:47]\n\t"
    "v_mfma_f32_16x16x16_bf16 v[48:51], v[8:9], v[24:25], v[48:51]\n\t"
    "s_nop 5\n\t"
    "v_rcp_f32 v26, v52\n\t"
    "s_nop 0\n\t"
    "v_pk_mul_f32 v[44:45], v[44:45], v[26:27] op_sel_hi:[1,0]\n\t"
    "v_pk_mul_f32 v[46:47], v[46:47], v[26:27] op_sel_hi:[1,0]\n\t"
    "v_pk_mul_f32 v[48:49], v[48:49], v[26:27] op_sel_hi:[1,0]\n\t"
    "v_pk_mul_f32 v[50:51], v[50:51], v[26:27] op_sel_hi:[1,0]\n\t"
    "v_cvt_pk_bf16_f32 v36, v44, v45\n\t"
    "v_cvt_pk_bf16_f32 v37, v46, v47\n\t"
    "v_cvt_pk_bf16_f32 v38, v48, v49\n\t"
    "v_cvt_pk_bf16_f32 v39, v50, v51\n\t"
    "v_min_u32 v36, -2, v36\n\t"
    "v_min_u32 v37, -2, v37\n\t"
    "v_min_u32 v38, -2, v38\n\t"
    "v_min_u32 v39, -2, v39\n\t"
    "global_store_dwordx4 v15, v[36:39], %[od] sc1\n\t"
    "s_barrier\n\t"
    "s_mov_b32 exec_hi, 0\n\t"
    "global_store_dwordx4 v14, v[6:9], %[kv] sc1\n\t"
    "s_mov_b32 exec_hi, -1\n\t"
    "s_nop 0\n\t"
    "s_waitcnt lgkmcnt(0)\n\t"
    "s_branch 9f\n\t"
    "1:\n\t"
    "s_add_u32 %[sp], %[sp], 1\n\t"
    "s_cmp_ge_u32 %[sp], %[cap]\n\t"
    "s_cbranch_scc1 7f\n\t"
    "global_load_dwordx4 v[40:43], v6, %[ag] sc1\n\t"
    "global_load_dwordx4 v[44:47], v6, %[ag] offset:1024 sc1\n\t"
    "s_waitcnt lgkmcnt(0)\n\t"
    "s_memtime %[tc]\n\t"
    "s_nop 7\n\t"
    "s_branch 3b\n\t"
    "7:\n\t"
    "s_mov_b32 %[cap], 0\n\t"
    "s_branch 2b\n\t"
    "20:\n\t"
    "s_barrier\n\t"
    "s_barrier\n\t"
    "s_barrier\n\t"
    "s_branch 9f\n\t"
    "4:\n\t"
    "s_add_u32 %[sp], %[sp], 0x10000\n\t"
    "s_lshr_b32 %[st], %[sp], 16\n\t"
    "s_cmp_ge_u32 %[st], %[cap]\n\t"
    "s_cbranch_scc1 8f\n\t"
    "s_mov_b32 exec_hi, 0\n\t"
    "global_load_dwordx4 v[6:9], v14, %[vs] sc1\n\t"
    "s_mov_b32 exec_hi, -1\n\t"
    "s_branch 5b\n\t"
    "8:\n\t"
    "s_mov_b32 %[cap], 0\n\t"
    "s_branch 6b\n\t"
    "10:\n\t"
    "v_max_f32 v11, v11, v10\n\t"
    "v_mov_b32 v26, v11\n\t"
    "s_nop 1\n\t"
    "v_permlane16_swap_b32 v11, v26\n\t"
    "v_max_f32 v11, v11, v26\n\t"
    "v_sub_f32 v28, v40, v11\n\t"
    "v_sub_f32 v29, v41, v11\n\t"
    "v_sub_f32 v30, v42, v11\n\t"
    "v_sub_f32 v31, v43, v11\n\t"
    "v_sub_f32 v11, v10, v11\n\t"
    "v_exp_f32 v11, v11\n\t"
    "v_exp_f32 v28, v28\n\t"
    "v_exp_f32 v29, v29\n\t"
    "v_exp_f32 v30, v30\n\t"
    "v_exp_f32 v31, v31\n\t"
    "v_mul_f32 v44, v44, v11\n\t"
    "v_mul_f32 v45, v45, v11\n\t"
    "v_mul_f32 v46, v46, v11\n\t"
    "v_mul_f32 v47, v47, v11\n\t"
    "v_mul_f32 v48, v48, v11\n\t"
    "v_mul_f32 v49, v49, v11\n\t"
    "v_mul_f32 v50, v50, v11\n\t"
    "v_mul_f32 v51, v51, v11\n\t"
    "v_mul_f32 v52, v52, v11\n\t"
    "v_mul_f32 v53, v53, v11\n\t"
    "v_mul_f32 v54, v54, v11\n\t"
    "v_mul_f32 v55, v55, v11\n\t"
    "s_branch 11b\n\t"
    "12:\n\t"
    "v_max3_f32 v10, v24, v25, v26\n\t"
    "v_max3_f32 v40, v27, v28, v29\n\t"
    "v_max3_f32 v10, v10, v30, v31\n\t"
    "v_max3_f32 v10, v10, v40, %[neg]\n\t"
    "v_mov_b32 v40, v10\n\t"
    "s_nop 1\n\t"
    "v_permlane16_swap_b32 v10, v40\n\t"
    "v_max_f32 v10, v10, v40\n\t"
    "v_mov_b32 v40, v10\n\t"
    "s_nop 1\n\t"
    "v_permlane32_swap_b32 v10, v40\n\t"
    "v_max_f32 v10, v10, v40\n\t"
    "v_sub_f32 v24, v24, v10\n\t"
    "v_sub_f32 v25, v25, v10\n\t"
    "v_sub_f32 v26, v26, v10\n\t"
    "v_sub_f32 v27, v27, v10\n\t"
    "v_sub_f32 v28, v28, v10\n\t"
    "v_sub_f32 v29, v29, v10\n\t"
    "v_sub_f32 v30, v30, v10\n\t"
    "v_sub_f32 v31, v31, v10\n\t"
    "s_mov_b32 %[bg], 0x7fc00000\n\t"
    "s_branch 13b\n\t"
    "9:"
    : [tc] "=&s"(tc), [sp] "=&s"(sp), [cap] "+s"(cap), [st] "=&s"(st), [bg] "=&s"(bg)
    : [ag] "s"(ag), [c44] "s"(0x4444u), [kv] "s"(kv), [vs] "s"(vs), [od] "s"(od), [od2] "s"(od + 262144), [neg] "s"(-1.0e38f), [lsc] "s"(LSC), [one] "s"(0x3F803F80u), [big] "s"(24.0f), "{v[64:95]}"(W.w0), "{v[96:127]}"(W.w1), "{v[128:159]}"(W.w2), "{v[160:191]}"(W.w3), "{v[192:223]}"(W.w4), "{v[224:255]}"(W.w5)
    : "v6", "v7", "v8", "v9", "v10", "v11", "v12", "v13", "v14", "v15", "v16", "v17", "v18", "v19", "v20", "v21", "v22", "v23", "v24", "v25", "v26", "v27", "v28", "v29", "v30", "v31", "v32", "v33", "v34", "v35", "v36", "v37", "v38", "v39", "v40", "v41", "v42", "v43", "v44", "v45", "v46", "v47", "v48", "v49", "v50", "v51", "v52", "v53", "v54", "v55", "v56", "v57", "v58", "v59", "v60", "v61", "v62", "v63", "vcc", "scc", "memory");
}
template <> __device__ __forceinline__ void qk_step<1>(const QKW& W, const char* ag, const char* vs, const char* od, const char* kv, unsigned& cap, unsigned& sp,
                                                        unsigned long long& tc, unsigned long long* ts, unsigned& d0, unsigned& d1) {
  unsigned st, bg;
  asm volatile("v_mbcnt_lo_u32_b32 v60, -1, 0\n\t"
    "v_mbcnt_hi_u32_b32 v60, -1, v60\n\t"
    "v_lshlrev_b32 v6, 4, v60\n\t"
    "s_mov_b32 %[sp], 0\n\t"
    "s_setprio 3\n\t"
    "global_load_dwordx4 v[40:43], v6, %[ag] sc1\n\t"
    "global_load_dwordx4 v[44:47], v6, %[ag] offset:1024 sc1\n\t"
    "s_memtime %[tc]\n\t"
    "v_add_u32 v7, 43008, v6\n\t"
    "v_add_u32 v8, 100352, v6\n\t"
    "ds_read_b128 v[24:27], v7 offset:0\n\t"
    "ds_read_b128 v[28:31], v7 offset:1024\n\t"
    "ds_read_b128 v[32:35], v7 offset:8192\n\t"
    "ds_read_b128 v[36:39], v7 offset:9216\n\t"
    "v_lshrrev_b32 v48, 4, v60\n\t"
    "v_and_b32 v49, 15, v60\n\t"
    "v_and_b32 v61, 4, v60\n\t"
    "v_mul_u32_u24 v61, 0x2aaa, v61\n\t"
    "v_lshrrev_b32 v9, 1, v60\n\t"
    "v_and_b32 v9, 2, v9\n\t"
    "v_or3_b32 v9, v9, v61, %[c44]\n\t"
    "v_add_u32 v14, 512, v6\n\t"
    "v_lshlrev_b32 v61, 2, v48\n\t"
    "v_lshl_or_b32 v10, v49, 4, v61\n\t"
    "v_lshrrev_b32 v50, 2, v49\n\t"
    "v_and_b32 v51, 3, v60\n\t"
    "v_and_b32 v61, 1, v60\n\t"
    "v_lshlrev_b32 v61, 3, v61\n\t"
    "v_lshl_or_b32 v11, v50, 4, v61\n\t"
    "v_lshl_or_b32 v11, v48, 7, v11\n\t"
    "v_xor_b32 v12, v48, v49\n\t"
    "v_lshlrev_b32 v12, 4, v12\n\t"
    "v_lshl_or_b32 v12, v49, 8, v12\n\t"
    "v_lshl_or_b32 v50, v48, 2, v50\n\t"
    "v_or_b32 v51, 4, v51\n\t"
    "v_xor_b32 v51, v51, v50\n\t"
    "v_lshlrev_b32 v13, 4, v51\n\t"
    "v_lshl_or_b32 v13, v50, 8, v13\n\t"
    "v_and_b32 v61, 3, v60\n\t"
    "v_lshlrev_b32 v61, 4, v61\n\t"
    "v_lshl_or_b32 v15, v48, 6, v61\n\t"
    "v_and_b32 v61, 8, v60\n\t"
    "v_lshl_or_b32 v15, v61, 14, v15\n\t"
    "v_and_b32 v61, 4, v60\n\t"
    "v_lshl_or_b32 v15, v61, 8, v15\n\t"
    "v_add_u32 v15, 256, v15\n\t"
    "3:\n\t"
    "v_mov_b32 v16, 0\n\t"
    "v_mov_b32 v17, 0\n\t"
    "v_mov_b32 v18, 0\n\t"
    "v_mov_b32 v19, 0\n\t"
    "v_mov_b32 v20, 0\n\t"
    "v_mov_b32 v21, 0\n\t"
    "v_mov_b32 v22, 0\n\t"
    "v_mov_b32 v23, 0\n\t"
    "s_waitcnt vmcnt(0) lgkmcnt(0)\n\t"
    "14:\n\t"
    "ds_write_b128 v6, v[40:43] offset:2048\n\t"
    "ds_write_b128 v6, v[44:47] offset:3072\n\t"
    "v_smfmac_f32_16x16x64_bf16 v[16:19], v[40:43], v[96:103], v9\n\t"
    "v_max3_u32 v60, v40, v41, v42\n\t"
    "v_max3_u32 v61, v43, v44, v45\n\t"
    "v_smfmac_f32_16x16x64_bf16 v[20:23], v[40:43], v[104:111], v9\n\t"
    "v_max3_u32 v60, v60, v46, v47\n\t"
    "v_max_u32 v60, v60, v61\n\t"
    "v_cmp_eq_u32 vcc, -1, v60\n\t"
    "s_cbranch_vccnz 1f\n\t"
    "2:\n\t"
    "s_waitcnt lgkmcnt(0)\n\t"
    "s_cmp_eq_u64 %[kv], 0\n\t"
    "s_barrier\n\t"
    "s_cbranch_scc1 20f\n\t"
    "ds_read_b128 v[40:43], v6 offset:12288\n\t"
    "ds_read_b128 v[48:51], v6 offset:0\n\t"
    "v_smfmac_f32_16x16x64_bf16 v[16:19], v[44:47], v[112:119], v9\n\t"
    "ds_read_b128 v[52:55], v6 offset:1024\n\t"
    "ds_read_b128 v[56:59], v6 offset:4096\n\t"
    "v_smfmac_f32_16x16x64_bf16 v[20:23], v[44:47], v[120:127], v9\n\t"
    "ds_read_b128 v[60:63], v6 offset:13312\n\t"
    "ds_read_b128 v[44:47], v6 offset:5120\n\t"
    "s_waitcnt lgkmcnt(5)\n\t"
    "v_smfmac_f32_16x16x64_bf16 v[16:19], v[40:43], v[24:31], v9\n\t"
    "ds_read_b128 v[24:27], v7 offset:16384\n\t"
    "ds_read_b128 v[28:31], v7 offset:17408\n\t"
    "v_smfmac_f32_16x16x64_bf16 v[20:23], v[40:43], v[32:39], v9\n\t"
    "ds_read_b128 v[32:35], v7 offset:24576\n\t"
    "ds_read_b128 v[36:39], v7 offset:25600\n\t"
    "ds_read_b128 v[40:43], v6 offset:6144\n\t"
    "s_waitcnt lgkmcnt(9)\n\t"
    "v_smfmac_f32_16x16x64_bf16 v[16:19], v[48:51], v[64:71], v9\n\t"
    "v_smfmac_f32_16x16x64_bf16 v[20:23], v[48:51], v[72:79], v9\n\t"
    "ds_read_b128 v[48:51], v6 offset:7168\n\t"
    "s_waitcnt lgkmcnt(9)\n\t"
    "v_smfmac_f32_16x16x64_bf16 v[16:19], v[52:55], v[80:87], v9\n\t"
    "v_smfmac_f32_16x16x64_bf16 v[20:23], v[52:55], v[88:95], v9\n\t"
    "ds_read_b128 v[52:55], v6 offset:14336\n\t"
    "s_waitcnt lgkmcnt(9)\n\t"
    "v_smfmac_f32_16x16x64_bf16 v[16:19], v[56:59], v[128:135], v9\n\t"
    "v_smfmac_f32_16x16x64_bf16 v[20:23], v[56:59], v[136:143], v9\n\t"
    "ds_read_b128 v[56:59], v6 offset:8192\n\t"
    "s_waitcnt lgkmcnt(6)\n\t"
    "v_smfmac_f32_16x16x64_bf16 v[16:19], v[60:63], v[24:31], v9\n\t"
    "ds_read_b128 v[24:27], v8 offset:0\n\t"
    "ds_read_b128 v[28:31], v8 offset:1024\n\t"
    "s_waitcnt lgkmcnt(6)\n\t"
    "v_smfmac_f32_16x16x64_bf16 v[20:23], v[60:63], v[32:39], v9\n\t"
    "ds_read_b128 v[32:35], v8 offset:8192\n\t"
    "ds_read_b128 v[36:39], v8 offset:9216\n\t"
    "ds_read_b128 v[60:63], v6 offset:9216\n\t"
    "v_smfmac_f32_16x16x64_bf16 v[16:19], v[44:47], v[144:151], v9\n\t"
    "v_smfmac_f32_16x16x64_bf16 v[20:23], v[44:47], v[152:159], v9\n\t"
    "ds_read_b128 v[44:47], v6 offset:10240\n\t"
    "s_waitcnt lgkmcnt(9)\n\t"
    "v_smfmac_f32_16x16x64_bf16 v[16:19], v[40:43], v[160:167], v9\n\t"
    "v_smfmac_f32_16x16x64_bf16 v[20:23], v[40:43], v[168:175], v9\n\t"
    "ds_read_b128 v[40:43], v6 offset:15360\n\t"
    "s_waitcnt lgkmcnt(9)\n\t"
    "v_smfmac_f32_16x16x64_bf16 v[16:19], v[48:51], v[176:183], v9\n\t"
    "v_smfmac_f32_16x16x64_bf16 v[20:23], v[48:51], v[184:191], v9\n\t"
    "ds_read_b128 v[48:51], v6 offset:11264\n\t"
    "s_waitcnt lgkmcnt(6)\n\t"
    "v_smfmac_f32_16x16x64_bf16 v[16:19], v[52:55], v[24:31], v9\n\t"
    "ds_read_b128 v[24:27], v8 offset:16384\n\t"
    "ds_read_b128 v[28:31], v8 offset:17408\n\t"
    "s_waitcnt lgkmcnt(6)\n\t"
    "v_smfmac_f32_16x16x64_bf16 v[20:23], v[52:55], v[32:39], v9\n\t"
    "ds_read_b128 v[32:35], v8 offset:24576\n\t"
    "ds_read_b128 v[36:39], v8 offset:25600\n\t"
    "v_smfmac_f32_16x16x64_bf16 v[16:19], v[56:59], v[192:199], v9\n\t"
    "v_smfmac_f32_16x16x64_bf16 v[20:23], v[56:59], v[200:207], v9\n\t"
    "s_waitcnt lgkmcnt(7)\n\t"
    "v_smfmac_f32_16x16x64_bf16 v[16:19], v[60:63], v[208:215], v9\n\t"
    "v_smfmac_f32_16x16x64_bf16 v[20:23], v[60:63], v[216:223], v9\n\t"
    "s_waitcnt lgkmcnt(6)\n\t"
    "v_smfmac_f32_16x16x64_bf16 v[16:19], v[44:47], v[224:231], v9\n\t"
    "v_smfmac_f32_16x16x64_bf16 v[20:23], v[44:47], v[232:239], v9\n\t"
    "s_waitcnt lgkmcnt(2)\n\t"
    "v_smfmac_f32_16x16x64_bf16 v[16:19], v[40:43], v[24:31], v9\n\t"
    "s_waitcnt lgkmcnt(0)\n\t"
    "v_smfmac_f32_16x16x64_bf16 v[20:23], v[40:43], v[32:39], v9\n\t"
    "v_smfmac_f32_16x16x64_bf16 v[16:19], v[48:51], v[240:247], v9\n\t"
    "v_smfmac_f32_16x16x64_bf16 v[20:23], v[48:51], v[248:255], v9\n\t"
    "v_xor_b32 v7, 64, v12\n\t"
    "v_xor_b32 v8, 0x80, v12\n\t"
    "v_xor_b32 v6, 0xc0, v12\n\t"
    "ds_read_b128 v[24:27], v12 offset:16384\n\t"
    "ds_read_b128 v[28:31], v12 offset:20480\n\t"
    "ds_read_b128 v[32:35], v7 offset:16384\n\t"
    "ds_read_b128 v[36:39], v7 offset:20480\n\t"
    "v_permlane16_swap_b32 v16, v18\n\t"
    "v_permlane16_swap_b32 v17, v19\n\t"
    "v_permlane16_swap_b32 v20, v22\n\t"
    "v_permlane16_swap_b32 v21, v23\n\t"
    "v_add_f32 v16, v16, v18\n\t"
    "v_add_f32 v17, v17, v19\n\t"
    "v_add_f32 v20, v20, v22\n\t"
    "v_mul_f32 v16, %[lsc], v16\n\t"
    "v_mul_f32 v17, %[lsc], v17\n\t"
    "v_cvt_pk_bf16_f32 v16, v16, v17\n\t"
    "v_add_f32 v21, v21, v23\n\t"
    "ds_write_b32 v10, v16 offset:33280\n\t"
    "v_mul_f32 v20, %[lsc], v20\n\t"
    "v_mul_f32 v21, %[lsc], v21\n\t"
    "v_cvt_pk_bf16_f32 v20, v20, v21\n\t"
    "ds_write_b32 v10, v20 offset:33536\n\t"
    "ds_read_b128 v[40:43], v8 offset:16384\n\t"
    "ds_read_b128 v[44:47], v8 offset:20480\n\t"
    "ds_read_b128 v[48:51], v6 offset:16384\n\t"
    "ds_read_b128 v[52:55], v6 offset:20480\n\t"
    "s_waitcnt lgkmcnt(4)\n\t"
    "s_barrier\n\t"
    "ds_read_b64_tr_b16 v[16:17], v11 offset:32768\n\t"
    "ds_read_b64_tr_b16 v[18:19], v11 offset:32832\n\t"
    "ds_read_b64_tr_b16 v[20:21], v11 offset:33280\n\t"
    "ds_read_b64_tr_b16 v[22:23], v11 offset:33344\n\t"
    "ds_read_b64_tr_b16 v[56:57], v11 offset:33792\n\t"
    "ds_read_b64_tr_b16 v[58:59], v11 offset:33856\n\t"
    "ds_read_b64_tr_b16 v[60:61], v11 offset:34304\n\t"
    "ds_read_b64_tr_b16 v[62:63], v11 offset:34368\n\t"
    "v_mov_b32 v6, 0\n\t"
    "v_mov_b32 v7, 0\n\t"
    "v_mov_b32 v8, 0\n\t"
    "v_mov_b32 v9, 0\n\t"
    "s_mov_b32 exec_hi, 0\n\t"
    "global_load_dwordx4 v[6:9], v14, %[vs] sc1\n\t"
    "s_mov_b32 exec_hi, -1\n\t"
    "s_waitcnt lgkmcnt(6)\n\t"
    "v_mfma_f32_16x16x32_bf16 v[24:27], v[24:27], v[16:19], 0\n\t"
    "v_mfma_f32_16x16x32_bf16 v[28:31], v[28:31], v[16:19], 0\n\t"
    "s_waitcnt lgkmcnt(4)\n\t"
    "v_mfma_f32_16x16x32_bf16 v[24:27], v[32:35], v[20:23], v[24:27]\n\t"
    "v_mfma_f32_16x16x32_bf16 v[28:31], v[36:39], v[20:23], v[28:31]\n\t"
    "ds_read_b64_tr_b16 v[32:33], v13 offset:24576\n\t"
    "ds_read_b64_tr_b16 v[34:35], v13 offset:28672\n\t"
    "ds_read_b64_tr_b16 v[36:37], v13 offset:24584\n\t"
    "ds_read_b64_tr_b16 v[38:39], v13 offset:28680\n\t"
    "s_waitcnt lgkmcnt(6)\n\t"
    "v_mfma_f32_16x16x32_bf16 v[24:27], v[40:43], v[56:59], v[24:27]\n\t"
    "v_mfma_f32_16x16x32_bf16 v[28:31], v[44:47], v[56:59], v[28:31]\n\t"
    "s_waitcnt lgkmcnt(4)\n\t"
    "v_mfma_f32_16x16x32_bf16 v[24:27], v[48:51], v[60:63], v[24:27]\n\t"
    "v_mfma_f32_16x16x32_bf16 v[28:31], v[52:55], v[60:63], v[28:31]\n\t"
    "s_setprio 1\n\t"
    "v_mov_b32 v52, %[one]\n\t"
    "v_mov_b32 v53, %[one]\n\t"
    "v_mov_b32 v54, %[one]\n\t"
    "v_mov_b32 v55, %[one]\n\t"
    "s_nop 2\n\t"
    "v_max3_f32 v10, |v24|, |v25|, |v26|\n\t"
    "v_max3_f32 v40, |v27|, |v28|, |v29|\n\t"
    "v_max3_f32 v10, v10, |v30|, |v31|\n\t"
    "v_max_f32 v10, v10, v40\n\t"
    "v_cmp_nge_f32 vcc, %[big], v10\n\t"
    "s_mov_b32 %[bg], %[big]\n\t"
    "v_mov_b32 v10, 0\n\t"
    "s_cbranch_vccnz 12f\n\t"
    "13:\n\t"
    "v_exp_f32 v24, v24\n\t"
    "v_exp_f32 v25, v25\n\t"
    "v_exp_f32 v26, v26\n\t"
    "v_exp_f32 v27, v27\n\t"
    "v_exp_f32 v28, v28\n\t"
    "v_exp_f32 v29, v29\n\t"
    "v_exp_f32 v30, v30\n\t"
    "v_exp_f32 v31, v31\n\t"
    "v_cvt_pk_bf16_f32 v40, v24, v25\n\t"
    "v_cvt_pk_bf16_f32 v41, v26, v27\n\t"
    "v_cvt_pk_bf16_f32 v42, v28, v29\n\t"
    "v_cvt_pk_bf16_f32 v43, v30, v31\n\t"
    "s_setprio 3\n\t"
    "s_waitcnt lgkmcnt(0)\n\t"
    "s_nop 0\n\t"
    "v_mfma_f32_16x16x32_bf16 v[44:47], v[32:35], v[40:43], 0\n\t"
    "v_mfma_f32_16x16x32_bf16 v[48:51], v[36:39], v[40:43], 0\n\t"
    "v_mfma_f32_16x16x32_bf16 v[52:55], v[52:55], v[40:43], 0\n\t"
    "v_mov_b32 v12, 0\n\t"
    "v_mov_b32 v13, 0\n\t"
    "s_mov_b32 exec_hi, 0\n\t"
    "v_mov_b32 v12, %[one]\n\t"
    "v_mov_b32 v13, %[one]\n\t"
    "s_mov_b32 exec_hi, -1\n\t"
    "s_barrier\n\t"
    "ds_read_b64_tr_b16 v[24:25], v11 offset:34816\n\t"
    "ds_read_b64_tr_b16 v[26:27], v11 offset:34880\n\t"
    "ds_read_b64_tr_b16 v[28:29], v11 offset:35328\n\t"
    "ds_read_b64_tr_b16 v[30:31], v11 offset:35392\n\t"
    "ds_read_b64_tr_b16 v[32:33], v11 offset:35840\n\t"
    "ds_read_b64_tr_b16 v[34:35], v11 offset:35904\n\t"
    "ds_read_b64_tr_b16 v[36:37], v11 offset:36352\n\t"
    "ds_read_b64_tr_b16 v[38:39], v11 offset:36416\n\t"
    "s_waitcnt lgkmcnt(6)\n\t"
    "v_mfma_f32_16x16x32_bf16 v[40:43], v[24:27], v[16:19], 0\n\t"
    "s_waitcnt lgkmcnt(4)\n\t"
    "v_mfma_f32_16x16x32_bf16 v[40:43], v[28:31], v[20:23], v[40:43]\n\t"
    "s_waitcnt lgkmcnt(2)\n\t"
    "v_mfma_f32_16x16x32_bf16 v[40:43], v[32:35], v[56:59], v[40:43]\n\t"
    "s_waitcnt lgkmcnt(0)\n\t"
    "v_mfma_f32_16x16x32_bf16 v[40:43], v[36:39], v[60:63], v[40:43]\n\t"
    "5:\n\t"
    "s_waitcnt vmcnt(0)\n\t"
    "v_max3_u32 v11, v6, v7, v8\n\t"
    "v_max_u32 v11, v11, v9\n\t"
    "v_cmp_eq_u32 vcc, -1, v11\n\t"
    "s_cbranch_vccnz 4f\n\t"
    "6:\n\t"
    "s_nop 2\n\t"
    "v_exp_f32 v28, v40\n\t"
    "v_max3_f32 v11, v40, v41, v42\n\t"
    "v_exp_f32 v29, v41\n\t"
    "v_max_f32 v11, v11, v43\n\t"
    "v_exp_f32 v30, v42\n\t"
    "v_cmp_nge_f32 vcc, %[bg], v11\n\t"
    "v_exp_f32 v31, v43\n\t"
    "s_cbranch_vccnz 10f\n\t"
    "11:\n\t"
    "v_cvt_pk_bf16_f32 v24, v28, v29\n\t"
    "v_cvt_pk_bf16_f32 v25, v30, v31\n\t"
    "s_nop 1\n\t"
    "v_mfma_f32_16x16x16_bf16 v[52:55], v[12:13], v[24:25], v[52:55]\n\t"
    "v_mfma_f32_16x16x16_bf16 v[44:47], v[6:7], v[24:25], v[44:47]\n\t"
    "v_mfma_f32_16x16x16_bf16 v[48:51], v[8:9], v[24:25], v[48:51]\n\t"
    "s_nop 5\n\t"
    "v_rcp_f32 v26, v52\n\t"
    "s_nop 0\n\t"
    "v_pk_mul_f32 v[44:45], v[44:45], v[26:27] op_sel_hi:[1,0]\n\t"
    "v_pk_mul_f32 v[46:47], v[46:47], v[26:27] op_sel_hi:[1,0]\n\t"
    "v_pk_mul_f32 v[48:49], v[48:49], v[26:27] op_sel_hi:[1,0]\n\t"
    "v_pk_mul_f32 v[50:51], v[50:51], v[26:27] op_sel_hi:[1,0]\n\t"
    "v_cvt_pk_bf16_f32 v36, v44, v45\n\t"
    "v_cvt_pk_bf16_f32 v37, v46, v47\n\t"
    "v_cvt_pk_bf16_f32 v38, v48, v49\n\t"
    "v_cvt_pk_bf16_f32 v39, v50, v51\n\t"
    "v_min_u32 v36, -2, v36\n\t"
    "v_min_u32 v37, -2, v37\n\t"
    "v_min_u32 v38, -2, v38\n\t"
    "v_min_u32 v39, -2, v39\n\t"
    "global_store_dwordx4 v15, v[36:39], %[od] sc1\n\t"
    "s_barrier\n\t"
    "s_mov_b32 exec_hi, 0\n\t"
    "global_store_dwordx4 v14, v[6:9], %[kv] sc1\n\t"
    "s_mov_b32 exec_hi, -1\n\t"
    "s_nop 0\n\t"
    "s_waitcnt lgkmcnt(0)\n\t"
    "s_branch 9f\n\t"
    "1:\n\t"
    "s_add_u32 %[sp], %[sp], 1\n\t"
    "s_cmp_ge_u32 %[sp], %[cap]\n\t"
    "s_cbranch_scc1 7f\n\t"
    "global_load_dwordx4 v[40:43], v6, %[ag] sc1\n\t"
    "global_load_dwordx4 v[44:47], v6, %[ag] offset:1024 sc1\n\t"
    "s_waitcnt lgkmcnt(0)\n\t"
    "s_memtime %[tc]\n\t"
    "s_nop 7\n\t"
    "s_branch 3b\n\t"
    "7:\n\t"
    "s_mov_b32 %[cap], 0\n\t"
    "s_branch 2b\n\t"
    "20:\n\t"
    "s_barrier\n\t"
    "s_barrier\n\t"
    "s_barrier\n\t"
    "s_branch 9f\n\t"
    "4:\n\t"
    "s_add_u32 %[sp], %[sp], 0x10000\n\t"
    "s_lshr_b32 %[st], %[sp], 16\n\t"
    "s_cmp_ge_u32 %[st], %[cap]\n\t"
    "s_cbranch_scc1 8f\n\t"
    "s_mov_b32 exec_hi, 0\n\t"
    "global_load_dwordx4 v[6:9], v14, %[vs] sc1\n\t"
    "s_mov_b32 exec_hi, -1\n\t"
    "s_branch 5b\n\t"
    "8:\n\t"
    "s_mov_b32 %[cap], 0\n\t"
    "s_branch 6b\n\t"
    "10:\n\t"
    "v_max_f32 v11, v11, v10\n\t"
    "v_mov_b32 v26, v11\n\t"
    "s_nop 1\n\t"
    "v_permlane16_swap_b32 v11, v26\n\t"
    "v_max_f32 v11, v11, v26\n\t"
    "v_sub_f32 v28, v40, v11\n\t"
    "v_sub_f32 v29, v41, v11\n\t"
    "v_sub_f32 v30, v42, v11\n\t"
    "v_sub_f32 v31, v43, v11\n\t"
    "v_sub_f32 v11, v10, v11\n\t"
    "v_exp_f32 v11, v11\n\t"
    "v_exp_f32 v28, v28\n\t"
    "v_exp_f32 v29, v29\n\t"
    "v_exp_f32 v30, v30\n\t"
    "v_exp_f32 v31, v31\n\t"
    "v_mul_f32 v44, v44, v11\n\t"
    "v_mul_f32 v45, v45, v11\n\t"
    "v_mul_f32 v46, v46, v11\n\t"
    "v_mul_f32 v47, v47, v11\n\t"
    "v_mul_f32 v48, v48, v11\n\t"
    "v_mul_f32 v49, v49, v11\n\t"
    "v_mul_f32 v50, v50, v11\n\t"
    "v_mul_f32 v51, v51, v11\n\t"
    "v_mul_f32 v52, v52, v11\n\t"
    "v_mul_f32 v53, v53, v11\n\t"
    "v_mul_f32 v54, v54, v11\n\t"
    "v_mul_f32 v55, v55, v11\n\t"
    "s_branch 11b\n\t"
    "12:\n\t"
    "v_max3_f32 v10, v24, v25, v26\n\t"
    "v_max3_f32 v40, v27, v28, v29\n\t"
    "v_max3_f32 v10, v10, v30, v31\n\t"
    "v_max3_f32 v10, v10, v40, %[neg]\n\t"
    "v_mov_b32 v40, v10\n\t"
    "s_nop 1\n\t"
    "v_permlane16_swap_b32 v10, v40\n\t"
    "v_max_f32 v10, v10, v40\n\t"
    "v_mov_b32 v40, v10\n\t"
    "s_nop 1\n\t"
    "v_permlane32_swap_b32 v10, v40\n\t"
    "v_max_f32 v10, v10, v40\n\t"
    "v_sub_f32 v24, v24, v10\n\t"
    "v_sub_f32 v25, v25, v10\n\t"
    "v_sub_f32 v26, v26, v10\n\t"
    "v_sub_f32 v27, v27, v10\n\t"
    "v_sub_f32 v28, v28, v10\n\t"
    "v_sub_f32 v29, v29, v10\n\t"
    "v_sub_f32 v30, v30, v10\n\t"
    "v_sub_f32 v31, v31, v10\n\t"
    "s_mov_b32 %[bg], 0x7fc00000\n\t"
    "s_branch 13b\n\t"
    "9:"
    : [tc] "=&s"(tc), [sp] "=&s"(sp), [cap] "+s"(cap), [st] "=&s"(st), [bg] "=&s"(bg)
    : [ag] "s"(ag), [c44] "s"(0x4444u), [kv] "s"(kv), [vs] "s"(vs), [od] "s"(od), [od2] "s"(od + 262144), [neg] "s"(-1.0e38f), [lsc] "s"(LSC), [one] "s"(0x3F803F80u), [big] "s"(24.0f), "{v[64:95]}"(W.w0), "{v[96:127]}"(W.w1), "{v[128:159]}"(W.w2), "{v[160:191]}"(W.w3), "{v[192:223]}"(W.w4), "{v[224:255]}"(W.w5)
    : "v6", "v7", "v8", "v9", "v10", "v11", "v12", "v13", "v14", "v15", "v16", "v17", "v18", "v19", "v20", "v21", "v22", "v23", "v24", "v25", "v26", "v27", "v28", "v29", "v30", "v31", "v32", "v33", "v34", "v35", "v36", "v37", "v38", "v39", "v40", "v41", "v42", "v43", "v44", "v45", "v46", "v47", "v48", "v49", "v50", "v51", "v52", "v53", "v54", "v55", "v56", "v57", "v58", "v59", "v60", "v61", "v62", "v63", "vcc", "scc", "memory");
}
template <> __device__ __forceinline__ void qk_step<2>(const QKW& W, const char* ag, const char* vs, const char* od, const char* kv, unsigned& cap, unsigned& sp,
                                                        unsigned long long& tc, unsigned long long* ts, unsigned& d0, unsigned& d1) {
  unsigned st, bg;
  asm volatile("v_mbcnt_lo_u32_b32 v60, -1, 0\n\t"
    "v_mbcnt_hi_u32_b32 v60, -1, v60\n\t"
    "v_lshlrev_b32 v6, 4, v60\n\t"
    "s_mov_b32 %[sp], 0\n\t"
    "s_setprio 3\n\t"
    "global_load_dwordx4 v[40:43], v6, %[ag] sc1\n\t"
    "global_load_dwordx4 v[44:47], v6, %[ag] offset:1024 sc1\n\t"
    "s_memtime %[tc]\n\t"
    "v_add_u32 v7, 45056, v6\n\t"
    "v_add_u32 v8, 102400, v6\n\t"
    "ds_read_b128 v[24:27], v7 offset:0\n\t"
    "ds_read_b128 v[28:31], v7 offset:1024\n\t"
    "ds_read_b128 v[32:35], v7 offset:8192\n\t"
    "ds_read_b128 v[36:39], v7 offset:9216\n\t"
    "v_lshrrev_b32 v48, 4, v60\n\t"
    "v_and_b32 v49, 15, v60\n\t"
    "v_and_b32 v61, 4, v60\n\t"
    "v_mul_u32_u24 v61, 0x2aaa, v61\n\t"
    "v_lshrrev_b32 v9, 1, v60\n\t"
    "v_and_b32 v9, 2, v9\n\t"
    "v_or3_b32 v9, v9, v61, %[c44]\n\t"
    "v_add_u32 v14, 1024, v6\n\t"
    "v_lshlrev_b32 v61, 2, v48\n\t"
    "v_lshl_or_b32 v10, v49, 4, v61\n\t"
    "v_lshrrev_b32 v50, 2, v49\n\t"
    "v_and_b32 v51, 3, v60\n\t"
    "v_and_b32 v61, 1, v60\n\t"
    "v_lshlrev_b32 v61, 3, v61\n\t"
    "v_lshl_or_b32 v11, v50, 4, v61\n\t"
    "v_lshl_or_b32 v11, v48, 7, v11\n\t"
    "v_xor_b32 v12, v48, v49\n\t"
    "v_lshlrev_b32 v12, 4, v12\n\t"
    "v_lshl_or_b32 v12, v49, 8, v12\n\t"
    "v_lshl_or_b32 v50, v48, 2, v50\n\t"
    "v_or_b32 v51, 8, v51\n\t"
    "v_xor_b32 v51, v51, v50\n\t"
    "v_lshlrev_b32 v13, 4, v51\n\t"
    "v_lshl_or_b32 v13, v50, 8, v13\n\t"
    "v_and_b32 v61, 3, v60\n\t"
    "v_lshlrev_b32 v61, 4, v61\n\t"
    "v_lshl_or_b32 v15, v48, 6, v61\n\t"
    "v_and_b32 v61, 8, v60\n\t"
    "v_lshl_or_b32 v15, v61, 14, v15\n\t"
    "v_and_b32 v61, 4, v60\n\t"
    "v_lshl_or_b32 v15, v61, 8, v15\n\t"
    "v_add_u32 v15, 512, v15\n\t"
    "3:\n\t"
    "v_mov_b32 v16, 0\n\t"
    "v_mov_b32 v17, 0\n\t"
    "v_mov_b32 v18, 0\n\t"
    "v_mov_b32 v19, 0\n\t"
    "v_mov_b32 v20, 0\n\t"
    "v_mov_b32 v21, 0\n\t"
    "v_mov_b32 v22, 0\n\t"
    "v_mov_b32 v23, 0\n\t"
    "s_waitcnt vmcnt(0) lgkmcnt(0)\n\t"
    "14:\n\t"
    "ds_write_b128 v6, v[40:43] offset:4096\n\t"
    "ds_write_b128 v6, v[44:47] offset:5120\n\t"
    "v_smfmac_f32_16x16x64_bf16 v[16:19], v[40:43], v[128:135], v9\n\t"
    "v_max3_u32 v60, v40, v41, v42\n\t"
    "v_max3_u32 v61, v43, v44, v45\n\t"
    "v_smfmac_f32_16x16x64_bf16 v[20:23], v[40:43], v[136:143], v9\n\t"
    "v_max3_u32 v60, v60, v46, v47\n\t"
    "v_max_u32 v60, v60, v61\n\t"
    "v_cmp_eq_u32 vcc, -1, v60\n\t"
    "s_cbranch_vccnz 1f\n\t"
    "2:\n\t"
    "s_waitcnt lgkmcnt(0)\n\t"
    "s_cmp_eq_u64 %[kv], 0\n\t"
    "s_barrier\n\t"
    "s_cbranch_scc1 20f\n\t"
    "ds_read_b128 v[40:43], v6 offset:12288\n\t"
    "ds_read_b128 v[48:51], v6 offset:0\n\t"
    "v_smfmac_f32_16x16x64_bf16 v[16:19], v[44:47], v[144:151], v9\n\t"
    "ds_read_b128 v[52:55], v6 offset:1024\n\t"
    "ds_read_b128 v[56:59], v6 offset:2048\n\t"
    "v_smfmac_f32_16x16x64_bf16 v[20:23], v[44:47], v[152:159], v9\n\t"
    "ds_read_b128 v[60:63], v6 offset:13312\n\t"
    "ds_read_b128 v[44:47], v6 offset:3072\n\t"
    "s_waitcnt lgkmcnt(5)\n\t"
    "v_smfmac_f32_16x16x64_bf16 v[16:19], v[40:43], v[24:31], v9\n\t"
    "ds_read_b128 v[24:27], v7 offset:16384\n\t"
    "ds_read_b128 v[28:31], v7 offset:17408\n\t"
    "v_smfmac_f32_16x16x64_bf16 v[20:23], v[40:43], v[32:39], v9\n\t"
    "ds_read_b128 v[32:35], v7 offset:24576\n\t"
    "ds_read_b128 v[36:39], v7 offset:25600\n\t"
    "ds_read_b128 v[40:43], v6 offset:6144\n\t"
    "s_waitcnt lgkmcnt(9)\n\t"
    "v_smfmac_f32_16x16x64_bf16 v[16:19], v[48:51], v[64:71], v9\n\t"
    "v_smfmac_f32_16x16x64_bf16 v[20:23], v[48:51], v[72:79], v9\n\t"
    "ds_read_b128 v[48:51], v6 offset:7168\n\t"
    "s_waitcnt lgkmcnt(9)\n\t"
    "v_smfmac_f32_16x16x64_bf16 v[16:19], v[52:55], v[80:87], v9\n\t"
    "v_smfmac_f32_16x16x64_bf16 v[20:23], v[52:55], v[88:95], v9\n\t"
    "ds_read_b128 v[52:55], v6 offset:14336\n\t"
    "s_waitcnt lgkmcnt(9)\n\t"
    "v_smfmac_f32_16x16x64_bf16 v[16:19], v[56:59], v[96:103], v9\n\t"
    "v_smfmac_f32_16x16x64_bf16 v[20:23], v[56:59], v[104:111], v9\n\t"
    "ds_read_b128 v[56:59], v6 offset:8192\n\t"
    "s_waitcnt lgkmcnt(6)\n\t"
    "v_smfmac_f32_16x16x64_bf16 v[16:19], v[60:63], v[24:31], v9\n\t"
    "ds_read_b128 v[24:27], v8 offset:0\n\t"
    "ds_read_b128 v[28:31], v8 offset:1024\n\t"
    "s_waitcnt lgkmcnt(6)\n\t"
    "v_smfmac_f32_16x16x64_bf16 v[20:23], v[60:63], v[32:39], v9\n\t"
    "ds_read_b128 v[32:35], v8 offset:8192\n\t"
    "ds_read_b128 v[36:39], v8 offset:9216\n\t"
    "ds_read_b128 v[60:63], v6 offset:9216\n\t"
    "v_smfmac_f32_16x16x64_bf16 v[16:19], v[44:47], v[112:119], v9\n\t"
    "v_smfmac_f32_16x16x64_bf16 v[20:23], v[44:47], v[120:127], v9\n\t"
    "ds_read_b128 v[44:47], v6 offset:10240\n\t"
    "s_waitcnt lgkmcnt(9)\n\t"
    "v_smfmac_f32_16x16x64_bf16 v[16:19], v[40:43], v[160:167], v9\n\t"
    "v_smfmac_f32_16x16x64_bf16 v[20:23], v[40:43], v[168:175], v9\n\t"
    "ds_read_b128 v[40:43], v6 offset:15360\n\t"
    "s_waitcnt lgkmcnt(9)\n\t"
    "v_smfmac_f32_16x16x64_bf16 v[16:19], v[48:51], v[176:183], v9\n\t"
    "v_smfmac_f32_16x16x64_bf16 v[20:23], v[48:51], v[184:191], v9\n\t"
    "ds_read_b128 v[48:51], v6 offset:11264\n\t"
    "s_waitcnt lgkmcnt(6)\n\t"
    "v_smfmac_f32_16x16x64_bf16 v[16:19], v[52:55], v[24:31], v9\n\t"
    "ds_read_b128 v[24:27], v8 offset:16384\n\t"
    "ds_read_b128 v[28:31], v8 offset:17408\n\t"
    "s_waitcnt lgkmcnt(6)\n\t"
    "v_smfmac_f32_16x16x64_bf16 v[20:23], v[52:55], v[32:39], v9\n\t"
    "ds_read_b128 v[32:35], v8 offset:24576\n\t"
    "ds_read_b128 v[36:39], v8 offset:25600\n\t"
    "v_smfmac_f32_16x16x64_bf16 v[16:19], v[56:59], v[192:199], v9\n\t"
    "v_smfmac_f32_16x16x64_bf16 v[20:23], v[56:59], v[200:207], v9\n\t"
    "s_waitcnt lgkmcnt(7)\n\t"
    "v_smfmac_f32_16x16x64_bf16 v[16:19], v[60:63], v[208:215], v9\n\t"
    "v_smfmac_f32_16x16x64_bf16 v[20:23], v[60:63], v[216:223], v9\n\t"
    "s_waitcnt lgkmcnt(6)\n\t"
    "v_smfmac_f32_16x16x64_bf16 v[16:19], v[44:47], v[224:231], v9\n\t"
    "v_smfmac_f32_16x16x64_bf16 v[20:23], v[44:47], v[232:239], v9\n\t"
    "s_waitcnt lgkmcnt(2)\n\t"
    "v_smfmac_f32_16x16x64_bf16 v[16:19], v[40:43], v[24:31], v9\n\t"
    "s_waitcnt lgkmcnt(0)\n\t"
    "v_smfmac_f32_16x16x64_bf16 v[20:23], v[40:43], v[32:39], v9\n\t"
    "v_smfmac_f32_16x16x64_bf16 v[16:19], v[48:51], v[240:247], v9\n\t"
    "v_smfmac_f32_16x16x64_bf16 v[20:23], v[48:51], v[248:255], v9\n\t"
    "v_xor_b32 v7, 64, v12\n\t"
    "v_xor_b32 v8, 0x80, v12\n\t"
    "v_xor_b32 v6, 0xc0, v12\n\t"
    "ds_read_b128 v[24:27], v12 offset:16384\n\t"
    "ds_read_b128 v[28:31], v12 offset:20480\n\t"
    "ds_read_b128 v[32:35], v7 offset:16384\n\t"
    "ds_read_b128 v[36:39], v7 offset:20480\n\t"
    "v_permlane16_swap_b32 v16, v18\n\t"
    "v_permlane16_swap_b32 v17, v19\n\t"
    "v_permlane16_swap_b32 v20, v22\n\t"
    "v_permlane16_swap_b32 v21, v23\n\t"
    "v_add_f32 v16, v16, v18\n\t"
    "v_add_f32 v17, v17, v19\n\t"
    "v_add_f32 v20, v20, v22\n\t"
    "v_mul_f32 v16, %[lsc], v16\n\t"
    "v_mul_f32 v17, %[lsc], v17\n\t"
    "v_cvt_pk_bf16_f32 v16, v16, v17\n\t"
    "v_add_f32 v21, v21, v23\n\t"
    "ds_write_b32 v10, v16 offset:33792\n\t"
    "v_mul_f32 v20, %[lsc], v20\n\t"
    "v_mul_f32 v21, %[lsc], v21\n\t"
    "v_cvt_pk_bf16_f32 v20, v20, v21\n\t"
    "ds_write_b32 v10, v20 offset:34048\n\t"
    "ds_read_b128 v[40:43], v8 offset:16384\n\t"
    "ds_read_b128 v[44:47], v8 offset:20480\n\t"
    "ds_read_b128 v[48:51], v6 offset:16384\n\t"
    "ds_read_b128 v[52:55], v6 offset:20480\n\t"
    "s_waitcnt lgkmcnt(4)\n\t"
    "s_barrier\n\t"
    "ds_read_b64_tr_b16 v[16:17], v11 offset:32768\n\t"
    "ds_read_b64_tr_b16 v[18:19], v11 offset:32832\n\t"
    "ds_read_b64_tr_b16 v[20:21], v11 offset:33280\n\t"
    "ds_read_b64_tr_b16 v[22:23], v11 offset:33344\n\t"
    "ds_read_b64_tr_b16 v[56:57], v11 offset:33792\n\t"
    "ds_read_b64_tr_b16 v[58:59], v11 offset:33856\n\t"
    "ds_read_b64_tr_b16 v[60:61], v11 offset:34304\n\t"
    "ds_read_b64_tr_b16 v[62:63], v11 offset:34368\n\t"
    "v_mov_b32 v6, 0\n\t"
    "v_mov_b32 v7, 0\n\t"
    "v_mov_b32 v8, 0\n\t"
    "v_mov_b32 v9, 0\n\t"
    "s_mov_b32 exec_hi, 0\n\t"
    "global_load_dwordx4 v[6:9], v14, %[vs] sc1\n\t"
    "s_mov_b32 exec_hi, -1\n\t"
    "s_waitcnt lgkmcnt(6)\n\t"
    "v_mfma_f32_16x16x32_bf16 v[24:27], v[24:27], v[16:19], 0\n\t"
    "v_mfma_f32_16x16x32_bf16 v[28:31], v[28:31], v[16:19], 0\n\t"
    "s_waitcnt lgkmcnt(4)\n\t"
    "v_mfma_f32_16x16x32_bf16 v[24:27], v[32:35], v[20:23], v[24:27]\n\t"
    "v_mfma_f32_16x16x32_bf16 v[28:31], v[36:39], v[20:23], v[28:31]\n\t"
    "ds_read_b64_tr_b16 v[32:33], v13 offset:24576\n\t"
    "ds_read_b64_tr_b16 v[34:35], v13 offset:28672\n\t"
    "ds_read_b64_tr_b16 v[36:37], v13 offset:24584\n\t"
    "ds_read_b64_tr_b16 v[38:39], v13 offset:28680\n\t"
    "s_waitcnt lgkmcnt(6)\n\t"
    "v_mfma_f32_16x16x32_bf16 v[24:27], v[40:43], v[56:59], v[24:27]\n\t"
    "v_mfma_f32_16x16x32_bf16 v[28:31], v[44:47], v[56:59], v[28:31]\n\t"
    "s_waitcnt lgkmcnt(4)\n\t"
    "v_mfma_f32_16x16x32_bf16 v[24:27], v[48:51], v[60:63], v[24:27]\n\t"
    "v_mfma_f32_16x16x32_bf16 v[28:31], v[52:55], v[60:63], v[28:31]\n\t"
    "s_setprio 1\n\t"
    "v_mov_b32 v52, %[one]\n\t"
    "v_mov_b32 v53, %[one]\n\t"
    "v_mov_b32 v54, %[one]\n\t"
    "v_mov_b32 v55, %[one]\n\t"
    "s_nop 2\n\t"
    "v_max3_f32 v10, |v24|, |v25|, |v26|\n\t"
    "v_max3_f32 v40, |v27|, |v28|, |v29|\n\t"
    "v_max3_f32 v10, v10, |v30|, |v31|\n\t"
    "v_max_f32 v10, v10, v40\n\t"
    "v_cmp_nge_f32 vcc, %[big], v10\n\t"
    "s_mov_b32 %[bg], %[big]\n\t"
    "v_mov_b32 v10, 0\n\t"
    "s_cbranch_vccnz 12f\n\t"
    "13:\n\t"
    "v_exp_f32 v24, v24\n\t"
    "v_exp_f32 v25, v25\n\t"
    "v_exp_f32 v26, v26\n\t"
    "v_exp_f32 v27, v27\n\t"
    "v_exp_f32 v28, v28\n\t"
    "v_exp_f32 v29, v29\n\t"
    "v_exp_f32 v30, v30\n\t"
    "v_exp_f32 v31, v31\n\t"
    "v_cvt_pk_bf16_f32 v40, v24, v25\n\t"
    "v_cvt_pk_bf16_f32 v41, v26, v27\n\t"
    "v_cvt_pk_bf16_f32 v42, v28, v29\n\t"
    "v_cvt_pk_bf16_f32 v43, v30, v31\n\t"
    "s_setprio 3\n\t"
    "s_waitcnt lgkmcnt(0)\n\t"
    "s_nop 0\n\t"
    "v_mfma_f32_16x16x32_bf16 v[44:47], v[32:35], v[40:43], 0\n\t"
    "v_mfma_f32_16x16x32_bf16 v[48:51], v[36:39], v[40:43], 0\n\t"
    "v_mfma_f32_16x16x32_bf16 v[52:55], v[52:55], v[40:43], 0\n\t"
    "v_mov_b32 v12, 0\n\t"
    "v_mov_b32 v13, 0\n\t"
    "s_mov_b32 exec_hi, 0\n\t"
    "v_mov_b32 v12, %[one]\n\t"
    "v_mov_b32 v13, %[one]\n\t"
    "s_mov_b32 exec_hi, -1\n\t"
    "s_barrier\n\t"
    "ds_read_b64_tr_b16 v[24:25], v11 offset:34816\n\t"
    "ds_read_b64_tr_b16 v[26:27], v11 offset:34880\n\t"
    "ds_read_b64_tr_b16 v[28:29], v11 offset:35328\n\t"
    "ds_read_b64_tr_b16 v[30:31], v11 offset:35392\n\t"
    "ds_read_b64_tr_b16 v[32:33], v11 offset:35840\n\t"
    "ds_read_b64_tr_b16 v[34:35], v11 offset:35904\n\t"
    "ds_read_b64_tr_b16 v[36:37], v11 offset:36352\n\t"
    "ds_read_b64_tr_b16 v[38:39], v11 offset:36416\n\t"
    "s_waitcnt lgkmcnt(6)\n\t"
    "v_mfma_f32_16x16x32_bf16 v[40:43], v[24:27], v[16:19], 0\n\t"
    "s_waitcnt lgkmcnt(4)\n\t"
    "v_mfma_f32_16x16x32_bf16 v[40:43], v[28:31], v[20:23], v[40:43]\n\t"
    "s_waitcnt lgkmcnt(2)\n\t"
    "v_mfma_f32_16x16x32_bf16 v[40:43], v[32:35], v[56:59], v[40:43]\n\t"
    "s_waitcnt lgkmcnt(0)\n\t"
    "v_mfma_f32_16x16x32_bf16 v[40:43], v[36:39], v[60:63], v[40:43]\n\t"
    "5:\n\t"
    "s_waitcnt vmcnt(0)\n\t"
    "v_max3_u32 v11, v6, v7, v8\n\t"
    "v_max_u32 v11, v11, v9\n\t"
    "v_cmp_eq_u32 vcc, -1, v11\n\t"
    "s_cbranch_vccnz 4f\n\t"
    "6:\n\t"
    "s_nop 2\n\t"
    "v_exp_f32 v28, v40\n\t"
    "v_max3_f32 v11, v40, v41, v42\n\t"
    "v_exp_f32 v29, v41\n\t"
    "v_max_f32 v11, v11, v43\n\t"
    "v_exp_f32 v30, v42\n\t"
    "v_cmp_nge_f32 vcc, %[bg], v11\n\t"
    "v_exp_f32 v31, v43\n\t"
    "s_cbranch_vccnz 10f\n\t"
    "11:\n\t"
    "v_cvt_pk_bf16_f32 v24, v28, v29\n\t"
    "v_cvt_pk_bf16_f32 v25, v30, v31\n\t"
    "s_nop 1\n\t"
    "v_mfma_f32_16x16x16_bf16 v[52:55], v[12:13], v[24:25], v[52:55]\n\t"
    "v_mfma_f32_16x16x16_bf16 v[44:47], v[6:7], v[24:25], v[44:47]\n\t"
    "v_mfma_f32_16x16x16_bf16 v[48:51], v[8:9], v[24:25], v[48:51]\n\t"
    "s_nop 5\n\t"
    "v_rcp_f32 v26, v52\n\t"
    "s_nop 0\n\t"
    "v_pk_mul_f32 v[44:45], v[44:45], v[26:27] op_sel_hi:[1,0]\n\t"
    "v_pk_mul_f32 v[46:47], v[46:47], v[26:27] op_sel_hi:[1,0]\n\t"
    "v_pk_mul_f32 v[48:49], v[48:49], v[26:27] op_sel_hi:[1,0]\n\t"
    "v_pk_mul_f32 v[50:51], v[50:51], v[26:27] op_sel_hi:[1,0]\n\t"
    "v_cvt_pk_bf16_f32 v36, v44, v45\n\t"
    "v_cvt_pk_bf16_f32 v37, v46, v47\n\t"
    "v_cvt_pk_bf16_f32 v38, v48, v49\n\t"
    "v_cvt_pk_bf16_f32 v39, v50, v51\n\t"
    "v_min_u32 v36, -2, v36\n\t"
    "v_min_u32 v37, -2, v37\n\t"
    "v_min_u32 v38, -2, v38\n\t"
    "v_min_u32 v39, -2, v39\n\t"
    "global_store_dwordx4 v15, v[36:39], %[od] sc1\n\t"
    "s_barrier\n\t"
    "s_mov_b32 exec_hi, 0\n\t"
    "global_store_dwordx4 v14, v[6:9], %[kv] sc1\n\t"
    "s_mov_b32 exec_hi, -1\n\t"
    "s_nop 0\n\t"
    "s_waitcnt lgkmcnt(0)\n\t"
    "s_branch 9f\n\t"
    "1:\n\t"
    "s_add_u32 %[sp], %[sp], 1\n\t"
    "s_cmp_ge_u32 %[sp], %[cap]\n\t"
    "s_cbranch_scc1 7f\n\t"
    "global_load_dwordx4 v[40:43], v6, %[ag] sc1\n\t"
    "global_load_dwordx4 v[44:47], v6, %[ag] offset:1024 sc1\n\t"
    "s_waitcnt lgkmcnt(0)\n\t"
    "s_memtime %[tc]\n\t"
    "s_nop 7\n\t"
    "s_branch 3b\n\t"
    "7:\n\t"
    "s_mov_b32 %[cap], 0\n\t"
    "s_branch 2b\n\t"
    "20:\n\t"
    "s_barrier\n\t"
    "s_barrier\n\t"
    "s_barrier\n\t"
    "s_branch 9f\n\t"
    "4:\n\t"
    "s_add_u32 %[sp], %[sp], 0x10000\n\t"
    "s_lshr_b32 %[st], %[sp], 16\n\t"
    "s_cmp_ge_u32 %[st], %[cap]\n\t"
    "s_cbranch_scc1 8f\n\t"
    "s_mov_b32 exec_hi, 0\n\t"
    "global_load_dwordx4 v[6:9], v14, %[vs] sc1\n\t"
    "s_mov_b32 exec_hi, -1\n\t"
    "s_branch 5b\n\t"
    "8:\n\t"
    "s_mov_b32 %[cap], 0\n\t"
    "s_branch 6b\n\t"
    "10:\n\t"
    "v_max_f32 v11, v11, v10\n\t"
    "v_mov_b32 v26, v11\n\t"
    "s_nop 1\n\t"
    "v_permlane16_swap_b32 v11, v26\n\t"
    "v_max_f32 v11, v11, v26\n\t"
    "v_sub_f32 v28, v40, v11\n\t"
    "v_sub_f32 v29, v41, v11\n\t"
    "v_sub_f32 v30, v42, v11\n\t"
    "v_sub_f32 v31, v43, v11\n\t"
    "v_sub_f32 v11, v10, v11\n\t"
    "v_exp_f32 v11, v11\n\t"
    "v_exp_f32 v28, v28\n\t"
    "v_exp_f32 v29, v29\n\t"
    "v_exp_f32 v30, v30\n\t"
    "v_exp_f32 v31, v31\n\t"
    "v_mul_f32 v44, v44, v11\n\t"
    "v_mul_f32 v45, v45, v11\n\t"
    "v_mul_f32 v46, v46, v11\n\t"
    "v_mul_f32 v47, v47, v11\n\t"
    "v_mul_f32 v48, v48, v11\n\t"
    "v_mul_f32 v49, v49, v11\n\t"
    "v_mul_f32 v50, v50, v11\n\t"
    "v_mul_f32 v51, v51, v11\n\t"
    "v_mul_f32 v52, v52, v11\n\t"
    "v_mul_f32 v53, v53, v11\n\t"
    "v_mul_f32 v54, v54, v11\n\t"
    "v_mul_f32 v55, v55, v11\n\t"
    "s_branch 11b\n\t"
    "12:\n\t"
    "v_max3_f32 v10, v24, v25, v26\n\t"
    "v_max3_f32 v40, v27, v28, v29\n\t"
    "v_max3_f32 v10, v10, v30, v31\n\t"
    "v_max3_f32 v10, v10, v40, %[neg]\n\t"
    "v_mov_b32 v40, v10\n\t"
    "s_nop 1\n\t"
    "v_permlane16_swap_b32 v10, v40\n\t"
    "v_max_f32 v10, v10, v40\n\t"
    "v_mov_b32 v40, v10\n\t"
    "s_nop 1\n\t"
    "v_permlane32_swap_b32 v10, v40\n\t"
    "v_max_f32 v10, v10, v40\n\t"
    "v_sub_f32 v24, v24, v10\n\t"
    "v_sub_f32 v25, v25, v10\n\t"
    "v_sub_f32 v26, v26, v10\n\t"
    "v_sub_f32 v27, v27, v10\n\t"
    "v_sub_f32 v28, v28, v10\n\t"
    "v_sub_f32 v29, v29, v10\n\t"
    "v_sub_f32 v30, v30, v10\n\t"
    "v_sub_f32 v31, v31, v10\n\t"
    "s_mov_b32 %[bg], 0x7fc00000\n\t"
    "s_branch 13b\n\t"
    "9:"
    : [tc] "=&s"(tc), [sp] "=&s"(sp), [cap] "+s"(cap), [st] "=&s"(st), [bg] "=&s"(bg)
    : [ag] "s"(ag), [c44] "s"(0x4444u), [kv] "s"(kv), [vs] "s"(vs), [od] "s"(od), [od2] "s"(od + 262144), [neg] "s"(-1.0e38f), [lsc] "s"(LSC), [one] "s"(0x3F803F80u), [big] "s"(24.0f), "{v[64:95]}"(W.w0), "{v[96:127]}"(W.w1), "{v[128:159]}"(W.w2), "{v[160:191]}"(W.w3), "{v[192:223]}"(W.w4), "{v[224:255]}"(W.w5)
    : "v6", "v7", "v8", "v9", "v10", "v11", "v12", "v13", "v14", "v15", "v16", "v17", "v18", "v19", "v20", "v21", "v22", "v23", "v24", "v25", "v26", "v27", "v28", "v29", "v30", "v31", "v32", "v33", "v34", "v35", "v36", "v37", "v38", "v39", "v40", "v41", "v42", "v43", "v44", "v45", "v46", "v47", "v48", "v49", "v50", "v51", "v52", "v53", "v54", "v55", "v56", "v57", "v58", "v59", "v60", "v61", "v62", "v63", "vcc", "scc", "memory");
}
template <> __device__ __forceinline__ void qk_step<3>(const QKW& W, const char* ag, const char* vs, const char* od, const char* kv, unsigned& cap, unsigned& sp,
                                                        unsigned long long& tc, unsigned long long* ts, unsigned& d0, unsigned& d1) {
  unsigned st, bg;
  asm volatile("v_mbcnt_lo_u32_b32 v60, -1, 0\n\t"
    "v_mbcnt_hi_u32_b32 v60, -1, v60\n\t"
    "v_lshlrev_b32 v6, 4, v60\n\t"
    "s_mov_b32 %[sp], 0\n\t"
    "s_setprio 3\n\t"
    "global_load_dwordx4 v[40:43], v6, %[ag] sc1\n\t"
    "global_load_dwordx4 v[44:47], v6, %[ag] offset:1024 sc1\n\t"
    "s_memtime %[tc]\n\t"
    "v_add_u32 v7, 47104, v6\n\t"
    "v_add_u32 v8, 104448, v6\n\t"
    "ds_read_b128 v[24:27], v7 offset:0\n\t"
    "ds_read_b128 v[28:31], v7 offset:1024\n\t"
    "ds_read_b128 v[32:35], v7 offset:8192\n\t"
    "ds_read_b128 v[36:39], v7 offset:9216\n\t"
    "v_lshrrev_b32 v48, 4, v60\n\t"
    "v_and_b32 v49, 15, v60\n\t"
    "v_and_b32 v61, 4, v60\n\t"
    "v_mul_u32_u24 v61, 0x2aaa, v61\n\t"
    "v_lshrrev_b32 v9, 1, v60\n\t"
    "v_and_b32 v9, 2, v9\n\t"
    "v_or3_b32 v9, v9, v61, %[c44]\n\t"
    "v_add_u32 v14, 1536, v6\n\t"
    "v_lshlrev_b32 v61, 2, v48\n\t"
    "v_lshl_or_b32 v10, v49, 4, v61\n\t"
    "v_lshrrev_b32 v50, 2, v49\n\t"
    "v_and_b32 v51, 3, v60\n\t"
    "v_and_b32 v61, 1, v60\n\t"
    "v_lshlrev_b32 v61, 3, v61\n\t"
    "v_lshl_or_b32 v11, v50, 4, v61\n\t"
    "v_lshl_or_b32 v11, v48, 7, v11\n\t"
    "v_xor_b32 v12, v48, v49\n\t"
    "v_lshlrev_b32 v12, 4, v12\n\t"
    "v_lshl_or_b32 v12, v49, 8, v12\n\t"
    "v_lshl_or_b32 v50, v48, 2, v50\n\t"
    "v_or_b32 v51, 12, v51\n\t"
    "v_xor_b32 v51, v51, v50\n\t"
    "v_lshlrev_b32 v13, 4, v51\n\t"
    "v_lshl_or_b32 v13, v50, 8, v13\n\t"
    "v_and_b32 v61, 3, v60\n\t"
    "v_lshlrev_b32 v61, 4, v61\n\t"
    "v_lshl_or_b32 v15, v48, 6, v61\n\t"
    "v_and_b32 v61, 8, v60\n\t"
    "v_lshl_or_b32 v15, v61, 14, v15\n\t"
    "v_and_b32 v61, 4, v60\n\t"
    "v_lshl_or_b32 v15, v61, 8, v15\n\t"
    "v_add_u32 v15, 768, v15\n\t"
    "3:\n\t"
    "v_mov_b32 v16, 0\n\t"
    "v_mov_b32 v17, 0\n\t"
    "v_mov_b32 v18, 0\n\t"
    "v_mov_b32 v19, 0\n\t"
    "v_mov_b32 v20, 0\n\t"
    "v_mov_b32 v21, 0\n\t"
    "v_mov_b32 v22, 0\n\t"
    "v_mov_b32 v23, 0\n\t"
    "s_waitcnt vmcnt(0) lgkmcnt(0)\n\t"
    "14:\n\t"
    "ds_write_b128 v6, v[40:43] offset:6144\n\t"
    "ds_write_b128 v6, v[44:47] offset:7168\n\t"
    "v_smfmac_f32_16x16x64_bf16 v[16:19], v[40:43], v[160:167], v9\n\t"
    "v_max3_u32 v60, v40, v41, v42\n\t"
    "v_max3_u32 v61, v43, v44, v45\n\t"
    "v_smfmac_f32_16x16x64_bf16 v[20:23], v[40:43], v[168:175], v9\n\t"
    "v_max3_u32 v60, v60, v46, v47\n\t"
    "v_max_u32 v60, v60, v61\n\t"
    "v_cmp_eq_u32 vcc, -1, v60\n\t"
    "s_cbranch_vccnz 1f\n\t"
    "2:\n\t"
    "s_waitcnt lgkmcnt(0)\n\t"
    "s_cmp_eq_u64 %[kv], 0\n\t"
    "s_barrier\n\t"
    "s_cbranch_scc1 20f\n\t"
    "ds_read_b128 v[40:43], v6 offset:12288\n\t"
    "ds_read_b128 v[48:51], v6 offset:0\n\t"
    "v_smfmac_f32_16x16x64_bf16 v[16:19], v[44:47], v[176:183], v9\n\t"
    "ds_read_b128 v[52:55], v6 offset:1024\n\t"
    "ds_read_b128 v[56:59], v6 offset:2048\n\t"
    "v_smfmac_f32_16x16x64_bf16 v[20:23], v[44:47], v[184:191], v9\n\t"
    "ds_read_b128 v[60:63], v6 offset:13312\n\t"
    "ds_read_b128 v[44:47], v6 offset:3072\n\t"
    "s_waitcnt lgkmcnt(5)\n\t"
    "v_smfmac_f32_16x16x64_bf16 v[16:19], v[40:43], v[24:31], v9\n\t"
    "ds_read_b128 v[24:27], v7 offset:16384\n\t"
    "ds_read_b128 v[28:31], v7 offset:17408\n\t"
    "v_smfmac_f32_16x16x64_bf16 v[20:23], v[40:43], v[32:39], v9\n\t"
    "ds_read_b128 v[32:35], v7 offset:24576\n\t"
    "ds_read_b128 v[36:39], v7 offset:25600\n\t"
    "ds_read_b128 v[40:43], v6 offset:4096\n\t"
    "s_waitcnt lgkmcnt(9)\n\t"
    "v_smfmac_f32_16x16x64_bf16 v[16:19], v[48:51], v[64:71], v9\n\t"
    "v_smfmac_f32_16x16x64_bf16 v[20:23], v[48:51], v[72:79], v9\n\t"
    "ds_read_b128 v[48:51], v6 offset:5120\n\t"
    "s_waitcnt lgkmcnt(9)\n\t"
    "v_smfmac_f32_16x16x64_bf16 v[16:19], v[52:55], v[80:87], v9\n\t"
    "v_smfmac_f32_16x16x64_bf16 v[20:23], v[52:55], v[88:95], v9\n\t"
    "ds_read_b128 v[52:55], v6 offset:14336\n\t"
    "s_waitcnt lgkmcnt(9)\n\t"
    "v_smfmac_f32_16x16x64_bf16 v[16:19], v[56:59], v[96:103], v9\n\t"
    "v_smfmac_f32_16x16x64_bf16 v[20:23], v[56:59], v[104:111], v9\n\t"
    "ds_read_b128 v[56:59], v6 offset:8192\n\t"
    "s_waitcnt lgkmcnt(6)\n\t"
    "v_smfmac_f32_16x16x64_bf16 v[16:19], v[60:63], v[24:31], v9\n\t"
    "ds_read_b128 v[24:27], v8 offset:0\n\t"
    "ds_read_b128 v[28:31], v8 offset:1024\n\t"
    "s_waitcnt lgkmcnt(6)\n\t"
    "v_smfmac_f32_16x16x64_bf16 v[20:23], v[60:63], v[32:39], v9\n\t"
    "ds_read_b128 v[32:35], v8 offset:8192\n\t"
    "ds_read_b128 v[36:39], v8 offset:9216\n\t"
    "ds_read_b128 v[60:63], v6 offset:9216\n\t"
    "v_smfmac_f32_16x16x64_bf16 v[16:19], v[44:47], v[112:119], v9\n\t"
    "v_smfmac_f32_16x16x64_bf16 v[20:23], v[44:47], v[120:127], v9\n\t"
    "ds_read_b128 v[44:47], v6 offset:10240\n\t"
    "s_waitcnt lgkmcnt(9)\n\t"
    "v_smfmac_f32_16x16x64_bf16 v[16:19], v[40:43], v[128:135], v9\n\t"
    "v_smfmac_f32_16x16x64_bf16 v[20:23], v[40:43], v[136:143], v9\n\t"
    "ds_read_b128 v[40:43], v6 offset:15360\n\t"
    "s_waitcnt lgkmcnt(9)\n\t"
    "v_smfmac_f32_16x16x64_bf16 v[16:19], v[48:51], v[144:151], v9\n\t"
    "v_smfmac_f32_16x16x64_bf16 v[20:23], v[48:51], v[152:159], v9\n\t"
    "ds_read_b128 v[48:51], v6 offset:11264\n\t"
    "s_waitcnt lgkmcnt(6)\n\t"
    "v_smfmac_f32_16x16x64_bf16 v[16:19], v[52:55], v[24:31], v9\n\t"
    "ds_read_b128 v[24:27], v8 offset:16384\n\t"
    "ds_read_b128 v[28:31], v8 offset:17408\n\t"
    "s_waitcnt lgkmcnt(6)\n\t"
    "v_smfmac_f32_16x16x64_bf16 v[20:23], v[52:55], v[32:39], v9\n\t"
    "ds_read_b128 v[32:35], v8 offset:24576\n\t"
    "ds_read_b128 v[36:39], v8 offset:25600\n\t"
    "v_smfmac_f32_16x16x64_bf16 v[16:19], v[56:59], v[192:199], v9\n\t"
    "v_smfmac_f32_16x16x64_bf16 v[20:23], v[56:59], v[200:207], v9\n\t"
    "s_waitcnt lgkmcnt(7)\n\t"
    "v_smfmac_f32_16x16x64_bf16 v[16:19], v[60:63], v[208:215], v9\n\t"
    "v_smfmac_f32_16x16x64_bf16 v[20:23], v[60:63], v[216:223], v9\n\t"
    "s_waitcnt lgkmcnt(6)\n\t"
    "v_smfmac_f32_16x16x64_bf16 v[16:19], v[44:47], v[224:231], v9\n\t"
    "v_smfmac_f32_16x16x64_bf16 v[20:23], v[44:47], v[232:239], v9\n\t"
    "s_waitcnt lgkmcnt(2)\n\t"
    "v_smfmac_f32_16x16x64_bf16 v[16:19], v[40:43], v[24:31], v9\n\t"
    "s_waitcnt lgkmcnt(0)\n\t"
    "v_smfmac_f32_16x16x64_bf16 v[20:23], v[40:43], v[32:39], v9\n\t"
    "v_smfmac_f32_16x16x64_bf16 v[16:19], v[48:51], v[240:247], v9\n\t"
    "v_smfmac_f32_16x16x64_bf16 v[20:23], v[48:51], v[248:255], v9\n\t"
    "v_xor_b32 v7, 64, v12\n\t"
    "v_xor_b32 v8, 0x80, v12\n\t"
    "v_xor_b32 v6, 0xc0, v12\n\t"
    "ds_read_b128 v[24:27], v12 offset:16384\n\t"
    "ds_read_b128 v[28:31], v12 offset:20480\n\t"
    "ds_read_b128 v[32:35], v7 offset:16384\n\t"
    "ds_read_b128 v[36:39], v7 offset:20480\n\t"
    "v_permlane16_swap_b32 v16, v18\n\t"
    "v_permlane16_swap_b32 v17, v19\n\t"
    "v_permlane16_swap_b32 v20, v22\n\t"
    "v_permlane16_swap_b32 v21, v23\n\t"
    "v_add_f32 v16, v16, v18\n\t"
    "v_add_f32 v17, v17, v19\n\t"
    "v_add_f32 v20, v20, v22\n\t"
    "v_mul_f32 v16, %[lsc], v16\n\t"
    "v_mul_f32 v17, %[lsc], v17\n\t"
    "v_cvt_pk_bf16_f32 v16, v16, v17\n\t"
    "v_add_f32 v21, v21, v23\n\t"
    "ds_write_b32 v10, v16 offset:34304\n\t"
    "v_mul_f32 v20, %[lsc], v20\n\t"
    "v_mul_f32 v21, %[lsc], v21\n\t"
    "v_cvt_pk_bf16_f32 v20, v20, v21\n\t"
    "ds_write_b32 v10, v20 offset:34560\n\t"
    "ds_read_b128 v[40:43], v8 offset:16384\n\t"
    "ds_read_b128 v[44:47], v8 offset:20480\n\t"
    "ds_read_b128 v[48:51], v6 offset:16384\n\t"
    "ds_read_b128 v[52:55], v6 offset:20480\n\t"
    "s_waitcnt lgkmcnt(4)\n\t"
    "s_barrier\n\t"
    "ds_read_b64_tr_b16 v[16:17], v11 offset:32768\n\t"
    "ds_read_b64_tr_b16 v[18:19], v11 offset:32832\n\t"
    "ds_read_b64_tr_b16 v[20:21], v11 offset:33280\n\t"
    "ds_read_b64_tr_b16 v[22:23], v11 offset:33344\n\t"
    "ds_read_b64_tr_b16 v[56:57], v11 offset:33792\n\t"
    "ds_read_b64_tr_b16 v[58:59], v11 offset:33856\n\t"
    "ds_read_b64_tr_b16 v[60:61], v11 offset:34304\n\t"
    "ds_read_b64_tr_b16 v[62:63], v11 offset:34368\n\t"
    "v_mov_b32 v6, 0\n\t"
    "v_mov_b32 v7, 0\n\t"
    "v_mov_b32 v8, 0\n\t"
    "v_mov_b32 v9, 0\n\t"
    "s_mov_b32 exec_hi, 0\n\t"
    "global_load_dwordx4 v[6:9], v14, %[vs] sc1\n\t"
    "s_mov_b32 exec_hi, -1\n\t"
    "s_waitcnt lgkmcnt(6)\n\t"
    "v_mfma_f32_16x16x32_bf16 v[24:27], v[24:27], v[16:19], 0\n\t"
    "v_mfma_f32_16x16x32_bf16 v[28:31], v[28:31], v[16:19], 0\n\t"
    "s_waitcnt lgkmcnt(4)\n\t"
    "v_mfma_f32_16x16x32_bf16 v[24:27], v[32:35], v[20:23], v[24:27]\n\t"
    "v_mfma_f32_16x16x32_bf16 v[28:31], v[36:39], v[20:23], v[28:31]\n\t"
    "ds_read_b64_tr_b16 v[32:33], v13 offset:24576\n\t"
    "ds_read_b64_tr_b16 v[34:35], v13 offset:28672\n\t"
    "ds_read_b64_tr_b16 v[36:37], v13 offset:24584\n\t"
    "ds_read_b64_tr_b16 v[38:39], v13 offset:28680\n\t"
    "s_waitcnt lgkmcnt(6)\n\t"
    "v_mfma_f32_16x16x32_bf16 v[24:27], v[40:43], v[56:59], v[24:27]\n\t"
    "v_mfma_f32_16x16x32_bf16 v[28:31], v[44:47], v[56:59], v[28:31]\n\t"
    "s_waitcnt lgkmcnt(4)\n\t"
    "v_mfma_f32_16x16x32_bf16 v[24:27], v[48:51], v[60:63], v[24:27]\n\t"
    "v_mfma_f32_16x16x32_bf16 v[28:31], v[52:55], v[60:63], v[28:31]\n\t"
    "s_setprio 1\n\t"
    "v_mov_b32 v52, %[one]\n\t"
    "v_mov_b32 v53, %[one]\n\t"
    "v_mov_b32 v54, %[one]\n\t"
    "v_mov_b32 v55, %[one]\n\t"
    "s_nop 2\n\t"
    "v_max3_f32 v10, |v24|, |v25|, |v26|\n\t"
    "v_max3_f32 v40, |v27|, |v28|, |v29|\n\t"
    "v_max3_f32 v10, v10, |v30|, |v31|\n\t"
    "v_max_f32 v10, v10, v40\n\t"
    "v_cmp_nge_f32 vcc, %[big], v10\n\t"
    "s_mov_b32 %[bg], %[big]\n\t"
    "v_mov_b32 v10, 0\n\t"
    "s_cbranch_vccnz 12f\n\t"
    "13:\n\t"
    "v_exp_f32 v24, v24\n\t"
    "v_exp_f32 v25, v25\n\t"
    "v_exp_f32 v26, v26\n\t"
    "v_exp_f32 v27, v27\n\t"
    "v_exp_f32 v28, v28\n\t"
    "v_exp_f32 v29, v29\n\t"
    "v_exp_f32 v30, v30\n\t"
    "v_exp_f32 v31, v31\n\t"
    "v_cvt_pk_bf16_f32 v40, v24, v25\n\t"
    "v_cvt_pk_bf16_f32 v41, v26, v27\n\t"
    "v_cvt_pk_bf16_f32 v42, v28, v29\n\t"
    "v_cvt_pk_bf16_f32 v43, v30, v31\n\t"
    "s_setprio 3\n\t"
    "s_waitcnt lgkmcnt(0)\n\t"
    "s_nop 0\n\t"
    "v_mfma_f32_16x16x32_bf16 v[44:47], v[32:35], v[40:43], 0\n\t"
    "v_mfma_f32_16x16x32_bf16 v[48:51], v[36:39], v[40:43], 0\n\t"
    "v_mfma_f32_16x16x32_bf16 v[52:55], v[52:55], v[40:43], 0\n\t"
    "v_mov_b32 v12, 0\n\t"
    "v_mov_b32 v13, 0\n\t"
    "s_mov_b32 exec_hi, 0\n\t"
    "v_mov_b32 v12, %[one]\n\t"
    "v_mov_b32 v13, %[one]\n\t"
    "s_mov_b32 exec_hi, -1\n\t"
    "s_barrier\n\t"
    "ds_read_b64_tr_b16 v[24:25], v11 offset:34816\n\t"
    "ds_read_b64_tr_b16 v[26:27], v11 offset:34880\n\t"
    "ds_read_b64_tr_b16 v[28:29], v11 offset:35328\n\t"
    "ds_read_b64_tr_b16 v[30:31], v11 offset:35392\n\t"
    "ds_read_b64_tr_b16 v[32:33], v11 offset:35840\n\t"
    "ds_read_b64_tr_b16 v[34:35], v11 offset:35904\n\t"
    "ds_read_b64_tr_b16 v[36:37], v11 offset:36352\n\t"
    "ds_read_b64_tr_b16 v[38:39], v11 offset:36416\n\t"
    "s_waitcnt lgkmcnt(6)\n\t"
    "v_mfma_f32_16x16x32_bf16 v[40:43], v[24:27], v[16:19], 0\n\t"
    "s_waitcnt lgkmcnt(4)\n\t"
    "v_mfma_f32_16x16x32_bf16 v[40:43], v[28:31], v[20:23], v[40:43]\n\t"
    "s_waitcnt lgkmcnt(2)\n\t"
    "v_mfma_f32_16x16x32_bf16 v[40:43], v[32:35], v[56:59], v[40:43]\n\t"
    "s_waitcnt lgkmcnt(0)\n\t"
    "v_mfma_f32_16x16x32_bf16 v[40:43], v[36:39], v[60:63], v[40:43]\n\t"
    "5:\n\t"
    "s_waitcnt vmcnt(0)\n\t"
    "v_max3_u32 v11, v6, v7, v8\n\t"
    "v_max_u32 v11, v11, v9\n\t"
    "v_cmp_eq_u32 vcc, -1, v11\n\t"
    "s_cbranch_vccnz 4f\n\t"
    "6:\n\t"
    "s_nop 2\n\t"
    "v_exp_f32 v28, v40\n\t"
    "v_max3_f32 v11, v40, v41, v42\n\t"
    "v_exp_f32 v29, v41\n\t"
    "v_max_f32 v11, v11, v43\n\t"
    "v_exp_f32 v30, v42\n\t"
    "v_cmp_nge_f32 vcc, %[bg], v11\n\t"
    "v_exp_f32 v31, v43\n\t"
    "s_cbranch_vccnz 10f\n\t"
    "11:\n\t"
    "v_cvt_pk_bf16_f32 v24, v28, v29\n\t"
    "v_cvt_pk_bf16_f32 v25, v30, v31\n\t"
    "s_nop 1\n\t"
    "v_mfma_f32_16x16x16_bf16 v[52:55], v[12:13], v[24:25], v[52:55]\n\t"
    "v_mfma_f32_16x16x16_bf16 v[44:47], v[6:7], v[24:25], v[44:47]\n\t"
    "v_mfma_f32_16x16x16_bf16 v[48:51], v[8:9], v[24:25], v[48:51]\n\t"
    "s_nop 5\n\t"
    "v_rcp_f32 v26, v52\n\t"
    "s_nop 0\n\t"
    "v_pk_mul_f32 v[44:45], v[44:45], v[26:27] op_sel_hi:[1,0]\n\t"
    "v_pk_mul_f32 v[46:47], v[46:47], v[26:27] op_sel_hi:[1,0]\n\t"
    "v_pk_mul_f32 v[48:49], v[48:49], v[26:27] op_sel_hi:[1,0]\n\t"
    "v_pk_mul_f32 v[50:51], v[50:51], v[26:27] op_sel_hi:[1,0]\n\t"
    "v_cvt_pk_bf16_f32 v36, v44, v45\n\t"
    "v_cvt_pk_bf16_f32 v37, v46, v47\n\t"
    "v_cvt_pk_bf16_f32 v38, v48, v49\n\t"
    "v_cvt_pk_bf16_f32 v39, v50, v51\n\t"
    "v_min_u32 v36, -2, v36\n\t"
    "v_min_u32 v37, -2, v37\n\t"
    "v_min_u32 v38, -2, v38\n\t"
    "v_min_u32 v39, -2, v39\n\t"
    "global_store_dwordx4 v15, v[36:39], %[od] sc1\n\t"
    "s_barrier\n\t"
    "s_mov_b32 exec_hi, 0\n\t"
    "global_store_dwordx4 v14, v[6:9], %[kv] sc1\n\t"
    "s_mov_b32 exec_hi, -1\n\t"
    "s_nop 0\n\t"
    "s_waitcnt lgkmcnt(0)\n\t"
    "s_branch 9f\n\t"
    "1:\n\t"
    "s_add_u32 %[sp], %[sp], 1\n\t"
    "s_cmp_ge_u32 %[sp], %[cap]\n\t"
    "s_cbranch_scc1 7f\n\t"
    "global_load_dwordx4 v[40:43], v6, %[ag] sc1\n\t"
    "global_load_dwordx4 v[44:47], v6, %[ag] offset:1024 sc1\n\t"
    "s_waitcnt lgkmcnt(0)\n\t"
    "s_memtime %[tc]\n\t"
    "s_nop 7\n\t"
    "s_branch 3b\n\t"
    "7:\n\t"
    "s_mov_b32 %[cap], 0\n\t"
    "s_branch 2b\n\t"
    "20:\n\t"
    "s_barrier\n\t"
    "s_barrier\n\t"
    "s_barrier\n\t"
    "s_branch 9f\n\t"
    "4:\n\t"
    "s_add_u32 %[sp], %[sp], 0x10000\n\t"
    "s_lshr_b32 %[st], %[sp], 16\n\t"
    "s_cmp_ge_u32 %[st], %[cap]\n\t"
    "s_cbranch_scc1 8f\n\t"
    "s_mov_b32 exec_hi, 0\n\t"
    "global_load_dwordx4 v[6:9], v14, %[vs] sc1\n\t"
    "s_mov_b32 exec_hi, -1\n\t"
    "s_branch 5b\n\t"
    "8:\n\t"
    "s_mov_b32 %[cap], 0\n\t"
    "s_branch 6b\n\t"
    "10:\n\t"
    "v_max_f32 v11, v11, v10\n\t"
    "v_mov_b32 v26, v11\n\t"
    "s_nop 1\n\t"
    "v_permlane16_swap_b32 v11, v26\n\t"
    "v_max_f32 v11, v11, v26\n\t"
    "v_sub_f32 v28, v40, v11\n\t"
    "v_sub_f32 v29, v41, v11\n\t"
    "v_sub_f32 v30, v42, v11\n\t"
    "v_sub_f32 v31, v43, v11\n\t"
    "v_sub_f32 v11, v10, v11\n\t"
    "v_exp_f32 v11, v11\n\t"
    "v_exp_f32 v28, v28\n\t"
    "v_exp_f32 v29, v29\n\t"
    "v_exp_f32 v30, v30\n\t"
    "v_exp_f32 v31, v31\n\t"
    "v_mul_f32 v44, v44, v11\n\t"
    "v_mul_f32 v45, v45, v11\n\t"
    "v_mul_f32 v46, v46, v11\n\t"
    "v_mul_f32 v47, v47, v11\n\t"
    "v_mul_f32 v48, v48, v11\n\t"
    "v_mul_f32 v49, v49, v11\n\t"
    "v_mul_f32 v50, v50, v11\n\t"
    "v_mul_f32 v51, v51, v11\n\t"
    "v_mul_f32 v52, v52, v11\n\t"
    "v_mul_f32 v53, v53, v11\n\t"
    "v_mul_f32 v54, v54, v11\n\t"
    "v_mul_f32 v55, v55, v11\n\t"
    "s_branch 11b\n\t"
    "12:\n\t"
    "v_max3_f32 v10, v24, v25, v26\n\t"
    "v_max3_f32 v40, v27, v28, v29\n\t"
    "v_max3_f32 v10, v10, v30, v31\n\t"
    "v_max3_f32 v10, v10, v40, %[neg]\n\t"
    "v_mov_b32 v40, v10\n\t"
    "s_nop 1\n\t"
    "v_permlane16_swap_b32 v10, v40\n\t"
    "v_max_f32 v10, v10, v40\n\t"
    "v_mov_b32 v40, v10\n\t"
    "s_nop 1\n\t"
    "v_permlane32_swap_b32 v10, v40\n\t"
    "v_max_f32 v10, v10, v40\n\t"
    "v_sub_f32 v24, v24, v10\n\t"
    "v_sub_f32 v25, v25, v10\n\t"
    "v_sub_f32 v26, v26, v10\n\t"
    "v_sub_f32 v27, v27, v10\n\t"
    "v_sub_f32 v28, v28, v10\n\t"
    "v_sub_f32 v29, v29, v10\n\t"
    "v_sub_f32 v30, v30, v10\n\t"
    "v_sub_f32 v31, v31, v10\n\t"
    "s_mov_b32 %[bg], 0x7fc00000\n\t"
    "s_branch 13b\n\t"
    "9:"
    : [tc] "=&s"(tc), [sp] "=&s"(sp), [cap] "+s"(cap), [st] "=&s"(st), [bg] "=&s"(bg)
    : [ag] "s"(ag), [c44] "s"(0x4444u), [kv] "s"(kv), [vs] "s"(vs), [od] "s"(od), [od2] "s"(od + 262144), [neg] "s"(-1.0e38f), [lsc] "s"(LSC), [one] "s"(0x3F803F80u), [big] "s"(24.0f), "{v[64:95]}"(W.w0), "{v[96:127]}"(W.w1), "{v[128:159]}"(W.w2), "{v[160:191]}"(W.w3), "{v[192:223]}"(W.w4), "{v[224:255]}"(W.w5)
    : "v6", "v7", "v8", "v9", "v10", "v11", "v12", "v13", "v14", "v15", "v16", "v17", "v18", "v19", "v20", "v21", "v22", "v23", "v24", "v25", "v26", "v27", "v28", "v29", "v30", "v31", "v32", "v33", "v34", "v35", "v36", "v37", "v38", "v39", "v40", "v41", "v42", "v43", "v44", "v45", "v46", "v47", "v48", "v49", "v50", "v51", "v52", "v53", "v54", "v55", "v56", "v57", "v58", "v59", "v60", "v61", "v62", "v63", "vcc", "scc", "memory");
}
template <> __device__ __forceinline__ void qk_step<4>(const QKW& W, const char* ag, const char* vs, const char* od, const char* kv, unsigned& cap, unsigned& sp,
                                                        unsigned long long& tc, unsigned long long* ts, unsigned& d0, unsigned& d1) {
  unsigned st, bg;
  asm volatile("v_mbcnt_lo_u32_b32 v52, -1, 0\n\t"
    "v_mbcnt_hi_u32_b32 v52, -1, v52\n\t"
    "v_lshlrev_b32 v6, 4, v52\n\t"
    "s_mov_b32 %[sp], 0\n\t"
    "global_load_dwordx4 v[36:39], v6, %[ag] sc1\n\t"
    "global_load_dwordx4 v[40:43], v6, %[ag] offset:1024 sc1\n\t"
    "s_memtime %[tc]\n\t"
    "v_add_u32 v7, 73728, v6\n\t"
    "v_add_u32 v8, 131072, v6\n\t"
    "ds_read_b128 v[20:23], v7 offset:8192\n\t"
    "ds_read_b128 v[24:27], v7 offset:9216\n\t"
    "ds_read_b128 v[28:31], v7 offset:0\n\t"
    "ds_read_b128 v[32:35], v7 offset:1024\n\t"
    "v_lshrrev_b32 v44, 4, v52\n\t"
    "v_and_b32 v45, 15, v52\n\t"
    "v_and_b32 v53, 4, v52\n\t"
    "v_mul_u32_u24 v53, 0x2aaa, v53\n\t"
    "v_lshrrev_b32 v9, 1, v52\n\t"
    "v_and_b32 v9, 2, v9\n\t"
    "v_or3_b32 v9, v9, v53, %[c44]\n\t"
    "v_lshlrev_b32 v53, 2, v44\n\t"
    "v_lshl_or_b32 v10, v45, 4, v53\n\t"
    "3:\n\t"
    "v_mov_b32 v12, 0\n\t"
    "v_mov_b32 v13, 0\n\t"
    "v_mov_b32 v14, 0\n\t"
    "v_mov_b32 v15, 0\n\t"
    "v_mov_b32 v16, 0\n\t"
    "v_mov_b32 v17, 0\n\t"
    "v_mov_b32 v18, 0\n\t"
    "v_mov_b32 v19, 0\n\t"
    "s_waitcnt vmcnt(0) lgkmcnt(0)\n\t"
    "14:\n\t"
    "ds_write_b128 v6, v[36:39] offset:8192\n\t"
    "ds_write_b128 v6, v[40:43] offset:9216\n\t"
    "v_smfmac_f32_16x16x64_bf16 v[12:15], v[36:39], v[128:135], v9\n\t"
    "v_max3_u32 v52, v36, v37, v38\n\t"
    "v_max3_u32 v53, v39, v40, v41\n\t"
    "v_smfmac_f32_16x16x64_bf16 v[16:19], v[36:39], v[136:143], v9\n\t"
    "v_max3_u32 v52, v52, v42, v43\n\t"
    "v_max_u32 v52, v52, v53\n\t"
    "v_cmp_eq_u32 vcc, -1, v52\n\t"
    "s_cbranch_vccnz 1f\n\t"
    "2:\n\t"
    "s_waitcnt lgkmcnt(0)\n\t"
    "s_barrier\n\t"
    "s_memrealtime s[36:37]\n\t"
    "ds_read_b128 v[36:39], v6 offset:0\n\t"
    "ds_read_b128 v[44:47], v6 offset:4096\n\t"
    "v_smfmac_f32_16x16x64_bf16 v[12:15], v[40:43], v[144:151], v9\n\t"
    "ds_read_b128 v[48:51], v6 offset:5120\n\t"
    "ds_read_b128 v[52:55], v6 offset:6144\n\t"
    "v_smfmac_f32_16x16x64_bf16 v[16:19], v[40:43], v[152:159], v9\n\t"
    "ds_read_b128 v[40:43], v6 offset:1024\n\t"
    "s_waitcnt lgkmcnt(0)\n\t"
    "s_mov_b32 s38, s36\n\t"
    "s_min_u32 s37, %[d0], %[d1]\n\t"
    "s_add_u32 s36, s36, s37\n\t"
    "s_and_b32 s36, s36, 0xffffff\n\t"
    "s_or_b32 s36, s36, %[sn]\n\t"
    "v_lshlrev_b32 v11, 10, v6\n\t"
    "v_mov_b32 v5, s36\n\t"
    "s_mov_b64 exec, 15\n\t"
    "global_store_dword v11, v5, %[vs] offset:-4096 sc1\n\t"
    "s_mov_b64 exec, -1\n\t"
    "s_sleep 0\n\t"
    "v_smfmac_f32_16x16x64_bf16 v[12:15], v[36:39], v[56:63], v9\n\t"
    "v_smfmac_f32_16x16x64_bf16 v[16:19], v[36:39], v[28:35], v9\n\t"
    "ds_read_b128 v[28:31], v7 offset:16384\n\t"
    "ds_read_b128 v[32:35], v7 offset:17408\n\t"
    "ds_read_b128 v[36:39], v6 offset:7168\n\t"
    "v_smfmac_f32_16x16x64_bf16 v[12:15], v[44:47], v[64:71], v9\n\t"
    "v_smfmac_f32_16x16x64_bf16 v[16:19], v[44:47], v[72:79], v9\n\t"
    "ds_read_b128 v[44:47], v6 offset:10240\n\t"
    "v_smfmac_f32_16x16x64_bf16 v[12:15], v[48:51], v[80:87], v9\n\t"
    "v_smfmac_f32_16x16x64_bf16 v[16:19], v[48:51], v[88:95], v9\n\t"
    "ds_read_b128 v[48:51], v6 offset:11264\n\t"
    "v_smfmac_f32_16x16x64_bf16 v[12:15], v[52:55], v[96:103], v9\n\t"
    "v_smfmac_f32_16x16x64_bf16 v[16:19], v[52:55], v[104:111], v9\n\t"
    "ds_read_b128 v[52:55], v6 offset:2048\n\t"
    "v_smfmac_f32_16x16x64_bf16 v[12:15], v[40:43], v[20:27], v9\n\t"
    "ds_read_b128 v[20:23], v8 offset:0\n\t"
    "ds_read_b128 v[24:27], v8 offset:1024\n\t"
    "s_waitcnt lgkmcnt(6)\n\t"
    "v_smfmac_f32_16x16x64_bf16 v[16:19], v[40:43], v[28:35], v9\n\t"
    "ds_read_b128 v[28:31], v8 offset:8192\n\t"
    "ds_read_b128 v[32:35], v8 offset:9216\n\t"
    "ds_read_b128 v[40:43], v6 offset:12288\n\t"
    "s_waitcnt lgkmcnt(8)\n\t"
    "v_smfmac_f32_16x16x64_bf16 v[12:15], v[36:39], v[112:119], v9\n\t"
    "v_smfmac_f32_16x16x64_bf16 v[16:19], v[36:39], v[120:127], v9\n\t"
    "ds_read_b128 v[36:39], v6 offset:13312\n\t"
    "s_waitcnt lgkmcnt(8)\n\t"
    "v_smfmac_f32_16x16x64_bf16 v[12:15], v[44:47], v[160:167], v9\n\t"
    "v_smfmac_f32_16x16x64_bf16 v[16:19], v[44:47], v[168:175], v9\n\t"
    "ds_read_b128 v[44:47], v6 offset:14336\n\t"
    "s_barrier\n\t"
    "s_setprio 2\n\t"
    "s_waitcnt lgkmcnt(8)\n\t"
    "v_smfmac_f32_16x16x64_bf16 v[12:15], v[48:51], v[176:183], v9\n\t"
    "v_smfmac_f32_16x16x64_bf16 v[16:19], v[48:51], v[184:191], v9\n\t"
    "ds_read_b128 v[48:51], v6 offset:3072\n\t"
    "s_waitcnt lgkmcnt(6)\n\t"
    "v_smfmac_f32_16x16x64_bf16 v[12:15], v[52:55], v[20:27], v9\n\t"
    "ds_read_b128 v[20:23], v8 offset:16384\n\t"
    "ds_read_b128 v[24:27], v8 offset:17408\n\t"
    "s_waitcnt lgkmcnt(6)\n\t"
    "v_smfmac_f32_16x16x64_bf16 v[16:19], v[52:55], v[28:35], v9\n\t"
    "ds_read_b128 v[28:31], v8 offset:24576\n\t"
    "ds_read_b128 v[32:35], v8 offset:25600\n\t"
    "ds_read_b128 v[52:55], v6 offset:15360\n\t"
    "s_waitcnt lgkmcnt(8)\n\t"
    "v_smfmac_f32_16x16x64_bf16 v[12:15], v[40:43], v[192:199], v9\n\t"
    "v_smfmac_f32_16x16x64_bf16 v[16:19], v[40:43], v[200:207], v9\n\t"
    "s_waitcnt lgkmcnt(7)\n\t"
    "v_smfmac_f32_16x16x64_bf16 v[12:15], v[36:39], v[208:215], v9\n\t"
    "v_smfmac_f32_16x16x64_bf16 v[16:19], v[36:39], v[216:223], v9\n\t"
    "s_waitcnt lgkmcnt(6)\n\t"
    "v_smfmac_f32_16x16x64_bf16 v[12:15], v[44:47], v[224:231], v9\n\t"
    "v_smfmac_f32_16x16x64_bf16 v[16:19], v[44:47], v[232:239], v9\n\t"
    "s_waitcnt lgkmcnt(3)\n\t"
    "v_smfmac_f32_16x16x64_bf16 v[12:15], v[48:51], v[20:27], v9\n\t"
    "s_waitcnt lgkmcnt(1)\n\t"
    "v_smfmac_f32_16x16x64_bf16 v[16:19], v[48:51], v[28:35], v9\n\t"
    "s_waitcnt lgkmcnt(0)\n\t"
    "v_smfmac_f32_16x16x64_bf16 v[12:15], v[52:55], v[240:247], v9\n\t"
    "v_smfmac_f32_16x16x64_bf16 v[16:19], v[52:55], v[248:255], v9\n\t"
    "s_nop 6\n\t"
    "v_permlane16_swap_b32 v12, v14\n\t"
    "v_permlane16_swap_b32 v13, v15\n\t"
    "v_permlane16_swap_b32 v16, v18\n\t"
    "v_permlane16_swap_b32 v17, v19\n\t"
    "v_add_f32 v12, v12, v14\n\t"
    "v_add_f32 v13, v13, v15\n\t"
    "v_add_f32 v16, v16, v18\n\t"
    "v_cvt_pk_bf16_f32 v12, v12, v13\n\t"
    "v_add_f32 v17, v17, v19\n\t"
    "ds_write_b32 v10, v12 offset:34816\n\t"
    "v_cvt_pk_bf16_f32 v16, v16, v17\n\t"
    "ds_write_b32 v10, v16 offset:35072\n\t"
    "s_waitcnt lgkmcnt(0)\n\t"
    "s_setprio 0\n\t"
    "s_barrier\n\t"
    "s_memrealtime s[36:37]\n\t"
    "s_waitcnt lgkmcnt(0)\n\t"
    "s_and_b32 s36, s36, 0xffffff\n\t"
    "s_or_b32 s36, s36, %[sn]\n\t"
    "v_lshlrev_b32 v7, 10, v6\n\t"
    "v_mov_b32 v8, s36\n\t"
    "s_mov_b64 exec, 15\n\t"
    "global_store_dword v7, v8, %[vs] offset:0 sc1\n\t"
    "s_mov_b64 exec, -1\n\t"
    "s_barrier\n\t"
    "s_memrealtime s[36:37]\n\t"
    "s_mov_b32 %[d1], %[d0]\n\t"
    "s_waitcnt lgkmcnt(0)\n\t"
    "s_sub_u32 %[d0], s36, s38\n\t"
    "s_min_u32 %[d0], %[d0], 200\n\t"
    "s_waitcnt lgkmcnt(0)\n\t"
    "s_branch 9f\n\t"
    "1:\n\t"
    "s_add_u32 %[sp], %[sp], 1\n\t"
    "s_cmp_ge_u32 %[sp], %[cap]\n\t"
    "s_cbranch_scc1 7f\n\t"
    "global_load_dwordx4 v[36:39], v6, %[ag] sc1\n\t"
    "global_load_dwordx4 v[40:43], v6, %[ag] offset:1024 sc1\n\t"
    "s_waitcnt lgkmcnt(0)\n\t"
    "s_memtime %[tc]\n\t"
    "s_nop 7\n\t"
    "s_branch 3b\n\t"
    "7:\n\t"
    "s_mov_b32 %[cap], 0\n\t"
    "s_branch 2b\n\t"
    "9:"
    : [tc] "=&s"(tc), [sp] "=&s"(sp), [cap] "+s"(cap), [d0] "+s"(d0), [d1] "+s"(d1)
    : [ag] "s"(ag), [c44] "s"(0x4444u), [vs] "s"(vs), [sn] "s"((unsigned)(size_t)od), "{v[64:95]}"(W.w0), "{v[96:127]}"(W.w1), "{v[128:159]}"(W.w2), "{v[160:191]}"(W.w3), "{v[192:223]}"(W.w4), "{v[224:255]}"(W.w5), "{v[56:63]}"(W.x)
    : "v5", "v6", "v7", "v8", "v9", "v10", "v11", "v12", "v13", "v14", "v15", "v16", "v17", "v18", "v19", "v20", "v21", "v22", "v23", "v24", "v25", "v26", "v27", "v28", "v29", "v30", "v31", "v32", "v33", "v34", "v35", "v36", "v37", "v38", "v39", "v40", "v41", "v42", "v43", "v44", "v45", "v46", "v47", "v48", "v49", "v50", "v51", "v52", "v53", "v54", "v55", "vcc", "scc", "memory", "s36", "s37", "s38");
}
template <> __device__ __forceinline__ void qk_step<5>(const QKW& W, const char* ag, const char* vs, const char* od, const char* kv, unsigned& cap, unsigned& sp,
                                                        unsigned long long& tc, unsigned long long* ts, unsigned& d0, unsigned& d1) {
  unsigned st, bg;
  asm volatile("v_mbcnt_lo_u32_b32 v52, -1, 0\n\t"
    "v_mbcnt_hi_u32_b32 v52, -1, v52\n\t"
    "v_lshlrev_b32 v6, 4, v52\n\t"
    "s_mov_b32 %[sp], 0\n\t"
    "global_load_dwordx4 v[36:39], v6, %[ag] sc1\n\t"
    "global_load_dwordx4 v[40:43], v6, %[ag] offset:1024 sc1\n\t"
    "s_memtime %[tc]\n\t"
    "v_add_u32 v7, 75776, v6\n\t"
    "v_add_u32 v8, 133120, v6\n\t"
    "ds_read_b128 v[20:23], v7 offset:8192\n\t"
    "ds_read_b128 v[24:27], v7 offset:9216\n\t"
    "ds_read_b128 v[28:31], v7 offset:0\n\t"
    "ds_read_b128 v[32:35], v7 offset:1024\n\t"
    "v_lshrrev_b32 v44, 4, v52\n\t"
    "v_and_b32 v45, 15, v52\n\t"
    "v_and_b32 v53, 4, v52\n\t"
    "v_mul_u32_u24 v53, 0x2aaa, v53\n\t"
    "v_lshrrev_b32 v9, 1, v52\n\t"
    "v_and_b32 v9, 2, v9\n\t"
    "v_or3_b32 v9, v9, v53, %[c44]\n\t"
    "v_lshlrev_b32 v53, 2, v44\n\t"
    "v_lshl_or_b32 v10, v45, 4, v53\n\t"
    "v_and_b32 v53, 1, v52\n\t"
    "v_lshlrev_b32 v53, 3, v53\n\t"
    "v_lshl_or_b32 v11, v44, 7, v53\n\t"
    "v_and_b32 v53, 12, v52\n\t"
    "v_lshl_or_b32 v11, v53, 2, v11\n\t"
    "v_and_b32 v53, 2, v52\n\t"
    "v_lshl_or_b32 v11, v53, 8, v11\n\t"
    "3:\n\t"
    "v_mov_b32 v12, 0\n\t"
    "v_mov_b32 v13, 0\n\t"
    "v_mov_b32 v14, 0\n\t"
    "v_mov_b32 v15, 0\n\t"
    "v_mov_b32 v16, 0\n\t"
    "v_mov_b32 v17, 0\n\t"
    "v_mov_b32 v18, 0\n\t"
    "v_mov_b32 v19, 0\n\t"
    "s_waitcnt vmcnt(0) lgkmcnt(0)\n\t"
    "14:\n\t"
    "ds_write_b128 v6, v[36:39] offset:10240\n\t"
    "ds_write_b128 v6, v[40:43] offset:11264\n\t"
    "v_smfmac_f32_16x16x64_bf16 v[12:15], v[36:39], v[160:167], v9\n\t"
    "v_max3_u32 v52, v36, v37, v38\n\t"
    "v_max3_u32 v53, v39, v40, v41\n\t"
    "v_smfmac_f32_16x16x64_bf16 v[16:19], v[36:39], v[168:175], v9\n\t"
    "v_max3_u32 v52, v52, v42, v43\n\t"
    "v_max_u32 v52, v52, v53\n\t"
    "v_cmp_eq_u32 vcc, -1, v52\n\t"
    "s_cbranch_vccnz 1f\n\t"
    "2:\n\t"
    "s_waitcnt lgkmcnt(0)\n\t"
    "s_barrier\n\t"
    "ds_read_b128 v[36:39], v6 offset:0\n\t"
    "ds_read_b128 v[44:47], v6 offset:4096\n\t"
    "v_smfmac_f32_16x16x64_bf16 v[12:15], v[40:43], v[176:183], v9\n\t"
    "ds_read_b128 v[48:51], v6 offset:5120\n\t"
    "ds_read_b128 v[52:55], v6 offset:6144\n\t"
    "v_smfmac_f32_16x16x64_bf16 v[16:19], v[40:43], v[184:191], v9\n\t"
    "ds_read_b128 v[40:43], v6 offset:1024\n\t"
    "s_sleep 0\n\t"
    "s_waitcnt lgkmcnt(4)\n\t"
    "v_smfmac_f32_16x16x64_bf16 v[12:15], v[36:39], v[56:63], v9\n\t"
    "v_smfmac_f32_16x16x64_bf16 v[16:19], v[36:39], v[28:35], v9\n\t"
    "ds_read_b128 v[28:31], v7 offset:16384\n\t"
    "ds_read_b128 v[32:35], v7 offset:17408\n\t"
    "ds_read_b128 v[36:39], v6 offset:7168\n\t"
    "s_waitcnt lgkmcnt(6)\n\t"
    "v_smfmac_f32_16x16x64_bf16 v[12:15], v[44:47], v[64:71], v9\n\t"
    "v_smfmac_f32_16x16x64_bf16 v[16:19], v[44:47], v[72:79], v9\n\t"
    "ds_read_b128 v[44:47], v6 offset:8192\n\t"
    "s_waitcnt lgkmcnt(6)\n\t"
    "v_smfmac_f32_16x16x64_bf16 v[12:15], v[48:51], v[80:87], v9\n\t"
    "v_smfmac_f32_16x16x64_bf16 v[16:19], v[48:51], v[88:95], v9\n\t"
    "ds_read_b128 v[48:51], v6 offset:9216\n\t"
    "s_waitcnt lgkmcnt(6)\n\t"
    "v_smfmac_f32_16x16x64_bf16 v[12:15], v[52:55], v[96:103], v9\n\t"
    "v_smfmac_f32_16x16x64_bf16 v[16:19], v[52:55], v[104:111], v9\n\t"
    "ds_read_b128 v[52:55], v6 offset:2048\n\t"
    "s_waitcnt lgkmcnt(6)\n\t"
    "v_smfmac_f32_16x16x64_bf16 v[12:15], v[40:43], v[20:27], v9\n\t"
    "ds_read_b128 v[20:23], v8 offset:0\n\t"
    "ds_read_b128 v[24:27], v8 offset:1024\n\t"
    "s_waitcnt lgkmcnt(6)\n\t"
    "v_smfmac_f32_16x16x64_bf16 v[16:19], v[40:43], v[28:35], v9\n\t"
    "ds_read_b128 v[28:31], v8 offset:8192\n\t"
    "ds_read_b128 v[32:35], v8 offset:9216\n\t"
    "ds_read_b128 v[40:43], v6 offset:12288\n\t"
    "s_waitcnt lgkmcnt(8)\n\t"
    "v_smfmac_f32_16x16x64_bf16 v[12:15], v[36:39], v[112:119], v9\n\t"
    "v_smfmac_f32_16x16x64_bf16 v[16:19], v[36:39], v[120:127], v9\n\t"
    "ds_read_b128 v[36:39], v6 offset:13312\n\t"
    "s_waitcnt lgkmcnt(8)\n\t"
    "v_smfmac_f32_16x16x64_bf16 v[12:15], v[44:47], v[128:135], v9\n\t"
    "v_smfmac_f32_16x16x64_bf16 v[16:19], v[44:47], v[136:143], v9\n\t"
    "ds_read_b128 v[44:47], v6 offset:14336\n\t"
    "s_barrier\n\t"
    "s_setprio 2\n\t"
    "s_waitcnt lgkmcnt(8)\n\t"
    "v_smfmac_f32_16x16x64_bf16 v[12:15], v[48:51], v[144:151], v9\n\t"
    "v_smfmac_f32_16x16x64_bf16 v[16:19], v[48:51], v[152:159], v9\n\t"
    "ds_read_b128 v[48:51], v6 offset:3072\n\t"
    "s_waitcnt lgkmcnt(6)\n\t"
    "v_smfmac_f32_16x16x64_bf16 v[12:15], v[52:55], v[20:27], v9\n\t"
    "ds_read_b128 v[20:23], v8 offset:16384\n\t"
    "ds_read_b128 v[24:27], v8 offset:17408\n\t"
    "s_waitcnt lgkmcnt(6)\n\t"
    "v_smfmac_f32_16x16x64_bf16 v[16:19], v[52:55], v[28:35], v9\n\t"
    "ds_read_b128 v[28:31], v8 offset:24576\n\t"
    "ds_read_b128 v[32:35], v8 offset:25600\n\t"
    "ds_read_b128 v[52:55], v6 offset:15360\n\t"
    "s_waitcnt lgkmcnt(8)\n\t"
    "v_smfmac_f32_16x16x64_bf16 v[12:15], v[40:43], v[192:199], v9\n\t"
    "v_smfmac_f32_16x16x64_bf16 v[16:19], v[40:43], v[200:207], v9\n\t"
    "s_waitcnt lgkmcnt(7)\n\t"
    "v_smfmac_f32_16x16x64_bf16 v[12:15], v[36:39], v[208:215], v9\n\t"
    "v_smfmac_f32_16x16x64_bf16 v[16:19], v[36:39], v[216:223], v9\n\t"
    "s_waitcnt lgkmcnt(6)\n\t"
    "v_smfmac_f32_16x16x64_bf16 v[12:15], v[44:47], v[224:231], v9\n\t"
    "v_smfmac_f32_16x16x64_bf16 v[16:19], v[44:47], v[232:239], v9\n\t"
    "s_waitcnt lgkmcnt(3)\n\t"
    "v_smfmac_f32_16x16x64_bf16 v[12:15], v[48:51], v[20:27], v9\n\t"
    "s_waitcnt lgkmcnt(1)\n\t"
    "v_smfmac_f32_16x16x64_bf16 v[16:19], v[48:51], v[28:35], v9\n\t"
    "s_waitcnt lgkmcnt(0)\n\t"
    "v_smfmac_f32_16x16x64_bf16 v[12:15], v[52:55], v[240:247], v9\n\t"
    "v_smfmac_f32_16x16x64_bf16 v[16:19], v[52:55], v[248:255], v9\n\t"
    "s_nop 6\n\t"
    "v_permlane16_swap_b32 v12, v14\n\t"
    "v_permlane16_swap_b32 v13, v15\n\t"
    "v_permlane16_swap_b32 v16, v18\n\t"
    "v_permlane16_swap_b32 v17, v19\n\t"
    "v_add_f32 v12, v12, v14\n\t"
    "v_add_f32 v13, v13, v15\n\t"
    "v_add_f32 v16, v16, v18\n\t"
    "v_cvt_pk_bf16_f32 v12, v12, v13\n\t"
    "v_add_f32 v17, v17, v19\n\t"
    "ds_write_b32 v10, v12 offset:35328\n\t"
    "v_cvt_pk_bf16_f32 v16, v16, v17\n\t"
    "ds_write_b32 v10, v16 offset:35584\n\t"
    "s_waitcnt lgkmcnt(0)\n\t"
    "s_setprio 0\n\t"
    "s_barrier\n\t"
    "ds_read_b64_tr_b16 v[36:37], v11 offset:34816\n\t"
    "ds_read_b64_tr_b16 v[38:39], v11 offset:34880\n\t"
    "ds_read_b64_tr_b16 v[40:41], v11 offset:35840\n\t"
    "ds_read_b64_tr_b16 v[42:43], v11 offset:35904\n\t"
    "s_waitcnt lgkmcnt(0)\n\t"
    "v_min_u32 v36, -2, v36\n\t"
    "v_min_u32 v37, -2, v37\n\t"
    "v_min_u32 v38, -2, v38\n\t"
    "v_min_u32 v39, -2, v39\n\t"
    "v_min_u32 v40, -2, v40\n\t"
    "v_min_u32 v41, -2, v41\n\t"
    "v_min_u32 v42, -2, v42\n\t"
    "v_min_u32 v43, -2, v43\n\t"
    "global_store_dwordx4 v6, v[36:39], %[vs] sc1\n\t"
    "global_store_dwordx4 v6, v[40:43], %[vs] offset:1024 sc1\n\t"
    "s_nop 1\n\t"
    "s_barrier\n\t"
    "s_waitcnt lgkmcnt(0)\n\t"
    "s_branch 9f\n\t"
    "1:\n\t"
    "s_add_u32 %[sp], %[sp], 1\n\t"
    "s_cmp_ge_u32 %[sp], %[cap]\n\t"
    "s_cbranch_scc1 7f\n\t"
    "global_load_dwordx4 v[36:39], v6, %[ag] sc1\n\t"
    "global_load_dwordx4 v[40:43], v6, %[ag] offset:1024 sc1\n\t"
    "s_waitcnt lgkmcnt(0)\n\t"
    "s_memtime %[tc]\n\t"
    "s_nop 7\n\t"
    "s_branch 3b\n\t"
    "7:\n\t"
    "s_mov_b32 %[cap], 0\n\t"
    "s_branch 2b\n\t"
    "9:"
    : [tc] "=&s"(tc), [sp] "=&s"(sp), [cap] "+s"(cap)
    : [ag] "s"(ag), [c44] "s"(0x4444u), [vs] "s"(vs), "{v[64:95]}"(W.w0), "{v[96:127]}"(W.w1), "{v[128:159]}"(W.w2), "{v[160:191]}"(W.w3), "{v[192:223]}"(W.w4), "{v[224:255]}"(W.w5), "{v[56:63]}"(W.x)
    : "v6", "v7", "v8", "v9", "v10", "v11", "v12", "v13", "v14", "v15", "v16", "v17", "v18", "v19", "v20", "v21", "v22", "v23", "v24", "v25", "v26", "v27", "v28", "v29", "v30", "v31", "v32", "v33", "v34", "v35", "v36", "v37", "v38", "v39", "v40", "v41", "v42", "v43", "v44", "v45", "v46", "v47", "v48", "v49", "v50", "v51", "v52", "v53", "v54", "v55", "vcc", "scc", "memory");
}
template <> __device__ __forceinline__ void qk_step<6>(const QKW& W, const char* ag, const char* vs, const char* od, const char* kv, unsigned& cap, unsigned& sp,
                                                        unsigned long long& tc, unsigned long long* ts, unsigned& d0, unsigned& d1) {
  unsigned st, bg;
  asm volatile("v_mbcnt_lo_u32_b32 v52, -1, 0\n\t"
    "v_mbcnt_hi_u32_b32 v52, -1, v52\n\t"
    "v_lshlrev_b32 v6, 4, v52\n\t"
    "s_mov_b32 %[sp], 0\n\t"
    "global_load_dwordx4 v[36:39], v6, %[ag] sc1\n\t"
    "global_load_dwordx4 v[40:43], v6, %[ag] offset:1024 sc1\n\t"
    "s_memtime %[tc]\n\t"
    "v_add_u32 v7, 77824, v6\n\t"
    "v_add_u32 v8, 135168, v6\n\t"
    "ds_read_b128 v[20:23], v7 offset:8192\n\t"
    "ds_read_b128 v[24:27], v7 offset:9216\n\t"
    "ds_read_b128 v[28:31], v7 offset:0\n\t"
    "ds_read_b128 v[32:35], v7 offset:1024\n\t"
    "v_lshrrev_b32 v44, 4, v52\n\t"
    "v_and_b32 v45, 15, v52\n\t"
    "v_and_b32 v53, 4, v52\n\t"
    "v_mul_u32_u24 v53, 0x2aaa, v53\n\t"
    "v_lshrrev_b32 v9, 1, v52\n\t"
    "v_and_b32 v9, 2, v9\n\t"
    "v_or3_b32 v9, v9, v53, %[c44]\n\t"
    "v_lshlrev_b32 v53, 2, v44\n\t"
    "v_lshl_or_b32 v10, v45, 4, v53\n\t"
    "3:\n\t"
    "v_mov_b32 v12, 0\n\t"
    "v_mov_b32 v13, 0\n\t"
    "v_mov_b32 v14, 0\n\t"
    "v_mov_b32 v15, 0\n\t"
    "v_mov_b32 v16, 0\n\t"
    "v_mov_b32 v17, 0\n\t"
    "v_mov_b32 v18, 0\n\t"
    "v_mov_b32 v19, 0\n\t"
    "s_waitcnt vmcnt(0) lgkmcnt(0)\n\t"
    "14:\n\t"
    "ds_write_b128 v6, v[36:39] offset:12288\n\t"
    "ds_write_b128 v6, v[40:43] offset:13312\n\t"
    "v_smfmac_f32_16x16x64_bf16 v[12:15], v[36:39], v[192:199], v9\n\t"
    "v_max3_u32 v52, v36, v37, v38\n\t"
    "v_max3_u32 v53, v39, v40, v41\n\t"
    "v_smfmac_f32_16x16x64_bf16 v[16:19], v[36:39], v[200:207], v9\n\t"
    "v_max3_u32 v52, v52, v42, v43\n\t"
    "v_max_u32 v52, v52, v53\n\t"
    "v_cmp_eq_u32 vcc, -1, v52\n\t"
    "s_cbranch_vccnz 1f\n\t"
    "2:\n\t"
    "s_waitcnt lgkmcnt(0)\n\t"
    "s_barrier\n\t"
    "ds_read_b128 v[36:39], v6 offset:0\n\t"
    "ds_read_b128 v[44:47], v6 offset:4096\n\t"
    "v_smfmac_f32_16x16x64_bf16 v[12:15], v[40:43], v[208:215], v9\n\t"
    "ds_read_b128 v[48:51], v6 offset:5120\n\t"
    "ds_read_b128 v[52:55], v6 offset:6144\n\t"
    "v_smfmac_f32_16x16x64_bf16 v[16:19], v[40:43], v[216:223], v9\n\t"
    "ds_read_b128 v[40:43], v6 offset:1024\n\t"
    "s_sleep 0\n\t"
    "s_waitcnt lgkmcnt(4)\n\t"
    "v_smfmac_f32_16x16x64_bf16 v[12:15], v[36:39], v[56:63], v9\n\t"
    "v_smfmac_f32_16x16x64_bf16 v[16:19], v[36:39], v[28:35], v9\n\t"
    "ds_read_b128 v[28:31], v7 offset:16384\n\t"
    "ds_read_b128 v[32:35], v7 offset:17408\n\t"
    "ds_read_b128 v[36:39], v6 offset:7168\n\t"
    "s_waitcnt lgkmcnt(6)\n\t"
    "v_smfmac_f32_16x16x64_bf16 v[12:15], v[44:47], v[64:71], v9\n\t"
    "v_smfmac_f32_16x16x64_bf16 v[16:19], v[44:47], v[72:79], v9\n\t"
    "ds_read_b128 v[44:47], v6 offset:8192\n\t"
    "s_waitcnt lgkmcnt(6)\n\t"
    "v_smfmac_f32_16x16x64_bf16 v[12:15], v[48:51], v[80:87], v9\n\t"
    "v_smfmac_f32_16x16x64_bf16 v[16:19], v[48:51], v[88:95], v9\n\t"
    "ds_read_b128 v[48:51], v6 offset:9216\n\t"
    "s_waitcnt lgkmcnt(6)\n\t"
    "v_smfmac_f32_16x16x64_bf16 v[12:15], v[52:55], v[96:103], v9\n\t"
    "v_smfmac_f32_16x16x64_bf16 v[16:19], v[52:55], v[104:111], v9\n\t"
    "ds_read_b128 v[52:55], v6 offset:2048\n\t"
    "s_waitcnt lgkmcnt(6)\n\t"
    "v_smfmac_f32_16x16x64_bf16 v[12:15], v[40:43], v[20:27], v9\n\t"
    "ds_read_b128 v[20:23], v8 offset:0\n\t"
    "ds_read_b128 v[24:27], v8 offset:1024\n\t"
    "s_waitcnt lgkmcnt(6)\n\t"
    "v_smfmac_f32_16x16x64_bf16 v[16:19], v[40:43], v[28:35], v9\n\t"
    "ds_read_b128 v[28:31], v8 offset:8192\n\t"
    "ds_read_b128 v[32:35], v8 offset:9216\n\t"
    "ds_read_b128 v[40:43], v6 offset:10240\n\t"
    "s_waitcnt lgkmcnt(8)\n\t"
    "v_smfmac_f32_16x16x64_bf16 v[12:15], v[36:39], v[112:119], v9\n\t"
    "v_smfmac_f32_16x16x64_bf16 v[16:19], v[36:39], v[120:127], v9\n\t"
    "ds_read_b128 v[36:39], v6 offset:11264\n\t"
    "s_waitcnt lgkmcnt(8)\n\t"
    "v_smfmac_f32_16x16x64_bf16 v[12:15], v[44:47], v[128:135], v9\n\t"
    "v_smfmac_f32_16x16x64_bf16 v[16:19], v[44:47], v[136:143], v9\n\t"
    "ds_read_b128 v[44:47], v6 offset:14336\n\t"
    "s_barrier\n\t"
    "s_setprio 2\n\t"
    "s_waitcnt lgkmcnt(8)\n\t"
    "v_smfmac_f32_16x16x64_bf16 v[12:15], v[48:51], v[144:151], v9\n\t"
    "v_smfmac_f32_16x16x64_bf16 v[16:19], v[48:51], v[152:159], v9\n\t"
    "ds_read_b128 v[48:51], v6 offset:3072\n\t"
    "s_waitcnt lgkmcnt(6)\n\t"
    "v_smfmac_f32_16x16x64_bf16 v[12:15], v[52:55], v[20:27], v9\n\t"
    "ds_read_b128 v[20:23], v8 offset:16384\n\t"
    "ds_read_b128 v[24:27], v8 offset:17408\n\t"
    "s_waitcnt lgkmcnt(6)\n\t"
    "v_smfmac_f32_16x16x64_bf16 v[16:19], v[52:55], v[28:35], v9\n\t"
    "ds_read_b128 v[28:31], v8 offset:24576\n\t"
    "ds_read_b128 v[32:35], v8 offset:25600\n\t"
    "ds_read_b128 v[52:55], v6 offset:15360\n\t"
    "s_waitcnt lgkmcnt(8)\n\t"
    "v_smfmac_f32_16x16x64_bf16 v[12:15], v[40:43], v[160:167], v9\n\t"
    "v_smfmac_f32_16x16x64_bf16 v[16:19], v[40:43], v[168:175], v9\n\t"
    "s_waitcnt lgkmcnt(7)\n\t"
    "v_smfmac_f32_16x16x64_bf16 v[12:15], v[36:39], v[176:183], v9\n\t"
    "v_smfmac_f32_16x16x64_bf16 v[16:19], v[36:39], v[184:191], v9\n\t"
    "s_waitcnt lgkmcnt(6)\n\t"
    "v_smfmac_f32_16x16x64_bf16 v[12:15], v[44:47], v[224:231], v9\n\t"
    "v_smfmac_f32_16x16x64_bf16 v[16:19], v[44:47], v[232:239], v9\n\t"
    "s_waitcnt lgkmcnt(3)\n\t"
    "v_smfmac_f32_16x16x64_bf16 v[12:15], v[48:51], v[20:27], v9\n\t"
    "s_waitcnt lgkmcnt(1)\n\t"
    "v_smfmac_f32_16x16x64_bf16 v[16:19], v[48:51], v[28:35], v9\n\t"
    "s_waitcnt lgkmcnt(0)\n\t"
    "v_smfmac_f32_16x16x64_bf16 v[12:15], v[52:55], v[240:247], v9\n\t"
    "v_smfmac_f32_16x16x64_bf16 v[16:19], v[52:55], v[248:255], v9\n\t"
    "s_nop 6\n\t"
    "v_permlane16_swap_b32 v12, v14\n\t"
    "v_permlane16_swap_b32 v13, v15\n\t"
    "v_permlane16_swap_b32 v16, v18\n\t"
    "v_permlane16_swap_b32 v17, v19\n\t"
    "v_add_f32 v12, v12, v14\n\t"
    "v_add_f32 v13, v13, v15\n\t"
    "v_add_f32 v16, v16, v18\n\t"
    "v_cvt_pk_bf16_f32 v12, v12, v13\n\t"
    "v_add_f32 v17, v17, v19\n\t"
    "ds_write_b32 v10, v12 offset:35840\n\t"
    "v_cvt_pk_bf16_f32 v16, v16, v17\n\t"
    "ds_write_b32 v10, v16 offset:36096\n\t"
    "s_waitcnt lgkmcnt(0)\n\t"
    "s_setprio 0\n\t"
    "s_barrier\n\t"
    "s_barrier\n\t"
    "s_waitcnt lgkmcnt(0)\n\t"
    "s_branch 9f\n\t"
    "1:\n\t"
    "s_add_u32 %[sp], %[sp], 1\n\t"
    "s_cmp_ge_u32 %[sp], %[cap]\n\t"
    "s_cbranch_scc1 7f\n\t"
    "global_load_dwordx4 v[36:39], v6, %[ag] sc1\n\t"
    "global_load_dwordx4 v[40:43], v6, %[ag] offset:1024 sc1\n\t"
    "s_waitcnt lgkmcnt(0)\n\t"
    "s_memtime %[tc]\n\t"
    "s_nop 7\n\t"
    "s_branch 3b\n\t"
    "7:\n\t"
    "s_mov_b32 %[cap], 0\n\t"
    "s_branch 2b\n\t"
    "9:"
    : [tc] "=&s"(tc), [sp] "=&s"(sp), [cap] "+s"(cap)
    : [ag] "s"(ag), [c44] "s"(0x4444u), "{v[64:95]}"(W.w0), "{v[96:127]}"(W.w1), "{v[128:159]}"(W.w2), "{v[160:191]}"(W.w3), "{v[192:223]}"(W.w4), "{v[224:255]}"(W.w5), "{v[56:63]}"(W.x)
    : "v6", "v7", "v8", "v9", "v10", "v11", "v12", "v13", "v14", "v15", "v16", "v17", "v18", "v19", "v20", "v21", "v22", "v23", "v24", "v25", "v26", "v27", "v28", "v29", "v30", "v31", "v32", "v33", "v34", "v35", "v36", "v37", "v38", "v39", "v40", "v41", "v42", "v43", "v44", "v45", "v46", "v47", "v48", "v49", "v50", "v51", "v52", "v53", "v54", "v55", "vcc", "scc", "memory");
}
template <> __device__ __forceinline__ void qk_step<7>(const QKW& W, const char* ag, const char* vs, const char* od, const char* kv, unsigned& cap, unsigned& sp,
                                                        unsigned long long& tc, unsigned long long* ts, unsigned& d0, unsigned& d1) {
  unsigned st, bg;
  asm volatile("v_mbcnt_lo_u32_b32 v52, -1, 0\n\t"
    "v_mbcnt_hi_u32_b32 v52, -1, v52\n\t"
    "v_lshlrev_b32 v6, 4, v52\n\t"
    "s_mov_b32 %[sp], 0\n\t"
    "global_load_dwordx4 v[36:39], v6, %[ag] sc1\n\t"
    "global_load_dwordx4 v[40:43], v6, %[ag] offset:1024 sc1\n\t"
    "s_memtime %[tc]\n\t"
    "v_add_u32 v7, 79872, v6\n\t"
    "v_add_u32 v8, 137216, v6\n\t"
    "ds_read_b128 v[20:23], v7 offset:8192\n\t"
    "ds_read_b128 v[24:27], v7 offset:9216\n\t"
    "ds_read_b128 v[28:31], v7 offset:0\n\t"
    "ds_read_b128 v[32:35], v7 offset:1024\n\t"
    "v_lshrrev_b32 v44, 4, v52\n\t"
    "v_and_b32 v45, 15, v52\n\t"
    "v_and_b32 v53, 4, v52\n\t"
    "v_mul_u32_u24 v53, 0x2aaa, v53\n\t"
    "v_lshrrev_b32 v9, 1, v52\n\t"
    "v_and_b32 v9, 2, v9\n\t"
    "v_or3_b32 v9, v9, v53, %[c44]\n\t"
    "v_lshlrev_b32 v53, 2, v44\n\t"
    "v_lshl_or_b32 v10, v45, 4, v53\n\t"
    "3:\n\t"
    "v_mov_b32 v12, 0\n\t"
    "v_mov_b32 v13, 0\n\t"
    "v_mov_b32 v14, 0\n\t"
    "v_mov_b32 v15, 0\n\t"
    "v_mov_b32 v16, 0\n\t"
    "v_mov_b32 v17, 0\n\t"
    "v_mov_b32 v18, 0\n\t"
    "v_mov_b32 v19, 0\n\t"
    "s_waitcnt vmcnt(0) lgkmcnt(0)\n\t"
    "14:\n\t"
    "ds_write_b128 v6, v[36:39] offset:14336\n\t"
    "ds_write_b128 v6, v[40:43] offset:15360\n\t"
    "v_smfmac_f32_16x16x64_bf16 v[12:15], v[36:39], v[224:231], v9\n\t"
    "v_max3_u32 v52, v36, v37, v38\n\t"
    "v_max3_u32 v53, v39, v40, v41\n\t"
    "v_smfmac_f32_16x16x64_bf16 v[16:19], v[36:39], v[232:239], v9\n\t"
    "v_max3_u32 v52, v52, v42, v43\n\t"
    "v_max_u32 v52, v52, v53\n\t"
    "v_cmp_eq_u32 vcc, -1, v52\n\t"
    "s_cbranch_vccnz 1f\n\t"
    "2:\n\t"
    "s_waitcnt lgkmcnt(0)\n\t"
    "s_barrier\n\t"
    "ds_read_b128 v[36:39], v6 offset:0\n\t"
    "ds_read_b128 v[44:47], v6 offset:4096\n\t"
    "v_smfmac_f32_16x16x64_bf16 v[12:15], v[40:43], v[240:247], v9\n\t"
    "ds_read_b128 v[48:51], v6 offset:5120\n\t"
    "ds_read_b128 v[52:55], v6 offset:6144\n\t"
    "v_smfmac_f32_16x16x64_bf16 v[16:19], v[40:43], v[248:255], v9\n\t"
    "ds_read_b128 v[40:43], v6 offset:1024\n\t"
    "s_sleep 0\n\t"
    "s_waitcnt lgkmcnt(4)\n\t"
    "v_smfmac_f32_16x16x64_bf16 v[12:15], v[36:39], v[56:63], v9\n\t"
    "v_smfmac_f32_16x16x64_bf16 v[16:19], v[36:39], v[28:35], v9\n\t"
    "ds_read_b128 v[28:31], v7 offset:16384\n\t"
    "ds_read_b128 v[32:35], v7 offset:17408\n\t"
    "ds_read_b128 v[36:39], v6 offset:7168\n\t"
    "s_waitcnt lgkmcnt(6)\n\t"
    "v_smfmac_f32_16x16x64_bf16 v[12:15], v[44:47], v[64:71], v9\n\t"
    "v_smfmac_f32_16x16x64_bf16 v[16:19], v[44:47], v[72:79], v9\n\t"
    "ds_read_b128 v[44:47], v6 offset:8192\n\t"
    "s_waitcnt lgkmcnt(6)\n\t"
    "v_smfmac_f32_16x16x64_bf16 v[12:15], v[48:51], v[80:87], v9\n\t"
    "v_smfmac_f32_16x16x64_bf16 v[16:19], v[48:51], v[88:95], v9\n\t"
    "ds_read_b128 v[48:51], v6 offset:9216\n\t"
    "s_waitcnt lgkmcnt(6)\n\t"
    "v_smfmac_f32_16x16x64_bf16 v[12:15], v[52:55], v[96:103], v9\n\t"
    "v_smfmac_f32_16x16x64_bf16 v[16:19], v[52:55], v[104:111], v9\n\t"
    "ds_read_b128 v[52:55], v6 offset:2048\n\t"
    "s_waitcnt lgkmcnt(6)\n\t"
    "v_smfmac_f32_16x16x64_bf16 v[12:15], v[40:43], v[20:27], v9\n\t"
    "ds_read_b128 v[20:23], v8 offset:0\n\t"
    "ds_read_b128 v[24:27], v8 offset:1024\n\t"
    "s_waitcnt lgkmcnt(6)\n\t"
    "v_smfmac_f32_16x16x64_bf16 v[16:19], v[40:43], v[28:35], v9\n\t"
    "ds_read_b128 v[28:31], v8 offset:8192\n\t"
    "ds_read_b128 v[32:35], v8 offset:9216\n\t"
    "ds_read_b128 v[40:43], v6 offset:10240\n\t"
    "s_waitcnt lgkmcnt(8)\n\t"
    "v_smfmac_f32_16x16x64_bf16 v[12:15], v[36:39], v[112:119], v9\n\t"
    "v_smfmac_f32_16x16x64_bf16 v[16:19], v[36:39], v[120:127], v9\n\t"
    "ds_read_b128 v[36:39], v6 offset:11264\n\t"
    "s_waitcnt lgkmcnt(8)\n\t"
    "v_smfmac_f32_16x16x64_bf16 v[12:15], v[44:47], v[128:135], v9\n\t"
    "v_smfmac_f32_16x16x64_bf16 v[16:19], v[44:47], v[136:143], v9\n\t"
    "ds_read_b128 v[44:47], v6 offset:12288\n\t"
    "s_barrier\n\t"
    "s_setprio 2\n\t"
    "s_waitcnt lgkmcnt(8)\n\t"
    "v_smfmac_f32_16x16x64_bf16 v[12:15], v[48:51], v[144:151], v9\n\t"
    "v_smfmac_f32_16x16x64_bf16 v[16:19], v[48:51], v[152:159], v9\n\t"
    "ds_read_b128 v[48:51], v6 offset:3072\n\t"
    "s_waitcnt lgkmcnt(6)\n\t"
    "v_smfmac_f32_16x16x64_bf16 v[12:15], v[52:55], v[20:27], v9\n\t"
    "ds_read_b128 v[20:23], v8 offset:16384\n\t"
    "ds_read_b128 v[24:27], v8 offset:17408\n\t"
    "s_waitcnt lgkmcnt(6)\n\t"
    "v_smfmac_f32_16x16x64_bf16 v[16:19], v[52:55], v[28:35], v9\n\t"
    "ds_read_b128 v[28:31], v8 offset:24576\n\t"
    "ds_read_b128 v[32:35], v8 offset:25600\n\t"
    "ds_read_b128 v[52:55], v6 offset:13312\n\t"
    "s_waitcnt lgkmcnt(8)\n\t"
    "v_smfmac_f32_16x16x64_bf16 v[12:15], v[40:43], v[160:167], v9\n\t"
    "v_smfmac_f32_16x16x64_bf16 v[16:19], v[40:43], v[168:175], v9\n\t"
    "s_waitcnt lgkmcnt(7)\n\t"
    "v_smfmac_f32_16x16x64_bf16 v[12:15], v[36:39], v[176:183], v9\n\t"
    "v_smfmac_f32_16x16x64_bf16 v[16:19], v[36:39], v[184:191], v9\n\t"
    "s_waitcnt lgkmcnt(6)\n\t"
    "v_smfmac_f32_16x16x64_bf16 v[12:15], v[44:47], v[192:199], v9\n\t"
    "v_smfmac_f32_16x16x64_bf16 v[16:19], v[44:47], v[200:207], v9\n\t"
    "s_waitcnt lgkmcnt(3)\n\t"
    "v_smfmac_f32_16x16x64_bf16 v[12:15], v[48:51], v[20:27], v9\n\t"
    "s_waitcnt lgkmcnt(1)\n\t"
    "v_smfmac_f32_16x16x64_bf16 v[16:19], v[48:51], v[28:35], v9\n\t"
    "s_waitcnt lgkmcnt(0)\n\t"
    "v_smfmac_f32_16x16x64_bf16 v[12:15], v[52:55], v[208:215], v9\n\t"
    "v_smfmac_f32_16x16x64_bf16 v[16:19], v[52:55], v[216:223], v9\n\t"
    "s_nop 6\n\t"
    "v_permlane16_swap_b32 v12, v14\n\t"
    "v_permlane16_swap_b32 v13, v15\n\t"
    "v_permlane16_swap_b32 v16, v18\n\t"
    "v_permlane16_swap_b32 v17, v19\n\t"
    "v_add_f32 v12, v12, v14\n\t"
    "v_add_f32 v13, v13, v15\n\t"
    "v_add_f32 v16, v16, v18\n\t"
    "v_cvt_pk_bf16_f32 v12, v12, v13\n\t"
    "v_add_f32 v17, v17, v19\n\t"
    "ds_write_b32 v10, v12 offset:36352\n\t"
    "v_cvt_pk_bf16_f32 v16, v16, v17\n\t"
    "ds_write_b32 v10, v16 offset:36608\n\t"
    "s_waitcnt lgkmcnt(0)\n\t"
    "s_setprio 0\n\t"
    "s_barrier\n\t"
    "s_barrier\n\t"
    "s_waitcnt lgkmcnt(0)\n\t"
    "s_branch 9f\n\t"
    "1:\n\t"
    "s_add_u32 %[sp], %[sp], 1\n\t"
    "s_cmp_ge_u32 %[sp], %[cap]\n\t"
    "s_cbranch_scc1 7f\n\t"
    "global_load_dwordx4 v[36:39], v6, %[ag] sc1\n\t"
    "global_load_dwordx4 v[40:43], v6, %[ag] offset:1024 sc1\n\t"
    "s_waitcnt lgkmcnt(0)\n\t"
    "s_memtime %[tc]\n\t"
    "s_nop 7\n\t"
    "s_branch 3b\n\t"
    "7:\n\t"
    "s_mov_b32 %[cap], 0\n\t"
    "s_branch 2b\n\t"
    "9:"
    : [tc] "=&s"(tc), [sp] "=&s"(sp), [cap] "+s"(cap)
    : [ag] "s"(ag), [c44] "s"(0x4444u), "{v[64:95]}"(W.w0), "{v[96:127]}"(W.w1), "{v[128:159]}"(W.w2), "{v[160:191]}"(W.w3), "{v[192:223]}"(W.w4), "{v[224:255]}"(W.w5), "{v[56:63]}"(W.x)
    : "v6", "v7", "v8", "v9", "v10", "v11", "v12", "v13", "v14", "v15", "v16", "v17", "v18", "v19", "v20", "v21", "v22", "v23", "v24", "v25", "v26", "v27", "v28", "v29", "v30", "v31", "v32", "v33", "v34", "v35", "v36", "v37", "v38", "v39", "v40", "v41", "v42", "v43", "v44", "v45", "v46", "v47", "v48", "v49", "v50", "v51", "v52", "v53", "v54", "v55", "vcc", "scc", "memory");
}
template <int I> __device__ __forceinline__ void qk_wload(u32x32& w, unsigned a0, unsigned a1);
template <> __device__ __forceinline__ void qk_wload<0>(u32x32& w, unsigned a0, unsigned a1) {
  asm volatile("ds_read_b64_tr_b16 v[64:65], %1 offset:0\n\t"
    "ds_read_b64_tr_b16 v[66:67], %1 offset:512\n\t"
    "ds_read_b64_tr_b16 v[68:69], %1 offset:4096\n\t"
    "ds_read_b64_tr_b16 v[70:71], %1 offset:4608\n\t"
    "ds_read_b64_tr_b16 v[72:73], %2 offset:0\n\t"
    "ds_read_b64_tr_b16 v[74:75], %2 offset:512\n\t"
    "ds_read_b64_tr_b16 v[76:77], %2 offset:4096\n\t"
    "ds_read_b64_tr_b16 v[78:79], %2 offset:4608\n\t"
    "s_waitcnt lgkmcnt(0)\n\t"
    "ds_read_b64_tr_b16 v[80:81], %1 offset:16384\n\t"
    "ds_read_b64_tr_b16 v[82:83], %1 offset:16896\n\t"
    "ds_read_b64_tr_b16 v[84:85], %1 offset:20480\n\t"
    "ds_read_b64_tr_b16 v[86:87], %1 offset:20992\n\t"
    "ds_read_b64_tr_b16 v[88:89], %2 offset:16384\n\t"
    "ds_read_b64_tr_b16 v[90:91], %2 offset:16896\n\t"
    "ds_read_b64_tr_b16 v[92:93], %2 offset:20480\n\t"
    "ds_read_b64_tr_b16 v[94:95], %2 offset:20992\n\t"
    "s_waitcnt lgkmcnt(0)"
    : "=&{v[64:95]}"(w) : "v"(a0), "v"(a1) : "memory");
}
template <> __device__ __forceinline__ void qk_wload<1>(u32x32& w, unsigned a0, unsigned a1) {
  asm volatile("ds_read_b64_tr_b16 v[96:97], %1 offset:0\n\t"
    "ds_read_b64_tr_b16 v[98:99], %1 offset:512\n\t"
    "ds_read_b64_tr_b16 v[100:101], %1 offset:4096\n\t"
    "ds_read_b64_tr_b16 v[102:103], %1 offset:4608\n\t"
    "ds_read_b64_tr_b16 v[104:105], %2 offset:0\n\t"
    "ds_read_b64_tr_b16 v[106:107], %2 offset:512\n\t"
    "ds_read_b64_tr_b16 v[108:109], %2 offset:4096\n\t"
    "ds_read_b64_tr_b16 v[110:111], %2 offset:4608\n\t"
    "s_waitcnt lgkmcnt(0)\n\t"
    "ds_read_b64_tr_b16 v[112:113], %1 offset:16384\n\t"
    "ds_read_b64_tr_b16 v[114:115], %1 offset:16896\n\t"
    "ds_read_b64_tr_b16 v[116:117], %1 offset:20480\n\t"
    "ds_read_b64_tr_b16 v[118:119], %1 offset:20992\n\t"
    "ds_read_b64_tr_b16 v[120:121], %2 offset:16384\n\t"
    "ds_read_b64_tr_b16 v[122:123], %2 offset:16896\n\t"
    "ds_read_b64_tr_b16 v[124:125], %2 offset:20480\n\t"
    "ds_read_b64_tr_b16 v[126:127], %2 offset:20992\n\t"
    "s_waitcnt lgkmcnt(0)"
    : "=&{v[96:127]}"(w) : "v"(a0), "v"(a1) : "memory");
}
template <> __device__ __forceinline__ void qk_wload<2>(u32x32& w, unsigned a0, unsigned a1) {
  asm volatile("ds_read_b64_tr_b16 v[128:129], %1 offset:0\n\t"
    "ds_read_b64_tr_b16 v[130:131], %1 offset:512\n\t"
    "ds_read_b64_tr_b16 v[132:133], %1 offset:4096\n\t"
    "ds_read_b64_tr_b16 v[134:135], %1 offset:4608\n\t"
    "ds_read_b64_tr_b16 v[136:137], %2 offset:0\n\t"
    "ds_read_b64_tr_b16 v[138:139], %2 offset:512\n\t"
    "ds_read_b64_tr_b16 v[140:141], %2 offset:4096\n\t"
    "ds_read_b64_tr_b16 v[142:143], %2 offset:4608\n\t"
    "s_waitcnt lgkmcnt(0)\n\t"
    "ds_read_b64_tr_b16 v[144:145], %1 offset:16384\n\t"
    "ds_read_b64_tr_b16 v[146:147], %1 offset:16896\n\t"
    "ds_read_b64_tr_b16 v[148:149], %1 offset:20480\n\t"
    "ds_read_b64_tr_b16 v[150:151], %1 offset:20992\n\t"
    "ds_read_b64_tr_b16 v[152:153], %2 offset:16384\n\t"
    "ds_read_b64_tr_b16 v[154:155], %2 offset:16896\n\t"
    "ds_read_b64_tr_b16 v[156:157], %2 offset:20480\n\t"
    "ds_read_b64_tr_b16 v[158:159], %2 offset:20992\n\t"
    "s_waitcnt lgkmcnt(0)"
    : "=&{v[128:159]}"(w) : "v"(a0), "v"(a1) : "memory");
}
template <> __device__ __forceinline__ void qk_wload<3>(u32x32& w, unsigned a0, unsigned a1) {
  asm volatile("ds_read_b64_tr_b16 v[160:161], %1 offset:0\n\t"
    "ds_read_b64_tr_b16 v[162:163], %1 offset:512\n\t"
    "ds_read_b64_tr_b16 v[164:165], %1 offset:4096\n\t"
    "ds_read_b64_tr_b16 v[166:167], %1 offset:4608\n\t"
    "ds_read_b64_tr_b16 v[168:169], %2 offset:0\n\t"
    "ds_read_b64_tr_b16 v[170:171], %2 offset:512\n\t"
    "ds_read_b64_tr_b16 v[172:173], %2 offset:4096\n\t"
    "ds_read_b64_tr_b16 v[174:175], %2 offset:4608\n\t"
    "s_waitcnt lgkmcnt(0)\n\t"
    "ds_read_b64_tr_b16 v[176:177], %1 offset:16384\n\t"
    "ds_read_b64_tr_b16 v[178:179], %1 offset:16896\n\t"
    "ds_read_b64_tr_b16 v[180:181], %1 offset:20480\n\t"
    "ds_read_b64_tr_b16 v[182:183], %1 offset:20992\n\t"
    "ds_read_b64_tr_b16 v[184:185], %2 offset:16384\n\t"
    "ds_read_b64_tr_b16 v[186:187], %2 offset:16896\n\t"
    "ds_read_b64_tr_b16 v[188:189], %2 offset:20480\n\t"
    "ds_read_b64_tr_b16 v[190:191], %2 offset:20992\n\t"
    "s_waitcnt lgkmcnt(0)"
    : "=&{v[160:191]}"(w) : "v"(a0), "v"(a1) : "memory");
}
template <> __device__ __forceinline__ void qk_wload<4>(u32x32& w, unsigned a0, unsigned a1) {
  asm volatile("ds_read_b64_tr_b16 v[192:193], %1 offset:0\n\t"
    "ds_read_b64_tr_b16 v[194:195], %1 offset:512\n\t"
    "ds_read_b64_tr_b16 v[196:197], %1 offset:4096\n\t"
    "ds_read_b64_tr_b16 v[198:199], %1 offset:4608\n\t"
    "ds_read_b64_tr_b16 v[200:201], %2 offset:0\n\t"
    "ds_read_b64_tr_b16 v[202:203], %2 offset:512\n\t"
    "ds_read_b64_tr_b16 v[204:205], %2 offset:4096\n\t"
    "ds_read_b64_tr_b16 v[206:207], %2 offset:4608\n\t"
    "s_waitcnt lgkmcnt(0)\n\t"
    "ds_read_b64_tr_b16 v[208:209], %1 offset:16384\n\t"
    "ds_read_b64_tr_b16 v[210:211], %1 offset:16896\n\t"
    "ds_read_b64_tr_b16 v[212:213], %1 offset:20480\n\t"
    "ds_read_b64_tr_b16 v[214:215], %1 offset:20992\n\t"
    "ds_read_b64_tr_b16 v[216:217], %2 offset:16384\n\t"
    "ds_read_b64_tr_b16 v[218:219], %2 offset:16896\n\t"
    "ds_read_b64_tr_b16 v[220:221], %2 offset:20480\n\t"
    "ds_read_b64_tr_b16 v[222:223], %2 offset:20992\n\t"
    "s_waitcnt lgkmcnt(0)"
    : "=&{v[192:223]}"(w) : "v"(a0), "v"(a1) : "memory");
}
template <> __device__ __forceinline__ void qk_wload<5>(u32x32& w, unsigned a0, unsigned a1) {
  asm volatile("ds_read_b64_tr_b16 v[224:225], %1 offset:0\n\t"
    "ds_read_b64_tr_b16 v[226:227], %1 offset:512\n\t"
    "ds_read_b64_tr_b16 v[228:229], %1 offset:4096\n\t"
    "ds_read_b64_tr_b16 v[230:231], %1 offset:4608\n\t"
    "ds_read_b64_tr_b16 v[232:233], %2 offset:0\n\t"
    "ds_read_b64_tr_b16 v[234:235], %2 offset:512\n\t"
    "ds_read_b64_tr_b16 v[236:237], %2 offset:4096\n\t"
    "ds_read_b64_tr_b16 v[238:239], %2 offset:4608\n\t"
    "s_waitcnt lgkmcnt(0)\n\t"
    "ds_read_b64_tr_b16 v[240:241], %1 offset:16384\n\t"
    "ds_read_b64_tr_b16 v[242:243], %1 offset:16896\n\t"
    "ds_read_b64_tr_b16 v[244:245], %1 offset:20480\n\t"
    "ds_read_b64_tr_b16 v[246:247], %1 offset:20992\n\t"
    "ds_read_b64_tr_b16 v[248:249], %2 offset:16384\n\t"
    "ds_read_b64_tr_b16 v[250:251], %2 offset:16896\n\t"
    "ds_read_b64_tr_b16 v[252:253], %2 offset:20480\n\t"
    "ds_read_b64_tr_b16 v[254:255], %2 offset:20992\n\t"
    "s_waitcnt lgkmcnt(0)"
    : "=&{v[224:255]}"(w) : "v"(a0), "v"(a1) : "memory");
}
#define QK_OWN 0
template <int I> __device__ __forceinline__ void qk_swap(u32x32& w, unsigned a);
template <> __device__ __forceinline__ void qk_swap<0>(u32x32& w, unsigned a) {
  asm volatile("ds_read_b128 v[8:11], %1 offset:0\n\t"
    "ds_read_b128 v[12:15], %1 offset:1024\n\t"
    "ds_read_b128 v[16:19], %1 offset:8192\n\t"
    "ds_read_b128 v[20:23], %1 offset:9216\n\t"
    "ds_read_b128 v[24:27], %1 offset:16384\n\t"
    "ds_read_b128 v[28:31], %1 offset:17408\n\t"
    "ds_read_b128 v[32:35], %1 offset:24576\n\t"
    "ds_read_b128 v[36:39], %1 offset:25600\n\t"
    "ds_write_b128 %1, v[64:67] offset:0\n\t"
    "ds_write_b128 %1, v[68:71] offset:1024\n\t"
    "ds_write_b128 %1, v[72:75] offset:8192\n\t"
    "ds_write_b128 %1, v[76:79] offset:9216\n\t"
    "ds_write_b128 %1, v[80:83] offset:16384\n\t"
    "ds_write_b128 %1, v[84:87] offset:17408\n\t"
    "ds_write_b128 %1, v[88:91] offset:24576\n\t"
    "ds_write_b128 %1, v[92:95] offset:25600\n\t"
    "s_waitcnt lgkmcnt(0)\n\t"
    "v_mov_b32 v64, v8\n\t"
    "v_mov_b32 v65, v9\n\t"
    "v_mov_b32 v66, v10\n\t"
    "v_mov_b32 v67, v11\n\t"
    "v_mov_b32 v68, v12\n\t"
    "v_mov_b32 v69, v13\n\t"
    "v_mov_b32 v70, v14\n\t"
    "v_mov_b32 v71, v15\n\t"
    "v_mov_b32 v72, v16\n\t"
    "v_mov_b32 v73, v17\n\t"
    "v_mov_b32 v74, v18\n\t"
    "v_mov_b32 v75, v19\n\t"
    "v_mov_b32 v76, v20\n\t"
    "v_mov_b32 v77, v21\n\t"
    "v_mov_b32 v78, v22\n\t"
    "v_mov_b32 v79, v23\n\t"
    "v_mov_b32 v80, v24\n\t"
    "v_mov_b32 v81, v25\n\t"
    "v_mov_b32 v82, v26\n\t"
    "v_mov_b32 v83, v27\n\t"
    "v_mov_b32 v84, v28\n\t"
    "v_mov_b32 v85, v29\n\t"
    "v_mov_b32 v86, v30\n\t"
    "v_mov_b32 v87, v31\n\t"
    "v_mov_b32 v88, v32\n\t"
    "v_mov_b32 v89, v33\n\t"
    "v_mov_b32 v90, v34\n\t"
    "v_mov_b32 v91, v35\n\t"
    "v_mov_b32 v92, v36\n\t"
    "v_mov_b32 v93, v37\n\t"
    "v_mov_b32 v94, v38\n\t"
    "v_mov_b32 v95, v39"
    :: "{v[64:95]}"(w), "v"(a) : "v8", "v9", "v10", "v11", "v12", "v13", "v14", "v15", "v16", "v17", "v18", "v19", "v20", "v21", "v22", "v23", "v24", "v25", "v26", "v27", "v28", "v29", "v30", "v31", "v32", "v33", "v34", "v35", "v36", "v37", "v38", "v39", "memory");
}
template <> __device__ __forceinline__ void qk_swap<1>(u32x32& w, unsigned a) {
  asm volatile("ds_read_b128 v[8:11], %1 offset:0\n\t"
    "ds_read_b128 v[12:15], %1 offset:1024\n\t"
    "ds_read_b128 v[16:19], %1 offset:8192\n\t"
    "ds_read_b128 v[20:23], %1 offset:9216\n\t"
    "ds_read_b128 v[24:27], %1 offset:16384\n\t"
    "ds_read_b128 v[28:31], %1 offset:17408\n\t"
    "ds_read_b128 v[32:35], %1 offset:24576\n\t"
    "ds_read_b128 v[36:39], %1 offset:25600\n\t"
    "ds_write_b128 %1, v[96:99] offset:0\n\t"
    "ds_write_b128 %1, v[100:103] offset:1024\n\t"
    "ds_write_b128 %1, v[104:107] offset:8192\n\t"
    "ds_write_b128 %1, v[108:111] offset:9216\n\t"
    "ds_write_b128 %1, v[112:115] offset:16384\n\t"
    "ds_write_b128 %1, v[116:119] offset:17408\n\t"
    "ds_write_b128 %1, v[120:123] offset:24576\n\t"
    "ds_write_b128 %1, v[124:127] offset:25600\n\t"
    "s_waitcnt lgkmcnt(0)\n\t"
    "v_mov_b32 v96, v8\n\t"
    "v_mov_b32 v97, v9\n\t"
    "v_mov_b32 v98, v10\n\t"
    "v_mov_b32 v99, v11\n\t"
    "v_mov_b32 v100, v12\n\t"
    "v_mov_b32 v101, v13\n\t"
    "v_mov_b32 v102, v14\n\t"
    "v_mov_b32 v103, v15\n\t"
    "v_mov_b32 v104, v16\n\t"
    "v_mov_b32 v105, v17\n\t"
    "v_mov_b32 v106, v18\n\t"
    "v_mov_b32 v107, v19\n\t"
    "v_mov_b32 v108, v20\n\t"
    "v_mov_b32 v109, v21\n\t"
    "v_mov_b32 v110, v22\n\t"
    "v_mov_b32 v111, v23\n\t"
    "v_mov_b32 v112, v24\n\t"
    "v_mov_b32 v113, v25\n\t"
    "v_mov_b32 v114, v26\n\t"
    "v_mov_b32 v115, v27\n\t"
    "v_mov_b32 v116, v28\n\t"
    "v_mov_b32 v117, v29\n\t"
    "v_mov_b32 v118, v30\n\t"
    "v_mov_b32 v119, v31\n\t"
    "v_mov_b32 v120, v32\n\t"
    "v_mov_b32 v121, v33\n\t"
    "v_mov_b32 v122, v34\n\t"
    "v_mov_b32 v123, v35\n\t"
    "v_mov_b32 v124, v36\n\t"
    "v_mov_b32 v125, v37\n\t"
    "v_mov_b32 v126, v38\n\t"
    "v_mov_b32 v127, v39"
    :: "{v[96:127]}"(w), "v"(a) : "v8", "v9", "v10", "v11", "v12", "v13", "v14", "v15", "v16", "v17", "v18", "v19", "v20", "v21", "v22", "v23", "v24", "v25", "v26", "v27", "v28", "v29", "v30", "v31", "v32", "v33", "v34", "v35", "v36", "v37", "v38", "v39", "memory");
}
template <> __device__ __forceinline__ void qk_swap<2>(u32x32& w, unsigned a) {
  asm volatile("ds_read_b128 v[8:11], %1 offset:0\n\t"
    "ds_read_b128 v[12:15], %1 offset:1024\n\t"
    "ds_read_b128 v[16:19], %1 offset:8192\n\t"
    "ds_read_b128 v[20:23], %1 offset:9216\n\t"
    "ds_read_b128 v[24:27], %1 offset:16384\n\t"
    "ds_read_b128 v[28:31], %1 offset:17408\n\t"
    "ds_read_b128 v[32:35], %1 offset:24576\n\t"
    "ds_read_b128 v[36:39], %1 offset:25600\n\t"
    "ds_write_b128 %1, v[128:131] offset:0\n\t"
    "ds_write_b128 %1, v[132:135] offset:1024\n\t"
    "ds_write_b128 %1, v[136:139] offset:8192\n\t"
    "ds_write_b128 %1, v[140:143] offset:9216\n\t"
    "ds_write_b128 %1, v[144:147] offset:16384\n\t"
    "ds_write_b128 %1, v[148:151] offset:17408\n\t"
    "ds_write_b128 %1, v[152:155] offset:24576\n\t"
    "ds_write_b128 %1, v[156:159] offset:25600\n\t"
    "s_waitcnt lgkmcnt(0)\n\t"
    "v_mov_b32 v128, v8\n\t"
    "v_mov_b32 v129, v9\n\t"
    "v_mov_b32 v130, v10\n\t"
    "v_mov_b32 v131, v11\n\t"
    "v_mov_b32 v132, v12\n\t"
    "v_mov_b32 v133, v13\n\t"
    "v_mov_b32 v134, v14\n\t"
    "v_mov_b32 v135, v15\n\t"
    "v_mov_b32 v136, v16\n\t"
    "v_mov_b32 v137, v17\n\t"
    "v_mov_b32 v138, v18\n\t"
    "v_mov_b32 v139, v19\n\t"
    "v_mov_b32 v140, v20\n\t"
    "v_mov_b32 v141, v21\n\t"
    "v_mov_b32 v142, v22\n\t"
    "v_mov_b32 v143, v23\n\t"
    "v_mov_b32 v144, v24\n\t"
    "v_mov_b32 v145, v25\n\t"
    "v_mov_b32 v146, v26\n\t"
    "v_mov_b32 v147, v27\n\t"
    "v_mov_b32 v148, v28\n\t"
    "v_mov_b32 v149, v29\n\t"
    "v_mov_b32 v150, v30\n\t"
    "v_mov_b32 v151, v31\n\t"
    "v_mov_b32 v152, v32\n\t"
    "v_mov_b32 v153, v33\n\t"
    "v_mov_b32 v154, v34\n\t"
    "v_mov_b32 v155, v35\n\t"
    "v_mov_b32 v156, v36\n\t"
    "v_mov_b32 v157, v37\n\t"
    "v_mov_b32 v158, v38\n\t"
    "v_mov_b32 v159, v39"
    :: "{v[128:159]}"(w), "v"(a) : "v8", "v9", "v10", "v11", "v12", "v13", "v14", "v15", "v16", "v17", "v18", "v19", "v20", "v21", "v22", "v23", "v24", "v25", "v26", "v27", "v28", "v29", "v30", "v31", "v32", "v33", "v34", "v35", "v36", "v37", "v38", "v39", "memory");
}
template <> __device__ __forceinline__ void qk_swap<3>(u32x32& w, unsigned a) {
  asm volatile("ds_read_b128 v[8:11], %1 offset:0\n\t"
    "ds_read_b128 v[12:15], %1 offset:1024\n\t"
    "ds_read_b128 v[16:19], %1 offset:8192\n\t"
    "ds_read_b128 v[20:23], %1 offset:9216\n\t"
    "ds_read_b128 v[24:27], %1 offset:16384\n\t"
    "ds_read_b128 v[28:31], %1 offset:17408\n\t"
    "ds_read_b128 v[32:35], %1 offset:24576\n\t"
    "ds_read_b128 v[36:39], %1 offset:25600\n\t"
    "ds_write_b128 %1, v[160:163] offset:0\n\t"
    "ds_write_b128 %1, v[164:167] offset:1024\n\t"
    "ds_write_b128 %1, v[168:171] offset:8192\n\t"
    "ds_write_b128 %1, v[172:175] offset:9216\n\t"
    "ds_write_b128 %1, v[176:179] offset:16384\n\t"
    "ds_write_b128 %1, v[180:183] offset:17408\n\t"
    "ds_write_b128 %1, v[184:187] offset:24576\n\t"
    "ds_write_b128 %1, v[188:191] offset:25600\n\t"
    "s_waitcnt lgkmcnt(0)\n\t"
    "v_mov_b32 v160, v8\n\t"
    "v_mov_b32 v161, v9\n\t"
    "v_mov_b32 v162, v10\n\t"
    "v_mov_b32 v163, v11\n\t"
    "v_mov_b32 v164, v12\n\t"
    "v_mov_b32 v165, v13\n\t"
    "v_mov_b32 v166, v14\n\t"
    "v_mov_b32 v167, v15\n\t"
    "v_mov_b32 v168, v16\n\t"
    "v_mov_b32 v169, v17\n\t"
    "v_mov_b32 v170, v18\n\t"
    "v_mov_b32 v171, v19\n\t"
    "v_mov_b32 v172, v20\n\t"
    "v_mov_b32 v173, v21\n\t"
    "v_mov_b32 v174, v22\n\t"
    "v_mov_b32 v175, v23\n\t"
    "v_mov_b32 v176, v24\n\t"
    "v_mov_b32 v177, v25\n\t"
    "v_mov_b32 v178, v26\n\t"
    "v_mov_b32 v179, v27\n\t"
    "v_mov_b32 v180, v28\n\t"
    "v_mov_b32 v181, v29\n\t"
    "v_mov_b32 v182, v30\n\t"
    "v_mov_b32 v183, v31\n\t"
    "v_mov_b32 v184, v32\n\t"
    "v_mov_b32 v185, v33\n\t"
    "v_mov_b32 v186, v34\n\t"
    "v_mov_b32 v187, v35\n\t"
    "v_mov_b32 v188, v36\n\t"
    "v_mov_b32 v189, v37\n\t"
    "v_mov_b32 v190, v38\n\t"
    "v_mov_b32 v191, v39"
    :: "{v[160:191]}"(w), "v"(a) : "v8", "v9", "v10", "v11", "v12", "v13", "v14", "v15", "v16", "v17", "v18", "v19", "v20", "v21", "v22", "v23", "v24", "v25", "v26", "v27", "v28", "v29", "v30", "v31", "v32", "v33", "v34", "v35", "v36", "v37", "v38", "v39", "memory");
}
template <> __device__ __forceinline__ void qk_swap<4>(u32x32& w, unsigned a) {
  asm volatile("ds_read_b128 v[8:11], %1 offset:0\n\t"
    "ds_read_b128 v[12:15], %1 offset:1024\n\t"
    "ds_read_b128 v[16:19], %1 offset:8192\n\t"
    "ds_read_b128 v[20:23], %1 offset:9216\n\t"
    "ds_read_b128 v[24:27], %1 offset:16384\n\t"
    "ds_read_b128 v[28:31], %1 offset:17408\n\t"
    "ds_read_b128 v[32:35], %1 offset:24576\n\t"
    "ds_read_b128 v[36:39], %1 offset:25600\n\t"
    "ds_write_b128 %1, v[192:195] offset:0\n\t"
    "ds_write_b128 %1, v[196:199] offset:1024\n\t"
    "ds_write_b128 %1, v[200:203] offset:8192\n\t"
    "ds_write_b128 %1, v[204:207] offset:9216\n\t"
    "ds_write_b128 %1, v[208:211] offset:16384\n\t"
    "ds_write_b128 %1, v[212:215] offset:17408\n\t"
    "ds_write_b128 %1, v[216:219] offset:24576\n\t"
    "ds_write_b128 %1, v[220:223] offset:25600\n\t"
    "s_waitcnt lgkmcnt(0)\n\t"
    "v_mov_b32 v192, v8\n\t"
    "v_mov_b32 v193, v9\n\t"
    "v_mov_b32 v194, v10\n\t"
    "v_mov_b32 v195, v11\n\t"
    "v_mov_b32 v196, v12\n\t"
    "v_mov_b32 v197, v13\n\t"
    "v_mov_b32 v198, v14\n\t"
    "v_mov_b32 v199, v15\n\t"
    "v_mov_b32 v200, v16\n\t"
    "v_mov_b32 v201, v17\n\t"
    "v_mov_b32 v202, v18\n\t"
    "v_mov_b32 v203, v19\n\t"
    "v_mov_b32 v204, v20\n\t"
    "v_mov_b32 v205, v21\n\t"
    "v_mov_b32 v206, v22\n\t"
    "v_mov_b32 v207, v23\n\t"
    "v_mov_b32 v208, v24\n\t"
    "v_mov_b32 v209, v25\n\t"
    "v_mov_b32 v210, v26\n\t"
    "v_mov_b32 v211, v27\n\t"
    "v_mov_b32 v212, v28\n\t"
    "v_mov_b32 v213, v29\n\t"
    "v_mov_b32 v214, v30\n\t"
    "v_mov_b32 v215, v31\n\t"
    "v_mov_b32 v216, v32\n\t"
    "v_mov_b32 v217, v33\n\t"
    "v_mov_b32 v218, v34\n\t"
    "v_mov_b32 v219, v35\n\t"
    "v_mov_b32 v220, v36\n\t"
    "v_mov_b32 v221, v37\n\t"
    "v_mov_b32 v222, v38\n\t"
    "v_mov_b32 v223, v39"
    :: "{v[192:223]}"(w), "v"(a) : "v8", "v9", "v10", "v11", "v12", "v13", "v14", "v15", "v16", "v17", "v18", "v19", "v20", "v21", "v22", "v23", "v24", "v25", "v26", "v27", "v28", "v29", "v30", "v31", "v32", "v33", "v34", "v35", "v36", "v37", "v38", "v39", "memory");
}
template <> __device__ __forceinline__ void qk_swap<5>(u32x32& w, unsigned a) {
  asm volatile("ds_read_b128 v[8:11], %1 offset:0\n\t"
    "ds_read_b128 v[12:15], %1 offset:1024\n\t"
    "ds_read_b128 v[16:19], %1 offset:8192\n\t"
    "ds_read_b128 v[20:23], %1 offset:9216\n\t"
    "ds_read_b128 v[24:27], %1 offset:16384\n\t"
    "ds_read_b128 v[28:31], %1 offset:17408\n\t"
    "ds_read_b128 v[32:35], %1 offset:24576\n\t"
    "ds_read_b128 v[36:39], %1 offset:25600\n\t"
    "ds_write_b128 %1, v[224:227] offset:0\n\t"
    "ds_write_b128 %1, v[228:231] offset:1024\n\t"
    "ds_write_b128 %1, v[232:235] offset:8192\n\t"
    "ds_write_b128 %1, v[236:239] offset:9216\n\t"
    "ds_write_b128 %1, v[240:243] offset:16384\n\t"
    "ds_write_b128 %1, v[244:247] offset:17408\n\t"
    "ds_write_b128 %1, v[248:251] offset:24576\n\t"
    "ds_write_b128 %1, v[252:255] offset:25600\n\t"
    "s_waitcnt lgkmcnt(0)\n\t"
    "v_mov_b32 v224, v8\n\t"
    "v_mov_b32 v225, v9\n\t"
    "v_mov_b32 v226, v10\n\t"
    "v_mov_b32 v227, v11\n\t"
    "v_mov_b32 v228, v12\n\t"
    "v_mov_b32 v229, v13\n\t"
    "v_mov_b32 v230, v14\n\t"
    "v_mov_b32 v231, v15\n\t"
    "v_mov_b32 v232, v16\n\t"
    "v_mov_b32 v233, v17\n\t"
    "v_mov_b32 v234, v18\n\t"
    "v_mov_b32 v235, v19\n\t"
    "v_mov_b32 v236, v20\n\t"
    "v_mov_b32 v237, v21\n\t"
    "v_mov_b32 v238, v22\n\t"
    "v_mov_b32 v239, v23\n\t"
    "v_mov_b32 v240, v24\n\t"
    "v_mov_b32 v241, v25\n\t"
    "v_mov_b32 v242, v26\n\t"
    "v_mov_b32 v243, v27\n\t"
    "v_mov_b32 v244, v28\n\t"
    "v_mov_b32 v245, v29\n\t"
    "v_mov_b32 v246, v30\n\t"
    "v_mov_b32 v247, v31\n\t"
    "v_mov_b32 v248, v32\n\t"
    "v_mov_b32 v249, v33\n\t"
    "v_mov_b32 v250, v34\n\t"
    "v_mov_b32 v251, v35\n\t"
    "v_mov_b32 v252, v36\n\t"
    "v_mov_b32 v253, v37\n\t"
    "v_mov_b32 v254, v38\n\t"
    "v_mov_b32 v255, v39"
    :: "{v[224:255]}"(w), "v"(a) : "v8", "v9", "v10", "v11", "v12", "v13", "v14", "v15", "v16", "v17", "v18", "v19", "v20", "v21", "v22", "v23", "v24", "v25", "v26", "v27", "v28", "v29", "v30", "v31", "v32", "v33", "v34", "v35", "v36", "v37", "v38", "v39", "memory");
}
template <int I> __device__ __forceinline__ void vr_wload(u32x32& w, unsigned a);
template <> __device__ __forceinline__ void vr_wload<0>(u32x32& w, unsigned a) {
  asm volatile("ds_read_b64_tr_b16 v[64:65], %1 offset:0\n\t"
    "ds_read_b64_tr_b16 v[66:67], %1 offset:256\n\t"
    "ds_read_b64_tr_b16 v[68:69], %1 offset:2048\n\t"
    "ds_read_b64_tr_b16 v[70:71], %1 offset:2304\n\t"
    "ds_read_b64_tr_b16 v[72:73], %1 offset:32\n\t"
    "ds_read_b64_tr_b16 v[74:75], %1 offset:288\n\t"
    "ds_read_b64_tr_b16 v[76:77], %1 offset:2080\n\t"
    "ds_read_b64_tr_b16 v[78:79], %1 offset:2336\n\t"
    "s_waitcnt lgkmcnt(0)\n\t"
    "ds_read_b64_tr_b16 v[80:81], %1 offset:64\n\t"
    "ds_read_b64_tr_b16 v[82:83], %1 offset:320\n\t"
    "ds_read_b64_tr_b16 v[84:85], %1 offset:2112\n\t"
    "ds_read_b64_tr_b16 v[86:87], %1 offset:2368\n\t"
    "ds_read_b64_tr_b16 v[88:89], %1 offset:96\n\t"
    "ds_read_b64_tr_b16 v[90:91], %1 offset:352\n\t"
    "ds_read_b64_tr_b16 v[92:93], %1 offset:2144\n\t"
    "ds_read_b64_tr_b16 v[94:95], %1 offset:2400\n\t"
    "s_waitcnt lgkmcnt(0)"
    : "=&{v[64:95]}"(w) : "v"(a) : "memory");
}
template <> __device__ __forceinline__ void vr_wload<1>(u32x32& w, unsigned a) {
  asm volatile("ds_read_b64_tr_b16 v[96:97], %1 offset:8192\n\t"
    "ds_read_b64_tr_b16 v[98:99], %1 offset:8448\n\t"
    "ds_read_b64_tr_b16 v[100:101], %1 offset:10240\n\t"
    "ds_read_b64_tr_b16 v[102:103], %1 offset:10496\n\t"
    "ds_read_b64_tr_b16 v[104:105], %1 offset:8224\n\t"
    "ds_read_b64_tr_b16 v[106:107], %1 offset:8480\n\t"
    "ds_read_b64_tr_b16 v[108:109], %1 offset:10272\n\t"
    "ds_read_b64_tr_b16 v[110:111], %1 offset:10528\n\t"
    "s_waitcnt lgkmcnt(0)\n\t"
    "ds_read_b64_tr_b16 v[112:113], %1 offset:8256\n\t"
    "ds_read_b64_tr_b16 v[114:115], %1 offset:8512\n\t"
    "ds_read_b64_tr_b16 v[116:117], %1 offset:10304\n\t"
    "ds_read_b64_tr_b16 v[118:119], %1 offset:10560\n\t"
    "ds_read_b64_tr_b16 v[120:121], %1 offset:8288\n\t"
    "ds_read_b64_tr_b16 v[122:123], %1 offset:8544\n\t"
    "ds_read_b64_tr_b16 v[124:125], %1 offset:10336\n\t"
    "ds_read_b64_tr_b16 v[126:127], %1 offset:10592\n\t"
    "s_waitcnt lgkmcnt(0)"
    : "=&{v[96:127]}"(w) : "v"(a) : "memory");
}
template <> __device__ __forceinline__ void vr_wload<2>(u32x32& w, unsigned a) {
  asm volatile("ds_read_b64_tr_b16 v[128:129], %1 offset:0\n\t"
    "ds_read_b64_tr_b16 v[130:131], %1 offset:128\n\t"
    "ds_read_b64_tr_b16 v[132:133], %1 offset:1024\n\t"
    "ds_read_b64_tr_b16 v[134:135], %1 offset:1152\n\t"
    "ds_read_b64_tr_b16 v[136:137], %1 offset:32\n\t"
    "ds_read_b64_tr_b16 v[138:139], %1 offset:160\n\t"
    "ds_read_b64_tr_b16 v[140:141], %1 offset:1056\n\t"
    "ds_read_b64_tr_b16 v[142:143], %1 offset:1184\n\t"
    "s_waitcnt lgkmcnt(0)\n\t"
    "ds_read_b64_tr_b16 v[144:145], %1 offset:4096\n\t"
    "ds_read_b64_tr_b16 v[146:147], %1 offset:4224\n\t"
    "ds_read_b64_tr_b16 v[148:149], %1 offset:5120\n\t"
    "ds_read_b64_tr_b16 v[150:151], %1 offset:5248\n\t"
    "ds_read_b64_tr_b16 v[152:153], %1 offset:4128\n\t"
    "ds_read_b64_tr_b16 v[154:155], %1 offset:4256\n\t"
    "ds_read_b64_tr_b16 v[156:157], %1 offset:5152\n\t"
    "ds_read_b64_tr_b16 v[158:159], %1 offset:5280\n\t"
    "s_waitcnt lgkmcnt(0)"
    : "=&{v[128:159]}"(w) : "v"(a) : "memory");
}
template <> __device__ __forceinline__ void vr_wload<3>(u32x32& w, unsigned a) {
  asm volatile("ds_read_b64_tr_b16 v[160:161], %1 offset:8192\n\t"
    "ds_read_b64_tr_b16 v[162:163], %1 offset:8320\n\t"
    "ds_read_b64_tr_b16 v[164:165], %1 offset:9216\n\t"
    "ds_read_b64_tr_b16 v[166:167], %1 offset:9344\n\t"
    "ds_read_b64_tr_b16 v[168:169], %1 offset:8224\n\t"
    "ds_read_b64_tr_b16 v[170:171], %1 offset:8352\n\t"
    "ds_read_b64_tr_b16 v[172:173], %1 offset:9248\n\t"
    "ds_read_b64_tr_b16 v[174:175], %1 offset:9376\n\t"
    "s_waitcnt lgkmcnt(0)\n\t"
    "ds_read_b64_tr_b16 v[176:177], %1 offset:12288\n\t"
    "ds_read_b64_tr_b16 v[178:179], %1 offset:12416\n\t"
    "ds_read_b64_tr_b16 v[180:181], %1 offset:13312\n\t"
    "ds_read_b64_tr_b16 v[182:183], %1 offset:13440\n\t"
    "ds_read_b64_tr_b16 v[184:185], %1 offset:12320\n\t"
    "ds_read_b64_tr_b16 v[186:187], %1 offset:12448\n\t"
    "ds_read_b64_tr_b16 v[188:189], %1 offset:13344\n\t"
    "ds_read_b64_tr_b16 v[190:191], %1 offset:13472\n\t"
    "s_waitcnt lgkmcnt(0)"
    : "=&{v[160:191]}"(w) : "v"(a) : "memory");
}
template <> __device__ __forceinline__ void vr_wload<4>(u32x32& w, unsigned a) {
  asm volatile("ds_read_b64_tr_b16 v[192:193], %1 offset:0\n\t"
    "ds_read_b64_tr_b16 v[194:195], %1 offset:128\n\t"
    "ds_read_b64_tr_b16 v[196:197], %1 offset:1024\n\t"
    "ds_read_b64_tr_b16 v[198:199], %1 offset:1152\n\t"
    "ds_read_b64_tr_b16 v[200:201], %1 offset:32\n\t"
    "ds_read_b64_tr_b16 v[202:203], %1 offset:160\n\t"
    "ds_read_b64_tr_b16 v[204:205], %1 offset:1056\n\t"
    "ds_read_b64_tr_b16 v[206:207], %1 offset:1184\n\t"
    "s_waitcnt lgkmcnt(0)\n\t"
    "ds_read_b64_tr_b16 v[208:209], %1 offset:4096\n\t"
    "ds_read_b64_tr_b16 v[210:211], %1 offset:4224\n\t"
    "ds_read_b64_tr_b16 v[212:213], %1 offset:5120\n\t"
    "ds_read_b64_tr_b16 v[214:215], %1 offset:5248\n\t"
    "ds_read_b64_tr_b16 v[216:217], %1 offset:4128\n\t"
    "ds_read_b64_tr_b16 v[218:219], %1 offset:4256\n\t"
    "ds_read_b64_tr_b16 v[220:221], %1 offset:5152\n\t"
    "ds_read_b64_tr_b16 v[222:223], %1 offset:5280\n\t"
    "s_waitcnt lgkmcnt(0)"
    : "=&{v[192:223]}"(w) : "v"(a) : "memory");
}
template <> __device__ __forceinline__ void vr_wload<5>(u32x32& w, unsigned a) {
  asm volatile("ds_read_b64_tr_b16 v[224:225], %1 offset:8192\n\t"
    "ds_read_b64_tr_b16 v[226:227], %1 offset:8320\n\t"
    "ds_read_b64_tr_b16 v[228:229], %1 offset:9216\n\t"
    "ds_read_b64_tr_b16 v[230:231], %1 offset:9344\n\t"
    "ds_read_b64_tr_b16 v[232:233], %1 offset:8224\n\t"
    "ds_read_b64_tr_b16 v[234:235], %1 offset:8352\n\t"
    "ds_read_b64_tr_b16 v[236:237], %1 offset:9248\n\t"
    "ds_read_b64_tr_b16 v[238:239], %1 offset:9376\n\t"
    "s_waitcnt lgkmcnt(0)\n\t"
    "ds_read_b64_tr_b16 v[240:241], %1 offset:12288\n\t"
    "ds_read_b64_tr_b16 v[242:243], %1 offset:12416\n\t"
    "ds_read_b64_tr_b16 v[244:245], %1 offset:13312\n\t"
    "ds_read_b64_tr_b16 v[246:247], %1 offset:13440\n\t"
    "ds_read_b64_tr_b16 v[248:249], %1 offset:12320\n\t"
    "ds_read_b64_tr_b16 v[250:251], %1 offset:12448\n\t"
    "ds_read_b64_tr_b16 v[252:253], %1 offset:13344\n\t"
    "ds_read_b64_tr_b16 v[254:255], %1 offset:13472\n\t"
    "s_waitcnt lgkmcnt(0)"
    : "=&{v[224:255]}"(w) : "v"(a) : "memory");
}
#define OREP 2
#define AREP 2
struct VRW { u32x32 v0, v1, o0, o1, o2, o3; };
__device__ __forceinline__ void vr_v(const VRW& W, const char* ag, const char* vd, unsigned wa, unsigned ra, unsigned& cap, unsigned& sp, unsigned long long& tc, unsigned long long* ts) {
  asm volatile("v_mbcnt_lo_u32_b32 v16, -1, 0\n\t"
    "v_mbcnt_hi_u32_b32 v16, -1, v16\n\t"
    "v_lshlrev_b32 v17, 4, v16\n\t"
    "s_mov_b32 %[sp], 0\n\t"
    "global_load_dwordx4 v[24:27], v17, %[ag] sc1\n\t"
    "global_load_dwordx4 v[28:31], v17, %[ag] offset:1024 sc1\n\t"
    "s_memtime %[tc]\n\t"
    "v_and_b32 v22, 4, v16\n\t"
    "v_mul_u32_u24 v22, 0x2aaa, v22\n\t"
    "v_lshrrev_b32 v18, 1, v16\n\t"
    "v_and_b32 v18, 2, v18\n\t"
    "v_or3_b32 v18, v18, v22, %[c44]\n\t"
    "v_lshl_add_u32 v19, v16, 3, %[wa]\n\t"
    "v_lshl_add_u32 v20, v16, 3, %[ra]\n\t"
    "v_and_b32 v22, 40, v16\n\t"
    "v_lshlrev_b32 v21, 3, v22\n\t"
    "v_and_b32 v22, 3, v16\n\t"
    "v_lshl_or_b32 v21, v22, 4, v21\n\t"
    "v_and_b32 v22, 4, v16\n\t"
    "v_lshl_or_b32 v21, v22, 1, v21\n\t"
    "v_lshrrev_b32 v22, 2, v16\n\t"
    "v_and_or_b32 v21, v22, 4, v21\n\t"
    "3:\n\t"
    "v_mov_b32 v32, 0\n\t"
    "v_mov_b32 v33, 0\n\t"
    "v_mov_b32 v34, 0\n\t"
    "v_mov_b32 v35, 0\n\t"
    "v_mov_b32 v36, 0\n\t"
    "v_mov_b32 v37, 0\n\t"
    "v_mov_b32 v38, 0\n\t"
    "v_mov_b32 v39, 0\n\t"
    "v_mov_b32 v40, 0\n\t"
    "v_mov_b32 v41, 0\n\t"
    "v_mov_b32 v42, 0\n\t"
    "v_mov_b32 v43, 0\n\t"
    "v_mov_b32 v44, 0\n\t"
    "v_mov_b32 v45, 0\n\t"
    "v_mov_b32 v46, 0\n\t"
    "v_mov_b32 v47, 0\n\t"
    "s_waitcnt vmcnt(0) lgkmcnt(0)\n\t"
    "v_smfmac_f32_16x16x64_bf16 v[32:35], v[24:27], v[64:71], v18\n\t"
    "v_smfmac_f32_16x16x64_bf16 v[32:35], v[28:31], v[96:103], v18\n\t"
    "v_max3_u32 v22, v24, v25, v26\n\t"
    "v_max3_u32 v23, v27, v28, v29\n\t"
    "v_smfmac_f32_16x16x64_bf16 v[36:39], v[24:27], v[72:79], v18\n\t"
    "v_max3_u32 v22, v22, v30, v31\n\t"
    "v_max_u32 v22, v22, v23\n\t"
    "v_smfmac_f32_16x16x64_bf16 v[36:39], v[28:31], v[104:111], v18\n\t"
    "v_cmp_eq_u32 vcc, -1, v22\n\t"
    "s_cbranch_vccnz 1f\n\t"
    "2:\n\t"
    "v_smfmac_f32_16x16x64_bf16 v[40:43], v[24:27], v[80:87], v18\n\t"
    "v_permlane16_swap_b32 v32, v34\n\t"
    "v_permlane16_swap_b32 v33, v35\n\t"
    "v_smfmac_f32_16x16x64_bf16 v[40:43], v[28:31], v[112:119], v18\n\t"
    "v_pk_add_f32 v[32:33], v[32:33], v[34:35]\n\t"
    "ds_write_b64 v19, v[32:33] offset:0\n\t"
    "v_smfmac_f32_16x16x64_bf16 v[44:47], v[24:27], v[88:95], v18\n\t"
    "s_nop 0\n\t"
    "v_permlane16_swap_b32 v36, v38\n\t"
    "v_permlane16_swap_b32 v37, v39\n\t"
    "v_smfmac_f32_16x16x64_bf16 v[44:47], v[28:31], v[120:127], v18\n\t"
    "v_pk_add_f32 v[36:37], v[36:37], v[38:39]\n\t"
    "ds_write_b64 v19, v[36:37] offset:512\n\t"
    "v_permlane16_swap_b32 v40, v42\n\t"
    "v_permlane16_swap_b32 v41, v43\n\t"
    "v_pk_add_f32 v[40:41], v[40:41], v[42:43]\n\t"
    "ds_write_b64 v19, v[40:41] offset:1024\n\t"
    "s_nop 1\n\t"
    "v_permlane16_swap_b32 v44, v46\n\t"
    "v_permlane16_swap_b32 v45, v47\n\t"
    "v_pk_add_f32 v[44:45], v[44:45], v[46:47]\n\t"
    "ds_write_b64 v19, v[44:45] offset:1536\n\t"
    "s_cmp_ge_u32 %[wa], 8192\n\t"
    "s_waitcnt lgkmcnt(0)\n\t"
    "s_barrier\n\t"
    "s_cbranch_scc1 9f\n\t"
    "ds_read_b64 v[48:49], v20 offset:0\n\t"
    "ds_read_b64 v[50:51], v20 offset:2048\n\t"
    "ds_read_b64 v[52:53], v20 offset:4096\n\t"
    "ds_read_b64 v[54:55], v20 offset:6144\n\t"
    "ds_read_b64 v[56:57], v20 offset:8192\n\t"
    "ds_read_b64 v[58:59], v20 offset:10240\n\t"
    "ds_read_b64 v[60:61], v20 offset:12288\n\t"
    "ds_read_b64 v[62:63], v20 offset:14336\n\t"
    "s_waitcnt lgkmcnt(6)\n\t"
    "v_pk_add_f32 v[48:49], v[48:49], v[50:51]\n\t"
    "s_waitcnt lgkmcnt(4)\n\t"
    "v_pk_add_f32 v[52:53], v[52:53], v[54:55]\n\t"
    "v_pk_add_f32 v[48:49], v[48:49], v[52:53]\n\t"
    "s_waitcnt lgkmcnt(2)\n\t"
    "v_pk_add_f32 v[56:57], v[56:57], v[58:59]\n\t"
    "s_waitcnt lgkmcnt(0)\n\t"
    "v_pk_add_f32 v[60:61], v[60:61], v[62:63]\n\t"
    "v_pk_add_f32 v[56:57], v[56:57], v[60:61]\n\t"
    "v_pk_add_f32 v[48:49], v[48:49], v[56:57]\n\t"
    "v_cvt_pk_bf16_f32 v22, v48, v49\n\t"
    "v_min_u32 v22, -2, v22\n\t"
    "global_store_dword v21, v22, %[vd] sc1\n\t"
    "s_waitcnt lgkmcnt(0)\n\t"
    "s_branch 9f\n\t"
    "1:\n\t"
    "s_add_u32 %[sp], %[sp], 1\n\t"
    "s_cmp_ge_u32 %[sp], %[cap]\n\t"
    "s_cbranch_scc1 7f\n\t"
    "global_load_dwordx4 v[24:27], v17, %[ag] sc1\n\t"
    "global_load_dwordx4 v[28:31], v17, %[ag] offset:1024 sc1\n\t"
    "s_waitcnt lgkmcnt(0)\n\t"
    "s_memtime %[tc]\n\t"
    "s_nop 7\n\t"
    "s_branch 3b\n\t"
    "7:\n\t"
    "s_mov_b32 %[cap], 0\n\t"
    "s_branch 2b\n\t"
    "9:"
    : [tc] "=&s"(tc), [sp] "=&s"(sp), [cap] "+s"(cap)
    : [ag] "s"(ag), [vd] "s"(vd), [wa] "s"(wa), [ra] "s"(ra), [c44] "s"(0x4444u), "{v[64:95]}"(W.v0), "{v[96:127]}"(W.v1), "{v[128:159]}"(W.o0), "{v[160:191]}"(W.o1), "{v[192:223]}"(W.o2), "{v[224:255]}"(W.o3)
    : "v16", "v17", "v18", "v19", "v20", "v21", "v22", "v23", "v24", "v25", "v26", "v27", "v28", "v29", "v30", "v31", "v32", "v33", "v34", "v35", "v36", "v37", "v38", "v39", "v40", "v41", "v42", "v43", "v44", "v45", "v46", "v47", "v48", "v49", "v50", "v51", "v52", "v53", "v54", "v55", "v56", "v57", "v58", "v59", "v60", "v61", "v62", "v63", "vcc", "scc", "memory");
}
__device__ __forceinline__ void vr_r(const VRW& W, const char* os, const char* ad, unsigned wa, unsigned jx, unsigned& cap, unsigned& sp, unsigned long long* ts) {
  asm volatile("v_mbcnt_lo_u32_b32 v16, -1, 0\n\t"
    "v_mbcnt_hi_u32_b32 v16, -1, v16\n\t"
    "v_lshl_or_b32 v17, v16, 4, %[jx]\n\t"
    "global_load_dwordx4 v[24:27], v17, %[os] sc1\n\t"
    "v_xor_b32 v18, 2048, v17\n\t"
    "global_load_dwordx4 v[28:31], v18, %[os] sc1\n\t"
    "v_xor_b32 v57, 4096, v17\n\t"
    "global_load_dwordx4 v[32:35], v57, %[os] sc1\n\t"
    "v_xor_b32 v58, 6144, v17\n\t"
    "global_load_dwordx4 v[36:39], v58, %[os] sc1\n\t"
    "s_mov_b32 %[sp], 0\n\t"
    "v_and_b32 v23, 4, v16\n\t"
    "v_mul_u32_u24 v23, 0x2aaa, v23\n\t"
    "v_lshrrev_b32 v19, 1, v16\n\t"
    "v_and_b32 v19, 2, v19\n\t"
    "v_or3_b32 v19, v19, v23, %[c44]\n\t"
    "v_and_b32 v23, 15, v16\n\t"
    "v_lshl_add_u32 v20, v23, 2, %[wa]\n\t"
    "v_and_b32 v23, 48, v16\n\t"
    "v_lshl_add_u32 v20, v23, 3, v20\n\t"
    "v_and_b32 v23, 32, v16\n\t"
    "v_and_b32 v21, 28, v16\n\t"
    "v_lshlrev_b32 v21, 4, v21\n\t"
    "v_or_b32 v21, v21, v23\n\t"
    "v_lshlrev_b32 v22, 3, v23\n\t"
    "v_and_b32 v23, 3, v16\n\t"
    "v_lshl_or_b32 v21, v23, 3, v21\n\t"
    "v_and_b32 v23, 31, v16\n\t"
    "v_lshl_or_b32 v22, v23, 2, v22\n\t"
    "3:\n\t"
    "v_mov_b32 v40, 0\n\t"
    "v_mov_b32 v41, 0\n\t"
    "v_mov_b32 v42, 0\n\t"
    "v_mov_b32 v43, 0\n\t"
    "v_mov_b32 v44, 0\n\t"
    "v_mov_b32 v45, 0\n\t"
    "v_mov_b32 v46, 0\n\t"
    "v_mov_b32 v47, 0\n\t"
    "v_mov_b32 v48, 0\n\t"
    "v_mov_b32 v49, 0\n\t"
    "v_mov_b32 v50, 0\n\t"
    "v_mov_b32 v51, 0\n\t"
    "v_mov_b32 v52, 0\n\t"
    "v_mov_b32 v53, 0\n\t"
    "v_mov_b32 v54, 0\n\t"
    "v_mov_b32 v55, 0\n\t"
    "v_mov_b32 v56, 0\n\t"
    "s_waitcnt vmcnt(3)\n\t"
    "v_smfmac_f32_16x16x64_bf16 v[44:47], v[24:27], v[144:151], v19\n\t"
    "v_max3_u32 v56, v56, v24, v25\n\t"
    "v_smfmac_f32_16x16x64_bf16 v[48:51], v[24:27], v[136:143], v19\n\t"
    "v_max3_u32 v56, v56, v26, v27\n\t"
    "v_smfmac_f32_16x16x64_bf16 v[40:43], v[24:27], v[128:135], v19\n\t"
    "v_smfmac_f32_16x16x64_bf16 v[52:55], v[24:27], v[152:159], v19\n\t"
    "s_waitcnt vmcnt(2)\n\t"
    "v_smfmac_f32_16x16x64_bf16 v[44:47], v[28:31], v[176:183], v19\n\t"
    "v_max3_u32 v56, v56, v28, v29\n\t"
    "v_smfmac_f32_16x16x64_bf16 v[48:51], v[28:31], v[168:175], v19\n\t"
    "v_max3_u32 v56, v56, v30, v31\n\t"
    "v_smfmac_f32_16x16x64_bf16 v[40:43], v[28:31], v[160:167], v19\n\t"
    "v_smfmac_f32_16x16x64_bf16 v[52:55], v[28:31], v[184:191], v19\n\t"
    "s_waitcnt vmcnt(1)\n\t"
    "v_smfmac_f32_16x16x64_bf16 v[44:47], v[32:35], v[208:215], v19\n\t"
    "v_max3_u32 v56, v56, v32, v33\n\t"
    "v_smfmac_f32_16x16x64_bf16 v[48:51], v[32:35], v[200:207], v19\n\t"
    "v_max3_u32 v56, v56, v34, v35\n\t"
    "v_smfmac_f32_16x16x64_bf16 v[40:43], v[32:35], v[192:199], v19\n\t"
    "v_smfmac_f32_16x16x64_bf16 v[52:55], v[32:35], v[216:223], v19\n\t"
    "s_waitcnt vmcnt(0)\n\t"
    "v_smfmac_f32_16x16x64_bf16 v[44:47], v[36:39], v[240:247], v19\n\t"
    "v_max3_u32 v56, v56, v36, v37\n\t"
    "v_smfmac_f32_16x16x64_bf16 v[48:51], v[36:39], v[232:239], v19\n\t"
    "v_max3_u32 v56, v56, v38, v39\n\t"
    "v_smfmac_f32_16x16x64_bf16 v[40:43], v[36:39], v[224:231], v19\n\t"
    "v_cmp_eq_u32 vcc, -1, v56\n\t"
    "v_smfmac_f32_16x16x64_bf16 v[52:55], v[36:39], v[248:255], v19\n\t"
    "s_cbranch_vccnz 4f\n\t"
    "5:\n\t"
    "s_nop 2\n\t"
    "v_permlane32_swap_b32 v44, v48\n\t"
    "v_permlane32_swap_b32 v45, v49\n\t"
    "v_permlane32_swap_b32 v46, v50\n\t"
    "v_permlane32_swap_b32 v47, v51\n\t"
    "s_mov_b32 exec_hi, 0\n\t"
    "v_pk_add_f32 v[40:41], v[40:41], v[48:49]\n\t"
    "v_pk_add_f32 v[42:43], v[42:43], v[50:51]\n\t"
    "s_mov_b64 exec, %[hi]\n\t"
    "v_pk_add_f32 v[40:41], v[44:45], v[52:53]\n\t"
    "v_pk_add_f32 v[42:43], v[46:47], v[54:55]\n\t"
    "s_mov_b32 exec_lo, -1\n\t"
    "s_nop 0\n\t"
    "v_permlane16_swap_b32 v40, v42\n\t"
    "v_permlane16_swap_b32 v41, v43\n\t"
    "v_add_f32 v40, v40, v42\n\t"
    "v_add_f32 v41, v41, v43\n\t"
    "ds_write_b32 v20, v40 offset:16384\n\t"
    "ds_write_b32 v20, v41 offset:16448\n\t"
    "s_cmp_ge_u32 %[wa], 1024\n\t"
    "s_waitcnt lgkmcnt(0)\n\t"
    "s_barrier\n\t"
    "s_cbranch_scc1 9f\n\t"
    "ds_read_b64 v[24:25], v21 offset:16384\n\t"
    "ds_read_b64 v[26:27], v21 offset:16896\n\t"
    "ds_read_b64 v[28:29], v21 offset:17408\n\t"
    "ds_read_b64 v[30:31], v21 offset:17920\n\t"
    "ds_read_b64 v[32:33], v21 offset:18432\n\t"
    "ds_read_b64 v[34:35], v21 offset:18944\n\t"
    "ds_read_b64 v[36:37], v21 offset:19456\n\t"
    "ds_read_b64 v[38:39], v21 offset:19968\n\t"
    "s_waitcnt lgkmcnt(6)\n\t"
    "v_pk_add_f32 v[24:25], v[24:25], v[26:27]\n\t"
    "s_waitcnt lgkmcnt(4)\n\t"
    "v_pk_add_f32 v[28:29], v[28:29], v[30:31]\n\t"
    "v_pk_add_f32 v[24:25], v[24:25], v[28:29]\n\t"
    "s_waitcnt lgkmcnt(2)\n\t"
    "v_pk_add_f32 v[32:33], v[32:33], v[34:35]\n\t"
    "s_waitcnt lgkmcnt(0)\n\t"
    "v_pk_add_f32 v[36:37], v[36:37], v[38:39]\n\t"
    "v_pk_add_f32 v[32:33], v[32:33], v[36:37]\n\t"
    "v_pk_add_f32 v[24:25], v[24:25], v[32:33]\n\t"
    "v_cvt_pk_bf16_f32 v23, v24, v25\n\t"
    "v_min_u32 v23, -2, v23\n\t"
    "global_store_dword v22, v23, %[ad] sc1\n\t"
    "s_waitcnt lgkmcnt(0)\n\t"
    "s_branch 9f\n\t"
    "4:\n\t"
    "s_add_u32 %[sp], %[sp], 1\n\t"
    "s_cmp_ge_u32 %[sp], %[cap]\n\t"
    "s_cbranch_scc1 7f\n\t"
    "global_load_dwordx4 v[24:27], v17, %[os] sc1\n\t"
    "global_load_dwordx4 v[28:31], v18, %[os] sc1\n\t"
    "global_load_dwordx4 v[32:35], v57, %[os] sc1\n\t"
    "global_load_dwordx4 v[36:39], v58, %[os] sc1\n\t"
    "s_nop 7\n\t"
    "s_branch 3b\n\t"
    "7:\n\t"
    "s_mov_b32 %[cap], 0\n\t"
    "s_branch 5b\n\t"
    "9:"
    : [sp] "=&s"(sp), [cap] "+s"(cap)
    : [os] "s"(os), [ad] "s"(ad), [wa] "s"(wa), [jx] "s"(jx), [c44] "s"(0x4444u), [hi] "s"(0xFFFFFFFF00000000ull), "{v[64:95]}"(W.v0), "{v[96:127]}"(W.v1), "{v[128:159]}"(W.o0), "{v[160:191]}"(W.o1), "{v[192:223]}"(W.o2), "{v[224:255]}"(W.o3)
    : "v16", "v17", "v18", "v19", "v20", "v21", "v22", "v23", "v24", "v25", "v26", "v27", "v28", "v29", "v30", "v31", "v32", "v33", "v34", "v35", "v36", "v37", "v38", "v39", "v40", "v41", "v42", "v43", "v44", "v45", "v46", "v47", "v48", "v49", "v50", "v51", "v52", "v53", "v54", "v55", "v56", "v57", "v58", "v59", "v60", "v61", "v62", "v63", "vcc", "scc", "memory");
}
__device__ __forceinline__ void qk_body(char* smem, int wave_id_, int xg, int h, int wg, const u16* __restrict__ Wq, const u16* __restrict__ Wk,
                                        const char* __restrict__ Kx, const char* __restrict__ V, char* ws, unsigned* tmo, u64 tag) {
  const int wave = WAVE_ID;
  const int ty = wave >> 2, w4 = wave & 3;
  const char* wq = uni((const char*)(Wq + h * DH)); const char* wk = uni((const char*)(Wk + h * DH));
  auto stage = [&](int u) {
    stage_proj((u & 1) ? wk + (size_t)(((u >> 1) + 2) & 7) * 128 * HD * 2 : wq + (size_t)(u >> 1) * 128 * HD * 2, ((u + 4) % 5) * 32768, wave * 64 + lane_id(), wave);
  };
  QKW W;
  if (wave == 0) STS(ws, wg, 7);
  stage(0); stage(1); stage(2); stage(3);
#define WLD(i, w) do { WAIT_V(12); WBAR(); stage(2 * (i) + 4); if (ty) { WAIT_V(12); WBAR(); stage(2 * (i) + 5); } \
    { const int l_ = lane_id(); const unsigned o_ = ty ? ((2 * (i) + 5) % 5) * 32768 : ((2 * (i) + 4) % 5) * 32768; qk_wload<i>(w, proj_base(2 * w4, l_) + o_, proj_base(2 * w4 + 1, l_) + o_); } \
    if (!ty) { WAIT_V(12); WBAR(); stage(2 * (i) + 5); } if (wave == 0) STS(ws, wg, 1 + (i)); } while (0)
  WLD(0, W.w0); WLD(1, W.w1); WLD(2, W.w2); WLD(3, W.w3); WLD(4, W.w4); WLD(5, W.w5);
#undef WLD
  unsigned in = 0;
#pragma unroll
  for (int i = 0; i < 2; ++i) {
    if (i == 0) WAIT_V(8); else WAIT_V(0);
    WBAR();
    if (i == 0 && wave == 1) init_issue(ws, Q_V);
    if (i == 1) in = init_get(ws, tag, smem + Q_V);
    const int lane = lane_id();
    const unsigned o = Q_RING + ty * 32768 + i * 65536, f0 = proj_base(2 * w4, lane) + o, f1 = proj_base(2 * w4 + 1, lane) + o;
    u32x8 x[4] = {proj_frag(smem, f0), proj_frag(smem, f1), proj_frag(smem, f0 + 16384), proj_frag(smem, f1 + 16384)};
    asm volatile("" : "+v"(x[0]), "+v"(x[1]), "+v"(x[2]), "+v"(x[3]));
    BAR();
    char* d = smem + Q_CB + i * 57344 + ty * (i ? 32768 : 24576) + w4 * 2048 + lane * 16;
#pragma unroll
    for (int j = 0; j < 4; ++j)
      if (i || j || !ty) { *(u32x4*)(d + j * 8192) = u32x4{x[j][0], x[j][1], x[j][2], x[j][3]}; *(u32x4*)(d + j * 8192 + 1024) = u32x4{x[j][4], x[j][5], x[j][6], x[j][7]}; }
    if (i == 0 && ty) { *(u32x4*)(smem + Q_MEM + wave * 2048 + lane * 16) = u32x4{x[0][0], x[0][1], x[0][2], x[0][3]}; *(u32x4*)(smem + Q_MEM + wave * 2048 + 1024 + lane * 16) = u32x4{x[0][4], x[0][5], x[0][6], x[0][7]}; }
  }
  if (wave == 4) *(volatile LAS u32x2*)(smem + Q_TM) = u32x2{0u, 0u};
  BAR();
  if (wave == 0) STS(ws, wg, 0);
  unsigned* cnt = cnt_of(ws, in);
  const char* xb = uni(xch_base(ws) + ((size_t)3 << 21) + (size_t)((in & 2u) << 12));
  const char* oh = uni(xch_base(ws) + (size_t)((in & 1u) << 13) + (size_t)(h >> 2) * 16384 + (h & 3) * 2048);
  auto rearm = [&](int ps) {
    unsigned z = SENT; asm volatile("" : "+v"(z));
    const unsigned l16 = lane_id() * 16;
#pragma unroll
    for (int r = 0; r < 2 * OREP; ++r) st16s(oh + (size_t)(ps * 4 + (r >> 1)) * 131072, (r & 1) * 1024u + l16, u32x4{z, z, z, z});
  };
  const char* dbw = xb + (size_t)48 * 16384 + 4096 + h * 128;
  unsigned avail = 0;
  auto stage_kv = [&](int s) {
    if (avail <= (unsigned)s) {
      for (int spins = 0;; ++spins) {
        const unsigned p = ld4_sc1(cnt + (PQKV + h) * 32 + (lane_id() & 15));
        avail = min(qkv_avail(p, 1), qkv_avail(p, 2));
        if (avail > (unsigned)s) break;
        __builtin_amdgcn_s_sleep(8);
        if (give_up(spins, tmo, 5)) break;
      }
    }
    const char* ks = uni(Kx + ((size_t)s * NH + h) * 8192); const char* vs = uni(V + ((size_t)s * NH + h) * 8192);
    const int lane = lane_id();
    head_to_lds(ks, lane, Q_K); head_to_lds(vs, lane, Q_V);
  };
  auto loop = [&](auto w_c) __attribute__((always_inline)) {
    constexpr int WV = decltype(w_c)::value;
    if (WV >= 4) asm volatile("ds_read_b128 v[56:59], %1\n\tds_read_b128 v[60:63], %1 offset:1024\n\ts_waitcnt lgkmcnt(0)" : "=&{v[56:63]}"(W.x) : "v"(Q_MEM + WV * 2048 + lane_id() * 16) : "memory");
    if (WV == 6) stage_kv(0);
    if (QK_OWN) {
      constexpr int PI = WV < 4 ? WV : WV - 2;
      qk_swap<PI>(PI == 0 ? W.w0 : PI == 1 ? W.w1 : PI == 2 ? W.w2 : PI == 3 ? W.w3 : PI == 4 ? W.w4 : W.w5, Q_CB + 57344 + (WV >> 2) * 32768 + (WV & 3) * 2048 + lane_id() * 16);
    }
    if (WV < 4) __builtin_amdgcn_s_setprio(2);
    unsigned t0 = 0, cap = 60000, d0 = 105, d1 = 200;
    int dq = D_Q;
    int sl = 0;
    for (int s = 0; s < NSEG; ++s) {
      TSDECL;
      const char* ag = xb + (size_t)((sl * 4 + (AREP == 4 ? xg : (AREP == 2 ? ARSEL : 0))) * 2 + (WV >> 2)) * 16384 + (WV & 3) * 2048;
      const char* vs = WV == 4 ? dbw : WV == 5 ? ws + OFF_KVM + ((size_t)s * NH + h) * 4096 : xb + (size_t)(24 + sl * 8 + (h >> 2)) * 16384 + (h & 3) * 2048;
      const char* od = WV == 4 ? (const char*)(size_t)((unsigned)(s + 1) << 24) : oh + (size_t)sl * 524288;
      if (s > 0) wait_rt(t0, dq);
      TS(0);
      unsigned long long tc; unsigned sp;
      qk_step<WV>(W, ag, vs, od, LAST && s == NSEG - 1 ? nullptr : ws + OFF_KVM + ((size_t)s * NH + h) * 4096 + 2048, cap, sp, tc, nullptr, d0, d1);
      dq = (sp & 0xFFFFu) ? min(dq + DQU, D_Q + DQM) : max(dq - DQD, D_Q);
      if ((WV < 4 || WV == 5) && !(LAST && s == NSEG - 1)) {
        const unsigned pv = (unsigned)(s + 1) << 24 | (rt_now() & 0xFFFFFFu);
        WAIT_V(0);
        if (lane_id() == 0) st4_sc1(cnt + (PROG + h) * 32 + WV, pv);
      }
      if (WV == 6 && s + 1 < NSEG - 1) stage_kv(s + 1);
      if (WV == 7 && s > 0 && s < NSEG - 1) rearm(sl == 0 ? 2 : sl - 1);
      if (WV == 7 && s >= 8 && s < 8 + NSEG - SDIR) {
        unsigned z = SENT; asm volatile("" : "+v"(z));
        const char* kd = ws + OFF_KVM + ((size_t)(s - 8 + SDIR) * NH + h) * 4096;
#pragma unroll
        for (int r = 0; r < 4; ++r) st16s(kd, r * 1024u + lane_id() * 16, u32x4{z, z, z, z});
      }
      if (s < NSEG - 1) {
        if (WV == 4) {
          delay64<E_B>();
          t0 = bell_time(xb + (size_t)(48 + xg) * 16384 + 4096, (lane_id() & 31) * 128, (unsigned)(s + 1), cap);
          *(volatile LAS u32x2*)(smem + Q_TM) = u32x2{(unsigned)(s + 1), t0};
        } else {
          for (;;) {
            const u32x2 v = *(volatile LAS u32x2*)(smem + Q_TM);
            t0 = __builtin_amdgcn_readfirstlane(v.y);
            if (__builtin_amdgcn_readfirstlane(v.x) == (unsigned)(s + 1)) break;
            __builtin_amdgcn_s_sleep(1);
          }
        }
      }
      sl = sl == 2 ? 0 : sl + 1;
    }
    if (cap == 0 && lane_id() == 0) st4_sc1(tmo, 1u);
  };
  switch (wave) {
    case 0: loop(std::integral_constant<int, 0>{}); break;
    case 1: loop(std::integral_constant<int, 1>{}); break;
    case 2: loop(std::integral_constant<int, 2>{}); break;
    case 3: loop(std::integral_constant<int, 3>{}); break;
    case 4: loop(std::integral_constant<int, 4>{}); break;
    case 5: loop(std::integral_constant<int, 5>{}); break;
    case 6: loop(std::integral_constant<int, 6>{}); break;
    default: loop(std::integral_constant<int, 7>{}); break;
  }
  __builtin_amdgcn_s_setprio(0);
}

__device__ __forceinline__ unsigned vr_body(char* smem, int wave_id_, int xg, int h, int c, int r, int wg, const u16* __restrict__ Wv, const u16* __restrict__ Wo,
                                        const u16* __restrict__ Wq, const u16* __restrict__ Wk, char* ws, unsigned* tmo, u64 tag, const char* code) {
  const int wave = WAVE_ID;
  const unsigned po = (unsigned)(r >> 5) * 16384u + ((r >> 2) & 7) * 1024 + (r & 1) * 512 + ((r >> 1) & 1) * 128;
  const unsigned vo_ = (unsigned)(24 + (h >> 2)) * 16384u + (h & 3) * 2048 + c * 1024;
  const int hg = (wave + ROTW * (r & 3)) & 7;
  const int jr = ROTH ? r & 3 : 0;
  VRW W;
  {
    const int lane = lane_id();
    const int g = lane >> 4, q4 = (lane & 15) >> 2, p4 = lane & 3, fr = 4 * g + (q4 & 1) + 32 * (q4 >> 1);
    const unsigned lw = wave * 16384;
    const char* sv_ = uni((const char*)(Wv + (size_t)(128 * wave) * HD + h * DH + 64 * c));
    const char* so = uni((const char*)(Wo + (size_t)(512 * hg) * DM + 64 * (r >> 2) + 16 * (r & 1)));
    const unsigned vo = (unsigned)((32 * (lane >> 5) + ((lane >> 2) & 7)) * DM * 2 + ((lane >> 1) & 1) * 64 + (lane & 1) * 16);
    auto issue = [&](int p) {
#pragma unroll
      for (int i = 0; i < 8; ++i) {
        if (p >= 2 && (NTM & 8)) gldsw(so + (size_t)(128 * ((p - 2) ^ jr) + 64 * (i & 1) + 8 * (i >> 1)) * DM * 2, vo, lw + (p & 1) * 8192 + i * 1024);
        else if (p >= 2) glds16s<0>(so + (size_t)(128 * ((p - 2) ^ jr) + 64 * (i & 1) + 8 * (i >> 1)) * DM * 2, vo, lw + (p & 1) * 8192 + i * 1024);
        else gldsw(sv_ + (size_t)(8 * p + i) * 8 * HD * 2, (unsigned)((lane >> 3) * HD * 2 + (lane & 7) * 16), lw + (p & 1) * 8192 + i * 1024);
      }
    };
#define VTS(p) do { } while (0)
    if (wave == 0) STS(ws, wg, 7);
    issue(0); issue(1);
    const unsigned av = lw + (unsigned)(fr * 128 + p4 * 8), ao = lw + (unsigned)(fr * 64 + p4 * 8);
    WAIT_V(8); VTS(0); vr_wload<0>(W.v0, av); issue(2);
    WAIT_V(8); VTS(1); vr_wload<1>(W.v1, av); issue(3);
    WAIT_V(8); VTS(2); vr_wload<2>(W.o0, ao); issue(4);
    WAIT_V(8); vr_wload<3>(W.o1, ao); issue(5);
    if (wave == 0) init_issue(ws, 131072);
    WAIT_V(8); vr_wload<4>(W.o2, ao);
    WAIT_V(0); VTS(3);
    if (VHN) {
      const int rl = r - 20 * xg, hn = ((xg == 1 ? rl : rl - (rl >= (xg ? 8 : 16) ? 4 : 0)) * 8 + wave) * (6 * VHN) + (lane >> 1);
      const int pm = hn / 384, hr = hn - pm * 384, p = 8 - VHN + (pm >> 1), hh = 12 * xg + 1 + 4 * (hr >> 7);
      const u16* hp = ((pm & 1) ? Wk : Wq) + (size_t)(((pm & 1) ? (p + 2) & 7 : p) * 128 + (hr & 127)) * HD + min(hh, 31) * DH + (lane & 1) * 64;
      if (((r >> 2) & 7) != 3 && lane < 12 * VHN && pm < 2 * VHN) asm volatile("s_mov_b32 m0, %0\n\ts_nop 0\n\tglobal_load_lds_dword %1, off" :: "s"(155648 + wave * 256), "v"(hp) : "memory", "m0");
    }
    vr_wload<5>(W.o3, ao);
#undef VTS
  }
  if (wave == 0) STS(ws, wg, 5);
  if (wave == 0) STS(ws, wg, 6);
  BAR();
  if (wave == 0) STS(ws, wg, 0);
  const unsigned in = init_get(ws, tag, smem + 131072);
  const char* xb = uni(xch_base(ws) + ((size_t)3 << 21) + (size_t)((in & 2u) << 12));
  const char* ob = uni(xch_base(ws) + (size_t)((in & 1u) << 13));
  auto rearm = [&](int ps, bool ag) {
    const int lane = lane_id();
    unsigned z = SENT; asm volatile("" : "+v"(z));
    const u32x4 sv = u32x4{z, z, z, z};
    if (ag && lane < 16 * AREP) st16s(xb, (unsigned)(ps * 8 + (lane >> 4) * 2) * 16384u + po + ((lane >> 3) & 1) * 256 + (lane & 7) * 16, sv);
    st16s(xb, (unsigned)ps * 131072u + vo_ + lane * 16, sv);
  };
  __builtin_amdgcn_s_setprio(2);
  unsigned cap = 60000;
  int sl = 0;
  for (int s = 0; s < NSEG; ++s) {
    TSDECL;
    const int sn = sl == 2 ? 0 : sl + 1;
    const char* ag = xb + (size_t)((sl * 4 + (AREP == 4 ? xg : (AREP == 2 ? ARSEL : 0))) * 2 + (wave >> 2)) * 16384 + (wave & 3) * 2048;
    const char* vd = (LAST && s == NSEG - 1 ? ws + OFF_KVM + ((size_t)s * NH + h) * 4096 + 2048 + c * 1024 : xb + (size_t)sl * 131072 + vo_) + (wave & 2) * 256 + (wave & 1) * 128;
    const char* os = ob + (size_t)((sl * 4 + (OREP == 4 ? xg : ORSEL)) * 8 + hg) * 16384 + ((r >> 1) & 1) * 1024;
    const char* ad = xb + (size_t)((sn * 4 + (wave & 3)) * 2) * 16384 + po;
    if (s > 0) delay64<E_AG>();
    TS(0);
    unsigned long long tc; unsigned sp;
    unsigned long long* tsp = nullptr;
    vr_v(W, ag, vd, wave * 2048, (wave & 3) * 512, cap, sp, tc, tsp);
    TN(12, sp + 1);
    if (s == NSEG - 1) break;
    if (wave == 7 && s > 0) rearm(sl == 0 ? 2 : sl - 1, true);
    if (wave == 6 && s == NSEG - 4 && code) touch(code, (unsigned)lane_id() * 128, 155648);
    wait_clk((unsigned)tc, E_O);
    TS(3);
    const unsigned tb = bell_time(xb + (size_t)(48 + xg) * 16384 + hg * 512, (lane_id() & 3) * 128, (unsigned)(s + 1), cap);
    wait_rt(tb, D_O);
    TS(4);
    vr_r(W, os, ad, wave * 512, jr * 2048, cap, sp, tsp);
    TN(14, (sp & 0xFFFF) + 1);
    if (wave == 1) TSFLUSH(0x6078);
    sl = sn;
  }
  __builtin_amdgcn_s_setprio(0);
  if (cap == 0 && lane_id() == 0) st4_sc1(tmo, 2u);
  return in;
}
struct ModIn { u32x4 w[8]; unsigned cv; };
__device__ __forceinline__ void mod_issue(ModIn& I, int wave_id_, int j, const u16* __restrict__ c, const u16* __restrict__ wmod) {
  const int tid = opaque_tid(), k0 = (j >> 5) * 512 + (tid >> 3);
  I.cv = c[k0 + 64 * (tid & 7)];
#pragma unroll
  for (int i = 0; i < 8; ++i) I.w[i] = ldnt<1>(wmod + (size_t)(k0 + 64 * i) * (6 * DM) + (j & 31) * 64 + (tid & 7) * 8);
}
__device__ __forceinline__ void mod_job(char* smem, int wave_id_, int j, u64 tag, const ModIn& I, char* ws) {
  const int tid = opaque_tid(), lane = tid & 63, wave = WAVE_ID, cg = tid & 7;
  const float cv = bflo(I.cv), sl = bfr(cv / (1.0f + __expf(-cv)));
  float acc[8] = {0, 0, 0, 0, 0, 0, 0, 0};
#pragma unroll
  for (int i = 0; i < 8; ++i) {
    const float sv = __shfl(sl, (lane & 56) + i);
#pragma unroll
    for (int e = 0; e < 4; ++e) { acc[2 * e] += sv * bflo(I.w[i][e]); acc[2 * e + 1] += sv * bfhi(I.w[i][e]); }
  }
#pragma unroll
  for (int e = 0; e < 8; ++e) acc[e] = red16_32_add(add_ror8(acc[e]));
  float* red = (float*)(smem + L_GB);
  if (lane < 8) { *(f32x4*)(red + wave * 64 + cg * 8) = f32x4{acc[0], acc[1], acc[2], acc[3]}; *(f32x4*)(red + wave * 64 + cg * 8 + 4) = f32x4{acc[4], acc[5], acc[6], acc[7]}; }
  __syncthreads();
  if (tid < 32) {
    f32x2 s = f32x2{0, 0};
#pragma unroll
    for (int r = 0; r < 8; ++r) s += *(const f32x2*)(red + r * 64 + tid * 2);
    st16_sc1(ws + OFF_MOD + (j * 32 + tid) * 16, u32x4{__float_as_uint(s[0]), (unsigned)tag, __float_as_uint(s[1]), tag2(tag)});
  }
  BAR();
}
#define NIMM 3
constexpr unsigned TK(int m, int hl, int part, int bo, int be) { return (unsigned)(m | hl << 2 | part << 5 | bo << 8 | be << 16); }
#define QX 30
#define CA 36
#define CB 42
#define C2 46
constexpr unsigned task_tab(int t, int it) {
  return t >= 8 && t < 13 ? (it == 0 ? TK(1, t - 8, 0, 0, 64) : it == 1 ? (t < 12 ? TK(2, t - 4, 1, C2, 64) : TK(1, 5, 1, C2, 64)) : 0)
       : it > 0 ? 0
       : t < 8 ? TK(0, t, 1, QX, 64)
       : t < 16 ? TK(1, t - 8, 0, 0, C2)
       : t < 24 ? TK(2, t - 16, 0, 0, t < 18 ? CA : (t < 20 ? CB : C2))
       : t < 26 ? TK(2, t - 24, 1, CA, 64)
       : t == 26 ? TK(1, 6, 1, C2, (C2 + 64) / 2) : t == 27 ? TK(1, 6, 2, (C2 + 64) / 2, 64)
       : t < 30 ? TK(2, t - 26, 1, CB, 64)
       : t == 30 ? TK(1, 7, 1, C2, (C2 + 64) / 2) : t == 31 ? TK(1, 7, 2, (C2 + 64) / 2, 64)
       : TK(0, t - 32, 0, 0, QX);
}
template <int IT, int I = 0> __device__ __forceinline__ unsigned task_sel(int t) {
  if constexpr (I == 40) return 0u;
  else { unsigned r = task_sel<IT, I + 1>(t); constexpr unsigned c = task_tab(I, IT); if (c) asm volatile("s_cmp_eq_u32 %1, %2\n\ts_cselect_b32 %0, %3, %0" : "+s"(r) : "s"(t), "i"(I), "i"(c) : "scc"); return r; }
}
__global__ void __launch_bounds__(512, 2) k_mega(const u16* __restrict__ Wq, const u16* __restrict__ Wk, const u16* __restrict__ Wv,
                                                 const u16* __restrict__ Wo, const u16* __restrict__ w1, const u16* __restrict__ w3,
                                                 const u16* __restrict__ w2, const u16* __restrict__ x, const u16* __restrict__ cvec,
                                                 const u16* __restrict__ wmod, const u16* __restrict__ mem0, char* ws, u16* out) {
  extern __shared__ __attribute__((aligned(16))) char smem[];
  int wave_id_ = __builtin_amdgcn_readfirstlane((int)threadIdx.x >> 6);
  unsigned bid = blockIdx.x;
  asm volatile("" : "+s"(wave_id_), "+s"(bid));
  unsigned* tmo = (unsigned*)(ws + OFF_TMO);
  const unsigned tst = (unsigned)__builtin_amdgcn_s_memrealtime();
  unsigned long long* mts = nullptr;
  const float* mod = (const float*)(ws + OFF_MOD);
  volatile LAS unsigned* tslot = (volatile LAS unsigned*)(smem + L_SLOT + 768);
#define CPF 131072
  u64 pc; asm volatile("s_getpc_b64 %0" : "=s"(pc));
  const u64 pc0 = pc & ~(u64)127;
  if (CPF && wave_id_ == 1) {
    const unsigned o = (((bid >> 3) & 31) * 64 + lane_id()) * 64;
    if (o < CPF) asm volatile("s_mov_b32 m0, %0\n\ts_nop 0\n\tglobal_load_lds_dword %1, %2" :: "s"(155648 + 1024), "v"(o), "s"(pc & ~(u64)127) : "memory", "m0");
  }
  unsigned xcc; asm volatile("s_getreg_b32 %0, hwreg(HW_REG_XCC_ID)" : "=s"(xcc));
  xcc &= 7;
  const unsigned pred = xcc * 32 + ((bid >> 3) & 31);
  ModIn mi;
  auto modj = [](unsigned sl) { return sl >= 128 && ((sl + 8) & 31) < 16 ? (int)(((sl >> 5) & 3) * 16 + (sl & 15)) : -1; };
  if (modj(pred) >= 0) mod_issue(mi, wave_id_, modj(pred), cvec, wmod);
  u64 tag;
  asm volatile("s_load_dwordx2 %0, %1, 0x0\n\ts_waitcnt lgkmcnt(0)" : "=s"(tag) : "s"(ws + OFF_TAG) : "memory");
  tag += 1;
  if (opaque_tid() == 0) {
    unsigned sl = pred;
    const unsigned s0 = sl;
    for (unsigned i = 1; i <= 256; ++i) {
      if (__hip_atomic_exchange((u64*)(ws + OFF_CLAIM + sl * 128), tag, __ATOMIC_RELAXED, __HIP_MEMORY_SCOPE_AGENT) != tag) break;
      sl = i == 256 ? 0xFFFFu : (((s0 + (i & ~31u)) & 0xE0u) | ((s0 + i) & 31u));
    }
    *tslot = sl;
  }
  BAR();
  const int slot = (int)__builtin_amdgcn_readfirstlane(*tslot);
  BAR();
  if (slot >= 256) return;
  if (modj(slot) >= 0) {
    if (slot != (int)pred) mod_issue(mi, wave_id_, modj(slot), cvec, wmod);
    mod_job(smem, wave_id_, modj(slot), tag, mi, ws);
  }
  unsigned* cnt = nullptr;
  const int xg = slot >> 5;
  int t = xg == 3 ? 24 + (slot & 7) : slot & 31;
  int hx = -1;
  if (xg < 3) {
    const int nq = xg == 2 ? 8 : 12;
    if (t < nq) { const int h = xg * 12 + t; qk_body(smem, wave_id_, xg, h, (h >> 3) * 32 + (h & 7), Wq, Wk, ws + OFF_K, ws + OFF_V, ws, tmo, tag); }
    else {
      const int u = t - nq, r = xg * 20 + u;
      cnt = cnt_of(ws, vr_body(smem, wave_id_, xg, r >> 1, r & 1, r, (r >> 4) * 32 + 8 + (r & 15), Wv, Wo, Wq, Wk, ws, tmo, tag, CPF && HB < NSEG && u < 16 ? (const char*)pc0 + u * 8192 : nullptr));
      if (HB < NSEG) hx = u < 8 ? xg * 8 + u : (u == 8 ? 24 + xg : (u == 9 && xg == 2 ? 27 : -1));
#define HPF 1
      if (HPF && HB < NSEG && u >= 10 && u < 22) {
        const int lane = lane_id(), wv = (u - 10) * 8 + wave_id_;
        if (wv >= 64) touch(w2 + (size_t)(3072 + 256 + (wv & 15) * 16) * DM + (wv >= 80 ? 3 : xg) * 256, (unsigned)((lane >> 2) * DM * 2 + (lane & 3) * 128), wave_id_ * 256);
        else {
#pragma unroll
          for (int j = 0; j < 2; ++j) touch(w2 + (size_t)(xg * 1024 + 256 + j * 128 + (wv & 32) * 16 + (wv & 31) * 4) * DM, (unsigned)lane * 128, wave_id_ * 256);
        }
        WAIT_V(0);
      }
    }
    if (hx < 0) return;
    t = 8;
  }
  const bool cx = xg == 3;
  const int xq = cx ? (slot >> 3) & 3 : xg & 3, wsl = xq * 32 + t;
#define GDEL 1200
#define GDX 1200
  if (cx || t < 8) l1_role(smem, wave_id_, cx, xq * 8 + (t & 7), x, mod, mem0, ws + OFF_H1, cnt, tag, tmo, mts);
#define KH 0
  if (KH && !cx && t >= 24) {
    const int it = (t & 7) * 8 + wave_id_, lane = lane_id();
    touch(((it & 16) ? Wk : Wv) + (size_t)(256 + (it & 8) * 64 + (it & 7) * 32) * HD + (8 * xq + 1 + 4 * (it >> 5)) * DH,
          (unsigned)((lane >> 1) * HD * 2 + (lane & 1) * 128), 155648 + wave_id_ * 256);
  }
  if (t < 8 || t >= 24) {
    while ((unsigned)__builtin_amdgcn_s_memrealtime() - tst < (cx ? GDX : GDEL)) __builtin_amdgcn_s_sleep(8);
    if (!cx && t >= 24) {
      cnt = cnt_of(ws, init_wait(ws, tag));
      mod2_job(smem, wave_id_, xq * 8 + t - 24, mod, cvec, wmod, cnt);
    }
  }
  auto u2_of = [&]() { return cx ? -1 : (t >= 13 && t < 16 ? t - 13 : (t >= 20 && t < 24 ? t - 17 : -1)); };
  auto f_of = [&]() {
    int fl = t < 8 ? t : (t < 18 ? t - 4 : (t < 20 ? t - 10 : t - 14));
    const int sa = (3 + 2 * xq) & 7, sb = sa + 8 < 14 ? sa + 8 : 13;
    if (fl == 12) fl = sa; else if (fl == sa) fl = 12;
    else if (fl == 13) fl = sb; else if (fl == sb) fl = 13;
    return (int)__builtin_amdgcn_readfirstlane(14 * xq + fl);
  };
  unsigned tk = task_sel<0>(hx >= 0 ? 99 : (cx ? t + 8 : t)), t2 = task_sel<1>(cx ? t + 8 : t), t3 = task_sel<2>(cx ? t + 8 : t);
  for (int it = 0; tk != 0; ++it, tk = t2, t2 = t3, t3 = 0) {
    const int m = tk & 3, hl = (tk >> 2) & 7, part = (tk >> 5) & 3, bo = (tk >> 8) & 255, ns = (int)(tk >> 16) - bo, h = 8 * xq + hl;
    const bool first = m != 0 && bo == 0, late = it > 0;
    const int lane = lane_id();
    const Dep d{nullptr, (unsigned)((lane < 32 ? PL1 : PL1B - 32) + lane) * 128, 8u, (unsigned)((lane < 32 ? 0 : 8) - bo)};
    if (wave_id_ == 0 && late) wait_l1(cnt + (PL1B - PL1) * 32, 7u, tmo);
    BAR();
    gemm7<R_QKV>(smem, wave_id_, (m == 0 ? Wq : (m == 1 ? Wk : Wv)) + h * DH, nullptr, HD, ws + OFF_H1 + (size_t)bo * 65536, 65536, d,
                    ws + (m == 0 ? OFF_Q : (m == 1 ? OFF_K : OFF_V)) + h * 8192 + (size_t)bo * 32 * 8192, 32 * 8192,
                    cnt, (PQKV + h) * 32 + 4 * part + m, tmo, wsl, mts, false, first ? NIMM : 0, ns, late ? ns : 0, bo * 257, tag);
  }
  __syncthreads();
  const int u2 = u2_of();
  const int f2 = hx >= 0 ? hx : (u2 < 0 ? -1 : (xq < 3 ? xq * 8 + u2 : (u2 < 3 ? u2 * 8 + 7 : 21 + u2)));
  const int fb = hx >= 0 ? HB : 0, fe = hx >= 0 ? NSEG : HB;
  if (cx || (f2 >= 0 && f2 < 24)) {
    const int ks = cx ? xq : f2 >> 3, cn = cx ? (t + 1) & 7 : f2 & 7;
    const int lane = lane_id();
    const Dep d{nullptr, (unsigned)(cx ? (PSA + 4 * xq + ((lane >> 3) & 3)) * 128 + (lane & 7) * 4 : (PF1 + 16 * ks + (lane & 15)) * 128), cx ? 4u : 1u, cx ? (unsigned)((lane >> 1) & 3) : 0u};
    gemm7<R_PART>(smem, wave_id_, (cx ? Wo : w2) + (size_t)ks * 1024 * DM + cn * 128, nullptr, DM, ws + (cx ? OFF_O : OFF_G) + ks * 16 * 4096, (cx ? 64 : 56) * 4096, d,
                  ws + (cx ? OFF_P : OFF_P2) + (ks * DM + cn * 128) * (TLP ? 64 : 2), (size_t)32 * PROW, cnt, ((cx ? PWO : PF2) + ks * 8 + cn) * 32 + (hx >= 0 ? 1 : 0), tmo,
                  hx >= 0 ? 280 + hx : 128 + xq * 32 + (cx ? cn : 22 + u2), mts, !cx, 0, cx ? NSEG : fe, 0, 0, 0, fb);
  } else if (f2 >= 0) {
    const int c4 = f2 - 24;
    const int lane = lane_id();
    const Dep d{nullptr, (unsigned)(PF1 + 48 + (lane & 7)) * 128, 1u, 0u};
    gemm7<R_PART2>(smem, wave_id_, w2 + (size_t)3072 * DM + c4 * 256, nullptr, DM, ws + OFF_G + 48 * 4096, 56 * 4096, d,
                   ws + OFF_P2 + (3 * DM + c4 * 256) * (TLP ? 64 : 2), (size_t)32 * PROW, cnt, (PF2 + 24 + c4) * 32 + (hx >= 0 ? 1 : 0), tmo, hx >= 0 ? 280 + hx : 128 + xq * 32 + 22 + u2, mts, true, 0, fe, 0, 0, 0, fb);
  } else if (t < 8 || (t >= 16 && t < 20) || t == 24 || t == 25) {
    int f = f_of();
    asm volatile("" : "+s"(f));
    const int lane = lane_id();
    const Dep d{nullptr, (unsigned)((PLN + (lane >> 3)) * 128 + (lane & 7) * 4), 1u, 0u};
    gemm7<R_F1>(smem, wave_id_, w1 + 64 * f, w3 + 64 * f, FF, ws + OFF_H2, 65536, d, ws + OFF_G + f * 4096, 56 * 4096, cnt, (PF1 + f) * 32, tmo, 128 + xq * 18 + 8 + f, mts, false);
  } else if (t >= 9 && t < 13) {
    fin_role(smem, wave_id_, xq * 4 + t - 9, mod, ws + OFF_P2, (const u16*)(ws + OFF_X1), out, cnt, tmo, mts);
    if (xq == 3 && t == 12 && opaque_tid() == 0) {
      const u32x4 v = ld16_sc1(ws + OFF_TAG);
      const u64 tn = (((u64)v.y << 32) | v.x) + 1;
      st8_sc1(ws + OFF_TAG, u32x2{(unsigned)tn, (unsigned)(tn >> 32)});
    }
  } else if (t == 28 || t == 29) {
    row_role(smem, wave_id_, 8 * xq, t - 28, mod, ws + OFF_P, x, (u16*)(ws + OFF_X1), ws + OFF_H2, cnt, tmo, 256 + xq, mts);
  } else if (t >= 26) {
    sa_role(smem, wave_id_, 8 * xq + 2 * (t >= 30 ? t - 30 : t - 24), ws + OFF_Q, ws + OFF_K, ws + OFF_V, ws + OFF_KVM, ws + OFF_O, cnt, tmo, 128 + xq * 32 + 30 + (t & 1), mts);
  }
}

extern "C" void kernel_launch(void* const* d_in, const int* in_sizes, int n_in, void* d_out, int out_size, void* d_ws, size_t ws_size,
                              hipStream_t stream) {
  const u16* x    = (const u16*)d_in[0];
  const u16* mem0 = (const u16*)d_in[1];
  const u16* Wq   = (const u16*)d_in[2];
  const u16* Wk   = (const u16*)d_in[3];
  const u16* Wv   = (const u16*)d_in[4];
  const u16* Wo   = (const u16*)d_in[5];
  const u16* w1   = (const u16*)d_in[8];
  const u16* w3   = (const u16*)d_in[9];
  const u16* w2   = (const u16*)d_in[10];
  const u16* c    = (const u16*)d_in[11];
  const u16* wmod = (const u16*)d_in[12];
#define WSHIFT 0
  char* ws = (char*)d_ws + ((size_t)WSHIFT << 20);
  hipFuncSetAttribute((const void*)k_mega, hipFuncAttributeMaxDynamicSharedMemorySize, MEGA_LDS);
  k_mega<<<256, 512, MEGA_LDS, stream>>>(Wq, Wk, Wv, Wo, w1, w3, w2, x, c, wmod, mem0, ws, (u16*)d_out);
}
```

```cpp
#include <hip/hip_runtime.h>
#include <hip/hip_bf16.h>
#include <stdint.h>
#include <type_traits>
#pragma clang diagnostic ignored "-Winline-asm"
#pragma clang diagnostic ignored "-Wunused-value"

typedef unsigned short u16;
typedef short s16x4 __attribute__((ext_vector_type(4)));
typedef __bf16 bf16x8 __attribute__((ext_vector_type(8)));
typedef float f32x4 __attribute__((ext_vector_type(4)));
typedef float f32x2 __attribute__((ext_vector_type(2)));
typedef unsigned u32x4 __attribute__((ext_vector_type(4)));
typedef unsigned u32x2 __attribute__((ext_vector_type(2)));
#define LAS __attribute__((address_space(3)))

constexpr int SEQ = 2048, DM = 1024, NH = 32, DH = 128, HD = 4096, NMEM = 8, SEG = 32, NSEG = 64, FF = 3584;
constexpr unsigned SENT = 0xFFFFFFFFu;

__device__ __forceinline__ int lane_id() { int l; asm volatile("v_mbcnt_lo_u32_b32 %0, -1, 0\n\tv_mbcnt_hi_u32_b32 %0, -1, %0" : "=v"(l)); return l; }
#define WAVE_ID wave_id_
#define opaque_tid() (WAVE_ID * 64 + lane_id())
__device__ __forceinline__ float bf2f(u16 v) { return __builtin_bit_cast(float, (unsigned)v << 16); }
__device__ __forceinline__ float bflo(unsigned v) { return __builtin_bit_cast(float, v << 16); }
__device__ __forceinline__ float bfhi(unsigned v) { return __builtin_bit_cast(float, v & 0xffff0000u); }
__device__ __forceinline__ float bfr(float x) { return (float)(__bf16)x; }
__device__ __forceinline__ unsigned pack2(float a, float b) {
  typedef __bf16 bf16x2 __attribute__((ext_vector_type(2)));
  bf16x2 v; v[0] = (__bf16)a; v[1] = (__bf16)b; return __builtin_bit_cast(unsigned, v);
}
__device__ __forceinline__ unsigned nosent(unsigned v) { return v == SENT ? 0x7FC07FC0u : v; }
__device__ __forceinline__ bf16x8 asbf8(u32x4 v) { return __builtin_bit_cast(bf16x8, v); }
__device__ __forceinline__ s16x4 ass4(u32x2 v) { return __builtin_bit_cast(s16x4, v); }
__device__ __forceinline__ u32x2 trread(const char* lds_base, unsigned off) {
  s16x4 v = __builtin_amdgcn_ds_read_tr16_b64_v4i16((LAS s16x4*)(lds_base + off));
  return __builtin_bit_cast(u32x2, v);
}
__device__ __forceinline__ void glds16(const void* gsrc, unsigned lds_dst) {
  unsigned keep;
  asm volatile("s_mov_b32 %0, m0\n\ts_mov_b32 m0, %2\n\ts_nop 0\n\tglobal_load_lds_dwordx4 %1, off\n\ts_mov_b32 m0, %0"
               : "=&s"(keep) : "v"(gsrc), "s"(lds_dst) : "memory");
}
#define CNT_ " nt"
#define WNT_ " nt"
__device__ __forceinline__ void glds16_w(const void* gsrc, unsigned lds_dst) {
  unsigned keep;
  asm volatile("s_mov_b32 %0, m0\n\ts_mov_b32 m0, %2\n\ts_nop 0\n\tglobal_load_lds_dwordx4 %1, off" CNT_ "\n\ts_mov_b32 m0, %0"
               : "=&s"(keep) : "v"(gsrc), "s"(lds_dst) : "memory");
}
__device__ __forceinline__ void glds16_sc1(const void* gsrc, unsigned lds_dst) {
  unsigned keep;
  asm volatile("s_mov_b32 %0, m0\n\ts_mov_b32 m0, %2\n\ts_nop 0\n\tglobal_load_lds_dwordx4 %1, off sc1\n\ts_mov_b32 m0, %0"
               : "=&s"(keep) : "v"(gsrc), "s"(lds_dst) : "memory");
}
#define NTM 1
template <int B> __device__ __forceinline__ u32x4 ldnt(const void* p) { if (NTM & B) return __builtin_nontemporal_load((const u32x4*)p); else return *(const u32x4*)p; }
#define MFMA32(a, b, c) __builtin_amdgcn_mfma_f32_16x16x32_bf16((a), (b), (c), 0, 0, 0)

__device__ __forceinline__ void st16_sc1(void* p, u32x4 v) { asm volatile("global_store_dwordx4 %0, %1, off sc1\n\ts_nop 1" :: "v"(p), "v"(v) : "memory"); }
__device__ __forceinline__ void st8_sc1(void* p, u32x2 v) { asm volatile("global_store_dwordx2 %0, %1, off sc1\n\ts_nop 1" :: "v"(p), "v"(v) : "memory"); }
template <bool WT> __device__ __forceinline__ void st8(void* p, u32x2 v) { if (WT) st8_sc1(p, v); else *(u32x2*)p = v; }
template <bool WT> __device__ __forceinline__ void st16(void* p, u32x4 v) { if (WT) st16_sc1(p, v); else *(u32x4*)p = v; }
__device__ __forceinline__ u32x4 ld16_sc1(const void* p) {
  u32x4 r; asm volatile("global_load_dwordx4 %0, %1, off sc1\n\ts_waitcnt vmcnt(0)" : "=&v"(r) : "v"(p) : "memory"); return r;
}
__device__ __forceinline__ void ld16x4_sc1(const void* p, u32x4& a, u32x4& b, u32x4& c, u32x4& d) {
  asm volatile("global_load_dwordx4 %0, %4, off sc1\n\tglobal_load_dwordx4 %1, %4, off offset:1024 sc1\n\t"
               "global_load_dwordx4 %2, %4, off offset:2048 sc1\n\tglobal_load_dwordx4 %3, %4, off offset:3072 sc1\n\ts_waitcnt vmcnt(0)"
               : "=&v"(a), "=&v"(b), "=&v"(c), "=&v"(d) : "v"(p) : "memory");
}
typedef __amdgpu_buffer_rsrc_t rsrc_t;
__device__ __forceinline__ rsrc_t mkrs(const void* p) { return __builtin_amdgcn_make_buffer_rsrc((void*)p, 0, 0x7FFFFFFF, 0x00020000); }
__device__ __forceinline__ u32x4 bld16(rsrc_t r, unsigned off) { return __builtin_amdgcn_raw_buffer_load_b128(r, off, 0, 16); }
__device__ __forceinline__ u32x2 bld8(rsrc_t r, unsigned off) { return __builtin_amdgcn_raw_buffer_load_b64(r, off, 0, 16); }
__device__ __forceinline__ bool ok4(u32x4 v) { return v.x != SENT && v.y != SENT && v.z != SENT && v.w != SENT; }
constexpr int SPIN_CAP = 2000000;
__device__ __forceinline__ bool give_up(int spins, unsigned* tmo, unsigned code) {
  if ((spins & 4095) != 4095) return false;
  if (spins > SPIN_CAP) { __hip_atomic_store(tmo, code, __ATOMIC_RELAXED, __HIP_MEMORY_SCOPE_AGENT); return true; }
  return __hip_atomic_load(tmo, __ATOMIC_RELAXED, __HIP_MEMORY_SCOPE_AGENT) != 0u;
}
template <bool ACQ = true>
__device__ __forceinline__ void wait_cnt(unsigned* c, unsigned n, unsigned* tmo) {
  for (int spins = 0;; ++spins) {
    const unsigned v = __hip_atomic_load(c, __ATOMIC_RELAXED, __HIP_MEMORY_SCOPE_AGENT);
    if (v >= n) break;
    __builtin_amdgcn_s_sleep(8);
    if (give_up(spins, tmo, 5)) break;
  }
  if (ACQ) {
    __builtin_amdgcn_fence(__ATOMIC_ACQUIRE, "agent");
    asm volatile("s_waitcnt vmcnt(0)" ::: "memory");
  }
}

__device__ __forceinline__ const char* uni(const char* p) {
  const unsigned long long v = (unsigned long long)p;
  const unsigned lo = __builtin_amdgcn_readfirstlane((unsigned)v), hi = __builtin_amdgcn_readfirstlane((unsigned)(v >> 32));
  return (const char*)(((unsigned long long)hi << 32) | lo);
}
#define TSCLK "s_memrealtime"
#define TC(k) do { } while (0)
#define TS(k) do { } while (0)
#define TSDECL do { } while (0)
#define TSFLUSH(mask) do { } while (0)

constexpr size_t OFF_MOD  = 0;
constexpr size_t OFF_MOD2 = 65536;
constexpr size_t OFF_TMO  = 98304;
constexpr size_t OFF_TAG  = 131072;
constexpr size_t OFF_CLAIM = 135168;
constexpr size_t OFF_INIT = 196608;
constexpr size_t OFF_H1   = 262144;
constexpr size_t OFF_Q    = OFF_H1 + (size_t)SEQ * DM * 2;
constexpr size_t OFF_K    = OFF_Q + (size_t)SEQ * HD * 2;
constexpr size_t OFF_V    = OFF_K + (size_t)SEQ * HD * 2;
constexpr size_t OFF_O    = OFF_V + (size_t)SEQ * HD * 2;
constexpr size_t OFF_X1   = OFF_O + (size_t)SEQ * HD * 2;
constexpr size_t OFF_H2   = OFF_X1 + (size_t)SEQ * DM * 2;
constexpr size_t OFF_G    = OFF_H2 + (size_t)SEQ * DM * 2;
constexpr size_t OFF_KVM  = (size_t)92 << 20;
constexpr size_t OFF_XCH  = (size_t)100 << 20;
constexpr size_t OFF_P    = (size_t)112 << 20;
constexpr size_t OFF_P2   = (size_t)128 << 20;
constexpr size_t OFF_DUMP = (size_t)144 << 20;
constexpr int PROW = 4 * DM * 2;
#define TLQ 1
#define TLP 1
static_assert(OFF_G + (size_t)SEQ * FF * 2 <= OFF_KVM, "layout");
__device__ __forceinline__ char* xch_base(char* ws) { return (char*)(((size_t)(ws + OFF_XCH) + ((2u << 20) - 1)) & ~(size_t)((2u << 20) - 1)); }
#define XPAR 0
constexpr int PROG = 16;
constexpr int PQKV = 64;
constexpr int PL1 = 96;
constexpr int PL1B = 350;
constexpr int PSA = 128;
constexpr int PWO = 192;
constexpr int PLN = 224;
constexpr int PF2 = 320;
constexpr int PF1 = 448;
#define SDIR 40
#define SB 52
#define SC 60
#define MARG 20
#define MARL 15
#define MARF 8
#define LAST 52
#define HB 61
#define MARH 35
#define SAW 0
constexpr int L_RED = 131072;
constexpr int L_SLOT = L_RED + 16384;
constexpr int L_GB = L_SLOT + 1024;
constexpr int MEGA_LDS = 163840;
static_assert(L_GB + 12288 + 2048 <= MEGA_LDS, "lds");
#define XPARC 0

__device__ __forceinline__ int bswz(int row) { return (((row >> 1) & 1) | (((row >> 3) & 1) << 1)) << 1; }
template <int OFF, bool SC1 = false>
__device__ __forceinline__ void glds16s(const char* sbase, unsigned voff, unsigned lds_base) {
  unsigned keep;
  if (SC1)
    asm volatile("s_mov_b32 %0, m0\n\ts_add_u32 m0, %3, %4\n\ts_nop 2\n\tglobal_load_lds_dwordx4 %1, %2 sc1\n\ts_mov_b32 m0, %0"
                 : "=&s"(keep) : "v"(voff), "s"(sbase), "s"(lds_base), "i"(OFF) : "memory", "scc");
  else
    asm volatile("s_mov_b32 %0, m0\n\ts_add_u32 m0, %3, %4\n\ts_nop 2\n\tglobal_load_lds_dwordx4 %1, %2\n\ts_mov_b32 m0, %0"
                 : "=&s"(keep) : "v"(voff), "s"(sbase), "s"(lds_base), "i"(OFF) : "memory", "scc");
}
template <int OFF>
__device__ __forceinline__ void glds16x2s(const char* sbase, unsigned voff, unsigned lds_base) {
  unsigned keep;
  asm volatile("s_mov_b32 %0, m0\n\ts_add_u32 m0, %3, %4\n\ts_nop 2\n\tglobal_load_lds_dwordx4 %1, %2 sc1\n\tglobal_load_lds_dwordx4 %1, %2 offset:1024 sc1\n\ts_mov_b32 m0, %0"
               : "=&s"(keep) : "v"(voff), "s"(sbase), "s"(lds_base), "i"(OFF) : "memory", "scc");
}
__device__ __forceinline__ void st16s(const char* sb, unsigned vo, u32x4 v) { asm volatile("global_store_dwordx4 %0, %1, %2 sc1\n\ts_nop 1" :: "v"(vo), "v"(v), "s"(sb) : "memory"); }
__device__ __forceinline__ void head_to_lds(const char* src, int lane, unsigned lds) {
  if (TLQ) {
    const int c = (lane & 15) ^ (lane >> 4);
    const unsigned v0 = (unsigned)((c >> 2) * 2048 + (lane >> 4) * 64 + (c & 3) * 16);
#pragma unroll
    for (int i = 0; i < 8; ++i) glds16s<0, true>(src + i * 256, v0 ^ ((i & 3) * 2048), lds + i * 1024);
  } else {
#pragma unroll
    for (int i = 0; i < 4; ++i) glds16x2s<0>(src + i * 2048, (unsigned)lane * 16, lds + i * 2048);
  }
}
#define TCHP ""
__device__ __forceinline__ void touch(const void* sb, unsigned vo, unsigned lds) {
  asm volatile("s_mov_b32 m0, %0\n\ts_nop 0\n\tglobal_load_lds_dword %1, %2" TCHP :: "s"(lds), "v"(vo), "s"(sb) : "memory", "m0");
}
#define WAIT_V(n) asm volatile("s_waitcnt vmcnt(" #n ")" ::: "memory")
#define BAR() do { asm volatile("s_waitcnt lgkmcnt(0)" ::: "memory"); __builtin_amdgcn_s_barrier(); asm volatile("" ::: "memory"); } while (0)
__device__ __forceinline__ void st4_sc1(void* p, unsigned v) { asm volatile("global_store_dword %0, %1, off sc1" :: "v"(p), "v"(v) : "memory"); }
__device__ __forceinline__ unsigned ld4_sc1(const void* p) {
  unsigned r; asm volatile("global_load_dword %0, %1, off sc1\n\ts_waitcnt vmcnt(0)" : "=&v"(r) : "v"(p) : "memory"); return r;
}
template <int CTRL> __device__ __forceinline__ unsigned dppu(unsigned x) { return (unsigned)__builtin_amdgcn_update_dpp(0, (int)x, CTRL, 0xf, 0xf, false); }
__device__ __forceinline__ unsigned rowmin16(unsigned v) {
  v = min(v, dppu<0x128>(v)); v = min(v, dppu<0x124>(v)); v = min(v, dppu<0x122>(v)); v = min(v, dppu<0x121>(v));
  return v;
}
__device__ __forceinline__ unsigned rdl(unsigned v, int l) { return (unsigned)__builtin_amdgcn_readlane((int)v, l); }
__device__ __forceinline__ unsigned qkv_avail(unsigned v, int m) {
  unsigned a = rdl(v, m);
#pragma unroll
  for (int j = 1; j < 4; ++j) { const unsigned p = rdl(v, 4 * j + m); if ((p >> 8) <= a) a = max(a, p & 255u); }
  return a;
}
constexpr float SCALE = 0.08838834764831845f;
constexpr float LOG2E = 1.4426950408889634f;
template <int N> __device__ __forceinline__ void delay64() {
  if (N >= 16) { __builtin_amdgcn_s_sleep(15); delay64<(N >= 16 ? N - 15 : 0)>(); } else if (N > 0) __builtin_amdgcn_s_sleep(N);
}
__device__ __forceinline__ void sw16(float x, float& a, float& b) {
  a = x; b = x; asm volatile("s_nop 1\n\tv_permlane16_swap_b32 %0, %1" : "+v"(a), "+v"(b));
}
__device__ __forceinline__ void sw32(float x, float& a, float& b) {
  a = x; b = x; asm volatile("s_nop 1\n\tv_permlane32_swap_b32 %0, %1" : "+v"(a), "+v"(b));
}
__device__ __forceinline__ float red16_32_max(float x) {
  float a, b; sw16(x, a, b); x = fmaxf(a, b); sw32(x, a, b); return fmaxf(a, b);
}
__device__ __forceinline__ float red16_32_add(float x) {
  float a, b; sw16(x, a, b); x = a + b; sw32(x, a, b); return a + b;
}
__device__ __forceinline__ unsigned cvtpk(float a, float b) { unsigned r; asm("v_cvt_pk_bf16_f32 %0, %1, %2" : "=v"(r) : "v"(a), "v"(b)); return r; }
__device__ __forceinline__ float max3(float a, float b, float c) { float r; asm("v_max3_f32 %0, %1, %2, %3" : "=v"(r) : "v"(a), "v"(b), "v"(c)); return r; }
__device__ __forceinline__ float max2(float a, float b) { float r; asm("v_max_f32 %0, %1, %2" : "=v"(r) : "v"(a), "v"(b)); return r; }
__device__ __forceinline__ unsigned ror8u(unsigned x) { return (unsigned)__builtin_amdgcn_update_dpp(0, (int)x, 0x128, 0xf, 0xf, false); }
__device__ __forceinline__ float ror8(float x) { return __builtin_bit_cast(float, ror8u(__builtin_bit_cast(unsigned, x))); }
__device__ __forceinline__ float add_ror8(float x) {
  return x + __builtin_bit_cast(float, __builtin_amdgcn_update_dpp(0, __builtin_bit_cast(int, x), 0x128, 0xf, 0xf, false));
}

__device__ __forceinline__ unsigned rt_now() { return (unsigned)__builtin_amdgcn_s_memrealtime(); }
__device__ __forceinline__ unsigned latest(unsigned v) {
  const unsigned now = rt_now() + 400u, age = rowmin16((now - v) & 0xFFFFFFu);
  return now - min(min(min(rdl(age, 0), rdl(age, 16)), min(rdl(age, 32), rdl(age, 48))), 900u);
}
__device__ __forceinline__ void wait_rt(unsigned t0, int d) {
  while ((int)(rt_now() - t0) < d) { }
}
__device__ __forceinline__ bool bells(bool ok, unsigned u, int b, int marg, int sb = SB) {
  if (b < sb || !__all(ok || (int)(u >> 24) > b)) return false;
  wait_rt(latest(!ok && (int)(u >> 24) == b + 1 ? u : rt_now() - 400u), marg);
  return true;
}
__device__ __forceinline__ void ld_fb(unsigned& v, unsigned& u, const void* sb, unsigned vo) {
  asm volatile("global_load_dword %0, %2, %3 sc1\n\tglobal_load_dword %1, %2, %3 offset:64 sc1\n\ts_waitcnt vmcnt(0)" : "=&v"(v), "=&v"(u) : "v"(vo), "s"(sb) : "memory");
}
__device__ __forceinline__ unsigned bell(int b, unsigned t) { return (unsigned)(b + 1) << 24 | (t & 0xFFFFFFu); }
typedef unsigned long long u64;
__device__ __forceinline__ unsigned tag2(u64 tag) { return (unsigned)(tag >> 32) ^ ((unsigned)tag * 0x9E3779B1u); }
__device__ __forceinline__ unsigned init_eval(u32x4 v, u64 tag) {
  if (!__all((v.x & ~63u) == (unsigned)tag << 6 && v.y == (unsigned)(tag >> 26))) return 0u;
  unsigned r = 8u;
#pragma unroll
  for (int c = 0; c < 3; ++c) r |= __popcll(__ballot(v.x & (1u << c))) > __popcll(__ballot(v.x & (8u << c))) ? 1u << c : 0u;
  return r ^ (XPAR * 3u + XPARC * 4u);
}
__device__ __forceinline__ unsigned init_wait(const char* ws, u64 tag) {
  for (int spins = 0; spins < SPIN_CAP; ++spins) {
    const unsigned r = init_eval(ld16_sc1(ws + OFF_INIT + (lane_id() & 31) * 128), tag);
    if (r) return r;
    __builtin_amdgcn_s_sleep(4);
  }
  return 8u;
}
__device__ __forceinline__ void init_issue(const char* ws, unsigned lds) { glds16s<0, true>(uni(ws + OFF_INIT), (unsigned)(lane_id() & 31) * 128u, lds); }
__device__ __forceinline__ unsigned init_get(const char* ws, u64 tag, const char* copy) {
  const unsigned r = init_eval(*(volatile LAS u32x4*)(copy + lane_id() * 16), tag);
  return r ? r : init_wait(ws, tag);
}
__device__ __forceinline__ unsigned* cnt_of(char* ws, unsigned in) { return (unsigned*)uni(xch_base(ws) + ((size_t)4 << 21) + (size_t)((in & 4u) << 11)); }
__device__ __forceinline__ void wait_l1(const unsigned* cnt, unsigned n, unsigned* tmo) {
  const int lane = lane_id();
  for (int spins = 0;; ++spins) {
    const unsigned v = ld4_sc1(cnt + (PL1 + (lane & 31)) * 32);
    if (__all(v >= n)) break;
    __builtin_amdgcn_s_sleep(8);
    if (give_up(spins, tmo, 12)) break;
  }
}
#define GKV 1
__device__ __forceinline__ void wait_k0(const unsigned* cnt, int h, unsigned* tmo) {
  for (int spins = 0;; ++spins) {
    const int l = lane_id();
    const unsigned v = ld4_sc1(cnt + (PQKV + h) * 32 + 1 + (l & 1));
    if (__all(v >= (unsigned)GKV)) break;
    __builtin_amdgcn_s_sleep(32);
    if (give_up(spins, tmo, 13)) break;
  }
}
#define WCLK(slot, b) do { } while (0)
#define WTM(slot, b, k) do { } while (0)
#define WTS(slot, b, k) do { } while (0)
#define WFB() do { } while (0)
#define STS(ws_, slot_, k_) do { } while (0)
typedef __bf16 bf16x2 __attribute__((ext_vector_type(2)));
__device__ __forceinline__ f32x2 pkfma(f32x2 a, f32x2 b, f32x2 c) { return __builtin_elementwise_fma(a, b, c); }
__device__ __forceinline__ float wavesum(float v) {
  v += __builtin_bit_cast(float, dppu<0x128>(__builtin_bit_cast(unsigned, v))); v += __builtin_bit_cast(float, dppu<0x124>(__builtin_bit_cast(unsigned, v)));
  v += __builtin_bit_cast(float, dppu<0x122>(__builtin_bit_cast(unsigned, v))); v += __builtin_bit_cast(float, dppu<0x121>(__builtin_bit_cast(unsigned, v)));
  return red16_32_add(v);
}
__device__ __forceinline__ float adds(unsigned v, unsigned sel, float c) { return __builtin_amdgcn_fdot2_f32_bf16(__builtin_bit_cast(bf16x2, v), __builtin_bit_cast(bf16x2, sel), c, false); }
__device__ __forceinline__ void sum4(const u32x4 (&p)[4], f32x2 (&a)[4]) {
  unsigned slo = 0x00003F80u, shi = 0x3F800000u;
  asm("" : "+s"(slo), "+s"(shi));
#pragma unroll
  for (int e = 0; e < 4; ++e) {
    float lo = bflo(p[0][e]), hi = bfhi(p[0][e]);
#pragma unroll
    for (int q = 1; q < 4; ++q) { lo = adds(p[q][e], slo, lo); hi = adds(p[q][e], shi, hi); }
    a[e] = f32x2{lo, hi};
  }
}
__device__ __forceinline__ u32x4 norm8(const f32x2 (&v)[4], const char* mt, float mb, float rden) {
  const f32x4 h0 = *(const f32x4*)(mt + 4096), h1 = *(const f32x4*)(mt + 4096 + 1024), c0 = *(const f32x4*)(mt + 8192), c1 = *(const f32x4*)(mt + 8192 + 1024);
  const f32x2 m2 = f32x2{mb, mb}, d2 = f32x2{rden, rden};
  const f32x2 r0 = pkfma(v[0] - m2, f32x2{c0[0], c0[1]} * d2, f32x2{h0[0], h0[1]}), r1 = pkfma(v[1] - m2, f32x2{c0[2], c0[3]} * d2, f32x2{h0[2], h0[3]});
  const f32x2 r2 = pkfma(v[2] - m2, f32x2{c1[0], c1[1]} * d2, f32x2{h1[0], h1[1]}), r3 = pkfma(v[3] - m2, f32x2{c1[2], c1[3]} * d2, f32x2{h1[2], h1[3]});
  return u32x4{pack2(r0[0], r0[1]), pack2(r1[0], r1[1]), pack2(r2[0], r2[1]), pack2(r3[0], r3[1])};
}
__device__ __forceinline__ void half_load(const char* P, const u16* res, int row, int hf, int lane, f32x2 (&a)[4], u32x4& rv) {
  const rsrc_t rp = mkrs(P), rr = mkrs(res);
  const unsigned po = TLP ? (unsigned)((row >> 5) * 32 * PROW + (hf * 16 + (lane >> 2)) * 2048 + (row & 31) * 64 + (lane & 3) * 16) : (unsigned)(row * PROW + hf * 1024 + lane * 16), ro = (unsigned)(row * DM * 2 + hf * 1024 + lane * 16);
  u32x4 p[4];
#pragma unroll
  for (int q = 0; q < 4; ++q) p[q] = bld16(rp, po + q * (TLP ? 65536 : 2048));
  if (res) rv = bld16(rr, ro);
  sum4(p, a);
}
__device__ __forceinline__ bool fin_half(bool fin, const char* P, const u16* res, const char* gbt, u16* __restrict__ o1, int row, int hf, int lane) {
  f32x2 a[4]; u32x4 rv;
  half_load(P, res, row, hf, lane, a, rv);
  const u32x4 gv = *(const u32x4*)(gbt + hf * 1024 + lane * 16);
  u32x4 o; f32x2 t = f32x2{0, 0};
#pragma unroll
  for (int e = 0; e < 4; ++e) { const f32x2 s = pkfma(f32x2{bflo(gv[e]), bfhi(gv[e])}, a[e], f32x2{bflo(rv[e]), bfhi(rv[e])}); o[e] = pack2(s[0], s[1]); t += s; }
  if (!fin && !__all(t[0] == t[0] && t[1] == t[1])) return false;
  st16_sc1(o1 + (size_t)row * DM + hf * 512 + lane * 8, o);
  return true;
}
#define DLN 10
#define LEB 64
#define MARE 25
#define LNE 1
template <int CTRL> __device__ __forceinline__ float dppf(float x) { return __builtin_bit_cast(float, dppu<CTRL>(__builtin_bit_cast(unsigned, x))); }
__device__ __forceinline__ bool ln_row(bool fin, int bb, unsigned* bl, const f32x2 (&a)[2][4], const f32x2 (&xf)[8], const f32x2 (&tg)[8], const f32x2 (&th)[8], const f32x2 (&tc)[8],
                                       const char* x1, const char* h2, unsigned xo, unsigned ho) {
  f32x2 v[8]; u32x4 o[2];
#pragma unroll
  for (int e = 0; e < 8; ++e) { const f32x2 t = pkfma(tg[e], a[e >> 2][e & 3], xf[e]); const unsigned u = pack2(t[0], t[1]); o[e >> 2][e & 3] = u; v[e] = f32x2{bflo(u), bfhi(u)}; }
  const float pv = __builtin_bit_cast(float, __builtin_amdgcn_readfirstlane(__float_as_int(v[0][0])));
  f32x2 d[8];
#pragma unroll
  for (int e = 0; e < 8; ++e) d[e] = v[e] - f32x2{pv, pv};
  const f32x2 s2 = ((d[0] + d[1]) + (d[2] + d[3])) + ((d[4] + d[5]) + (d[6] + d[7]));
  const f32x2 q2 = pkfma(d[3], d[3], pkfma(d[2], d[2], pkfma(d[1], d[1], d[0] * d[0]))) + pkfma(d[7], d[7], pkfma(d[6], d[6], pkfma(d[5], d[5], d[4] * d[4])));
  float s = s2[0] + s2[1], q = q2[0] + q2[1];
  if (!fin && !__all(s == s)) return false;
  if (LNE && bb >= SB && bl && lane_id() == 0) st4_sc1(bl, bell(bb, rt_now() + DLN + 30));
  s += dppf<0x128>(s); q += dppf<0x128>(q); s += dppf<0x124>(s); q += dppf<0x124>(q);
  s += dppf<0x122>(s); q += dppf<0x122>(q); s += dppf<0x121>(s); q += dppf<0x121>(q);
  asm volatile("s_nop 1\n\tv_permlane16_swap_b32 %0, %1" : "+v"(s), "+v"(q));
  s += q;
  { float x, y; sw32(s, x, y); s = x + y; }
  const float sh = __builtin_bit_cast(float, rdl(__builtin_bit_cast(unsigned, s), 0)) * (1.0f / DM), sq = __builtin_bit_cast(float, rdl(__builtin_bit_cast(unsigned, s), 16));
  const float mb = bfr(pv + sh), vb = bfr(fmaxf(sq * (1.0f / DM) - sh * sh, 0.0f));
  const float rden = __builtin_amdgcn_rcpf(bfr(__builtin_amdgcn_sqrtf(bfr(vb + 1e-5f))));
#pragma unroll
  for (int i = 0; i < 2; ++i) {
    u32x4 r;
#pragma unroll
    for (int e = 0; e < 4; ++e) { const f32x2 t = pkfma(v[4 * i + e] - f32x2{mb, mb}, tc[4 * i + e] * f32x2{rden, rden}, th[4 * i + e]); r[e] = pack2(t[0], t[1]); }
    st16s(h2, ho + i * 32768, r);
  }
  if (!LNE && bb >= SB && lane_id() == 0) st4_sc1(bl, bell(bb, rt_now() + DLN));
  st16s(x1, xo, o[0]); st16s(x1, xo + 1024, o[1]);
  return true;
}
__device__ __forceinline__ void row_table(char* tab, const float* mod, const unsigned* cnt, unsigned* tmo, int tid) {
  wait_l1(cnt + 1, 1u, tmo);
  const rsrc_t rm = mkrs((const char*)mod + OFF_MOD2);
#pragma unroll
  for (int i = 0; i < 2; ++i) {
    const int u = tid + 512 * i, a = u >> 8, c = u & 255;
    f32x4 v = __builtin_bit_cast(f32x4, bld16(rm, (unsigned)u * 16));
    v = f32x4{bfr(v[0]), bfr(v[1]), bfr(v[2]), bfr(v[3])};
    if (a == 2) v = f32x4{bfr(1.0f + v[0]), bfr(1.0f + v[1]), bfr(1.0f + v[2]), bfr(1.0f + v[3])};
    if (a < 3) *(f32x4*)(tab + a * 4096 + ((c >> 7) * 2 + (c & 1)) * 1024 + ((c >> 1) & 63) * 16) = v;
    else *(u32x2*)(tab + 12288 + c * 8) = u32x2{pack2(v[0], v[1]), pack2(v[2], v[3])};
  }
  __syncthreads();
}
__device__ __forceinline__ void row_role(char* smem, int wave_id_, int rbase, int par, const float* mod, const char* P, const u16* x, u16* x1, char* h2c,
                                         unsigned* cnt, unsigned* tmo, int slot, unsigned long long* mts) {
  const int tid = opaque_tid(), lane = tid & 63, wave = WAVE_ID;
  if (tid < 80) *(volatile LAS unsigned*)(smem + 16384 + tid * 4) = 0u;
  {
    static_assert(TLP && SB % 2 == 0, "");
    unsigned z = SENT; asm volatile("" : "+v"(z));
    const int r4 = rbase + 4 * par + (wave & 3);
    for (int bb = SB + (wave >> 2); bb < NSEG; bb += 2)
#pragma unroll
      for (int i = 0; i < 2; ++i) {
        st16s(uni((const char*)x1 + (size_t)bb * 65536), (unsigned)(r4 * DM * 2 + i * 1024 + lane * 16), u32x4{z, z, z, z});
        st16s(uni(h2c + (size_t)bb * 65536), (unsigned)(r4 * 128 + (lane & 7) * 16 + (lane >> 3) * 4096 + i * 32768), u32x4{z, z, z, z});
      }
    WAIT_V(0);
  }
  row_table(smem, mod, cnt, tmo, tid);
  const int rb = rbase + wave + 32 * par;
  const rsrc_t rp = mkrs(P), rr = mkrs(x);
  const unsigned l16 = lane * 16;
  const unsigned pwo = (unsigned)(PWO + (lane & 31)) * 128;
  unsigned* myp = cnt + (PLN + (rb >> 3)) * 32 + wave;
  f32x2 a[2][4];
  const int gp = wave >> 2, r4 = rbase + 4 * par + (wave & 3);
  volatile LAS unsigned* lwb = (volatile LAS unsigned*)(smem + 16384 + gp * 8);
  if (gp && lane == 0) st4_sc1(myp, 1u);
  f32x2 tg[8], th[8], tc[8];
#pragma unroll
  for (int i = 0; i < 4; ++i) {
    const char* mt = smem + lane * 16 + i * 1024;
    const f32x4 g_ = *(const f32x4*)mt, h_ = *(const f32x4*)(mt + 4096), c_ = *(const f32x4*)(mt + 8192);
    tg[2 * i] = f32x2{g_[0], g_[1]}; tg[2 * i + 1] = f32x2{g_[2], g_[3]}; th[2 * i] = f32x2{h_[0], h_[1]}; th[2 * i + 1] = f32x2{h_[2], h_[3]};
    tc[2 * i] = f32x2{c_[0], c_[1]}; tc[2 * i + 1] = f32x2{c_[2], c_[3]};
  }
  const unsigned xo = (unsigned)(r4 * DM * 2 + lane * 16), ho = (unsigned)(r4 * 128 + (((lane & 7) ^ (r4 & 7)) << 4) + (lane >> 3) * 4096);
  const unsigned po = (l16 >> 6) * 2048 + (l16 & 48);
  u32x4 rv[2];
#pragma unroll
  for (int i = 0; i < 2; ++i) rv[i] = __builtin_amdgcn_raw_buffer_load_b128(rr, xo + i * 1024, (unsigned)gp * 65536u, 0);
  unsigned wb = 0, d0 = 150, d1 = 150;
  for (int bb = gp; bb < NSEG; bb += 2) {
    f32x2 xf[8];
#pragma unroll
    for (int e = 0; e < 8; ++e) xf[e] = f32x2{bflo(rv[e >> 2][e & 3]), bfhi(rv[e >> 2][e & 3])};
    asm volatile("" : "+v"(xf[0]), "+v"(xf[1]), "+v"(xf[2]), "+v"(xf[3]), "+v"(xf[4]), "+v"(xf[5]), "+v"(xf[6]), "+v"(xf[7]));
    if (wb <= (unsigned)bb) {
      if ((wave & 3) == 0) {
        wb = (unsigned)(bb + 1);
        for (int spins = 0;; ++spins) {
          unsigned v, u;
          ld_fb(v, u, cnt, pwo);
          if (__all(v > (unsigned)bb + 2u)) { v = rowmin16(v); wb = min(rdl(v, 0), rdl(v, 16)); break; }
          if (__all(v > (unsigned)bb)) break;
          if (bb >= LEB && __all(v > (unsigned)bb || (u >> 24) > (unsigned)bb)) {
            const unsigned now = rt_now(), g0 = latest(v <= (unsigned)bb && (u >> 24) == (unsigned)bb + 1u ? u : now - 400u) + MARE, go = (int)(g0 - now) > 0 ? g0 : now;
            if (lane < 4) st4_sc1(myp + 16 + lane, bell(bb, go + max(d0, d1)));
            wait_rt(go, 0);
            wb |= 256u;
            break;
          }
          if (bells(v > (unsigned)bb, u, bb, MARL)) break;
          __builtin_amdgcn_s_sleep(2);
          if (give_up(spins, tmo, 11)) break;
        }
        lwb[0] = wb;
      } else {
        while (((wb = __builtin_amdgcn_readfirstlane(lwb[0])) & 255u) <= (unsigned)bb) __builtin_amdgcn_s_sleep(1);
      }
    }
    const bool rung = wb == (unsigned)bb + 257u;
    wb &= 255u;
    const unsigned t0 = rt_now();
    WTM(slot, bb, 0);
    bool fin = bb < SB;
    for (int spins = 0;; ++spins) {
      u32x4 pp[2][4];
#pragma unroll
      for (int i = 0; i < 2; ++i)
#pragma unroll
        for (int q = 0; q < 4; ++q) pp[i][q] = __builtin_amdgcn_raw_buffer_load_b128(rp, po + i * 32768, (unsigned)(bb * 32 * PROW + q * 65536 + r4 * 64), 16);
      sum4(pp[0], a[0]); sum4(pp[1], a[1]);
      if (ln_row(fin, bb, rung && spins == 0 ? nullptr : myp + 16, a, xf, tg, th, tc, uni((const char*)x1 + (size_t)bb * 65536), uni(h2c + (size_t)bb * 65536), xo, ho)) break;
      fin = __all(ld4_sc1((const char*)cnt + pwo) > (unsigned)bb) || give_up(spins, tmo, 11);
    }
    d1 = d0; d0 = min(rt_now() - t0 + 12u, 200u);
    WTM(slot, bb, 3);
#pragma unroll
    for (int i = 0; i < 2; ++i) rv[i] = __builtin_amdgcn_raw_buffer_load_b128(rr, xo + i * 1024, (unsigned)min(bb + 2, NSEG - 1) * 65536u, 0);
    WAIT_V(2);
    unsigned o = 0;
    if (lane == 0) o = __hip_atomic_fetch_add((LAS unsigned*)(smem + 16448 + bb * 4), 1u, __ATOMIC_RELAXED, __HIP_MEMORY_SCOPE_WORKGROUP);
    if (__builtin_amdgcn_readfirstlane(o) == 3u) {
      if (lane < 4) st4_sc1(myp - (wave & 3) + lane, (unsigned)(bb + 2));
      WTM(slot, bb, 2);
    }
  }
}
#define CALF 1
#define NCS 6
__device__ __forceinline__ void l1_role(char* smem, int wave_id_, bool fst, int c, const u16* __restrict__ x, const float* __restrict__ mod,
                                        const u16* __restrict__ mem0, char* __restrict__ h1c, unsigned*& cnt, u64 tag, unsigned* tmo, unsigned long long* mts) {
  const int tid = opaque_tid(), lane = tid & 63, wave = WAVE_ID;
  char* ws = (char*)mod - OFF_MOD;
  u32x4 xr[7][2];
  const int row0 = (fst ? 0 : 256) + 8 * c + wave;
  const u16* src = x + (size_t)row0 * DM + lane * 8;
  xr[0][0] = ldnt<2>(src); xr[0][1] = ldnt<2>(src + 512);
  volatile LAS unsigned* sg = (volatile LAS unsigned*)(smem + L_SLOT + 784);
  if (!fst) {
#pragma unroll
    for (int pp = 1; pp < 7; ++pp) { xr[pp][0] = ldnt<2>(src + (size_t)pp * 256 * DM); xr[pp][1] = ldnt<2>(src + (size_t)pp * 256 * DM + 512); }
  } else if (wave >= 5) {
    char* cal = xch_base(ws) + ((size_t)(wave == 5 ? 0 : wave - 3) << 21) + (126 + (c >> 4)) * 16384 + (c & 15) * 512;
    st16_sc1(cal + (lane >> 5) * 8192 + (lane & 31) * 16, u32x4{SENT, SENT, SENT, SENT});
    WAIT_V(0);
    unsigned best[2] = {1u << 30, 1u << 30};
    const char* sb = uni(cal);
#pragma unroll
    for (int r = 1; r < NCS; ++r) {
      unsigned long long t0, t1; unsigned v;
      asm volatile("s_memtime %0\n\ts_waitcnt lgkmcnt(0)\n\ts_load_dword %2, %3, %4 glc\n\ts_waitcnt lgkmcnt(0)\n\ts_memtime %1\n\ts_waitcnt lgkmcnt(0)"
                   : "=&s"(t0), "=&s"(t1), "=&s"(v) : "s"(sb), "i"((r & 1) * 8192 + (r >> 1) * 128) : "memory");
      if (r > 1) best[r & 1] = min(best[r & 1], (unsigned)(t1 - t0));
    }
    unsigned xcc; asm volatile("s_getreg_b32 %0, hwreg(HW_REG_XCC_ID)" : "=s"(xcc));
    const unsigned df = max(best[0], best[1]) - min(best[0], best[1]);
    sg[wave - 5] = df < 110u || df > 400u ? 0u : (((best[1] < best[0]) != ((xcc & 4) != 0)) ? 1u : 8u) << (wave - 5);
  }
  if (CALF) BAR();
  if (!fst || wave >= 5) {
  } else if (wave < 3) {
    const int b = c + 32 * wave;
    if (b < 89) {
      const char* p = uni(xch_base(ws) + (b < 48 ? (size_t)((b >> 4) * 32 + (b & 15)) * 16384
                                        : b < 56 ? ((size_t)3 << 21) + (size_t)(8 + ((b - 48) >> 2) * 8 + (b & 3)) * 16384
                                        : b < 84 ? ((size_t)3 << 21) + (size_t)(b - 56 + 24) * 16384 : ((size_t)4 << 21) + (size_t)(b - 84) * 16384));
      const unsigned v = b < 80 ? SENT : 0u;
#pragma unroll
      for (int i = 0; i < 16; ++i) st16s(p, (unsigned)(i * 1024 + lane * 16), u32x4{v, v, v, v});
    } else if (b == 89 && lane == 0) st4_sc1(tmo, 0u);
    WAIT_V(0);
  } else {
    const int pc = (c & 7) * 2 + (wave & 1), g = lane >> 4, hf = (lane >> 2) & 1, row = ((lane >> 3) & 1) * 4 + (lane & 3);
    u32x4 v = *(const u32x4*)(mem0 + row * DM + 32 * (2 * pc + hf) + 8 * g);
    v.x = nosent(v.x); v.y = nosent(v.y); v.z = nosent(v.z); v.w = nosent(v.w);
    st16_sc1(xch_base(ws) + ((size_t)3 << 21) + (size_t)(((c >> 3) & 1) * 2 + (pc >> 3)) * 16384 + (c >> 4) * 8192 + (pc & 7) * 1024 + lane * 16, v);
    WAIT_V(0);
  }
  BAR();
  if (fst && tid == 0) st8_sc1(ws + OFF_INIT + c * 128, u32x2{(unsigned)tag << 6 | sg[0] | sg[1] | sg[2], (unsigned)(tag >> 26)});
  if (fst && wave == 0) STS(ws, 128 + (c >> 3) * 32 + 24 + (c & 7), 2);
  {
    f32x4 v;
    for (int spins = 0; spins < SPIN_CAP; ++spins) {
      u32x4 m[4];
      asm volatile("s_nop 4\n\tglobal_load_dwordx4 %0, %4, %6 sc1\n\tglobal_load_dwordx4 %1, %4, %6 offset:16 sc1\n\t"
                   "global_load_dwordx4 %2, %5, %6 sc1\n\tglobal_load_dwordx4 %3, %5, %6 offset:16 sc1\n\ts_waitcnt vmcnt(0)"
                   : "=&v"(m[0]), "=&v"(m[1]), "=&v"(m[2]), "=&v"(m[3]) : "v"((unsigned)tid * 32), "v"((unsigned)tid * 32 + 16384), "s"(mod) : "memory");
      bool ok = true;
#pragma unroll
      for (int i = 0; i < 4; ++i) ok = ok && m[i].y == (unsigned)tag && m[i].w == tag2(tag);
      v = f32x4{__uint_as_float(m[0].x) + __uint_as_float(m[2].x), __uint_as_float(m[0].z) + __uint_as_float(m[2].z),
                __uint_as_float(m[1].x) + __uint_as_float(m[3].x), __uint_as_float(m[1].z) + __uint_as_float(m[3].z)};
      if (__all(ok)) break;
      __builtin_amdgcn_s_sleep(4);
    }
    v = f32x4{bfr(v[0]), bfr(v[1]), bfr(v[2]), bfr(v[3])};
    const int a = tid >> 8, cc = tid & 255;
    if (a) v = f32x4{bfr(1.0f + v[0]), bfr(1.0f + v[1]), bfr(1.0f + v[2]), bfr(1.0f + v[3])};
    *(f32x4*)(smem + 4096 + a * 4096 + ((cc >> 7) * 2 + (cc & 1)) * 1024 + ((cc >> 1) & 63) * 16) = v;
  }
  BAR();
  if (fst && wave == 0) STS(ws, 128 + (c >> 3) * 32 + 24 + (c & 7), 3);
#pragma unroll
  for (int p = 0; p < 7; ++p) {
    if (p > 0 && fst) break;
    f32x2 v[2][4];
#pragma unroll
    for (int i = 0; i < 2; ++i)
#pragma unroll
      for (int e = 0; e < 4; ++e) v[i][e] = f32x2{bflo(xr[p][i][e]), bfhi(xr[p][i][e])};
    const f32x2 s2 = ((v[0][0] + v[0][1]) + (v[0][2] + v[0][3])) + ((v[1][0] + v[1][1]) + (v[1][2] + v[1][3]));
    const float mean = wavesum(s2[0] + s2[1]) * (1.0f / DM);
    f32x2 q2 = f32x2{0, 0};
#pragma unroll
    for (int i = 0; i < 8; ++i) { const f32x2 d = v[i >> 2][i & 3] - f32x2{mean, mean}; q2 = pkfma(d, d, q2); }
    const float q = wavesum(q2[0] + q2[1]);
    if (p == 0 && fst && wave == 0) STS((char*)tmo - OFF_TMO, 128 + (c >> 3) * 32 + 24 + (c & 7), 5);
    const float mb = bfr(mean), vb = bfr(q * (1.0f / DM));
    const float rden = 1.0f / bfr(sqrtf(bfr(vb + 1e-5f)));
    const int row = 256 * p + row0, rb = row & 31;
    char* dst = h1c + (size_t)(row >> 5) * 65536 + (lane >> 3) * 4096 + rb * 128 + (((lane & 7) ^ (rb & 7)) << 4);
    const char* ml = smem + lane * 16;
#pragma unroll
    for (int i = 0; i < 2; ++i) st16_sc1(dst + i * 32768, norm8(v[i], ml + i * 2048, mb, rden));
  }
  cnt = cnt_of(ws, init_wait(ws, tag));
  WAIT_V(0);
  __builtin_amdgcn_s_barrier();
  if (tid == 0) st4_sc1(cnt + ((fst ? PL1 : PL1B) + c) * 32, fst ? 8u : 7u);
  if (wave == 0) STS(ws, 128 + (c >> 3) * 32 + 24 + (c & 7), fst ? 6 : 7);
}
__device__ __forceinline__ void mod2_job(char* smem, int wave_id_, int c, const float* __restrict__ mod, const u16* __restrict__ cvec, const u16* __restrict__ wmod, unsigned* cnt) {
  const int tid = opaque_tid();
  {
    const float sva = bf2f(cvec[tid]), svb = bf2f(cvec[tid + 512]);
    float* sv = (float*)(smem + 12288);
    sv[tid] = bfr(sva / (1.0f + __expf(-sva))); sv[tid + 512] = bfr(svb / (1.0f + __expf(-svb)));
  }
  __syncthreads();
  {
    const int cg = tid & 15, r0 = tid >> 4;
    const u16* wp = wmod + (size_t)r0 * (6 * DM) + 2 * DM + 128 * c + cg * 8;
    const float* sv = (const float*)(smem + 12288) + r0;
    f32x2 acc[4] = {f32x2{0, 0}, f32x2{0, 0}, f32x2{0, 0}, f32x2{0, 0}};
#pragma unroll 1
    for (int o = 0; o < 4; ++o) {
      u32x4 w[8];
#pragma unroll
      for (int i = 0; i < 8; ++i) w[i] = ldnt<1>(wp + (size_t)(o * 8 + i) * 32 * (6 * DM));
#pragma unroll
      for (int i = 0; i < 8; ++i) {
        const float s = sv[(o * 8 + i) * 32];
#pragma unroll
        for (int e = 0; e < 4; ++e) acc[e] = pkfma(f32x2{s, s}, f32x2{bflo(w[i][e]), bfhi(w[i][e])}, acc[e]);
      }
    }
    float* red = (float*)(smem + 16384);
    *(f32x4*)(red + r0 * 128 + cg * 8) = f32x4{acc[0][0], acc[0][1], acc[1][0], acc[1][1]};
    *(f32x4*)(red + r0 * 128 + cg * 8 + 4) = f32x4{acc[2][0], acc[2][1], acc[3][0], acc[3][1]};
    __syncthreads();
    if (tid < 128) {
      float s = 0;
#pragma unroll
      for (int r = 0; r < 32; ++r) s += red[r * 128 + tid];
      st4_sc1((char*)mod + OFF_MOD2 + (128 * c + tid) * 4, __builtin_bit_cast(unsigned, s));
    }
    WAIT_V(0);
    __builtin_amdgcn_s_barrier();
    if (tid == 0) st4_sc1(cnt + (PL1 + c) * 32 + 1, 1u);
  }
}
enum { R_QKV = 0, R_PART = 1, R_F1 = 2, R_PART2 = 3 };
struct Dep {
  const char* base; unsigned voff; unsigned mul, add;
};
__device__ __forceinline__ unsigned dep_eval(unsigned v, const Dep& d) {
  v = rowmin16(v * d.mul + (d.add + 64u));
  return min(min(rdl(v, 0), rdl(v, 16)), min(rdl(v, 32), rdl(v, 48))) - 64u;
}
typedef unsigned u32x32 __attribute__((ext_vector_type(32)));
typedef unsigned u32x16 __attribute__((ext_vector_type(16)));
__device__ __forceinline__ void fast_ld0(const u32x32 (&W)[4], u32x16& pd, unsigned ea, unsigned da, int& b, int& lf, int cp, unsigned up, unsigned sa, unsigned mu, unsigned ad,
    unsigned rw, unsigned ab, const char* p1, const char* p0, unsigned l1, unsigned l0, const unsigned (&av)[8], int la) {
  asm volatile("s_setprio 0\n\t"
    "s_cmp_eq_u32 %[la], 3\n\t"
    "s_waitcnt vmcnt(12)\n\t"
    "s_cbranch_scc1 20f\n\t"
    "s_waitcnt vmcnt(4)\n\t"
    "20:\n\t"
    "s_barrier\n\t"
    "ds_read_b128 v[96:99], %[ea] offset:0\n\t"
    "ds_read_b128 v[100:103], %[ea] offset:2048\n\t"
    "ds_read_b128 v[104:107], %[da] offset:0\n\t"
    "ds_read_b128 v[108:111], %[da] offset:2048\n\t"
    "ds_read_b128 v[112:115], %[ea] offset:4096\n\t"
    "ds_read_b128 v[116:119], %[ea] offset:6144\n\t"
    "s_branch 2f\n\t"
    ".p2align 6\n\t"
    "1:\n\t"
    "s_cmp_eq_u32 %[la], 3\n\t"
    "s_waitcnt vmcnt(12)\n\t"
    "s_cbranch_scc1 21f\n\t"
    "s_waitcnt vmcnt(4)\n\t"
    "21:\n\t"
    "s_barrier\n\t"
    "2:\n\t"
    "s_waitcnt lgkmcnt(4)\n\t"
    "v_mfma_f32_16x16x32_bf16 v[64:67], v[128:131], v[96:99], 0\n\t"
    "ds_read_b128 v[120:123], %[da] offset:4096\n\t"
    "s_add_u32 m0, %[l1], 0\n\t"
    "ds_write_b128 %[rw], v[80:83] offset:0\n\t"
    "v_mfma_f32_16x16x32_bf16 v[68:71], v[132:135], v[96:99], 0\n\t"
    "ds_read_b128 v[124:127], %[da] offset:6144\n\t"
    "global_load_lds_dwordx4 %[av0], s[36:37] sc1\n\t"
    "ds_write_b128 %[rw], v[84:87] offset:1024\n\t"
    "v_mfma_f32_16x16x32_bf16 v[72:75], v[128:131], v[100:103], 0\n\t"
    "s_add_u32 m0, %[l1], 32768\n\t"
    "ds_write_b128 %[rw], v[88:91] offset:2048\n\t"
    "v_mfma_f32_16x16x32_bf16 v[76:79], v[132:135], v[100:103], 0\n\t"
    "global_load_lds_dwordx4 %[av1], s[36:37] sc1\n\t"
    "ds_write_b128 %[rw], v[92:95] offset:3072\n\t"
    "s_waitcnt lgkmcnt(8)\n\t"
    "v_mfma_f32_16x16x32_bf16 v[64:67], v[136:139], v[104:107], v[64:67]\n\t"
    "ds_read_b128 v[96:99], %[ea] offset:8192\n\t"
    "s_add_u32 m0, %[l1], 4096\n\t"
    "v_mfma_f32_16x16x32_bf16 v[68:71], v[140:143], v[104:107], v[68:71]\n\t"
    "ds_read_b128 v[100:103], %[ea] offset:10240\n\t"
    "global_load_lds_dwordx4 %[av2], s[36:37] sc1\n\t"
    "v_mfma_f32_16x16x32_bf16 v[72:75], v[136:139], v[108:111], v[72:75]\n\t"
    "s_add_u32 m0, %[l1], 36864\n\t"
    "v_mfma_f32_16x16x32_bf16 v[76:79], v[140:143], v[108:111], v[76:79]\n\t"
    "global_load_lds_dwordx4 %[av3], s[36:37] sc1\n\t"
    "s_waitcnt lgkmcnt(8)\n\t"
    "v_mfma_f32_16x16x32_bf16 v[64:67], v[144:147], v[112:115], v[64:67]\n\t"
    "ds_read_b128 v[104:107], %[da] offset:8192\n\t"
    "s_add_u32 m0, %[l1], 8192\n\t"
    "v_mfma_f32_16x16x32_bf16 v[68:71], v[148:151], v[112:115], v[68:71]\n\t"
    "ds_read_b128 v[108:111], %[da] offset:10240\n\t"
    "global_load_lds_dwordx4 %[av4], s[36:37] sc1\n\t"
    "v_mfma_f32_16x16x32_bf16 v[72:75], v[144:147], v[116:119], v[72:75]\n\t"
    "s_add_u32 m0, %[l1], 40960\n\t"
    "v_mfma_f32_16x16x32_bf16 v[76:79], v[148:151], v[116:119], v[76:79]\n\t"
    "global_load_lds_dwordx4 %[av5], s[36:37] sc1\n\t"
    "s_waitcnt lgkmcnt(7)\n\t"
    "v_mfma_f32_16x16x32_bf16 v[64:67], v[152:155], v[120:123], v[64:67]\n\t"
    "ds_read_b128 v[112:115], %[ea] offset:12288\n\t"
    "s_add_u32 m0, %[l1], 12288\n\t"
    "v_mfma_f32_16x16x32_bf16 v[68:71], v[156:159], v[120:123], v[68:71]\n\t"
    "ds_read_b128 v[116:119], %[ea] offset:14336\n\t"
    "global_load_lds_dwordx4 %[av6], s[36:37] sc1\n\t"
    "v_mfma_f32_16x16x32_bf16 v[72:75], v[152:155], v[124:127], v[72:75]\n\t"
    "s_add_u32 m0, %[l1], 45056\n\t"
    "v_mfma_f32_16x16x32_bf16 v[76:79], v[156:159], v[124:127], v[76:79]\n\t"
    "global_load_lds_dwordx4 %[av7], s[36:37] sc1\n\t"
    "s_waitcnt lgkmcnt(4)\n\t"
    "v_mfma_f32_16x16x32_bf16 v[64:67], v[160:163], v[96:99], v[64:67]\n\t"
    "ds_read_b128 v[120:123], %[da] offset:12288\n\t"
    "v_mfma_f32_16x16x32_bf16 v[68:71], v[164:167], v[96:99], v[68:71]\n\t"
    "ds_read_b128 v[124:127], %[da] offset:14336\n\t"
    "v_mfma_f32_16x16x32_bf16 v[72:75], v[160:163], v[100:103], v[72:75]\n\t"
    "v_mfma_f32_16x16x32_bf16 v[76:79], v[164:167], v[100:103], v[76:79]\n\t"
    "s_waitcnt lgkmcnt(4)\n\t"
    "v_mfma_f32_16x16x32_bf16 v[64:67], v[168:171], v[104:107], v[64:67]\n\t"
    "ds_read_b128 v[96:99], %[ea] offset:16384\n\t"
    "v_mfma_f32_16x16x32_bf16 v[68:71], v[172:175], v[104:107], v[68:71]\n\t"
    "ds_read_b128 v[100:103], %[ea] offset:18432\n\t"
    "v_mfma_f32_16x16x32_bf16 v[72:75], v[168:171], v[108:111], v[72:75]\n\t"
    "v_mfma_f32_16x16x32_bf16 v[76:79], v[172:175], v[108:111], v[76:79]\n\t"
    "s_waitcnt lgkmcnt(4)\n\t"
    "v_mfma_f32_16x16x32_bf16 v[64:67], v[176:179], v[112:115], v[64:67]\n\t"
    "ds_read_b128 v[104:107], %[da] offset:16384\n\t"
    "v_mfma_f32_16x16x32_bf16 v[68:71], v[180:183], v[112:115], v[68:71]\n\t"
    "ds_read_b128 v[108:111], %[da] offset:18432\n\t"
    "v_mfma_f32_16x16x32_bf16 v[72:75], v[176:179], v[116:119], v[72:75]\n\t"
    "v_mfma_f32_16x16x32_bf16 v[76:79], v[180:183], v[116:119], v[76:79]\n\t"
    "s_waitcnt lgkmcnt(4)\n\t"
    "v_mfma_f32_16x16x32_bf16 v[64:67], v[184:187], v[120:123], v[64:67]\n\t"
    "ds_read_b128 v[112:115], %[ea] offset:20480\n\t"
    "v_mfma_f32_16x16x32_bf16 v[68:71], v[188:191], v[120:123], v[68:71]\n\t"
    "ds_read_b128 v[116:119], %[ea] offset:22528\n\t"
    "v_mfma_f32_16x16x32_bf16 v[72:75], v[184:187], v[124:127], v[72:75]\n\t"
    "v_mfma_f32_16x16x32_bf16 v[76:79], v[188:191], v[124:127], v[76:79]\n\t"
    "s_cmp_eq_u32 %[la], 3\n\t"
    "s_waitcnt vmcnt(12)\n\t"
    "s_cbranch_scc1 22f\n\t"
    "s_waitcnt vmcnt(4)\n\t"
    "22:\n\t"
    "s_barrier\n\t"
    "s_waitcnt lgkmcnt(4)\n\t"
    "v_mfma_f32_16x16x32_bf16 v[64:67], v[192:195], v[96:99], v[64:67]\n\t"
    "ds_read_b128 v[120:123], %[da] offset:20480\n\t"
    "s_add_u32 m0, %[l0], 0\n\t"
    "v_mfma_f32_16x16x32_bf16 v[68:71], v[196:199], v[96:99], v[68:71]\n\t"
    "ds_read_b128 v[124:127], %[da] offset:22528\n\t"
    "global_load_lds_dwordx4 %[av0], s[38:39] sc1\n\t"
    "v_mfma_f32_16x16x32_bf16 v[72:75], v[192:195], v[100:103], v[72:75]\n\t"
    "s_add_u32 m0, %[l0], 32768\n\t"
    "v_mfma_f32_16x16x32_bf16 v[76:79], v[196:199], v[100:103], v[76:79]\n\t"
    "global_load_lds_dwordx4 %[av1], s[38:39] sc1\n\t"
    "s_waitcnt lgkmcnt(4)\n\t"
    "v_mfma_f32_16x16x32_bf16 v[64:67], v[200:203], v[104:107], v[64:67]\n\t"
    "ds_read_b128 v[96:99], %[ea] offset:24576\n\t"
    "s_add_u32 m0, %[l0], 4096\n\t"
    "v_mfma_f32_16x16x32_bf16 v[68:71], v[204:207], v[104:107], v[68:71]\n\t"
    "ds_read_b128 v[100:103], %[ea] offset:26624\n\t"
    "global_load_lds_dwordx4 %[av2], s[38:39] sc1\n\t"
    "v_mfma_f32_16x16x32_bf16 v[72:75], v[200:203], v[108:111], v[72:75]\n\t"
    "s_add_u32 m0, %[l0], 36864\n\t"
    "v_mfma_f32_16x16x32_bf16 v[76:79], v[204:207], v[108:111], v[76:79]\n\t"
    "global_load_lds_dwordx4 %[av3], s[38:39] sc1\n\t"
    "s_add_u32 s36, s36, %[ab]\n\t"
    "s_addc_u32 s37, s37, 0\n\t"
    "s_waitcnt lgkmcnt(4)\n\t"
    "v_mfma_f32_16x16x32_bf16 v[64:67], v[208:211], v[112:115], v[64:67]\n\t"
    "ds_read_b128 v[104:107], %[da] offset:24576\n\t"
    "s_add_u32 m0, %[l0], 8192\n\t"
    "v_mfma_f32_16x16x32_bf16 v[68:71], v[212:215], v[112:115], v[68:71]\n\t"
    "ds_read_b128 v[108:111], %[da] offset:26624\n\t"
    "global_load_lds_dwordx4 %[av4], s[38:39] sc1\n\t"
    "v_mfma_f32_16x16x32_bf16 v[72:75], v[208:211], v[116:119], v[72:75]\n\t"
    "ds_read_b32 v39, %[sa]\n\t"
    "s_add_u32 m0, %[l0], 40960\n\t"
    "v_mfma_f32_16x16x32_bf16 v[76:79], v[212:215], v[116:119], v[76:79]\n\t"
    "global_load_lds_dwordx4 %[av5], s[38:39] sc1\n\t"
    "s_xor_b32 %[l1], %[l1], 0x10000\n\t"
    "s_waitcnt lgkmcnt(5)\n\t"
    "v_mfma_f32_16x16x32_bf16 v[64:67], v[216:219], v[120:123], v[64:67]\n\t"
    "ds_read_b128 v[112:115], %[ea] offset:28672\n\t"
    "s_add_u32 m0, %[l0], 12288\n\t"
    "v_mfma_f32_16x16x32_bf16 v[68:71], v[220:223], v[120:123], v[68:71]\n\t"
    "ds_read_b128 v[116:119], %[ea] offset:30720\n\t"
    "global_load_lds_dwordx4 %[av6], s[38:39] sc1\n\t"
    "v_mfma_f32_16x16x32_bf16 v[72:75], v[216:219], v[124:127], v[72:75]\n\t"
    "v_xor_b32 %[ea], 0x10000, %[ea]\n\t"
    "s_add_u32 m0, %[l0], 45056\n\t"
    "v_mfma_f32_16x16x32_bf16 v[76:79], v[220:223], v[124:127], v[76:79]\n\t"
    "global_load_lds_dwordx4 %[av7], s[38:39] sc1\n\t"
    "s_waitcnt lgkmcnt(5)\n\t"
    "v_mfma_f32_16x16x32_bf16 v[64:67], v[224:227], v[96:99], v[64:67]\n\t"
    "ds_read_b128 v[120:123], %[da] offset:28672\n\t"
    "v_mfma_f32_16x16x32_bf16 v[68:71], v[228:231], v[96:99], v[68:71]\n\t"
    "ds_read_b128 v[124:127], %[da] offset:30720\n\t"
    "v_mfma_f32_16x16x32_bf16 v[72:75], v[224:227], v[100:103], v[72:75]\n\t"
    "v_xor_b32 %[da], 0x10000, %[da]\n\t"
    "v_mfma_f32_16x16x32_bf16 v[76:79], v[228:231], v[100:103], v[76:79]\n\t"
    "s_waitcnt lgkmcnt(5)\n\t"
    "v_mfma_f32_16x16x32_bf16 v[64:67], v[232:235], v[104:107], v[64:67]\n\t"
    "ds_read_b128 v[96:99], %[ea] offset:0\n\t"
    "v_mfma_f32_16x16x32_bf16 v[68:71], v[236:239], v[104:107], v[68:71]\n\t"
    "ds_read_b128 v[100:103], %[ea] offset:2048\n\t"
    "v_mfma_f32_16x16x32_bf16 v[72:75], v[232:235], v[108:111], v[72:75]\n\t"
    "v_mfma_f32_16x16x32_bf16 v[76:79], v[236:239], v[108:111], v[76:79]\n\t"
    "s_waitcnt lgkmcnt(4)\n\t"
    "v_mfma_f32_16x16x32_bf16 v[64:67], v[240:243], v[112:115], v[64:67]\n\t"
    "ds_read_b128 v[104:107], %[da] offset:0\n\t"
    "v_mad_u32_u24 v39, v39, %[mu], %[ad]\n\t"
    "v_mfma_f32_16x16x32_bf16 v[68:71], v[244:247], v[112:115], v[68:71]\n\t"
    "ds_read_b128 v[108:111], %[da] offset:2048\n\t"
    "v_cmp_ge_i32 vcc, %[lf], v39\n\t"
    "s_cmp_eq_u64 vcc, 0\n\t"
    "s_addc_u32 %[lf], %[lf], 0\n\t"
    "v_mfma_f32_16x16x32_bf16 v[72:75], v[240:243], v[116:119], v[72:75]\n\t"
    "v_mfma_f32_16x16x32_bf16 v[76:79], v[244:247], v[116:119], v[76:79]\n\t"
    "v_cmp_ge_i32 vcc, %[lf], v39\n\t"
    "s_cmp_eq_u64 vcc, 0\n\t"
    "s_addc_u32 %[lf], %[lf], 0\n\t"
    "s_waitcnt lgkmcnt(4)\n\t"
    "v_mfma_f32_16x16x32_bf16 v[80:83], v[248:251], v[120:123], v[64:67]\n\t"
    "ds_read_b128 v[112:115], %[ea] offset:4096\n\t"
    "s_min_i32 %[lf], %[lf], %[cp]\n\t"
    "s_add_u32 %[b], %[b], 1\n\t"
    "v_mfma_f32_16x16x32_bf16 v[84:87], v[252:255], v[120:123], v[68:71]\n\t"
    "ds_read_b128 v[116:119], %[ea] offset:6144\n\t"
    "v_mfma_f32_16x16x32_bf16 v[88:91], v[248:251], v[124:127], v[72:75]\n\t"
    "v_mfma_f32_16x16x32_bf16 v[92:95], v[252:255], v[124:127], v[76:79]\n\t"
    "s_add_u32 s38, s38, %[ab]\n\t"
    "s_addc_u32 s39, s39, 0\n\t"
    "s_xor_b32 %[l0], %[l0], 0x10000\n\t"
    "s_sub_u32 m0, %[lf], %[b]\n\t"
    "s_sub_u32 m0, m0, 1\n\t"
    "s_cmp_lt_u32 m0, %[up]\n\t"
    "s_cbranch_scc1 1b\n\t"
    "s_waitcnt lgkmcnt(0)\n\t"
    "s_nop 4"
    : "=&{v[80:95]}"(pd), [ea] "+&v"(ea), [da] "+&v"(da), [b] "+&s"(b), [lf] "+&s"(lf), "+{s[36:37]}"(p1), "+{s[38:39]}"(p0), [sa] "+&v"(sa), [l1] "+&s"(l1), [l0] "+&s"(l0)
    : [rw] "v"(rw), [ab] "s"(ab), [cp] "s"(cp), [mu] "s"(mu), [ad] "v"(ad), [up] "s"(up), "{v[128:159]}"(W[0]), "{v[160:191]}"(W[1]), "{v[192:223]}"(W[2]), "{v[224:255]}"(W[3]), [av0] "v"(av[0]), [av1] "v"(av[1]), [av2] "v"(av[2]), [av3] "v"(av[3]), [av4] "v"(av[4]), [av5] "v"(av[5]), [av6] "v"(av[6]), [av7] "v"(av[7]), [la] "s"(la)
    : "v39", "v64", "v65", "v66", "v67", "v68", "v69", "v70", "v71", "v72", "v73", "v74", "v75", "v76", "v77", "v78", "v79", "v96", "v97", "v98", "v99", "v100", "v101", "v102", "v103", "v104", "v105", "v106", "v107", "v108", "v109", "v110", "v111", "v112", "v113", "v114", "v115", "v116", "v117", "v118", "v119", "v120", "v121", "v122", "v123", "v124", "v125", "v126", "v127", "vcc", "scc", "m0", "memory");
}
__device__ __forceinline__ void fast_ld1(const u32x32 (&W)[4], u32x16& pd, unsigned ea, unsigned da, int& b, int& lf, int cp, unsigned up, unsigned sa, unsigned mu, unsigned ad,
    unsigned rw, unsigned ab, const char* p1, const char* p0, unsigned l1, unsigned l0, const unsigned (&av)[8], int la) {
  asm volatile("s_setprio 0\n\t"
    "s_cmp_eq_u32 %[la], 3\n\t"
    "s_waitcnt vmcnt(12)\n\t"
    "s_cbranch_scc1 20f\n\t"
    "s_waitcnt vmcnt(4)\n\t"
    "20:\n\t"
    "s_barrier\n\t"
    "ds_read_b128 v[96:99], %[ea] offset:0\n\t"
    "ds_read_b128 v[100:103], %[ea] offset:2048\n\t"
    "ds_read_b128 v[104:107], %[da] offset:0\n\t"
    "ds_read_b128 v[108:111], %[da] offset:2048\n\t"
    "ds_read_b128 v[112:115], %[ea] offset:4096\n\t"
    "ds_read_b128 v[116:119], %[ea] offset:6144\n\t"
    "s_branch 2f\n\t"
    ".p2align 6\n\t"
    "1:\n\t"
    "s_cmp_eq_u32 %[la], 3\n\t"
    "s_waitcnt vmcnt(12)\n\t"
    "s_cbranch_scc1 21f\n\t"
    "s_waitcnt vmcnt(4)\n\t"
    "21:\n\t"
    "s_barrier\n\t"
    "2:\n\t"
    "s_waitcnt lgkmcnt(4)\n\t"
    "v_mfma_f32_16x16x32_bf16 v[64:67], v[128:131], v[96:99], 0\n\t"
    "ds_read_b128 v[120:123], %[da] offset:4096\n\t"
    "s_add_u32 m0, %[l1], 0\n\t"
    "ds_write_b128 %[rw], v[80:83] offset:0\n\t"
    "v_mfma_f32_16x16x32_bf16 v[68:71], v[132:135], v[96:99], 0\n\t"
    "ds_read_b128 v[124:127], %[da] offset:6144\n\t"
    "global_load_lds_dwordx4 %[av0], s[36:37] sc1\n\t"
    "ds_write_b128 %[rw], v[84:87] offset:1024\n\t"
    "v_mfma_f32_16x16x32_bf16 v[72:75], v[128:131], v[100:103], 0\n\t"
    "s_add_u32 m0, %[l1], 32768\n\t"
    "ds_write_b128 %[rw], v[88:91] offset:2048\n\t"
    "v_mfma_f32_16x16x32_bf16 v[76:79], v[132:135], v[100:103], 0\n\t"
    "global_load_lds_dwordx4 %[av1], s[36:37] sc1\n\t"
    "ds_write_b128 %[rw], v[92:95] offset:3072\n\t"
    "s_waitcnt lgkmcnt(8)\n\t"
    "v_mfma_f32_16x16x32_bf16 v[64:67], v[136:139], v[104:107], v[64:67]\n\t"
    "ds_read_b128 v[96:99], %[ea] offset:8192\n\t"
    "s_add_u32 m0, %[l1], 4096\n\t"
    "v_mfma_f32_16x16x32_bf16 v[68:71], v[140:143], v[104:107], v[68:71]\n\t"
    "ds_read_b128 v[100:103], %[ea] offset:10240\n\t"
    "global_load_lds_dwordx4 %[av2], s[36:37] sc1\n\t"
    "v_mfma_f32_16x16x32_bf16 v[72:75], v[136:139], v[108:111], v[72:75]\n\t"
    "s_add_u32 m0, %[l1], 36864\n\t"
    "v_mfma_f32_16x16x32_bf16 v[76:79], v[140:143], v[108:111], v[76:79]\n\t"
    "global_load_lds_dwordx4 %[av3], s[36:37] sc1\n\t"
    "s_waitcnt lgkmcnt(8)\n\t"
    "v_mfma_f32_16x16x32_bf16 v[64:67], v[144:147], v[112:115], v[64:67]\n\t"
    "ds_read_b128 v[104:107], %[da] offset:8192\n\t"
    "s_add_u32 m0, %[l1], 8192\n\t"
    "v_mfma_f32_16x16x32_bf16 v[68:71], v[148:151], v[112:115], v[68:71]\n\t"
    "ds_read_b128 v[108:111], %[da] offset:10240\n\t"
    "global_load_lds_dwordx4 %[av4], s[36:37] sc1\n\t"
    "v_mfma_f32_16x16x32_bf16 v[72:75], v[144:147], v[116:119], v[72:75]\n\t"
    "s_add_u32 m0, %[l1], 40960\n\t"
    "v_mfma_f32_16x16x32_bf16 v[76:79], v[148:151], v[116:119], v[76:79]\n\t"
    "global_load_lds_dwordx4 %[av5], s[36:37] sc1\n\t"
    "s_waitcnt lgkmcnt(7)\n\t"
    "v_mfma_f32_16x16x32_bf16 v[64:67], v[152:155], v[120:123], v[64:67]\n\t"
    "ds_read_b128 v[112:115], %[ea] offset:12288\n\t"
    "s_add_u32 m0, %[l1], 12288\n\t"
    "v_mfma_f32_16x16x32_bf16 v[68:71], v[156:159], v[120:123], v[68:71]\n\t"
    "ds_read_b128 v[116:119], %[ea] offset:14336\n\t"
    "global_load_lds_dwordx4 %[av6], s[36:37] sc1\n\t"
    "v_mfma_f32_16x16x32_bf16 v[72:75], v[152:155], v[124:127], v[72:75]\n\t"
    "s_add_u32 m0, %[l1], 45056\n\t"
    "v_mfma_f32_16x16x32_bf16 v[76:79], v[156:159], v[124:127], v[76:79]\n\t"
    "global_load_lds_dwordx4 %[av7], s[36:37] sc1\n\t"
    "s_waitcnt lgkmcnt(4)\n\t"
    "v_mfma_f32_16x16x32_bf16 v[64:67], v[160:163], v[96:99], v[64:67]\n\t"
    "ds_read_b128 v[120:123], %[da] offset:12288\n\t"
    "v_mfma_f32_16x16x32_bf16 v[68:71], v[164:167], v[96:99], v[68:71]\n\t"
    "ds_read_b128 v[124:127], %[da] offset:14336\n\t"
    "v_mfma_f32_16x16x32_bf16 v[72:75], v[160:163], v[100:103], v[72:75]\n\t"
    "v_mfma_f32_16x16x32_bf16 v[76:79], v[164:167], v[100:103], v[76:79]\n\t"
    "s_waitcnt lgkmcnt(4)\n\t"
    "v_mfma_f32_16x16x32_bf16 v[64:67], v[168:171], v[104:107], v[64:67]\n\t"
    "ds_read_b128 v[96:99], %[ea] offset:16384\n\t"
    "v_mfma_f32_16x16x32_bf16 v[68:71], v[172:175], v[104:107], v[68:71]\n\t"
    "ds_read_b128 v[100:103], %[ea] offset:18432\n\t"
    "v_mfma_f32_16x16x32_bf16 v[72:75], v[168:171], v[108:111], v[72:75]\n\t"
    "v_mfma_f32_16x16x32_bf16 v[76:79], v[172:175], v[108:111], v[76:79]\n\t"
    "s_waitcnt lgkmcnt(4)\n\t"
    "v_mfma_f32_16x16x32_bf16 v[64:67], v[176:179], v[112:115], v[64:67]\n\t"
    "ds_read_b128 v[104:107], %[da] offset:16384\n\t"
    "v_mfma_f32_16x16x32_bf16 v[68:71], v[180:183], v[112:115], v[68:71]\n\t"
    "ds_read_b128 v[108:111], %[da] offset:18432\n\t"
    "v_mfma_f32_16x16x32_bf16 v[72:75], v[176:179], v[116:119], v[72:75]\n\t"
    "v_mfma_f32_16x16x32_bf16 v[76:79], v[180:183], v[116:119], v[76:79]\n\t"
    "s_waitcnt lgkmcnt(4)\n\t"
    "v_mfma_f32_16x16x32_bf16 v[64:67], v[184:187], v[120:123], v[64:67]\n\t"
    "ds_read_b128 v[112:115], %[ea] offset:20480\n\t"
    "v_mfma_f32_16x16x32_bf16 v[68:71], v[188:191], v[120:123], v[68:71]\n\t"
    "ds_read_b128 v[116:119], %[ea] offset:22528\n\t"
    "v_mfma_f32_16x16x32_bf16 v[72:75], v[184:187], v[124:127], v[72:75]\n\t"
    "v_mfma_f32_16x16x32_bf16 v[76:79], v[188:191], v[124:127], v[76:79]\n\t"
    "s_cmp_eq_u32 %[la], 3\n\t"
    "s_waitcnt vmcnt(12)\n\t"
    "s_cbranch_scc1 22f\n\t"
    "s_waitcnt vmcnt(4)\n\t"
    "22:\n\t"
    "s_barrier\n\t"
    "s_waitcnt lgkmcnt(4)\n\t"
    "v_mfma_f32_16x16x32_bf16 v[64:67], v[192:195], v[96:99], v[64:67]\n\t"
    "ds_read_b128 v[120:123], %[da] offset:20480\n\t"
    "s_add_u32 m0, %[l0], 0\n\t"
    "v_mfma_f32_16x16x32_bf16 v[68:71], v[196:199], v[96:99], v[68:71]\n\t"
    "ds_read_b128 v[124:127], %[da] offset:22528\n\t"
    "global_load_lds_dwordx4 %[av0], s[38:39] sc1\n\t"
    "v_mfma_f32_16x16x32_bf16 v[72:75], v[192:195], v[100:103], v[72:75]\n\t"
    "s_add_u32 m0, %[l0], 32768\n\t"
    "v_mfma_f32_16x16x32_bf16 v[76:79], v[196:199], v[100:103], v[76:79]\n\t"
    "global_load_lds_dwordx4 %[av1], s[38:39] sc1\n\t"
    "s_waitcnt lgkmcnt(4)\n\t"
    "v_mfma_f32_16x16x32_bf16 v[64:67], v[200:203], v[104:107], v[64:67]\n\t"
    "ds_read_b128 v[96:99], %[ea] offset:24576\n\t"
    "s_add_u32 m0, %[l0], 4096\n\t"
    "v_mfma_f32_16x16x32_bf16 v[68:71], v[204:207], v[104:107], v[68:71]\n\t"
    "ds_read_b128 v[100:103], %[ea] offset:26624\n\t"
    "global_load_lds_dwordx4 %[av2], s[38:39] sc1\n\t"
    "v_mfma_f32_16x16x32_bf16 v[72:75], v[200:203], v[108:111], v[72:75]\n\t"
    "s_add_u32 m0, %[l0], 36864\n\t"
    "v_mfma_f32_16x16x32_bf16 v[76:79], v[204:207], v[108:111], v[76:79]\n\t"
    "global_load_lds_dwordx4 %[av3], s[38:39] sc1\n\t"
    "s_add_u32 s36, s36, %[ab]\n\t"
    "s_addc_u32 s37, s37, 0\n\t"
    "s_waitcnt lgkmcnt(4)\n\t"
    "v_mfma_f32_16x16x32_bf16 v[64:67], v[208:211], v[112:115], v[64:67]\n\t"
    "ds_read_b128 v[104:107], %[da] offset:24576\n\t"
    "s_add_u32 m0, %[l0], 8192\n\t"
    "v_mfma_f32_16x16x32_bf16 v[68:71], v[212:215], v[112:115], v[68:71]\n\t"
    "ds_read_b128 v[108:111], %[da] offset:26624\n\t"
    "global_load_lds_dwordx4 %[av4], s[38:39] sc1\n\t"
    "v_mfma_f32_16x16x32_bf16 v[72:75], v[208:211], v[116:119], v[72:75]\n\t"
    "ds_read_b32 v39, %[sa]\n\t"
    "s_add_u32 m0, %[l0], 40960\n\t"
    "v_mfma_f32_16x16x32_bf16 v[76:79], v[212:215], v[116:119], v[76:79]\n\t"
    "global_load_lds_dwordx4 %[av5], s[38:39] sc1\n\t"
    "s_xor_b32 %[l1], %[l1], 0x10000\n\t"
    "s_waitcnt lgkmcnt(5)\n\t"
    "v_mfma_f32_16x16x32_bf16 v[64:67], v[216:219], v[120:123], v[64:67]\n\t"
    "ds_read_b128 v[112:115], %[ea] offset:28672\n\t"
    "s_add_u32 m0, %[l0], 12288\n\t"
    "v_mfma_f32_16x16x32_bf16 v[68:71], v[220:223], v[120:123], v[68:71]\n\t"
    "ds_read_b128 v[116:119], %[ea] offset:30720\n\t"
    "global_load_lds_dwordx4 %[av6], s[38:39] sc1\n\t"
    "v_mfma_f32_16x16x32_bf16 v[72:75], v[216:219], v[124:127], v[72:75]\n\t"
    "v_xor_b32 %[ea], 0x10000, %[ea]\n\t"
    "s_add_u32 m0, %[l0], 45056\n\t"
    "v_mfma_f32_16x16x32_bf16 v[76:79], v[220:223], v[124:127], v[76:79]\n\t"
    "global_load_lds_dwordx4 %[av7], s[38:39] sc1\n\t"
    "s_waitcnt lgkmcnt(5)\n\t"
    "v_mfma_f32_16x16x32_bf16 v[64:67], v[224:227], v[96:99], v[64:67]\n\t"
    "ds_read_b128 v[120:123], %[da] offset:28672\n\t"
    "v_mfma_f32_16x16x32_bf16 v[68:71], v[228:231], v[96:99], v[68:71]\n\t"
    "ds_read_b128 v[124:127], %[da] offset:30720\n\t"
    "v_mfma_f32_16x16x32_bf16 v[72:75], v[224:227], v[100:103], v[72:75]\n\t"
    "v_xor_b32 %[da], 0x10000, %[da]\n\t"
    "v_mfma_f32_16x16x32_bf16 v[76:79], v[228:231], v[100:103], v[76:79]\n\t"
    "s_waitcnt lgkmcnt(5)\n\t"
    "v_mfma_f32_16x16x32_bf16 v[64:67], v[232:235], v[104:107], v[64:67]\n\t"
    "ds_read_b128 v[96:99], %[ea] offset:0\n\t"
    "v_mfma_f32_16x16x32_bf16 v[68:71], v[236:239], v[104:107], v[68:71]\n\t"
    "ds_read_b128 v[100:103], %[ea] offset:2048\n\t"
    "v_mfma_f32_16x16x32_bf16 v[72:75], v[232:235], v[108:111], v[72:75]\n\t"
    "v_mfma_f32_16x16x32_bf16 v[76:79], v[236:239], v[108:111], v[76:79]\n\t"
    "s_waitcnt lgkmcnt(4)\n\t"
    "v_mfma_f32_16x16x32_bf16 v[64:67], v[240:243], v[112:115], v[64:67]\n\t"
    "ds_read_b128 v[104:107], %[da] offset:0\n\t"
    "v_mad_u32_u24 v39, v39, %[mu], %[ad]\n\t"
    "v_mfma_f32_16x16x32_bf16 v[68:71], v[244:247], v[112:115], v[68:71]\n\t"
    "ds_read_b128 v[108:111], %[da] offset:2048\n\t"
    "v_cmp_ge_i32 vcc, %[lf], v39\n\t"
    "s_cmp_eq_u64 vcc, 0\n\t"
    "s_addc_u32 %[lf], %[lf], 0\n\t"
    "v_mfma_f32_16x16x32_bf16 v[72:75], v[240:243], v[116:119], v[72:75]\n\t"
    "v_mfma_f32_16x16x32_bf16 v[76:79], v[244:247], v[116:119], v[76:79]\n\t"
    "v_cmp_ge_i32 vcc, %[lf], v39\n\t"
    "s_cmp_eq_u64 vcc, 0\n\t"
    "s_addc_u32 %[lf], %[lf], 0\n\t"
    "s_waitcnt lgkmcnt(4)\n\t"
    "v_mfma_f32_16x16x32_bf16 v[80:83], v[248:251], v[120:123], v[64:67]\n\t"
    "ds_read_b128 v[112:115], %[ea] offset:4096\n\t"
    "s_min_i32 %[lf], %[lf], %[cp]\n\t"
    "s_add_u32 %[b], %[b], 1\n\t"
    "v_mfma_f32_16x16x32_bf16 v[84:87], v[252:255], v[120:123], v[68:71]\n\t"
    "ds_read_b128 v[116:119], %[ea] offset:6144\n\t"
    "v_mfma_f32_16x16x32_bf16 v[88:91], v[248:251], v[124:127], v[72:75]\n\t"
    "v_mfma_f32_16x16x32_bf16 v[92:95], v[252:255], v[124:127], v[76:79]\n\t"
    "s_add_u32 s38, s38, %[ab]\n\t"
    "s_addc_u32 s39, s39, 0\n\t"
    "s_xor_b32 %[l0], %[l0], 0x10000\n\t"
    "s_sub_u32 m0, %[lf], %[b]\n\t"
    "s_sub_u32 m0, m0, 1\n\t"
    "s_cmp_lt_u32 m0, %[up]\n\t"
    "s_cbranch_scc1 1b\n\t"
    "s_waitcnt lgkmcnt(0)\n\t"
    "s_nop 4"
    : "=&{v[80:95]}"(pd), [ea] "+&v"(ea), [da] "+&v"(da), [b] "+&s"(b), [lf] "+&s"(lf), "+{s[36:37]}"(p1), "+{s[38:39]}"(p0), [sa] "+&v"(sa), [l1] "+&s"(l1), [l0] "+&s"(l0)
    : [rw] "v"(rw), [ab] "s"(ab), [cp] "s"(cp), [mu] "s"(mu), [ad] "v"(ad), [up] "s"(up), "{v[128:159]}"(W[0]), "{v[160:191]}"(W[1]), "{v[192:223]}"(W[2]), "{v[224:255]}"(W[3]), [av0] "v"(av[0]), [av1] "v"(av[1]), [av2] "v"(av[2]), [av3] "v"(av[3]), [av4] "v"(av[4]), [av5] "v"(av[5]), [av6] "v"(av[6]), [av7] "v"(av[7]), [la] "s"(la)
    : "v39", "v64", "v65", "v66", "v67", "v68", "v69", "v70", "v71", "v72", "v73", "v74", "v75", "v76", "v77", "v78", "v79", "v96", "v97", "v98", "v99", "v100", "v101", "v102", "v103", "v104", "v105", "v106", "v107", "v108", "v109", "v110", "v111", "v112", "v113", "v114", "v115", "v116", "v117", "v118", "v119", "v120", "v121", "v122", "v123", "v124", "v125", "v126", "v127", "vcc", "scc", "m0", "memory");
}
__device__ __forceinline__ void fast_ld2(const u32x32 (&W)[4], u32x16& pd, unsigned ea, unsigned da, int& b, int& lf, int cp, unsigned up, unsigned sa, unsigned mu, unsigned ad,
    unsigned rw, unsigned ab, const char* p1, const char* p0, unsigned l1, unsigned l0, const unsigned (&av)[8], int la) {
  asm volatile("s_setprio 0\n\t"
    "s_cmp_eq_u32 %[la], 3\n\t"
    "s_waitcnt vmcnt(6)\n\t"
    "s_cbranch_scc1 20f\n\t"
    "s_waitcnt vmcnt(2)\n\t"
    "20:\n\t"
    "s_barrier\n\t"
    "ds_read_b128 v[96:99], %[ea] offset:0\n\t"
    "ds_read_b128 v[100:103], %[ea] offset:2048\n\t"
    "ds_read_b128 v[104:107], %[da] offset:0\n\t"
    "ds_read_b128 v[108:111], %[da] offset:2048\n\t"
    "ds_read_b128 v[112:115], %[ea] offset:4096\n\t"
    "ds_read_b128 v[116:119], %[ea] offset:6144\n\t"
    "s_branch 2f\n\t"
    ".p2align 6\n\t"
    "1:\n\t"
    "s_cmp_eq_u32 %[la], 3\n\t"
    "s_waitcnt vmcnt(6)\n\t"
    "s_cbranch_scc1 21f\n\t"
    "s_waitcnt vmcnt(2)\n\t"
    "21:\n\t"
    "s_barrier\n\t"
    "2:\n\t"
    "s_waitcnt lgkmcnt(4)\n\t"
    "v_mfma_f32_16x16x32_bf16 v[64:67], v[128:131], v[96:99], 0\n\t"
    "ds_read_b128 v[120:123], %[da] offset:4096\n\t"
    "s_add_u32 m0, %[l1], 0\n\t"
    "ds_write_b128 %[rw], v[80:83] offset:0\n\t"
    "v_mfma_f32_16x16x32_bf16 v[68:71], v[132:135], v[96:99], 0\n\t"
    "ds_read_b128 v[124:127], %[da] offset:6144\n\t"
    "global_load_lds_dwordx4 %[av0], s[36:37] sc1\n\t"
    "ds_write_b128 %[rw], v[84:87] offset:1024\n\t"
    "v_mfma_f32_16x16x32_bf16 v[72:75], v[128:131], v[100:103], 0\n\t"
    "ds_write_b128 %[rw], v[88:91] offset:2048\n\t"
    "v_mfma_f32_16x16x32_bf16 v[76:79], v[132:135], v[100:103], 0\n\t"
    "ds_write_b128 %[rw], v[92:95] offset:3072\n\t"
    "s_waitcnt lgkmcnt(8)\n\t"
    "v_mfma_f32_16x16x32_bf16 v[64:67], v[136:139], v[104:107], v[64:67]\n\t"
    "ds_read_b128 v[96:99], %[ea] offset:8192\n\t"
    "s_add_u32 m0, %[l1], 4096\n\t"
    "v_mfma_f32_16x16x32_bf16 v[68:71], v[140:143], v[104:107], v[68:71]\n\t"
    "ds_read_b128 v[100:103], %[ea] offset:10240\n\t"
    "global_load_lds_dwordx4 %[av2], s[36:37] sc1\n\t"
    "v_mfma_f32_16x16x32_bf16 v[72:75], v[136:139], v[108:111], v[72:75]\n\t"
    "v_mfma_f32_16x16x32_bf16 v[76:79], v[140:143], v[108:111], v[76:79]\n\t"
    "s_waitcnt lgkmcnt(8)\n\t"
    "v_mfma_f32_16x16x32_bf16 v[64:67], v[144:147], v[112:115], v[64:67]\n\t"
    "ds_read_b128 v[104:107], %[da] offset:8192\n\t"
    "s_add_u32 m0, %[l1], 8192\n\t"
    "v_mfma_f32_16x16x32_bf16 v[68:71], v[148:151], v[112:115], v[68:71]\n\t"
    "ds_read_b128 v[108:111], %[da] offset:10240\n\t"
    "global_load_lds_dwordx4 %[av4], s[36:37] sc1\n\t"
    "v_mfma_f32_16x16x32_bf16 v[72:75], v[144:147], v[116:119], v[72:75]\n\t"
    "v_mfma_f32_16x16x32_bf16 v[76:79], v[148:151], v[116:119], v[76:79]\n\t"
    "s_waitcnt lgkmcnt(7)\n\t"
    "v_mfma_f32_16x16x32_bf16 v[64:67], v[152:155], v[120:123], v[64:67]\n\t"
    "ds_read_b128 v[112:115], %[ea] offset:12288\n\t"
    "s_add_u32 m0, %[l1], 12288\n\t"
    "v_mfma_f32_16x16x32_bf16 v[68:71], v[156:159], v[120:123], v[68:71]\n\t"
    "ds_read_b128 v[116:119], %[ea] offset:14336\n\t"
    "global_load_lds_dwordx4 %[av6], s[36:37] sc1\n\t"
    "v_mfma_f32_16x16x32_bf16 v[72:75], v[152:155], v[124:127], v[72:75]\n\t"
    "v_mfma_f32_16x16x32_bf16 v[76:79], v[156:159], v[124:127], v[76:79]\n\t"
    "s_waitcnt lgkmcnt(4)\n\t"
    "v_mfma_f32_16x16x32_bf16 v[64:67], v[160:163], v[96:99], v[64:67]\n\t"
    "ds_read_b128 v[120:123], %[da] offset:12288\n\t"
    "v_mfma_f32_16x16x32_bf16 v[68:71], v[164:167], v[96:99], v[68:71]\n\t"
    "ds_read_b128 v[124:127], %[da] offset:14336\n\t"
    "v_mfma_f32_16x16x32_bf16 v[72:75], v[160:163], v[100:103], v[72:75]\n\t"
    "v_mfma_f32_16x16x32_bf16 v[76:79], v[164:167], v[100:103], v[76:79]\n\t"
    "s_waitcnt lgkmcnt(4)\n\t"
    "v_mfma_f32_16x16x32_bf16 v[64:67], v[168:171], v[104:107], v[64:67]\n\t"
    "ds_read_b128 v[96:99], %[ea] offset:16384\n\t"
    "v_mfma_f32_16x16x32_bf16 v[68:71], v[172:175], v[104:107], v[68:71]\n\t"
    "ds_read_b128 v[100:103], %[ea] offset:18432\n\t"
    "v_mfma_f32_16x16x32_bf16 v[72:75], v[168:171], v[108:111], v[72:75]\n\t"
    "v_mfma_f32_16x16x32_bf16 v[76:79], v[172:175], v[108:111], v[76:79]\n\t"
    "s_waitcnt lgkmcnt(4)\n\t"
    "v_mfma_f32_16x16x32_bf16 v[64:67], v[176:179], v[112:115], v[64:67]\n\t"
    "ds_read_b128 v[104:107], %[da] offset:16384\n\t"
    "v_mfma_f32_16x16x32_bf16 v[68:71], v[180:183], v[112:115], v[68:71]\n\t"
    "ds_read_b128 v[108:111], %[da] offset:18432\n\t"
    "v_mfma_f32_16x16x32_bf16 v[72:75], v[176:179], v[116:119], v[72:75]\n\t"
    "v_mfma_f32_16x16x32_bf16 v[76:79], v[180:183], v[116:119], v[76:79]\n\t"
    "s_waitcnt lgkmcnt(4)\n\t"
    "v_mfma_f32_16x16x32_bf16 v[64:67], v[184:187], v[120:123], v[64:67]\n\t"
    "ds_read_b128 v[112:115], %[ea] offset:20480\n\t"
    "v_mfma_f32_16x16x32_bf16 v[68:71], v[188:191], v[120:123], v[68:71]\n\t"
    "ds_read_b128 v[116:119], %[ea] offset:22528\n\t"
    "v_mfma_f32_16x16x32_bf16 v[72:75], v[184:187], v[124:127], v[72:75]\n\t"
    "v_mfma_f32_16x16x32_bf16 v[76:79], v[188:191], v[124:127], v[76:79]\n\t"
    "s_cmp_eq_u32 %[la], 3\n\t"
    "s_waitcnt vmcnt(6)\n\t"
    "s_cbranch_scc1 22f\n\t"
    "s_waitcnt vmcnt(2)\n\t"
    "22:\n\t"
    "s_barrier\n\t"
    "s_waitcnt lgkmcnt(4)\n\t"
    "v_mfma_f32_16x16x32_bf16 v[64:67], v[192:195], v[96:99], v[64:67]\n\t"
    "ds_read_b128 v[120:123], %[da] offset:20480\n\t"
    "s_add_u32 m0, %[l0], 0\n\t"
    "v_mfma_f32_16x16x32_bf16 v[68:71], v[196:199], v[96:99], v[68:71]\n\t"
    "ds_read_b128 v[124:127], %[da] offset:22528\n\t"
    "global_load_lds_dwordx4 %[av0], s[38:39] sc1\n\t"
    "v_mfma_f32_16x16x32_bf16 v[72:75], v[192:195], v[100:103], v[72:75]\n\t"
    "v_mfma_f32_16x16x32_bf16 v[76:79], v[196:199], v[100:103], v[76:79]\n\t"
    "s_waitcnt lgkmcnt(4)\n\t"
    "v_mfma_f32_16x16x32_bf16 v[64:67], v[200:203], v[104:107], v[64:67]\n\t"
    "ds_read_b128 v[96:99], %[ea] offset:24576\n\t"
    "s_add_u32 m0, %[l0], 4096\n\t"
    "v_mfma_f32_16x16x32_bf16 v[68:71], v[204:207], v[104:107], v[68:71]\n\t"
    "ds_read_b128 v[100:103], %[ea] offset:26624\n\t"
    "global_load_lds_dwordx4 %[av2], s[38:39] sc1\n\t"
    "v_mfma_f32_16x16x32_bf16 v[72:75], v[200:203], v[108:111], v[72:75]\n\t"
    "v_mfma_f32_16x16x32_bf16 v[76:79], v[204:207], v[108:111], v[76:79]\n\t"
    "s_add_u32 s36, s36, %[ab]\n\t"
    "s_addc_u32 s37, s37, 0\n\t"
    "s_waitcnt lgkmcnt(4)\n\t"
    "v_mfma_f32_16x16x32_bf16 v[64:67], v[208:211], v[112:115], v[64:67]\n\t"
    "ds_read_b128 v[104:107], %[da] offset:24576\n\t"
    "s_add_u32 m0, %[l0], 8192\n\t"
    "v_mfma_f32_16x16x32_bf16 v[68:71], v[212:215], v[112:115], v[68:71]\n\t"
    "ds_read_b128 v[108:111], %[da] offset:26624\n\t"
    "global_load_lds_dwordx4 %[av4], s[38:39] sc1\n\t"
    "v_mfma_f32_16x16x32_bf16 v[72:75], v[208:211], v[116:119], v[72:75]\n\t"
    "ds_read_b32 v39, %[sa]\n\t"
    "v_mfma_f32_16x16x32_bf16 v[76:79], v[212:215], v[116:119], v[76:79]\n\t"
    "s_xor_b32 %[l1], %[l1], 0x10000\n\t"
    "s_waitcnt lgkmcnt(5)\n\t"
    "v_mfma_f32_16x16x32_bf16 v[64:67], v[216:219], v[120:123], v[64:67]\n\t"
    "ds_read_b128 v[112:115], %[ea] offset:28672\n\t"
    "s_add_u32 m0, %[l0], 12288\n\t"
    "v_mfma_f32_16x16x32_bf16 v[68:71], v[220:223], v[120:123], v[68:71]\n\t"
    "ds_read_b128 v[116:119], %[ea] offset:30720\n\t"
    "global_load_lds_dwordx4 %[av6], s[38:39] sc1\n\t"
    "v_mfma_f32_16x16x32_bf16 v[72:75], v[216:219], v[124:127], v[72:75]\n\t"
    "v_xor_b32 %[ea], 0x10000, %[ea]\n\t"
    "v_mfma_f32_16x16x32_bf16 v[76:79], v[220:223], v[124:127], v[76:79]\n\t"
    "s_waitcnt lgkmcnt(5)\n\t"
    "v_mfma_f32_16x16x32_bf16 v[64:67], v[224:227], v[96:99], v[64:67]\n\t"
    "ds_read_b128 v[120:123], %[da] offset:28672\n\t"
    "v_mfma_f32_16x16x32_bf16 v[68:71], v[228:231], v[96:99], v[68:71]\n\t"
    "ds_read_b128 v[124:127], %[da] offset:30720\n\t"
    "v_mfma_f32_16x16x32_bf16 v[72:75], v[224:227], v[100:103], v[72:75]\n\t"
    "v_xor_b32 %[da], 0x10000, %[da]\n\t"
    "v_mfma_f32_16x16x32_bf16 v[76:79], v[228:231], v[100:103], v[76:79]\n\t"
    "s_waitcnt lgkmcnt(5)\n\t"
    "v_mfma_f32_16x16x32_bf16 v[64:67], v[232:235], v[104:107], v[64:67]\n\t"
    "ds_read_b128 v[96:99], %[ea] offset:0\n\t"
    "v_mfma_f32_16x16x32_bf16 v[68:71], v[236:239], v[104:107], v[68:71]\n\t"
    "ds_read_b128 v[100:103], %[ea] offset:2048\n\t"
    "v_mfma_f32_16x16x32_bf16 v[72:75], v[232:235], v[108:111], v[72:75]\n\t"
    "v_mfma_f32_16x16x32_bf16 v[76:79], v[236:239], v[108:111], v[76:79]\n\t"
    "s_waitcnt lgkmcnt(4)\n\t"
    "v_mfma_f32_16x16x32_bf16 v[64:67], v[240:243], v[112:115], v[64:67]\n\t"
    "ds_read_b128 v[104:107], %[da] offset:0\n\t"
    "v_mad_u32_u24 v39, v39, %[mu], %[ad]\n\t"
    "v_mfma_f32_16x16x32_bf16 v[68:71], v[244:247], v[112:115], v[68:71]\n\t"
    "ds_read_b128 v[108:111], %[da] offset:2048\n\t"
    "v_cmp_ge_i32 vcc, %[lf], v39\n\t"
    "s_cmp_eq_u64 vcc, 0\n\t"
    "s_addc_u32 %[lf], %[lf], 0\n\t"
    "v_mfma_f32_16x16x32_bf16 v[72:75], v[240:243], v[116:119], v[72:75]\n\t"
    "v_mfma_f32_16x16x32_bf16 v[76:79], v[244:247], v[116:119], v[76:79]\n\t"
    "v_cmp_ge_i32 vcc, %[lf], v39\n\t"
    "s_cmp_eq_u64 vcc, 0\n\t"
    "s_addc_u32 %[lf], %[lf], 0\n\t"
    "s_waitcnt lgkmcnt(4)\n\t"
    "v_mfma_f32_16x16x32_bf16 v[80:83], v[248:251], v[120:123], v[64:67]\n\t"
    "ds_read_b128 v[112:115], %[ea] offset:4096\n\t"
    "s_min_i32 %[lf], %[lf], %[cp]\n\t"
    "s_add_u32 %[b], %[b], 1\n\t"
    "v_mfma_f32_16x16x32_bf16 v[84:87], v[252:255], v[120:123], v[68:71]\n\t"
    "ds_read_b128 v[116:119], %[ea] offset:6144\n\t"
    "v_mfma_f32_16x16x32_bf16 v[88:91], v[248:251], v[124:127], v[72:75]\n\t"
    "v_mfma_f32_16x16x32_bf16 v[92:95], v[252:255], v[124:127], v[76:79]\n\t"
    "s_add_u32 s38, s38, %[ab]\n\t"
    "s_addc_u32 s39, s39, 0\n\t"
    "s_xor_b32 %[l0], %[l0], 0x10000\n\t"
    "s_sub_u32 m0, %[lf], %[b]\n\t"
    "s_sub_u32 m0, m0, 1\n\t"
    "s_cmp_lt_u32 m0, %[up]\n\t"
    "s_cbranch_scc1 1b\n\t"
    "s_waitcnt lgkmcnt(0)\n\t"
    "s_nop 4"
    : "=&{v[80:95]}"(pd), [ea] "+&v"(ea), [da] "+&v"(da), [b] "+&s"(b), [lf] "+&s"(lf), "+{s[36:37]}"(p1), "+{s[38:39]}"(p0), [sa] "+&v"(sa), [l1] "+&s"(l1), [l0] "+&s"(l0)
    : [rw] "v"(rw), [ab] "s"(ab), [cp] "s"(cp), [mu] "s"(mu), [ad] "v"(ad), [up] "s"(up), "{v[128:159]}"(W[0]), "{v[160:191]}"(W[1]), "{v[192:223]}"(W[2]), "{v[224:255]}"(W[3]), [av0] "v"(av[0]), [av1] "v"(av[1]), [av2] "v"(av[2]), [av3] "v"(av[3]), [av4] "v"(av[4]), [av5] "v"(av[5]), [av6] "v"(av[6]), [av7] "v"(av[7]), [la] "s"(la)
    : "v39", "v64", "v65", "v66", "v67", "v68", "v69", "v70", "v71", "v72", "v73", "v74", "v75", "v76", "v77", "v78", "v79", "v96", "v97", "v98", "v99", "v100", "v101", "v102", "v103", "v104", "v105", "v106", "v107", "v108", "v109", "v110", "v111", "v112", "v113", "v114", "v115", "v116", "v117", "v118", "v119", "v120", "v121", "v122", "v123", "v124", "v125", "v126", "v127", "vcc", "scc", "m0", "memory");
}
__device__ __forceinline__ void fast_st0(const u32x32 (&W)[4], u32x16& pd, unsigned ea, unsigned da, int& b, int& lf, int cp, unsigned up, unsigned sa, unsigned mu, unsigned ad,
    unsigned rw, unsigned ab, const char* p1, const char* p0, unsigned co0, unsigned co1, int po, int pf, unsigned w0, unsigned sl, unsigned dv, const char* db, const char* pg) {
  unsigned st;
  asm volatile("s_barrier\n\t"
    "ds_read_b128 v[96:99], %[ea] offset:0\n\t"
    "ds_read_b128 v[100:103], %[ea] offset:2048\n\t"
    "ds_read_b128 v[104:107], %[da] offset:0\n\t"
    "ds_read_b128 v[108:111], %[da] offset:2048\n\t"
    "ds_read_b128 v[112:115], %[ea] offset:4096\n\t"
    "ds_read_b128 v[116:119], %[ea] offset:6144\n\t"
    "s_branch 2f\n\t"
    ".p2align 6\n\t"
    "1:\n\t"
    "s_barrier\n\t"
    "2:\n\t"
    "s_waitcnt lgkmcnt(4)\n\t"
    "v_mfma_f32_16x16x32_bf16 v[64:67], v[128:131], v[96:99], 0\n\t"
    "ds_read_b128 v[120:123], %[da] offset:4096\n\t"
    "v_mfma_f32_16x16x32_bf16 v[68:71], v[132:135], v[96:99], 0\n\t"
    "ds_read_b128 v[124:127], %[da] offset:6144\n\t"
    "v_mfma_f32_16x16x32_bf16 v[72:75], v[128:131], v[100:103], 0\n\t"
    "v_mfma_f32_16x16x32_bf16 v[76:79], v[132:135], v[100:103], 0\n\t"
    "s_waitcnt lgkmcnt(4)\n\t"
    "v_mfma_f32_16x16x32_bf16 v[64:67], v[136:139], v[104:107], v[64:67]\n\t"
    "ds_read_b128 v[96:99], %[ea] offset:8192\n\t"
    "v_mfma_f32_16x16x32_bf16 v[68:71], v[140:143], v[104:107], v[68:71]\n\t"
    "ds_read_b128 v[100:103], %[ea] offset:10240\n\t"
    "v_mfma_f32_16x16x32_bf16 v[72:75], v[136:139], v[108:111], v[72:75]\n\t"
    "v_mfma_f32_16x16x32_bf16 v[76:79], v[140:143], v[108:111], v[76:79]\n\t"
    "s_cmp_eq_u32 %[w0], 1\n\t"
    "s_cbranch_scc1 7f\n\t"
    "8:\n\t"
    "s_waitcnt lgkmcnt(4)\n\t"
    "v_mfma_f32_16x16x32_bf16 v[64:67], v[144:147], v[112:115], v[64:67]\n\t"
    "ds_read_b128 v[104:107], %[da] offset:8192\n\t"
    "v_mfma_f32_16x16x32_bf16 v[68:71], v[148:151], v[112:115], v[68:71]\n\t"
    "ds_read_b128 v[108:111], %[da] offset:10240\n\t"
    "v_mfma_f32_16x16x32_bf16 v[72:75], v[144:147], v[116:119], v[72:75]\n\t"
    "v_mfma_f32_16x16x32_bf16 v[76:79], v[148:151], v[116:119], v[76:79]\n\t"
    "s_waitcnt lgkmcnt(4)\n\t"
    "v_mfma_f32_16x16x32_bf16 v[64:67], v[152:155], v[120:123], v[64:67]\n\t"
    "ds_read_b128 v[112:115], %[ea] offset:12288\n\t"
    "v_mfma_f32_16x16x32_bf16 v[68:71], v[156:159], v[120:123], v[68:71]\n\t"
    "ds_read_b128 v[116:119], %[ea] offset:14336\n\t"
    "v_mfma_f32_16x16x32_bf16 v[72:75], v[152:155], v[124:127], v[72:75]\n\t"
    "v_mfma_f32_16x16x32_bf16 v[76:79], v[156:159], v[124:127], v[76:79]\n\t"
    "s_waitcnt lgkmcnt(4)\n\t"
    "v_mfma_f32_16x16x32_bf16 v[64:67], v[160:163], v[96:99], v[64:67]\n\t"
    "ds_read_b128 v[120:123], %[da] offset:12288\n\t"
    "v_mfma_f32_16x16x32_bf16 v[68:71], v[164:167], v[96:99], v[68:71]\n\t"
    "ds_read_b128 v[124:127], %[da] offset:14336\n\t"
    "v_mfma_f32_16x16x32_bf16 v[72:75], v[160:163], v[100:103], v[72:75]\n\t"
    "v_mfma_f32_16x16x32_bf16 v[76:79], v[164:167], v[100:103], v[76:79]\n\t"
    "s_waitcnt lgkmcnt(4)\n\t"
    "v_mfma_f32_16x16x32_bf16 v[64:67], v[168:171], v[104:107], v[64:67]\n\t"
    "ds_read_b128 v[96:99], %[ea] offset:16384\n\t"
    "v_mfma_f32_16x16x32_bf16 v[68:71], v[172:175], v[104:107], v[68:71]\n\t"
    "ds_read_b128 v[100:103], %[ea] offset:18432\n\t"
    "v_mfma_f32_16x16x32_bf16 v[72:75], v[168:171], v[108:111], v[72:75]\n\t"
    "v_mfma_f32_16x16x32_bf16 v[76:79], v[172:175], v[108:111], v[76:79]\n\t"
    "s_waitcnt lgkmcnt(4)\n\t"
    "v_mfma_f32_16x16x32_bf16 v[64:67], v[176:179], v[112:115], v[64:67]\n\t"
    "ds_read_b128 v[104:107], %[da] offset:16384\n\t"
    "v_mfma_f32_16x16x32_bf16 v[68:71], v[180:183], v[112:115], v[68:71]\n\t"
    "ds_read_b128 v[108:111], %[da] offset:18432\n\t"
    "v_mfma_f32_16x16x32_bf16 v[72:75], v[176:179], v[116:119], v[72:75]\n\t"
    "v_mfma_f32_16x16x32_bf16 v[76:79], v[180:183], v[116:119], v[76:79]\n\t"
    "s_waitcnt lgkmcnt(4)\n\t"
    "v_mfma_f32_16x16x32_bf16 v[64:67], v[184:187], v[120:123], v[64:67]\n\t"
    "ds_read_b128 v[112:115], %[ea] offset:20480\n\t"
    "v_mfma_f32_16x16x32_bf16 v[68:71], v[188:191], v[120:123], v[68:71]\n\t"
    "ds_read_b128 v[116:119], %[ea] offset:22528\n\t"
    "v_mfma_f32_16x16x32_bf16 v[72:75], v[184:187], v[124:127], v[72:75]\n\t"
    "v_mfma_f32_16x16x32_bf16 v[76:79], v[188:191], v[124:127], v[76:79]\n\t"
    "s_waitcnt vmcnt(0)\n\t"
    "s_barrier\n\t"
    "s_waitcnt lgkmcnt(4)\n\t"
    "v_mfma_f32_16x16x32_bf16 v[64:67], v[192:195], v[96:99], v[64:67]\n\t"
    "ds_read_b128 v[120:123], %[da] offset:20480\n\t"
    "v_mfma_f32_16x16x32_bf16 v[68:71], v[196:199], v[96:99], v[68:71]\n\t"
    "ds_read_b128 v[124:127], %[da] offset:22528\n\t"
    "v_mfma_f32_16x16x32_bf16 v[72:75], v[192:195], v[100:103], v[72:75]\n\t"
    "ds_read_b128 v[48:51], %[rw] offset:0\n\t"
    "v_mfma_f32_16x16x32_bf16 v[76:79], v[196:199], v[100:103], v[76:79]\n\t"
    "ds_read_b128 v[52:55], %[rw] offset:1024\n\t"
    "s_cmp_eq_u32 %[w0], 0\n\t"
    "s_cbranch_scc1 5f\n\t"
    "6:\n\t"
    "s_waitcnt lgkmcnt(6)\n\t"
    "v_mfma_f32_16x16x32_bf16 v[64:67], v[200:203], v[104:107], v[64:67]\n\t"
    "ds_read_b128 v[96:99], %[ea] offset:24576\n\t"
    "v_mfma_f32_16x16x32_bf16 v[68:71], v[204:207], v[104:107], v[68:71]\n\t"
    "ds_read_b128 v[100:103], %[ea] offset:26624\n\t"
    "v_mfma_f32_16x16x32_bf16 v[72:75], v[200:203], v[108:111], v[72:75]\n\t"
    "ds_read_b128 v[56:59], %[rw] offset:2048\n\t"
    "v_mfma_f32_16x16x32_bf16 v[76:79], v[204:207], v[108:111], v[76:79]\n\t"
    "ds_read_b128 v[60:63], %[rw] offset:3072\n\t"
    "s_waitcnt lgkmcnt(0)\n\t"
    "v_mfma_f32_16x16x32_bf16 v[64:67], v[208:211], v[112:115], v[64:67]\n\t"
    "ds_read_b128 v[104:107], %[da] offset:24576\n\t"
    "v_add_f32 v48, v48, v80\n\t"
    "v_add_f32 v49, v49, v81\n\t"
    "v_mfma_f32_16x16x32_bf16 v[68:71], v[212:215], v[112:115], v[68:71]\n\t"
    "ds_read_b128 v[108:111], %[da] offset:26624\n\t"
    "v_add_f32 v50, v50, v82\n\t"
    "v_add_f32 v51, v51, v83\n\t"
    "v_mfma_f32_16x16x32_bf16 v[72:75], v[208:211], v[116:119], v[72:75]\n\t"
    "ds_read_b32 v39, %[sa]\n\t"
    "v_add_f32 v52, v52, v84\n\t"
    "v_mfma_f32_16x16x32_bf16 v[76:79], v[212:215], v[116:119], v[76:79]\n\t"
    "v_add_f32 v53, v53, v85\n\t"
    "v_add_f32 v54, v54, v86\n\t"
    "v_mfma_f32_16x16x32_bf16 v[64:67], v[216:219], v[120:123], v[64:67]\n\t"
    "ds_read_b128 v[112:115], %[ea] offset:28672\n\t"
    "v_add_f32 v55, v55, v87\n\t"
    "v_add_f32 v56, v56, v88\n\t"
    "v_mfma_f32_16x16x32_bf16 v[68:71], v[220:223], v[120:123], v[68:71]\n\t"
    "ds_read_b128 v[116:119], %[ea] offset:30720\n\t"
    "v_add_f32 v57, v57, v89\n\t"
    "v_mfma_f32_16x16x32_bf16 v[72:75], v[216:219], v[124:127], v[72:75]\n\t"
    "v_xor_b32 %[ea], 0x10000, %[ea]\n\t"
    "v_add_f32 v58, v58, v90\n\t"
    "v_add_f32 v59, v59, v91\n\t"
    "v_mfma_f32_16x16x32_bf16 v[76:79], v[220:223], v[124:127], v[76:79]\n\t"
    "v_add_f32 v60, v60, v92\n\t"
    "v_mfma_f32_16x16x32_bf16 v[64:67], v[224:227], v[96:99], v[64:67]\n\t"
    "ds_read_b128 v[120:123], %[da] offset:28672\n\t"
    "v_add_f32 v61, v61, v93\n\t"
    "v_add_f32 v62, v62, v94\n\t"
    "v_mfma_f32_16x16x32_bf16 v[68:71], v[228:231], v[96:99], v[68:71]\n\t"
    "ds_read_b128 v[124:127], %[da] offset:30720\n\t"
    "v_add_f32 v63, v63, v95\n\t"
    "v_cvt_pk_bf16_f32 v40, v48, v49\n\t"
    "v_mfma_f32_16x16x32_bf16 v[72:75], v[224:227], v[100:103], v[72:75]\n\t"
    "v_xor_b32 %[da], 0x10000, %[da]\n\t"
    "v_cvt_pk_bf16_f32 v41, v50, v51\n\t"
    "v_mfma_f32_16x16x32_bf16 v[76:79], v[228:231], v[100:103], v[76:79]\n\t"
    "v_cvt_pk_bf16_f32 v42, v52, v53\n\t"
    "v_cvt_pk_bf16_f32 v43, v54, v55\n\t"
    "s_waitcnt lgkmcnt(5)\n\t"
    "v_mfma_f32_16x16x32_bf16 v[64:67], v[232:235], v[104:107], v[64:67]\n\t"
    "ds_read_b128 v[96:99], %[ea] offset:0\n\t"
    "global_store_dwordx4 %[co0], v[40:43], s[36:37] sc1\n\t"
    "v_cvt_pk_bf16_f32 v44, v56, v57\n\t"
    "v_mfma_f32_16x16x32_bf16 v[68:71], v[236:239], v[104:107], v[68:71]\n\t"
    "ds_read_b128 v[100:103], %[ea] offset:2048\n\t"
    "v_cvt_pk_bf16_f32 v45, v58, v59\n\t"
    "v_mfma_f32_16x16x32_bf16 v[72:75], v[232:235], v[108:111], v[72:75]\n\t"
    "v_cvt_pk_bf16_f32 v46, v60, v61\n\t"
    "v_cvt_pk_bf16_f32 v47, v62, v63\n\t"
    "v_mfma_f32_16x16x32_bf16 v[76:79], v[236:239], v[108:111], v[76:79]\n\t"
    "global_store_dwordx4 %[co1], v[44:47], s[36:37] sc1\n\t"
    "s_waitcnt lgkmcnt(4)\n\t"
    "v_mfma_f32_16x16x32_bf16 v[64:67], v[240:243], v[112:115], v[64:67]\n\t"
    "ds_read_b128 v[104:107], %[da] offset:0\n\t"
    "v_mad_u32_u24 v39, v39, %[mu], %[ad]\n\t"
    "v_mfma_f32_16x16x32_bf16 v[68:71], v[244:247], v[112:115], v[68:71]\n\t"
    "ds_read_b128 v[108:111], %[da] offset:2048\n\t"
    "v_cmp_ge_i32 vcc, %[lf], v39\n\t"
    "s_cmp_eq_u64 vcc, 0\n\t"
    "s_addc_u32 %[lf], %[lf], 0\n\t"
    "v_mfma_f32_16x16x32_bf16 v[72:75], v[240:243], v[116:119], v[72:75]\n\t"
    "v_mfma_f32_16x16x32_bf16 v[76:79], v[244:247], v[116:119], v[76:79]\n\t"
    "v_cmp_ge_i32 vcc, %[lf], v39\n\t"
    "s_cmp_eq_u64 vcc, 0\n\t"
    "s_addc_u32 %[lf], %[lf], 0\n\t"
    "s_waitcnt lgkmcnt(4)\n\t"
    "v_mfma_f32_16x16x32_bf16 v[80:83], v[248:251], v[120:123], v[64:67]\n\t"
    "ds_read_b128 v[112:115], %[ea] offset:4096\n\t"
    "s_min_i32 %[lf], %[lf], %[cp]\n\t"
    "s_add_u32 %[b], %[b], 1\n\t"
    "v_mfma_f32_16x16x32_bf16 v[84:87], v[252:255], v[120:123], v[68:71]\n\t"
    "ds_read_b128 v[116:119], %[ea] offset:6144\n\t"
    "v_mfma_f32_16x16x32_bf16 v[88:91], v[248:251], v[124:127], v[72:75]\n\t"
    "v_mfma_f32_16x16x32_bf16 v[92:95], v[252:255], v[124:127], v[76:79]\n\t"
    "s_mov_b64 s[36:37], s[38:39]\n\t"
    "s_add_u32 s38, s38, %[ab]\n\t"
    "s_addc_u32 s39, s39, 0\n\t"
    "s_sub_u32 m0, %[lf], %[b]\n\t"
    "s_sub_u32 m0, m0, 1\n\t"
    "s_cmp_lt_u32 m0, %[up]\n\t"
    "s_cbranch_scc1 1b\n\t"
    "s_waitcnt lgkmcnt(0)\n\t"
    "s_nop 4\n\t"
    "s_branch 9f\n\t"
    "5:\n\t"
    "s_add_u32 %[st], %[b], %[po]\n\t"
    "s_max_i32 %[st], %[st], %[p0]\n\t"
    "v_mov_b32 v39, %[st]\n\t"
    "v_mov_b32 v38, 0\n\t"
    "s_mov_b64 exec, 1\n\t"
    "global_store_dword v38, v39, %[pg] sc1\n\t"
    "s_mov_b64 exec, -1\n\t"
    "s_mov_b32 %[p0], %[p1]\n\t"
    "s_branch 6b\n\t"
    "7:\n\t"
    "s_cmp_ge_i32 %[lf], %[cp]\n\t"
    "s_cbranch_scc1 8b\n\t"
    "s_mov_b32 m0, %[sl]\n\t"
    "s_nop 0\n\t"
    "global_load_lds_dword %[dv], %[db] sc1\n\t"
    "s_branch 8b\n\t"
    "9:"
    : "=&{v[80:95]}"(pd), [ea] "+&v"(ea), [da] "+&v"(da), [b] "+&s"(b), [lf] "+&s"(lf), "+{s[36:37]}"(p1), "+{s[38:39]}"(p0), [sa] "+&v"(sa), [st] "=&s"(st), [sl] "+&s"(sl), [p0] "+&s"(pf)
    : [rw] "v"(rw), [ab] "s"(ab), [cp] "s"(cp), [mu] "s"(mu), [ad] "v"(ad), [up] "s"(up), "{v[128:159]}"(W[0]), "{v[160:191]}"(W[1]), "{v[192:223]}"(W[2]), "{v[224:255]}"(W[3]), [co0] "v"(co0), [w0] "s"(w0), [dv] "v"(dv), [db] "s"(db), [pg] "s"(pg), [po] "s"(po - 1), [p1] "s"(b + po), [co1] "v"(co1)
    : "v38", "v39", "v40", "v41", "v42", "v43", "v44", "v45", "v46", "v47", "v48", "v49", "v50", "v51", "v52", "v53", "v54", "v55", "v56", "v57", "v58", "v59", "v60", "v61", "v62", "v63", "v64", "v65", "v66", "v67", "v68", "v69", "v70", "v71", "v72", "v73", "v74", "v75", "v76", "v77", "v78", "v79", "v96", "v97", "v98", "v99", "v100", "v101", "v102", "v103", "v104", "v105", "v106", "v107", "v108", "v109", "v110", "v111", "v112", "v113", "v114", "v115", "v116", "v117", "v118", "v119", "v120", "v121", "v122", "v123", "v124", "v125", "v126", "v127", "vcc", "scc", "m0", "memory");
}
__device__ __forceinline__ void fast_st1(const u32x32 (&W)[4], u32x16& pd, unsigned ea, unsigned da, int& b, int& lf, int cp, unsigned up, unsigned sa, unsigned mu, unsigned ad,
    unsigned rw, unsigned ab, const char* p1, const char* p0, unsigned co0, unsigned co1, int po, int pf, unsigned w0, unsigned sl, unsigned dv, const char* db, const char* pg) {
  unsigned st;
  asm volatile("s_barrier\n\t"
    "ds_read_b128 v[96:99], %[ea] offset:0\n\t"
    "ds_read_b128 v[100:103], %[ea] offset:2048\n\t"
    "ds_read_b128 v[104:107], %[da] offset:0\n\t"
    "ds_read_b128 v[108:111], %[da] offset:2048\n\t"
    "ds_read_b128 v[112:115], %[ea] offset:4096\n\t"
    "ds_read_b128 v[116:119], %[ea] offset:6144\n\t"
    "s_branch 2f\n\t"
    ".p2align 6\n\t"
    "1:\n\t"
    "s_barrier\n\t"
    "2:\n\t"
    "s_waitcnt lgkmcnt(4)\n\t"
    "v_mfma_f32_16x16x32_bf16 v[64:67], v[128:131], v[96:99], 0\n\t"
    "ds_read_b128 v[120:123], %[da] offset:4096\n\t"
    "v_mfma_f32_16x16x32_bf16 v[68:71], v[132:135], v[96:99], 0\n\t"
    "ds_read_b128 v[124:127], %[da] offset:6144\n\t"
    "v_mfma_f32_16x16x32_bf16 v[72:75], v[128:131], v[100:103], 0\n\t"
    "v_mfma_f32_16x16x32_bf16 v[76:79], v[132:135], v[100:103], 0\n\t"
    "s_waitcnt lgkmcnt(4)\n\t"
    "v_mfma_f32_16x16x32_bf16 v[64:67], v[136:139], v[104:107], v[64:67]\n\t"
    "ds_read_b128 v[96:99], %[ea] offset:8192\n\t"
    "v_mfma_f32_16x16x32_bf16 v[68:71], v[140:143], v[104:107], v[68:71]\n\t"
    "ds_read_b128 v[100:103], %[ea] offset:10240\n\t"
    "v_mfma_f32_16x16x32_bf16 v[72:75], v[136:139], v[108:111], v[72:75]\n\t"
    "v_mfma_f32_16x16x32_bf16 v[76:79], v[140:143], v[108:111], v[76:79]\n\t"
    "s_cmp_eq_u32 %[w0], 1\n\t"
    "s_cbranch_scc1 7f\n\t"
    "8:\n\t"
    "s_waitcnt lgkmcnt(4)\n\t"
    "v_mfma_f32_16x16x32_bf16 v[64:67], v[144:147], v[112:115], v[64:67]\n\t"
    "ds_read_b128 v[104:107], %[da] offset:8192\n\t"
    "v_mfma_f32_16x16x32_bf16 v[68:71], v[148:151], v[112:115], v[68:71]\n\t"
    "ds_read_b128 v[108:111], %[da] offset:10240\n\t"
    "v_mfma_f32_16x16x32_bf16 v[72:75], v[144:147], v[116:119], v[72:75]\n\t"
    "v_mfma_f32_16x16x32_bf16 v[76:79], v[148:151], v[116:119], v[76:79]\n\t"
    "s_waitcnt lgkmcnt(4)\n\t"
    "v_mfma_f32_16x16x32_bf16 v[64:67], v[152:155], v[120:123], v[64:67]\n\t"
    "ds_read_b128 v[112:115], %[ea] offset:12288\n\t"
    "v_mfma_f32_16x16x32_bf16 v[68:71], v[156:159], v[120:123], v[68:71]\n\t"
    "ds_read_b128 v[116:119], %[ea] offset:14336\n\t"
    "v_mfma_f32_16x16x32_bf16 v[72:75], v[152:155], v[124:127], v[72:75]\n\t"
    "v_mfma_f32_16x16x32_bf16 v[76:79], v[156:159], v[124:127], v[76:79]\n\t"
    "s_waitcnt lgkmcnt(4)\n\t"
    "v_mfma_f32_16x16x32_bf16 v[64:67], v[160:163], v[96:99], v[64:67]\n\t"
    "ds_read_b128 v[120:123], %[da] offset:12288\n\t"
    "v_mfma_f32_16x16x32_bf16 v[68:71], v[164:167], v[96:99], v[68:71]\n\t"
    "ds_read_b128 v[124:127], %[da] offset:14336\n\t"
    "v_mfma_f32_16x16x32_bf16 v[72:75], v[160:163], v[100:103], v[72:75]\n\t"
    "v_mfma_f32_16x16x32_bf16 v[76:79], v[164:167], v[100:103], v[76:79]\n\t"
    "s_waitcnt lgkmcnt(4)\n\t"
    "v_mfma_f32_16x16x32_bf16 v[64:67], v[168:171], v[104:107], v[64:67]\n\t"
    "ds_read_b128 v[96:99], %[ea] offset:16384\n\t"
    "v_mfma_f32_16x16x32_bf16 v[68:71], v[172:175], v[104:107], v[68:71]\n\t"
    "ds_read_b128 v[100:103], %[ea] offset:18432\n\t"
    "v_mfma_f32_16x16x32_bf16 v[72:75], v[168:171], v[108:111], v[72:75]\n\t"
    "v_mfma_f32_16x16x32_bf16 v[76:79], v[172:175], v[108:111], v[76:79]\n\t"
    "s_waitcnt lgkmcnt(4)\n\t"
    "v_mfma_f32_16x16x32_bf16 v[64:67], v[176:179], v[112:115], v[64:67]\n\t"
    "ds_read_b128 v[104:107], %[da] offset:16384\n\t"
    "v_mfma_f32_16x16x32_bf16 v[68:71], v[180:183], v[112:115], v[68:71]\n\t"
    "ds_read_b128 v[108:111], %[da] offset:18432\n\t"
    "v_mfma_f32_16x16x32_bf16 v[72:75], v[176:179], v[116:119], v[72:75]\n\t"
    "v_mfma_f32_16x16x32_bf16 v[76:79], v[180:183], v[116:119], v[76:79]\n\t"
    "s_waitcnt lgkmcnt(4)\n\t"
    "v_mfma_f32_16x16x32_bf16 v[64:67], v[184:187], v[120:123], v[64:67]\n\t"
    "ds_read_b128 v[112:115], %[ea] offset:20480\n\t"
    "v_mfma_f32_16x16x32_bf16 v[68:71], v[188:191], v[120:123], v[68:71]\n\t"
    "ds_read_b128 v[116:119], %[ea] offset:22528\n\t"
    "v_mfma_f32_16x16x32_bf16 v[72:75], v[184:187], v[124:127], v[72:75]\n\t"
    "v_mfma_f32_16x16x32_bf16 v[76:79], v[188:191], v[124:127], v[76:79]\n\t"
    "s_waitcnt vmcnt(0)\n\t"
    "s_barrier\n\t"
    "s_waitcnt lgkmcnt(4)\n\t"
    "v_mfma_f32_16x16x32_bf16 v[64:67], v[192:195], v[96:99], v[64:67]\n\t"
    "ds_read_b128 v[120:123], %[da] offset:20480\n\t"
    "v_mfma_f32_16x16x32_bf16 v[68:71], v[196:199], v[96:99], v[68:71]\n\t"
    "ds_read_b128 v[124:127], %[da] offset:22528\n\t"
    "v_mfma_f32_16x16x32_bf16 v[72:75], v[192:195], v[100:103], v[72:75]\n\t"
    "ds_read_b128 v[48:51], %[rw] offset:0\n\t"
    "v_mfma_f32_16x16x32_bf16 v[76:79], v[196:199], v[100:103], v[76:79]\n\t"
    "ds_read_b128 v[52:55], %[rw] offset:1024\n\t"
    "s_cmp_eq_u32 %[w0], 0\n\t"
    "s_cbranch_scc1 5f\n\t"
    "6:\n\t"
    "s_waitcnt lgkmcnt(6)\n\t"
    "v_mfma_f32_16x16x32_bf16 v[64:67], v[200:203], v[104:107], v[64:67]\n\t"
    "ds_read_b128 v[96:99], %[ea] offset:24576\n\t"
    "v_mfma_f32_16x16x32_bf16 v[68:71], v[204:207], v[104:107], v[68:71]\n\t"
    "ds_read_b128 v[100:103], %[ea] offset:26624\n\t"
    "v_mfma_f32_16x16x32_bf16 v[72:75], v[200:203], v[108:111], v[72:75]\n\t"
    "ds_read_b128 v[56:59], %[rw] offset:2048\n\t"
    "v_mfma_f32_16x16x32_bf16 v[76:79], v[204:207], v[108:111], v[76:79]\n\t"
    "ds_read_b128 v[60:63], %[rw] offset:3072\n\t"
    "s_waitcnt lgkmcnt(0)\n\t"
    "v_mfma_f32_16x16x32_bf16 v[64:67], v[208:211], v[112:115], v[64:67]\n\t"
    "ds_read_b128 v[104:107], %[da] offset:24576\n\t"
    "v_add_f32 v48, v48, v80\n\t"
    "v_add_f32 v49, v49, v81\n\t"
    "v_add_f32 v50, v50, v82\n\t"
    "v_mfma_f32_16x16x32_bf16 v[68:71], v[212:215], v[112:115], v[68:71]\n\t"
    "ds_read_b128 v[108:111], %[da] offset:26624\n\t"
    "v_add_f32 v51, v51, v83\n\t"
    "v_add_f32 v52, v52, v84\n\t"
    "v_add_f32 v53, v53, v85\n\t"
    "v_mfma_f32_16x16x32_bf16 v[72:75], v[208:211], v[116:119], v[72:75]\n\t"
    "ds_read_b32 v39, %[sa]\n\t"
    "v_add_f32 v54, v54, v86\n\t"
    "v_add_f32 v55, v55, v87\n\t"
    "v_add_f32 v56, v56, v88\n\t"
    "v_mfma_f32_16x16x32_bf16 v[76:79], v[212:215], v[116:119], v[76:79]\n\t"
    "v_add_f32 v57, v57, v89\n\t"
    "v_add_f32 v58, v58, v90\n\t"
    "v_add_f32 v59, v59, v91\n\t"
    "v_mfma_f32_16x16x32_bf16 v[64:67], v[216:219], v[120:123], v[64:67]\n\t"
    "ds_read_b128 v[112:115], %[ea] offset:28672\n\t"
    "v_add_f32 v60, v60, v92\n\t"
    "v_add_f32 v61, v61, v93\n\t"
    "v_add_f32 v62, v62, v94\n\t"
    "v_mfma_f32_16x16x32_bf16 v[68:71], v[220:223], v[120:123], v[68:71]\n\t"
    "ds_read_b128 v[116:119], %[ea] offset:30720\n\t"
    "v_add_f32 v63, v63, v95\n\t"
    "v_mul_f32 v40, %[nl], v48\n\t"
    "v_mul_f32 v41, %[nl], v49\n\t"
    "v_mfma_f32_16x16x32_bf16 v[72:75], v[216:219], v[124:127], v[72:75]\n\t"
    "v_xor_b32 %[ea], 0x10000, %[ea]\n\t"
    "v_mul_f32 v42, %[nl], v50\n\t"
    "v_mul_f32 v43, %[nl], v51\n\t"
    "v_mul_f32 v44, %[nl], v56\n\t"
    "v_mfma_f32_16x16x32_bf16 v[76:79], v[220:223], v[124:127], v[76:79]\n\t"
    "v_mul_f32 v45, %[nl], v57\n\t"
    "v_mul_f32 v46, %[nl], v58\n\t"
    "v_mul_f32 v47, %[nl], v59\n\t"
    "v_mfma_f32_16x16x32_bf16 v[64:67], v[224:227], v[96:99], v[64:67]\n\t"
    "ds_read_b128 v[120:123], %[da] offset:28672\n\t"
    "v_exp_f32 v40, v40\n\t"
    "v_exp_f32 v41, v41\n\t"
    "v_exp_f32 v42, v42\n\t"
    "v_mfma_f32_16x16x32_bf16 v[68:71], v[228:231], v[96:99], v[68:71]\n\t"
    "ds_read_b128 v[124:127], %[da] offset:30720\n\t"
    "v_exp_f32 v43, v43\n\t"
    "v_exp_f32 v44, v44\n\t"
    "v_exp_f32 v45, v45\n\t"
    "v_mfma_f32_16x16x32_bf16 v[72:75], v[224:227], v[100:103], v[72:75]\n\t"
    "v_xor_b32 %[da], 0x10000, %[da]\n\t"
    "v_exp_f32 v46, v46\n\t"
    "v_exp_f32 v47, v47\n\t"
    "v_add_f32 v40, 1.0, v40\n\t"
    "v_mfma_f32_16x16x32_bf16 v[76:79], v[228:231], v[100:103], v[76:79]\n\t"
    "v_add_f32 v41, 1.0, v41\n\t"
    "v_add_f32 v42, 1.0, v42\n\t"
    "v_add_f32 v43, 1.0, v43\n\t"
    "s_waitcnt lgkmcnt(5)\n\t"
    "v_mfma_f32_16x16x32_bf16 v[64:67], v[232:235], v[104:107], v[64:67]\n\t"
    "ds_read_b128 v[96:99], %[ea] offset:0\n\t"
    "v_add_f32 v44, 1.0, v44\n\t"
    "v_add_f32 v45, 1.0, v45\n\t"
    "v_add_f32 v46, 1.0, v46\n\t"
    "v_mfma_f32_16x16x32_bf16 v[68:71], v[236:239], v[104:107], v[68:71]\n\t"
    "ds_read_b128 v[100:103], %[ea] offset:2048\n\t"
    "v_add_f32 v47, 1.0, v47\n\t"
    "v_rcp_f32 v40, v40\n\t"
    "v_rcp_f32 v41, v41\n\t"
    "v_mfma_f32_16x16x32_bf16 v[72:75], v[232:235], v[108:111], v[72:75]\n\t"
    "v_rcp_f32 v42, v42\n\t"
    "v_rcp_f32 v43, v43\n\t"
    "v_rcp_f32 v44, v44\n\t"
    "v_mfma_f32_16x16x32_bf16 v[76:79], v[236:239], v[108:111], v[76:79]\n\t"
    "v_rcp_f32 v45, v45\n\t"
    "v_rcp_f32 v46, v46\n\t"
    "v_rcp_f32 v47, v47\n\t"
    "s_waitcnt lgkmcnt(4)\n\t"
    "v_mfma_f32_16x16x32_bf16 v[64:67], v[240:243], v[112:115], v[64:67]\n\t"
    "ds_read_b128 v[104:107], %[da] offset:0\n\t"
    "v_mad_u32_u24 v39, v39, %[mu], %[ad]\n\t"
    "v_mul_f32 v40, v48, v40\n\t"
    "v_mul_f32 v41, v49, v41\n\t"
    "v_mul_f32 v42, v50, v42\n\t"
    "v_mfma_f32_16x16x32_bf16 v[68:71], v[244:247], v[112:115], v[68:71]\n\t"
    "ds_read_b128 v[108:111], %[da] offset:2048\n\t"
    "v_cmp_ge_i32 vcc, %[lf], v39\n\t"
    "s_cmp_eq_u64 vcc, 0\n\t"
    "s_addc_u32 %[lf], %[lf], 0\n\t"
    "v_mul_f32 v43, v51, v43\n\t"
    "v_mul_f32 v44, v56, v44\n\t"
    "v_mul_f32 v45, v57, v45\n\t"
    "v_mfma_f32_16x16x32_bf16 v[72:75], v[240:243], v[116:119], v[72:75]\n\t"
    "v_mul_f32 v46, v58, v46\n\t"
    "v_mul_f32 v47, v59, v47\n\t"
    "v_mul_f32 v40, v40, v52\n\t"
    "v_mfma_f32_16x16x32_bf16 v[76:79], v[244:247], v[116:119], v[76:79]\n\t"
    "v_cmp_ge_i32 vcc, %[lf], v39\n\t"
    "s_cmp_eq_u64 vcc, 0\n\t"
    "s_addc_u32 %[lf], %[lf], 0\n\t"
    "v_mul_f32 v41, v41, v53\n\t"
    "v_mul_f32 v42, v42, v54\n\t"
    "v_mul_f32 v43, v43, v55\n\t"
    "s_waitcnt lgkmcnt(4)\n\t"
    "v_mfma_f32_16x16x32_bf16 v[80:83], v[248:251], v[120:123], v[64:67]\n\t"
    "ds_read_b128 v[112:115], %[ea] offset:4096\n\t"
    "s_min_i32 %[lf], %[lf], %[cp]\n\t"
    "s_add_u32 %[b], %[b], 1\n\t"
    "v_mul_f32 v44, v44, v60\n\t"
    "v_mul_f32 v45, v45, v61\n\t"
    "v_mul_f32 v46, v46, v62\n\t"
    "v_mfma_f32_16x16x32_bf16 v[84:87], v[252:255], v[120:123], v[68:71]\n\t"
    "ds_read_b128 v[116:119], %[ea] offset:6144\n\t"
    "v_mul_f32 v47, v47, v63\n\t"
    "v_cvt_pk_bf16_f32 v48, v40, v41\n\t"
    "v_cvt_pk_bf16_f32 v49, v42, v43\n\t"
    "v_mfma_f32_16x16x32_bf16 v[88:91], v[248:251], v[124:127], v[72:75]\n\t"
    "v_cvt_pk_bf16_f32 v50, v44, v45\n\t"
    "v_cvt_pk_bf16_f32 v51, v46, v47\n\t"
    "s_nop 1\n\t"
    "v_mfma_f32_16x16x32_bf16 v[92:95], v[252:255], v[124:127], v[76:79]\n\t"
    "v_permlane16_swap_b32 v48, v50\n\t"
    "v_permlane16_swap_b32 v49, v51\n\t"
    "global_store_dwordx4 %[co0], v[48:51], s[36:37] sc1\n\t"
    "s_mov_b64 s[36:37], s[38:39]\n\t"
    "s_add_u32 s38, s38, %[ab]\n\t"
    "s_addc_u32 s39, s39, 0\n\t"
    "s_sub_u32 m0, %[lf], %[b]\n\t"
    "s_sub_u32 m0, m0, 1\n\t"
    "s_cmp_lt_u32 m0, %[up]\n\t"
    "s_cbranch_scc1 1b\n\t"
    "s_waitcnt lgkmcnt(0)\n\t"
    "s_nop 4\n\t"
    "s_branch 9f\n\t"
    "5:\n\t"
    "s_add_u32 %[st], %[b], %[po]\n\t"
    "s_max_i32 %[st], %[st], %[p0]\n\t"
    "v_mov_b32 v39, %[st]\n\t"
    "v_mov_b32 v38, 0\n\t"
    "s_mov_b64 exec, 1\n\t"
    "global_store_dword v38, v39, %[pg] sc1\n\t"
    "s_mov_b64 exec, -1\n\t"
    "s_mov_b32 %[p0], %[p1]\n\t"
    "s_branch 6b\n\t"
    "7:\n\t"
    "s_cmp_ge_i32 %[lf], %[cp]\n\t"
    "s_cbranch_scc1 8b\n\t"
    "s_mov_b32 m0, %[sl]\n\t"
    "s_nop 0\n\t"
    "global_load_lds_dword %[dv], %[db] sc1\n\t"
    "s_branch 8b\n\t"
    "9:"
    : "=&{v[80:95]}"(pd), [ea] "+&v"(ea), [da] "+&v"(da), [b] "+&s"(b), [lf] "+&s"(lf), "+{s[36:37]}"(p1), "+{s[38:39]}"(p0), [sa] "+&v"(sa), [st] "=&s"(st), [sl] "+&s"(sl), [p0] "+&s"(pf)
    : [rw] "v"(rw), [ab] "s"(ab), [cp] "s"(cp), [mu] "s"(mu), [ad] "v"(ad), [up] "s"(up), "{v[128:159]}"(W[0]), "{v[160:191]}"(W[1]), "{v[192:223]}"(W[2]), "{v[224:255]}"(W[3]), [co0] "v"(co0), [w0] "s"(w0), [dv] "v"(dv), [db] "s"(db), [pg] "s"(pg), [po] "s"(po - 1), [p1] "s"(b + po), [nl] "s"(-LOG2E)
    : "v38", "v39", "v40", "v41", "v42", "v43", "v44", "v45", "v46", "v47", "v48", "v49", "v50", "v51", "v52", "v53", "v54", "v55", "v56", "v57", "v58", "v59", "v60", "v61", "v62", "v63", "v64", "v65", "v66", "v67", "v68", "v69", "v70", "v71", "v72", "v73", "v74", "v75", "v76", "v77", "v78", "v79", "v96", "v97", "v98", "v99", "v100", "v101", "v102", "v103", "v104", "v105", "v106", "v107", "v108", "v109", "v110", "v111", "v112", "v113", "v114", "v115", "v116", "v117", "v118", "v119", "v120", "v121", "v122", "v123", "v124", "v125", "v126", "v127", "vcc", "scc", "m0", "memory");
}
__device__ __forceinline__ void fast_st2(const u32x32 (&W)[4], u32x16& pd, unsigned ea, unsigned da, int& b, int& lf, int cp, unsigned up, unsigned sa, unsigned mu, unsigned ad,
    unsigned rw, unsigned ab, const char* p1, const char* p0, unsigned co0, unsigned co1, int po, int pf, unsigned w0, unsigned sl, unsigned dv, const char* db, const char* pg) {
  unsigned st;
  asm volatile("s_barrier\n\t"
    "ds_read_b128 v[96:99], %[ea] offset:0\n\t"
    "ds_read_b128 v[100:103], %[ea] offset:2048\n\t"
    "ds_read_b128 v[104:107], %[da] offset:0\n\t"
    "ds_read_b128 v[108:111], %[da] offset:2048\n\t"
    "ds_read_b128 v[112:115], %[ea] offset:4096\n\t"
    "ds_read_b128 v[116:119], %[ea] offset:6144\n\t"
    "s_branch 2f\n\t"
    ".p2align 6\n\t"
    "1:\n\t"
    "s_barrier\n\t"
    "2:\n\t"
    "s_waitcnt lgkmcnt(4)\n\t"
    "v_mfma_f32_16x16x32_bf16 v[64:67], v[128:131], v[96:99], 0\n\t"
    "ds_read_b128 v[120:123], %[da] offset:4096\n\t"
    "v_mfma_f32_16x16x32_bf16 v[68:71], v[132:135], v[96:99], 0\n\t"
    "ds_read_b128 v[124:127], %[da] offset:6144\n\t"
    "v_mfma_f32_16x16x32_bf16 v[72:75], v[128:131], v[100:103], 0\n\t"
    "v_mfma_f32_16x16x32_bf16 v[76:79], v[132:135], v[100:103], 0\n\t"
    "s_waitcnt lgkmcnt(4)\n\t"
    "v_mfma_f32_16x16x32_bf16 v[64:67], v[136:139], v[104:107], v[64:67]\n\t"
    "ds_read_b128 v[96:99], %[ea] offset:8192\n\t"
    "v_mfma_f32_16x16x32_bf16 v[68:71], v[140:143], v[104:107], v[68:71]\n\t"
    "ds_read_b128 v[100:103], %[ea] offset:10240\n\t"
    "v_mfma_f32_16x16x32_bf16 v[72:75], v[136:139], v[108:111], v[72:75]\n\t"
    "v_mfma_f32_16x16x32_bf16 v[76:79], v[140:143], v[108:111], v[76:79]\n\t"
    "s_cmp_eq_u32 %[w0], 1\n\t"
    "s_cbranch_scc1 7f\n\t"
    "8:\n\t"
    "s_waitcnt lgkmcnt(4)\n\t"
    "v_mfma_f32_16x16x32_bf16 v[64:67], v[144:147], v[112:115], v[64:67]\n\t"
    "ds_read_b128 v[104:107], %[da] offset:8192\n\t"
    "v_mfma_f32_16x16x32_bf16 v[68:71], v[148:151], v[112:115], v[68:71]\n\t"
    "ds_read_b128 v[108:111], %[da] offset:10240\n\t"
    "v_mfma_f32_16x16x32_bf16 v[72:75], v[144:147], v[116:119], v[72:75]\n\t"
    "v_mfma_f32_16x16x32_bf16 v[76:79], v[148:151], v[116:119], v[76:79]\n\t"
    "s_waitcnt lgkmcnt(4)\n\t"
    "v_mfma_f32_16x16x32_bf16 v[64:67], v[152:155], v[120:123], v[64:67]\n\t"
    "ds_read_b128 v[112:115], %[ea] offset:12288\n\t"
    "v_mfma_f32_16x16x32_bf16 v[68:71], v[156:159], v[120:123], v[68:71]\n\t"
    "ds_read_b128 v[116:119], %[ea] offset:14336\n\t"
    "v_mfma_f32_16x16x32_bf16 v[72:75], v[152:155], v[124:127], v[72:75]\n\t"
    "v_mfma_f32_16x16x32_bf16 v[76:79], v[156:159], v[124:127], v[76:79]\n\t"
    "s_waitcnt lgkmcnt(4)\n\t"
    "v_mfma_f32_16x16x32_bf16 v[64:67], v[160:163], v[96:99], v[64:67]\n\t"
    "ds_read_b128 v[120:123], %[da] offset:12288\n\t"
    "v_mfma_f32_16x16x32_bf16 v[68:71], v[164:167], v[96:99], v[68:71]\n\t"
    "ds_read_b128 v[124:127], %[da] offset:14336\n\t"
    "v_mfma_f32_16x16x32_bf16 v[72:75], v[160:163], v[100:103], v[72:75]\n\t"
    "v_mfma_f32_16x16x32_bf16 v[76:79], v[164:167], v[100:103], v[76:79]\n\t"
    "s_waitcnt lgkmcnt(4)\n\t"
    "v_mfma_f32_16x16x32_bf16 v[64:67], v[168:171], v[104:107], v[64:67]\n\t"
    "ds_read_b128 v[96:99], %[ea] offset:16384\n\t"
    "v_mfma_f32_16x16x32_bf16 v[68:71], v[172:175], v[104:107], v[68:71]\n\t"
    "ds_read_b128 v[100:103], %[ea] offset:18432\n\t"
    "v_mfma_f32_16x16x32_bf16 v[72:75], v[168:171], v[108:111], v[72:75]\n\t"
    "v_mfma_f32_16x16x32_bf16 v[76:79], v[172:175], v[108:111], v[76:79]\n\t"
    "s_waitcnt lgkmcnt(4)\n\t"
    "v_mfma_f32_16x16x32_bf16 v[64:67], v[176:179], v[112:115], v[64:67]\n\t"
    "ds_read_b128 v[104:107], %[da] offset:16384\n\t"
    "v_mfma_f32_16x16x32_bf16 v[68:71], v[180:183], v[112:115], v[68:71]\n\t"
    "ds_read_b128 v[108:111], %[da] offset:18432\n\t"
    "v_mfma_f32_16x16x32_bf16 v[72:75], v[176:179], v[116:119], v[72:75]\n\t"
    "v_mfma_f32_16x16x32_bf16 v[76:79], v[180:183], v[116:119], v[76:79]\n\t"
    "s_waitcnt lgkmcnt(4)\n\t"
    "v_mfma_f32_16x16x32_bf16 v[64:67], v[184:187], v[120:123], v[64:67]\n\t"
    "ds_read_b128 v[112:115], %[ea] offset:20480\n\t"
    "v_mfma_f32_16x16x32_bf16 v[68:71], v[188:191], v[120:123], v[68:71]\n\t"
    "ds_read_b128 v[116:119], %[ea] offset:22528\n\t"
    "v_mfma_f32_16x16x32_bf16 v[72:75], v[184:187], v[124:127], v[72:75]\n\t"
    "v_mfma_f32_16x16x32_bf16 v[76:79], v[188:191], v[124:127], v[76:79]\n\t"
    "s_waitcnt vmcnt(0)\n\t"
    "s_barrier\n\t"
    "s_waitcnt lgkmcnt(4)\n\t"
    "v_mfma_f32_16x16x32_bf16 v[64:67], v[192:195], v[96:99], v[64:67]\n\t"
    "ds_read_b128 v[120:123], %[da] offset:20480\n\t"
    "v_mfma_f32_16x16x32_bf16 v[68:71], v[196:199], v[96:99], v[68:71]\n\t"
    "ds_read_b128 v[124:127], %[da] offset:22528\n\t"
    "v_mfma_f32_16x16x32_bf16 v[72:75], v[192:195], v[100:103], v[72:75]\n\t"
    "ds_read_b128 v[48:51], %[rw] offset:0\n\t"
    "v_mfma_f32_16x16x32_bf16 v[76:79], v[196:199], v[100:103], v[76:79]\n\t"
    "ds_read_b128 v[52:55], %[rw] offset:1024\n\t"
    "s_cmp_eq_u32 %[w0], 0\n\t"
    "s_cbranch_scc1 5f\n\t"
    "6:\n\t"
    "s_waitcnt lgkmcnt(6)\n\t"
    "v_mfma_f32_16x16x32_bf16 v[64:67], v[200:203], v[104:107], v[64:67]\n\t"
    "ds_read_b128 v[96:99], %[ea] offset:24576\n\t"
    "v_mfma_f32_16x16x32_bf16 v[68:71], v[204:207], v[104:107], v[68:71]\n\t"
    "ds_read_b128 v[100:103], %[ea] offset:26624\n\t"
    "v_mfma_f32_16x16x32_bf16 v[72:75], v[200:203], v[108:111], v[72:75]\n\t"
    "ds_read_b128 v[56:59], %[rw] offset:2048\n\t"
    "v_mfma_f32_16x16x32_bf16 v[76:79], v[204:207], v[108:111], v[76:79]\n\t"
    "ds_read_b128 v[60:63], %[rw] offset:3072\n\t"
    "s_waitcnt lgkmcnt(0)\n\t"
    "v_mfma_f32_16x16x32_bf16 v[64:67], v[208:211], v[112:115], v[64:67]\n\t"
    "ds_read_b128 v[104:107], %[da] offset:24576\n\t"
    "v_cvt_pk_bf16_f32 v40, v80, v81\n\t"
    "v_cvt_pk_bf16_f32 v41, v82, v83\n\t"
    "v_mfma_f32_16x16x32_bf16 v[68:71], v[212:215], v[112:115], v[68:71]\n\t"
    "ds_read_b128 v[108:111], %[da] offset:26624\n\t"
    "v_cvt_pk_bf16_f32 v42, v84, v85\n\t"
    "v_mfma_f32_16x16x32_bf16 v[72:75], v[208:211], v[116:119], v[72:75]\n\t"
    "ds_read_b32 v39, %[sa]\n\t"
    "v_cvt_pk_bf16_f32 v43, v86, v87\n\t"
    "v_mfma_f32_16x16x32_bf16 v[76:79], v[212:215], v[116:119], v[76:79]\n\t"
    "global_store_dwordx4 %[co0], v[40:43], s[36:37] sc1\n\t"
    "v_mfma_f32_16x16x32_bf16 v[64:67], v[216:219], v[120:123], v[64:67]\n\t"
    "ds_read_b128 v[112:115], %[ea] offset:28672\n\t"
    "v_cvt_pk_bf16_f32 v44, v88, v89\n\t"
    "v_cvt_pk_bf16_f32 v45, v90, v91\n\t"
    "v_mfma_f32_16x16x32_bf16 v[68:71], v[220:223], v[120:123], v[68:71]\n\t"
    "ds_read_b128 v[116:119], %[ea] offset:30720\n\t"
    "v_cvt_pk_bf16_f32 v46, v92, v93\n\t"
    "v_mfma_f32_16x16x32_bf16 v[72:75], v[216:219], v[124:127], v[72:75]\n\t"
    "v_xor_b32 %[ea], 0x10000, %[ea]\n\t"
    "v_cvt_pk_bf16_f32 v47, v94, v95\n\t"
    "v_mfma_f32_16x16x32_bf16 v[76:79], v[220:223], v[124:127], v[76:79]\n\t"
    "global_store_dwordx4 %[co1], v[44:47], s[36:37] sc1\n\t"
    "v_mfma_f32_16x16x32_bf16 v[64:67], v[224:227], v[96:99], v[64:67]\n\t"
    "ds_read_b128 v[120:123], %[da] offset:28672\n\t"
    "v_cvt_pk_bf16_f32 v48, v48, v49\n\t"
    "v_cvt_pk_bf16_f32 v49, v50, v51\n\t"
    "v_mfma_f32_16x16x32_bf16 v[68:71], v[228:231], v[96:99], v[68:71]\n\t"
    "ds_read_b128 v[124:127], %[da] offset:30720\n\t"
    "v_cvt_pk_bf16_f32 v50, v52, v53\n\t"
    "v_mfma_f32_16x16x32_bf16 v[72:75], v[224:227], v[100:103], v[72:75]\n\t"
    "v_xor_b32 %[da], 0x10000, %[da]\n\t"
    "v_cvt_pk_bf16_f32 v51, v54, v55\n\t"
    "v_mfma_f32_16x16x32_bf16 v[76:79], v[228:231], v[100:103], v[76:79]\n\t"
    "global_store_dwordx4 %[co0], v[48:51], s[36:37] offset:2048 sc1\n\t"
    "s_waitcnt lgkmcnt(5)\n\t"
    "v_mfma_f32_16x16x32_bf16 v[64:67], v[232:235], v[104:107], v[64:67]\n\t"
    "ds_read_b128 v[96:99], %[ea] offset:0\n\t"
    "v_cvt_pk_bf16_f32 v56, v56, v57\n\t"
    "v_cvt_pk_bf16_f32 v57, v58, v59\n\t"
    "v_mfma_f32_16x16x32_bf16 v[68:71], v[236:239], v[104:107], v[68:71]\n\t"
    "ds_read_b128 v[100:103], %[ea] offset:2048\n\t"
    "v_cvt_pk_bf16_f32 v58, v60, v61\n\t"
    "v_mfma_f32_16x16x32_bf16 v[72:75], v[232:235], v[108:111], v[72:75]\n\t"
    "v_cvt_pk_bf16_f32 v59, v62, v63\n\t"
    "v_mfma_f32_16x16x32_bf16 v[76:79], v[236:239], v[108:111], v[76:79]\n\t"
    "global_store_dwordx4 %[co1], v[56:59], s[36:37] offset:2048 sc1\n\t"
    "s_waitcnt lgkmcnt(4)\n\t"
    "v_mfma_f32_16x16x32_bf16 v[64:67], v[240:243], v[112:115], v[64:67]\n\t"
    "ds_read_b128 v[104:107], %[da] offset:0\n\t"
    "v_mad_u32_u24 v39, v39, %[mu], %[ad]\n\t"
    "v_mfma_f32_16x16x32_bf16 v[68:71], v[244:247], v[112:115], v[68:71]\n\t"
    "ds_read_b128 v[108:111], %[da] offset:2048\n\t"
    "v_cmp_ge_i32 vcc, %[lf], v39\n\t"
    "s_cmp_eq_u64 vcc, 0\n\t"
    "s_addc_u32 %[lf], %[lf], 0\n\t"
    "v_mfma_f32_16x16x32_bf16 v[72:75], v[240:243], v[116:119], v[72:75]\n\t"
    "v_mfma_f32_16x16x32_bf16 v[76:79], v[244:247], v[116:119], v[76:79]\n\t"
    "v_cmp_ge_i32 vcc, %[lf], v39\n\t"
    "s_cmp_eq_u64 vcc, 0\n\t"
    "s_addc_u32 %[lf], %[lf], 0\n\t"
    "s_waitcnt lgkmcnt(4)\n\t"
    "v_mfma_f32_16x16x32_bf16 v[80:83], v[248:251], v[120:123], v[64:67]\n\t"
    "ds_read_b128 v[112:115], %[ea] offset:4096\n\t"
    "s_min_i32 %[lf], %[lf], %[cp]\n\t"
    "s_add_u32 %[b], %[b], 1\n\t"
    "v_mfma_f32_16x16x32_bf16 v[84:87], v[252:255], v[120:123], v[68:71]\n\t"
    "ds_read_b128 v[116:119], %[ea] offset:6144\n\t"
    "v_mfma_f32_16x16x32_bf16 v[88:91], v[248:251], v[124:127], v[72:75]\n\t"
    "v_mfma_f32_16x16x32_bf16 v[92:95], v[252:255], v[124:127], v[76:79]\n\t"
    "s_mov_b64 s[36:37], s[38:39]\n\t"
    "s_add_u32 s38, s38, %[ab]\n\t"
    "s_addc_u32 s39, s39, 0\n\t"
    "s_sub_u32 m0, %[lf], %[b]\n\t"
    "s_sub_u32 m0, m0, 1\n\t"
    "s_cmp_lt_u32 m0, %[up]\n\t"
    "s_cbranch_scc1 1b\n\t"
    "s_waitcnt lgkmcnt(0)\n\t"
    "s_nop 4\n\t"
    "s_branch 9f\n\t"
    "5:\n\t"
    "s_add_u32 %[st], %[b], %[po]\n\t"
    "s_max_i32 %[st], %[st], %[p0]\n\t"
    "v_mov_b32 v39, %[st]\n\t"
    "v_mov_b32 v38, 0\n\t"
    "s_mov_b64 exec, 1\n\t"
    "global_store_dword v38, v39, %[pg] sc1\n\t"
    "s_mov_b64 exec, -1\n\t"
    "s_mov_b32 %[p0], %[p1]\n\t"
    "s_branch 6b\n\t"
    "7:\n\t"
    "s_cmp_ge_i32 %[lf], %[cp]\n\t"
    "s_cbranch_scc1 8b\n\t"
    "s_mov_b32 m0, %[sl]\n\t"
    "s_nop 0\n\t"
    "global_load_lds_dword %[dv], %[db] sc1\n\t"
    "s_branch 8b\n\t"
    "9:"
    : "=&{v[80:95]}"(pd), [ea] "+&v"(ea), [da] "+&v"(da), [b] "+&s"(b), [lf] "+&s"(lf), "+{s[36:37]}"(p1), "+{s[38:39]}"(p0), [sa] "+&v"(sa), [st] "=&s"(st), [sl] "+&s"(sl), [p0] "+&s"(pf)
    : [rw] "v"(rw), [ab] "s"(ab), [cp] "s"(cp), [mu] "s"(mu), [ad] "v"(ad), [up] "s"(up), "{v[128:159]}"(W[0]), "{v[160:191]}"(W[1]), "{v[192:223]}"(W[2]), "{v[224:255]}"(W[3]), [co0] "v"(co0), [w0] "s"(w0), [dv] "v"(dv), [db] "s"(db), [pg] "s"(pg), [po] "s"(po - 1), [p1] "s"(b + po), [co1] "v"(co1)
    : "v38", "v39", "v40", "v41", "v42", "v43", "v44", "v45", "v46", "v47", "v48", "v49", "v50", "v51", "v52", "v53", "v54", "v55", "v56", "v57", "v58", "v59", "v60", "v61", "v62", "v63", "v64", "v65", "v66", "v67", "v68", "v69", "v70", "v71", "v72", "v73", "v74", "v75", "v76", "v77", "v78", "v79", "v96", "v97", "v98", "v99", "v100", "v101", "v102", "v103", "v104", "v105", "v106", "v107", "v108", "v109", "v110", "v111", "v112", "v113", "v114", "v115", "v116", "v117", "v118", "v119", "v120", "v121", "v122", "v123", "v124", "v125", "v126", "v127", "vcc", "scc", "m0", "memory");
}
__device__ __forceinline__ void lean_ld0(const u32x32 (&W)[4], unsigned ea, unsigned da, unsigned x, unsigned lb, const char* s0, const char* s1, const unsigned (&av)[8], unsigned ps, unsigned dv, const char* db, unsigned rw, unsigned fa, unsigned fv,
    unsigned ga, unsigned mu, unsigned ad, int th, int tb, unsigned& sv, unsigned& su, unsigned& got) {
  asm volatile("s_setprio 2\n\t"
    "v_mov_b32 v76, %[ga]\n\t"
    "ds_read_b32 v76, v76 offset:24\n\t"
    "ds_read_b128 v[96:99], %[ea] offset:0\n\t"
    "ds_read_b128 v[100:103], %[ea] offset:2048\n\t"
    "ds_read_b128 v[104:107], %[da] offset:0\n\t"
    "ds_read_b128 v[108:111], %[da] offset:2048\n\t"
    "ds_read_b128 v[112:115], %[ea] offset:4096\n\t"
    "ds_read_b128 v[116:119], %[ea] offset:6144\n\t"
    "s_waitcnt lgkmcnt(6)\n\t"
    "v_cmp_ne_u32 vcc, 0, v76\n\t"
    "s_mov_b32 %[got], -1\n\t"
    "s_cbranch_vccnz 12f\n\t"
    "s_waitcnt lgkmcnt(4)\n\t"
    "v_mfma_f32_16x16x32_bf16 v[80:83], v[128:131], v[96:99], 0\n\t"
    "ds_read_b128 v[120:123], %[da] offset:4096\n\t"
    "v_mfma_f32_16x16x32_bf16 v[84:87], v[132:135], v[96:99], 0\n\t"
    "ds_read_b128 v[124:127], %[da] offset:6144\n\t"
    "v_mfma_f32_16x16x32_bf16 v[88:91], v[128:131], v[100:103], 0\n\t"
    "v_mfma_f32_16x16x32_bf16 v[92:95], v[132:135], v[100:103], 0\n\t"
    "s_waitcnt vmcnt(24)\n\t"
    "s_cmp_lt_u32 %[x], 2\n\t"
    "s_cbranch_scc0 41f\n\t"
    "s_waitcnt vmcnt(8)\n\t"
    "41:\n\t"
    "s_barrier\n\t"
    "s_waitcnt lgkmcnt(4)\n\t"
    "v_mfma_f32_16x16x32_bf16 v[80:83], v[136:139], v[104:107], v[80:83]\n\t"
    "ds_read_b128 v[96:99], %[ea] offset:8192\n\t"
    "v_mfma_f32_16x16x32_bf16 v[84:87], v[140:143], v[104:107], v[84:87]\n\t"
    "ds_read_b128 v[100:103], %[ea] offset:10240\n\t"
    "v_mfma_f32_16x16x32_bf16 v[88:91], v[136:139], v[108:111], v[88:91]\n\t"
    "v_mfma_f32_16x16x32_bf16 v[92:95], v[140:143], v[108:111], v[92:95]\n\t"
    "s_waitcnt lgkmcnt(4)\n\t"
    "v_mfma_f32_16x16x32_bf16 v[80:83], v[144:147], v[112:115], v[80:83]\n\t"
    "ds_read_b128 v[104:107], %[da] offset:8192\n\t"
    "v_mfma_f32_16x16x32_bf16 v[84:87], v[148:151], v[112:115], v[84:87]\n\t"
    "ds_read_b128 v[108:111], %[da] offset:10240\n\t"
    "v_mfma_f32_16x16x32_bf16 v[88:91], v[144:147], v[116:119], v[88:91]\n\t"
    "v_mfma_f32_16x16x32_bf16 v[92:95], v[148:151], v[116:119], v[92:95]\n\t"
    "s_waitcnt lgkmcnt(4)\n\t"
    "v_mfma_f32_16x16x32_bf16 v[80:83], v[152:155], v[120:123], v[80:83]\n\t"
    "ds_read_b128 v[112:115], %[ea] offset:12288\n\t"
    "v_mfma_f32_16x16x32_bf16 v[84:87], v[156:159], v[120:123], v[84:87]\n\t"
    "ds_read_b128 v[116:119], %[ea] offset:14336\n\t"
    "v_mfma_f32_16x16x32_bf16 v[88:91], v[152:155], v[124:127], v[88:91]\n\t"
    "v_mfma_f32_16x16x32_bf16 v[92:95], v[156:159], v[124:127], v[92:95]\n\t"
    "s_waitcnt lgkmcnt(4)\n\t"
    "v_mfma_f32_16x16x32_bf16 v[80:83], v[160:163], v[96:99], v[80:83]\n\t"
    "ds_read_b128 v[120:123], %[da] offset:12288\n\t"
    "v_mfma_f32_16x16x32_bf16 v[84:87], v[164:167], v[96:99], v[84:87]\n\t"
    "ds_read_b128 v[124:127], %[da] offset:14336\n\t"
    "v_mfma_f32_16x16x32_bf16 v[88:91], v[160:163], v[100:103], v[88:91]\n\t"
    "v_mfma_f32_16x16x32_bf16 v[92:95], v[164:167], v[100:103], v[92:95]\n\t"
    "s_waitcnt vmcnt(20)\n\t"
    "s_cmp_lt_u32 %[x], 2\n\t"
    "s_cbranch_scc0 45f\n\t"
    "s_waitcnt vmcnt(4)\n\t"
    "45:\n\t"
    "s_barrier\n\t"
    "s_waitcnt lgkmcnt(4)\n\t"
    "v_mfma_f32_16x16x32_bf16 v[80:83], v[168:171], v[104:107], v[80:83]\n\t"
    "ds_read_b128 v[96:99], %[ea] offset:16384\n\t"
    "v_mfma_f32_16x16x32_bf16 v[84:87], v[172:175], v[104:107], v[84:87]\n\t"
    "ds_read_b128 v[100:103], %[ea] offset:18432\n\t"
    "v_mfma_f32_16x16x32_bf16 v[88:91], v[168:171], v[108:111], v[88:91]\n\t"
    "v_mfma_f32_16x16x32_bf16 v[92:95], v[172:175], v[108:111], v[92:95]\n\t"
    "s_waitcnt lgkmcnt(4)\n\t"
    "v_mfma_f32_16x16x32_bf16 v[80:83], v[176:179], v[112:115], v[80:83]\n\t"
    "ds_read_b128 v[104:107], %[da] offset:16384\n\t"
    "v_mfma_f32_16x16x32_bf16 v[84:87], v[180:183], v[112:115], v[84:87]\n\t"
    "ds_read_b128 v[108:111], %[da] offset:18432\n\t"
    "v_mfma_f32_16x16x32_bf16 v[88:91], v[176:179], v[116:119], v[88:91]\n\t"
    "v_mfma_f32_16x16x32_bf16 v[92:95], v[180:183], v[116:119], v[92:95]\n\t"
    "s_waitcnt lgkmcnt(4)\n\t"
    "v_mfma_f32_16x16x32_bf16 v[80:83], v[184:187], v[120:123], v[80:83]\n\t"
    "ds_read_b128 v[112:115], %[ea] offset:20480\n\t"
    "v_mfma_f32_16x16x32_bf16 v[84:87], v[188:191], v[120:123], v[84:87]\n\t"
    "ds_read_b128 v[116:119], %[ea] offset:22528\n\t"
    "v_mfma_f32_16x16x32_bf16 v[88:91], v[184:187], v[124:127], v[88:91]\n\t"
    "v_mfma_f32_16x16x32_bf16 v[92:95], v[188:191], v[124:127], v[92:95]\n\t"
    "s_waitcnt lgkmcnt(4)\n\t"
    "v_mfma_f32_16x16x32_bf16 v[80:83], v[192:195], v[96:99], v[80:83]\n\t"
    "ds_read_b128 v[120:123], %[da] offset:20480\n\t"
    "v_mfma_f32_16x16x32_bf16 v[84:87], v[196:199], v[96:99], v[84:87]\n\t"
    "ds_read_b128 v[124:127], %[da] offset:22528\n\t"
    "v_mfma_f32_16x16x32_bf16 v[88:91], v[192:195], v[100:103], v[88:91]\n\t"
    "v_mfma_f32_16x16x32_bf16 v[92:95], v[196:199], v[100:103], v[92:95]\n\t"
    "s_waitcnt vmcnt(16)\n\t"
    "s_cmp_lt_u32 %[x], 2\n\t"
    "s_cbranch_scc0 49f\n\t"
    "s_waitcnt vmcnt(0)\n\t"
    "49:\n\t"
    "s_barrier\n\t"
    "s_waitcnt lgkmcnt(4)\n\t"
    "v_mfma_f32_16x16x32_bf16 v[80:83], v[200:203], v[104:107], v[80:83]\n\t"
    "ds_read_b128 v[96:99], %[ea] offset:24576\n\t"
    "v_mfma_f32_16x16x32_bf16 v[84:87], v[204:207], v[104:107], v[84:87]\n\t"
    "ds_read_b128 v[100:103], %[ea] offset:26624\n\t"
    "v_mfma_f32_16x16x32_bf16 v[88:91], v[200:203], v[108:111], v[88:91]\n\t"
    "v_mfma_f32_16x16x32_bf16 v[92:95], v[204:207], v[108:111], v[92:95]\n\t"
    "s_waitcnt lgkmcnt(4)\n\t"
    "v_mfma_f32_16x16x32_bf16 v[80:83], v[208:211], v[112:115], v[80:83]\n\t"
    "ds_read_b128 v[104:107], %[da] offset:24576\n\t"
    "v_mfma_f32_16x16x32_bf16 v[84:87], v[212:215], v[112:115], v[84:87]\n\t"
    "ds_read_b128 v[108:111], %[da] offset:26624\n\t"
    "v_mfma_f32_16x16x32_bf16 v[88:91], v[208:211], v[116:119], v[88:91]\n\t"
    "v_mfma_f32_16x16x32_bf16 v[92:95], v[212:215], v[116:119], v[92:95]\n\t"
    "s_cmp_eq_u32 %[ps], 0\n\t"
    "s_cbranch_scc1 10f\n\t"
    "global_load_dword %[sv], %[dv], %[db] sc1\n\t"
    "global_load_dword %[su], %[dv], %[db] offset:64 sc1\n\t"
    "10:\n\t"
    "s_waitcnt lgkmcnt(4)\n\t"
    "v_mfma_f32_16x16x32_bf16 v[80:83], v[216:219], v[120:123], v[80:83]\n\t"
    "ds_read_b128 v[112:115], %[ea] offset:28672\n\t"
    "v_mfma_f32_16x16x32_bf16 v[84:87], v[220:223], v[120:123], v[84:87]\n\t"
    "ds_read_b128 v[116:119], %[ea] offset:30720\n\t"
    "v_mfma_f32_16x16x32_bf16 v[88:91], v[216:219], v[124:127], v[88:91]\n\t"
    "v_mfma_f32_16x16x32_bf16 v[92:95], v[220:223], v[124:127], v[92:95]\n\t"
    "s_waitcnt lgkmcnt(4)\n\t"
    "v_mfma_f32_16x16x32_bf16 v[80:83], v[224:227], v[96:99], v[80:83]\n\t"
    "ds_read_b128 v[120:123], %[da] offset:28672\n\t"
    "v_mfma_f32_16x16x32_bf16 v[84:87], v[228:231], v[96:99], v[84:87]\n\t"
    "ds_read_b128 v[124:127], %[da] offset:30720\n\t"
    "v_mfma_f32_16x16x32_bf16 v[88:91], v[224:227], v[100:103], v[88:91]\n\t"
    "v_mfma_f32_16x16x32_bf16 v[92:95], v[228:231], v[100:103], v[92:95]\n\t"
    "s_waitcnt lgkmcnt(4)\n\t"
    "v_mfma_f32_16x16x32_bf16 v[80:83], v[232:235], v[104:107], v[80:83]\n\t"
    "v_mfma_f32_16x16x32_bf16 v[84:87], v[236:239], v[104:107], v[84:87]\n\t"
    "v_mfma_f32_16x16x32_bf16 v[88:91], v[232:235], v[108:111], v[88:91]\n\t"
    "v_mfma_f32_16x16x32_bf16 v[92:95], v[236:239], v[108:111], v[92:95]\n\t"
    "s_waitcnt lgkmcnt(2)\n\t"
    "v_mfma_f32_16x16x32_bf16 v[80:83], v[240:243], v[112:115], v[80:83]\n\t"
    "v_mfma_f32_16x16x32_bf16 v[84:87], v[244:247], v[112:115], v[84:87]\n\t"
    "v_mfma_f32_16x16x32_bf16 v[88:91], v[240:243], v[116:119], v[88:91]\n\t"
    "v_mfma_f32_16x16x32_bf16 v[92:95], v[244:247], v[116:119], v[92:95]\n\t"
    "s_waitcnt lgkmcnt(0)\n\t"
    "v_mfma_f32_16x16x32_bf16 v[80:83], v[248:251], v[120:123], v[80:83]\n\t"
    "s_memrealtime s[36:37]\n\t"
    "v_mfma_f32_16x16x32_bf16 v[84:87], v[252:255], v[120:123], v[84:87]\n\t"
    "v_mfma_f32_16x16x32_bf16 v[88:91], v[248:251], v[124:127], v[88:91]\n\t"
    "v_mfma_f32_16x16x32_bf16 v[92:95], v[252:255], v[124:127], v[92:95]\n\t"
    "s_mov_b32 %[got], 0\n\t"
    "v_mov_b32 v76, %[fa]\n\t"
    "v_mov_b32 v77, %[fv]\n\t"
    "v_add_u32 v78, 1, v77\n\t"
    "v_mov_b32 v79, %[ga]\n\t"
    "s_waitcnt lgkmcnt(0)\n\t"
    "ds_write_b128 %[rw], v[80:83] offset:0\n\t"
    "ds_write_b128 %[rw], v[84:87] offset:1024\n\t"
    "ds_write_b128 %[rw], v[88:91] offset:2048\n\t"
    "ds_write_b128 %[rw], v[92:95] offset:3072\n\t"
    "ds_write_b32 v76, v77\n\t"
    "s_cmp_eq_u32 %[ps], 0\n\t"
    "s_cbranch_scc1 9f\n\t"
    "s_waitcnt vmcnt(0)\n\t"
    "v_mad_u32_u24 v76, %[sv], %[mu], %[ad]\n\t"
    "v_cmp_ge_i32 vcc, %[th], v76\n\t"
    "s_cbranch_vccz 11f\n\t"
    "s_sub_u32 s36, s36, 20\n\t"
    "v_sub_u32 v76, s36, %[su]\n\t"
    "v_lshrrev_b32 v77, 24, %[su]\n\t"
    "v_lshlrev_b32 v76, 8, v76\n\t"
    "v_ashrrev_i32 v76, 31, v76\n\t"
    "v_add_u32 v77, v77, v76\n\t"
    "v_cmp_lt_i32 s[36:37], %[tb], v77\n\t"
    "s_andn2_b64 vcc, vcc, s[36:37]\n\t"
    "s_cbranch_scc1 9f\n\t"
    "11:\n\t"
    "ds_write2_b32 v79, v78, v78 offset1:1\n\t"
    "s_add_u32 m0, %[lb], 0\n\t"
    "s_nop 0\n\t"
    "global_load_lds_dwordx4 %[av0], %[s0] sc1\n\t"
    "s_add_u32 m0, %[lb], 32768\n\t"
    "s_nop 0\n\t"
    "global_load_lds_dwordx4 %[av1], %[s0] sc1\n\t"
    "s_add_u32 m0, %[lb], 4096\n\t"
    "s_nop 0\n\t"
    "global_load_lds_dwordx4 %[av2], %[s0] sc1\n\t"
    "s_add_u32 m0, %[lb], 36864\n\t"
    "s_nop 0\n\t"
    "global_load_lds_dwordx4 %[av3], %[s0] sc1\n\t"
    "s_add_u32 m0, %[lb], 8192\n\t"
    "s_nop 0\n\t"
    "global_load_lds_dwordx4 %[av4], %[s0] sc1\n\t"
    "s_add_u32 m0, %[lb], 40960\n\t"
    "s_nop 0\n\t"
    "global_load_lds_dwordx4 %[av5], %[s0] sc1\n\t"
    "s_add_u32 m0, %[lb], 12288\n\t"
    "s_nop 0\n\t"
    "global_load_lds_dwordx4 %[av6], %[s0] sc1\n\t"
    "s_add_u32 m0, %[lb], 45056\n\t"
    "s_nop 0\n\t"
    "global_load_lds_dwordx4 %[av7], %[s0] sc1\n\t"
    "s_add_u32 m0, %[lb], 16384\n\t"
    "s_nop 0\n\t"
    "global_load_lds_dwordx4 %[av0], %[s1] sc1\n\t"
    "s_add_u32 m0, %[lb], 49152\n\t"
    "s_nop 0\n\t"
    "global_load_lds_dwordx4 %[av1], %[s1] sc1\n\t"
    "s_add_u32 m0, %[lb], 20480\n\t"
    "s_nop 0\n\t"
    "global_load_lds_dwordx4 %[av2], %[s1] sc1\n\t"
    "s_add_u32 m0, %[lb], 53248\n\t"
    "s_nop 0\n\t"
    "global_load_lds_dwordx4 %[av3], %[s1] sc1\n\t"
    "s_add_u32 m0, %[lb], 24576\n\t"
    "s_nop 0\n\t"
    "global_load_lds_dwordx4 %[av4], %[s1] sc1\n\t"
    "s_add_u32 m0, %[lb], 57344\n\t"
    "s_nop 0\n\t"
    "global_load_lds_dwordx4 %[av5], %[s1] sc1\n\t"
    "s_add_u32 m0, %[lb], 28672\n\t"
    "s_nop 0\n\t"
    "global_load_lds_dwordx4 %[av6], %[s1] sc1\n\t"
    "s_add_u32 m0, %[lb], 61440\n\t"
    "s_nop 0\n\t"
    "global_load_lds_dwordx4 %[av7], %[s1] sc1\n\t"
    "s_mov_b32 %[got], 1\n\t"
    "s_branch 9f\n\t"
    "12:\n\t"
    "s_waitcnt lgkmcnt(0)\n\t"
    "9:"
    : [sv] "+&v"(sv), [su] "+&v"(su), [got] "=&s"(got)
    : [ea] "v"(ea), [da] "v"(da), [x] "s"(x), "{v[128:159]}"(W[0]), "{v[160:191]}"(W[1]), "{v[192:223]}"(W[2]), "{v[224:255]}"(W[3]), [lb] "s"(lb), [s0] "s"(s0), [s1] "s"(s1), [av0] "v"(av[0]), [av1] "v"(av[1]), [av2] "v"(av[2]), [av3] "v"(av[3]), [av4] "v"(av[4]), [av5] "v"(av[5]), [av6] "v"(av[6]), [av7] "v"(av[7]), [ps] "s"(ps), [dv] "v"(dv), [db] "s"(db), [rw] "v"(rw), [fa] "s"(fa), [fv] "s"(fv), [ga] "s"(ga), [mu] "s"(mu), [ad] "v"(ad), [th] "s"(th), [tb] "s"(tb)
    : "v96", "v97", "v98", "v99", "v100", "v101", "v102", "v103", "v104", "v105", "v106", "v107", "v108", "v109", "v110", "v111", "v112", "v113", "v114", "v115", "v116", "v117", "v118", "v119", "v120", "v121", "v122", "v123", "v124", "v125", "v126", "v127", "v76", "v77", "v78", "v79", "v80", "v81", "v82", "v83", "v84", "v85", "v86", "v87", "v88", "v89", "v90", "v91", "v92", "v93", "v94", "v95", "vcc", "scc", "m0", "memory", "s36", "s37", "s38", "s39");
}
__device__ __forceinline__ void lean_dm0(const u32x32 (&W)[4], unsigned ea, unsigned da, unsigned x, unsigned lb, const char* s0, const char* s1, const unsigned (&av)[8], unsigned ps, unsigned dv, const char* db, unsigned rw, unsigned fa, unsigned fv,
    unsigned ga, unsigned mu, unsigned ad, int th, int tb, unsigned& sv, unsigned& su, unsigned& got) {
  asm volatile("s_setprio 2\n\t"
    "v_mov_b32 v76, %[ga]\n\t"
    "ds_read_b32 v76, v76 offset:24\n\t"
    "ds_read_b128 v[96:99], %[ea] offset:0\n\t"
    "ds_read_b128 v[100:103], %[ea] offset:2048\n\t"
    "ds_read_b128 v[104:107], %[da] offset:0\n\t"
    "ds_read_b128 v[108:111], %[da] offset:2048\n\t"
    "ds_read_b128 v[112:115], %[ea] offset:4096\n\t"
    "ds_read_b128 v[116:119], %[ea] offset:6144\n\t"
    "s_waitcnt lgkmcnt(6)\n\t"
    "v_cmp_ne_u32 vcc, 0, v76\n\t"
    "s_mov_b32 %[got], -1\n\t"
    "s_cbranch_vccnz 12f\n\t"
    "s_waitcnt lgkmcnt(4)\n\t"
    "v_mfma_f32_16x16x32_bf16 v[80:83], v[128:131], v[96:99], 0\n\t"
    "ds_read_b128 v[120:123], %[da] offset:4096\n\t"
    "s_add_u32 m0, %[lb], 0\n\t"
    "v_mfma_f32_16x16x32_bf16 v[84:87], v[132:135], v[96:99], 0\n\t"
    "ds_read_b128 v[124:127], %[da] offset:6144\n\t"
    "global_load_lds_dwordx4 %[av0], %[s0] sc1\n\t"
    "v_mfma_f32_16x16x32_bf16 v[88:91], v[128:131], v[100:103], 0\n\t"
    "s_add_u32 m0, %[lb], 32768\n\t"
    "v_mfma_f32_16x16x32_bf16 v[92:95], v[132:135], v[100:103], 0\n\t"
    "global_load_lds_dwordx4 %[av1], %[s0] sc1\n\t"
    "s_waitcnt vmcnt(10)\n\t"
    "s_barrier\n\t"
    "s_waitcnt lgkmcnt(4)\n\t"
    "v_mfma_f32_16x16x32_bf16 v[80:83], v[136:139], v[104:107], v[80:83]\n\t"
    "ds_read_b128 v[96:99], %[ea] offset:8192\n\t"
    "s_add_u32 m0, %[lb], 4096\n\t"
    "v_mfma_f32_16x16x32_bf16 v[84:87], v[140:143], v[104:107], v[84:87]\n\t"
    "ds_read_b128 v[100:103], %[ea] offset:10240\n\t"
    "global_load_lds_dwordx4 %[av2], %[s0] sc1\n\t"
    "v_mfma_f32_16x16x32_bf16 v[88:91], v[136:139], v[108:111], v[88:91]\n\t"
    "s_add_u32 m0, %[lb], 36864\n\t"
    "v_mfma_f32_16x16x32_bf16 v[92:95], v[140:143], v[108:111], v[92:95]\n\t"
    "global_load_lds_dwordx4 %[av3], %[s0] sc1\n\t"
    "s_waitcnt lgkmcnt(4)\n\t"
    "v_mfma_f32_16x16x32_bf16 v[80:83], v[144:147], v[112:115], v[80:83]\n\t"
    "ds_read_b128 v[104:107], %[da] offset:8192\n\t"
    "s_add_u32 m0, %[lb], 8192\n\t"
    "v_mfma_f32_16x16x32_bf16 v[84:87], v[148:151], v[112:115], v[84:87]\n\t"
    "ds_read_b128 v[108:111], %[da] offset:10240\n\t"
    "global_load_lds_dwordx4 %[av4], %[s0] sc1\n\t"
    "v_mfma_f32_16x16x32_bf16 v[88:91], v[144:147], v[116:119], v[88:91]\n\t"
    "s_add_u32 m0, %[lb], 40960\n\t"
    "v_mfma_f32_16x16x32_bf16 v[92:95], v[148:151], v[116:119], v[92:95]\n\t"
    "global_load_lds_dwordx4 %[av5], %[s0] sc1\n\t"
    "s_waitcnt lgkmcnt(4)\n\t"
    "v_mfma_f32_16x16x32_bf16 v[80:83], v[152:155], v[120:123], v[80:83]\n\t"
    "ds_read_b128 v[112:115], %[ea] offset:12288\n\t"
    "s_add_u32 m0, %[lb], 12288\n\t"
    "v_mfma_f32_16x16x32_bf16 v[84:87], v[156:159], v[120:123], v[84:87]\n\t"
    "ds_read_b128 v[116:119], %[ea] offset:14336\n\t"
    "global_load_lds_dwordx4 %[av6], %[s0] sc1\n\t"
    "v_mfma_f32_16x16x32_bf16 v[88:91], v[152:155], v[124:127], v[88:91]\n\t"
    "s_add_u32 m0, %[lb], 45056\n\t"
    "v_mfma_f32_16x16x32_bf16 v[92:95], v[156:159], v[124:127], v[92:95]\n\t"
    "global_load_lds_dwordx4 %[av7], %[s0] sc1\n\t"
    "s_waitcnt lgkmcnt(4)\n\t"
    "v_mfma_f32_16x16x32_bf16 v[80:83], v[160:163], v[96:99], v[80:83]\n\t"
    "ds_read_b128 v[120:123], %[da] offset:12288\n\t"
    "s_add_u32 m0, %[lb], 16384\n\t"
    "v_mfma_f32_16x16x32_bf16 v[84:87], v[164:167], v[96:99], v[84:87]\n\t"
    "ds_read_b128 v[124:127], %[da] offset:14336\n\t"
    "global_load_lds_dwordx4 %[av0], %[s1] sc1\n\t"
    "v_mfma_f32_16x16x32_bf16 v[88:91], v[160:163], v[100:103], v[88:91]\n\t"
    "s_add_u32 m0, %[lb], 49152\n\t"
    "v_mfma_f32_16x16x32_bf16 v[92:95], v[164:167], v[100:103], v[92:95]\n\t"
    "global_load_lds_dwordx4 %[av1], %[s1] sc1\n\t"
    "s_waitcnt vmcnt(14)\n\t"
    "s_barrier\n\t"
    "s_waitcnt lgkmcnt(4)\n\t"
    "v_mfma_f32_16x16x32_bf16 v[80:83], v[168:171], v[104:107], v[80:83]\n\t"
    "ds_read_b128 v[96:99], %[ea] offset:16384\n\t"
    "s_add_u32 m0, %[lb], 20480\n\t"
    "v_mfma_f32_16x16x32_bf16 v[84:87], v[172:175], v[104:107], v[84:87]\n\t"
    "ds_read_b128 v[100:103], %[ea] offset:18432\n\t"
    "global_load_lds_dwordx4 %[av2], %[s1] sc1\n\t"
    "v_mfma_f32_16x16x32_bf16 v[88:91], v[168:171], v[108:111], v[88:91]\n\t"
    "s_add_u32 m0, %[lb], 53248\n\t"
    "v_mfma_f32_16x16x32_bf16 v[92:95], v[172:175], v[108:111], v[92:95]\n\t"
    "global_load_lds_dwordx4 %[av3], %[s1] sc1\n\t"
    "s_waitcnt lgkmcnt(4)\n\t"
    "v_mfma_f32_16x16x32_bf16 v[80:83], v[176:179], v[112:115], v[80:83]\n\t"
    "ds_read_b128 v[104:107], %[da] offset:16384\n\t"
    "s_add_u32 m0, %[lb], 24576\n\t"
    "v_mfma_f32_16x16x32_bf16 v[84:87], v[180:183], v[112:115], v[84:87]\n\t"
    "ds_read_b128 v[108:111], %[da] offset:18432\n\t"
    "global_load_lds_dwordx4 %[av4], %[s1] sc1\n\t"
    "v_mfma_f32_16x16x32_bf16 v[88:91], v[176:179], v[116:119], v[88:91]\n\t"
    "s_add_u32 m0, %[lb], 57344\n\t"
    "v_mfma_f32_16x16x32_bf16 v[92:95], v[180:183], v[116:119], v[92:95]\n\t"
    "global_load_lds_dwordx4 %[av5], %[s1] sc1\n\t"
    "s_waitcnt lgkmcnt(4)\n\t"
    "v_mfma_f32_16x16x32_bf16 v[80:83], v[184:187], v[120:123], v[80:83]\n\t"
    "ds_read_b128 v[112:115], %[ea] offset:20480\n\t"
    "s_add_u32 m0, %[lb], 28672\n\t"
    "v_mfma_f32_16x16x32_bf16 v[84:87], v[188:191], v[120:123], v[84:87]\n\t"
    "ds_read_b128 v[116:119], %[ea] offset:22528\n\t"
    "global_load_lds_dwordx4 %[av6], %[s1] sc1\n\t"
    "v_mfma_f32_16x16x32_bf16 v[88:91], v[184:187], v[124:127], v[88:91]\n\t"
    "s_add_u32 m0, %[lb], 61440\n\t"
    "v_mfma_f32_16x16x32_bf16 v[92:95], v[188:191], v[124:127], v[92:95]\n\t"
    "global_load_lds_dwordx4 %[av7], %[s1] sc1\n\t"
    "s_waitcnt lgkmcnt(4)\n\t"
    "v_mfma_f32_16x16x32_bf16 v[80:83], v[192:195], v[96:99], v[80:83]\n\t"
    "ds_read_b128 v[120:123], %[da] offset:20480\n\t"
    "v_mfma_f32_16x16x32_bf16 v[84:87], v[196:199], v[96:99], v[84:87]\n\t"
    "ds_read_b128 v[124:127], %[da] offset:22528\n\t"
    "v_mfma_f32_16x16x32_bf16 v[88:91], v[192:195], v[100:103], v[88:91]\n\t"
    "v_mfma_f32_16x16x32_bf16 v[92:95], v[196:199], v[100:103], v[92:95]\n\t"
    "s_waitcnt vmcnt(16)\n\t"
    "s_barrier\n\t"
    "s_waitcnt lgkmcnt(4)\n\t"
    "v_mfma_f32_16x16x32_bf16 v[80:83], v[200:203], v[104:107], v[80:83]\n\t"
    "ds_read_b128 v[96:99], %[ea] offset:24576\n\t"
    "v_mfma_f32_16x16x32_bf16 v[84:87], v[204:207], v[104:107], v[84:87]\n\t"
    "ds_read_b128 v[100:103], %[ea] offset:26624\n\t"
    "v_mfma_f32_16x16x32_bf16 v[88:91], v[200:203], v[108:111], v[88:91]\n\t"
    "v_mfma_f32_16x16x32_bf16 v[92:95], v[204:207], v[108:111], v[92:95]\n\t"
    "s_waitcnt lgkmcnt(4)\n\t"
    "v_mfma_f32_16x16x32_bf16 v[80:83], v[208:211], v[112:115], v[80:83]\n\t"
    "ds_read_b128 v[104:107], %[da] offset:24576\n\t"
    "v_mfma_f32_16x16x32_bf16 v[84:87], v[212:215], v[112:115], v[84:87]\n\t"
    "ds_read_b128 v[108:111], %[da] offset:26624\n\t"
    "v_mfma_f32_16x16x32_bf16 v[88:91], v[208:211], v[116:119], v[88:91]\n\t"
    "v_mfma_f32_16x16x32_bf16 v[92:95], v[212:215], v[116:119], v[92:95]\n\t"
    "s_waitcnt lgkmcnt(4)\n\t"
    "v_mfma_f32_16x16x32_bf16 v[80:83], v[216:219], v[120:123], v[80:83]\n\t"
    "ds_read_b128 v[112:115], %[ea] offset:28672\n\t"
    "v_mfma_f32_16x16x32_bf16 v[84:87], v[220:223], v[120:123], v[84:87]\n\t"
    "ds_read_b128 v[116:119], %[ea] offset:30720\n\t"
    "v_mfma_f32_16x16x32_bf16 v[88:91], v[216:219], v[124:127], v[88:91]\n\t"
    "v_mfma_f32_16x16x32_bf16 v[92:95], v[220:223], v[124:127], v[92:95]\n\t"
    "s_waitcnt lgkmcnt(4)\n\t"
    "v_mfma_f32_16x16x32_bf16 v[80:83], v[224:227], v[96:99], v[80:83]\n\t"
    "ds_read_b128 v[120:123], %[da] offset:28672\n\t"
    "v_mfma_f32_16x16x32_bf16 v[84:87], v[228:231], v[96:99], v[84:87]\n\t"
    "ds_read_b128 v[124:127], %[da] offset:30720\n\t"
    "v_mfma_f32_16x16x32_bf16 v[88:91], v[224:227], v[100:103], v[88:91]\n\t"
    "v_mfma_f32_16x16x32_bf16 v[92:95], v[228:231], v[100:103], v[92:95]\n\t"
    "s_waitcnt lgkmcnt(4)\n\t"
    "v_mfma_f32_16x16x32_bf16 v[80:83], v[232:235], v[104:107], v[80:83]\n\t"
    "v_mfma_f32_16x16x32_bf16 v[84:87], v[236:239], v[104:107], v[84:87]\n\t"
    "v_mfma_f32_16x16x32_bf16 v[88:91], v[232:235], v[108:111], v[88:91]\n\t"
    "v_mfma_f32_16x16x32_bf16 v[92:95], v[236:239], v[108:111], v[92:95]\n\t"
    "s_waitcnt lgkmcnt(2)\n\t"
    "v_mfma_f32_16x16x32_bf16 v[80:83], v[240:243], v[112:115], v[80:83]\n\t"
    "v_mfma_f32_16x16x32_bf16 v[84:87], v[244:247], v[112:115], v[84:87]\n\t"
    "v_mfma_f32_16x16x32_bf16 v[88:91], v[240:243], v[116:119], v[88:91]\n\t"
    "v_mfma_f32_16x16x32_bf16 v[92:95], v[244:247], v[116:119], v[92:95]\n\t"
    "s_waitcnt lgkmcnt(0)\n\t"
    "v_mfma_f32_16x16x32_bf16 v[80:83], v[248:251], v[120:123], v[80:83]\n\t"
    "v_mfma_f32_16x16x32_bf16 v[84:87], v[252:255], v[120:123], v[84:87]\n\t"
    "v_mfma_f32_16x16x32_bf16 v[88:91], v[248:251], v[124:127], v[88:91]\n\t"
    "v_mfma_f32_16x16x32_bf16 v[92:95], v[252:255], v[124:127], v[92:95]\n\t"
    "s_cmp_eq_u32 %[ps], 0\n\t"
    "s_cbranch_scc1 10f\n\t"
    "global_load_dword %[sv], %[dv], %[db] sc1\n\t"
    "global_load_dword %[su], %[dv], %[db] offset:64 sc1\n\t"
    "10:\n\t"
    "s_mov_b32 %[got], 0\n\t"
    "v_mov_b32 v76, %[fa]\n\t"
    "v_mov_b32 v77, %[fv]\n\t"
    "v_add_u32 v78, 1, v77\n\t"
    "v_mov_b32 v79, %[ga]\n\t"
    "ds_write_b128 %[rw], v[80:83] offset:0\n\t"
    "ds_write_b128 %[rw], v[84:87] offset:1024\n\t"
    "ds_write_b128 %[rw], v[88:91] offset:2048\n\t"
    "ds_write_b128 %[rw], v[92:95] offset:3072\n\t"
    "ds_write_b32 v76, v77\n\t"
    "s_cmp_eq_u32 %[ps], 0\n\t"
    "s_cbranch_scc1 9f\n\t"
    "s_waitcnt vmcnt(0)\n\t"
    "s_branch 9f\n\t"
    "12:\n\t"
    "s_waitcnt lgkmcnt(0)\n\t"
    "9:"
    : [sv] "+&v"(sv), [su] "+&v"(su), [got] "=&s"(got)
    : [ea] "v"(ea), [da] "v"(da), [x] "s"(x), "{v[128:159]}"(W[0]), "{v[160:191]}"(W[1]), "{v[192:223]}"(W[2]), "{v[224:255]}"(W[3]), [lb] "s"(lb), [s0] "s"(s0), [s1] "s"(s1), [av0] "v"(av[0]), [av1] "v"(av[1]), [av2] "v"(av[2]), [av3] "v"(av[3]), [av4] "v"(av[4]), [av5] "v"(av[5]), [av6] "v"(av[6]), [av7] "v"(av[7]), [ps] "s"(ps), [dv] "v"(dv), [db] "s"(db), [rw] "v"(rw), [fa] "s"(fa), [fv] "s"(fv), [ga] "s"(ga), [mu] "s"(mu), [ad] "v"(ad), [th] "s"(th), [tb] "s"(tb)
    : "v96", "v97", "v98", "v99", "v100", "v101", "v102", "v103", "v104", "v105", "v106", "v107", "v108", "v109", "v110", "v111", "v112", "v113", "v114", "v115", "v116", "v117", "v118", "v119", "v120", "v121", "v122", "v123", "v124", "v125", "v126", "v127", "v76", "v77", "v78", "v79", "v80", "v81", "v82", "v83", "v84", "v85", "v86", "v87", "v88", "v89", "v90", "v91", "v92", "v93", "v94", "v95", "vcc", "scc", "m0", "memory");
}
__device__ __forceinline__ void lean_ld1(const u32x32 (&W)[4], unsigned ea, unsigned da, unsigned x, unsigned lb, const char* s0, const char* s1, const unsigned (&av)[8], unsigned ps, unsigned dv, const char* db, unsigned rw, unsigned fa, unsigned fv,
    unsigned ga, unsigned mu, unsigned ad, int th, int tb, unsigned& sv, unsigned& su, unsigned& got) {
  asm volatile("s_setprio 2\n\t"
    "v_mov_b32 v76, %[ga]\n\t"
    "ds_read_b32 v76, v76 offset:24\n\t"
    "ds_read_b128 v[96:99], %[ea] offset:0\n\t"
    "ds_read_b128 v[100:103], %[ea] offset:2048\n\t"
    "ds_read_b128 v[104:107], %[da] offset:0\n\t"
    "ds_read_b128 v[108:111], %[da] offset:2048\n\t"
    "ds_read_b128 v[112:115], %[ea] offset:4096\n\t"
    "ds_read_b128 v[116:119], %[ea] offset:6144\n\t"
    "s_waitcnt lgkmcnt(6)\n\t"
    "v_cmp_ne_u32 vcc, 0, v76\n\t"
    "s_mov_b32 %[got], -1\n\t"
    "s_cbranch_vccnz 12f\n\t"
    "s_waitcnt lgkmcnt(4)\n\t"
    "v_mfma_f32_16x16x32_bf16 v[80:83], v[128:131], v[96:99], 0\n\t"
    "ds_read_b128 v[120:123], %[da] offset:4096\n\t"
    "v_mfma_f32_16x16x32_bf16 v[84:87], v[132:135], v[96:99], 0\n\t"
    "ds_read_b128 v[124:127], %[da] offset:6144\n\t"
    "v_mfma_f32_16x16x32_bf16 v[88:91], v[128:131], v[100:103], 0\n\t"
    "v_mfma_f32_16x16x32_bf16 v[92:95], v[132:135], v[100:103], 0\n\t"
    "s_waitcnt vmcnt(12)\n\t"
    "s_cmp_lt_u32 %[x], 2\n\t"
    "s_cbranch_scc0 41f\n\t"
    "s_waitcnt vmcnt(4)\n\t"
    "41:\n\t"
    "s_barrier\n\t"
    "s_waitcnt lgkmcnt(4)\n\t"
    "v_mfma_f32_16x16x32_bf16 v[80:83], v[136:139], v[104:107], v[80:83]\n\t"
    "ds_read_b128 v[96:99], %[ea] offset:8192\n\t"
    "v_mfma_f32_16x16x32_bf16 v[84:87], v[140:143], v[104:107], v[84:87]\n\t"
    "ds_read_b128 v[100:103], %[ea] offset:10240\n\t"
    "v_mfma_f32_16x16x32_bf16 v[88:91], v[136:139], v[108:111], v[88:91]\n\t"
    "v_mfma_f32_16x16x32_bf16 v[92:95], v[140:143], v[108:111], v[92:95]\n\t"
    "s_waitcnt lgkmcnt(4)\n\t"
    "v_mfma_f32_16x16x32_bf16 v[80:83], v[144:147], v[112:115], v[80:83]\n\t"
    "ds_read_b128 v[104:107], %[da] offset:8192\n\t"
    "v_mfma_f32_16x16x32_bf16 v[84:87], v[148:151], v[112:115], v[84:87]\n\t"
    "ds_read_b128 v[108:111], %[da] offset:10240\n\t"
    "v_mfma_f32_16x16x32_bf16 v[88:91], v[144:147], v[116:119], v[88:91]\n\t"
    "v_mfma_f32_16x16x32_bf16 v[92:95], v[148:151], v[116:119], v[92:95]\n\t"
    "s_waitcnt lgkmcnt(4)\n\t"
    "v_mfma_f32_16x16x32_bf16 v[80:83], v[152:155], v[120:123], v[80:83]\n\t"
    "ds_read_b128 v[112:115], %[ea] offset:12288\n\t"
    "v_mfma_f32_16x16x32_bf16 v[84:87], v[156:159], v[120:123], v[84:87]\n\t"
    "ds_read_b128 v[116:119], %[ea] offset:14336\n\t"
    "v_mfma_f32_16x16x32_bf16 v[88:91], v[152:155], v[124:127], v[88:91]\n\t"
    "v_mfma_f32_16x16x32_bf16 v[92:95], v[156:159], v[124:127], v[92:95]\n\t"
    "s_waitcnt lgkmcnt(4)\n\t"
    "v_mfma_f32_16x16x32_bf16 v[80:83], v[160:163], v[96:99], v[80:83]\n\t"
    "ds_read_b128 v[120:123], %[da] offset:12288\n\t"
    "v_mfma_f32_16x16x32_bf16 v[84:87], v[164:167], v[96:99], v[84:87]\n\t"
    "ds_read_b128 v[124:127], %[da] offset:14336\n\t"
    "v_mfma_f32_16x16x32_bf16 v[88:91], v[160:163], v[100:103], v[88:91]\n\t"
    "v_mfma_f32_16x16x32_bf16 v[92:95], v[164:167], v[100:103], v[92:95]\n\t"
    "s_waitcnt vmcnt(10)\n\t"
    "s_cmp_lt_u32 %[x], 2\n\t"
    "s_cbranch_scc0 45f\n\t"
    "s_waitcnt vmcnt(2)\n\t"
    "45:\n\t"
    "s_barrier\n\t"
    "s_waitcnt lgkmcnt(4)\n\t"
    "v_mfma_f32_16x16x32_bf16 v[80:83], v[168:171], v[104:107], v[80:83]\n\t"
    "ds_read_b128 v[96:99], %[ea] offset:16384\n\t"
    "v_mfma_f32_16x16x32_bf16 v[84:87], v[172:175], v[104:107], v[84:87]\n\t"
    "ds_read_b128 v[100:103], %[ea] offset:18432\n\t"
    "v_mfma_f32_16x16x32_bf16 v[88:91], v[168:171], v[108:111], v[88:91]\n\t"
    "v_mfma_f32_16x16x32_bf16 v[92:95], v[172:175], v[108:111], v[92:95]\n\t"
    "s_waitcnt lgkmcnt(4)\n\t"
    "v_mfma_f32_16x16x32_bf16 v[80:83], v[176:179], v[112:115], v[80:83]\n\t"
    "ds_read_b128 v[104:107], %[da] offset:16384\n\t"
    "v_mfma_f32_16x16x32_bf16 v[84:87], v[180:183], v[112:115], v[84:87]\n\t"
    "ds_read_b128 v[108:111], %[da] offset:18432\n\t"
    "v_mfma_f32_16x16x32_bf16 v[88:91], v[176:179], v[116:119], v[88:91]\n\t"
    "v_mfma_f32_16x16x32_bf16 v[92:95], v[180:183], v[116:119], v[92:95]\n\t"
    "s_waitcnt lgkmcnt(4)\n\t"
    "v_mfma_f32_16x16x32_bf16 v[80:83], v[184:187], v[120:123], v[80:83]\n\t"
    "ds_read_b128 v[112:115], %[ea] offset:20480\n\t"
    "v_mfma_f32_16x16x32_bf16 v[84:87], v[188:191], v[120:123], v[84:87]\n\t"
    "ds_read_b128 v[116:119], %[ea] offset:22528\n\t"
    "v_mfma_f32_16x16x32_bf16 v[88:91], v[184:187], v[124:127], v[88:91]\n\t"
    "v_mfma_f32_16x16x32_bf16 v[92:95], v[188:191], v[124:127], v[92:95]\n\t"
    "s_waitcnt lgkmcnt(4)\n\t"
    "v_mfma_f32_16x16x32_bf16 v[80:83], v[192:195], v[96:99], v[80:83]\n\t"
    "ds_read_b128 v[120:123], %[da] offset:20480\n\t"
    "v_mfma_f32_16x16x32_bf16 v[84:87], v[196:199], v[96:99], v[84:87]\n\t"
    "ds_read_b128 v[124:127], %[da] offset:22528\n\t"
    "v_mfma_f32_16x16x32_bf16 v[88:91], v[192:195], v[100:103], v[88:91]\n\t"
    "v_mfma_f32_16x16x32_bf16 v[92:95], v[196:199], v[100:103], v[92:95]\n\t"
    "s_waitcnt vmcnt(8)\n\t"
    "s_cmp_lt_u32 %[x], 2\n\t"
    "s_cbranch_scc0 49f\n\t"
    "s_waitcnt vmcnt(0)\n\t"
    "49:\n\t"
    "s_barrier\n\t"
    "s_waitcnt lgkmcnt(4)\n\t"
    "v_mfma_f32_16x16x32_bf16 v[80:83], v[200:203], v[104:107], v[80:83]\n\t"
    "ds_read_b128 v[96:99], %[ea] offset:24576\n\t"
    "v_mfma_f32_16x16x32_bf16 v[84:87], v[204:207], v[104:107], v[84:87]\n\t"
    "ds_read_b128 v[100:103], %[ea] offset:26624\n\t"
    "v_mfma_f32_16x16x32_bf16 v[88:91], v[200:203], v[108:111], v[88:91]\n\t"
    "v_mfma_f32_16x16x32_bf16 v[92:95], v[204:207], v[108:111], v[92:95]\n\t"
    "s_waitcnt lgkmcnt(4)\n\t"
    "v_mfma_f32_16x16x32_bf16 v[80:83], v[208:211], v[112:115], v[80:83]\n\t"
    "ds_read_b128 v[104:107], %[da] offset:24576\n\t"
    "v_mfma_f32_16x16x32_bf16 v[84:87], v[212:215], v[112:115], v[84:87]\n\t"
    "ds_read_b128 v[108:111], %[da] offset:26624\n\t"
    "v_mfma_f32_16x16x32_bf16 v[88:91], v[208:211], v[116:119], v[88:91]\n\t"
    "v_mfma_f32_16x16x32_bf16 v[92:95], v[212:215], v[116:119], v[92:95]\n\t"
    "s_cmp_eq_u32 %[ps], 0\n\t"
    "s_cbranch_scc1 10f\n\t"
    "global_load_dword %[sv], %[dv], %[db] sc1\n\t"
    "global_load_dword %[su], %[dv], %[db] offset:64 sc1\n\t"
    "10:\n\t"
    "s_waitcnt lgkmcnt(4)\n\t"
    "v_mfma_f32_16x16x32_bf16 v[80:83], v[216:219], v[120:123], v[80:83]\n\t"
    "ds_read_b128 v[112:115], %[ea] offset:28672\n\t"
    "v_mfma_f32_16x16x32_bf16 v[84:87], v[220:223], v[120:123], v[84:87]\n\t"
    "ds_read_b128 v[116:119], %[ea] offset:30720\n\t"
    "v_mfma_f32_16x16x32_bf16 v[88:91], v[216:219], v[124:127], v[88:91]\n\t"
    "v_mfma_f32_16x16x32_bf16 v[92:95], v[220:223], v[124:127], v[92:95]\n\t"
    "s_waitcnt lgkmcnt(4)\n\t"
    "v_mfma_f32_16x16x32_bf16 v[80:83], v[224:227], v[96:99], v[80:83]\n\t"
    "ds_read_b128 v[120:123], %[da] offset:28672\n\t"
    "v_mfma_f32_16x16x32_bf16 v[84:87], v[228:231], v[96:99], v[84:87]\n\t"
    "ds_read_b128 v[124:127], %[da] offset:30720\n\t"
    "v_mfma_f32_16x16x32_bf16 v[88:91], v[224:227], v[100:103], v[88:91]\n\t"
    "v_mfma_f32_16x16x32_bf16 v[92:95], v[228:231], v[100:103], v[92:95]\n\t"
    "s_waitcnt lgkmcnt(4)\n\t"
    "v_mfma_f32_16x16x32_bf16 v[80:83], v[232:235], v[104:107], v[80:83]\n\t"
    "v_mfma_f32_16x16x32_bf16 v[84:87], v[236:239], v[104:107], v[84:87]\n\t"
    "v_mfma_f32_16x16x32_bf16 v[88:91], v[232:235], v[108:111], v[88:91]\n\t"
    "v_mfma_f32_16x16x32_bf16 v[92:95], v[236:239], v[108:111], v[92:95]\n\t"
    "s_waitcnt lgkmcnt(2)\n\t"
    "v_mfma_f32_16x16x32_bf16 v[80:83], v[240:243], v[112:115], v[80:83]\n\t"
    "v_mfma_f32_16x16x32_bf16 v[84:87], v[244:247], v[112:115], v[84:87]\n\t"
    "v_mfma_f32_16x16x32_bf16 v[88:91], v[240:243], v[116:119], v[88:91]\n\t"
    "v_mfma_f32_16x16x32_bf16 v[92:95], v[244:247], v[116:119], v[92:95]\n\t"
    "s_waitcnt lgkmcnt(0)\n\t"
    "v_mfma_f32_16x16x32_bf16 v[80:83], v[248:251], v[120:123], v[80:83]\n\t"
    "s_memrealtime s[36:37]\n\t"
    "v_mfma_f32_16x16x32_bf16 v[84:87], v[252:255], v[120:123], v[84:87]\n\t"
    "v_mfma_f32_16x16x32_bf16 v[88:91], v[248:251], v[124:127], v[88:91]\n\t"
    "v_mfma_f32_16x16x32_bf16 v[92:95], v[252:255], v[124:127], v[92:95]\n\t"
    "s_mov_b32 %[got], 0\n\t"
    "v_mov_b32 v76, %[fa]\n\t"
    "v_mov_b32 v77, %[fv]\n\t"
    "v_add_u32 v78, 1, v77\n\t"
    "v_mov_b32 v79, %[ga]\n\t"
    "s_waitcnt lgkmcnt(0)\n\t"
    "ds_write_b128 %[rw], v[80:83] offset:0\n\t"
    "ds_write_b128 %[rw], v[84:87] offset:1024\n\t"
    "ds_write_b128 %[rw], v[88:91] offset:2048\n\t"
    "ds_write_b128 %[rw], v[92:95] offset:3072\n\t"
    "ds_write_b32 v76, v77\n\t"
    "s_cmp_eq_u32 %[ps], 0\n\t"
    "s_cbranch_scc1 9f\n\t"
    "s_waitcnt vmcnt(0)\n\t"
    "v_mad_u32_u24 v76, %[sv], %[mu], %[ad]\n\t"
    "v_cmp_ge_i32 vcc, %[th], v76\n\t"
    "s_cbranch_vccz 11f\n\t"
    "s_sub_u32 s36, s36, 20\n\t"
    "v_sub_u32 v76, s36, %[su]\n\t"
    "v_lshrrev_b32 v77, 24, %[su]\n\t"
    "v_lshlrev_b32 v76, 8, v76\n\t"
    "v_ashrrev_i32 v76, 31, v76\n\t"
    "v_add_u32 v77, v77, v76\n\t"
    "v_cmp_lt_i32 s[36:37], %[tb], v77\n\t"
    "s_andn2_b64 vcc, vcc, s[36:37]\n\t"
    "s_cbranch_scc1 9f\n\t"
    "11:\n\t"
    "ds_write2_b32 v79, v78, v78 offset1:1\n\t"
    "s_add_u32 m0, %[lb], 0\n\t"
    "s_nop 0\n\t"
    "global_load_lds_dwordx4 %[av0], %[s0] sc1\n\t"
    "s_add_u32 m0, %[lb], 4096\n\t"
    "s_nop 0\n\t"
    "global_load_lds_dwordx4 %[av2], %[s0] sc1\n\t"
    "s_add_u32 m0, %[lb], 8192\n\t"
    "s_nop 0\n\t"
    "global_load_lds_dwordx4 %[av4], %[s0] sc1\n\t"
    "s_add_u32 m0, %[lb], 12288\n\t"
    "s_nop 0\n\t"
    "global_load_lds_dwordx4 %[av6], %[s0] sc1\n\t"
    "s_add_u32 m0, %[lb], 16384\n\t"
    "s_nop 0\n\t"
    "global_load_lds_dwordx4 %[av0], %[s1] sc1\n\t"
    "s_add_u32 m0, %[lb], 20480\n\t"
    "s_nop 0\n\t"
    "global_load_lds_dwordx4 %[av2], %[s1] sc1\n\t"
    "s_add_u32 m0, %[lb], 24576\n\t"
    "s_nop 0\n\t"
    "global_load_lds_dwordx4 %[av4], %[s1] sc1\n\t"
    "s_add_u32 m0, %[lb], 28672\n\t"
    "s_nop 0\n\t"
    "global_load_lds_dwordx4 %[av6], %[s1] sc1\n\t"
    "s_mov_b32 %[got], 1\n\t"
    "s_branch 9f\n\t"
    "12:\n\t"
    "s_waitcnt lgkmcnt(0)\n\t"
    "9:"
    : [sv] "+&v"(sv), [su] "+&v"(su), [got] "=&s"(got)
    : [ea] "v"(ea), [da] "v"(da), [x] "s"(x), "{v[128:159]}"(W[0]), "{v[160:191]}"(W[1]), "{v[192:223]}"(W[2]), "{v[224:255]}"(W[3]), [lb] "s"(lb), [s0] "s"(s0), [s1] "s"(s1), [av0] "v"(av[0]), [av1] "v"(av[1]), [av2] "v"(av[2]), [av3] "v"(av[3]), [av4] "v"(av[4]), [av5] "v"(av[5]), [av6] "v"(av[6]), [av7] "v"(av[7]), [ps] "s"(ps), [dv] "v"(dv), [db] "s"(db), [rw] "v"(rw), [fa] "s"(fa), [fv] "s"(fv), [ga] "s"(ga), [mu] "s"(mu), [ad] "v"(ad), [th] "s"(th), [tb] "s"(tb)
    : "v96", "v97", "v98", "v99", "v100", "v101", "v102", "v103", "v104", "v105", "v106", "v107", "v108", "v109", "v110", "v111", "v112", "v113", "v114", "v115", "v116", "v117", "v118", "v119", "v120", "v121", "v122", "v123", "v124", "v125", "v126", "v127", "v76", "v77", "v78", "v79", "v80", "v81", "v82", "v83", "v84", "v85", "v86", "v87", "v88", "v89", "v90", "v91", "v92", "v93", "v94", "v95", "vcc", "scc", "m0", "memory", "s36", "s37", "s38", "s39");
}
__device__ __forceinline__ void lean_dm1(const u32x32 (&W)[4], unsigned ea, unsigned da, unsigned x, unsigned lb, const char* s0, const char* s1, const unsigned (&av)[8], unsigned ps, unsigned dv, const char* db, unsigned rw, unsigned fa, unsigned fv,
    unsigned ga, unsigned mu, unsigned ad, int th, int tb, unsigned& sv, unsigned& su, unsigned& got) {
  asm volatile("s_setprio 2\n\t"
    "v_mov_b32 v76, %[ga]\n\t"
    "ds_read_b32 v76, v76 offset:24\n\t"
    "ds_read_b128 v[96:99], %[ea] offset:0\n\t"
    "ds_read_b128 v[100:103], %[ea] offset:2048\n\t"
    "ds_read_b128 v[104:107], %[da] offset:0\n\t"
    "ds_read_b128 v[108:111], %[da] offset:2048\n\t"
    "ds_read_b128 v[112:115], %[ea] offset:4096\n\t"
    "ds_read_b128 v[116:119], %[ea] offset:6144\n\t"
    "s_waitcnt lgkmcnt(6)\n\t"
    "v_cmp_ne_u32 vcc, 0, v76\n\t"
    "s_mov_b32 %[got], -1\n\t"
    "s_cbranch_vccnz 12f\n\t"
    "s_waitcnt lgkmcnt(4)\n\t"
    "v_mfma_f32_16x16x32_bf16 v[80:83], v[128:131], v[96:99], 0\n\t"
    "ds_read_b128 v[120:123], %[da] offset:4096\n\t"
    "s_add_u32 m0, %[lb], 0\n\t"
    "v_mfma_f32_16x16x32_bf16 v[84:87], v[132:135], v[96:99], 0\n\t"
    "ds_read_b128 v[124:127], %[da] offset:6144\n\t"
    "global_load_lds_dwordx4 %[av0], %[s0] sc1\n\t"
    "v_mfma_f32_16x16x32_bf16 v[88:91], v[128:131], v[100:103], 0\n\t"
    "v_mfma_f32_16x16x32_bf16 v[92:95], v[132:135], v[100:103], 0\n\t"
    "s_waitcnt vmcnt(5)\n\t"
    "s_barrier\n\t"
    "s_waitcnt lgkmcnt(4)\n\t"
    "v_mfma_f32_16x16x32_bf16 v[80:83], v[136:139], v[104:107], v[80:83]\n\t"
    "ds_read_b128 v[96:99], %[ea] offset:8192\n\t"
    "s_add_u32 m0, %[lb], 4096\n\t"
    "v_mfma_f32_16x16x32_bf16 v[84:87], v[140:143], v[104:107], v[84:87]\n\t"
    "ds_read_b128 v[100:103], %[ea] offset:10240\n\t"
    "global_load_lds_dwordx4 %[av2], %[s0] sc1\n\t"
    "v_mfma_f32_16x16x32_bf16 v[88:91], v[136:139], v[108:111], v[88:91]\n\t"
    "v_mfma_f32_16x16x32_bf16 v[92:95], v[140:143], v[108:111], v[92:95]\n\t"
    "s_waitcnt lgkmcnt(4)\n\t"
    "v_mfma_f32_16x16x32_bf16 v[80:83], v[144:147], v[112:115], v[80:83]\n\t"
    "ds_read_b128 v[104:107], %[da] offset:8192\n\t"
    "s_add_u32 m0, %[lb], 8192\n\t"
    "v_mfma_f32_16x16x32_bf16 v[84:87], v[148:151], v[112:115], v[84:87]\n\t"
    "ds_read_b128 v[108:111], %[da] offset:10240\n\t"
    "global_load_lds_dwordx4 %[av4], %[s0] sc1\n\t"
    "v_mfma_f32_16x16x32_bf16 v[88:91], v[144:147], v[116:119], v[88:91]\n\t"
    "v_mfma_f32_16x16x32_bf16 v[92:95], v[148:151], v[116:119], v[92:95]\n\t"
    "s_waitcnt lgkmcnt(4)\n\t"
    "v_mfma_f32_16x16x32_bf16 v[80:83], v[152:155], v[120:123], v[80:83]\n\t"
    "ds_read_b128 v[112:115], %[ea] offset:12288\n\t"
    "s_add_u32 m0, %[lb], 12288\n\t"
    "v_mfma_f32_16x16x32_bf16 v[84:87], v[156:159], v[120:123], v[84:87]\n\t"
    "ds_read_b128 v[116:119], %[ea] offset:14336\n\t"
    "global_load_lds_dwordx4 %[av6], %[s0] sc1\n\t"
    "v_mfma_f32_16x16x32_bf16 v[88:91], v[152:155], v[124:127], v[88:91]\n\t"
    "v_mfma_f32_16x16x32_bf16 v[92:95], v[156:159], v[124:127], v[92:95]\n\t"
    "s_waitcnt lgkmcnt(4)\n\t"
    "v_mfma_f32_16x16x32_bf16 v[80:83], v[160:163], v[96:99], v[80:83]\n\t"
    "ds_read_b128 v[120:123], %[da] offset:12288\n\t"
    "s_add_u32 m0, %[lb], 16384\n\t"
    "v_mfma_f32_16x16x32_bf16 v[84:87], v[164:167], v[96:99], v[84:87]\n\t"
    "ds_read_b128 v[124:127], %[da] offset:14336\n\t"
    "global_load_lds_dwordx4 %[av0], %[s1] sc1\n\t"
    "v_mfma_f32_16x16x32_bf16 v[88:91], v[160:163], v[100:103], v[88:91]\n\t"
    "v_mfma_f32_16x16x32_bf16 v[92:95], v[164:167], v[100:103], v[92:95]\n\t"
    "s_waitcnt vmcnt(7)\n\t"
    "s_barrier\n\t"
    "s_waitcnt lgkmcnt(4)\n\t"
    "v_mfma_f32_16x16x32_bf16 v[80:83], v[168:171], v[104:107], v[80:83]\n\t"
    "ds_read_b128 v[96:99], %[ea] offset:16384\n\t"
    "s_add_u32 m0, %[lb], 20480\n\t"
    "v_mfma_f32_16x16x32_bf16 v[84:87], v[172:175], v[104:107], v[84:87]\n\t"
    "ds_read_b128 v[100:103], %[ea] offset:18432\n\t"
    "global_load_lds_dwordx4 %[av2], %[s1] sc1\n\t"
    "v_mfma_f32_16x16x32_bf16 v[88:91], v[168:171], v[108:111], v[88:91]\n\t"
    "v_mfma_f32_16x16x32_bf16 v[92:95], v[172:175], v[108:111], v[92:95]\n\t"
    "s_waitcnt lgkmcnt(4)\n\t"
    "v_mfma_f32_16x16x32_bf16 v[80:83], v[176:179], v[112:115], v[80:83]\n\t"
    "ds_read_b128 v[104:107], %[da] offset:16384\n\t"
    "s_add_u32 m0, %[lb], 24576\n\t"
    "v_mfma_f32_16x16x32_bf16 v[84:87], v[180:183], v[112:115], v[84:87]\n\t"
    "ds_read_b128 v[108:111], %[da] offset:18432\n\t"
    "global_load_lds_dwordx4 %[av4], %[s1] sc1\n\t"
    "v_mfma_f32_16x16x32_bf16 v[88:91], v[176:179], v[116:119], v[88:91]\n\t"
    "v_mfma_f32_16x16x32_bf16 v[92:95], v[180:183], v[116:119], v[92:95]\n\t"
    "s_waitcnt lgkmcnt(4)\n\t"
    "v_mfma_f32_16x16x32_bf16 v[80:83], v[184:187], v[120:123], v[80:83]\n\t"
    "ds_read_b128 v[112:115], %[ea] offset:20480\n\t"
    "s_add_u32 m0, %[lb], 28672\n\t"
    "v_mfma_f32_16x16x32_bf16 v[84:87], v[188:191], v[120:123], v[84:87]\n\t"
    "ds_read_b128 v[116:119], %[ea] offset:22528\n\t"
    "global_load_lds_dwordx4 %[av6], %[s1] sc1\n\t"
    "v_mfma_f32_16x16x32_bf16 v[88:91], v[184:187], v[124:127], v[88:91]\n\t"
    "v_mfma_f32_16x16x32_bf16 v[92:95], v[188:191], v[124:127], v[92:95]\n\t"
    "s_waitcnt lgkmcnt(4)\n\t"
    "v_mfma_f32_16x16x32_bf16 v[80:83], v[192:195], v[96:99], v[80:83]\n\t"
    "ds_read_b128 v[120:123], %[da] offset:20480\n\t"
    "v_mfma_f32_16x16x32_bf16 v[84:87], v[196:199], v[96:99], v[84:87]\n\t"
    "ds_read_b128 v[124:127], %[da] offset:22528\n\t"
    "v_mfma_f32_16x16x32_bf16 v[88:91], v[192:195], v[100:103], v[88:91]\n\t"
    "v_mfma_f32_16x16x32_bf16 v[92:95], v[196:199], v[100:103], v[92:95]\n\t"
    "s_waitcnt vmcnt(8)\n\t"
    "s_barrier\n\t"
    "s_waitcnt lgkmcnt(4)\n\t"
    "v_mfma_f32_16x16x32_bf16 v[80:83], v[200:203], v[104:107], v[80:83]\n\t"
    "ds_read_b128 v[96:99], %[ea] offset:24576\n\t"
    "v_mfma_f32_16x16x32_bf16 v[84:87], v[204:207], v[104:107], v[84:87]\n\t"
    "ds_read_b128 v[100:103], %[ea] offset:26624\n\t"
    "v_mfma_f32_16x16x32_bf16 v[88:91], v[200:203], v[108:111], v[88:91]\n\t"
    "v_mfma_f32_16x16x32_bf16 v[92:95], v[204:207], v[108:111], v[92:95]\n\t"
    "s_waitcnt lgkmcnt(4)\n\t"
    "v_mfma_f32_16x16x32_bf16 v[80:83], v[208:211], v[112:115], v[80:83]\n\t"
    "ds_read_b128 v[104:107], %[da] offset:24576\n\t"
    "v_mfma_f32_16x16x32_bf16 v[84:87], v[212:215], v[112:115], v[84:87]\n\t"
    "ds_read_b128 v[108:111], %[da] offset:26624\n\t"
    "v_mfma_f32_16x16x32_bf16 v[88:91], v[208:211], v[116:119], v[88:91]\n\t"
    "v_mfma_f32_16x16x32_bf16 v[92:95], v[212:215], v[116:119], v[92:95]\n\t"
    "s_waitcnt lgkmcnt(4)\n\t"
    "v_mfma_f32_16x16x32_bf16 v[80:83], v[216:219], v[120:123], v[80:83]\n\t"
    "ds_read_b128 v[112:115], %[ea] offset:28672\n\t"
    "v_mfma_f32_16x16x32_bf16 v[84:87], v[220:223], v[120:123], v[84:87]\n\t"
    "ds_read_b128 v[116:119], %[ea] offset:30720\n\t"
    "v_mfma_f32_16x16x32_bf16 v[88:91], v[216:219], v[124:127], v[88:91]\n\t"
    "v_mfma_f32_16x16x32_bf16 v[92:95], v[220:223], v[124:127], v[92:95]\n\t"
    "s_waitcnt lgkmcnt(4)\n\t"
    "v_mfma_f32_16x16x32_bf16 v[80:83], v[224:227], v[96:99], v[80:83]\n\t"
    "ds_read_b128 v[120:123], %[da] offset:28672\n\t"
    "v_mfma_f32_16x16x32_bf16 v[84:87], v[228:231], v[96:99], v[84:87]\n\t"
    "ds_read_b128 v[124:127], %[da] offset:30720\n\t"
    "v_mfma_f32_16x16x32_bf16 v[88:91], v[224:227], v[100:103], v[88:91]\n\t"
    "v_mfma_f32_16x16x32_bf16 v[92:95], v[228:231], v[100:103], v[92:95]\n\t"
    "s_waitcnt lgkmcnt(4)\n\t"
    "v_mfma_f32_16x16x32_bf16 v[80:83], v[232:235], v[104:107], v[80:83]\n\t"
    "v_mfma_f32_16x16x32_bf16 v[84:87], v[236:239], v[104:107], v[84:87]\n\t"
    "v_mfma_f32_16x16x32_bf16 v[88:91], v[232:235], v[108:111], v[88:91]\n\t"
    "v_mfma_f32_16x16x32_bf16 v[92:95], v[236:239], v[108:111], v[92:95]\n\t"
    "s_waitcnt lgkmcnt(2)\n\t"
    "v_mfma_f32_16x16x32_bf16 v[80:83], v[240:243], v[112:115], v[80:83]\n\t"
    "v_mfma_f32_16x16x32_bf16 v[84:87], v[244:247], v[112:115], v[84:87]\n\t"
    "v_mfma_f32_16x16x32_bf16 v[88:91], v[240:243], v[116:119], v[88:91]\n\t"
    "v_mfma_f32_16x16x32_bf16 v[92:95], v[244:247], v[116:119], v[92:95]\n\t"
    "s_waitcnt lgkmcnt(0)\n\t"
    "v_mfma_f32_16x16x32_bf16 v[80:83], v[248:251], v[120:123], v[80:83]\n\t"
    "v_mfma_f32_16x16x32_bf16 v[84:87], v[252:255], v[120:123], v[84:87]\n\t"
    "v_mfma_f32_16x16x32_bf16 v[88:91], v[248:251], v[124:127], v[88:91]\n\t"
    "v_mfma_f32_16x16x32_bf16 v[92:95], v[252:255], v[124:127], v[92:95]\n\t"
    "s_cmp_eq_u32 %[ps], 0\n\t"
    "s_cbranch_scc1 10f\n\t"
    "global_load_dword %[sv], %[dv], %[db] sc1\n\t"
    "global_load_dword %[su], %[dv], %[db] offset:64 sc1\n\t"
    "10:\n\t"
    "s_mov_b32 %[got], 0\n\t"
    "v_mov_b32 v76, %[fa]\n\t"
    "v_mov_b32 v77, %[fv]\n\t"
    "v_add_u32 v78, 1, v77\n\t"
    "v_mov_b32 v79, %[ga]\n\t"
    "ds_write_b128 %[rw], v[80:83] offset:0\n\t"
    "ds_write_b128 %[rw], v[84:87] offset:1024\n\t"
    "ds_write_b128 %[rw], v[88:91] offset:2048\n\t"
    "ds_write_b128 %[rw], v[92:95] offset:3072\n\t"
    "ds_write_b32 v76, v77\n\t"
    "s_cmp_eq_u32 %[ps], 0\n\t"
    "s_cbranch_scc1 9f\n\t"
    "s_waitcnt vmcnt(0)\n\t"
    "s_branch 9f\n\t"
    "12:\n\t"
    "s_waitcnt lgkmcnt(0)\n\t"
    "9:"
    : [sv] "+&v"(sv), [su] "+&v"(su), [got] "=&s"(got)
    : [ea] "v"(ea), [da] "v"(da), [x] "s"(x), "{v[128:159]}"(W[0]), "{v[160:191]}"(W[1]), "{v[192:223]}"(W[2]), "{v[224:255]}"(W[3]), [lb] "s"(lb), [s0] "s"(s0), [s1] "s"(s1), [av0] "v"(av[0]), [av1] "v"(av[1]), [av2] "v"(av[2]), [av3] "v"(av[3]), [av4] "v"(av[4]), [av5] "v"(av[5]), [av6] "v"(av[6]), [av7] "v"(av[7]), [ps] "s"(ps), [dv] "v"(dv), [db] "s"(db), [rw] "v"(rw), [fa] "s"(fa), [fv] "s"(fv), [ga] "s"(ga), [mu] "s"(mu), [ad] "v"(ad), [th] "s"(th), [tb] "s"(tb)
    : "v96", "v97", "v98", "v99", "v100", "v101", "v102", "v103", "v104", "v105", "v106", "v107", "v108", "v109", "v110", "v111", "v112", "v113", "v114", "v115", "v116", "v117", "v118", "v119", "v120", "v121", "v122", "v123", "v124", "v125", "v126", "v127", "v76", "v77", "v78", "v79", "v80", "v81", "v82", "v83", "v84", "v85", "v86", "v87", "v88", "v89", "v90", "v91", "v92", "v93", "v94", "v95", "vcc", "scc", "m0", "memory");
}
__device__ __forceinline__ void lean_st0(const u32x32 (&W)[4], unsigned ea, unsigned da, unsigned x, u32x16& pd, unsigned pv, unsigned cn, const char* pg, unsigned bl, unsigned& tk, unsigned& st) {
  asm volatile("v_mov_b32 v76, %[cn]\n\t"
    "ds_read_b32 v76, v76 offset:28\n\t"
    "ds_read_b128 v[96:99], %[ea] offset:0\n\t"
    "ds_read_b128 v[100:103], %[ea] offset:2048\n\t"
    "ds_read_b128 v[104:107], %[da] offset:0\n\t"
    "ds_read_b128 v[108:111], %[da] offset:2048\n\t"
    "ds_read_b128 v[112:115], %[ea] offset:4096\n\t"
    "ds_read_b128 v[116:119], %[ea] offset:6144\n\t"
    "s_waitcnt lgkmcnt(6)\n\t"
    "v_cmp_ne_u32 vcc, 0, v76\n\t"
    "s_mov_b32 %[st], -1\n\t"
    "s_cbranch_vccnz 12f\n\t"
    "s_waitcnt lgkmcnt(4)\n\t"
    "v_mfma_f32_16x16x32_bf16 v[80:83], v[128:131], v[96:99], 0\n\t"
    "ds_read_b128 v[120:123], %[da] offset:4096\n\t"
    "v_mfma_f32_16x16x32_bf16 v[84:87], v[132:135], v[96:99], 0\n\t"
    "ds_read_b128 v[124:127], %[da] offset:6144\n\t"
    "v_mfma_f32_16x16x32_bf16 v[88:91], v[128:131], v[100:103], 0\n\t"
    "v_mfma_f32_16x16x32_bf16 v[92:95], v[132:135], v[100:103], 0\n\t"
    "s_barrier\n\t"
    "s_bitcmp1_b32 %[x], 2\n\t"
    "s_cbranch_scc1 7f\n\t"
    "8:\n\t"
    "s_bitcmp1_b32 %[x], 3\n\t"
    "s_cbranch_scc1 13f\n\t"
    "14:\n\t"
    "s_waitcnt lgkmcnt(4)\n\t"
    "v_mfma_f32_16x16x32_bf16 v[80:83], v[136:139], v[104:107], v[80:83]\n\t"
    "ds_read_b128 v[96:99], %[ea] offset:8192\n\t"
    "v_mfma_f32_16x16x32_bf16 v[84:87], v[140:143], v[104:107], v[84:87]\n\t"
    "ds_read_b128 v[100:103], %[ea] offset:10240\n\t"
    "v_mfma_f32_16x16x32_bf16 v[88:91], v[136:139], v[108:111], v[88:91]\n\t"
    "v_mfma_f32_16x16x32_bf16 v[92:95], v[140:143], v[108:111], v[92:95]\n\t"
    "s_waitcnt lgkmcnt(4)\n\t"
    "v_mfma_f32_16x16x32_bf16 v[80:83], v[144:147], v[112:115], v[80:83]\n\t"
    "ds_read_b128 v[104:107], %[da] offset:8192\n\t"
    "v_mfma_f32_16x16x32_bf16 v[84:87], v[148:151], v[112:115], v[84:87]\n\t"
    "ds_read_b128 v[108:111], %[da] offset:10240\n\t"
    "v_mfma_f32_16x16x32_bf16 v[88:91], v[144:147], v[116:119], v[88:91]\n\t"
    "v_mfma_f32_16x16x32_bf16 v[92:95], v[148:151], v[116:119], v[92:95]\n\t"
    "s_waitcnt lgkmcnt(4)\n\t"
    "v_mfma_f32_16x16x32_bf16 v[80:83], v[152:155], v[120:123], v[80:83]\n\t"
    "ds_read_b128 v[112:115], %[ea] offset:12288\n\t"
    "v_mfma_f32_16x16x32_bf16 v[84:87], v[156:159], v[120:123], v[84:87]\n\t"
    "ds_read_b128 v[116:119], %[ea] offset:14336\n\t"
    "v_mfma_f32_16x16x32_bf16 v[88:91], v[152:155], v[124:127], v[88:91]\n\t"
    "v_mfma_f32_16x16x32_bf16 v[92:95], v[156:159], v[124:127], v[92:95]\n\t"
    "s_waitcnt lgkmcnt(4)\n\t"
    "v_mfma_f32_16x16x32_bf16 v[80:83], v[160:163], v[96:99], v[80:83]\n\t"
    "ds_read_b128 v[120:123], %[da] offset:12288\n\t"
    "v_mfma_f32_16x16x32_bf16 v[84:87], v[164:167], v[96:99], v[84:87]\n\t"
    "ds_read_b128 v[124:127], %[da] offset:14336\n\t"
    "v_mfma_f32_16x16x32_bf16 v[88:91], v[160:163], v[100:103], v[88:91]\n\t"
    "v_mfma_f32_16x16x32_bf16 v[92:95], v[164:167], v[100:103], v[92:95]\n\t"
    "s_barrier\n\t"
    "s_waitcnt lgkmcnt(4)\n\t"
    "v_mfma_f32_16x16x32_bf16 v[80:83], v[168:171], v[104:107], v[80:83]\n\t"
    "ds_read_b128 v[96:99], %[ea] offset:16384\n\t"
    "v_mfma_f32_16x16x32_bf16 v[84:87], v[172:175], v[104:107], v[84:87]\n\t"
    "ds_read_b128 v[100:103], %[ea] offset:18432\n\t"
    "v_mfma_f32_16x16x32_bf16 v[88:91], v[168:171], v[108:111], v[88:91]\n\t"
    "v_mfma_f32_16x16x32_bf16 v[92:95], v[172:175], v[108:111], v[92:95]\n\t"
    "s_waitcnt lgkmcnt(4)\n\t"
    "v_mfma_f32_16x16x32_bf16 v[80:83], v[176:179], v[112:115], v[80:83]\n\t"
    "ds_read_b128 v[104:107], %[da] offset:16384\n\t"
    "v_mfma_f32_16x16x32_bf16 v[84:87], v[180:183], v[112:115], v[84:87]\n\t"
    "ds_read_b128 v[108:111], %[da] offset:18432\n\t"
    "v_mfma_f32_16x16x32_bf16 v[88:91], v[176:179], v[116:119], v[88:91]\n\t"
    "v_mfma_f32_16x16x32_bf16 v[92:95], v[180:183], v[116:119], v[92:95]\n\t"
    "s_waitcnt lgkmcnt(4)\n\t"
    "v_mfma_f32_16x16x32_bf16 v[80:83], v[184:187], v[120:123], v[80:83]\n\t"
    "ds_read_b128 v[112:115], %[ea] offset:20480\n\t"
    "v_mfma_f32_16x16x32_bf16 v[84:87], v[188:191], v[120:123], v[84:87]\n\t"
    "ds_read_b128 v[116:119], %[ea] offset:22528\n\t"
    "v_mfma_f32_16x16x32_bf16 v[88:91], v[184:187], v[124:127], v[88:91]\n\t"
    "v_mfma_f32_16x16x32_bf16 v[92:95], v[188:191], v[124:127], v[92:95]\n\t"
    "s_waitcnt lgkmcnt(4)\n\t"
    "v_mfma_f32_16x16x32_bf16 v[80:83], v[192:195], v[96:99], v[80:83]\n\t"
    "ds_read_b128 v[120:123], %[da] offset:20480\n\t"
    "v_mfma_f32_16x16x32_bf16 v[84:87], v[196:199], v[96:99], v[84:87]\n\t"
    "ds_read_b128 v[124:127], %[da] offset:22528\n\t"
    "v_mfma_f32_16x16x32_bf16 v[88:91], v[192:195], v[100:103], v[88:91]\n\t"
    "v_mfma_f32_16x16x32_bf16 v[92:95], v[196:199], v[100:103], v[92:95]\n\t"
    "s_barrier\n\t"
    "s_and_b32 %[st], %[x], 3\n\t"
    "s_cbranch_scc1 3f\n\t"
    "4:\n\t"
    "s_waitcnt lgkmcnt(4)\n\t"
    "v_mfma_f32_16x16x32_bf16 v[80:83], v[200:203], v[104:107], v[80:83]\n\t"
    "ds_read_b128 v[96:99], %[ea] offset:24576\n\t"
    "v_mfma_f32_16x16x32_bf16 v[84:87], v[204:207], v[104:107], v[84:87]\n\t"
    "ds_read_b128 v[100:103], %[ea] offset:26624\n\t"
    "v_mfma_f32_16x16x32_bf16 v[88:91], v[200:203], v[108:111], v[88:91]\n\t"
    "v_mfma_f32_16x16x32_bf16 v[92:95], v[204:207], v[108:111], v[92:95]\n\t"
    "s_waitcnt lgkmcnt(4)\n\t"
    "v_mfma_f32_16x16x32_bf16 v[80:83], v[208:211], v[112:115], v[80:83]\n\t"
    "ds_read_b128 v[104:107], %[da] offset:24576\n\t"
    "v_mfma_f32_16x16x32_bf16 v[84:87], v[212:215], v[112:115], v[84:87]\n\t"
    "ds_read_b128 v[108:111], %[da] offset:26624\n\t"
    "v_mfma_f32_16x16x32_bf16 v[88:91], v[208:211], v[116:119], v[88:91]\n\t"
    "v_mfma_f32_16x16x32_bf16 v[92:95], v[212:215], v[116:119], v[92:95]\n\t"
    "s_waitcnt lgkmcnt(4)\n\t"
    "v_mfma_f32_16x16x32_bf16 v[80:83], v[216:219], v[120:123], v[80:83]\n\t"
    "ds_read_b128 v[112:115], %[ea] offset:28672\n\t"
    "v_mfma_f32_16x16x32_bf16 v[84:87], v[220:223], v[120:123], v[84:87]\n\t"
    "ds_read_b128 v[116:119], %[ea] offset:30720\n\t"
    "v_mfma_f32_16x16x32_bf16 v[88:91], v[216:219], v[124:127], v[88:91]\n\t"
    "v_mfma_f32_16x16x32_bf16 v[92:95], v[220:223], v[124:127], v[92:95]\n\t"
    "s_waitcnt lgkmcnt(4)\n\t"
    "v_mfma_f32_16x16x32_bf16 v[80:83], v[224:227], v[96:99], v[80:83]\n\t"
    "ds_read_b128 v[120:123], %[da] offset:28672\n\t"
    "v_mfma_f32_16x16x32_bf16 v[84:87], v[228:231], v[96:99], v[84:87]\n\t"
    "ds_read_b128 v[124:127], %[da] offset:30720\n\t"
    "v_mfma_f32_16x16x32_bf16 v[88:91], v[224:227], v[100:103], v[88:91]\n\t"
    "v_mfma_f32_16x16x32_bf16 v[92:95], v[228:231], v[100:103], v[92:95]\n\t"
    "s_waitcnt lgkmcnt(4)\n\t"
    "v_mfma_f32_16x16x32_bf16 v[80:83], v[232:235], v[104:107], v[80:83]\n\t"
    "v_mfma_f32_16x16x32_bf16 v[84:87], v[236:239], v[104:107], v[84:87]\n\t"
    "v_mfma_f32_16x16x32_bf16 v[88:91], v[232:235], v[108:111], v[88:91]\n\t"
    "v_mfma_f32_16x16x32_bf16 v[92:95], v[236:239], v[108:111], v[92:95]\n\t"
    "s_waitcnt lgkmcnt(2)\n\t"
    "v_mfma_f32_16x16x32_bf16 v[80:83], v[240:243], v[112:115], v[80:83]\n\t"
    "v_mfma_f32_16x16x32_bf16 v[84:87], v[244:247], v[112:115], v[84:87]\n\t"
    "v_mfma_f32_16x16x32_bf16 v[88:91], v[240:243], v[116:119], v[88:91]\n\t"
    "v_mfma_f32_16x16x32_bf16 v[92:95], v[244:247], v[116:119], v[92:95]\n\t"
    "s_waitcnt lgkmcnt(0)\n\t"
    "v_mfma_f32_16x16x32_bf16 v[80:83], v[248:251], v[120:123], v[80:83]\n\t"
    "v_mfma_f32_16x16x32_bf16 v[84:87], v[252:255], v[120:123], v[84:87]\n\t"
    "v_mfma_f32_16x16x32_bf16 v[88:91], v[248:251], v[124:127], v[88:91]\n\t"
    "v_mfma_f32_16x16x32_bf16 v[92:95], v[252:255], v[124:127], v[92:95]\n\t"
    "s_mov_b32 %[st], 0\n\t"
    "s_nop 5\n\t"
    "s_branch 9f\n\t"
    "13:\n\t"
    "s_memrealtime s[36:37]\n\t"
    "v_mov_b32 v79, 0\n\t"
    "s_waitcnt lgkmcnt(0)\n\t"
    "s_mov_b32 %[tk], s36\n\t"
    "s_add_u32 s36, s36, %[bl]\n\t"
    "s_and_b32 s36, s36, 0xffffff\n\t"
    "s_and_b32 s37, %[bl], 0xff000000\n\t"
    "s_or_b32 s36, s36, s37\n\t"
    "v_mov_b32 v78, s36\n\t"
    "s_mov_b64 exec, 1\n\t"
    "global_store_dword v79, v78, %[pg] offset:64 sc1\n\t"
    "s_mov_b64 exec, -1\n\t"
    "s_branch 14b\n\t"
    "3:\n\t"
    "7:\n\t"
    "s_and_b32 %[st], %[pv], 1\n\t"
    "s_mul_i32 %[st], %[st], 36\n\t"
    "s_add_u32 %[st], %[st], %[cn]\n\t"
    "v_mov_b32 v76, %[st]\n\t"
    "v_mov_b32 v77, 1\n\t"
    "v_mov_b32 v78, %[pv]\n\t"
    "v_mov_b32 v79, 0\n\t"
    "s_waitcnt vmcnt(0)\n\t"
    "s_mov_b64 exec, 1\n\t"
    "ds_add_rtn_u32 v77, v76, v77\n\t"
    "s_waitcnt lgkmcnt(0)\n\t"
    "v_readfirstlane_b32 %[st], v77\n\t"
    "s_and_b32 %[st], %[st], 3\n\t"
    "s_cmp_lg_u32 %[st], 3\n\t"
    "s_cbranch_scc1 6f\n\t"
    "global_store_dword v79, v78, %[pg] sc1\n\t"
    "6:\n\t"
    "s_mov_b64 exec, -1\n\t"
    "s_bitcmp1_b32 %[x], 2\n\t"
    "s_cbranch_scc1 8b\n\t"
    "s_branch 4b\n\t"
    "12:\n\t"
    "s_waitcnt lgkmcnt(0)\n\t"
    "9:"
    : "=&{v[80:95]}"(pd), [st] "=&s"(st), [tk] "+&s"(tk)
    : [ea] "v"(ea), [da] "v"(da), [x] "s"(x), "{v[128:159]}"(W[0]), "{v[160:191]}"(W[1]), "{v[192:223]}"(W[2]), "{v[224:255]}"(W[3]), [pv] "s"(pv), [cn] "s"(cn), [pg] "s"(pg), [bl] "s"(bl)
    : "v96", "v97", "v98", "v99", "v100", "v101", "v102", "v103", "v104", "v105", "v106", "v107", "v108", "v109", "v110", "v111", "v112", "v113", "v114", "v115", "v116", "v117", "v118", "v119", "v120", "v121", "v122", "v123", "v124", "v125", "v126", "v127", "v76", "v77", "v78", "v79", "vcc", "scc", "m0", "memory", "s36", "s37", "s38", "s39");
}
__device__ __forceinline__ void lean_ep0(const u32x32 (&W)[4], unsigned ea, unsigned da, unsigned x, unsigned pv, unsigned cn, const char* pg, unsigned bl, unsigned& tk, unsigned& st,
    unsigned rw, unsigned fa, unsigned fv, const char* cb, unsigned co0, unsigned co1, unsigned& bad, unsigned& pk) {
  asm volatile("v_mov_b32 v76, %[cn]\n\t"
    "ds_read_b32 v76, v76 offset:28\n\t"
    "ds_read_b128 v[96:99], %[ea] offset:0\n\t"
    "ds_read_b128 v[100:103], %[ea] offset:2048\n\t"
    "ds_read_b128 v[104:107], %[da] offset:0\n\t"
    "ds_read_b128 v[108:111], %[da] offset:2048\n\t"
    "ds_read_b128 v[112:115], %[ea] offset:4096\n\t"
    "ds_read_b128 v[116:119], %[ea] offset:6144\n\t"
    "s_waitcnt lgkmcnt(6)\n\t"
    "v_cmp_ne_u32 vcc, 0, v76\n\t"
    "s_mov_b32 %[st], -1\n\t"
    "s_cbranch_vccnz 12f\n\t"
    "s_waitcnt lgkmcnt(4)\n\t"
    "v_mfma_f32_16x16x32_bf16 v[80:83], v[128:131], v[96:99], 0\n\t"
    "ds_read_b128 v[120:123], %[da] offset:4096\n\t"
    "v_mfma_f32_16x16x32_bf16 v[84:87], v[132:135], v[96:99], 0\n\t"
    "ds_read_b128 v[124:127], %[da] offset:6144\n\t"
    "v_mfma_f32_16x16x32_bf16 v[88:91], v[128:131], v[100:103], 0\n\t"
    "v_mfma_f32_16x16x32_bf16 v[92:95], v[132:135], v[100:103], 0\n\t"
    "s_barrier\n\t"
    "s_bitcmp1_b32 %[x], 2\n\t"
    "s_cbranch_scc1 7f\n\t"
    "8:\n\t"
    "s_bitcmp1_b32 %[x], 3\n\t"
    "s_cbranch_scc1 13f\n\t"
    "14:\n\t"
    "s_waitcnt lgkmcnt(4)\n\t"
    "v_mfma_f32_16x16x32_bf16 v[80:83], v[136:139], v[104:107], v[80:83]\n\t"
    "ds_read_b128 v[96:99], %[ea] offset:8192\n\t"
    "v_mfma_f32_16x16x32_bf16 v[84:87], v[140:143], v[104:107], v[84:87]\n\t"
    "ds_read_b128 v[100:103], %[ea] offset:10240\n\t"
    "v_mfma_f32_16x16x32_bf16 v[88:91], v[136:139], v[108:111], v[88:91]\n\t"
    "v_mfma_f32_16x16x32_bf16 v[92:95], v[140:143], v[108:111], v[92:95]\n\t"
    "s_waitcnt lgkmcnt(4)\n\t"
    "v_mfma_f32_16x16x32_bf16 v[80:83], v[144:147], v[112:115], v[80:83]\n\t"
    "ds_read_b128 v[104:107], %[da] offset:8192\n\t"
    "v_mfma_f32_16x16x32_bf16 v[84:87], v[148:151], v[112:115], v[84:87]\n\t"
    "ds_read_b128 v[108:111], %[da] offset:10240\n\t"
    "v_mfma_f32_16x16x32_bf16 v[88:91], v[144:147], v[116:119], v[88:91]\n\t"
    "v_mfma_f32_16x16x32_bf16 v[92:95], v[148:151], v[116:119], v[92:95]\n\t"
    "s_waitcnt lgkmcnt(4)\n\t"
    "v_mfma_f32_16x16x32_bf16 v[80:83], v[152:155], v[120:123], v[80:83]\n\t"
    "ds_read_b128 v[112:115], %[ea] offset:12288\n\t"
    "v_mfma_f32_16x16x32_bf16 v[84:87], v[156:159], v[120:123], v[84:87]\n\t"
    "ds_read_b128 v[116:119], %[ea] offset:14336\n\t"
    "v_mfma_f32_16x16x32_bf16 v[88:91], v[152:155], v[124:127], v[88:91]\n\t"
    "v_mfma_f32_16x16x32_bf16 v[92:95], v[156:159], v[124:127], v[92:95]\n\t"
    "s_waitcnt lgkmcnt(4)\n\t"
    "v_mfma_f32_16x16x32_bf16 v[80:83], v[160:163], v[96:99], v[80:83]\n\t"
    "ds_read_b128 v[120:123], %[da] offset:12288\n\t"
    "v_mfma_f32_16x16x32_bf16 v[84:87], v[164:167], v[96:99], v[84:87]\n\t"
    "ds_read_b128 v[124:127], %[da] offset:14336\n\t"
    "v_mfma_f32_16x16x32_bf16 v[88:91], v[160:163], v[100:103], v[88:91]\n\t"
    "v_mfma_f32_16x16x32_bf16 v[92:95], v[164:167], v[100:103], v[92:95]\n\t"
    "s_barrier\n\t"
    "s_waitcnt lgkmcnt(4)\n\t"
    "v_mfma_f32_16x16x32_bf16 v[80:83], v[168:171], v[104:107], v[80:83]\n\t"
    "ds_read_b128 v[96:99], %[ea] offset:16384\n\t"
    "v_mfma_f32_16x16x32_bf16 v[84:87], v[172:175], v[104:107], v[84:87]\n\t"
    "ds_read_b128 v[100:103], %[ea] offset:18432\n\t"
    "v_mfma_f32_16x16x32_bf16 v[88:91], v[168:171], v[108:111], v[88:91]\n\t"
    "v_mfma_f32_16x16x32_bf16 v[92:95], v[172:175], v[108:111], v[92:95]\n\t"
    "s_waitcnt lgkmcnt(4)\n\t"
    "v_mfma_f32_16x16x32_bf16 v[80:83], v[176:179], v[112:115], v[80:83]\n\t"
    "ds_read_b128 v[104:107], %[da] offset:16384\n\t"
    "v_mfma_f32_16x16x32_bf16 v[84:87], v[180:183], v[112:115], v[84:87]\n\t"
    "ds_read_b128 v[108:111], %[da] offset:18432\n\t"
    "v_mfma_f32_16x16x32_bf16 v[88:91], v[176:179], v[116:119], v[88:91]\n\t"
    "v_mfma_f32_16x16x32_bf16 v[92:95], v[180:183], v[116:119], v[92:95]\n\t"
    "s_waitcnt lgkmcnt(4)\n\t"
    "v_mfma_f32_16x16x32_bf16 v[80:83], v[184:187], v[120:123], v[80:83]\n\t"
    "ds_read_b128 v[112:115], %[ea] offset:20480\n\t"
    "v_mfma_f32_16x16x32_bf16 v[84:87], v[188:191], v[120:123], v[84:87]\n\t"
    "ds_read_b128 v[116:119], %[ea] offset:22528\n\t"
    "v_mfma_f32_16x16x32_bf16 v[88:91], v[184:187], v[124:127], v[88:91]\n\t"
    "v_mfma_f32_16x16x32_bf16 v[92:95], v[188:191], v[124:127], v[92:95]\n\t"
    "s_waitcnt lgkmcnt(4)\n\t"
    "v_mfma_f32_16x16x32_bf16 v[80:83], v[192:195], v[96:99], v[80:83]\n\t"
    "ds_read_b128 v[120:123], %[da] offset:20480\n\t"
    "v_mfma_f32_16x16x32_bf16 v[84:87], v[196:199], v[96:99], v[84:87]\n\t"
    "ds_read_b128 v[124:127], %[da] offset:22528\n\t"
    "v_mfma_f32_16x16x32_bf16 v[88:91], v[192:195], v[100:103], v[88:91]\n\t"
    "v_mfma_f32_16x16x32_bf16 v[92:95], v[196:199], v[100:103], v[92:95]\n\t"
    "s_barrier\n\t"
    "s_and_b32 %[st], %[x], 3\n\t"
    "s_cbranch_scc1 3f\n\t"
    "4:\n\t"
    "s_waitcnt lgkmcnt(4)\n\t"
    "v_mfma_f32_16x16x32_bf16 v[80:83], v[200:203], v[104:107], v[80:83]\n\t"
    "ds_read_b128 v[96:99], %[ea] offset:24576\n\t"
    "v_mfma_f32_16x16x32_bf16 v[84:87], v[204:207], v[104:107], v[84:87]\n\t"
    "ds_read_b128 v[100:103], %[ea] offset:26624\n\t"
    "v_mfma_f32_16x16x32_bf16 v[88:91], v[200:203], v[108:111], v[88:91]\n\t"
    "v_mfma_f32_16x16x32_bf16 v[92:95], v[204:207], v[108:111], v[92:95]\n\t"
    "s_waitcnt lgkmcnt(4)\n\t"
    "v_mfma_f32_16x16x32_bf16 v[80:83], v[208:211], v[112:115], v[80:83]\n\t"
    "ds_read_b128 v[104:107], %[da] offset:24576\n\t"
    "v_mfma_f32_16x16x32_bf16 v[84:87], v[212:215], v[112:115], v[84:87]\n\t"
    "ds_read_b128 v[108:111], %[da] offset:26624\n\t"
    "v_mfma_f32_16x16x32_bf16 v[88:91], v[208:211], v[116:119], v[88:91]\n\t"
    "v_mfma_f32_16x16x32_bf16 v[92:95], v[212:215], v[116:119], v[92:95]\n\t"
    "s_waitcnt lgkmcnt(4)\n\t"
    "v_mfma_f32_16x16x32_bf16 v[80:83], v[216:219], v[120:123], v[80:83]\n\t"
    "ds_read_b128 v[112:115], %[ea] offset:28672\n\t"
    "v_mfma_f32_16x16x32_bf16 v[84:87], v[220:223], v[120:123], v[84:87]\n\t"
    "ds_read_b128 v[116:119], %[ea] offset:30720\n\t"
    "v_mfma_f32_16x16x32_bf16 v[88:91], v[216:219], v[124:127], v[88:91]\n\t"
    "v_mfma_f32_16x16x32_bf16 v[92:95], v[220:223], v[124:127], v[92:95]\n\t"
    "s_waitcnt lgkmcnt(4)\n\t"
    "v_mfma_f32_16x16x32_bf16 v[80:83], v[224:227], v[96:99], v[80:83]\n\t"
    "ds_read_b128 v[120:123], %[da] offset:28672\n\t"
    "v_mfma_f32_16x16x32_bf16 v[84:87], v[228:231], v[96:99], v[84:87]\n\t"
    "ds_read_b128 v[124:127], %[da] offset:30720\n\t"
    "v_mfma_f32_16x16x32_bf16 v[88:91], v[224:227], v[100:103], v[88:91]\n\t"
    "v_mfma_f32_16x16x32_bf16 v[92:95], v[228:231], v[100:103], v[92:95]\n\t"
    "s_waitcnt lgkmcnt(4)\n\t"
    "v_mfma_f32_16x16x32_bf16 v[80:83], v[232:235], v[104:107], v[80:83]\n\t"
    "v_mfma_f32_16x16x32_bf16 v[84:87], v[236:239], v[104:107], v[84:87]\n\t"
    "v_mfma_f32_16x16x32_bf16 v[88:91], v[232:235], v[108:111], v[88:91]\n\t"
    "v_mfma_f32_16x16x32_bf16 v[92:95], v[236:239], v[108:111], v[92:95]\n\t"
    "s_waitcnt lgkmcnt(2)\n\t"
    "v_mfma_f32_16x16x32_bf16 v[80:83], v[240:243], v[112:115], v[80:83]\n\t"
    "ds_read_b32 v39, %[fa]\n\t"
    "ds_read_b128 v[48:51], %[rw] offset:0\n\t"
    "v_mfma_f32_16x16x32_bf16 v[84:87], v[244:247], v[112:115], v[84:87]\n\t"
    "ds_read_b128 v[52:55], %[rw] offset:1024\n\t"
    "v_mfma_f32_16x16x32_bf16 v[88:91], v[240:243], v[116:119], v[88:91]\n\t"
    "ds_read_b128 v[56:59], %[rw] offset:2048\n\t"
    "v_mfma_f32_16x16x32_bf16 v[92:95], v[244:247], v[116:119], v[92:95]\n\t"
    "ds_read_b128 v[60:63], %[rw] offset:3072\n\t"
    "s_waitcnt lgkmcnt(5)\n\t"
    "v_mfma_f32_16x16x32_bf16 v[80:83], v[248:251], v[120:123], v[80:83]\n\t"
    "v_mfma_f32_16x16x32_bf16 v[84:87], v[252:255], v[120:123], v[84:87]\n\t"
    "v_mfma_f32_16x16x32_bf16 v[88:91], v[248:251], v[124:127], v[88:91]\n\t"
    "v_mfma_f32_16x16x32_bf16 v[92:95], v[252:255], v[124:127], v[92:95]\n\t"
    "s_mov_b32 %[st], 0\n\t"
    "21:\n\t"
    "s_waitcnt lgkmcnt(0)\n\t"
    "v_cmp_ne_u32 vcc, %[fv], v39\n\t"
    "s_cbranch_vccnz 20f\n\t"
    "v_mov_b32 v39, %[cn]\n\t"
    "s_nop 0\n\t"
    "v_pk_add_f32 v[48:49], v[48:49], v[80:81]\n\t"
    "v_pk_add_f32 v[50:51], v[50:51], v[82:83]\n\t"
    "v_pk_add_f32 v[52:53], v[52:53], v[84:85]\n\t"
    "v_pk_add_f32 v[54:55], v[54:55], v[86:87]\n\t"
    "v_pk_add_f32 v[56:57], v[56:57], v[88:89]\n\t"
    "v_pk_add_f32 v[58:59], v[58:59], v[90:91]\n\t"
    "v_pk_add_f32 v[60:61], v[60:61], v[92:93]\n\t"
    "v_pk_add_f32 v[62:63], v[62:63], v[94:95]\n\t"
    "v_cvt_pk_bf16_f32 v40, v48, v49\n\t"
    "v_cvt_pk_bf16_f32 v41, v50, v51\n\t"
    "v_cvt_pk_bf16_f32 v42, v52, v53\n\t"
    "v_cvt_pk_bf16_f32 v43, v54, v55\n\t"
    "global_store_dwordx4 %[co0], v[40:43], %[cb] sc1\n\t"
    "ds_read_b32 v38, v39 offset:4\n\t"
    "v_cvt_pk_bf16_f32 v44, v56, v57\n\t"
    "v_cvt_pk_bf16_f32 v45, v58, v59\n\t"
    "v_cvt_pk_bf16_f32 v46, v60, v61\n\t"
    "v_cvt_pk_bf16_f32 v47, v62, v63\n\t"
    "global_store_dwordx4 %[co1], v[44:47], %[cb] sc1\n\t"
    "s_memrealtime s[36:37]\n\t"
    "v_cmp_u_f32 vcc, v48, v56\n\t"
    "s_waitcnt lgkmcnt(0)\n\t"
    "s_or_b32 %[bad], vcc_lo, vcc_hi\n\t"
    "v_readfirstlane_b32 %[pk], v38\n\t"
    "s_sub_u32 %[tk], s36, %[tk]\n\t"
    "s_branch 9f\n\t"
    "20:\n\t"
    "ds_read_b32 v39, %[fa]\n\t"
    "ds_read_b128 v[48:51], %[rw] offset:0\n\t"
    "ds_read_b128 v[52:55], %[rw] offset:1024\n\t"
    "ds_read_b128 v[56:59], %[rw] offset:2048\n\t"
    "ds_read_b128 v[60:63], %[rw] offset:3072\n\t"
    "s_branch 21b\n\t"
    "13:\n\t"
    "s_memrealtime s[36:37]\n\t"
    "v_mov_b32 v79, 0\n\t"
    "s_waitcnt lgkmcnt(0)\n\t"
    "s_mov_b32 %[tk], s36\n\t"
    "s_add_u32 s36, s36, %[bl]\n\t"
    "s_and_b32 s36, s36, 0xffffff\n\t"
    "s_and_b32 s37, %[bl], 0xff000000\n\t"
    "s_or_b32 s36, s36, s37\n\t"
    "v_mov_b32 v78, s36\n\t"
    "s_mov_b64 exec, 1\n\t"
    "global_store_dword v79, v78, %[pg] offset:64 sc1\n\t"
    "s_mov_b64 exec, -1\n\t"
    "s_branch 14b\n\t"
    "3:\n\t"
    "7:\n\t"
    "s_and_b32 %[st], %[pv], 1\n\t"
    "s_mul_i32 %[st], %[st], 36\n\t"
    "s_add_u32 %[st], %[st], %[cn]\n\t"
    "v_mov_b32 v76, %[st]\n\t"
    "v_mov_b32 v77, 1\n\t"
    "v_mov_b32 v78, %[pv]\n\t"
    "v_mov_b32 v79, 0\n\t"
    "s_waitcnt vmcnt(0)\n\t"
    "s_mov_b64 exec, 1\n\t"
    "ds_add_rtn_u32 v77, v76, v77\n\t"
    "s_waitcnt lgkmcnt(0)\n\t"
    "v_readfirstlane_b32 %[st], v77\n\t"
    "s_and_b32 %[st], %[st], 3\n\t"
    "s_cmp_lg_u32 %[st], 3\n\t"
    "s_cbranch_scc1 6f\n\t"
    "global_store_dword v79, v78, %[pg] sc1\n\t"
    "6:\n\t"
    "s_mov_b64 exec, -1\n\t"
    "s_bitcmp1_b32 %[x], 2\n\t"
    "s_cbranch_scc1 8b\n\t"
    "s_branch 4b\n\t"
    "12:\n\t"
    "s_waitcnt lgkmcnt(0)\n\t"
    "9:"
    : [st] "=&s"(st), [tk] "+&s"(tk), [bad] "=&s"(bad), [pk] "=&s"(pk)
    : [ea] "v"(ea), [da] "v"(da), [x] "s"(x), "{v[128:159]}"(W[0]), "{v[160:191]}"(W[1]), "{v[192:223]}"(W[2]), "{v[224:255]}"(W[3]), [pv] "s"(pv), [cn] "s"(cn), [pg] "s"(pg), [bl] "s"(bl), [rw] "v"(rw), [fa] "v"(fa), [fv] "s"(fv), [cb] "s"(cb), [co0] "v"(co0), [co1] "v"(co1)
    : "v96", "v97", "v98", "v99", "v100", "v101", "v102", "v103", "v104", "v105", "v106", "v107", "v108", "v109", "v110", "v111", "v112", "v113", "v114", "v115", "v116", "v117", "v118", "v119", "v120", "v121", "v122", "v123", "v124", "v125", "v126", "v127", "v76", "v77", "v78", "v79", "v38", "v39", "v40", "v41", "v42", "v43", "v44", "v45", "v46", "v47", "v48", "v49", "v50", "v51", "v52", "v53", "v54", "v55", "v56", "v57", "v58", "v59", "v60", "v61", "v62", "v63", "v80", "v81", "v82", "v83", "v84", "v85", "v86", "v87", "v88", "v89", "v90", "v91", "v92", "v93", "v94", "v95", "vcc", "scc", "m0", "memory", "s36", "s37", "s38", "s39");
}
__device__ __forceinline__ void lean_ep1(const u32x32 (&W)[4], unsigned ea, unsigned da, unsigned x, unsigned pv, unsigned cn, const char* pg, unsigned bl, unsigned& tk, unsigned& st,
    unsigned rw, unsigned fa, unsigned fv, const char* cb, unsigned co0, unsigned co1, unsigned& bad, unsigned& pk) {
  asm volatile("v_mov_b32 v76, %[cn]\n\t"
    "ds_read_b32 v76, v76 offset:28\n\t"
    "ds_read_b128 v[96:99], %[ea] offset:0\n\t"
    "ds_read_b128 v[100:103], %[ea] offset:2048\n\t"
    "ds_read_b128 v[104:107], %[da] offset:0\n\t"
    "ds_read_b128 v[108:111], %[da] offset:2048\n\t"
    "ds_read_b128 v[112:115], %[ea] offset:4096\n\t"
    "ds_read_b128 v[116:119], %[ea] offset:6144\n\t"
    "s_waitcnt lgkmcnt(6)\n\t"
    "v_cmp_ne_u32 vcc, 0, v76\n\t"
    "s_mov_b32 %[st], -1\n\t"
    "s_cbranch_vccnz 12f\n\t"
    "s_waitcnt lgkmcnt(4)\n\t"
    "v_mfma_f32_16x16x32_bf16 v[80:83], v[128:131], v[96:99], 0\n\t"
    "ds_read_b128 v[120:123], %[da] offset:4096\n\t"
    "v_mfma_f32_16x16x32_bf16 v[84:87], v[132:135], v[96:99], 0\n\t"
    "ds_read_b128 v[124:127], %[da] offset:6144\n\t"
    "v_mfma_f32_16x16x32_bf16 v[88:91], v[128:131], v[100:103], 0\n\t"
    "v_mfma_f32_16x16x32_bf16 v[92:95], v[132:135], v[100:103], 0\n\t"
    "s_barrier\n\t"
    "s_bitcmp1_b32 %[x], 2\n\t"
    "s_cbranch_scc1 7f\n\t"
    "8:\n\t"
    "s_bitcmp1_b32 %[x], 3\n\t"
    "s_cbranch_scc1 13f\n\t"
    "14:\n\t"
    "s_waitcnt lgkmcnt(4)\n\t"
    "v_mfma_f32_16x16x32_bf16 v[80:83], v[136:139], v[104:107], v[80:83]\n\t"
    "ds_read_b128 v[96:99], %[ea] offset:8192\n\t"
    "v_mfma_f32_16x16x32_bf16 v[84:87], v[140:143], v[104:107], v[84:87]\n\t"
    "ds_read_b128 v[100:103], %[ea] offset:10240\n\t"
    "v_mfma_f32_16x16x32_bf16 v[88:91], v[136:139], v[108:111], v[88:91]\n\t"
    "v_mfma_f32_16x16x32_bf16 v[92:95], v[140:143], v[108:111], v[92:95]\n\t"
    "s_waitcnt lgkmcnt(4)\n\t"
    "v_mfma_f32_16x16x32_bf16 v[80:83], v[144:147], v[112:115], v[80:83]\n\t"
    "ds_read_b128 v[104:107], %[da] offset:8192\n\t"
    "v_mfma_f32_16x16x32_bf16 v[84:87], v[148:151], v[112:115], v[84:87]\n\t"
    "ds_read_b128 v[108:111], %[da] offset:10240\n\t"
    "v_mfma_f32_16x16x32_bf16 v[88:91], v[144:147], v[116:119], v[88:91]\n\t"
    "v_mfma_f32_16x16x32_bf16 v[92:95], v[148:151], v[116:119], v[92:95]\n\t"
    "s_waitcnt lgkmcnt(4)\n\t"
    "v_mfma_f32_16x16x32_bf16 v[80:83], v[152:155], v[120:123], v[80:83]\n\t"
    "ds_read_b128 v[112:115], %[ea] offset:12288\n\t"
    "v_mfma_f32_16x16x32_bf16 v[84:87], v[156:159], v[120:123], v[84:87]\n\t"
    "ds_read_b128 v[116:119], %[ea] offset:14336\n\t"
    "v_mfma_f32_16x16x32_bf16 v[88:91], v[152:155], v[124:127], v[88:91]\n\t"
    "v_mfma_f32_16x16x32_bf16 v[92:95], v[156:159], v[124:127], v[92:95]\n\t"
    "s_waitcnt lgkmcnt(4)\n\t"
    "v_mfma_f32_16x16x32_bf16 v[80:83], v[160:163], v[96:99], v[80:83]\n\t"
    "ds_read_b128 v[120:123], %[da] offset:12288\n\t"
    "v_mfma_f32_16x16x32_bf16 v[84:87], v[164:167], v[96:99], v[84:87]\n\t"
    "ds_read_b128 v[124:127], %[da] offset:14336\n\t"
    "v_mfma_f32_16x16x32_bf16 v[88:91], v[160:163], v[100:103], v[88:91]\n\t"
    "v_mfma_f32_16x16x32_bf16 v[92:95], v[164:167], v[100:103], v[92:95]\n\t"
    "s_barrier\n\t"
    "s_waitcnt lgkmcnt(4)\n\t"
    "v_mfma_f32_16x16x32_bf16 v[80:83], v[168:171], v[104:107], v[80:83]\n\t"
    "ds_read_b128 v[96:99], %[ea] offset:16384\n\t"
    "v_mfma_f32_16x16x32_bf16 v[84:87], v[172:175], v[104:107], v[84:87]\n\t"
    "ds_read_b128 v[100:103], %[ea] offset:18432\n\t"
    "v_mfma_f32_16x16x32_bf16 v[88:91], v[168:171], v[108:111], v[88:91]\n\t"
    "v_mfma_f32_16x16x32_bf16 v[92:95], v[172:175], v[108:111], v[92:95]\n\t"
    "s_waitcnt lgkmcnt(4)\n\t"
    "v_mfma_f32_16x16x32_bf16 v[80:83], v[176:179], v[112:115], v[80:83]\n\t"
    "ds_read_b128 v[104:107], %[da] offset:16384\n\t"
    "v_mfma_f32_16x16x32_bf16 v[84:87], v[180:183], v[112:115], v[84:87]\n\t"
    "ds_read_b128 v[108:111], %[da] offset:18432\n\t"
    "v_mfma_f32_16x16x32_bf16 v[88:91], v[176:179], v[116:119], v[88:91]\n\t"
    "v_mfma_f32_16x16x32_bf16 v[92:95], v[180:183], v[116:119], v[92:95]\n\t"
    "s_waitcnt lgkmcnt(4)\n\t"
    "v_mfma_f32_16x16x32_bf16 v[80:83], v[184:187], v[120:123], v[80:83]\n\t"
    "ds_read_b128 v[112:115], %[ea] offset:20480\n\t"
    "v_mfma_f32_16x16x32_bf16 v[84:87], v[188:191], v[120:123], v[84:87]\n\t"
    "ds_read_b128 v[116:119], %[ea] offset:22528\n\t"
    "v_mfma_f32_16x16x32_bf16 v[88:91], v[184:187], v[124:127], v[88:91]\n\t"
    "v_mfma_f32_16x16x32_bf16 v[92:95], v[188:191], v[124:127], v[92:95]\n\t"
    "s_waitcnt lgkmcnt(4)\n\t"
    "v_mfma_f32_16x16x32_bf16 v[80:83], v[192:195], v[96:99], v[80:83]\n\t"
    "ds_read_b128 v[120:123], %[da] offset:20480\n\t"
    "v_mfma_f32_16x16x32_bf16 v[84:87], v[196:199], v[96:99], v[84:87]\n\t"
    "ds_read_b128 v[124:127], %[da] offset:22528\n\t"
    "v_mfma_f32_16x16x32_bf16 v[88:91], v[192:195], v[100:103], v[88:91]\n\t"
    "v_mfma_f32_16x16x32_bf16 v[92:95], v[196:199], v[100:103], v[92:95]\n\t"
    "s_barrier\n\t"
    "s_and_b32 %[st], %[x], 3\n\t"
    "s_cbranch_scc1 3f\n\t"
    "4:\n\t"
    "s_waitcnt lgkmcnt(4)\n\t"
    "v_mfma_f32_16x16x32_bf16 v[80:83], v[200:203], v[104:107], v[80:83]\n\t"
    "ds_read_b128 v[96:99], %[ea] offset:24576\n\t"
    "v_mfma_f32_16x16x32_bf16 v[84:87], v[204:207], v[104:107], v[84:87]\n\t"
    "ds_read_b128 v[100:103], %[ea] offset:26624\n\t"
    "v_mfma_f32_16x16x32_bf16 v[88:91], v[200:203], v[108:111], v[88:91]\n\t"
    "v_mfma_f32_16x16x32_bf16 v[92:95], v[204:207], v[108:111], v[92:95]\n\t"
    "s_waitcnt lgkmcnt(4)\n\t"
    "v_mfma_f32_16x16x32_bf16 v[80:83], v[208:211], v[112:115], v[80:83]\n\t"
    "ds_read_b128 v[104:107], %[da] offset:24576\n\t"
    "v_mfma_f32_16x16x32_bf16 v[84:87], v[212:215], v[112:115], v[84:87]\n\t"
    "ds_read_b128 v[108:111], %[da] offset:26624\n\t"
    "v_mfma_f32_16x16x32_bf16 v[88:91], v[208:211], v[116:119], v[88:91]\n\t"
    "v_mfma_f32_16x16x32_bf16 v[92:95], v[212:215], v[116:119], v[92:95]\n\t"
    "s_waitcnt lgkmcnt(4)\n\t"
    "v_mfma_f32_16x16x32_bf16 v[80:83], v[216:219], v[120:123], v[80:83]\n\t"
    "ds_read_b128 v[112:115], %[ea] offset:28672\n\t"
    "v_mfma_f32_16x16x32_bf16 v[84:87], v[220:223], v[120:123], v[84:87]\n\t"
    "ds_read_b128 v[116:119], %[ea] offset:30720\n\t"
    "v_mfma_f32_16x16x32_bf16 v[88:91], v[216:219], v[124:127], v[88:91]\n\t"
    "v_mfma_f32_16x16x32_bf16 v[92:95], v[220:223], v[124:127], v[92:95]\n\t"
    "s_waitcnt lgkmcnt(4)\n\t"
    "v_mfma_f32_16x16x32_bf16 v[80:83], v[224:227], v[96:99], v[80:83]\n\t"
    "ds_read_b128 v[120:123], %[da] offset:28672\n\t"
    "v_mfma_f32_16x16x32_bf16 v[84:87], v[228:231], v[96:99], v[84:87]\n\t"
    "ds_read_b128 v[124:127], %[da] offset:30720\n\t"
    "v_mfma_f32_16x16x32_bf16 v[88:91], v[224:227], v[100:103], v[88:91]\n\t"
    "v_mfma_f32_16x16x32_bf16 v[92:95], v[228:231], v[100:103], v[92:95]\n\t"
    "s_waitcnt lgkmcnt(4)\n\t"
    "v_mfma_f32_16x16x32_bf16 v[80:83], v[232:235], v[104:107], v[80:83]\n\t"
    "v_mfma_f32_16x16x32_bf16 v[84:87], v[236:239], v[104:107], v[84:87]\n\t"
    "v_mfma_f32_16x16x32_bf16 v[88:91], v[232:235], v[108:111], v[88:91]\n\t"
    "v_mfma_f32_16x16x32_bf16 v[92:95], v[236:239], v[108:111], v[92:95]\n\t"
    "s_waitcnt lgkmcnt(2)\n\t"
    "v_mfma_f32_16x16x32_bf16 v[80:83], v[240:243], v[112:115], v[80:83]\n\t"
    "ds_read_b32 v39, %[fa]\n\t"
    "ds_read_b128 v[48:51], %[rw] offset:0\n\t"
    "v_mfma_f32_16x16x32_bf16 v[84:87], v[244:247], v[112:115], v[84:87]\n\t"
    "ds_read_b128 v[52:55], %[rw] offset:1024\n\t"
    "v_mfma_f32_16x16x32_bf16 v[88:91], v[240:243], v[116:119], v[88:91]\n\t"
    "ds_read_b128 v[56:59], %[rw] offset:2048\n\t"
    "v_mfma_f32_16x16x32_bf16 v[92:95], v[244:247], v[116:119], v[92:95]\n\t"
    "ds_read_b128 v[60:63], %[rw] offset:3072\n\t"
    "s_waitcnt lgkmcnt(5)\n\t"
    "v_mfma_f32_16x16x32_bf16 v[80:83], v[248:251], v[120:123], v[80:83]\n\t"
    "v_mfma_f32_16x16x32_bf16 v[84:87], v[252:255], v[120:123], v[84:87]\n\t"
    "v_mfma_f32_16x16x32_bf16 v[88:91], v[248:251], v[124:127], v[88:91]\n\t"
    "v_mfma_f32_16x16x32_bf16 v[92:95], v[252:255], v[124:127], v[92:95]\n\t"
    "s_mov_b32 %[st], 0\n\t"
    "21:\n\t"
    "s_waitcnt lgkmcnt(0)\n\t"
    "v_cmp_ne_u32 vcc, %[fv], v39\n\t"
    "s_cbranch_vccnz 20f\n\t"
    "v_mov_b32 v39, %[cn]\n\t"
    "s_nop 0\n\t"
    "v_pk_add_f32 v[48:49], v[48:49], v[80:81]\n\t"
    "v_pk_add_f32 v[50:51], v[50:51], v[82:83]\n\t"
    "v_pk_add_f32 v[52:53], v[52:53], v[84:85]\n\t"
    "v_pk_add_f32 v[54:55], v[54:55], v[86:87]\n\t"
    "v_pk_add_f32 v[56:57], v[56:57], v[88:89]\n\t"
    "v_pk_add_f32 v[58:59], v[58:59], v[90:91]\n\t"
    "v_pk_add_f32 v[60:61], v[60:61], v[92:93]\n\t"
    "v_pk_add_f32 v[62:63], v[62:63], v[94:95]\n\t"
    "v_mul_f32 v40, %[nl], v48\n\t"
    "v_mul_f32 v41, %[nl], v49\n\t"
    "v_mul_f32 v42, %[nl], v50\n\t"
    "v_mul_f32 v43, %[nl], v51\n\t"
    "v_mul_f32 v44, %[nl], v56\n\t"
    "v_mul_f32 v45, %[nl], v57\n\t"
    "v_mul_f32 v46, %[nl], v58\n\t"
    "v_mul_f32 v47, %[nl], v59\n\t"
    "v_exp_f32 v40, v40\n\t"
    "v_exp_f32 v41, v41\n\t"
    "v_exp_f32 v42, v42\n\t"
    "v_exp_f32 v43, v43\n\t"
    "v_exp_f32 v44, v44\n\t"
    "v_exp_f32 v45, v45\n\t"
    "v_exp_f32 v46, v46\n\t"
    "v_exp_f32 v47, v47\n\t"
    "v_pk_add_f32 v[40:41], v[40:41], 1.0 op_sel_hi:[1,0]\n\t"
    "v_pk_add_f32 v[42:43], v[42:43], 1.0 op_sel_hi:[1,0]\n\t"
    "v_pk_add_f32 v[44:45], v[44:45], 1.0 op_sel_hi:[1,0]\n\t"
    "v_pk_add_f32 v[46:47], v[46:47], 1.0 op_sel_hi:[1,0]\n\t"
    "v_rcp_f32 v40, v40\n\t"
    "v_rcp_f32 v41, v41\n\t"
    "v_rcp_f32 v42, v42\n\t"
    "v_rcp_f32 v43, v43\n\t"
    "v_rcp_f32 v44, v44\n\t"
    "v_rcp_f32 v45, v45\n\t"
    "v_rcp_f32 v46, v46\n\t"
    "v_rcp_f32 v47, v47\n\t"
    "v_pk_mul_f32 v[40:41], v[48:49], v[40:41]\n\t"
    "v_pk_mul_f32 v[42:43], v[50:51], v[42:43]\n\t"
    "v_pk_mul_f32 v[44:45], v[56:57], v[44:45]\n\t"
    "v_pk_mul_f32 v[46:47], v[58:59], v[46:47]\n\t"
    "v_pk_mul_f32 v[40:41], v[40:41], v[52:53]\n\t"
    "v_pk_mul_f32 v[42:43], v[42:43], v[54:55]\n\t"
    "v_pk_mul_f32 v[44:45], v[44:45], v[60:61]\n\t"
    "v_pk_mul_f32 v[46:47], v[46:47], v[62:63]\n\t"
    "v_cvt_pk_bf16_f32 v48, v40, v41\n\t"
    "v_cvt_pk_bf16_f32 v49, v42, v43\n\t"
    "v_cvt_pk_bf16_f32 v50, v44, v45\n\t"
    "ds_read_b32 v38, v39 offset:4\n\t"
    "v_cvt_pk_bf16_f32 v51, v46, v47\n\t"
    "s_nop 1\n\t"
    "v_permlane16_swap_b32 v48, v50\n\t"
    "v_permlane16_swap_b32 v49, v51\n\t"
    "global_store_dwordx4 %[co0], v[48:51], %[cb] sc1\n\t"
    "s_memrealtime s[36:37]\n\t"
    "v_cmp_u_f32 vcc, v52, v60\n\t"
    "s_waitcnt lgkmcnt(0)\n\t"
    "s_or_b32 %[bad], vcc_lo, vcc_hi\n\t"
    "v_readfirstlane_b32 %[pk], v38\n\t"
    "s_sub_u32 %[tk], s36, %[tk]\n\t"
    "s_branch 9f\n\t"
    "20:\n\t"
    "ds_read_b32 v39, %[fa]\n\t"
    "ds_read_b128 v[48:51], %[rw] offset:0\n\t"
    "ds_read_b128 v[52:55], %[rw] offset:1024\n\t"
    "ds_read_b128 v[56:59], %[rw] offset:2048\n\t"
    "ds_read_b128 v[60:63], %[rw] offset:3072\n\t"
    "s_branch 21b\n\t"
    "13:\n\t"
    "s_memrealtime s[36:37]\n\t"
    "v_mov_b32 v79, 0\n\t"
    "s_waitcnt lgkmcnt(0)\n\t"
    "s_mov_b32 %[tk], s36\n\t"
    "s_add_u32 s36, s36, %[bl]\n\t"
    "s_and_b32 s36, s36, 0xffffff\n\t"
    "s_and_b32 s37, %[bl], 0xff000000\n\t"
    "s_or_b32 s36, s36, s37\n\t"
    "v_mov_b32 v78, s36\n\t"
    "s_mov_b64 exec, 1\n\t"
    "global_store_dword v79, v78, %[pg] offset:64 sc1\n\t"
    "s_mov_b64 exec, -1\n\t"
    "s_branch 14b\n\t"
    "3:\n\t"
    "7:\n\t"
    "s_and_b32 %[st], %[pv], 1\n\t"
    "s_mul_i32 %[st], %[st], 36\n\t"
    "s_add_u32 %[st], %[st], %[cn]\n\t"
    "v_mov_b32 v76, %[st]\n\t"
    "v_mov_b32 v77, 1\n\t"
    "v_mov_b32 v78, %[pv]\n\t"
    "v_mov_b32 v79, 0\n\t"
    "s_waitcnt vmcnt(0)\n\t"
    "s_mov_b64 exec, 1\n\t"
    "ds_add_rtn_u32 v77, v76, v77\n\t"
    "s_waitcnt lgkmcnt(0)\n\t"
    "v_readfirstlane_b32 %[st], v77\n\t"
    "s_and_b32 %[st], %[st], 3\n\t"
    "s_cmp_lg_u32 %[st], 3\n\t"
    "s_cbranch_scc1 6f\n\t"
    "global_store_dword v79, v78, %[pg] sc1\n\t"
    "6:\n\t"
    "s_mov_b64 exec, -1\n\t"
    "s_bitcmp1_b32 %[x], 2\n\t"
    "s_cbranch_scc1 8b\n\t"
    "s_branch 4b\n\t"
    "12:\n\t"
    "s_waitcnt lgkmcnt(0)\n\t"
    "9:"
    : [st] "=&s"(st), [tk] "+&s"(tk), [bad] "=&s"(bad), [pk] "=&s"(pk)
    : [ea] "v"(ea), [da] "v"(da), [x] "s"(x), "{v[128:159]}"(W[0]), "{v[160:191]}"(W[1]), "{v[192:223]}"(W[2]), "{v[224:255]}"(W[3]), [pv] "s"(pv), [cn] "s"(cn), [pg] "s"(pg), [bl] "s"(bl), [rw] "v"(rw), [fa] "v"(fa), [fv] "s"(fv), [cb] "s"(cb), [co0] "v"(co0), [nl] "s"(-LOG2E)
    : "v96", "v97", "v98", "v99", "v100", "v101", "v102", "v103", "v104", "v105", "v106", "v107", "v108", "v109", "v110", "v111", "v112", "v113", "v114", "v115", "v116", "v117", "v118", "v119", "v120", "v121", "v122", "v123", "v124", "v125", "v126", "v127", "v76", "v77", "v78", "v79", "v38", "v39", "v40", "v41", "v42", "v43", "v44", "v45", "v46", "v47", "v48", "v49", "v50", "v51", "v52", "v53", "v54", "v55", "v56", "v57", "v58", "v59", "v60", "v61", "v62", "v63", "v80", "v81", "v82", "v83", "v84", "v85", "v86", "v87", "v88", "v89", "v90", "v91", "v92", "v93", "v94", "v95", "vcc", "scc", "m0", "memory", "s36", "s37", "s38", "s39");
}
__device__ __forceinline__ void lean_ep2(const u32x32 (&W)[4], unsigned ea, unsigned da, unsigned x, unsigned pv, unsigned cn, const char* pg, unsigned bl, unsigned& tk, unsigned& st,
    unsigned rw, unsigned fa, unsigned fv, const char* cb, unsigned co0, unsigned co1, unsigned& bad, unsigned& pk) {
  asm volatile("v_mov_b32 v76, %[cn]\n\t"
    "ds_read_b32 v76, v76 offset:28\n\t"
    "ds_read_b128 v[96:99], %[ea] offset:0\n\t"
    "ds_read_b128 v[100:103], %[ea] offset:2048\n\t"
    "ds_read_b128 v[104:107], %[da] offset:0\n\t"
    "ds_read_b128 v[108:111], %[da] offset:2048\n\t"
    "ds_read_b128 v[112:115], %[ea] offset:4096\n\t"
    "ds_read_b128 v[116:119], %[ea] offset:6144\n\t"
    "s_waitcnt lgkmcnt(6)\n\t"
    "v_cmp_ne_u32 vcc, 0, v76\n\t"
    "s_mov_b32 %[st], -1\n\t"
    "s_cbranch_vccnz 12f\n\t"
    "s_waitcnt lgkmcnt(4)\n\t"
    "v_mfma_f32_16x16x32_bf16 v[80:83], v[128:131], v[96:99], 0\n\t"
    "ds_read_b128 v[120:123], %[da] offset:4096\n\t"
    "v_mfma_f32_16x16x32_bf16 v[84:87], v[132:135], v[96:99], 0\n\t"
    "ds_read_b128 v[124:127], %[da] offset:6144\n\t"
    "v_mfma_f32_16x16x32_bf16 v[88:91], v[128:131], v[100:103], 0\n\t"
    "v_mfma_f32_16x16x32_bf16 v[92:95], v[132:135], v[100:103], 0\n\t"
    "s_barrier\n\t"
    "s_bitcmp1_b32 %[x], 2\n\t"
    "s_cbranch_scc1 7f\n\t"
    "8:\n\t"
    "s_bitcmp1_b32 %[x], 3\n\t"
    "s_cbranch_scc1 13f\n\t"
    "14:\n\t"
    "s_waitcnt lgkmcnt(4)\n\t"
    "v_mfma_f32_16x16x32_bf16 v[80:83], v[136:139], v[104:107], v[80:83]\n\t"
    "ds_read_b128 v[96:99], %[ea] offset:8192\n\t"
    "v_mfma_f32_16x16x32_bf16 v[84:87], v[140:143], v[104:107], v[84:87]\n\t"
    "ds_read_b128 v[100:103], %[ea] offset:10240\n\t"
    "v_mfma_f32_16x16x32_bf16 v[88:91], v[136:139], v[108:111], v[88:91]\n\t"
    "v_mfma_f32_16x16x32_bf16 v[92:95], v[140:143], v[108:111], v[92:95]\n\t"
    "s_waitcnt lgkmcnt(4)\n\t"
    "v_mfma_f32_16x16x32_bf16 v[80:83], v[144:147], v[112:115], v[80:83]\n\t"
    "ds_read_b128 v[104:107], %[da] offset:8192\n\t"
    "v_mfma_f32_16x16x32_bf16 v[84:87], v[148:151], v[112:115], v[84:87]\n\t"
    "ds_read_b128 v[108:111], %[da] offset:10240\n\t"
    "v_mfma_f32_16x16x32_bf16 v[88:91], v[144:147], v[116:119], v[88:91]\n\t"
    "v_mfma_f32_16x16x32_bf16 v[92:95], v[148:151], v[116:119], v[92:95]\n\t"
    "s_waitcnt lgkmcnt(4)\n\t"
    "v_mfma_f32_16x16x32_bf16 v[80:83], v[152:155], v[120:123], v[80:83]\n\t"
    "ds_read_b128 v[112:115], %[ea] offset:12288\n\t"
    "v_mfma_f32_16x16x32_bf16 v[84:87], v[156:159], v[120:123], v[84:87]\n\t"
    "ds_read_b128 v[116:119], %[ea] offset:14336\n\t"
    "v_mfma_f32_16x16x32_bf16 v[88:91], v[152:155], v[124:127], v[88:91]\n\t"
    "v_mfma_f32_16x16x32_bf16 v[92:95], v[156:159], v[124:127], v[92:95]\n\t"
    "s_waitcnt lgkmcnt(4)\n\t"
    "v_mfma_f32_16x16x32_bf16 v[80:83], v[160:163], v[96:99], v[80:83]\n\t"
    "ds_read_b128 v[120:123], %[da] offset:12288\n\t"
    "v_mfma_f32_16x16x32_bf16 v[84:87], v[164:167], v[96:99], v[84:87]\n\t"
    "ds_read_b128 v[124:127], %[da] offset:14336\n\t"
    "v_mfma_f32_16x16x32_bf16 v[88:91], v[160:163], v[100:103], v[88:91]\n\t"
    "v_mfma_f32_16x16x32_bf16 v[92:95], v[164:167], v[100:103], v[92:95]\n\t"
    "s_barrier\n\t"
    "s_waitcnt lgkmcnt(4)\n\t"
    "v_mfma_f32_16x16x32_bf16 v[80:83], v[168:171], v[104:107], v[80:83]\n\t"
    "ds_read_b128 v[96:99], %[ea] offset:16384\n\t"
    "v_mfma_f32_16x16x32_bf16 v[84:87], v[172:175], v[104:107], v[84:87]\n\t"
    "ds_read_b128 v[100:103], %[ea] offset:18432\n\t"
    "v_mfma_f32_16x16x32_bf16 v[88:91], v[168:171], v[108:111], v[88:91]\n\t"
    "v_mfma_f32_16x16x32_bf16 v[92:95], v[172:175], v[108:111], v[92:95]\n\t"
    "s_waitcnt lgkmcnt(4)\n\t"
    "v_mfma_f32_16x16x32_bf16 v[80:83], v[176:179], v[112:115], v[80:83]\n\t"
    "ds_read_b128 v[104:107], %[da] offset:16384\n\t"
    "v_mfma_f32_16x16x32_bf16 v[84:87], v[180:183], v[112:115], v[84:87]\n\t"
    "ds_read_b128 v[108:111], %[da] offset:18432\n\t"
    "v_mfma_f32_16x16x32_bf16 v[88:91], v[176:179], v[116:119], v[88:91]\n\t"
    "v_mfma_f32_16x16x32_bf16 v[92:95], v[180:183], v[116:119], v[92:95]\n\t"
    "s_waitcnt lgkmcnt(4)\n\t"
    "v_mfma_f32_16x16x32_bf16 v[80:83], v[184:187], v[120:123], v[80:83]\n\t"
    "ds_read_b128 v[112:115], %[ea] offset:20480\n\t"
    "v_mfma_f32_16x16x32_bf16 v[84:87], v[188:191], v[120:123], v[84:87]\n\t"
    "ds_read_b128 v[116:119], %[ea] offset:22528\n\t"
    "v_mfma_f32_16x16x32_bf16 v[88:91], v[184:187], v[124:127], v[88:91]\n\t"
    "v_mfma_f32_16x16x32_bf16 v[92:95], v[188:191], v[124:127], v[92:95]\n\t"
    "s_waitcnt lgkmcnt(4)\n\t"
    "v_mfma_f32_16x16x32_bf16 v[80:83], v[192:195], v[96:99], v[80:83]\n\t"
    "ds_read_b128 v[120:123], %[da] offset:20480\n\t"
    "v_mfma_f32_16x16x32_bf16 v[84:87], v[196:199], v[96:99], v[84:87]\n\t"
    "ds_read_b128 v[124:127], %[da] offset:22528\n\t"
    "v_mfma_f32_16x16x32_bf16 v[88:91], v[192:195], v[100:103], v[88:91]\n\t"
    "v_mfma_f32_16x16x32_bf16 v[92:95], v[196:199], v[100:103], v[92:95]\n\t"
    "s_barrier\n\t"
    "s_and_b32 %[st], %[x], 3\n\t"
    "s_cbranch_scc1 3f\n\t"
    "4:\n\t"
    "s_waitcnt lgkmcnt(4)\n\t"
    "v_mfma_f32_16x16x32_bf16 v[80:83], v[200:203], v[104:107], v[80:83]\n\t"
    "ds_read_b128 v[96:99], %[ea] offset:24576\n\t"
    "v_mfma_f32_16x16x32_bf16 v[84:87], v[204:207], v[104:107], v[84:87]\n\t"
    "ds_read_b128 v[100:103], %[ea] offset:26624\n\t"
    "v_mfma_f32_16x16x32_bf16 v[88:91], v[200:203], v[108:111], v[88:91]\n\t"
    "v_mfma_f32_16x16x32_bf16 v[92:95], v[204:207], v[108:111], v[92:95]\n\t"
    "s_waitcnt lgkmcnt(4)\n\t"
    "v_mfma_f32_16x16x32_bf16 v[80:83], v[208:211], v[112:115], v[80:83]\n\t"
    "ds_read_b128 v[104:107], %[da] offset:24576\n\t"
    "v_mfma_f32_16x16x32_bf16 v[84:87], v[212:215], v[112:115], v[84:87]\n\t"
    "ds_read_b128 v[108:111], %[da] offset:26624\n\t"
    "v_mfma_f32_16x16x32_bf16 v[88:91], v[208:211], v[116:119], v[88:91]\n\t"
    "v_mfma_f32_16x16x32_bf16 v[92:95], v[212:215], v[116:119], v[92:95]\n\t"
    "s_waitcnt lgkmcnt(4)\n\t"
    "v_mfma_f32_16x16x32_bf16 v[80:83], v[216:219], v[120:123], v[80:83]\n\t"
    "ds_read_b128 v[112:115], %[ea] offset:28672\n\t"
    "v_mfma_f32_16x16x32_bf16 v[84:87], v[220:223], v[120:123], v[84:87]\n\t"
    "ds_read_b128 v[116:119], %[ea] offset:30720\n\t"
    "v_mfma_f32_16x16x32_bf16 v[88:91], v[216:219], v[124:127], v[88:91]\n\t"
    "v_mfma_f32_16x16x32_bf16 v[92:95], v[220:223], v[124:127], v[92:95]\n\t"
    "s_waitcnt lgkmcnt(4)\n\t"
    "v_mfma_f32_16x16x32_bf16 v[80:83], v[224:227], v[96:99], v[80:83]\n\t"
    "ds_read_b128 v[120:123], %[da] offset:28672\n\t"
    "v_mfma_f32_16x16x32_bf16 v[84:87], v[228:231], v[96:99], v[84:87]\n\t"
    "ds_read_b128 v[124:127], %[da] offset:30720\n\t"
    "v_mfma_f32_16x16x32_bf16 v[88:91], v[224:227], v[100:103], v[88:91]\n\t"
    "v_mfma_f32_16x16x32_bf16 v[92:95], v[228:231], v[100:103], v[92:95]\n\t"
    "s_waitcnt lgkmcnt(4)\n\t"
    "v_mfma_f32_16x16x32_bf16 v[80:83], v[232:235], v[104:107], v[80:83]\n\t"
    "v_mfma_f32_16x16x32_bf16 v[84:87], v[236:239], v[104:107], v[84:87]\n\t"
    "v_mfma_f32_16x16x32_bf16 v[88:91], v[232:235], v[108:111], v[88:91]\n\t"
    "v_mfma_f32_16x16x32_bf16 v[92:95], v[236:239], v[108:111], v[92:95]\n\t"
    "s_waitcnt lgkmcnt(2)\n\t"
    "v_mfma_f32_16x16x32_bf16 v[80:83], v[240:243], v[112:115], v[80:83]\n\t"
    "ds_read_b32 v39, %[fa]\n\t"
    "ds_read_b128 v[48:51], %[rw] offset:0\n\t"
    "v_mfma_f32_16x16x32_bf16 v[84:87], v[244:247], v[112:115], v[84:87]\n\t"
    "ds_read_b128 v[52:55], %[rw] offset:1024\n\t"
    "v_mfma_f32_16x16x32_bf16 v[88:91], v[240:243], v[116:119], v[88:91]\n\t"
    "ds_read_b128 v[56:59], %[rw] offset:2048\n\t"
    "v_mfma_f32_16x16x32_bf16 v[92:95], v[244:247], v[116:119], v[92:95]\n\t"
    "ds_read_b128 v[60:63], %[rw] offset:3072\n\t"
    "s_waitcnt lgkmcnt(5)\n\t"
    "v_mfma_f32_16x16x32_bf16 v[80:83], v[248:251], v[120:123], v[80:83]\n\t"
    "v_mfma_f32_16x16x32_bf16 v[84:87], v[252:255], v[120:123], v[84:87]\n\t"
    "v_mfma_f32_16x16x32_bf16 v[88:91], v[248:251], v[124:127], v[88:91]\n\t"
    "v_mfma_f32_16x16x32_bf16 v[92:95], v[252:255], v[124:127], v[92:95]\n\t"
    "s_mov_b32 %[st], 0\n\t"
    "21:\n\t"
    "s_waitcnt lgkmcnt(0)\n\t"
    "v_cmp_ne_u32 vcc, %[fv], v39\n\t"
    "s_cbranch_vccnz 20f\n\t"
    "v_mov_b32 v39, %[cn]\n\t"
    "s_nop 0\n\t"
    "v_cvt_pk_bf16_f32 v40, v80, v81\n\t"
    "v_cvt_pk_bf16_f32 v41, v82, v83\n\t"
    "v_cvt_pk_bf16_f32 v42, v84, v85\n\t"
    "v_cvt_pk_bf16_f32 v43, v86, v87\n\t"
    "global_store_dwordx4 %[co0], v[40:43], %[cb] sc1\n\t"
    "v_cvt_pk_bf16_f32 v44, v88, v89\n\t"
    "v_cvt_pk_bf16_f32 v45, v90, v91\n\t"
    "v_cvt_pk_bf16_f32 v46, v92, v93\n\t"
    "v_cvt_pk_bf16_f32 v47, v94, v95\n\t"
    "global_store_dwordx4 %[co1], v[44:47], %[cb] sc1\n\t"
    "v_cvt_pk_bf16_f32 v48, v48, v49\n\t"
    "v_cvt_pk_bf16_f32 v49, v50, v51\n\t"
    "v_cvt_pk_bf16_f32 v50, v52, v53\n\t"
    "v_cvt_pk_bf16_f32 v51, v54, v55\n\t"
    "global_store_dwordx4 %[co0], v[48:51], %[cb] offset:2048 sc1\n\t"
    "ds_read_b32 v38, v39 offset:4\n\t"
    "v_cvt_pk_bf16_f32 v56, v56, v57\n\t"
    "v_cvt_pk_bf16_f32 v57, v58, v59\n\t"
    "v_cvt_pk_bf16_f32 v58, v60, v61\n\t"
    "v_cvt_pk_bf16_f32 v59, v62, v63\n\t"
    "global_store_dwordx4 %[co1], v[56:59], %[cb] offset:2048 sc1\n\t"
    "s_memrealtime s[36:37]\n\t"
    "v_cmp_u_f32 vcc, v80, v88\n\t"
    "s_waitcnt lgkmcnt(0)\n\t"
    "s_or_b32 %[bad], vcc_lo, vcc_hi\n\t"
    "v_readfirstlane_b32 %[pk], v38\n\t"
    "s_sub_u32 %[tk], s36, %[tk]\n\t"
    "s_branch 9f\n\t"
    "20:\n\t"
    "ds_read_b32 v39, %[fa]\n\t"
    "ds_read_b128 v[48:51], %[rw] offset:0\n\t"
    "ds_read_b128 v[52:55], %[rw] offset:1024\n\t"
    "ds_read_b128 v[56:59], %[rw] offset:2048\n\t"
    "ds_read_b128 v[60:63], %[rw] offset:3072\n\t"
    "s_branch 21b\n\t"
    "13:\n\t"
    "s_memrealtime s[36:37]\n\t"
    "v_mov_b32 v79, 0\n\t"
    "s_waitcnt lgkmcnt(0)\n\t"
    "s_mov_b32 %[tk], s36\n\t"
    "s_add_u32 s36, s36, %[bl]\n\t"
    "s_and_b32 s36, s36, 0xffffff\n\t"
    "s_and_b32 s37, %[bl], 0xff000000\n\t"
    "s_or_b32 s36, s36, s37\n\t"
    "v_mov_b32 v78, s36\n\t"
    "s_mov_b64 exec, 1\n\t"
    "global_store_dword v79, v78, %[pg] offset:64 sc1\n\t"
    "s_mov_b64 exec, -1\n\t"
    "s_branch 14b\n\t"
    "3:\n\t"
    "7:\n\t"
    "s_and_b32 %[st], %[pv], 1\n\t"
    "s_mul_i32 %[st], %[st], 36\n\t"
    "s_add_u32 %[st], %[st], %[cn]\n\t"
    "v_mov_b32 v76, %[st]\n\t"
    "v_mov_b32 v77, 1\n\t"
    "v_mov_b32 v78, %[pv]\n\t"
    "v_mov_b32 v79, 0\n\t"
    "s_waitcnt vmcnt(0)\n\t"
    "s_mov_b64 exec, 1\n\t"
    "ds_add_rtn_u32 v77, v76, v77\n\t"
    "s_waitcnt lgkmcnt(0)\n\t"
    "v_readfirstlane_b32 %[st], v77\n\t"
    "s_and_b32 %[st], %[st], 3\n\t"
    "s_cmp_lg_u32 %[st], 3\n\t"
    "s_cbranch_scc1 6f\n\t"
    "global_store_dword v79, v78, %[pg] sc1\n\t"
    "6:\n\t"
    "s_mov_b64 exec, -1\n\t"
    "s_bitcmp1_b32 %[x], 2\n\t"
    "s_cbranch_scc1 8b\n\t"
    "s_branch 4b\n\t"
    "12:\n\t"
    "s_waitcnt lgkmcnt(0)\n\t"
    "9:"
    : [st] "=&s"(st), [tk] "+&s"(tk), [bad] "=&s"(bad), [pk] "=&s"(pk)
    : [ea] "v"(ea), [da] "v"(da), [x] "s"(x), "{v[128:159]}"(W[0]), "{v[160:191]}"(W[1]), "{v[192:223]}"(W[2]), "{v[224:255]}"(W[3]), [pv] "s"(pv), [cn] "s"(cn), [pg] "s"(pg), [bl] "s"(bl), [rw] "v"(rw), [fa] "v"(fa), [fv] "s"(fv), [cb] "s"(cb), [co0] "v"(co0), [co1] "v"(co1)
    : "v96", "v97", "v98", "v99", "v100", "v101", "v102", "v103", "v104", "v105", "v106", "v107", "v108", "v109", "v110", "v111", "v112", "v113", "v114", "v115", "v116", "v117", "v118", "v119", "v120", "v121", "v122", "v123", "v124", "v125", "v126", "v127", "v76", "v77", "v78", "v79", "v38", "v39", "v40", "v41", "v42", "v43", "v44", "v45", "v46", "v47", "v48", "v49", "v50", "v51", "v52", "v53", "v54", "v55", "v56", "v57", "v58", "v59", "v60", "v61", "v62", "v63", "v80", "v81", "v82", "v83", "v84", "v85", "v86", "v87", "v88", "v89", "v90", "v91", "v92", "v93", "v94", "v95", "vcc", "scc", "m0", "memory", "s36", "s37", "s38", "s39");
}
#define LEAN_QB 1
#define PREK 10
#define NEARLY 16
__device__ __forceinline__ void fin_ld(const u32x16& pd, unsigned rw, unsigned fa, unsigned fv) {
  asm volatile("ds_write_b128 %[rw], v[80:83] offset:0\n\t"
    "ds_write_b128 %[rw], v[84:87] offset:1024\n\t"
    "ds_write_b128 %[rw], v[88:91] offset:2048\n\t"
    "ds_write_b128 %[rw], v[92:95] offset:3072\n\t"
    "ds_write_b32 %[fa], %[fv]"
    :
    : [rw] "v"(rw), "{v[80:95]}"(pd), [fa] "v"(fa), [fv] "v"(fv)
    : "memory");
}
__device__ __forceinline__ void fin_st0(const u32x16& pd, unsigned rw, unsigned fa, unsigned fv, const char* cb, unsigned co0, unsigned co1, unsigned pa, unsigned& bad, unsigned& pk) {
  asm volatile("1:\n\t"
    "ds_read_b32 v39, %[fa]\n\t"
    "ds_read_b128 v[48:51], %[rw] offset:0\n\t"
    "ds_read_b128 v[52:55], %[rw] offset:1024\n\t"
    "ds_read_b128 v[56:59], %[rw] offset:2048\n\t"
    "ds_read_b128 v[60:63], %[rw] offset:3072\n\t"
    "s_waitcnt lgkmcnt(2)\n\t"
    "v_cmp_ne_u32 vcc, v39, %[fv]\n\t"
    "s_cbranch_vccz 2f\n\t"
    "s_waitcnt lgkmcnt(0)\n\t"
    "s_branch 1b\n\t"
    "2:\n\t"
    "v_pk_add_f32 v[48:49], v[48:49], v[80:81]\n\t"
    "v_pk_add_f32 v[50:51], v[50:51], v[82:83]\n\t"
    "v_pk_add_f32 v[52:53], v[52:53], v[84:85]\n\t"
    "v_pk_add_f32 v[54:55], v[54:55], v[86:87]\n\t"
    "v_cvt_pk_bf16_f32 v40, v48, v49\n\t"
    "v_cvt_pk_bf16_f32 v41, v50, v51\n\t"
    "v_cvt_pk_bf16_f32 v42, v52, v53\n\t"
    "v_cvt_pk_bf16_f32 v43, v54, v55\n\t"
    "global_store_dwordx4 %[co0], v[40:43], %[cb] sc1\n\t"
    "s_waitcnt lgkmcnt(0)\n\t"
    "v_mov_b32 v39, %[pa]\n\t"
    "ds_read_b32 v39, v39\n\t"
    "v_pk_add_f32 v[56:57], v[56:57], v[88:89]\n\t"
    "v_pk_add_f32 v[58:59], v[58:59], v[90:91]\n\t"
    "v_pk_add_f32 v[60:61], v[60:61], v[92:93]\n\t"
    "v_pk_add_f32 v[62:63], v[62:63], v[94:95]\n\t"
    "v_cvt_pk_bf16_f32 v44, v56, v57\n\t"
    "v_cvt_pk_bf16_f32 v45, v58, v59\n\t"
    "v_cvt_pk_bf16_f32 v46, v60, v61\n\t"
    "v_cvt_pk_bf16_f32 v47, v62, v63\n\t"
    "global_store_dwordx4 %[co1], v[44:47], %[cb] sc1\n\t"
    "v_cmp_u_f32 vcc, v48, v56\n\t"
    "s_waitcnt lgkmcnt(0)\n\t"
    "s_or_b32 %[bad], vcc_lo, vcc_hi\n\t"
    "v_readfirstlane_b32 %[pk], v39"
    : [bad] "=&s"(bad), [pk] "=&s"(pk)
    : [rw] "v"(rw), "{v[80:95]}"(pd), [fa] "v"(fa), [fv] "v"(fv), [cb] "s"(cb), [co0] "v"(co0), [co1] "v"(co1), [pa] "s"(pa)
    : "v39", "v40", "v41", "v42", "v43", "v44", "v45", "v46", "v47", "v48", "v49", "v50", "v51", "v52", "v53", "v54", "v55", "v56", "v57", "v58", "v59", "v60", "v61", "v62", "v63", "vcc", "scc", "memory");
}
__device__ __forceinline__ void fin_st1(const u32x16& pd, unsigned rw, unsigned fa, unsigned fv, const char* cb, unsigned co0, unsigned co1, unsigned pa, unsigned& bad, unsigned& pk) {
  asm volatile("1:\n\t"
    "ds_read_b32 v39, %[fa]\n\t"
    "ds_read_b128 v[48:51], %[rw] offset:0\n\t"
    "ds_read_b128 v[52:55], %[rw] offset:1024\n\t"
    "ds_read_b128 v[56:59], %[rw] offset:2048\n\t"
    "ds_read_b128 v[60:63], %[rw] offset:3072\n\t"
    "s_waitcnt lgkmcnt(2)\n\t"
    "v_cmp_ne_u32 vcc, v39, %[fv]\n\t"
    "s_cbranch_vccz 2f\n\t"
    "s_waitcnt lgkmcnt(0)\n\t"
    "s_branch 1b\n\t"
    "2:\n\t"
    "v_pk_add_f32 v[48:49], v[48:49], v[80:81]\n\t"
    "v_pk_add_f32 v[50:51], v[50:51], v[82:83]\n\t"
    "v_pk_add_f32 v[52:53], v[52:53], v[84:85]\n\t"
    "v_pk_add_f32 v[54:55], v[54:55], v[86:87]\n\t"
    "s_waitcnt lgkmcnt(0)\n\t"
    "v_mov_b32 v39, %[pa]\n\t"
    "ds_read_b32 v39, v39\n\t"
    "v_pk_add_f32 v[56:57], v[56:57], v[88:89]\n\t"
    "v_pk_add_f32 v[58:59], v[58:59], v[90:91]\n\t"
    "v_pk_add_f32 v[60:61], v[60:61], v[92:93]\n\t"
    "v_pk_add_f32 v[62:63], v[62:63], v[94:95]\n\t"
    "v_mul_f32 v40, %[nl], v48\n\t"
    "v_mul_f32 v41, %[nl], v49\n\t"
    "v_mul_f32 v42, %[nl], v50\n\t"
    "v_mul_f32 v43, %[nl], v51\n\t"
    "v_mul_f32 v44, %[nl], v56\n\t"
    "v_mul_f32 v45, %[nl], v57\n\t"
    "v_mul_f32 v46, %[nl], v58\n\t"
    "v_mul_f32 v47, %[nl], v59\n\t"
    "v_exp_f32 v40, v40\n\t"
    "v_exp_f32 v41, v41\n\t"
    "v_exp_f32 v42, v42\n\t"
    "v_exp_f32 v43, v43\n\t"
    "v_exp_f32 v44, v44\n\t"
    "v_exp_f32 v45, v45\n\t"
    "v_exp_f32 v46, v46\n\t"
    "v_exp_f32 v47, v47\n\t"
    "v_pk_add_f32 v[40:41], v[40:41], 1.0 op_sel_hi:[1,0]\n\t"
    "v_pk_add_f32 v[42:43], v[42:43], 1.0 op_sel_hi:[1,0]\n\t"
    "v_pk_add_f32 v[44:45], v[44:45], 1.0 op_sel_hi:[1,0]\n\t"
    "v_pk_add_f32 v[46:47], v[46:47], 1.0 op_sel_hi:[1,0]\n\t"
    "v_rcp_f32 v40, v40\n\t"
    "v_rcp_f32 v41, v41\n\t"
    "v_rcp_f32 v42, v42\n\t"
    "v_rcp_f32 v43, v43\n\t"
    "v_rcp_f32 v44, v44\n\t"
    "v_rcp_f32 v45, v45\n\t"
    "v_rcp_f32 v46, v46\n\t"
    "v_rcp_f32 v47, v47\n\t"
    "v_pk_mul_f32 v[40:41], v[48:49], v[40:41]\n\t"
    "v_pk_mul_f32 v[42:43], v[50:51], v[42:43]\n\t"
    "v_pk_mul_f32 v[44:45], v[56:57], v[44:45]\n\t"
    "v_pk_mul_f32 v[46:47], v[58:59], v[46:47]\n\t"
    "v_pk_mul_f32 v[40:41], v[40:41], v[52:53]\n\t"
    "v_pk_mul_f32 v[42:43], v[42:43], v[54:55]\n\t"
    "v_pk_mul_f32 v[44:45], v[44:45], v[60:61]\n\t"
    "v_pk_mul_f32 v[46:47], v[46:47], v[62:63]\n\t"
    "v_cvt_pk_bf16_f32 v48, v40, v41\n\t"
    "v_cvt_pk_bf16_f32 v49, v42, v43\n\t"
    "v_cvt_pk_bf16_f32 v50, v44, v45\n\t"
    "v_cvt_pk_bf16_f32 v51, v46, v47\n\t"
    "s_nop 1\n\t"
    "v_permlane16_swap_b32 v48, v50\n\t"
    "v_permlane16_swap_b32 v49, v51\n\t"
    "global_store_dwordx4 %[co0], v[48:51], %[cb] sc1\n\t"
    "v_cmp_u_f32 vcc, v52, v60\n\t"
    "s_waitcnt lgkmcnt(0)\n\t"
    "s_or_b32 %[bad], vcc_lo, vcc_hi\n\t"
    "v_readfirstlane_b32 %[pk], v39"
    : [bad] "=&s"(bad), [pk] "=&s"(pk)
    : [rw] "v"(rw), "{v[80:95]}"(pd), [fa] "v"(fa), [fv] "v"(fv), [cb] "s"(cb), [co0] "v"(co0), [nl] "s"(-LOG2E), [pa] "s"(pa)
    : "v39", "v40", "v41", "v42", "v43", "v44", "v45", "v46", "v47", "v48", "v49", "v50", "v51", "v52", "v53", "v54", "v55", "v56", "v57", "v58", "v59", "v60", "v61", "v62", "v63", "vcc", "scc", "memory");
}
__device__ __forceinline__ void fin_st2(const u32x16& pd, unsigned rw, unsigned fa, unsigned fv, const char* cb, unsigned co0, unsigned co1, unsigned pa, unsigned& bad, unsigned& pk) {
  asm volatile("s_nop 1\n\t"
    "v_cvt_pk_bf16_f32 v40, v80, v81\n\t"
    "v_cvt_pk_bf16_f32 v41, v82, v83\n\t"
    "v_cvt_pk_bf16_f32 v42, v84, v85\n\t"
    "v_cvt_pk_bf16_f32 v43, v86, v87\n\t"
    "global_store_dwordx4 %[co0], v[40:43], %[cb] sc1\n\t"
    "v_cvt_pk_bf16_f32 v44, v88, v89\n\t"
    "v_cvt_pk_bf16_f32 v45, v90, v91\n\t"
    "v_cvt_pk_bf16_f32 v46, v92, v93\n\t"
    "v_cvt_pk_bf16_f32 v47, v94, v95\n\t"
    "global_store_dwordx4 %[co1], v[44:47], %[cb] sc1\n\t"
    "1:\n\t"
    "ds_read_b32 v39, %[fa]\n\t"
    "ds_read_b128 v[48:51], %[rw] offset:0\n\t"
    "ds_read_b128 v[52:55], %[rw] offset:1024\n\t"
    "ds_read_b128 v[56:59], %[rw] offset:2048\n\t"
    "ds_read_b128 v[60:63], %[rw] offset:3072\n\t"
    "s_waitcnt lgkmcnt(2)\n\t"
    "v_cmp_ne_u32 vcc, v39, %[fv]\n\t"
    "s_cbranch_vccz 2f\n\t"
    "s_waitcnt lgkmcnt(0)\n\t"
    "s_branch 1b\n\t"
    "2:\n\t"
    "v_cvt_pk_bf16_f32 v48, v48, v49\n\t"
    "v_cvt_pk_bf16_f32 v49, v50, v51\n\t"
    "v_cvt_pk_bf16_f32 v50, v52, v53\n\t"
    "v_cvt_pk_bf16_f32 v51, v54, v55\n\t"
    "global_store_dwordx4 %[co0], v[48:51], %[cb] offset:2048 sc1\n\t"
    "s_waitcnt lgkmcnt(0)\n\t"
    "v_mov_b32 v39, %[pa]\n\t"
    "ds_read_b32 v39, v39\n\t"
    "v_cvt_pk_bf16_f32 v56, v56, v57\n\t"
    "v_cvt_pk_bf16_f32 v57, v58, v59\n\t"
    "v_cvt_pk_bf16_f32 v58, v60, v61\n\t"
    "v_cvt_pk_bf16_f32 v59, v62, v63\n\t"
    "global_store_dwordx4 %[co1], v[56:59], %[cb] offset:2048 sc1\n\t"
    "v_cmp_u_f32 vcc, v80, v88\n\t"
    "s_waitcnt lgkmcnt(0)\n\t"
    "s_or_b32 %[bad], vcc_lo, vcc_hi\n\t"
    "v_readfirstlane_b32 %[pk], v39"
    : [bad] "=&s"(bad), [pk] "=&s"(pk)
    : [rw] "v"(rw), "{v[80:95]}"(pd), [fa] "v"(fa), [fv] "v"(fv), [cb] "s"(cb), [co0] "v"(co0), [co1] "v"(co1), [pa] "s"(pa)
    : "v39", "v40", "v41", "v42", "v43", "v44", "v45", "v46", "v47", "v48", "v49", "v50", "v51", "v52", "v53", "v54", "v55", "v56", "v57", "v58", "v59", "v60", "v61", "v62", "v63", "vcc", "scc", "memory");
}
template <int R> __device__ __forceinline__ void gw_load(u32x32& w, unsigned a0, unsigned a1);
template <> __device__ __forceinline__ void gw_load<0>(u32x32& w, unsigned a0, unsigned a1) {
  asm volatile("ds_read_b64_tr_b16 v[128:129], %1 offset:0\n\t"
    "ds_read_b64_tr_b16 v[130:131], %1 offset:512\n\t"
    "ds_read_b64_tr_b16 v[132:133], %2 offset:0\n\t"
    "ds_read_b64_tr_b16 v[134:135], %2 offset:512\n\t"
    "ds_read_b64_tr_b16 v[136:137], %1 offset:4096\n\t"
    "ds_read_b64_tr_b16 v[138:139], %1 offset:4608\n\t"
    "ds_read_b64_tr_b16 v[140:141], %2 offset:4096\n\t"
    "ds_read_b64_tr_b16 v[142:143], %2 offset:4608\n\t"
    "s_waitcnt lgkmcnt(0)\n\t"
    "ds_read_b64_tr_b16 v[144:145], %1 offset:16384\n\t"
    "ds_read_b64_tr_b16 v[146:147], %1 offset:16896\n\t"
    "ds_read_b64_tr_b16 v[148:149], %2 offset:16384\n\t"
    "ds_read_b64_tr_b16 v[150:151], %2 offset:16896\n\t"
    "ds_read_b64_tr_b16 v[152:153], %1 offset:20480\n\t"
    "ds_read_b64_tr_b16 v[154:155], %1 offset:20992\n\t"
    "ds_read_b64_tr_b16 v[156:157], %2 offset:20480\n\t"
    "ds_read_b64_tr_b16 v[158:159], %2 offset:20992\n\t"
    "s_waitcnt lgkmcnt(0)"
    : "=&{v[128:159]}"(w) : "v"(a0), "v"(a1) : "memory");
}
template <> __device__ __forceinline__ void gw_load<1>(u32x32& w, unsigned a0, unsigned a1) {
  asm volatile("ds_read_b64_tr_b16 v[160:161], %1 offset:0\n\t"
    "ds_read_b64_tr_b16 v[162:163], %1 offset:512\n\t"
    "ds_read_b64_tr_b16 v[164:165], %2 offset:0\n\t"
    "ds_read_b64_tr_b16 v[166:167], %2 offset:512\n\t"
    "ds_read_b64_tr_b16 v[168:169], %1 offset:4096\n\t"
    "ds_read_b64_tr_b16 v[170:171], %1 offset:4608\n\t"
    "ds_read_b64_tr_b16 v[172:173], %2 offset:4096\n\t"
    "ds_read_b64_tr_b16 v[174:175], %2 offset:4608\n\t"
    "s_waitcnt lgkmcnt(0)\n\t"
    "ds_read_b64_tr_b16 v[176:177], %1 offset:16384\n\t"
    "ds_read_b64_tr_b16 v[178:179], %1 offset:16896\n\t"
    "ds_read_b64_tr_b16 v[180:181], %2 offset:16384\n\t"
    "ds_read_b64_tr_b16 v[182:183], %2 offset:16896\n\t"
    "ds_read_b64_tr_b16 v[184:185], %1 offset:20480\n\t"
    "ds_read_b64_tr_b16 v[186:187], %1 offset:20992\n\t"
    "ds_read_b64_tr_b16 v[188:189], %2 offset:20480\n\t"
    "ds_read_b64_tr_b16 v[190:191], %2 offset:20992\n\t"
    "s_waitcnt lgkmcnt(0)"
    : "=&{v[160:191]}"(w) : "v"(a0), "v"(a1) : "memory");
}
template <> __device__ __forceinline__ void gw_load<2>(u32x32& w, unsigned a0, unsigned a1) {
  asm volatile("ds_read_b64_tr_b16 v[192:193], %1 offset:0\n\t"
    "ds_read_b64_tr_b16 v[194:195], %1 offset:512\n\t"
    "ds_read_b64_tr_b16 v[196:197], %2 offset:0\n\t"
    "ds_read_b64_tr_b16 v[198:199], %2 offset:512\n\t"
    "ds_read_b64_tr_b16 v[200:201], %1 offset:4096\n\t"
    "ds_read_b64_tr_b16 v[202:203], %1 offset:4608\n\t"
    "ds_read_b64_tr_b16 v[204:205], %2 offset:4096\n\t"
    "ds_read_b64_tr_b16 v[206:207], %2 offset:4608\n\t"
    "s_waitcnt lgkmcnt(0)\n\t"
    "ds_read_b64_tr_b16 v[208:209], %1 offset:16384\n\t"
    "ds_read_b64_tr_b16 v[210:211], %1 offset:16896\n\t"
    "ds_read_b64_tr_b16 v[212:213], %2 offset:16384\n\t"
    "ds_read_b64_tr_b16 v[214:215], %2 offset:16896\n\t"
    "ds_read_b64_tr_b16 v[216:217], %1 offset:20480\n\t"
    "ds_read_b64_tr_b16 v[218:219], %1 offset:20992\n\t"
    "ds_read_b64_tr_b16 v[220:221], %2 offset:20480\n\t"
    "ds_read_b64_tr_b16 v[222:223], %2 offset:20992\n\t"
    "s_waitcnt lgkmcnt(0)"
    : "=&{v[192:223]}"(w) : "v"(a0), "v"(a1) : "memory");
}
template <> __device__ __forceinline__ void gw_load<3>(u32x32& w, unsigned a0, unsigned a1) {
  asm volatile("ds_read_b64_tr_b16 v[224:225], %1 offset:0\n\t"
    "ds_read_b64_tr_b16 v[226:227], %1 offset:512\n\t"
    "ds_read_b64_tr_b16 v[228:229], %2 offset:0\n\t"
    "ds_read_b64_tr_b16 v[230:231], %2 offset:512\n\t"
    "ds_read_b64_tr_b16 v[232:233], %1 offset:4096\n\t"
    "ds_read_b64_tr_b16 v[234:235], %1 offset:4608\n\t"
    "ds_read_b64_tr_b16 v[236:237], %2 offset:4096\n\t"
    "ds_read_b64_tr_b16 v[238:239], %2 offset:4608\n\t"
    "s_waitcnt lgkmcnt(0)\n\t"
    "ds_read_b64_tr_b16 v[240:241], %1 offset:16384\n\t"
    "ds_read_b64_tr_b16 v[242:243], %1 offset:16896\n\t"
    "ds_read_b64_tr_b16 v[244:245], %2 offset:16384\n\t"
    "ds_read_b64_tr_b16 v[246:247], %2 offset:16896\n\t"
    "ds_read_b64_tr_b16 v[248:249], %1 offset:20480\n\t"
    "ds_read_b64_tr_b16 v[250:251], %1 offset:20992\n\t"
    "ds_read_b64_tr_b16 v[252:253], %2 offset:20480\n\t"
    "ds_read_b64_tr_b16 v[254:255], %2 offset:20992\n\t"
    "s_waitcnt lgkmcnt(0)"
    : "=&{v[224:255]}"(w) : "v"(a0), "v"(a1) : "memory");
}
template <int I, int E, class F> __device__ __forceinline__ void sfor(F&& f) { if constexpr (I < E) { f(std::integral_constant<int, I>{}); sfor<I + 1, E>(f); } }
#define W2C 0
#define FL63 0
template <int ROLE, int KQ, class F>
__device__ __forceinline__ void wload(char* smem, int wave_id_, const u16* W0, const u16* W1, int ldb, u32x32 (&bw)[4], int rot, F&& hook) {
  constexpr int ABUF = 65536;
  const int tid = opaque_tid(), lane = tid & 63, wave = WAVE_ID;
  const int kq = KQ == 2 ? wave >> 2 : 0, nq = KQ == 2 ? wave & 3 : wave;
  const int g = lane >> 4, r16 = lane & 15, q4 = r16 >> 2, p4 = lane & 3;
  const unsigned ldsb = __builtin_amdgcn_readfirstlane((unsigned)(size_t)(LAS char*)smem);
  {
    const int srow = 8 * wave + (lane >> 3), ch = (lane & 7) ^ bswz(srow);
    const unsigned voff = (unsigned)(srow * ldb + ch * 8) * 2;
    const char* w0 = uni((const char*)W0); const char* w1 = uni((const char*)(ROLE == R_F1 ? W1 : W0 + 64));
    const size_t kb = (size_t)64 * ldb * 2;
    auto wstage = [&](int r) {
      const unsigned ld = ldsb + (r & 1) * ABUF + wave * 1024;
#pragma unroll
      for (int i = 0; i < 8; ++i) {
        const int kbi = KQ == 2 ? (ROLE == R_PART2 ? 0 : (i >> 2) * 8) + ((2 * r + ((i >> 1) & 1)) ^ rot) : 2 * r + (i >> 2);
        const char* src = KQ == 2 ? ((i & 1) ? w1 : w0) + kbi * kb + (ROLE == R_PART2 ? (i >> 2) * 256 : 0) : w0 + kbi * kb + (i & 3) * 128;
        unsigned keep;
        if (W2C && (ROLE == R_PART || ROLE == R_PART2)) glds16s<0>(src, voff, ld + i * 8192);
        else
        asm volatile("s_mov_b32 %0, m0\n\ts_mov_b32 m0, %3\n\ts_nop 2\n\tglobal_load_lds_dwordx4 %1, %2" WNT_ "\n\ts_mov_b32 m0, %0"
                     : "=&s"(keep) : "v"(voff), "s"(src), "s"(ld + i * 8192) : "memory");
      }
    };
    const int brow = 8 * g + q4, bs = bswz(brow);
    unsigned la[2];
#pragma unroll
    for (int j = 0; j < 2; ++j) {
      int blk, c16, sub;
      if (ROLE == R_F1) { blk = j; c16 = nq * 2 + (p4 >> 1); sub = (p4 & 1) * 8; }
      else { blk = nq >> 1; c16 = (nq & 1) * 4 + p4; sub = j * 8; }
      int set = kq;
      if (ROLE == R_PART2 && TLP) { set = nq >> 1; blk = nq & 1; c16 = kq * 4 + p4; }
      la[j] = (unsigned)((KQ == 2 ? set * 4 + blk : blk) * 8192 + brow * 128 + ((c16 ^ bs) << 4) + sub);
    }
    hook(4);
    wstage(0); wstage(1);
    sfor<0, 4>([&](auto r_c) __attribute__((always_inline)) {
      constexpr int r = decltype(r_c)::value;
      if (r < 3) WAIT_V(8); else WAIT_V(0);
      __builtin_amdgcn_s_barrier(); asm volatile("" ::: "memory");
      gw_load<r>(bw[r], ldsb + (r & 1) * ABUF + la[0], ldsb + (r & 1) * ABUF + la[1]);
      BAR();
      if (r + 2 < 4) wstage(r + 2);
      hook(r);
    });
  }
}
#define DSR2(d0, d1, a0_, a1_, off) asm volatile("ds_read_b128 %0, %2 offset:%4\n\tds_read_b128 %1, %3 offset:%4" : "=&v"(d0), "=&v"(d1) : "v"(a0_), "v"(a1_), "i"(off))
template <int N> __device__ __forceinline__ void waitl(u32x4& a, u32x4& b) { asm volatile("s_waitcnt lgkmcnt(%2)" : "+v"(a), "+v"(b) : "i"(N)); }
__device__ __forceinline__ void glds1(const char* sbase, unsigned voff, unsigned lds) {
  unsigned keep;
  asm volatile("s_mov_b32 %0, m0\n\ts_mov_b32 m0, %3\n\ts_nop 2\n\tglobal_load_lds_dwordx4 %1, %2 sc1\n\ts_mov_b32 m0, %0"
               : "=&s"(keep) : "v"(voff), "s"(sbase), "s"(lds) : "memory");
}
#define XS(i) do { } while (0)
#define XFL(b_) do { } while (0)
#define XA_ 0
#define XF_ 0
#define XE_ 0
#define XP_ 0
#define PUBK 9
#define LATE 0
#define NOWP 0
#define TB 48
#define TDM 1
#define TDA 15
#define TPUB 1
#define DUR0 160
#define PUP 4
#define PDQ 5
#define PDM 2
#define PDEEP 5
template <int ROLE>
__device__ __forceinline__ void gemm7(char* smem, int wave_id_, const u16* W0, const u16* W1, int ldb, const char* A_, size_t a_blk, Dep dep,
                                      char* C_, size_t c_blk, unsigned*& cnt, int myw, unsigned* tmo, int slot, unsigned long long* mts,
                                      bool gl, int nimm = 0, int nseg = NSEG, int wm0 = 0, int pofs = 0, u64 tag = 0, int b0 = 0) {
  dep.base = (const char*)cnt;
  unsigned* myprog = cnt + myw;
  volatile LAS unsigned* wsl = (volatile LAS unsigned*)(smem + L_SLOT + 512);
  if (opaque_tid() < 144) *(volatile LAS unsigned*)(smem + L_SLOT + opaque_tid() * 4) = 0u;
  const int tid = opaque_tid(), lane = tid & 63, wave = WAVE_ID;
  const int kq = wave >> 2, nq = wave & 3;
  const unsigned ldsb = __builtin_amdgcn_readfirstlane((unsigned)(size_t)(LAS char*)smem);
  constexpr int rot = 0;
  const char* A = uni(A_) + nq * 1024;
  const char* Cu = uni(C_);
  constexpr int PD = PDM;
  constexpr bool P2C = ROLE == R_PART2;
  int pw = 0;
  int wm = wm0, nh = 2 * b0;
  constexpr bool SPEC = ROLE != R_QKV;
  const int sbi = gl ? SC : SB, sbo = ROLE == R_F1 || gl ? SC : SB;
  auto lgo = [&](int b, unsigned& v, unsigned& u, bool pre, bool force) {
    if (wave == 4) {
      for (int spins = 0; __builtin_amdgcn_readfirstlane(wsl[2]) != (unsigned)(b + 1); ++spins) {
        if (!pre) ld_fb(v, u, dep.base, dep.voff);
        const bool ok = (int)(v * dep.mul + dep.add) > b;
        if (__all(ok)) break;
        if (SPEC && !force && bells(ok, u, b, b0 ? MARH : MARG, sbi)) break;
        if (!pre) __builtin_amdgcn_s_sleep(2);
        pre = false;
        if (give_up(spins, tmo, 9)) { v = 255u; break; }
      }
      wsl[2] = (unsigned)(b + 1);
    } else {
      while (__builtin_amdgcn_readfirstlane(wsl[2]) != (unsigned)(b + 1)) __builtin_amdgcn_s_sleep(1);
    }
  };
  auto lwm = [&](int b, unsigned v) {
    unsigned w;
    if (wave == 4) {
      const int w2 = (int)dep_eval(v, dep);
      w = (unsigned)min(max(max(w2, wm), b + 1), 255);
      wsl[0] = ((unsigned)(b + 1) << 8) | w;
    } else {
      for (;;) {
        w = __builtin_amdgcn_readfirstlane(wsl[0]);
        if ((w >> 8) == (unsigned)(b + 1)) break;
        __builtin_amdgcn_s_sleep(1);
      }
      w &= 255u;
    }
    return (int)w;
  };
  auto pubw = [&](int n) {
    WAIT_V(0);
    if (lane == 0) {
      const unsigned o = __hip_atomic_fetch_add((LAS unsigned*)(smem + L_SLOT + 516 + ((n + pofs) & 1) * 36), 1u, __ATOMIC_RELAXED, __HIP_MEMORY_SCOPE_WORKGROUP);
      if ((o & 3u) == 3u) {
        st4_sc1(myprog, (unsigned)(n + pofs));
      }
    }
    asm volatile("s_waitcnt lgkmcnt(0)" ::: "memory");
  };
  auto hsrc = [&](int hh) { return A + (size_t)(hh >> 1) * a_blk + ((hh ^ (rot >> 2)) & 1) * 16384; };
  auto hlds = [&](int hh) { return ldsb + (unsigned)(((hh >> 1) & 1) * 65536 + (hh & 1) * 16384 + nq * 1024); };
#define POFF(i_) (((i_) & 1) * 32768 + ((i_) >> 1) * 4096)
#define PIECE(s_, l_, i_) asm volatile("s_add_u32 m0, %2, %3\n\ts_nop 0\n\tglobal_load_lds_dwordx4 %0, %1 sc1" :: "v"(av[i_]), "s"(s_), "s"(l_), "i"(POFF(i_)) : "memory", "scc", "m0")
  int na = max(sbo, b0);
  auto arm = [&](int bb) {
    unsigned z = SENT; asm volatile("" : "+v"(z));
    constexpr int N = ROLE == R_F1 ? 1 : (P2C ? 4 : 2);
    const unsigned o = nq * N * 1024 + lane_id() * 16;
    const char* p = Cu + (size_t)bb * c_blk;
#pragma unroll
    for (int i = 0; i < N; ++i) st16s(p, o + i * 1024, u32x4{z, z, z, z});
  };
  u32x32 BW[4];
  wload<ROLE, 2>(smem, wave_id_, W0, W1, ldb, BW, rot, [&](int r) {
    if (ROLE == R_QKV && nimm > 0 && wave == 0) STS((char*)tmo - OFF_TMO, 128 + slot, 3 + r);
    if (SPEC && r == 2 && (ROLE != R_PART || gl)) { for (na += kq; na < nseg; na += 2) arm(na); na = nseg; }
    if (ROLE == R_QKV && nimm > 0 && r == 0 && wave == 4) init_issue((char*)tmo - OFF_TMO, ldsb + L_RED);
    if (ROLE == R_QKV && nimm > 0 && r == 2) {
      cnt = cnt_of((char*)tmo - OFF_TMO, init_get((char*)tmo - OFF_TMO, tag, smem + L_RED));
      dep.base = (const char*)cnt; myprog = cnt + myw;
      if (kq) {
        unsigned v0 = 0, u0;
        lgo(0, v0, u0, false, true);
        wm = lwm(0, v0);
        const unsigned l16 = lane_id() * 16;
#pragma unroll
        for (int i = 0; i < 16; ++i) glds1(hsrc(i >> 3) + (i & 1) * 32768 + ((((i & 7) >> 1) ^ rot) & 3) * 4096, l16, hlds(i >> 3) + POFF(i & 7));
      }
      nh = 2;
    }
  });
  if (ROLE == R_QKV && nimm > 0 && !kq) wm = (int)(__builtin_amdgcn_readfirstlane(wsl[0]) & 255u);
  if (ROLE == R_QKV && nimm > 0 && opaque_tid() == 0) STS((char*)tmo - OFF_TMO, 128 + slot, 0);
  if (wave == 0) WFB();
  auto body = [&](auto ldr_c) __attribute__((always_inline)) {
    constexpr bool LDR = decltype(ldr_c)::value;
    if (XP_ == 0 ? !LDR : (XP_ == 1 ? LDR : false)) __builtin_amdgcn_s_setprio(1);
    unsigned av[8];
#pragma unroll
    for (int i = 0; i < 8; ++i) av[i] = lane * 16 + (P2C ? 0 : (i & 1) * 32768) + (((i >> 1) ^ rot) & 3) * 4096;
    auto issue_all = [&](int hh) {
      if (LDR) {
        const char* s_ = hsrc(hh); const unsigned l_ = hlds(hh);
#pragma unroll
        for (int i = 0; i < 8; ++i) if (!(P2C && (i & 1))) PIECE(s_, l_, i);
      }
    };
    int b = b0;
    bool pre = false;
    unsigned smu = 0, tk = 0;
    unsigned smp = 0, got = 0;
    while (b < nseg) {
      const bool fast = (wm < nseg ? wm : nseg) >= b + PD && b >= nimm && nh <= 2 * b + 2;
      if (SPEC && b >= TB && !fast) break;
      const bool pl = wm <= b || pre, cold = nh < 2 * b + 2, nh3 = nh == 2 * b + 3;
      if (!fast) {
        XS(0);
        if (LDR && wm <= b && !got) lgo(b, smp, smu, false, true);
        XS(1);
        if (got) nh += NEARLY / 8;
        { const int upto = nh3 ? 2 * b + 4 : 2 * b + 2; for (; nh < upto; ++nh) issue_all(nh); }
        if (LDR && pl) wm = lwm(b, smp);
        XS(2);
      }
      const int ln = lane_id(), g = ln >> 4, r16 = ln & 15;
      const unsigned e0 = ldsb + (unsigned)((P2C ? 0 : kq * 32768) + (b & 1) * 65536 + (gl ? (g >> 1) * 512 + r16 * 32 + (g & 1) * 16 : r16 * 128 + ((g ^ (r16 & 7)) << 4)));
      const unsigned d0 = e0 ^ (gl ? 1024u : 64u);
      const unsigned rw = ldsb + L_RED + nq * 4096 + ln * 16;
      const unsigned co0 = ROLE == R_F1 ? (unsigned)((g & 1) * 2048 + nq * 512 + r16 * 32 + (g >> 1) * 16)
                         : ((ROLE == R_QKV ? TLQ : TLP) ? (unsigned)(nq * (P2C ? 4096 : 2048) + r16 * 64 + g * 16)
                         : (ROLE == R_QKV ? (unsigned)(r16 * 256 + (((4 * nq + g) ^ r16) << 4)) : (unsigned)(r16 * PROW + (32 * nq + 8 * g) * 2)));
      const unsigned co1 = co0 + ((ROLE == R_QKV ? TLQ : TLP) ? 1024u : (ROLE == R_QKV ? 4096u : 16u * PROW));
      u32x16 pv_;
      bool pr2 = false;

      if (fast) {
        __builtin_amdgcn_s_barrier(); asm volatile("" ::: "memory");
        const int LA = (wm < nseg ? wm : nseg) >= b + (ROLE == R_QKV ? PDQ : PDEEP) ? 3 : 2;
        for (; nh < 2 * b + LA; ++nh) issue_all(nh);
        int lf = (wm < nseg ? wm : nseg) - (LA - 1), cp = nseg - (LA - 1);
        const unsigned sa = ldsb + L_SLOT + ln * 4, ad = dep.add - (unsigned)(LA - 1);
        const unsigned up = LA == 3 ? 1000u : (unsigned)PUP;
#define FARGS BW, pv_, e0, d0, b, lf, cp, up, sa, dep.mul, ad, rw, (unsigned)a_blk, hsrc(2 * b + LA), hsrc(2 * b + LA + 1), hlds(2 * b + LA), hlds(2 * b + LA + 1), av
        if constexpr (LDR) (P2C ? fast_ld2 : (ROLE == R_F1 ? fast_ld1 : fast_ld0))(FARGS, LA);
#undef FARGS
        else {
          const char* c0_ = uni((const char*)tmo - OFF_TMO + OFF_DUMP); const char* c1_ = Cu + (size_t)b * c_blk;
          const int sf = b + pofs;
          const char* pg = uni((const char*)myprog);
          (ROLE == R_F1 ? fast_st1 : (P2C ? fast_st2 : fast_st0))(BW, pv_, e0, d0, b, lf, cp, up, sa, dep.mul, ad, rw, (unsigned)c_blk, c0_, c1_, co0, co1, pofs, sf, (unsigned)wave,
                                                                   ldsb + L_SLOT, dep.voff, dep.base, pg);
        }
        wm = lf + LA - 1 > wm ? lf + LA - 1 : wm;
        nh = 2 * b + 2;
        --b;
        __builtin_amdgcn_s_barrier(); asm volatile("" ::: "memory");
      } else {
        if (LDR) { if (nh >= 2 * b + 4) { if (P2C) WAIT_V(14); else WAIT_V(28); } else { if (P2C) WAIT_V(6); else WAIT_V(12); } }
        BAR();
        XS(3);
        if (ROLE == R_QKV && nimm > 0 && b < 2 && opaque_tid() == 0) STS((char*)tmo - OFF_TMO, 128 + slot, 1 + b);
        if constexpr (!LDR) {
          const unsigned wv = wsl[0];
          unsigned st;
          lean_st0(BW, e0, d0, __builtin_amdgcn_readfirstlane(pw > 0 ? (cold ? 4u : 1u) : 0u), pv_, (unsigned)(pw + pofs), ldsb + L_SLOT + 516, uni((const char*)myprog), 0u, tk, st);
          if (pl) wm = (int)(__builtin_amdgcn_readfirstlane(wv) & 255u);
        }
        const bool dm = nh == 2 * b + 2 && wm > b + 1 && b + 1 < nseg;
        pr2 = b + 1 < nseg && wm < nseg;
        if constexpr (LDR) {
          const bool er = nh == 2 * b + 2 && !dm;
          const unsigned ps = pr2 && (wave == 4 || er) ? (unsigned)(b + 1) : 0u;
#define LARGS BW, e0, d0, (unsigned)(nh - 2 * b - 2), hlds(nh), hsrc(nh), hsrc(nh + 1), av, ps, dep.voff, dep.base, \
              rw, ldsb + L_SLOT + 528 + nq * 4, (unsigned)(b + 1), ldsb + L_SLOT + 520, dep.mul, dep.add, er ? b + 1 : 0x7FFFFFFF, 0x7FFFFFFF, smp, smu, got
          if (dm) (P2C ? lean_dm1 : lean_dm0)(LARGS); else (P2C ? lean_ld1 : lean_ld0)(LARGS);
#undef LARGS
          got = __builtin_amdgcn_readfirstlane(got);
        }
        if (dm) nh += 2;
        XS(5);
      }
      unsigned bad = 0, pk = 0;
      if constexpr (LDR) { if (fast) fin_ld(pv_, rw, ldsb + L_SLOT + 528 + nq * 4, (unsigned)(b + 1)); }
      else (ROLE == R_F1 ? fin_st1 : (P2C ? fin_st2 : fin_st0))(pv_, rw, ldsb + L_SLOT + 528 + nq * 4, (unsigned)(b + 1), Cu + (size_t)b * c_blk, co0, co1, ldsb + L_SLOT + 524, bad, pk);
      XS(6);
      pre = pr2;
      bool now = (nh < 2 * b + 4 && wm < b + 1 + PD) || b + 1 >= nseg || b < nimm;
      if (!LDR && now) {
        if (NOWP == 0 && b >= nimm && pk == (unsigned)(b + 2)) now = false;
        else pubw(b + 1);
      }
      XS(7);
      XFL(b);
      pw = now ? 0 : b + 1;
      if (SPEC && !LDR && !fast && b >= 16 && na < nseg && na > b + 1) { arm(na); arm(na + 1); na += 2; }
      ++b;
    }
    if (SPEC && b < nseg) {
      static_assert(!SPEC || (NEARLY == 16 && TLP), "");
      const int ln = lane_id(), g = ln >> 4, r16 = ln & 15;
      const unsigned e0 = ldsb + (unsigned)((P2C ? 0 : kq * 32768) + (gl ? (g >> 1) * 512 + r16 * 32 + (g & 1) * 16 : r16 * 128 + ((g ^ (r16 & 7)) << 4)));
      const unsigned rw = ldsb + L_RED + nq * 4096 + ln * 16;
      const unsigned co0 = ROLE == R_F1 ? (unsigned)((g & 1) * 2048 + nq * 512 + r16 * 32 + (g >> 1) * 16) : (unsigned)(nq * (P2C ? 4096 : 2048) + r16 * 64 + g * 16);
      const char* pg = uni((const char*)myprog);
      unsigned fa = ldsb + L_SLOT + 528 + nq * 4; asm volatile("" : "+v"(fa));
      bool force = false, dmx = false;
      unsigned dur = DUR0, dp = 0, x8 = __builtin_amdgcn_readfirstlane(wave == 0 ? 8u : 0u);
      if (nh >= 2 * b + 2) got = 1;
      pre = false;
      for (;;) {
        unsigned rd;
        XS(0);
        if (LDR && b < nseg) {
          if (!got) { lgo(b, smp, smu, pre, force); issue_all(2 * b); issue_all(2 * b + 1); }
          XS(2);
          if (P2C) WAIT_V(6); else WAIT_V(12);
        }
        if (!LDR && b >= sbo && na <= nseg) { for (na = max(na, b); na < nseg; ++na) arm(na); WAIT_V(0); ++na; }
        const unsigned ea = e0 + (b & 1) * 65536;
        BAR();
        XS(3);
        if (b >= nseg) rd = __builtin_amdgcn_readfirstlane(wsl[8]);
        else if constexpr (LDR) {
          const int th = b + 1 < nseg ? b + 1 : 0;
#define LARGS BW, ea, ea ^ (gl ? 1024u : 64u), 0u, hlds(2 * b + 2), hsrc(2 * b + 2), hsrc(2 * b + 3), av, (unsigned)th, dep.voff, dep.base, \
              rw, ldsb + L_SLOT + 528 + nq * 4, (unsigned)(b + 1), ldsb + L_SLOT + 520, dep.mul, dep.add, th, th >= sbi ? th : 0x7FFFFFFF, smp, smu, got
          if (dmx) {
            *(volatile LAS u32x2*)(wsl + 2) = u32x2{(unsigned)(b + 2), (unsigned)(b + 2)};
            (P2C ? lean_dm1 : lean_dm0)(LARGS);
          } else (P2C ? lean_ld1 : lean_ld0)(LARGS);
#undef LARGS
          got = __builtin_amdgcn_readfirstlane(got) | (dmx ? 1u : 0u);
          rd = got >> 1;
          dmx = TDM && got == 1u && b + 2 < nseg
             && __all((int)(smp * dep.mul + dep.add) > b + 2 || (b + 2 >= sbi && (int)(smu >> 24) + ((int)((rt_now() + (dmx ? TDA : 0) - MARG - smu) << 8) >> 31) > b + 2));
          pre = true;
        } else {
          unsigned bad, pk;
          (ROLE == R_F1 ? lean_ep1 : (P2C ? lean_ep2 : lean_ep0))(BW, ea, ea ^ (gl ? 1024u : 64u), (unsigned)min(pw, 1) | (b >= sbo - 2 ? x8 : 0u), (unsigned)pw, ldsb + L_SLOT + 516, pg,
              (unsigned)(b + 1) << 24 | dur, tk, rd, rw, fa, (unsigned)(b + 1), Cu + (size_t)b * c_blk, co0, co0 + 1024u, bad, pk);
          XS(6);
          if (!rd) {
            pw = b + 1;
            { const unsigned d = min(tk, 300u); dur = max(d, dp); dp = d; }
            if (bad && !force && b >= sbi) wsl[8] = 1u;
            else if (b == sbo - 1 || pk != (unsigned)(b + 2)) {
              pubw(b + 1);
              pw = 0;
            }
            if (na < nseg && na > b + 1) { arm(na); arm(na + 1); na += 2; }
          }
        }
        XS(7);
        if (rd) {
          WAIT_V(0);
          BAR();
          if (wave == 4 && ln < 11) wsl[ln] = 0u;
          BAR();
          --b; pw = 0; pre = false; got = 0; force = true;
          continue;
        }
        if (b >= nseg) break;
        force = false;
        ++b;
      }
    }
    __builtin_amdgcn_s_setprio(0);
  };
  if (kq) body(std::true_type{}); else body(std::false_type{});
#undef PIECE
#undef POFF
  WAIT_V(0);
  BAR();
}
__device__ __forceinline__ void fin_role(char* smem, int wave_id_, int fidx, const float* mod, const char* P2, const u16* x1, u16* out, unsigned* cnt, unsigned* tmo,
                                         unsigned long long* mts) {
  const int tid = opaque_tid(), lane = tid & 63, wave = WAVE_ID;
  row_table(smem + L_GB, mod, cnt, tmo, tid);
  volatile LAS unsigned* wsl = (volatile LAS unsigned*)(smem + L_SLOT + 512);
  int wf = 0, par = 0;
  const unsigned pf2 = (unsigned)(PF2 + ((lane & 31) < 28 ? (lane & 31) : 0)) * 128, pln = (unsigned)((PLN + (lane >> 3)) * 128 + (lane & 7) * 4);
  for (int nf = fidx >> 3; nf < NSEG; nf += !FL63 || nf < NSEG - 3 ? 2 : (nf == NSEG - 2 ? 1 : 3)) {
    if (wf <= nf) {
      if (wave == 0) {
        unsigned w;
        for (int spins = 0;; ++spins) {
          unsigned v, u;
          ld_fb(v, u, cnt, pf2 + (nf >= HB ? 4 : 0));
          if (!__all(v > (unsigned)nf) && bells(v > (unsigned)nf, u, nf, nf >= HB ? MARL : MARF, SC)) { w = 0; break; }
          v = rowmin16(v);
          w = min(rdl(v, 0), rdl(v, 16));
          if ((int)w > nf) break;
          __builtin_amdgcn_s_sleep(2);
          if (give_up(spins, tmo, 9)) { w = NSEG; break; }
        }
        if (lane == 0) wsl[par] = w;
      }
      BAR();
      wf = (int)__builtin_amdgcn_readfirstlane(wsl[par]);
      par ^= 1;
    }
    int ln = lane; asm volatile("" : "+v"(ln));
    if (wave == 0) WTS(256 + 8 + fidx, nf / 2, 0);
    bool fin = nf < SB;
    for (int spins = 0; !fin_half(fin, P2, x1, smem + L_GB + 12288, out, nf * 32 + 4 * (fidx & 7) + (wave >> 1), wave & 1, ln); ++spins)
      fin = __all(ld4_sc1((const char*)cnt + pf2 + (nf >= HB ? 4 : 0)) > (unsigned)nf && ld4_sc1((const char*)cnt + pln) > (unsigned)nf) || give_up(spins, tmo, 9);
    if (wave == 0) WTS(256 + 8 + fidx, nf / 2, 2);
  }
}
#define LD8(r, vo, sb) asm volatile("global_load_dwordx4 %0, %8, %12 sc1\n\tglobal_load_dwordx4 %1, %9, %12 sc1\n\t" \
    "global_load_dwordx4 %2, %10, %12 sc1\n\tglobal_load_dwordx4 %3, %11, %12 sc1\n\t" \
    "global_load_dwordx4 %4, %8, %13 sc1\n\tglobal_load_dwordx4 %5, %9, %13 sc1\n\t" \
    "global_load_dwordx4 %6, %10, %13 sc1\n\tglobal_load_dwordx4 %7, %11, %13 sc1" \
    : "=&v"(r[0][0]), "=&v"(r[0][1]), "=&v"(r[0][2]), "=&v"(r[0][3]), "=&v"(r[1][0]), "=&v"(r[1][1]), "=&v"(r[1][2]), "=&v"(r[1][3]) \
    : "v"(vo), "v"(vo ^ (TLQ ? 2048u : 64u)), "v"(vo ^ (TLQ ? 4096u : 128u)), "v"(vo ^ (TLQ ? 6144u : 192u)), "s"(sb), "s"((sb) + (TLQ ? 1024 : 4096)) : "memory")
#define TIE8(r) "+v"(r[0][0]), "+v"(r[0][1]), "+v"(r[0][2]), "+v"(r[0][3]), "+v"(r[1][0]), "+v"(r[1][1]), "+v"(r[1][2]), "+v"(r[1][3])
#define SADL 255
#define SAE 110
#define DSA 20
#define SASL 8
__device__ __forceinline__ void sa_role(char* smem, int wave_id_, int hbase, const char* __restrict__ Q, const char* __restrict__ Kx, const char* __restrict__ V,
                                        const char* kvm, char* __restrict__ O, unsigned* cnt, unsigned* tmo, int slot, unsigned long long* mts) {
  constexpr float LS = LOG2E * SCALE;
  const int tid = opaque_tid(), wave = WAVE_ID;
  const int h = hbase + (wave & 1), s0 = wave >> 1;
  char* vl = smem + wave * 17408;
  const unsigned ldsw = __builtin_amdgcn_readfirstlane((unsigned)(size_t)(LAS char*)vl);
  unsigned avail = 0;
  {
    static_assert(SB % 4 == 0, "");
    unsigned z = SENT; asm volatile("" : "+v"(z));
    for (int s = SB + s0; s < NSEG; s += 4)
#pragma unroll
      for (int i = 0; i < 8; ++i) st16s(uni(O + ((size_t)s * 64 + 2 * h) * 4096), (unsigned)(i * 1024 + (tid & 63) * 16), u32x4{z, z, z, z});
  }
  for (int s = s0, it = 1; s < NSEG; s += 4, ++it) {
    u32x4 qv[2][4], kv[2][4];
    int lane = tid & 63; asm volatile("" : "+v"(lane));
    const int g = lane >> 4, r16 = lane & 15, q4 = r16 >> 2, p4 = lane & 3;
    auto request = [&](int sr) {
      const size_t o = ((size_t)sr * NH + h) * 8192;
      const char* qs = uni(Q + o); const char* ks = uni(Kx + o); const char* vs = uni(V + o);
      const unsigned fo = TLQ ? (unsigned)(r16 * 64 + g * 16) : (unsigned)(r16 * 256 + ((g ^ r16) << 4));
      LD8(qv, fo, qs); LD8(kv, fo, ks);
      head_to_lds(vs, lane, ldsw);
    };
    {
      for (int spins = 0; avail <= (unsigned)s; ++spins) {
        const unsigned p = ld4_sc1(cnt + (PQKV + h) * 32 + (lane & 15));
        avail = min(min(qkv_avail(p, 0), qkv_avail(p, 1)), qkv_avail(p, 2));
        if (avail > (unsigned)s) break;
        __builtin_amdgcn_s_sleep(32);
        if (give_up(spins, tmo, 10)) break;
      }
      request(s);
      asm volatile("s_waitcnt vmcnt(0)" : TIE8(qv), TIE8(kv) :: "memory");
    }
    WTM(slot, s, 0);
    f32x4 s00 = f32x4{0, 0, 0, 0}, s01 = s00, s11 = s00;
#pragma unroll
    for (int ks = 0; ks < 4; ++ks) {
      s00 = MFMA32(asbf8(kv[0][ks]), asbf8(qv[0][ks]), s00);
      s01 = MFMA32(asbf8(kv[0][ks]), asbf8(qv[1][ks]), s01);
      s11 = MFMA32(asbf8(kv[1][ks]), asbf8(qv[1][ks]), s11);
    }
    float mx[2], sum[2];
    u32x4 pseg[2];
    {
#pragma unroll
      for (int r = 0; r < 4; ++r) { const bool ok = 4 * g + r <= r16; s00[r] = ok ? s00[r] : -3.0e38f; s11[r] = ok ? s11[r] : -3.0e38f; }
      mx[0] = red16_32_max(max3(max3(s00[0], s00[1], s00[2]), s00[3], -1.0e38f));
      mx[1] = red16_32_max(max3(max3(fmaxf(fmaxf(s01[0], s01[1]), fmaxf(s01[2], s01[3])), s11[0], -1.0e38f), max3(s11[1], s11[2], s11[3]), -1.0e38f));
      const float n0 = -mx[0] * LS, n1 = -mx[1] * LS;
#pragma unroll
      for (int r = 0; r < 4; ++r) {
        s00[r] = __builtin_amdgcn_exp2f(s00[r] * LS + n0); s01[r] = __builtin_amdgcn_exp2f(s01[r] * LS + n1); s11[r] = __builtin_amdgcn_exp2f(s11[r] * LS + n1);
      }
      sum[0] = (s00[0] + s00[1]) + (s00[2] + s00[3]);
      sum[1] = ((s01[0] + s01[1]) + (s01[2] + s01[3])) + ((s11[0] + s11[1]) + (s11[2] + s11[3]));
      pseg[0] = u32x4{pack2(s00[0], s00[1]), pack2(s00[2], s00[3]), 0u, 0u};
      pseg[1] = u32x4{pack2(s01[0], s01[1]), pack2(s01[2], s01[3]), pack2(s11[0], s11[1]), pack2(s11[2], s11[3])};
    }
    f32x4 o[8][2];
#pragma unroll
    for (int dt = 0; dt < 8; ++dt) {
      const int row0 = 4 * g + q4;
      const unsigned xo = (unsigned)(row0 * 256 + (((4 * (dt >> 1) + p4) ^ row0) << 4) + (dt & 1) * 8);
      const u32x2 lo = trread(vl, xo), hi = trread(vl, xo + 4096);
      o[dt][0] = MFMA32(asbf8(u32x4{lo.x, lo.y, 0u, 0u}), asbf8(pseg[0]), (f32x4{0, 0, 0, 0}));
      o[dt][1] = MFMA32(asbf8(u32x4{lo.x, lo.y, hi.x, hi.y}), asbf8(pseg[1]), (f32x4{0, 0, 0, 0}));
    }
    float n0[2]; f32x4 sm[2];
#pragma unroll
    for (int b = 0; b < 2; ++b) { const float t = red16_32_add(sum[b]); sm[b] = f32x4{t, t, t, t}; n0[b] = -mx[b] * LS; }
    unsigned oa = (unsigned)(r16 * 128 + ((g ^ (r16 & 7)) << 4)), ka = (unsigned)(g * 256 + (r16 & 7) * 16), va = (unsigned)lane * 16;
    unsigned one = g < 2 ? 0x3F803F80u : 0u;
    u32x4 kmv[4], vmv[4];
#pragma unroll
    for (int i = 0; i < 4; ++i) vmv[i] = u32x4{0u, 0u, 0u, 0u};
#pragma unroll
    for (int dt = 0; dt < 8; ++dt) asm volatile("" : "+v"(o[dt][0]), "+v"(o[dt][1]), "+v"(vmv[dt >> 1]));
    asm volatile("s_waitcnt lgkmcnt(0)" : "+v"(sm[0]), "+v"(sm[1]), "+v"(n0[0]), "+v"(n0[1]), "+v"(oa), "+v"(ka), "+v"(va), "+v"(one) :: "memory");
    unsigned pg;
    for (int spins = 0;; ++spins) {
      pg = ld4_sc1(cnt + (PROG + h) * 32 + ((lane & 4) ? 5 : (lane & 3)));
      if (__all((pg >> 24) + (s >= SDIR ? 1u : 0u) > (unsigned)s)) break;
      __builtin_amdgcn_s_sleep(SASL);
      if (give_up(spins, tmo, 6)) break;
    }
    if (__all((pg >> 24) == (unsigned)s)) wait_rt(latest(pg), s == NSEG - 1 ? SADL - LAST : SADL);
    const char* mk = uni(kvm + ((size_t)s * NH + h) * 4096);
    for (int spins = 0;; ++spins) {
      asm volatile("global_load_dwordx4 %0, %8, %10 sc1\n\tglobal_load_dwordx4 %1, %8, %10 offset:128 sc1\n\t"
                   "global_load_dwordx4 %2, %8, %10 offset:1024 sc1\n\tglobal_load_dwordx4 %3, %8, %10 offset:1152 sc1\n\ts_mov_b32 exec_hi, 0\n\t"
                   "global_load_dwordx4 %4, %9, %10 offset:2048 sc1\n\tglobal_load_dwordx4 %5, %9, %10 offset:2560 sc1\n\t"
                   "global_load_dwordx4 %6, %9, %10 offset:3072 sc1\n\tglobal_load_dwordx4 %7, %9, %10 offset:3584 sc1\n\ts_mov_b32 exec_hi, -1\n\ts_waitcnt vmcnt(0)"
                   : "=&v"(kmv[0]), "=&v"(kmv[1]), "=&v"(kmv[2]), "=&v"(kmv[3]), "+v"(vmv[0]), "+v"(vmv[1]), "+v"(vmv[2]), "+v"(vmv[3])
                   : "v"(ka), "v"(va), "s"(mk) : "memory");
      unsigned mx_ = 0;
#pragma unroll
      for (int i = 0; i < 4; ++i) mx_ = max(max(max(max(mx_, kmv[i].x), kmv[i].y), max(kmv[i].z, kmv[i].w)), max(max(vmv[i].x, vmv[i].y), max(vmv[i].z, vmv[i].w)));
      if (__all(mx_ != SENT)) break;
      __builtin_amdgcn_s_sleep(2);
      if (give_up(spins, tmo, 6)) break;
    }
    if (SAE && s >= SB && lane == 0) st4_sc1(cnt + (PSA + (hbase >> 1)) * 32 + 16 + wave, bell(s, rt_now() + SAE));
    WTM(slot, s, 1);
    f32x4 tt[2] = {f32x4{0, 0, 0, 0}, f32x4{0, 0, 0, 0}};
#pragma unroll
    for (int ks = 0; ks < 4; ++ks) { tt[0] = MFMA32(asbf8(kmv[ks]), asbf8(qv[0][ks]), tt[0]); tt[1] = MFMA32(asbf8(kmv[ks]), asbf8(qv[1][ks]), tt[1]); }
#pragma unroll
    for (int b = 0; b < 2; ++b)
#pragma unroll
      for (int r = 0; r < 4; ++r) tt[b][r] = tt[b][r] * LS + n0[b];
    if (!__all(max3(max3(tt[0][0], tt[0][1], tt[0][2]), max3(tt[0][3], tt[1][0], tt[1][1]), max2(tt[1][2], tt[1][3])) <= 40.0f)) {
#pragma unroll
      for (int b = 0; b < 2; ++b) {
        float m = max3(max3(tt[b][0], tt[b][1], tt[b][2]), tt[b][3], 0.0f);
        { float x, y; sw16(m, x, y); m = max2(x, y); }
        const float f = __builtin_amdgcn_exp2f(-m);
        tt[b] -= m; sm[b] *= f;
#pragma unroll
        for (int dt = 0; dt < 8; ++dt) o[dt][b] *= f;
      }
    }
    u32x2 pm[2]; float inv[2];
#pragma unroll
    for (int b = 0; b < 2; ++b) {
      pm[b] = u32x2{pack2(__builtin_amdgcn_exp2f(tt[b][0]), __builtin_amdgcn_exp2f(tt[b][1])), pack2(__builtin_amdgcn_exp2f(tt[b][2]), __builtin_amdgcn_exp2f(tt[b][3]))};
      sm[b] = __builtin_amdgcn_mfma_f32_16x16x16bf16_1k(ass4(u32x2{one, one}), ass4(pm[b]), sm[b], 0, 0, 0);
    }
#pragma unroll
    for (int dt = 0; dt < 8; ++dt)
#pragma unroll
      for (int b = 0; b < 2; ++b) o[dt][b] = __builtin_amdgcn_mfma_f32_16x16x16bf16_1k(ass4((dt & 1) ? u32x2{vmv[dt >> 1].z, vmv[dt >> 1].w} : u32x2{vmv[dt >> 1].x, vmv[dt >> 1].y}), ass4(pm[b]), o[dt][b], 0, 0, 0);
    inv[0] = __builtin_amdgcn_rcpf(sm[0][0]); inv[1] = __builtin_amdgcn_rcpf(sm[1][0]);
    const char* od = uni(O + ((size_t)s * 64 + 2 * h) * 4096);
#pragma unroll
    for (int j = 0; j < 4; ++j) {
#pragma unroll
      for (int b = 0; b < 2; ++b) {
        const f32x4 r0 = o[2 * j][b] * inv[b], r1 = o[2 * j + 1][b] * inv[b];
        st16s(od + (j >> 1) * 4096 + b * 2048, oa ^ ((j & 1) * 64u), u32x4{pack2(r0[0], r0[1]), pack2(r0[2], r0[3]), pack2(r1[0], r1[1]), pack2(r1[2], r1[3])});
      }
    }
    if (!SAE && s >= SB && lane == 0) st4_sc1(cnt + (PSA + (hbase >> 1)) * 32 + 16 + wave, bell(s, rt_now() + DSA));
    WTM(slot, s, 3);
    WAIT_V(0);
    if (lane == 0) st4_sc1(cnt + (PSA + (hbase >> 1)) * 32 + wave, (unsigned)it);
    WTM(slot, s, 2);
  }
}
#undef LD8
#undef TIE8

constexpr int Q_MEM = 0;
constexpr int Q_K   = 16384;
constexpr int Q_V   = 24576;
constexpr int Q_QK  = 32768;
constexpr int Q_TM  = 36864;
constexpr int Q_CB  = 40960;
constexpr int Q_RING = 32768;
constexpr int V_P1  = 0;
constexpr int V_P2  = 16384;
constexpr int NCHAIN = 96;
#define VHN 2
#define ROTH 0
#define NQQ 6
#define NQK 4
#define E_AG 9
#define RDYI -2
#define ROTW 2
#define DQU 6
#define DQM 18
#define DQD 1
#define ARSEL (xg >> 1)
#define ORSEL (xg >> 1)
#define D_Q 105
#define D_O 4
#define E_B 6
#define E_O 1900
#define KDEL 5
#define VPOS 0

typedef __bf16 bf16x16 __attribute__((ext_vector_type(16)));
typedef unsigned u32x8 __attribute__((ext_vector_type(8)));
__device__ __forceinline__ f32x4 SMF(u32x4 a, u32x8 b, f32x4 c, int idx) {
  return __builtin_amdgcn_smfmac_f32_16x16x64_bf16(__builtin_bit_cast(bf16x8, a), __builtin_bit_cast(bf16x16, b), c, idx, 0, 0);
}
__device__ __forceinline__ float fold32(float a, float b) {
  const auto p = __builtin_amdgcn_permlane32_swap(__float_as_uint(a), __float_as_uint(b), false, false);
  return __uint_as_float(p[0]) + __uint_as_float(p[1]);
}
__device__ __forceinline__ f32x2 rowsum(f32x4 x) { return f32x2{fold32(x[0], x[2]), fold32(x[1], x[3])}; }
__device__ __forceinline__ unsigned nosent1(unsigned v) { return v < 0xFFFFFFFEu ? v : 0xFFFFFFFEu; }
__device__ __forceinline__ void st4s(const char* sb, unsigned vo, unsigned v) { asm volatile("global_store_dword %0, %1, %2 sc1\n\ts_nop 0" :: "v"(vo), "v"(v), "s"(sb) : "memory"); }
__device__ __forceinline__ void st8s(const char* sb, unsigned vo, u32x2 v) { asm volatile("global_store_dwordx2 %0, %1, %2 sc1\n\ts_nop 1" :: "v"(vo), "v"(v), "s"(sb) : "memory"); }
__device__ __forceinline__ void gldsw(const char* sbase, unsigned voff, unsigned lds) {
  unsigned keep;
  asm volatile("s_mov_b32 %0, m0\n\ts_mov_b32 m0, %3\n\ts_nop 2\n\tglobal_load_lds_dwordx4 %1, %2" CNT_ "\n\ts_mov_b32 m0, %0"
               : "=&s"(keep) : "v"(voff), "s"(sbase), "s"(lds) : "memory");
}
__device__ __forceinline__ void wait_clk(unsigned t0, unsigned d) {
  while ((unsigned)__builtin_amdgcn_s_memtime() - t0 < d) __builtin_amdgcn_s_sleep(1);
}
__device__ __forceinline__ unsigned bell_time(const char* a, unsigned vo, unsigned tag, unsigned cap) {
  unsigned v;
  for (unsigned spins = 0;; ++spins) {
    asm volatile("global_load_dword %0, %1, %2 sc1\n\ts_waitcnt vmcnt(0)" : "=&v"(v) : "v"(vo), "s"(a) : "memory");
    if (__all((v >> 24) >= tag) || spins >= cap) break;
  }
  const unsigned now = (unsigned)__builtin_amdgcn_s_memrealtime() + 250u;
  const unsigned age = rowmin16((now - v) & 0xFFFFFFu);
  return now - min(min(rdl(age, 0), rdl(age, 16)), 500u);
}
#define WBAR() do { __builtin_amdgcn_s_barrier(); asm volatile("" ::: "memory"); } while (0)
#define TN(k, v) do { } while (0)
__device__ __forceinline__ void stage_proj(const char* src, unsigned lds, int tid, int wave) {
  const int row = tid >> 4, f = 2 * ((row & 1) | (((row >> 2) & 1) << 1));
#pragma unroll
  for (int j = 0; j < 4; ++j)
    gldsw(src + (size_t)j * 32 * HD * 2, (unsigned)(row * HD * 2 + (((tid & 15) ^ f ^ ((j & 1) << 3)) << 4)), lds + (wave * 64 + 512 * j) * 16);
}
__device__ __forceinline__ unsigned proj_base(int T, int lane) {
  const int g = lane >> 4, q4 = (lane & 15) >> 2, p4 = lane & 3, fs = 2 * ((q4 & 1) | ((g & 1) << 1) | ((q4 >> 1) << 2));
  return (unsigned)((4 * g + (q4 & 1) + 32 * (q4 >> 1)) * 256 + (((2 * T) ^ fs) << 4) + p4 * 8);
}
__device__ __forceinline__ u32x8 proj_frag(const char* smem, unsigned a) {
  const u32x2 t0 = trread(smem, a), t1 = trread(smem, a + 512), t2 = trread(smem, a + 4096), t3 = trread(smem, a + 4608);
  return u32x8{t0.x, t0.y, t1.x, t1.y, t2.x, t2.y, t3.x, t3.y};
}
__device__ __forceinline__ void flag_frags(unsigned fa, unsigned a, unsigned want, bf16x8 (&f)[4]) {
  u32x2 t[8];
  for (;;) {
    u32x4 fl;
    asm volatile("ds_read_b128 %0, %9\n\t"
                 "ds_read_b64_tr_b16 %1, %10\n\tds_read_b64_tr_b16 %2, %10 offset:64\n\t"
                 "ds_read_b64_tr_b16 %3, %10 offset:512\n\tds_read_b64_tr_b16 %4, %10 offset:576\n\t"
                 "ds_read_b64_tr_b16 %5, %10 offset:1024\n\tds_read_b64_tr_b16 %6, %10 offset:1088\n\t"
                 "ds_read_b64_tr_b16 %7, %10 offset:1536\n\tds_read_b64_tr_b16 %8, %10 offset:1600\n\ts_waitcnt lgkmcnt(0)"
                 : "=&v"(fl), "=&v"(t[0]), "=&v"(t[1]), "=&v"(t[2]), "=&v"(t[3]), "=&v"(t[4]), "=&v"(t[5]), "=&v"(t[6]), "=&v"(t[7])
                 : "v"(fa), "v"(a) : "memory");
    if (__builtin_amdgcn_readfirstlane(min(min(fl.x, fl.y), min(fl.z, fl.w))) >= want) break;
  }
#pragma unroll
  for (int ks = 0; ks < 4; ++ks) f[ks] = asbf8(u32x4{t[2 * ks].x, t[2 * ks].y, t[2 * ks + 1].x, t[2 * ks + 1].y});
}
__device__ __forceinline__ void wait_flags(const char* p, unsigned want) {
  for (;;) {
    const u32x4 fl = *(volatile LAS u32x4*)p;
    if (__builtin_amdgcn_readfirstlane(min(min(fl.x, fl.y), min(fl.z, fl.w))) >= want) break;
  }
}
__device__ __forceinline__ f32x4 zero4() { float z = 0.0f; asm volatile("" : "+v"(z)); return f32x4{z, z, z, z}; }
__device__ __forceinline__ unsigned lds_word(const char* p) { return __builtin_amdgcn_readfirstlane(*(volatile LAS unsigned*)p); }

#define MFMA16(a, b, c) __builtin_amdgcn_mfma_f32_16x16x16bf16_1k((a), (b), (c), 0, 0, 0)
#define UF(x) __uint_as_float(x)
__device__ __forceinline__ unsigned umax3(unsigned a, unsigned b, unsigned c) { return max(max(a, b), c); }
__device__ __forceinline__ f32x2 logit2(float a, float b) {
  return f32x2{a, b};
}
constexpr float LSC = LOG2E * SCALE;

struct QKW { u32x32 w0, w1, w2, w3, w4, w5; u32x8 x; };
template <int W_> __device__ __forceinline__ void qk_step(const QKW& W, const char* ag, const char* vs, const char* od, const char* kv, unsigned& cap, unsigned& sp,
                                                          unsigned long long& tc, unsigned long long* ts, unsigned& d0, unsigned& d1);
template <> __device__ __forceinline__ void qk_step<0>(const QKW& W, const char* ag, const char* vs, const char* od, const char* kv, unsigned& cap, unsigned& sp,
                                                        unsigned long long& tc, unsigned long long* ts, unsigned& d0, unsigned& d1) {
  unsigned st, bg;
  asm volatile("v_mbcnt_lo_u32_b32 v60, -1, 0\n\t"
    "v_mbcnt_hi_u32_b32 v60, -1, v60\n\t"
    "v_lshlrev_b32 v6, 4, v60\n\t"
    "s_mov_b32 %[sp], 0\n\t"
    "s_setprio 3\n\t"
    "global_load_dwordx4 v[40:43], v6, %[ag] sc1\n\t"
    "global_load_dwordx4 v[44:47], v6, %[ag] offset:1024 sc1\n\t"
    "s_memtime %[tc]\n\t"
    "v_add_u32 v7, 40960, v6\n\t"
    "v_add_u32 v8, 98304, v6\n\t"
    "ds_read_b128 v[24:27], v7 offset:0\n\t"
    "ds_read_b128 v[28:31], v7 offset:1024\n\t"
    "ds_read_b128 v[32:35], v7 offset:8192\n\t"
    "ds_read_b128 v[36:39], v7 offset:9216\n\t"
    "v_lshrrev_b32 v48, 4, v60\n\t"
    "v_and_b32 v49, 15, v60\n\t"
    "v_and_b32 v61, 4, v60\n\t"
    "v_mul_u32_u24 v61, 0x2aaa, v61\n\t"
    "v_lshrrev_b32 v9, 1, v60\n\t"
    "v_and_b32 v9, 2, v9\n\t"
    "v_or3_b32 v9, v9, v61, %[c44]\n\t"
    "v_add_u32 v14, 0, v6\n\t"
    "v_lshlrev_b32 v61, 2, v48\n\t"
    "v_lshl_or_b32 v10, v49, 4, v61\n\t"
    "v_lshrrev_b32 v50, 2, v49\n\t"
    "v_and_b32 v51, 3, v60\n\t"
    "v_and_b32 v61, 1, v60\n\t"
    "v_lshlrev_b32 v61, 3, v61\n\t"
    "v_lshl_or_b32 v11, v50, 4, v61\n\t"
    "v_lshl_or_b32 v11, v48, 7, v11\n\t"
    "v_xor_b32 v12, v48, v49\n\t"
    "v_lshlrev_b32 v12, 4, v12\n\t"
    "v_lshl_or_b32 v12, v49, 8, v12\n\t"
    "v_lshl_or_b32 v50, v48, 2, v50\n\t"
    "v_or_b32 v51, 0, v51\n\t"
    "v_xor_b32 v51, v51, v50\n\t"
    "v_lshlrev_b32 v13, 4, v51\n\t"
    "v_lshl_or_b32 v13, v50, 8, v13\n\t"
    "v_and_b32 v61, 3, v60\n\t"
    "v_lshlrev_b32 v61, 4, v61\n\t"
    "v_lshl_or_b32 v15, v48, 6, v61\n\t"
    "v_and_b32 v61, 8, v60\n\t"
    "v_lshl_or_b32 v15, v61, 14, v15\n\t"
    "v_and_b32 v61, 4, v60\n\t"
    "v_lshl_or_b32 v15, v61, 8, v15\n\t"
    "v_add_u32 v15, 0, v15\n\t"
    "3:\n\t"
    "v_mov_b32 v16, 0\n\t"
    "v_mov_b32 v17, 0\n\t"
    "v_mov_b32 v18, 0\n\t"
    "v_mov_b32 v19, 0\n\t"
    "v_mov_b32 v20, 0\n\t"
    "v_mov_b32 v21, 0\n\t"
    "v_mov_b32 v22, 0\n\t"
    "v_mov_b32 v23, 0\n\t"
    "s_waitcnt vmcnt(0) lgkmcnt(0)\n\t"
    "14:\n\t"
    "ds_write_b128 v6, v[40:43] offset:0\n\t"
    "ds_write_b128 v6, v[44:47] offset:1024\n\t"
    "v_smfmac_f32_16x16x64_bf16 v[16:19], v[40:43], v[64:71], v9\n\t"
    "v_max3_u32 v60, v40, v41, v42\n\t"
    "v_max3_u32 v61, v43, v44, v45\n\t"
    "v_smfmac_f32_16x16x64_bf16 v[20:23], v[40:43], v[72:79], v9\n\t"
    "v_max3_u32 v60, v60, v46, v47\n\t"
    "v_max_u32 v60, v60, v61\n\t"
    "v_cmp_eq_u32 vcc, -1, v60\n\t"
    "s_cbranch_vccnz 1f\n\t"
    "2:\n\t"
    "s_waitcnt lgkmcnt(0)\n\t"
    "s_cmp_eq_u64 %[kv], 0\n\t"
    "s_barrier\n\t"
    "s_cbranch_scc1 20f\n\t"
    "ds_read_b128 v[40:43], v6 offset:12288\n\t"
    "ds_read_b128 v[48:51], v6 offset:2048\n\t"
    "v_smfmac_f32_16x16x64_bf16 v[16:19], v[44:47], v[80:87], v9\n\t"
    "ds_read_b128 v[52:55], v6 offset:3072\n\t"
    "ds_read_b128 v[56:59], v6 offset:4096\n\t"
    "v_smfmac_f32_16x16x64_bf16 v[20:23], v[44:47], v[88:95], v9\n\t"
    "ds_read_b128 v[60:63], v6 offset:13312\n\t"
    "ds_read_b128 v[44:47], v6 offset:5120\n\t"
    "s_waitcnt lgkmcnt(5)\n\t"
    "v_smfmac_f32_16x16x64_bf16 v[16:19], v[40:43], v[24:31], v9\n\t"
    "ds_read_b128 v[24:27], v7 offset:16384\n\t"
    "ds_read_b128 v[28:31], v7 offset:17408\n\t"
    "v_smfmac_f32_16x16x64_bf16 v[20:23], v[40:43], v[32:39], v9\n\t"
    "ds_read_b128 v[32:35], v7 offset:24576\n\t"
    "ds_read_b128 v[36:39], v7 offset:25600\n\t"
    "ds_read_b128 v[40:43], v6 offset:6144\n\t"
    "s_waitcnt lgkmcnt(9)\n\t"
    "v_smfmac_f32_16x16x64_bf16 v[16:19], v[48:51], v[96:103], v9\n\t"
    "v_smfmac_f32_16x16x64_bf16 v[20:23], v[48:51], v[104:111], v9\n\t"
    "ds_read_b128 v[48:51], v6 offset:7168\n\t"
    "s_waitcnt lgkmcnt(9)\n\t"
    "v_smfmac_f32_16x16x64_bf16 v[16:19], v[52:55], v[112:119], v9\n\t"
    "v_smfmac_f32_16x16x64_bf16 v[20:23], v[52:55], v[120:127], v9\n\t"
    "ds_read_b128 v[52:55], v6 offset:14336\n\t"
    "s_waitcnt lgkmcnt(9)\n\t"
    "v_smfmac_f32_16x16x64_bf16 v[16:19], v[56:59], v[128:135], v9\n\t"
    "v_smfmac_f32_16x16x64_bf16 v[20:23], v[56:59], v[136:143], v9\n\t"
    "ds_read_b128 v[56:59], v6 offset:8192\n\t"
    "s_waitcnt lgkmcnt(6)\n\t"
    "v_smfmac_f32_16x16x64_bf16 v[16:19], v[60:63], v[24:31], v9\n\t"
    "ds_read_b128 v[24:27], v8 offset:0\n\t"
    "ds_read_b128 v[28:31], v8 offset:1024\n\t"
    "s_waitcnt lgkmcnt(6)\n\t"
    "v_smfmac_f32_16x16x64_bf16 v[20:23], v[60:63], v[32:39], v9\n\t"
    "ds_read_b128 v[32:35], v8 offset:8192\n\t"
    "ds_read_b128 v[36:39], v8 offset:9216\n\t"
    "ds_read_b128 v[60:63], v6 offset:9216\n\t"
    "v_smfmac_f32_16x16x64_bf16 v[16:19], v[44:47], v[144:151], v9\n\t"
    "v_smfmac_f32_16x16x64_bf16 v[20:23], v[44:47], v[152:159], v9\n\t"
    "ds_read_b128 v[44:47], v6 offset:10240\n\t"
    "s_waitcnt lgkmcnt(9)\n\t"
    "v_smfmac_f32_16x16x64_bf16 v[16:19], v[40:43], v[160:167], v9\n\t"
    "v_smfmac_f32_16x16x64_bf16 v[20:23], v[40:43], v[168:175], v9\n\t"
    "ds_read_b128 v[40:43], v6 offset:15360\n\t"
    "s_waitcnt lgkmcnt(9)\n\t"
    "v_smfmac_f32_16x16x64_bf16 v[16:19], v[48:51], v[176:183], v9\n\t"
    "v_smfmac_f32_16x16x64_bf16 v[20:23], v[48:51], v[184:191], v9\n\t"
    "ds_read_b128 v[48:51], v6 offset:11264\n\t"
    "s_waitcnt lgkmcnt(6)\n\t"
    "v_smfmac_f32_16x16x64_bf16 v[16:19], v[52:55], v[24:31], v9\n\t"
    "ds_read_b128 v[24:27], v8 offset:16384\n\t"
    "ds_read_b128 v[28:31], v8 offset:17408\n\t"
    "s_waitcnt lgkmcnt(6)\n\t"
    "v_smfmac_f32_16x16x64_bf16 v[20:23], v[52:55], v[32:39], v9\n\t"
    "ds_read_b128 v[32:35], v8 offset:24576\n\t"
    "ds_read_b128 v[36:39], v8 offset:25600\n\t"
    "v_smfmac_f32_16x16x64_bf16 v[16:19], v[56:59], v[192:199], v9\n\t"
    "v_smfmac_f32_16x16x64_bf16 v[20:23], v[56:59], v[200:207], v9\n\t"
    "s_waitcnt lgkmcnt(7)\n\t"
    "v_smfmac_f32_16x16x64_bf16 v[16:19], v[60:63], v[208:215], v9\n\t"
    "v_smfmac_f32_16x16x64_bf16 v[20:23], v[60:63], v[216:223], v9\n\t"
    "s_waitcnt lgkmcnt(6)\n\t"
    "v_smfmac_f32_16x16x64_bf16 v[16:19], v[44:47], v[224:231], v9\n\t"
    "v_smfmac_f32_16x16x64_bf16 v[20:23], v[44:47], v[232:239], v9\n\t"
    "s_waitcnt lgkmcnt(2)\n\t"
    "v_smfmac_f32_16x16x64_bf16 v[16:19], v[40:43], v[24:31], v9\n\t"
    "s_waitcnt lgkmcnt(0)\n\t"
    "v_smfmac_f32_16x16x64_bf16 v[20:23], v[40:43], v[32:39], v9\n\t"
    "v_smfmac_f32_16x16x64_bf16 v[16:19], v[48:51], v[240:247], v9\n\t"
    "v_smfmac_f32_16x16x64_bf16 v[20:23], v[48:51], v[248:255], v9\n\t"
    "v_xor_b32 v7, 64, v12\n\t"
    "v_xor_b32 v8, 0x80, v12\n\t"
    "v_xor_b32 v6, 0xc0, v12\n\t"
    "ds_read_b128 v[24:27], v12 offset:16384\n\t"
    "ds_read_b128 v[28:31], v12 offset:20480\n\t"
    "ds_read_b128 v[32:35], v7 offset:16384\n\t"
    "ds_read_b128 v[36:39], v7 offset:20480\n\t"
    "v_permlane16_swap_b32 v16, v18\n\t"
    "v_permlane16_swap_b32 v17, v19\n\t"
    "v_permlane16_swap_b32 v20, v22\n\t"
    "v_permlane16_swap_b32 v21, v23\n\t"
    "v_add_f32 v16, v16, v18\n\t"
    "v_add_f32 v17, v17, v19\n\t"
    "v_add_f32 v20, v20, v22\n\t"
    "v_mul_f32 v16, %[lsc], v16\n\t"
    "v_mul_f32 v17, %[lsc], v17\n\t"
    "v_cvt_pk_bf16_f32 v16, v16, v17\n\t"
    "v_add_f32 v21, v21, v23\n\t"
    "ds_write_b32 v10, v16 offset:32768\n\t"
    "v_mul_f32 v20, %[lsc], v20\n\t"
    "v_mul_f32 v21, %[lsc], v21\n\t"
    "v_cvt_pk_bf16_f32 v20, v20, v21\n\t"
    "ds_write_b32 v10, v20 offset:33024\n\t"
    "ds_read_b128 v[40:43], v8 offset:16384\n\t"
    "ds_read_b128 v[44:47], v8 offset:20480\n\t"
    "ds_read_b128 v[48:51], v6 offset:16384\n\t"
    "ds_read_b128 v[52:55], v6 offset:20480\n\t"
    "s_waitcnt lgkmcnt(4)\n\t"
    "s_barrier\n\t"
    "ds_read_b64_tr_b16 v[16:17], v11 offset:32768\n\t"
    "ds_read_b64_tr_b16 v[18:19], v11 offset:32832\n\t"
    "ds_read_b64_tr_b16 v[20:21], v11 offset:33280\n\t"
    "ds_read_b64_tr_b16 v[22:23], v11 offset:33344\n\t"
    "ds_read_b64_tr_b16 v[56:57], v11 offset:33792\n\t"
    "ds_read_b64_tr_b16 v[58:59], v11 offset:33856\n\t"
    "ds_read_b64_tr_b16 v[60:61], v11 offset:34304\n\t"
    "ds_read_b64_tr_b16 v[62:63], v11 offset:34368\n\t"
    "v_mov_b32 v6, 0\n\t"
    "v_mov_b32 v7, 0\n\t"
    "v_mov_b32 v8, 0\n\t"
    "v_mov_b32 v9, 0\n\t"
    "s_mov_b32 exec_hi, 0\n\t"
    "global_load_dwordx4 v[6:9], v14, %[vs] sc1\n\t"
    "s_mov_b32 exec_hi, -1\n\t"
    "s_waitcnt lgkmcnt(6)\n\t"
    "v_mfma_f32_16x16x32_bf16 v[24:27], v[24:27], v[16:19], 0\n\t"
    "v_mfma_f32_16x16x32_bf16 v[28:31], v[28:31], v[16:19], 0\n\t"
    "s_waitcnt lgkmcnt(4)\n\t"
    "v_mfma_f32_16x16x32_bf16 v[24:27], v[32:35], v[20:23], v[24:27]\n\t"
    "v_mfma_f32_16x16x32_bf16 v[28:31], v[36:39], v[20:23], v[28:31]\n\t"
    "ds_read_b64_tr_b16 v[32:33], v13 offset:24576\n\t"
    "ds_read_b64_tr_b16 v[34:35], v13 offset:28672\n\t"
    "ds_read_b64_tr_b16 v[36:37], v13 offset:24584\n\t"
    "ds_read_b64_tr_b16 v[38:39], v13 offset:28680\n\t"
    "s_waitcnt lgkmcnt(6)\n\t"
    "v_mfma_f32_16x16x32_bf16 v[24:27], v[40:43], v[56:59], v[24:27]\n\t"
    "v_mfma_f32_16x16x32_bf16 v[28:31], v[44:47], v[56:59], v[28:31]\n\t"
    "s_waitcnt lgkmcnt(4)\n\t"
    "v_mfma_f32_16x16x32_bf16 v[24:27], v[48:51], v[60:63], v[24:27]\n\t"
    "v_mfma_f32_16x16x32_bf16 v[28:31], v[52:55], v[60:63], v[28:31]\n\t"
    "s_setprio 1\n\t"
    "v_mov_b32 v52, %[one]\n\t"
    "v_mov_b32 v53, %[one]\n\t"
    "v_mov_b32 v54, %[one]\n\t"
    "v_mov_b32 v55, %[one]\n\t"
    "s_nop 2\n\t"
    "v_max3_f32 v10, |v24|, |v25|, |v26|\n\t"
    "v_max3_f32 v40, |v27|, |v28|, |v29|\n\t"
    "v_max3_f32 v10, v10, |v30|, |v31|\n\t"
    "v_max_f32 v10, v10, v40\n\t"
    "v_cmp_nge_f32 vcc, %[big], v10\n\t"
    "s_mov_b32 %[bg], %[big]\n\t"
    "v_mov_b32 v10, 0\n\t"
    "s_cbranch_vccnz 12f\n\t"
    "13:\n\t"
    "v_exp_f32 v24, v24\n\t"
    "v_exp_f32 v25, v25\n\t"
    "v_exp_f32 v26, v26\n\t"
    "v_exp_f32 v27, v27\n\t"
    "v_exp_f32 v28, v28\n\t"
    "v_exp_f32 v29, v29\n\t"
    "v_exp_f32 v30, v30\n\t"
    "v_exp_f32 v31, v31\n\t"
    "v_cvt_pk_bf16_f32 v40, v24, v25\n\t"
    "v_cvt_pk_bf16_f32 v41, v26, v27\n\t"
    "v_cvt_pk_bf16_f32 v42, v28, v29\n\t"
    "v_cvt_pk_bf16_f32 v43, v30, v31\n\t"
    "s_setprio 3\n\t"
    "s_waitcnt lgkmcnt(0)\n\t"
    "s_nop 0\n\t"
    "v_mfma_f32_16x16x32_bf16 v[44:47], v[32:35], v[40:43], 0\n\t"
    "v_mfma_f32_16x16x32_bf16 v[48:51], v[36:39], v[40:43], 0\n\t"
    "v_mfma_f32_16x16x32_bf16 v[52:55], v[52:55], v[40:43], 0\n\t"
    "v_mov_b32 v12, 0\n\t"
    "v_mov_b32 v13, 0\n\t"
    "s_mov_b32 exec_hi, 0\n\t"
    "v_mov_b32 v12, %[one]\n\t"
    "v_mov_b32 v13, %[one]\n\t"
    "s_mov_b32 exec_hi, -1\n\t"
    "s_barrier\n\t"
    "ds_read_b64_tr_b16 v[24:25], v11 offset:34816\n\t"
    "ds_read_b64_tr_b16 v[26:27], v11 offset:34880\n\t"
    "ds_read_b64_tr_b16 v[28:29], v11 offset:35328\n\t"
    "ds_read_b64_tr_b16 v[30:31], v11 offset:35392\n\t"
    "ds_read_b64_tr_b16 v[32:33], v11 offset:35840\n\t"
    "ds_read_b64_tr_b16 v[34:35], v11 offset:35904\n\t"
    "ds_read_b64_tr_b16 v[36:37], v11 offset:36352\n\t"
    "ds_read_b64_tr_b16 v[38:39], v11 offset:36416\n\t"
    "s_waitcnt lgkmcnt(6)\n\t"
    "v_mfma_f32_16x16x32_bf16 v[40:43], v[24:27], v[16:19], 0\n\t"
    "s_waitcnt lgkmcnt(4)\n\t"
    "v_mfma_f32_16x16x32_bf16 v[40:43], v[28:31], v[20:23], v[40:43]\n\t"
    "s_waitcnt lgkmcnt(2)\n\t"
    "v_mfma_f32_16x16x32_bf16 v[40:43], v[32:35], v[56:59], v[40:43]\n\t"
    "s_waitcnt lgkmcnt(0)\n\t"
    "v_mfma_f32_16x16x32_bf16 v[40:43], v[36:39], v[60:63], v[40:43]\n\t"
    "5:\n\t"
    "s_waitcnt vmcnt(0)\n\t"
    "v_max3_u32 v11, v6, v7, v8\n\t"
    "v_max_u32 v11, v11, v9\n\t"
    "v_cmp_eq_u32 vcc, -1, v11\n\t"
    "s_cbranch_vccnz 4f\n\t"
    "6:\n\t"
    "s_nop 2\n\t"
    "v_exp_f32 v28, v40\n\t"
    "v_max3_f32 v11, v40, v41, v42\n\t"
    "v_exp_f32 v29, v41\n\t"
    "v_max_f32 v11, v11, v43\n\t"
    "v_exp_f32 v30, v42\n\t"
    "v_cmp_nge_f32 vcc, %[bg], v11\n\t"
    "v_exp_f32 v31, v43\n\t"
    "s_cbranch_vccnz 10f\n\t"
    "11:\n\t"
    "v_cvt_pk_bf16_f32 v24, v28, v29\n\t"
    "v_cvt_pk_bf16_f32 v25, v30, v31\n\t"
    "s_nop 1\n\t"
    "v_mfma_f32_16x16x16_bf16 v[52:55], v[12:13], v[24:25], v[52:55]\n\t"
    "v_mfma_f32_16x16x16_bf16 v[44:47], v[6:7], v[24:25], v[44:47]\n\t"
    "v_mfma_f32_16x16x16_bf16 v[48:51], v[8:9], v[24:25], v[48:51]\n\t"
    "s_nop 5\n\t"
    "v_rcp_f32 v26, v52\n\t"
    "s_nop 0\n\t"
    "v_pk_mul_f32 v[44:45], v[44:45], v[26:27] op_sel_hi:[1,0]\n\t"
    "v_pk_mul_f32 v[46:47], v[46:47], v[26:27] op_sel_hi:[1,0]\n\t"
    "v_pk_mul_f32 v[48:49], v[48:49], v[26:27] op_sel_hi:[1,0]\n\t"
    "v_pk_mul_f32 v[50:51], v[50:51], v[26:27] op_sel_hi:[1,0]\n\t"
    "v_cvt_pk_bf16_f32 v36, v44, v45\n\t"
    "v_cvt_pk_bf16_f32 v37, v46, v47\n\t"
    "v_cvt_pk_bf16_f32 v38, v48, v49\n\t"
    "v_cvt_pk_bf16_f32 v39, v50, v51\n\t"
    "v_min_u32 v36, -2, v36\n\t"
    "v_min_u32 v37, -2, v37\n\t"
    "v_min_u32 v38, -2, v38\n\t"
    "v_min_u32 v39, -2, v39\n\t"
    "global_store_dwordx4 v15, v[36:39], %[od] sc1\n\t"
    "s_barrier\n\t"
    "s_mov_b32 exec_hi, 0\n\t"
    "global_store_dwordx4 v14, v[6:9], %[kv] sc1\n\t"
    "s_mov_b32 exec_hi, -1\n\t"
    "s_nop 0\n\t"
    "s_waitcnt lgkmcnt(0)\n\t"
    "s_branch 9f\n\t"
    "1:\n\t"
    "s_add_u32 %[sp], %[sp], 1\n\t"
    "s_cmp_ge_u32 %[sp], %[cap]\n\t"
    "s_cbranch_scc1 7f\n\t"
    "global_load_dwordx4 v[40:43], v6, %[ag] sc1\n\t"
    "global_load_dwordx4 v[44:47], v6, %[ag] offset:1024 sc1\n\t"
    "s_waitcnt lgkmcnt(0)\n\t"
    "s_memtime %[tc]\n\t"
    "s_nop 7\n\t"
    "s_branch 3b\n\t"
    "7:\n\t"
    "s_mov_b32 %[cap], 0\n\t"
    "s_branch 2b\n\t"
    "20:\n\t"
    "s_barrier\n\t"
    "s_barrier\n\t"
    "s_barrier\n\t"
    "s_branch 9f\n\t"
    "4:\n\t"
    "s_add_u32 %[sp], %[sp], 0x10000\n\t"
    "s_lshr_b32 %[st], %[sp], 16\n\t"
    "s_cmp_ge_u32 %[st], %[cap]\n\t"
    "s_cbranch_scc1 8f\n\t"
    "s_mov_b32 exec_hi, 0\n\t"
    "global_load_dwordx4 v[6:9], v14, %[vs] sc1\n\t"
    "s_mov_b32 exec_hi, -1\n\t"
    "s_branch 5b\n\t"
    "8:\n\t"
    "s_mov_b32 %[cap], 0\n\t"
    "s_branch 6b\n\t"
    "10:\n\t"
    "v_max_f32 v11, v11, v10\n\t"
    "v_mov_b32 v26, v11\n\t"
    "s_nop 1\n\t"
    "v_permlane16_swap_b32 v11, v26\n\t"
    "v_max_f32 v11, v11, v26\n\t"
    "v_sub_f32 v28, v40, v11\n\t"
    "v_sub_f32 v29, v41, v11\n\t"
    "v_sub_f32 v30, v42, v11\n\t"
    "v_sub_f32 v31, v43, v11\n\t"
    "v_sub_f32 v11, v10, v11\n\t"
    "v_exp_f32 v11, v11\n\t"
    "v_exp_f32 v28, v28\n\t"
    "v_exp_f32 v29, v29\n\t"
    "v_exp_f32 v30, v30\n\t"
    "v_exp_f32 v31, v31\n\t"
    "v_mul_f32 v44, v44, v11\n\t"
    "v_mul_f32 v45, v45, v11\n\t"
    "v_mul_f32 v46, v46, v11\n\t"
    "v_mul_f32 v47, v47, v11\n\t"
    "v_mul_f32 v48, v48, v11\n\t"
    "v_mul_f32 v49, v49, v11\n\t"
    "v_mul_f32 v50, v50, v11\n\t"
    "v_mul_f32 v51, v51, v11\n\t"
    "v_mul_f32 v52, v52, v11\n\t"
    "v_mul_f32 v53, v53, v11\n\t"
    "v_mul_f32 v54, v54, v11\n\t"
    "v_mul_f32 v55, v55, v11\n\t"
    "s_branch 11b\n\t"
    "12:\n\t"
    "v_max3_f32 v10, v24, v25, v26\n\t"
    "v_max3_f32 v40, v27, v28, v29\n\t"
    "v_max3_f32 v10, v10, v30, v31\n\t"
    "v_max3_f32 v10, v10, v40, %[neg]\n\t"
    "v_mov_b32 v40, v10\n\t"
    "s_nop 1\n\t"
    "v_permlane16_swap_b32 v10, v40\n\t"
    "v_max_f32 v10, v10, v40\n\t"
    "v_mov_b32 v40, v10\n\t"
    "s_nop 1\n\t"
    "v_permlane32_swap_b32 v10, v40\n\t"
    "v_max_f32 v10, v10, v40\n\t"
    "v_sub_f32 v24, v24, v10\n\t"
    "v_sub_f32 v25, v25, v10\n\t"
    "v_sub_f32 v26, v26, v10\n\t"
    "v_sub_f32 v27, v27, v10\n\t"
    "v_sub_f32 v28, v28, v10\n\t"
    "v_sub_f32 v29, v29, v10\n\t"
    "v_sub_f32 v30, v30, v10\n\t"
    "v_sub_f32 v31, v31, v10\n\t"
    "s_mov_b32 %[bg], 0x7fc00000\n\t"
    "s_branch 13b\n\t"
    "9:"
    : [tc] "=&s"(tc), [sp] "=&s"(sp), [cap] "+s"(cap), [st] "=&s"(st), [bg] "=&s"(bg)
    : [ag] "s"(ag), [c44] "s"(0x4444u), [kv] "s"(kv), [vs] "s"(vs), [od] "s"(od), [od2] "s"(od + 262144), [neg] "s"(-1.0e38f), [lsc] "s"(LSC), [one] "s"(0x3F803F80u), [big] "s"(24.0f), "{v[64:95]}"(W.w0), "{v[96:127]}"(W.w1), "{v[128:159]}"(W.w2), "{v[160:191]}"(W.w3), "{v[192:223]}"(W.w4), "{v[224:255]}"(W.w5)
    : "v6", "v7", "v8", "v9", "v10", "v11", "v12", "v13", "v14", "v15", "v16", "v17", "v18", "v19", "v20", "v21", "v22", "v23", "v24", "v25", "v26", "v27", "v28", "v29", "v30", "v31", "v32", "v33", "v34", "v35", "v36", "v37", "v38", "v39", "v40", "v41", "v42", "v43", "v44", "v45", "v46", "v47", "v48", "v49", "v50", "v51", "v52", "v53", "v54", "v55", "v56", "v57", "v58", "v59", "v60", "v61", "v62", "v63", "vcc", "scc", "memory");
}
template <> __device__ __forceinline__ void qk_step<1>(const QKW& W, const char* ag, const char* vs, const char* od, const char* kv, unsigned& cap, unsigned& sp,
                                                        unsigned long long& tc, unsigned long long* ts, unsigned& d0, unsigned& d1) {
  unsigned st, bg;
  asm volatile("v_mbcnt_lo_u32_b32 v60, -1, 0\n\t"
    "v_mbcnt_hi_u32_b32 v60, -1, v60\n\t"
    "v_lshlrev_b32 v6, 4, v60\n\t"
    "s_mov_b32 %[sp], 0\n\t"
    "s_setprio 3\n\t"
    "global_load_dwordx4 v[40:43], v6, %[ag] sc1\n\t"
    "global_load_dwordx4 v[44:47], v6, %[ag] offset:1024 sc1\n\t"
    "s_memtime %[tc]\n\t"
    "v_add_u32 v7, 43008, v6\n\t"
    "v_add_u32 v8, 100352, v6\n\t"
    "ds_read_b128 v[24:27], v7 offset:0\n\t"
    "ds_read_b128 v[28:31], v7 offset:1024\n\t"
    "ds_read_b128 v[32:35], v7 offset:8192\n\t"
    "ds_read_b128 v[36:39], v7 offset:9216\n\t"
    "v_lshrrev_b32 v48, 4, v60\n\t"
    "v_and_b32 v49, 15, v60\n\t"
    "v_and_b32 v61, 4, v60\n\t"
    "v_mul_u32_u24 v61, 0x2aaa, v61\n\t"
    "v_lshrrev_b32 v9, 1, v60\n\t"
    "v_and_b32 v9, 2, v9\n\t"
    "v_or3_b32 v9, v9, v61, %[c44]\n\t"
    "v_add_u32 v14, 512, v6\n\t"
    "v_lshlrev_b32 v61, 2, v48\n\t"
    "v_lshl_or_b32 v10, v49, 4, v61\n\t"
    "v_lshrrev_b32 v50, 2, v49\n\t"
    "v_and_b32 v51, 3, v60\n\t"
    "v_and_b32 v61, 1, v60\n\t"
    "v_lshlrev_b32 v61, 3, v61\n\t"
    "v_lshl_or_b32 v11, v50, 4, v61\n\t"
    "v_lshl_or_b32 v11, v48, 7, v11\n\t"
    "v_xor_b32 v12, v48, v49\n\t"
    "v_lshlrev_b32 v12, 4, v12\n\t"
    "v_lshl_or_b32 v12, v49, 8, v12\n\t"
    "v_lshl_or_b32 v50, v48, 2, v50\n\t"
    "v_or_b32 v51, 4, v51\n\t"
    "v_xor_b32 v51, v51, v50\n\t"
    "v_lshlrev_b32 v13, 4, v51\n\t"
    "v_lshl_or_b32 v13, v50, 8, v13\n\t"
    "v_and_b32 v61, 3, v60\n\t"
    "v_lshlrev_b32 v61, 4, v61\n\t"
    "v_lshl_or_b32 v15, v48, 6, v61\n\t"
    "v_and_b32 v61, 8, v60\n\t"
    "v_lshl_or_b32 v15, v61, 14, v15\n\t"
    "v_and_b32 v61, 4, v60\n\t"
    "v_lshl_or_b32 v15, v61, 8, v15\n\t"
    "v_add_u32 v15, 256, v15\n\t"
    "3:\n\t"
    "v_mov_b32 v16, 0\n\t"
    "v_mov_b32 v17, 0\n\t"
    "v_mov_b32 v18, 0\n\t"
    "v_mov_b32 v19, 0\n\t"
    "v_mov_b32 v20, 0\n\t"
    "v_mov_b32 v21, 0\n\t"
    "v_mov_b32 v22, 0\n\t"
    "v_mov_b32 v23, 0\n\t"
    "s_waitcnt vmcnt(0) lgkmcnt(0)\n\t"
    "14:\n\t"
    "ds_write_b128 v6, v[40:43] offset:2048\n\t"
    "ds_write_b128 v6, v[44:47] offset:3072\n\t"
    "v_smfmac_f32_16x16x64_bf16 v[16:19], v[40:43], v[96:103], v9\n\t"
    "v_max3_u32 v60, v40, v41, v42\n\t"
    "v_max3_u32 v61, v43, v44, v45\n\t"
    "v_smfmac_f32_16x16x64_bf16 v[20:23], v[40:43], v[104:111], v9\n\t"
    "v_max3_u32 v60, v60, v46, v47\n\t"
    "v_max_u32 v60, v60, v61\n\t"
    "v_cmp_eq_u32 vcc, -1, v60\n\t"
    "s_cbranch_vccnz 1f\n\t"
    "2:\n\t"
    "s_waitcnt lgkmcnt(0)\n\t"
    "s_cmp_eq_u64 %[kv], 0\n\t"
    "s_barrier\n\t"
    "s_cbranch_scc1 20f\n\t"
    "ds_read_b128 v[40:43], v6 offset:12288\n\t"
    "ds_read_b128 v[48:51], v6 offset:0\n\t"
    "v_smfmac_f32_16x16x64_bf16 v[16:19], v[44:47], v[112:119], v9\n\t"
    "ds_read_b128 v[52:55], v6 offset:1024\n\t"
    "ds_read_b128 v[56:59], v6 offset:4096\n\t"
    "v_smfmac_f32_16x16x64_bf16 v[20:23], v[44:47], v[120:127], v9\n\t"
    "ds_read_b128 v[60:63], v6 offset:13312\n\t"
    "ds_read_b128 v[44:47], v6 offset:5120\n\t"
    "s_waitcnt lgkmcnt(5)\n\t"
    "v_smfmac_f32_16x16x64_bf16 v[16:19], v[40:43], v[24:31], v9\n\t"
    "ds_read_b128 v[24:27], v7 offset:16384\n\t"
    "ds_read_b128 v[28:31], v7 offset:17408\n\t"
    "v_smfmac_f32_16x16x64_bf16 v[20:23], v[40:43], v[32:39], v9\n\t"
    "ds_read_b128 v[32:35], v7 offset:24576\n\t"
    "ds_read_b128 v[36:39], v7 offset:25600\n\t"
    "ds_read_b128 v[40:43], v6 offset:6144\n\t"
    "s_waitcnt lgkmcnt(9)\n\t"
    "v_smfmac_f32_16x16x64_bf16 v[16:19], v[48:51], v[64:71], v9\n\t"
    "v_smfmac_f32_16x16x64_bf16 v[20:23], v[48:51], v[72:79], v9\n\t"
    "ds_read_b128 v[48:51], v6 offset:7168\n\t"
    "s_waitcnt lgkmcnt(9)\n\t"
    "v_smfmac_f32_16x16x64_bf16 v[16:19], v[52:55], v[80:87], v9\n\t"
    "v_smfmac_f32_16x16x64_bf16 v[20:23], v[52:55], v[88:95], v9\n\t"
    "ds_read_b128 v[52:55], v6 offset:14336\n\t"
    "s_waitcnt lgkmcnt(9)\n\t"
    "v_smfmac_f32_16x16x64_bf16 v[16:19], v[56:59], v[128:135], v9\n\t"
    "v_smfmac_f32_16x16x64_bf16 v[20:23], v[56:59], v[136:143], v9\n\t"
    "ds_read_b128 v[56:59], v6 offset:8192\n\t"
    "s_waitcnt lgkmcnt(6)\n\t"
    "v_smfmac_f32_16x16x64_bf16 v[16:19], v[60:63], v[24:31], v9\n\t"
    "ds_read_b128 v[24:27], v8 offset:0\n\t"
    "ds_read_b128 v[28:31], v8 offset:1024\n\t"
    "s_waitcnt lgkmcnt(6)\n\t"
    "v_smfmac_f32_16x16x64_bf16 v[20:23], v[60:63], v[32:39], v9\n\t"
    "ds_read_b128 v[32:35], v8 offset:8192\n\t"
    "ds_read_b128 v[36:39], v8 offset:9216\n\t"
    "ds_read_b128 v[60:63], v6 offset:9216\n\t"
    "v_smfmac_f32_16x16x64_bf16 v[16:19], v[44:47], v[144:151], v9\n\t"
    "v_smfmac_f32_16x16x64_bf16 v[20:23], v[44:47], v[152:159], v9\n\t"
    "ds_read_b128 v[44:47], v6 offset:10240\n\t"
    "s_waitcnt lgkmcnt(9)\n\t"
    "v_smfmac_f32_16x16x64_bf16 v[16:19], v[40:43], v[160:167], v9\n\t"
    "v_smfmac_f32_16x16x64_bf16 v[20:23], v[40:43], v[168:175], v9\n\t"
    "ds_read_b128 v[40:43], v6 offset:15360\n\t"
    "s_waitcnt lgkmcnt(9)\n\t"
    "v_smfmac_f32_16x16x64_bf16 v[16:19], v[48:51], v[176:183], v9\n\t"
    "v_smfmac_f32_16x16x64_bf16 v[20:23], v[48:51], v[184:191], v9\n\t"
    "ds_read_b128 v[48:51], v6 offset:11264\n\t"
    "s_waitcnt lgkmcnt(6)\n\t"
    "v_smfmac_f32_16x16x64_bf16 v[16:19], v[52:55], v[24:31], v9\n\t"
    "ds_read_b128 v[24:27], v8 offset:16384\n\t"
    "ds_read_b128 v[28:31], v8 offset:17408\n\t"
    "s_waitcnt lgkmcnt(6)\n\t"
    "v_smfmac_f32_16x16x64_bf16 v[20:23], v[52:55], v[32:39], v9\n\t"
    "ds_read_b128 v[32:35], v8 offset:24576\n\t"
    "ds_read_b128 v[36:39], v8 offset:25600\n\t"
    "v_smfmac_f32_16x16x64_bf16 v[16:19], v[56:59], v[192:199], v9\n\t"
    "v_smfmac_f32_16x16x64_bf16 v[20:23], v[56:59], v[200:207], v9\n\t"
    "s_waitcnt lgkmcnt(7)\n\t"
    "v_smfmac_f32_16x16x64_bf16 v[16:19], v[60:63], v[208:215], v9\n\t"
    "v_smfmac_f32_16x16x64_bf16 v[20:23], v[60:63], v[216:223], v9\n\t"
    "s_waitcnt lgkmcnt(6)\n\t"
    "v_smfmac_f32_16x16x64_bf16 v[16:19], v[44:47], v[224:231], v9\n\t"
    "v_smfmac_f32_16x16x64_bf16 v[20:23], v[44:47], v[232:239], v9\n\t"
    "s_waitcnt lgkmcnt(2)\n\t"
    "v_smfmac_f32_16x16x64_bf16 v[16:19], v[40:43], v[24:31], v9\n\t"
    "s_waitcnt lgkmcnt(0)\n\t"
    "v_smfmac_f32_16x16x64_bf16 v[20:23], v[40:43], v[32:39], v9\n\t"
    "v_smfmac_f32_16x16x64_bf16 v[16:19], v[48:51], v[240:247], v9\n\t"
    "v_smfmac_f32_16x16x64_bf16 v[20:23], v[48:51], v[248:255], v9\n\t"
    "v_xor_b32 v7, 64, v12\n\t"
    "v_xor_b32 v8, 0x80, v12\n\t"
    "v_xor_b32 v6, 0xc0, v12\n\t"
    "ds_read_b128 v[24:27], v12 offset:16384\n\t"
    "ds_read_b128 v[28:31], v12 offset:20480\n\t"
    "ds_read_b128 v[32:35], v7 offset:16384\n\t"
    "ds_read_b128 v[36:39], v7 offset:20480\n\t"
    "v_permlane16_swap_b32 v16, v18\n\t"
    "v_permlane16_swap_b32 v17, v19\n\t"
    "v_permlane16_swap_b32 v20, v22\n\t"
    "v_permlane16_swap_b32 v21, v23\n\t"
    "v_add_f32 v16, v16, v18\n\t"
    "v_add_f32 v17, v17, v19\n\t"
    "v_add_f32 v20, v20, v22\n\t"
    "v_mul_f32 v16, %[lsc], v16\n\t"
    "v_mul_f32 v17, %[lsc], v17\n\t"
    "v_cvt_pk_bf16_f32 v16, v16, v17\n\t"
    "v_add_f32 v21, v21, v23\n\t"
    "ds_write_b32 v10, v16 offset:33280\n\t"
    "v_mul_f32 v20, %[lsc], v20\n\t"
    "v_mul_f32 v21, %[lsc], v21\n\t"
    "v_cvt_pk_bf16_f32 v20, v20, v21\n\t"
    "ds_write_b32 v10, v20 offset:33536\n\t"
    "ds_read_b128 v[40:43], v8 offset:16384\n\t"
    "ds_read_b128 v[44:47], v8 offset:20480\n\t"
    "ds_read_b128 v[48:51], v6 offset:16384\n\t"
    "ds_read_b128 v[52:55], v6 offset:20480\n\t"
    "s_waitcnt lgkmcnt(4)\n\t"
    "s_barrier\n\t"
    "ds_read_b64_tr_b16 v[16:17], v11 offset:32768\n\t"
    "ds_read_b64_tr_b16 v[18:19], v11 offset:32832\n\t"
    "ds_read_b64_tr_b16 v[20:21], v11 offset:33280\n\t"
    "ds_read_b64_tr_b16 v[22:23], v11 offset:33344\n\t"
    "ds_read_b64_tr_b16 v[56:57], v11 offset:33792\n\t"
    "ds_read_b64_tr_b16 v[58:59], v11 offset:33856\n\t"
    "ds_read_b64_tr_b16 v[60:61], v11 offset:34304\n\t"
    "ds_read_b64_tr_b16 v[62:63], v11 offset:34368\n\t"
    "v_mov_b32 v6, 0\n\t"
    "v_mov_b32 v7, 0\n\t"
    "v_mov_b32 v8, 0\n\t"
    "v_mov_b32 v9, 0\n\t"
    "s_mov_b32 exec_hi, 0\n\t"
    "global_load_dwordx4 v[6:9], v14, %[vs] sc1\n\t"
    "s_mov_b32 exec_hi, -1\n\t"
    "s_waitcnt lgkmcnt(6)\n\t"
    "v_mfma_f32_16x16x32_bf16 v[24:27], v[24:27], v[16:19], 0\n\t"
    "v_mfma_f32_16x16x32_bf16 v[28:31], v[28:31], v[16:19], 0\n\t"
    "s_waitcnt lgkmcnt(4)\n\t"
    "v_mfma_f32_16x16x32_bf16 v[24:27], v[32:35], v[20:23], v[24:27]\n\t"
    "v_mfma_f32_16x16x32_bf16 v[28:31], v[36:39], v[20:23], v[28:31]\n\t"
    "ds_read_b64_tr_b16 v[32:33], v13 offset:24576\n\t"
    "ds_read_b64_tr_b16 v[34:35], v13 offset:28672\n\t"
    "ds_read_b64_tr_b16 v[36:37], v13 offset:24584\n\t"
    "ds_read_b64_tr_b16 v[38:39], v13 offset:28680\n\t"
    "s_waitcnt lgkmcnt(6)\n\t"
    "v_mfma_f32_16x16x32_bf16 v[24:27], v[40:43], v[56:59], v[24:27]\n\t"
    "v_mfma_f32_16x16x32_bf16 v[28:31], v[44:47], v[56:59], v[28:31]\n\t"
    "s_waitcnt lgkmcnt(4)\n\t"
    "v_mfma_f32_16x16x32_bf16 v[24:27], v[48:51], v[60:63], v[24:27]\n\t"
    "v_mfma_f32_16x16x32_bf16 v[28:31], v[52:55], v[60:63], v[28:31]\n\t"
    "s_setprio 1\n\t"
    "v_mov_b32 v52, %[one]\n\t"
    "v_mov_b32 v53, %[one]\n\t"
    "v_mov_b32 v54, %[one]\n\t"
    "v_mov_b32 v55, %[one]\n\t"
    "s_nop 2\n\t"
    "v_max3_f32 v10, |v24|, |v25|, |v26|\n\t"
    "v_max3_f32 v40, |v27|, |v28|, |v29|\n\t"
    "v_max3_f32 v10, v10, |v30|, |v31|\n\t"
    "v_max_f32 v10, v10, v40\n\t"
    "v_cmp_nge_f32 vcc, %[big], v10\n\t"
    "s_mov_b32 %[bg], %[big]\n\t"
    "v_mov_b32 v10, 0\n\t"
    "s_cbranch_vccnz 12f\n\t"
    "13:\n\t"
    "v_exp_f32 v24, v24\n\t"
    "v_exp_f32 v25, v25\n\t"
    "v_exp_f32 v26, v26\n\t"
    "v_exp_f32 v27, v27\n\t"
    "v_exp_f32 v28, v28\n\t"
    "v_exp_f32 v29, v29\n\t"
    "v_exp_f32 v30, v30\n\t"
    "v_exp_f32 v31, v31\n\t"
    "v_cvt_pk_bf16_f32 v40, v24, v25\n\t"
    "v_cvt_pk_bf16_f32 v41, v26, v27\n\t"
    "v_cvt_pk_bf16_f32 v42, v28, v29\n\t"
    "v_cvt_pk_bf16_f32 v43, v30, v31\n\t"
    "s_setprio 3\n\t"
    "s_waitcnt lgkmcnt(0)\n\t"
    "s_nop 0\n\t"
    "v_mfma_f32_16x16x32_bf16 v[44:47], v[32:35], v[40:43], 0\n\t"
    "v_mfma_f32_16x16x32_bf16 v[48:51], v[36:39], v[40:43], 0\n\t"
    "v_mfma_f32_16x16x32_bf16 v[52:55], v[52:55], v[40:43], 0\n\t"
    "v_mov_b32 v12, 0\n\t"
    "v_mov_b32 v13, 0\n\t"
    "s_mov_b32 exec_hi, 0\n\t"
    "v_mov_b32 v12, %[one]\n\t"
    "v_mov_b32 v13, %[one]\n\t"
    "s_mov_b32 exec_hi, -1\n\t"
    "s_barrier\n\t"
    "ds_read_b64_tr_b16 v[24:25], v11 offset:34816\n\t"
    "ds_read_b64_tr_b16 v[26:27], v11 offset:34880\n\t"
    "ds_read_b64_tr_b16 v[28:29], v11 offset:35328\n\t"
    "ds_read_b64_tr_b16 v[30:31], v11 offset:35392\n\t"
    "ds_read_b64_tr_b16 v[32:33], v11 offset:35840\n\t"
    "ds_read_b64_tr_b16 v[34:35], v11 offset:35904\n\t"
    "ds_read_b64_tr_b16 v[36:37], v11 offset:36352\n\t"
    "ds_read_b64_tr_b16 v[38:39], v11 offset:36416\n\t"
    "s_waitcnt lgkmcnt(6)\n\t"
    "v_mfma_f32_16x16x32_bf16 v[40:43], v[24:27], v[16:19], 0\n\t"
    "s_waitcnt lgkmcnt(4)\n\t"
    "v_mfma_f32_16x16x32_bf16 v[40:43], v[28:31], v[20:23], v[40:43]\n\t"
    "s_waitcnt lgkmcnt(2)\n\t"
    "v_mfma_f32_16x16x32_bf16 v[40:43], v[32:35], v[56:59], v[40:43]\n\t"
    "s_waitcnt lgkmcnt(0)\n\t"
    "v_mfma_f32_16x16x32_bf16 v[40:43], v[36:39], v[60:63], v[40:43]\n\t"
    "5:\n\t"
    "s_waitcnt vmcnt(0)\n\t"
    "v_max3_u32 v11, v6, v7, v8\n\t"
    "v_max_u32 v11, v11, v9\n\t"
    "v_cmp_eq_u32 vcc, -1, v11\n\t"
    "s_cbranch_vccnz 4f\n\t"
    "6:\n\t"
    "s_nop 2\n\t"
    "v_exp_f32 v28, v40\n\t"
    "v_max3_f32 v11, v40, v41, v42\n\t"
    "v_exp_f32 v29, v41\n\t"
    "v_max_f32 v11, v11, v43\n\t"
    "v_exp_f32 v30, v42\n\t"
    "v_cmp_nge_f32 vcc, %[bg], v11\n\t"
    "v_exp_f32 v31, v43\n\t"
    "s_cbranch_vccnz 10f\n\t"
    "11:\n\t"
    "v_cvt_pk_bf16_f32 v24, v28, v29\n\t"
    "v_cvt_pk_bf16_f32 v25, v30, v31\n\t"
    "s_nop 1\n\t"
    "v_mfma_f32_16x16x16_bf16 v[52:55], v[12:13], v[24:25], v[52:55]\n\t"
    "v_mfma_f32_16x16x16_bf16 v[44:47], v[6:7], v[24:25], v[44:47]\n\t"
    "v_mfma_f32_16x16x16_bf16 v[48:51], v[8:9], v[24:25], v[48:51]\n\t"
    "s_nop 5\n\t"
    "v_rcp_f32 v26, v52\n\t"
    "s_nop 0\n\t"
    "v_pk_mul_f32 v[44:45], v[44:45], v[26:27] op_sel_hi:[1,0]\n\t"
    "v_pk_mul_f32 v[46:47], v[46:47], v[26:27] op_sel_hi:[1,0]\n\t"
    "v_pk_mul_f32 v[48:49], v[48:49], v[26:27] op_sel_hi:[1,0]\n\t"
    "v_pk_mul_f32 v[50:51], v[50:51], v[26:27] op_sel_hi:[1,0]\n\t"
    "v_cvt_pk_bf16_f32 v36, v44, v45\n\t"
    "v_cvt_pk_bf16_f32 v37, v46, v47\n\t"
    "v_cvt_pk_bf16_f32 v38, v48, v49\n\t"
    "v_cvt_pk_bf16_f32 v39, v50, v51\n\t"
    "v_min_u32 v36, -2, v36\n\t"
    "v_min_u32 v37, -2, v37\n\t"
    "v_min_u32 v38, -2, v38\n\t"
    "v_min_u32 v39, -2, v39\n\t"
    "global_store_dwordx4 v15, v[36:39], %[od] sc1\n\t"
    "s_barrier\n\t"
    "s_mov_b32 exec_hi, 0\n\t"
    "global_store_dwordx4 v14, v[6:9], %[kv] sc1\n\t"
    "s_mov_b32 exec_hi, -1\n\t"
    "s_nop 0\n\t"
    "s_waitcnt lgkmcnt(0)\n\t"
    "s_branch 9f\n\t"
    "1:\n\t"
    "s_add_u32 %[sp], %[sp], 1\n\t"
    "s_cmp_ge_u32 %[sp], %[cap]\n\t"
    "s_cbranch_scc1 7f\n\t"
    "global_load_dwordx4 v[40:43], v6, %[ag] sc1\n\t"
    "global_load_dwordx4 v[44:47], v6, %[ag] offset:1024 sc1\n\t"
    "s_waitcnt lgkmcnt(0)\n\t"
    "s_memtime %[tc]\n\t"
    "s_nop 7\n\t"
    "s_branch 3b\n\t"
    "7:\n\t"
    "s_mov_b32 %[cap], 0\n\t"
    "s_branch 2b\n\t"
    "20:\n\t"
    "s_barrier\n\t"
    "s_barrier\n\t"
    "s_barrier\n\t"
    "s_branch 9f\n\t"
    "4:\n\t"
    "s_add_u32 %[sp], %[sp], 0x10000\n\t"
    "s_lshr_b32 %[st], %[sp], 16\n\t"
    "s_cmp_ge_u32 %[st], %[cap]\n\t"
    "s_cbranch_scc1 8f\n\t"
    "s_mov_b32 exec_hi, 0\n\t"
    "global_load_dwordx4 v[6:9], v14, %[vs] sc1\n\t"
    "s_mov_b32 exec_hi, -1\n\t"
    "s_branch 5b\n\t"
    "8:\n\t"
    "s_mov_b32 %[cap], 0\n\t"
    "s_branch 6b\n\t"
    "10:\n\t"
    "v_max_f32 v11, v11, v10\n\t"
    "v_mov_b32 v26, v11\n\t"
    "s_nop 1\n\t"
    "v_permlane16_swap_b32 v11, v26\n\t"
    "v_max_f32 v11, v11, v26\n\t"
    "v_sub_f32 v28, v40, v11\n\t"
    "v_sub_f32 v29, v41, v11\n\t"
    "v_sub_f32 v30, v42, v11\n\t"
    "v_sub_f32 v31, v43, v11\n\t"
    "v_sub_f32 v11, v10, v11\n\t"
    "v_exp_f32 v11, v11\n\t"
    "v_exp_f32 v28, v28\n\t"
    "v_exp_f32 v29, v29\n\t"
    "v_exp_f32 v30, v30\n\t"
    "v_exp_f32 v31, v31\n\t"
    "v_mul_f32 v44, v44, v11\n\t"
    "v_mul_f32 v45, v45, v11\n\t"
    "v_mul_f32 v46, v46, v11\n\t"
    "v_mul_f32 v47, v47, v11\n\t"
    "v_mul_f32 v48, v48, v11\n\t"
    "v_mul_f32 v49, v49, v11\n\t"
    "v_mul_f32 v50, v50, v11\n\t"
    "v_mul_f32 v51, v51, v11\n\t"
    "v_mul_f32 v52, v52, v11\n\t"
    "v_mul_f32 v53, v53, v11\n\t"
    "v_mul_f32 v54, v54, v11\n\t"
    "v_mul_f32 v55, v55, v11\n\t"
    "s_branch 11b\n\t"
    "12:\n\t"
    "v_max3_f32 v10, v24, v25, v26\n\t"
    "v_max3_f32 v40, v27, v28, v29\n\t"
    "v_max3_f32 v10, v10, v30, v31\n\t"
    "v_max3_f32 v10, v10, v40, %[neg]\n\t"
    "v_mov_b32 v40, v10\n\t"
    "s_nop 1\n\t"
    "v_permlane16_swap_b32 v10, v40\n\t"
    "v_max_f32 v10, v10, v40\n\t"
    "v_mov_b32 v40, v10\n\t"
    "s_nop 1\n\t"
    "v_permlane32_swap_b32 v10, v40\n\t"
    "v_max_f32 v10, v10, v40\n\t"
    "v_sub_f32 v24, v24, v10\n\t"
    "v_sub_f32 v25, v25, v10\n\t"
    "v_sub_f32 v26, v26, v10\n\t"
    "v_sub_f32 v27, v27, v10\n\t"
    "v_sub_f32 v28, v28, v10\n\t"
    "v_sub_f32 v29, v29, v10\n\t"
    "v_sub_f32 v30, v30, v10\n\t"
    "v_sub_f32 v31, v31, v10\n\t"
    "s_mov_b32 %[bg], 0x7fc00000\n\t"
    "s_branch 13b\n\t"
    "9:"
    : [tc] "=&s"(tc), [sp] "=&s"(sp), [cap] "+s"(cap), [st] "=&s"(st), [bg] "=&s"(bg)
    : [ag] "s"(ag), [c44] "s"(0x4444u), [kv] "s"(kv), [vs] "s"(vs), [od] "s"(od), [od2] "s"(od + 262144), [neg] "s"(-1.0e38f), [lsc] "s"(LSC), [one] "s"(0x3F803F80u), [big] "s"(24.0f), "{v[64:95]}"(W.w0), "{v[96:127]}"(W.w1), "{v[128:159]}"(W.w2), "{v[160:191]}"(W.w3), "{v[192:223]}"(W.w4), "{v[224:255]}"(W.w5)
    : "v6", "v7", "v8", "v9", "v10", "v11", "v12", "v13", "v14", "v15", "v16", "v17", "v18", "v19", "v20", "v21", "v22", "v23", "v24", "v25", "v26", "v27", "v28", "v29", "v30", "v31", "v32", "v33", "v34", "v35", "v36", "v37", "v38", "v39", "v40", "v41", "v42", "v43", "v44", "v45", "v46", "v47", "v48", "v49", "v50", "v51", "v52", "v53", "v54", "v55", "v56", "v57", "v58", "v59", "v60", "v61", "v62", "v63", "vcc", "scc", "memory");
}
template <> __device__ __forceinline__ void qk_step<2>(const QKW& W, const char* ag, const char* vs, const char* od, const char* kv, unsigned& cap, unsigned& sp,
                                                        unsigned long long& tc, unsigned long long* ts, unsigned& d0, unsigned& d1) {
  unsigned st, bg;
  asm volatile("v_mbcnt_lo_u32_b32 v60, -1, 0\n\t"
    "v_mbcnt_hi_u32_b32 v60, -1, v60\n\t"
    "v_lshlrev_b32 v6, 4, v60\n\t"
    "s_mov_b32 %[sp], 0\n\t"
    "s_setprio 3\n\t"
    "global_load_dwordx4 v[40:43], v6, %[ag] sc1\n\t"
    "global_load_dwordx4 v[44:47], v6, %[ag] offset:1024 sc1\n\t"
    "s_memtime %[tc]\n\t"
    "v_add_u32 v7, 45056, v6\n\t"
    "v_add_u32 v8, 102400, v6\n\t"
    "ds_read_b128 v[24:27], v7 offset:0\n\t"
    "ds_read_b128 v[28:31], v7 offset:1024\n\t"
    "ds_read_b128 v[32:35], v7 offset:8192\n\t"
    "ds_read_b128 v[36:39], v7 offset:9216\n\t"
    "v_lshrrev_b32 v48, 4, v60\n\t"
    "v_and_b32 v49, 15, v60\n\t"
    "v_and_b32 v61, 4, v60\n\t"
    "v_mul_u32_u24 v61, 0x2aaa, v61\n\t"
    "v_lshrrev_b32 v9, 1, v60\n\t"
    "v_and_b32 v9, 2, v9\n\t"
    "v_or3_b32 v9, v9, v61, %[c44]\n\t"
    "v_add_u32 v14, 1024, v6\n\t"
    "v_lshlrev_b32 v61, 2, v48\n\t"
    "v_lshl_or_b32 v10, v49, 4, v61\n\t"
    "v_lshrrev_b32 v50, 2, v49\n\t"
    "v_and_b32 v51, 3, v60\n\t"
    "v_and_b32 v61, 1, v60\n\t"
    "v_lshlrev_b32 v61, 3, v61\n\t"
    "v_lshl_or_b32 v11, v50, 4, v61\n\t"
    "v_lshl_or_b32 v11, v48, 7, v11\n\t"
    "v_xor_b32 v12, v48, v49\n\t"
    "v_lshlrev_b32 v12, 4, v12\n\t"
    "v_lshl_or_b32 v12, v49, 8, v12\n\t"
    "v_lshl_or_b32 v50, v48, 2, v50\n\t"
    "v_or_b32 v51, 8, v51\n\t"
    "v_xor_b32 v51, v51, v50\n\t"
    "v_lshlrev_b32 v13, 4, v51\n\t"
    "v_lshl_or_b32 v13, v50, 8, v13\n\t"
    "v_and_b32 v61, 3, v60\n\t"
    "v_lshlrev_b32 v61, 4, v61\n\t"
    "v_lshl_or_b32 v15, v48, 6, v61\n\t"
    "v_and_b32 v61, 8, v60\n\t"
    "v_lshl_or_b32 v15, v61, 14, v15\n\t"
    "v_and_b32 v61, 4, v60\n\t"
    "v_lshl_or_b32 v15, v61, 8, v15\n\t"
    "v_add_u32 v15, 512, v15\n\t"
    "3:\n\t"
    "v_mov_b32 v16, 0\n\t"
    "v_mov_b32 v17, 0\n\t"
    "v_mov_b32 v18, 0\n\t"
    "v_mov_b32 v19, 0\n\t"
    "v_mov_b32 v20, 0\n\t"
    "v_mov_b32 v21, 0\n\t"
    "v_mov_b32 v22, 0\n\t"
    "v_mov_b32 v23, 0\n\t"
    "s_waitcnt vmcnt(0) lgkmcnt(0)\n\t"
    "14:\n\t"
    "ds_write_b128 v6, v[40:43] offset:4096\n\t"
    "ds_write_b128 v6, v[44:47] offset:5120\n\t"
    "v_smfmac_f32_16x16x64_bf16 v[16:19], v[40:43], v[128:135], v9\n\t"
    "v_max3_u32 v60, v40, v41, v42\n\t"
    "v_max3_u32 v61, v43, v44, v45\n\t"
    "v_smfmac_f32_16x16x64_bf16 v[20:23], v[40:43], v[136:143], v9\n\t"
    "v_max3_u32 v60, v60, v46, v47\n\t"
    "v_max_u32 v60, v60, v61\n\t"
    "v_cmp_eq_u32 vcc, -1, v60\n\t"
    "s_cbranch_vccnz 1f\n\t"
    "2:\n\t"
    "s_waitcnt lgkmcnt(0)\n\t"
    "s_cmp_eq_u64 %[kv], 0\n\t"
    "s_barrier\n\t"
    "s_cbranch_scc1 20f\n\t"
    "ds_read_b128 v[40:43], v6 offset:12288\n\t"
    "ds_read_b128 v[48:51], v6 offset:0\n\t"
    "v_smfmac_f32_16x16x64_bf16 v[16:19], v[44:47], v[144:151], v9\n\t"
    "ds_read_b128 v[52:55], v6 offset:1024\n\t"
    "ds_read_b128 v[56:59], v6 offset:2048\n\t"
    "v_smfmac_f32_16x16x64_bf16 v[20:23], v[44:47], v[152:159], v9\n\t"
    "ds_read_b128 v[60:63], v6 offset:13312\n\t"
    "ds_read_b128 v[44:47], v6 offset:3072\n\t"
    "s_waitcnt lgkmcnt(5)\n\t"
    "v_smfmac_f32_16x16x64_bf16 v[16:19], v[40:43], v[24:31], v9\n\t"
    "ds_read_b128 v[24:27], v7 offset:16384\n\t"
    "ds_read_b128 v[28:31], v7 offset:17408\n\t"
    "v_smfmac_f32_16x16x64_bf16 v[20:23], v[40:43], v[32:39], v9\n\t"
    "ds_read_b128 v[32:35], v7 offset:24576\n\t"
    "ds_read_b128 v[36:39], v7 offset:25600\n\t"
    "ds_read_b128 v[40:43], v6 offset:6144\n\t"
    "s_waitcnt lgkmcnt(9)\n\t"
    "v_smfmac_f32_16x16x64_bf16 v[16:19], v[48:51], v[64:71], v9\n\t"
    "v_smfmac_f32_16x16x64_bf16 v[20:23], v[48:51], v[72:79], v9\n\t"
    "ds_read_b128 v[48:51], v6 offset:7168\n\t"
    "s_waitcnt lgkmcnt(9)\n\t"
    "v_smfmac_f32_16x16x64_bf16 v[16:19], v[52:55], v[80:87], v9\n\t"
    "v_smfmac_f32_16x16x64_bf16 v[20:23], v[52:55], v[88:95], v9\n\t"
    "ds_read_b128 v[52:55], v6 offset:14336\n\t"
    "s_waitcnt lgkmcnt(9)\n\t"
    "v_smfmac_f32_16x16x64_bf16 v[16:19], v[56:59], v[96:103], v9\n\t"
    "v_smfmac_f32_16x16x64_bf16 v[20:23], v[56:59], v[104:111], v9\n\t"
    "ds_read_b128 v[56:59], v6 offset:8192\n\t"
    "s_waitcnt lgkmcnt(6)\n\t"
    "v_smfmac_f32_16x16x64_bf16 v[16:19], v[60:63], v[24:31], v9\n\t"
    "ds_read_b128 v[24:27], v8 offset:0\n\t"
    "ds_read_b128 v[28:31], v8 offset:1024\n\t"
    "s_waitcnt lgkmcnt(6)\n\t"
    "v_smfmac_f32_16x16x64_bf16 v[20:23], v[60:63], v[32:39], v9\n\t"
    "ds_read_b128 v[32:35], v8 offset:8192\n\t"
    "ds_read_b128 v[36:39], v8 offset:9216\n\t"
    "ds_read_b128 v[60:63], v6 offset:9216\n\t"
    "v_smfmac_f32_16x16x64_bf16 v[16:19], v[44:47], v[112:119], v9\n\t"
    "v_smfmac_f32_16x16x64_bf16 v[20:23], v[44:47], v[120:127], v9\n\t"
    "ds_read_b128 v[44:47], v6 offset:10240\n\t"
    "s_waitcnt lgkmcnt(9)\n\t"
    "v_smfmac_f32_16x16x64_bf16 v[16:19], v[40:43], v[160:167], v9\n\t"
    "v_smfmac_f32_16x16x64_bf16 v[20:23], v[40:43], v[168:175], v9\n\t"
    "ds_read_b128 v[40:43], v6 offset:15360\n\t"
    "s_waitcnt lgkmcnt(9)\n\t"
    "v_smfmac_f32_16x16x64_bf16 v[16:19], v[48:51], v[176:183], v9\n\t"
    "v_smfmac_f32_16x16x64_bf16 v[20:23], v[48:51], v[184:191], v9\n\t"
    "ds_read_b128 v[48:51], v6 offset:11264\n\t"
    "s_waitcnt lgkmcnt(6)\n\t"
    "v_smfmac_f32_16x16x64_bf16 v[16:19], v[52:55], v[24:31], v9\n\t"
    "ds_read_b128 v[24:27], v8 offset:16384\n\t"
    "ds_read_b128 v[28:31], v8 offset:17408\n\t"
    "s_waitcnt lgkmcnt(6)\n\t"
    "v_smfmac_f32_16x16x64_bf16 v[20:23], v[52:55], v[32:39], v9\n\t"
    "ds_read_b128 v[32:35], v8 offset:24576\n\t"
    "ds_read_b128 v[36:39], v8 offset:25600\n\t"
    "v_smfmac_f32_16x16x64_bf16 v[16:19], v[56:59], v[192:199], v9\n\t"
    "v_smfmac_f32_16x16x64_bf16 v[20:23], v[56:59], v[200:207], v9\n\t"
    "s_waitcnt lgkmcnt(7)\n\t"
    "v_smfmac_f32_16x16x64_bf16 v[16:19], v[60:63], v[208:215], v9\n\t"
    "v_smfmac_f32_16x16x64_bf16 v[20:23], v[60:63], v[216:223], v9\n\t"
    "s_waitcnt lgkmcnt(6)\n\t"
    "v_smfmac_f32_16x16x64_bf16 v[16:19], v[44:47], v[224:231], v9\n\t"
    "v_smfmac_f32_16x16x64_bf16 v[20:23], v[44:47], v[232:239], v9\n\t"
    "s_waitcnt lgkmcnt(2)\n\t"
    "v_smfmac_f32_16x16x64_bf16 v[16:19], v[40:43], v[24:31], v9\n\t"
    "s_waitcnt lgkmcnt(0)\n\t"
    "v_smfmac_f32_16x16x64_bf16 v[20:23], v[40:43], v[32:39], v9\n\t"
    "v_smfmac_f32_16x16x64_bf16 v[16:19], v[48:51], v[240:247], v9\n\t"
    "v_smfmac_f32_16x16x64_bf16 v[20:23], v[48:51], v[248:255], v9\n\t"
    "v_xor_b32 v7, 64, v12\n\t"
    "v_xor_b32 v8, 0x80, v12\n\t"
    "v_xor_b32 v6, 0xc0, v12\n\t"
    "ds_read_b128 v[24:27], v12 offset:16384\n\t"
    "ds_read_b128 v[28:31], v12 offset:20480\n\t"
    "ds_read_b128 v[32:35], v7 offset:16384\n\t"
    "ds_read_b128 v[36:39], v7 offset:20480\n\t"
    "v_permlane16_swap_b32 v16, v18\n\t"
    "v_permlane16_swap_b32 v17, v19\n\t"
    "v_permlane16_swap_b32 v20, v22\n\t"
    "v_permlane16_swap_b32 v21, v23\n\t"
    "v_add_f32 v16, v16, v18\n\t"
    "v_add_f32 v17, v17, v19\n\t"
    "v_add_f32 v20, v20, v22\n\t"
    "v_mul_f32 v16, %[lsc], v16\n\t"
    "v_mul_f32 v17, %[lsc], v17\n\t"
    "v_cvt_pk_bf16_f32 v16, v16, v17\n\t"
    "v_add_f32 v21, v21, v23\n\t"
    "ds_write_b32 v10, v16 offset:33792\n\t"
    "v_mul_f32 v20, %[lsc], v20\n\t"
    "v_mul_f32 v21, %[lsc], v21\n\t"
    "v_cvt_pk_bf16_f32 v20, v20, v21\n\t"
    "ds_write_b32 v10, v20 offset:34048\n\t"
    "ds_read_b128 v[40:43], v8 offset:16384\n\t"
    "ds_read_b128 v[44:47], v8 offset:20480\n\t"
    "ds_read_b128 v[48:51], v6 offset:16384\n\t"
    "ds_read_b128 v[52:55], v6 offset:20480\n\t"
    "s_waitcnt lgkmcnt(4)\n\t"
    "s_barrier\n\t"
    "ds_read_b64_tr_b16 v[16:17], v11 offset:32768\n\t"
    "ds_read_b64_tr_b16 v[18:19], v11 offset:32832\n\t"
    "ds_read_b64_tr_b16 v[20:21], v11 offset:33280\n\t"
    "ds_read_b64_tr_b16 v[22:23], v11 offset:33344\n\t"
    "ds_read_b64_tr_b16 v[56:57], v11 offset:33792\n\t"
    "ds_read_b64_tr_b16 v[58:59], v11 offset:33856\n\t"
    "ds_read_b64_tr_b16 v[60:61], v11 offset:34304\n\t"
    "ds_read_b64_tr_b16 v[62:63], v11 offset:34368\n\t"
    "v_mov_b32 v6, 0\n\t"
    "v_mov_b32 v7, 0\n\t"
    "v_mov_b32 v8, 0\n\t"
    "v_mov_b32 v9, 0\n\t"
    "s_mov_b32 exec_hi, 0\n\t"
    "global_load_dwordx4 v[6:9], v14, %[vs] sc1\n\t"
    "s_mov_b32 exec_hi, -1\n\t"
    "s_waitcnt lgkmcnt(6)\n\t"
    "v_mfma_f32_16x16x32_bf16 v[24:27], v[24:27], v[16:19], 0\n\t"
    "v_mfma_f32_16x16x32_bf16 v[28:31], v[28:31], v[16:19], 0\n\t"
    "s_waitcnt lgkmcnt(4)\n\t"
    "v_mfma_f32_16x16x32_bf16 v[24:27], v[32:35], v[20:23], v[24:27]\n\t"
    "v_mfma_f32_16x16x32_bf16 v[28:31], v[36:39], v[20:23], v[28:31]\n\t"
    "ds_read_b64_tr_b16 v[32:33], v13 offset:24576\n\t"
    "ds_read_b64_tr_b16 v[34:35], v13 offset:28672\n\t"
    "ds_read_b64_tr_b16 v[36:37], v13 offset:24584\n\t"
    "ds_read_b64_tr_b16 v[38:39], v13 offset:28680\n\t"
    "s_waitcnt lgkmcnt(6)\n\t"
    "v_mfma_f32_16x16x32_bf16 v[24:27], v[40:43], v[56:59], v[24:27]\n\t"
    "v_mfma_f32_16x16x32_bf16 v[28:31], v[44:47], v[56:59], v[28:31]\n\t"
    "s_waitcnt lgkmcnt(4)\n\t"
    "v_mfma_f32_16x16x32_bf16 v[24:27], v[48:51], v[60:63], v[24:27]\n\t"
    "v_mfma_f32_16x16x32_bf16 v[28:31], v[52:55], v[60:63], v[28:31]\n\t"
    "s_setprio 1\n\t"
    "v_mov_b32 v52, %[one]\n\t"
    "v_mov_b32 v53, %[one]\n\t"
    "v_mov_b32 v54, %[one]\n\t"
    "v_mov_b32 v55, %[one]\n\t"
    "s_nop 2\n\t"
    "v_max3_f32 v10, |v24|, |v25|, |v26|\n\t"
    "v_max3_f32 v40, |v27|, |v28|, |v29|\n\t"
    "v_max3_f32 v10, v10, |v30|, |v31|\n\t"
    "v_max_f32 v10, v10, v40\n\t"
    "v_cmp_nge_f32 vcc, %[big], v10\n\t"
    "s_mov_b32 %[bg], %[big]\n\t"
    "v_mov_b32 v10, 0\n\t"
    "s_cbranch_vccnz 12f\n\t"
    "13:\n\t"
    "v_exp_f32 v24, v24\n\t"
    "v_exp_f32 v25, v25\n\t"
    "v_exp_f32 v26, v26\n\t"
    "v_exp_f32 v27, v27\n\t"
    "v_exp_f32 v28, v28\n\t"
    "v_exp_f32 v29, v29\n\t"
    "v_exp_f32 v30, v30\n\t"
    "v_exp_f32 v31, v31\n\t"
    "v_cvt_pk_bf16_f32 v40, v24, v25\n\t"
    "v_cvt_pk_bf16_f32 v41, v26, v27\n\t"
    "v_cvt_pk_bf16_f32 v42, v28, v29\n\t"
    "v_cvt_pk_bf16_f32 v43, v30, v31\n\t"
    "s_setprio 3\n\t"
    "s_waitcnt lgkmcnt(0)\n\t"
    "s_nop 0\n\t"
    "v_mfma_f32_16x16x32_bf16 v[44:47], v[32:35], v[40:43], 0\n\t"
    "v_mfma_f32_16x16x32_bf16 v[48:51], v[36:39], v[40:43], 0\n\t"
    "v_mfma_f32_16x16x32_bf16 v[52:55], v[52:55], v[40:43], 0\n\t"
    "v_mov_b32 v12, 0\n\t"
    "v_mov_b32 v13, 0\n\t"
    "s_mov_b32 exec_hi, 0\n\t"
    "v_mov_b32 v12, %[one]\n\t"
    "v_mov_b32 v13, %[one]\n\t"
    "s_mov_b32 exec_hi, -1\n\t"
    "s_barrier\n\t"
    "ds_read_b64_tr_b16 v[24:25], v11 offset:34816\n\t"
    "ds_read_b64_tr_b16 v[26:27], v11 offset:34880\n\t"
    "ds_read_b64_tr_b16 v[28:29], v11 offset:35328\n\t"
    "ds_read_b64_tr_b16 v[30:31], v11 offset:35392\n\t"
    "ds_read_b64_tr_b16 v[32:33], v11 offset:35840\n\t"
    "ds_read_b64_tr_b16 v[34:35], v11 offset:35904\n\t"
    "ds_read_b64_tr_b16 v[36:37], v11 offset:36352\n\t"
    "ds_read_b64_tr_b16 v[38:39], v11 offset:36416\n\t"
    "s_waitcnt lgkmcnt(6)\n\t"
    "v_mfma_f32_16x16x32_bf16 v[40:43], v[24:27], v[16:19], 0\n\t"
    "s_waitcnt lgkmcnt(4)\n\t"
    "v_mfma_f32_16x16x32_bf16 v[40:43], v[28:31], v[20:23], v[40:43]\n\t"
    "s_waitcnt lgkmcnt(2)\n\t"
    "v_mfma_f32_16x16x32_bf16 v[40:43], v[32:35], v[56:59], v[40:43]\n\t"
    "s_waitcnt lgkmcnt(0)\n\t"
    "v_mfma_f32_16x16x32_bf16 v[40:43], v[36:39], v[60:63], v[40:43]\n\t"
    "5:\n\t"
    "s_waitcnt vmcnt(0)\n\t"
    "v_max3_u32 v11, v6, v7, v8\n\t"
    "v_max_u32 v11, v11, v9\n\t"
    "v_cmp_eq_u32 vcc, -1, v11\n\t"
    "s_cbranch_vccnz 4f\n\t"
    "6:\n\t"
    "s_nop 2\n\t"
    "v_exp_f32 v28, v40\n\t"
    "v_max3_f32 v11, v40, v41, v42\n\t"
    "v_exp_f32 v29, v41\n\t"
    "v_max_f32 v11, v11, v43\n\t"
    "v_exp_f32 v30, v42\n\t"
    "v_cmp_nge_f32 vcc, %[bg], v11\n\t"
    "v_exp_f32 v31, v43\n\t"
    "s_cbranch_vccnz 10f\n\t"
    "11:\n\t"
    "v_cvt_pk_bf16_f32 v24, v28, v29\n\t"
    "v_cvt_pk_bf16_f32 v25, v30, v31\n\t"
    "s_nop 1\n\t"
    "v_mfma_f32_16x16x16_bf16 v[52:55], v[12:13], v[24:25], v[52:55]\n\t"
    "v_mfma_f32_16x16x16_bf16 v[44:47], v[6:7], v[24:25], v[44:47]\n\t"
    "v_mfma_f32_16x16x16_bf16 v[48:51], v[8:9], v[24:25], v[48:51]\n\t"
    "s_nop 5\n\t"
    "v_rcp_f32 v26, v52\n\t"
    "s_nop 0\n\t"
    "v_pk_mul_f32 v[44:45], v[44:45], v[26:27] op_sel_hi:[1,0]\n\t"
    "v_pk_mul_f32 v[46:47], v[46:47], v[26:27] op_sel_hi:[1,0]\n\t"
    "v_pk_mul_f32 v[48:49], v[48:49], v[26:27] op_sel_hi:[1,0]\n\t"
    "v_pk_mul_f32 v[50:51], v[50:51], v[26:27] op_sel_hi:[1,0]\n\t"
    "v_cvt_pk_bf16_f32 v36, v44, v45\n\t"
    "v_cvt_pk_bf16_f32 v37, v46, v47\n\t"
    "v_cvt_pk_bf16_f32 v38, v48, v49\n\t"
    "v_cvt_pk_bf16_f32 v39, v50, v51\n\t"
    "v_min_u32 v36, -2, v36\n\t"
    "v_min_u32 v37, -2, v37\n\t"
    "v_min_u32 v38, -2, v38\n\t"
    "v_min_u32 v39, -2, v39\n\t"
    "global_store_dwordx4 v15, v[36:39], %[od] sc1\n\t"
    "s_barrier\n\t"
    "s_mov_b32 exec_hi, 0\n\t"
    "global_store_dwordx4 v14, v[6:9], %[kv] sc1\n\t"
    "s_mov_b32 exec_hi, -1\n\t"
    "s_nop 0\n\t"
    "s_waitcnt lgkmcnt(0)\n\t"
    "s_branch 9f\n\t"
    "1:\n\t"
    "s_add_u32 %[sp], %[sp], 1\n\t"
    "s_cmp_ge_u32 %[sp], %[cap]\n\t"
    "s_cbranch_scc1 7f\n\t"
    "global_load_dwordx4 v[40:43], v6, %[ag] sc1\n\t"
    "global_load_dwordx4 v[44:47], v6, %[ag] offset:1024 sc1\n\t"
    "s_waitcnt lgkmcnt(0)\n\t"
    "s_memtime %[tc]\n\t"
    "s_nop 7\n\t"
    "s_branch 3b\n\t"
    "7:\n\t"
    "s_mov_b32 %[cap], 0\n\t"
    "s_branch 2b\n\t"
    "20:\n\t"
    "s_barrier\n\t"
    "s_barrier\n\t"
    "s_barrier\n\t"
    "s_branch 9f\n\t"
    "4:\n\t"
    "s_add_u32 %[sp], %[sp], 0x10000\n\t"
    "s_lshr_b32 %[st], %[sp], 16\n\t"
    "s_cmp_ge_u32 %[st], %[cap]\n\t"
    "s_cbranch_scc1 8f\n\t"
    "s_mov_b32 exec_hi, 0\n\t"
    "global_load_dwordx4 v[6:9], v14, %[vs] sc1\n\t"
    "s_mov_b32 exec_hi, -1\n\t"
    "s_branch 5b\n\t"
    "8:\n\t"
    "s_mov_b32 %[cap], 0\n\t"
    "s_branch 6b\n\t"
    "10:\n\t"
    "v_max_f32 v11, v11, v10\n\t"
    "v_mov_b32 v26, v11\n\t"
    "s_nop 1\n\t"
    "v_permlane16_swap_b32 v11, v26\n\t"
    "v_max_f32 v11, v11, v26\n\t"
    "v_sub_f32 v28, v40, v11\n\t"
    "v_sub_f32 v29, v41, v11\n\t"
    "v_sub_f32 v30, v42, v11\n\t"
    "v_sub_f32 v31, v43, v11\n\t"
    "v_sub_f32 v11, v10, v11\n\t"
    "v_exp_f32 v11, v11\n\t"
    "v_exp_f32 v28, v28\n\t"
    "v_exp_f32 v29, v29\n\t"
    "v_exp_f32 v30, v30\n\t"
    "v_exp_f32 v31, v31\n\t"
    "v_mul_f32 v44, v44, v11\n\t"
    "v_mul_f32 v45, v45, v11\n\t"
    "v_mul_f32 v46, v46, v11\n\t"
    "v_mul_f32 v47, v47, v11\n\t"
    "v_mul_f32 v48, v48, v11\n\t"
    "v_mul_f32 v49, v49, v11\n\t"
    "v_mul_f32 v50, v50, v11\n\t"
    "v_mul_f32 v51, v51, v11\n\t"
    "v_mul_f32 v52, v52, v11\n\t"
    "v_mul_f32 v53, v53, v11\n\t"
    "v_mul_f32 v54, v54, v11\n\t"
    "v_mul_f32 v55, v55, v11\n\t"
    "s_branch 11b\n\t"
    "12:\n\t"
    "v_max3_f32 v10, v24, v25, v26\n\t"
    "v_max3_f32 v40, v27, v28, v29\n\t"
    "v_max3_f32 v10, v10, v30, v31\n\t"
    "v_max3_f32 v10, v10, v40, %[neg]\n\t"
    "v_mov_b32 v40, v10\n\t"
    "s_nop 1\n\t"
    "v_permlane16_swap_b32 v10, v40\n\t"
    "v_max_f32 v10, v10, v40\n\t"
    "v_mov_b32 v40, v10\n\t"
    "s_nop 1\n\t"
    "v_permlane32_swap_b32 v10, v40\n\t"
    "v_max_f32 v10, v10, v40\n\t"
    "v_sub_f32 v24, v24, v10\n\t"
    "v_sub_f32 v25, v25, v10\n\t"
    "v_sub_f32 v26, v26, v10\n\t"
    "v_sub_f32 v27, v27, v10\n\t"
    "v_sub_f32 v28, v28, v10\n\t"
    "v_sub_f32 v29, v29, v10\n\t"
    "v_sub_f32 v30, v30, v10\n\t"
    "v_sub_f32 v31, v31, v10\n\t"
    "s_mov_b32 %[bg], 0x7fc00000\n\t"
    "s_branch 13b\n\t"
    "9:"
    : [tc] "=&s"(tc), [sp] "=&s"(sp), [cap] "+s"(cap), [st] "=&s"(st), [bg] "=&s"(bg)
    : [ag] "s"(ag), [c44] "s"(0x4444u), [kv] "s"(kv), [vs] "s"(vs), [od] "s"(od), [od2] "s"(od + 262144), [neg] "s"(-1.0e38f), [lsc] "s"(LSC), [one] "s"(0x3F803F80u), [big] "s"(24.0f), "{v[64:95]}"(W.w0), "{v[96:127]}"(W.w1), "{v[128:159]}"(W.w2), "{v[160:191]}"(W.w3), "{v[192:223]}"(W.w4), "{v[224:255]}"(W.w5)
    : "v6", "v7", "v8", "v9", "v10", "v11", "v12", "v13", "v14", "v15", "v16", "v17", "v18", "v19", "v20", "v21", "v22", "v23", "v24", "v25", "v26", "v27", "v28", "v29", "v30", "v31", "v32", "v33", "v34", "v35", "v36", "v37", "v38", "v39", "v40", "v41", "v42", "v43", "v44", "v45", "v46", "v47", "v48", "v49", "v50", "v51", "v52", "v53", "v54", "v55", "v56", "v57", "v58", "v59", "v60", "v61", "v62", "v63", "vcc", "scc", "memory");
}
template <> __device__ __forceinline__ void qk_step<3>(const QKW& W, const char* ag, const char* vs, const char* od, const char* kv, unsigned& cap, unsigned& sp,
                                                        unsigned long long& tc, unsigned long long* ts, unsigned& d0, unsigned& d1) {
  unsigned st, bg;
  asm volatile("v_mbcnt_lo_u32_b32 v60, -1, 0\n\t"
    "v_mbcnt_hi_u32_b32 v60, -1, v60\n\t"
    "v_lshlrev_b32 v6, 4, v60\n\t"
    "s_mov_b32 %[sp], 0\n\t"
    "s_setprio 3\n\t"
    "global_load_dwordx4 v[40:43], v6, %[ag] sc1\n\t"
    "global_load_dwordx4 v[44:47], v6, %[ag] offset:1024 sc1\n\t"
    "s_memtime %[tc]\n\t"
    "v_add_u32 v7, 47104, v6\n\t"
    "v_add_u32 v8, 104448, v6\n\t"
    "ds_read_b128 v[24:27], v7 offset:0\n\t"
    "ds_read_b128 v[28:31], v7 offset:1024\n\t"
    "ds_read_b128 v[32:35], v7 offset:8192\n\t"
    "ds_read_b128 v[36:39], v7 offset:9216\n\t"
    "v_lshrrev_b32 v48, 4, v60\n\t"
    "v_and_b32 v49, 15, v60\n\t"
    "v_and_b32 v61, 4, v60\n\t"
    "v_mul_u32_u24 v61, 0x2aaa, v61\n\t"
    "v_lshrrev_b32 v9, 1, v60\n\t"
    "v_and_b32 v9, 2, v9\n\t"
    "v_or3_b32 v9, v9, v61, %[c44]\n\t"
    "v_add_u32 v14, 1536, v6\n\t"
    "v_lshlrev_b32 v61, 2, v48\n\t"
    "v_lshl_or_b32 v10, v49, 4, v61\n\t"
    "v_lshrrev_b32 v50, 2, v49\n\t"
    "v_and_b32 v51, 3, v60\n\t"
    "v_and_b32 v61, 1, v60\n\t"
    "v_lshlrev_b32 v61, 3, v61\n\t"
    "v_lshl_or_b32 v11, v50, 4, v61\n\t"
    "v_lshl_or_b32 v11, v48, 7, v11\n\t"
    "v_xor_b32 v12, v48, v49\n\t"
    "v_lshlrev_b32 v12, 4, v12\n\t"
    "v_lshl_or_b32 v12, v49, 8, v12\n\t"
    "v_lshl_or_b32 v50, v48, 2, v50\n\t"
    "v_or_b32 v51, 12, v51\n\t"
    "v_xor_b32 v51, v51, v50\n\t"
    "v_lshlrev_b32 v13, 4, v51\n\t"
    "v_lshl_or_b32 v13, v50, 8, v13\n\t"
    "v_and_b32 v61, 3, v60\n\t"
    "v_lshlrev_b32 v61, 4, v61\n\t"
    "v_lshl_or_b32 v15, v48, 6, v61\n\t"
    "v_and_b32 v61, 8, v60\n\t"
    "v_lshl_or_b32 v15, v61, 14, v15\n\t"
    "v_and_b32 v61, 4, v60\n\t"
    "v_lshl_or_b32 v15, v61, 8, v15\n\t"
    "v_add_u32 v15, 768, v15\n\t"
    "3:\n\t"
    "v_mov_b32 v16, 0\n\t"
    "v_mov_b32 v17, 0\n\t"
    "v_mov_b32 v18, 0\n\t"
    "v_mov_b32 v19, 0\n\t"
    "v_mov_b32 v20, 0\n\t"
    "v_mov_b32 v21, 0\n\t"
    "v_mov_b32 v22, 0\n\t"
    "v_mov_b32 v23, 0\n\t"
    "s_waitcnt vmcnt(0) lgkmcnt(0)\n\t"
    "14:\n\t"
    "ds_write_b128 v6, v[40:43] offset:6144\n\t"
    "ds_write_b128 v6, v[44:47] offset:7168\n\t"
    "v_smfmac_f32_16x16x64_bf16 v[16:19], v[40:43], v[160:167], v9\n\t"
    "v_max3_u32 v60, v40, v41, v42\n\t"
    "v_max3_u32 v61, v43, v44, v45\n\t"
    "v_smfmac_f32_16x16x64_bf16 v[20:23], v[40:43], v[168:175], v9\n\t"
    "v_max3_u32 v60, v60, v46, v47\n\t"
    "v_max_u32 v60, v60, v61\n\t"
    "v_cmp_eq_u32 vcc, -1, v60\n\t"
    "s_cbranch_vccnz 1f\n\t"
    "2:\n\t"
    "s_waitcnt lgkmcnt(0)\n\t"
    "s_cmp_eq_u64 %[kv], 0\n\t"
    "s_barrier\n\t"
    "s_cbranch_scc1 20f\n\t"
    "ds_read_b128 v[40:43], v6 offset:12288\n\t"
    "ds_read_b128 v[48:51], v6 offset:0\n\t"
    "v_smfmac_f32_16x16x64_bf16 v[16:19], v[44:47], v[176:183], v9\n\t"
    "ds_read_b128 v[52:55], v6 offset:1024\n\t"
    "ds_read_b128 v[56:59], v6 offset:2048\n\t"
    "v_smfmac_f32_16x16x64_bf16 v[20:23], v[44:47], v[184:191], v9\n\t"
    "ds_read_b128 v[60:63], v6 offset:13312\n\t"
    "ds_read_b128 v[44:47], v6 offset:3072\n\t"
    "s_waitcnt lgkmcnt(5)\n\t"
    "v_smfmac_f32_16x16x64_bf16 v[16:19], v[40:43], v[24:31], v9\n\t"
    "ds_read_b128 v[24:27], v7 offset:16384\n\t"
    "ds_read_b128 v[28:31], v7 offset:17408\n\t"
    "v_smfmac_f32_16x16x64_bf16 v[20:23], v[40:43], v[32:39], v9\n\t"
    "ds_read_b128 v[32:35], v7 offset:24576\n\t"
    "ds_read_b128 v[36:39], v7 offset:25600\n\t"
    "ds_read_b128 v[40:43], v6 offset:4096\n\t"
    "s_waitcnt lgkmcnt(9)\n\t"
    "v_smfmac_f32_16x16x64_bf16 v[16:19], v[48:51], v[64:71], v9\n\t"
    "v_smfmac_f32_16x16x64_bf16 v[20:23], v[48:51], v[72:79], v9\n\t"
    "ds_read_b128 v[48:51], v6 offset:5120\n\t"
    "s_waitcnt lgkmcnt(9)\n\t"
    "v_smfmac_f32_16x16x64_bf16 v[16:19], v[52:55], v[80:87], v9\n\t"
    "v_smfmac_f32_16x16x64_bf16 v[20:23], v[52:55], v[88:95], v9\n\t"
    "ds_read_b128 v[52:55], v6 offset:14336\n\t"
    "s_waitcnt lgkmcnt(9)\n\t"
    "v_smfmac_f32_16x16x64_bf16 v[16:19], v[56:59], v[96:103], v9\n\t"
    "v_smfmac_f32_16x16x64_bf16 v[20:23], v[56:59], v[104:111], v9\n\t"
    "ds_read_b128 v[56:59], v6 offset:8192\n\t"
    "s_waitcnt lgkmcnt(6)\n\t"
    "v_smfmac_f32_16x16x64_bf16 v[16:19], v[60:63], v[24:31], v9\n\t"
    "ds_read_b128 v[24:27], v8 offset:0\n\t"
    "ds_read_b128 v[28:31], v8 offset:1024\n\t"
    "s_waitcnt lgkmcnt(6)\n\t"
    "v_smfmac_f32_16x16x64_bf16 v[20:23], v[60:63], v[32:39], v9\n\t"
    "ds_read_b128 v[32:35], v8 offset:8192\n\t"
    "ds_read_b128 v[36:39], v8 offset:9216\n\t"
    "ds_read_b128 v[60:63], v6 offset:9216\n\t"
    "v_smfmac_f32_16x16x64_bf16 v[16:19], v[44:47], v[112:119], v9\n\t"
    "v_smfmac_f32_16x16x64_bf16 v[20:23], v[44:47], v[120:127], v9\n\t"
    "ds_read_b128 v[44:47], v6 offset:10240\n\t"
    "s_waitcnt lgkmcnt(9)\n\t"
    "v_smfmac_f32_16x16x64_bf16 v[16:19], v[40:43], v[128:135], v9\n\t"
    "v_smfmac_f32_16x16x64_bf16 v[20:23], v[40:43], v[136:143], v9\n\t"
    "ds_read_b128 v[40:43], v6 offset:15360\n\t"
    "s_waitcnt lgkmcnt(9)\n\t"
    "v_smfmac_f32_16x16x64_bf16 v[16:19], v[48:51], v[144:151], v9\n\t"
    "v_smfmac_f32_16x16x64_bf16 v[20:23], v[48:51], v[152:159], v9\n\t"
    "ds_read_b128 v[48:51], v6 offset:11264\n\t"
    "s_waitcnt lgkmcnt(6)\n\t"
    "v_smfmac_f32_16x16x64_bf16 v[16:19], v[52:55], v[24:31], v9\n\t"
    "ds_read_b128 v[24:27], v8 offset:16384\n\t"
    "ds_read_b128 v[28:31], v8 offset:17408\n\t"
    "s_waitcnt lgkmcnt(6)\n\t"
    "v_smfmac_f32_16x16x64_bf16 v[20:23], v[52:55], v[32:39], v9\n\t"
    "ds_read_b128 v[32:35], v8 offset:24576\n\t"
    "ds_read_b128 v[36:39], v8 offset:25600\n\t"
    "v_smfmac_f32_16x16x64_bf16 v[16:19], v[56:59], v[192:199], v9\n\t"
    "v_smfmac_f32_16x16x64_bf16 v[20:23], v[56:59], v[200:207], v9\n\t"
    "s_waitcnt lgkmcnt(7)\n\t"
    "v_smfmac_f32_16x16x64_bf16 v[16:19], v[60:63], v[208:215], v9\n\t"
    "v_smfmac_f32_16x16x64_bf16 v[20:23], v[60:63], v[216:223], v9\n\t"
    "s_waitcnt lgkmcnt(6)\n\t"
    "v_smfmac_f32_16x16x64_bf16 v[16:19], v[44:47], v[224:231], v9\n\t"
    "v_smfmac_f32_16x16x64_bf16 v[20:23], v[44:47], v[232:239], v9\n\t"
    "s_waitcnt lgkmcnt(2)\n\t"
    "v_smfmac_f32_16x16x64_bf16 v[16:19], v[40:43], v[24:31], v9\n\t"
    "s_waitcnt lgkmcnt(0)\n\t"
    "v_smfmac_f32_16x16x64_bf16 v[20:23], v[40:43], v[32:39], v9\n\t"
    "v_smfmac_f32_16x16x64_bf16 v[16:19], v[48:51], v[240:247], v9\n\t"
    "v_smfmac_f32_16x16x64_bf16 v[20:23], v[48:51], v[248:255], v9\n\t"
    "v_xor_b32 v7, 64, v12\n\t"
    "v_xor_b32 v8, 0x80, v12\n\t"
    "v_xor_b32 v6, 0xc0, v12\n\t"
    "ds_read_b128 v[24:27], v12 offset:16384\n\t"
    "ds_read_b128 v[28:31], v12 offset:20480\n\t"
    "ds_read_b128 v[32:35], v7 offset:16384\n\t"
    "ds_read_b128 v[36:39], v7 offset:20480\n\t"
    "v_permlane16_swap_b32 v16, v18\n\t"
    "v_permlane16_swap_b32 v17, v19\n\t"
    "v_permlane16_swap_b32 v20, v22\n\t"
    "v_permlane16_swap_b32 v21, v23\n\t"
    "v_add_f32 v16, v16, v18\n\t"
    "v_add_f32 v17, v17, v19\n\t"
    "v_add_f32 v20, v20, v22\n\t"
    "v_mul_f32 v16, %[lsc], v16\n\t"
    "v_mul_f32 v17, %[lsc], v17\n\t"
    "v_cvt_pk_bf16_f32 v16, v16, v17\n\t"
    "v_add_f32 v21, v21, v23\n\t"
    "ds_write_b32 v10, v16 offset:34304\n\t"
    "v_mul_f32 v20, %[lsc], v20\n\t"
    "v_mul_f32 v21, %[lsc], v21\n\t"
    "v_cvt_pk_bf16_f32 v20, v20, v21\n\t"
    "ds_write_b32 v10, v20 offset:34560\n\t"
    "ds_read_b128 v[40:43], v8 offset:16384\n\t"
    "ds_read_b128 v[44:47], v8 offset:20480\n\t"
    "ds_read_b128 v[48:51], v6 offset:16384\n\t"
    "ds_read_b128 v[52:55], v6 offset:20480\n\t"
    "s_waitcnt lgkmcnt(4)\n\t"
    "s_barrier\n\t"
    "ds_read_b64_tr_b16 v[16:17], v11 offset:32768\n\t"
    "ds_read_b64_tr_b16 v[18:19], v11 offset:32832\n\t"
    "ds_read_b64_tr_b16 v[20:21], v11 offset:33280\n\t"
    "ds_read_b64_tr_b16 v[22:23], v11 offset:33344\n\t"
    "ds_read_b64_tr_b16 v[56:57], v11 offset:33792\n\t"
    "ds_read_b64_tr_b16 v[58:59], v11 offset:33856\n\t"
    "ds_read_b64_tr_b16 v[60:61], v11 offset:34304\n\t"
    "ds_read_b64_tr_b16 v[62:63], v11 offset:34368\n\t"
    "v_mov_b32 v6, 0\n\t"
    "v_mov_b32 v7, 0\n\t"
    "v_mov_b32 v8, 0\n\t"
    "v_mov_b32 v9, 0\n\t"
    "s_mov_b32 exec_hi, 0\n\t"
    "global_load_dwordx4 v[6:9], v14, %[vs] sc1\n\t"
    "s_mov_b32 exec_hi, -1\n\t"
    "s_waitcnt lgkmcnt(6)\n\t"
    "v_mfma_f32_16x16x32_bf16 v[24:27], v[24:27], v[16:19], 0\n\t"
    "v_mfma_f32_16x16x32_bf16 v[28:31], v[28:31], v[16:19], 0\n\t"
    "s_waitcnt lgkmcnt(4)\n\t"
    "v_mfma_f32_16x16x32_bf16 v[24:27], v[32:35], v[20:23], v[24:27]\n\t"
    "v_mfma_f32_16x16x32_bf16 v[28:31], v[36:39], v[20:23], v[28:31]\n\t"
    "ds_read_b64_tr_b16 v[32:33], v13 offset:24576\n\t"
    "ds_read_b64_tr_b16 v[34:35], v13 offset:28672\n\t"
    "ds_read_b64_tr_b16 v[36:37], v13 offset:24584\n\t"
    "ds_read_b64_tr_b16 v[38:39], v13 offset:28680\n\t"
    "s_waitcnt lgkmcnt(6)\n\t"
    "v_mfma_f32_16x16x32_bf16 v[24:27], v[40:43], v[56:59], v[24:27]\n\t"
    "v_mfma_f32_16x16x32_bf16 v[28:31], v[44:47], v[56:59], v[28:31]\n\t"
    "s_waitcnt lgkmcnt(4)\n\t"
    "v_mfma_f32_16x16x32_bf16 v[24:27], v[48:51], v[60:63], v[24:27]\n\t"
    "v_mfma_f32_16x16x32_bf16 v[28:31], v[52:55], v[60:63], v[28:31]\n\t"
    "s_setprio 1\n\t"
    "v_mov_b32 v52, %[one]\n\t"
    "v_mov_b32 v53, %[one]\n\t"
    "v_mov_b32 v54, %[one]\n\t"
    "v_mov_b32 v55, %[one]\n\t"
    "s_nop 2\n\t"
    "v_max3_f32 v10, |v24|, |v25|, |v26|\n\t"
    "v_max3_f32 v40, |v27|, |v28|, |v29|\n\t"
    "v_max3_f32 v10, v10, |v30|, |v31|\n\t"
    "v_max_f32 v10, v10, v40\n\t"
    "v_cmp_nge_f32 vcc, %[big], v10\n\t"
    "s_mov_b32 %[bg], %[big]\n\t"
    "v_mov_b32 v10, 0\n\t"
    "s_cbranch_vccnz 12f\n\t"
    "13:\n\t"
    "v_exp_f32 v24, v24\n\t"
    "v_exp_f32 v25, v25\n\t"
    "v_exp_f32 v26, v26\n\t"
    "v_exp_f32 v27, v27\n\t"
    "v_exp_f32 v28, v28\n\t"
    "v_exp_f32 v29, v29\n\t"
    "v_exp_f32 v30, v30\n\t"
    "v_exp_f32 v31, v31\n\t"
    "v_cvt_pk_bf16_f32 v40, v24, v25\n\t"
    "v_cvt_pk_bf16_f32 v41, v26, v27\n\t"
    "v_cvt_pk_bf16_f32 v42, v28, v29\n\t"
    "v_cvt_pk_bf16_f32 v43, v30, v31\n\t"
    "s_setprio 3\n\t"
    "s_waitcnt lgkmcnt(0)\n\t"
    "s_nop 0\n\t"
    "v_mfma_f32_16x16x32_bf16 v[44:47], v[32:35], v[40:43], 0\n\t"
    "v_mfma_f32_16x16x32_bf16 v[48:51], v[36:39], v[40:43], 0\n\t"
    "v_mfma_f32_16x16x32_bf16 v[52:55], v[52:55], v[40:43], 0\n\t"
    "v_mov_b32 v12, 0\n\t"
    "v_mov_b32 v13, 0\n\t"
    "s_mov_b32 exec_hi, 0\n\t"
    "v_mov_b32 v12, %[one]\n\t"
    "v_mov_b32 v13, %[one]\n\t"
    "s_mov_b32 exec_hi, -1\n\t"
    "s_barrier\n\t"
    "ds_read_b64_tr_b16 v[24:25], v11 offset:34816\n\t"
    "ds_read_b64_tr_b16 v[26:27], v11 offset:34880\n\t"
    "ds_read_b64_tr_b16 v[28:29], v11 offset:35328\n\t"
    "ds_read_b64_tr_b16 v[30:31], v11 offset:35392\n\t"
    "ds_read_b64_tr_b16 v[32:33], v11 offset:35840\n\t"
    "ds_read_b64_tr_b16 v[34:35], v11 offset:35904\n\t"
    "ds_read_b64_tr_b16 v[36:37], v11 offset:36352\n\t"
    "ds_read_b64_tr_b16 v[38:39], v11 offset:36416\n\t"
    "s_waitcnt lgkmcnt(6)\n\t"
    "v_mfma_f32_16x16x32_bf16 v[40:43], v[24:27], v[16:19], 0\n\t"
    "s_waitcnt lgkmcnt(4)\n\t"
    "v_mfma_f32_16x16x32_bf16 v[40:43], v[28:31], v[20:23], v[40:43]\n\t"
    "s_waitcnt lgkmcnt(2)\n\t"
    "v_mfma_f32_16x16x32_bf16 v[40:43], v[32:35], v[56:59], v[40:43]\n\t"
    "s_waitcnt lgkmcnt(0)\n\t"
    "v_mfma_f32_16x16x32_bf16 v[40:43], v[36:39], v[60:63], v[40:43]\n\t"
    "5:\n\t"
    "s_waitcnt vmcnt(0)\n\t"
    "v_max3_u32 v11, v6, v7, v8\n\t"
    "v_max_u32 v11, v11, v9\n\t"
    "v_cmp_eq_u32 vcc, -1, v11\n\t"
    "s_cbranch_vccnz 4f\n\t"
    "6:\n\t"
    "s_nop 2\n\t"
    "v_exp_f32 v28, v40\n\t"
    "v_max3_f32 v11, v40, v41, v42\n\t"
    "v_exp_f32 v29, v41\n\t"
    "v_max_f32 v11, v11, v43\n\t"
    "v_exp_f32 v30, v42\n\t"
    "v_cmp_nge_f32 vcc, %[bg], v11\n\t"
    "v_exp_f32 v31, v43\n\t"
    "s_cbranch_vccnz 10f\n\t"
    "11:\n\t"
    "v_cvt_pk_bf16_f32 v24, v28, v29\n\t"
    "v_cvt_pk_bf16_f32 v25, v30, v31\n\t"
    "s_nop 1\n\t"
    "v_mfma_f32_16x16x16_bf16 v[52:55], v[12:13], v[24:25], v[52:55]\n\t"
    "v_mfma_f32_16x16x16_bf16 v[44:47], v[6:7], v[24:25], v[44:47]\n\t"
    "v_mfma_f32_16x16x16_bf16 v[48:51], v[8:9], v[24:25], v[48:51]\n\t"
    "s_nop 5\n\t"
    "v_rcp_f32 v26, v52\n\t"
    "s_nop 0\n\t"
    "v_pk_mul_f32 v[44:45], v[44:45], v[26:27] op_sel_hi:[1,0]\n\t"
    "v_pk_mul_f32 v[46:47], v[46:47], v[26:27] op_sel_hi:[1,0]\n\t"
    "v_pk_mul_f32 v[48:49], v[48:49], v[26:27] op_sel_hi:[1,0]\n\t"
    "v_pk_mul_f32 v[50:51], v[50:51], v[26:27] op_sel_hi:[1,0]\n\t"
    "v_cvt_pk_bf16_f32 v36, v44, v45\n\t"
    "v_cvt_pk_bf16_f32 v37, v46, v47\n\t"
    "v_cvt_pk_bf16_f32 v38, v48, v49\n\t"
    "v_cvt_pk_bf16_f32 v39, v50, v51\n\t"
    "v_min_u32 v36, -2, v36\n\t"
    "v_min_u32 v37, -2, v37\n\t"
    "v_min_u32 v38, -2, v38\n\t"
    "v_min_u32 v39, -2, v39\n\t"
    "global_store_dwordx4 v15, v[36:39], %[od] sc1\n\t"
    "s_barrier\n\t"
    "s_mov_b32 exec_hi, 0\n\t"
    "global_store_dwordx4 v14, v[6:9], %[kv] sc1\n\t"
    "s_mov_b32 exec_hi, -1\n\t"
    "s_nop 0\n\t"
    "s_waitcnt lgkmcnt(0)\n\t"
    "s_branch 9f\n\t"
    "1:\n\t"
    "s_add_u32 %[sp], %[sp], 1\n\t"
    "s_cmp_ge_u32 %[sp], %[cap]\n\t"
    "s_cbranch_scc1 7f\n\t"
    "global_load_dwordx4 v[40:43], v6, %[ag] sc1\n\t"
    "global_load_dwordx4 v[44:47], v6, %[ag] offset:1024 sc1\n\t"
    "s_waitcnt lgkmcnt(0)\n\t"
    "s_memtime %[tc]\n\t"
    "s_nop 7\n\t"
    "s_branch 3b\n\t"
    "7:\n\t"
    "s_mov_b32 %[cap], 0\n\t"
    "s_branch 2b\n\t"
    "20:\n\t"
    "s_barrier\n\t"
    "s_barrier\n\t"
    "s_barrier\n\t"
    "s_branch 9f\n\t"
    "4:\n\t"
    "s_add_u32 %[sp], %[sp], 0x10000\n\t"
    "s_lshr_b32 %[st], %[sp], 16\n\t"
    "s_cmp_ge_u32 %[st], %[cap]\n\t"
    "s_cbranch_scc1 8f\n\t"
    "s_mov_b32 exec_hi, 0\n\t"
    "global_load_dwordx4 v[6:9], v14, %[vs] sc1\n\t"
    "s_mov_b32 exec_hi, -1\n\t"
    "s_branch 5b\n\t"
    "8:\n\t"
    "s_mov_b32 %[cap], 0\n\t"
    "s_branch 6b\n\t"
    "10:\n\t"
    "v_max_f32 v11, v11, v10\n\t"
    "v_mov_b32 v26, v11\n\t"
    "s_nop 1\n\t"
    "v_permlane16_swap_b32 v11, v26\n\t"
    "v_max_f32 v11, v11, v26\n\t"
    "v_sub_f32 v28, v40, v11\n\t"
    "v_sub_f32 v29, v41, v11\n\t"
    "v_sub_f32 v30, v42, v11\n\t"
    "v_sub_f32 v31, v43, v11\n\t"
    "v_sub_f32 v11, v10, v11\n\t"
    "v_exp_f32 v11, v11\n\t"
    "v_exp_f32 v28, v28\n\t"
    "v_exp_f32 v29, v29\n\t"
    "v_exp_f32 v30, v30\n\t"
    "v_exp_f32 v31, v31\n\t"
    "v_mul_f32 v44, v44, v11\n\t"
    "v_mul_f32 v45, v45, v11\n\t"
    "v_mul_f32 v46, v46, v11\n\t"
    "v_mul_f32 v47, v47, v11\n\t"
    "v_mul_f32 v48, v48, v11\n\t"
    "v_mul_f32 v49, v49, v11\n\t"
    "v_mul_f32 v50, v50, v11\n\t"
    "v_mul_f32 v51, v51, v11\n\t"
    "v_mul_f32 v52, v52, v11\n\t"
    "v_mul_f32 v53, v53, v11\n\t"
    "v_mul_f32 v54, v54, v11\n\t"
    "v_mul_f32 v55, v55, v11\n\t"
    "s_branch 11b\n\t"
    "12:\n\t"
    "v_max3_f32 v10, v24, v25, v26\n\t"
    "v_max3_f32 v40, v27, v28, v29\n\t"
    "v_max3_f32 v10, v10, v30, v31\n\t"
    "v_max3_f32 v10, v10, v40, %[neg]\n\t"
    "v_mov_b32 v40, v10\n\t"
    "s_nop 1\n\t"
    "v_permlane16_swap_b32 v10, v40\n\t"
    "v_max_f32 v10, v10, v40\n\t"
    "v_mov_b32 v40, v10\n\t"
    "s_nop 1\n\t"
    "v_permlane32_swap_b32 v10, v40\n\t"
    "v_max_f32 v10, v10, v40\n\t"
    "v_sub_f32 v24, v24, v10\n\t"
    "v_sub_f32 v25, v25, v10\n\t"
    "v_sub_f32 v26, v26, v10\n\t"
    "v_sub_f32 v27, v27, v10\n\t"
    "v_sub_f32 v28, v28, v10\n\t"
    "v_sub_f32 v29, v29, v10\n\t"
    "v_sub_f32 v30, v30, v10\n\t"
    "v_sub_f32 v31, v31, v10\n\t"
    "s_mov_b32 %[bg], 0x7fc00000\n\t"
    "s_branch 13b\n\t"
    "9:"
    : [tc] "=&s"(tc), [sp] "=&s"(sp), [cap] "+s"(cap), [st] "=&s"(st), [bg] "=&s"(bg)
    : [ag] "s"(ag), [c44] "s"(0x4444u), [kv] "s"(kv), [vs] "s"(vs), [od] "s"(od), [od2] "s"(od + 262144), [neg] "s"(-1.0e38f), [lsc] "s"(LSC), [one] "s"(0x3F803F80u), [big] "s"(24.0f), "{v[64:95]}"(W.w0), "{v[96:127]}"(W.w1), "{v[128:159]}"(W.w2), "{v[160:191]}"(W.w3), "{v[192:223]}"(W.w4), "{v[224:255]}"(W.w5)
    : "v6", "v7", "v8", "v9", "v10", "v11", "v12", "v13", "v14", "v15", "v16", "v17", "v18", "v19", "v20", "v21", "v22", "v23", "v24", "v25", "v26", "v27", "v28", "v29", "v30", "v31", "v32", "v33", "v34", "v35", "v36", "v37", "v38", "v39", "v40", "v41", "v42", "v43", "v44", "v45", "v46", "v47", "v48", "v49", "v50", "v51", "v52", "v53", "v54", "v55", "v56", "v57", "v58", "v59", "v60", "v61", "v62", "v63", "vcc", "scc", "memory");
}
template <> __device__ __forceinline__ void qk_step<4>(const QKW& W, const char* ag, const char* vs, const char* od, const char* kv, unsigned& cap, unsigned& sp,
                                                        unsigned long long& tc, unsigned long long* ts, unsigned& d0, unsigned& d1) {
  unsigned st, bg;
  asm volatile("v_mbcnt_lo_u32_b32 v52, -1, 0\n\t"
    "v_mbcnt_hi_u32_b32 v52, -1, v52\n\t"
    "v_lshlrev_b32 v6, 4, v52\n\t"
    "s_mov_b32 %[sp], 0\n\t"
    "global_load_dwordx4 v[36:39], v6, %[ag] sc1\n\t"
    "global_load_dwordx4 v[40:43], v6, %[ag] offset:1024 sc1\n\t"
    "s_memtime %[tc]\n\t"
    "v_add_u32 v7, 73728, v6\n\t"
    "v_add_u32 v8, 131072, v6\n\t"
    "ds_read_b128 v[20:23], v7 offset:8192\n\t"
    "ds_read_b128 v[24:27], v7 offset:9216\n\t"
    "ds_read_b128 v[28:31], v7 offset:0\n\t"
    "ds_read_b128 v[32:35], v7 offset:1024\n\t"
    "v_lshrrev_b32 v44, 4, v52\n\t"
    "v_and_b32 v45, 15, v52\n\t"
    "v_and_b32 v53, 4, v52\n\t"
    "v_mul_u32_u24 v53, 0x2aaa, v53\n\t"
    "v_lshrrev_b32 v9, 1, v52\n\t"
    "v_and_b32 v9, 2, v9\n\t"
    "v_or3_b32 v9, v9, v53, %[c44]\n\t"
    "v_lshlrev_b32 v53, 2, v44\n\t"
    "v_lshl_or_b32 v10, v45, 4, v53\n\t"
    "3:\n\t"
    "v_mov_b32 v12, 0\n\t"
    "v_mov_b32 v13, 0\n\t"
    "v_mov_b32 v14, 0\n\t"
    "v_mov_b32 v15, 0\n\t"
    "v_mov_b32 v16, 0\n\t"
    "v_mov_b32 v17, 0\n\t"
    "v_mov_b32 v18, 0\n\t"
    "v_mov_b32 v19, 0\n\t"
    "s_waitcnt vmcnt(0) lgkmcnt(0)\n\t"
    "14:\n\t"
    "ds_write_b128 v6, v[36:39] offset:8192\n\t"
    "ds_write_b128 v6, v[40:43] offset:9216\n\t"
    "v_smfmac_f32_16x16x64_bf16 v[12:15], v[36:39], v[128:135], v9\n\t"
    "v_max3_u32 v52, v36, v37, v38\n\t"
    "v_max3_u32 v53, v39, v40, v41\n\t"
    "v_smfmac_f32_16x16x64_bf16 v[16:19], v[36:39], v[136:143], v9\n\t"
    "v_max3_u32 v52, v52, v42, v43\n\t"
    "v_max_u32 v52, v52, v53\n\t"
    "v_cmp_eq_u32 vcc, -1, v52\n\t"
    "s_cbranch_vccnz 1f\n\t"
    "2:\n\t"
    "s_waitcnt lgkmcnt(0)\n\t"
    "s_barrier\n\t"
    "s_memrealtime s[36:37]\n\t"
    "ds_read_b128 v[36:39], v6 offset:0\n\t"
    "ds_read_b128 v[44:47], v6 offset:4096\n\t"
    "v_smfmac_f32_16x16x64_bf16 v[12:15], v[40:43], v[144:151], v9\n\t"
    "ds_read_b128 v[48:51], v6 offset:5120\n\t"
    "ds_read_b128 v[52:55], v6 offset:6144\n\t"
    "v_smfmac_f32_16x16x64_bf16 v[16:19], v[40:43], v[152:159], v9\n\t"
    "ds_read_b128 v[40:43], v6 offset:1024\n\t"
    "s_waitcnt lgkmcnt(0)\n\t"
    "s_mov_b32 s38, s36\n\t"
    "s_min_u32 s37, %[d0], %[d1]\n\t"
    "s_add_u32 s36, s36, s37\n\t"
    "s_and_b32 s36, s36, 0xffffff\n\t"
    "s_or_b32 s36, s36, %[sn]\n\t"
    "v_lshlrev_b32 v11, 10, v6\n\t"
    "v_mov_b32 v5, s36\n\t"
    "s_mov_b64 exec, 15\n\t"
    "global_store_dword v11, v5, %[vs] offset:-4096 sc1\n\t"
    "s_mov_b64 exec, -1\n\t"
    "s_sleep 0\n\t"
    "v_smfmac_f32_16x16x64_bf16 v[12:15], v[36:39], v[56:63], v9\n\t"
    "v_smfmac_f32_16x16x64_bf16 v[16:19], v[36:39], v[28:35], v9\n\t"
    "ds_read_b128 v[28:31], v7 offset:16384\n\t"
    "ds_read_b128 v[32:35], v7 offset:17408\n\t"
    "ds_read_b128 v[36:39], v6 offset:7168\n\t"
    "v_smfmac_f32_16x16x64_bf16 v[12:15], v[44:47], v[64:71], v9\n\t"
    "v_smfmac_f32_16x16x64_bf16 v[16:19], v[44:47], v[72:79], v9\n\t"
    "ds_read_b128 v[44:47], v6 offset:10240\n\t"
    "v_smfmac_f32_16x16x64_bf16 v[12:15], v[48:51], v[80:87], v9\n\t"
    "v_smfmac_f32_16x16x64_bf16 v[16:19], v[48:51], v[88:95], v9\n\t"
    "ds_read_b128 v[48:51], v6 offset:11264\n\t"
    "v_smfmac_f32_16x16x64_bf16 v[12:15], v[52:55], v[96:103], v9\n\t"
    "v_smfmac_f32_16x16x64_bf16 v[16:19], v[52:55], v[104:111], v9\n\t"
    "ds_read_b128 v[52:55], v6 offset:2048\n\t"
    "v_smfmac_f32_16x16x64_bf16 v[12:15], v[40:43], v[20:27], v9\n\t"
    "ds_read_b128 v[20:23], v8 offset:0\n\t"
    "ds_read_b128 v[24:27], v8 offset:1024\n\t"
    "s_waitcnt lgkmcnt(6)\n\t"
    "v_smfmac_f32_16x16x64_bf16 v[16:19], v[40:43], v[28:35], v9\n\t"
    "ds_read_b128 v[28:31], v8 offset:8192\n\t"
    "ds_read_b128 v[32:35], v8 offset:9216\n\t"
    "ds_read_b128 v[40:43], v6 offset:12288\n\t"
    "s_waitcnt lgkmcnt(8)\n\t"
    "v_smfmac_f32_16x16x64_bf16 v[12:15], v[36:39], v[112:119], v9\n\t"
    "v_smfmac_f32_16x16x64_bf16 v[16:19], v[36:39], v[120:127], v9\n\t"
    "ds_read_b128 v[36:39], v6 offset:13312\n\t"
    "s_waitcnt lgkmcnt(8)\n\t"
    "v_smfmac_f32_16x16x64_bf16 v[12:15], v[44:47], v[160:167], v9\n\t"
    "v_smfmac_f32_16x16x64_bf16 v[16:19], v[44:47], v[168:175], v9\n\t"
    "ds_read_b128 v[44:47], v6 offset:14336\n\t"
    "s_barrier\n\t"
    "s_setprio 2\n\t"
    "s_waitcnt lgkmcnt(8)\n\t"
    "v_smfmac_f32_16x16x64_bf16 v[12:15], v[48:51], v[176:183], v9\n\t"
    "v_smfmac_f32_16x16x64_bf16 v[16:19], v[48:51], v[184:191], v9\n\t"
    "ds_read_b128 v[48:51], v6 offset:3072\n\t"
    "s_waitcnt lgkmcnt(6)\n\t"
    "v_smfmac_f32_16x16x64_bf16 v[12:15], v[52:55], v[20:27], v9\n\t"
    "ds_read_b128 v[20:23], v8 offset:16384\n\t"
    "ds_read_b128 v[24:27], v8 offset:17408\n\t"
    "s_waitcnt lgkmcnt(6)\n\t"
    "v_smfmac_f32_16x16x64_bf16 v[16:19], v[52:55], v[28:35], v9\n\t"
    "ds_read_b128 v[28:31], v8 offset:24576\n\t"
    "ds_read_b128 v[32:35], v8 offset:25600\n\t"
    "ds_read_b128 v[52:55], v6 offset:15360\n\t"
    "s_waitcnt lgkmcnt(8)\n\t"
    "v_smfmac_f32_16x16x64_bf16 v[12:15], v[40:43], v[192:199], v9\n\t"
    "v_smfmac_f32_16x16x64_bf16 v[16:19], v[40:43], v[200:207], v9\n\t"
    "s_waitcnt lgkmcnt(7)\n\t"
    "v_smfmac_f32_16x16x64_bf16 v[12:15], v[36:39], v[208:215], v9\n\t"
    "v_smfmac_f32_16x16x64_bf16 v[16:19], v[36:39], v[216:223], v9\n\t"
    "s_waitcnt lgkmcnt(6)\n\t"
    "v_smfmac_f32_16x16x64_bf16 v[12:15], v[44:47], v[224:231], v9\n\t"
    "v_smfmac_f32_16x16x64_bf16 v[16:19], v[44:47], v[232:239], v9\n\t"
    "s_waitcnt lgkmcnt(3)\n\t"
    "v_smfmac_f32_16x16x64_bf16 v[12:15], v[48:51], v[20:27], v9\n\t"
    "s_waitcnt lgkmcnt(1)\n\t"
    "v_smfmac_f32_16x16x64_bf16 v[16:19], v[48:51], v[28:35], v9\n\t"
    "s_waitcnt lgkmcnt(0)\n\t"
    "v_smfmac_f32_16x16x64_bf16 v[12:15], v[52:55], v[240:247], v9\n\t"
    "v_smfmac_f32_16x16x64_bf16 v[16:19], v[52:55], v[248:255], v9\n\t"
    "s_nop 6\n\t"
    "v_permlane16_swap_b32 v12, v14\n\t"
    "v_permlane16_swap_b32 v13, v15\n\t"
    "v_permlane16_swap_b32 v16, v18\n\t"
    "v_permlane16_swap_b32 v17, v19\n\t"
    "v_add_f32 v12, v12, v14\n\t"
    "v_add_f32 v13, v13, v15\n\t"
    "v_add_f32 v16, v16, v18\n\t"
    "v_cvt_pk_bf16_f32 v12, v12, v13\n\t"
    "v_add_f32 v17, v17, v19\n\t"
    "ds_write_b32 v10, v12 offset:34816\n\t"
    "v_cvt_pk_bf16_f32 v16, v16, v17\n\t"
    "ds_write_b32 v10, v16 offset:35072\n\t"
    "s_waitcnt lgkmcnt(0)\n\t"
    "s_setprio 0\n\t"
    "s_barrier\n\t"
    "s_memrealtime s[36:37]\n\t"
    "s_waitcnt lgkmcnt(0)\n\t"
    "s_and_b32 s36, s36, 0xffffff\n\t"
    "s_or_b32 s36, s36, %[sn]\n\t"
    "v_lshlrev_b32 v7, 10, v6\n\t"
    "v_mov_b32 v8, s36\n\t"
    "s_mov_b64 exec, 15\n\t"
    "global_store_dword v7, v8, %[vs] offset:0 sc1\n\t"
    "s_mov_b64 exec, -1\n\t"
    "s_barrier\n\t"
    "s_memrealtime s[36:37]\n\t"
    "s_mov_b32 %[d1], %[d0]\n\t"
    "s_waitcnt lgkmcnt(0)\n\t"
    "s_sub_u32 %[d0], s36, s38\n\t"
    "s_min_u32 %[d0], %[d0], 200\n\t"
    "s_waitcnt lgkmcnt(0)\n\t"
    "s_branch 9f\n\t"
    "1:\n\t"
    "s_add_u32 %[sp], %[sp], 1\n\t"
    "s_cmp_ge_u32 %[sp], %[cap]\n\t"
    "s_cbranch_scc1 7f\n\t"
    "global_load_dwordx4 v[36:39], v6, %[ag] sc1\n\t"
    "global_load_dwordx4 v[40:43], v6, %[ag] offset:1024 sc1\n\t"
    "s_waitcnt lgkmcnt(0)\n\t"
    "s_memtime %[tc]\n\t"
    "s_nop 7\n\t"
    "s_branch 3b\n\t"
    "7:\n\t"
    "s_mov_b32 %[cap], 0\n\t"
    "s_branch 2b\n\t"
    "9:"
    : [tc] "=&s"(tc), [sp] "=&s"(sp), [cap] "+s"(cap), [d0] "+s"(d0), [d1] "+s"(d1)
    : [ag] "s"(ag), [c44] "s"(0x4444u), [vs] "s"(vs), [sn] "s"((unsigned)(size_t)od), "{v[64:95]}"(W.w0), "{v[96:127]}"(W.w1), "{v[128:159]}"(W.w2), "{v[160:191]}"(W.w3), "{v[192:223]}"(W.w4), "{v[224:255]}"(W.w5), "{v[56:63]}"(W.x)
    : "v5", "v6", "v7", "v8", "v9", "v10", "v11", "v12", "v13", "v14", "v15", "v16", "v17", "v18", "v19", "v20", "v21", "v22", "v23", "v24", "v25", "v26", "v27", "v28", "v29", "v30", "v31", "v32", "v33", "v34", "v35", "v36", "v37", "v38", "v39", "v40", "v41", "v42", "v43", "v44", "v45", "v46", "v47", "v48", "v49", "v50", "v51", "v52", "v53", "v54", "v55", "vcc", "scc", "memory", "s36", "s37", "s38");
}
template <> __device__ __forceinline__ void qk_step<5>(const QKW& W, const char* ag, const char* vs, const char* od, const char* kv, unsigned& cap, unsigned& sp,
                                                        unsigned long long& tc, unsigned long long* ts, unsigned& d0, unsigned& d1) {
  unsigned st, bg;
  asm volatile("v_mbcnt_lo_u32_b32 v52, -1, 0\n\t"
    "v_mbcnt_hi_u32_b32 v52, -1, v52\n\t"
    "v_lshlrev_b32 v6, 4, v52\n\t"
    "s_mov_b32 %[sp], 0\n\t"
    "global_load_dwordx4 v[36:39], v6, %[ag] sc1\n\t"
    "global_load_dwordx4 v[40:43], v6, %[ag] offset:1024 sc1\n\t"
    "s_memtime %[tc]\n\t"
    "v_add_u32 v7, 75776, v6\n\t"
    "v_add_u32 v8, 133120, v6\n\t"
    "ds_read_b128 v[20:23], v7 offset:8192\n\t"
    "ds_read_b128 v[24:27], v7 offset:9216\n\t"
    "ds_read_b128 v[28:31], v7 offset:0\n\t"
    "ds_read_b128 v[32:35], v7 offset:1024\n\t"
    "v_lshrrev_b32 v44, 4, v52\n\t"
    "v_and_b32 v45, 15, v52\n\t"
    "v_and_b32 v53, 4, v52\n\t"
    "v_mul_u32_u24 v53, 0x2aaa, v53\n\t"
    "v_lshrrev_b32 v9, 1, v52\n\t"
    "v_and_b32 v9, 2, v9\n\t"
    "v_or3_b32 v9, v9, v53, %[c44]\n\t"
    "v_lshlrev_b32 v53, 2, v44\n\t"
    "v_lshl_or_b32 v10, v45, 4, v53\n\t"
    "v_and_b32 v53, 1, v52\n\t"
    "v_lshlrev_b32 v53, 3, v53\n\t"
    "v_lshl_or_b32 v11, v44, 7, v53\n\t"
    "v_and_b32 v53, 12, v52\n\t"
    "v_lshl_or_b32 v11, v53, 2, v11\n\t"
    "v_and_b32 v53, 2, v52\n\t"
    "v_lshl_or_b32 v11, v53, 8, v11\n\t"
    "3:\n\t"
    "v_mov_b32 v12, 0\n\t"
    "v_mov_b32 v13, 0\n\t"
    "v_mov_b32 v14, 0\n\t"
    "v_mov_b32 v15, 0\n\t"
    "v_mov_b32 v16, 0\n\t"
    "v_mov_b32 v17, 0\n\t"
    "v_mov_b32 v18, 0\n\t"
    "v_mov_b32 v19, 0\n\t"
    "s_waitcnt vmcnt(0) lgkmcnt(0)\n\t"
    "14:\n\t"
    "ds_write_b128 v6, v[36:39] offset:10240\n\t"
    "ds_write_b128 v6, v[40:43] offset:11264\n\t"
    "v_smfmac_f32_16x16x64_bf16 v[12:15], v[36:39], v[160:167], v9\n\t"
    "v_max3_u32 v52, v36, v37, v38\n\t"
    "v_max3_u32 v53, v39, v40, v41\n\t"
    "v_smfmac_f32_16x16x64_bf16 v[16:19], v[36:39], v[168:175], v9\n\t"
    "v_max3_u32 v52, v52, v42, v43\n\t"
    "v_max_u32 v52, v52, v53\n\t"
    "v_cmp_eq_u32 vcc, -1, v52\n\t"
    "s_cbranch_vccnz 1f\n\t"
    "2:\n\t"
    "s_waitcnt lgkmcnt(0)\n\t"
    "s_barrier\n\t"
    "ds_read_b128 v[36:39], v6 offset:0\n\t"
    "ds_read_b128 v[44:47], v6 offset:4096\n\t"
    "v_smfmac_f32_16x16x64_bf16 v[12:15], v[40:43], v[176:183], v9\n\t"
    "ds_read_b128 v[48:51], v6 offset:5120\n\t"
    "ds_read_b128 v[52:55], v6 offset:6144\n\t"
    "v_smfmac_f32_16x16x64_bf16 v[16:19], v[40:43], v[184:191], v9\n\t"
    "ds_read_b128 v[40:43], v6 offset:1024\n\t"
    "s_sleep 0\n\t"
    "s_waitcnt lgkmcnt(4)\n\t"
    "v_smfmac_f32_16x16x64_bf16 v[12:15], v[36:39], v[56:63], v9\n\t"
    "v_smfmac_f32_16x16x64_bf16 v[16:19], v[36:39], v[28:35], v9\n\t"
    "ds_read_b128 v[28:31], v7 offset:16384\n\t"
    "ds_read_b128 v[32:35], v7 offset:17408\n\t"
    "ds_read_b128 v[36:39], v6 offset:7168\n\t"
    "s_waitcnt lgkmcnt(6)\n\t"
    "v_smfmac_f32_16x16x64_bf16 v[12:15], v[44:47], v[64:71], v9\n\t"
    "v_smfmac_f32_16x16x64_bf16 v[16:19], v[44:47], v[72:79], v9\n\t"
    "ds_read_b128 v[44:47], v6 offset:8192\n\t"
    "s_waitcnt lgkmcnt(6)\n\t"
    "v_smfmac_f32_16x16x64_bf16 v[12:15], v[48:51], v[80:87], v9\n\t"
    "v_smfmac_f32_16x16x64_bf16 v[16:19], v[48:51], v[88:95], v9\n\t"
    "ds_read_b128 v[48:51], v6 offset:9216\n\t"
    "s_waitcnt lgkmcnt(6)\n\t"
    "v_smfmac_f32_16x16x64_bf16 v[12:15], v[52:55], v[96:103], v9\n\t"
    "v_smfmac_f32_16x16x64_bf16 v[16:19], v[52:55], v[104:111], v9\n\t"
    "ds_read_b128 v[52:55], v6 offset:2048\n\t"
    "s_waitcnt lgkmcnt(6)\n\t"
    "v_smfmac_f32_16x16x64_bf16 v[12:15], v[40:43], v[20:27], v9\n\t"
    "ds_read_b128 v[20:23], v8 offset:0\n\t"
    "ds_read_b128 v[24:27], v8 offset:1024\n\t"
    "s_waitcnt lgkmcnt(6)\n\t"
    "v_smfmac_f32_16x16x64_bf16 v[16:19], v[40:43], v[28:35], v9\n\t"
    "ds_read_b128 v[28:31], v8 offset:8192\n\t"
    "ds_read_b128 v[32:35], v8 offset:9216\n\t"
    "ds_read_b128 v[40:43], v6 offset:12288\n\t"
    "s_waitcnt lgkmcnt(8)\n\t"
    "v_smfmac_f32_16x16x64_bf16 v[12:15], v[36:39], v[112:119], v9\n\t"
    "v_smfmac_f32_16x16x64_bf16 v[16:19], v[36:39], v[120:127], v9\n\t"
    "ds_read_b128 v[36:39], v6 offset:13312\n\t"
    "s_waitcnt lgkmcnt(8)\n\t"
    "v_smfmac_f32_16x16x64_bf16 v[12:15], v[44:47], v[128:135], v9\n\t"
    "v_smfmac_f32_16x16x64_bf16 v[16:19], v[44:47], v[136:143], v9\n\t"
    "ds_read_b128 v[44:47], v6 offset:14336\n\t"
    "s_barrier\n\t"
    "s_setprio 2\n\t"
    "s_waitcnt lgkmcnt(8)\n\t"
    "v_smfmac_f32_16x16x64_bf16 v[12:15], v[48:51], v[144:151], v9\n\t"
    "v_smfmac_f32_16x16x64_bf16 v[16:19], v[48:51], v[152:159], v9\n\t"
    "ds_read_b128 v[48:51], v6 offset:3072\n\t"
    "s_waitcnt lgkmcnt(6)\n\t"
    "v_smfmac_f32_16x16x64_bf16 v[12:15], v[52:55], v[20:27], v9\n\t"
    "ds_read_b128 v[20:23], v8 offset:16384\n\t"
    "ds_read_b128 v[24:27], v8 offset:17408\n\t"
    "s_waitcnt lgkmcnt(6)\n\t"
    "v_smfmac_f32_16x16x64_bf16 v[16:19], v[52:55], v[28:35], v9\n\t"
    "ds_read_b128 v[28:31], v8 offset:24576\n\t"
    "ds_read_b128 v[32:35], v8 offset:25600\n\t"
    "ds_read_b128 v[52:55], v6 offset:15360\n\t"
    "s_waitcnt lgkmcnt(8)\n\t"
    "v_smfmac_f32_16x16x64_bf16 v[12:15], v[40:43], v[192:199], v9\n\t"
    "v_smfmac_f32_16x16x64_bf16 v[16:19], v[40:43], v[200:207], v9\n\t"
    "s_waitcnt lgkmcnt(7)\n\t"
    "v_smfmac_f32_16x16x64_bf16 v[12:15], v[36:39], v[208:215], v9\n\t"
    "v_smfmac_f32_16x16x64_bf16 v[16:19], v[36:39], v[216:223], v9\n\t"
    "s_waitcnt lgkmcnt(6)\n\t"
    "v_smfmac_f32_16x16x64_bf16 v[12:15], v[44:47], v[224:231], v9\n\t"
    "v_smfmac_f32_16x16x64_bf16 v[16:19], v[44:47], v[232:239], v9\n\t"
    "s_waitcnt lgkmcnt(3)\n\t"
    "v_smfmac_f32_16x16x64_bf16 v[12:15], v[48:51], v[20:27], v9\n\t"
    "s_waitcnt lgkmcnt(1)\n\t"
    "v_smfmac_f32_16x16x64_bf16 v[16:19], v[48:51], v[28:35], v9\n\t"
    "s_waitcnt lgkmcnt(0)\n\t"
    "v_smfmac_f32_16x16x64_bf16 v[12:15], v[52:55], v[240:247], v9\n\t"
    "v_smfmac_f32_16x16x64_bf16 v[16:19], v[52:55], v[248:255], v9\n\t"
    "s_nop 6\n\t"
    "v_permlane16_swap_b32 v12, v14\n\t"
    "v_permlane16_swap_b32 v13, v15\n\t"
    "v_permlane16_swap_b32 v16, v18\n\t"
    "v_permlane16_swap_b32 v17, v19\n\t"
    "v_add_f32 v12, v12, v14\n\t"
    "v_add_f32 v13, v13, v15\n\t"
    "v_add_f32 v16, v16, v18\n\t"
    "v_cvt_pk_bf16_f32 v12, v12, v13\n\t"
    "v_add_f32 v17, v17, v19\n\t"
    "ds_write_b32 v10, v12 offset:35328\n\t"
    "v_cvt_pk_bf16_f32 v16, v16, v17\n\t"
    "ds_write_b32 v10, v16 offset:35584\n\t"
    "s_waitcnt lgkmcnt(0)\n\t"
    "s_setprio 0\n\t"
    "s_barrier\n\t"
    "ds_read_b64_tr_b16 v[36:37], v11 offset:34816\n\t"
    "ds_read_b64_tr_b16 v[38:39], v11 offset:34880\n\t"
    "ds_read_b64_tr_b16 v[40:41], v11 offset:35840\n\t"
    "ds_read_b64_tr_b16 v[42:43], v11 offset:35904\n\t"
    "s_waitcnt lgkmcnt(0)\n\t"
    "v_min_u32 v36, -2, v36\n\t"
    "v_min_u32 v37, -2, v37\n\t"
    "v_min_u32 v38, -2, v38\n\t"
    "v_min_u32 v39, -2, v39\n\t"
    "v_min_u32 v40, -2, v40\n\t"
    "v_min_u32 v41, -2, v41\n\t"
    "v_min_u32 v42, -2, v42\n\t"
    "v_min_u32 v43, -2, v43\n\t"
    "global_store_dwordx4 v6, v[36:39], %[vs] sc1\n\t"
    "global_store_dwordx4 v6, v[40:43], %[vs] offset:1024 sc1\n\t"
    "s_nop 1\n\t"
    "s_barrier\n\t"
    "s_waitcnt lgkmcnt(0)\n\t"
    "s_branch 9f\n\t"
    "1:\n\t"
    "s_add_u32 %[sp], %[sp], 1\n\t"
    "s_cmp_ge_u32 %[sp], %[cap]\n\t"
    "s_cbranch_scc1 7f\n\t"
    "global_load_dwordx4 v[36:39], v6, %[ag] sc1\n\t"
    "global_load_dwordx4 v[40:43], v6, %[ag] offset:1024 sc1\n\t"
    "s_waitcnt lgkmcnt(0)\n\t"
    "s_memtime %[tc]\n\t"
    "s_nop 7\n\t"
    "s_branch 3b\n\t"
    "7:\n\t"
    "s_mov_b32 %[cap], 0\n\t"
    "s_branch 2b\n\t"
    "9:"
    : [tc] "=&s"(tc), [sp] "=&s"(sp), [cap] "+s"(cap)
    : [ag] "s"(ag), [c44] "s"(0x4444u), [vs] "s"(vs), "{v[64:95]}"(W.w0), "{v[96:127]}"(W.w1), "{v[128:159]}"(W.w2), "{v[160:191]}"(W.w3), "{v[192:223]}"(W.w4), "{v[224:255]}"(W.w5), "{v[56:63]}"(W.x)
    : "v6", "v7", "v8", "v9", "v10", "v11", "v12", "v13", "v14", "v15", "v16", "v17", "v18", "v19", "v20", "v21", "v22", "v23", "v24", "v25", "v26", "v27", "v28", "v29", "v30", "v31", "v32", "v33", "v34", "v35", "v36", "v37", "v38", "v39", "v40", "v41", "v42", "v43", "v44", "v45", "v46", "v47", "v48", "v49", "v50", "v51", "v52", "v53", "v54", "v55", "vcc", "scc", "memory");
}
template <> __device__ __forceinline__ void qk_step<6>(const QKW& W, const char* ag, const char* vs, const char* od, const char* kv, unsigned& cap, unsigned& sp,
                                                        unsigned long long& tc, unsigned long long* ts, unsigned& d0, unsigned& d1) {
  unsigned st, bg;
  asm volatile("v_mbcnt_lo_u32_b32 v52, -1, 0\n\t"
    "v_mbcnt_hi_u32_b32 v52, -1, v52\n\t"
    "v_lshlrev_b32 v6, 4, v52\n\t"
    "s_mov_b32 %[sp], 0\n\t"
    "global_load_dwordx4 v[36:39], v6, %[ag] sc1\n\t"
    "global_load_dwordx4 v[40:43], v6, %[ag] offset:1024 sc1\n\t"
    "s_memtime %[tc]\n\t"
    "v_add_u32 v7, 77824, v6\n\t"
    "v_add_u32 v8, 135168, v6\n\t"
    "ds_read_b128 v[20:23], v7 offset:8192\n\t"
    "ds_read_b128 v[24:27], v7 offset:9216\n\t"
    "ds_read_b128 v[28:31], v7 offset:0\n\t"
    "ds_read_b128 v[32:35], v7 offset:1024\n\t"
    "v_lshrrev_b32 v44, 4, v52\n\t"
    "v_and_b32 v45, 15, v52\n\t"
    "v_and_b32 v53, 4, v52\n\t"
    "v_mul_u32_u24 v53, 0x2aaa, v53\n\t"
    "v_lshrrev_b32 v9, 1, v52\n\t"
    "v_and_b32 v9, 2, v9\n\t"
    "v_or3_b32 v9, v9, v53, %[c44]\n\t"
    "v_lshlrev_b32 v53, 2, v44\n\t"
    "v_lshl_or_b32 v10, v45, 4, v53\n\t"
    "3:\n\t"
    "v_mov_b32 v12, 0\n\t"
    "v_mov_b32 v13, 0\n\t"
    "v_mov_b32 v14, 0\n\t"
    "v_mov_b32 v15, 0\n\t"
    "v_mov_b32 v16, 0\n\t"
    "v_mov_b32 v17, 0\n\t"
    "v_mov_b32 v18, 0\n\t"
    "v_mov_b32 v19, 0\n\t"
    "s_waitcnt vmcnt(0) lgkmcnt(0)\n\t"
    "14:\n\t"
    "ds_write_b128 v6, v[36:39] offset:12288\n\t"
    "ds_write_b128 v6, v[40:43] offset:13312\n\t"
    "v_smfmac_f32_16x16x64_bf16 v[12:15], v[36:39], v[192:199], v9\n\t"
    "v_max3_u32 v52, v36, v37, v38\n\t"
    "v_max3_u32 v53, v39, v40, v41\n\t"
    "v_smfmac_f32_16x16x64_bf16 v[16:19], v[36:39], v[200:207], v9\n\t"
    "v_max3_u32 v52, v52, v42, v43\n\t"
    "v_max_u32 v52, v52, v53\n\t"
    "v_cmp_eq_u32 vcc, -1, v52\n\t"
    "s_cbranch_vccnz 1f\n\t"
    "2:\n\t"
    "s_waitcnt lgkmcnt(0)\n\t"
    "s_barrier\n\t"
    "ds_read_b128 v[36:39], v6 offset:0\n\t"
    "ds_read_b128 v[44:47], v6 offset:4096\n\t"
    "v_smfmac_f32_16x16x64_bf16 v[12:15], v[40:43], v[208:215], v9\n\t"
    "ds_read_b128 v[48:51], v6 offset:5120\n\t"
    "ds_read_b128 v[52:55], v6 offset:6144\n\t"
    "v_smfmac_f32_16x16x64_bf16 v[16:19], v[40:43], v[216:223], v9\n\t"
    "ds_read_b128 v[40:43], v6 offset:1024\n\t"
    "s_sleep 0\n\t"
    "s_waitcnt lgkmcnt(4)\n\t"
    "v_smfmac_f32_16x16x64_bf16 v[12:15], v[36:39], v[56:63], v9\n\t"
    "v_smfmac_f32_16x16x64_bf16 v[16:19], v[36:39], v[28:35], v9\n\t"
    "ds_read_b128 v[28:31], v7 offset:16384\n\t"
    "ds_read_b128 v[32:35], v7 offset:17408\n\t"
    "ds_read_b128 v[36:39], v6 offset:7168\n\t"
    "s_waitcnt lgkmcnt(6)\n\t"
    "v_smfmac_f32_16x16x64_bf16 v[12:15], v[44:47], v[64:71], v9\n\t"
    "v_smfmac_f32_16x16x64_bf16 v[16:19], v[44:47], v[72:79], v9\n\t"
    "ds_read_b128 v[44:47], v6 offset:8192\n\t"
    "s_waitcnt lgkmcnt(6)\n\t"
    "v_smfmac_f32_16x16x64_bf16 v[12:15], v[48:51], v[80:87], v9\n\t"
    "v_smfmac_f32_16x16x64_bf16 v[16:19], v[48:51], v[88:95], v9\n\t"
    "ds_read_b128 v[48:51], v6 offset:9216\n\t"
    "s_waitcnt lgkmcnt(6)\n\t"
    "v_smfmac_f32_16x16x64_bf16 v[12:15], v[52:55], v[96:103], v9\n\t"
    "v_smfmac_f32_16x16x64_bf16 v[16:19], v[52:55], v[104:111], v9\n\t"
    "ds_read_b128 v[52:55], v6 offset:2048\n\t"
    "s_waitcnt lgkmcnt(6)\n\t"
    "v_smfmac_f32_16x16x64_bf16 v[12:15], v[40:43], v[20:27], v9\n\t"
    "ds_read_b128 v[20:23], v8 offset:0\n\t"
    "ds_read_b128 v[24:27], v8 offset:1024\n\t"
    "s_waitcnt lgkmcnt(6)\n\t"
    "v_smfmac_f32_16x16x64_bf16 v[16:19], v[40:43], v[28:35], v9\n\t"
    "ds_read_b128 v[28:31], v8 offset:8192\n\t"
    "ds_read_b128 v[32:35], v8 offset:9216\n\t"
    "ds_read_b128 v[40:43], v6 offset:10240\n\t"
    "s_waitcnt lgkmcnt(8)\n\t"
    "v_smfmac_f32_16x16x64_bf16 v[12:15], v[36:39], v[112:119], v9\n\t"
    "v_smfmac_f32_16x16x64_bf16 v[16:19], v[36:39], v[120:127], v9\n\t"
    "ds_read_b128 v[36:39], v6 offset:11264\n\t"
    "s_waitcnt lgkmcnt(8)\n\t"
    "v_smfmac_f32_16x16x64_bf16 v[12:15], v[44:47], v[128:135], v9\n\t"
    "v_smfmac_f32_16x16x64_bf16 v[16:19], v[44:47], v[136:143], v9\n\t"
    "ds_read_b128 v[44:47], v6 offset:14336\n\t"
    "s_barrier\n\t"
    "s_setprio 2\n\t"
    "s_waitcnt lgkmcnt(8)\n\t"
    "v_smfmac_f32_16x16x64_bf16 v[12:15], v[48:51], v[144:151], v9\n\t"
    "v_smfmac_f32_16x16x64_bf16 v[16:19], v[48:51], v[152:159], v9\n\t"
    "ds_read_b128 v[48:51], v6 offset:3072\n\t"
    "s_waitcnt lgkmcnt(6)\n\t"
    "v_smfmac_f32_16x16x64_bf16 v[12:15], v[52:55], v[20:27], v9\n\t"
    "ds_read_b128 v[20:23], v8 offset:16384\n\t"
    "ds_read_b128 v[24:27], v8 offset:17408\n\t"
    "s_waitcnt lgkmcnt(6)\n\t"
    "v_smfmac_f32_16x16x64_bf16 v[16:19], v[52:55], v[28:35], v9\n\t"
    "ds_read_b128 v[28:31], v8 offset:24576\n\t"
    "ds_read_b128 v[32:35], v8 offset:25600\n\t"
    "ds_read_b128 v[52:55], v6 offset:15360\n\t"
    "s_waitcnt lgkmcnt(8)\n\t"
    "v_smfmac_f32_16x16x64_bf16 v[12:15], v[40:43], v[160:167], v9\n\t"
    "v_smfmac_f32_16x16x64_bf16 v[16:19], v[40:43], v[168:175], v9\n\t"
    "s_waitcnt lgkmcnt(7)\n\t"
    "v_smfmac_f32_16x16x64_bf16 v[12:15], v[36:39], v[176:183], v9\n\t"
    "v_smfmac_f32_16x16x64_bf16 v[16:19], v[36:39], v[184:191], v9\n\t"
    "s_waitcnt lgkmcnt(6)\n\t"
    "v_smfmac_f32_16x16x64_bf16 v[12:15], v[44:47], v[224:231], v9\n\t"
    "v_smfmac_f32_16x16x64_bf16 v[16:19], v[44:47], v[232:239], v9\n\t"
    "s_waitcnt lgkmcnt(3)\n\t"
    "v_smfmac_f32_16x16x64_bf16 v[12:15], v[48:51], v[20:27], v9\n\t"
    "s_waitcnt lgkmcnt(1)\n\t"
    "v_smfmac_f32_16x16x64_bf16 v[16:19], v[48:51], v[28:35], v9\n\t"
    "s_waitcnt lgkmcnt(0)\n\t"
    "v_smfmac_f32_16x16x64_bf16 v[12:15], v[52:55], v[240:247], v9\n\t"
    "v_smfmac_f32_16x16x64_bf16 v[16:19], v[52:55], v[248:255], v9\n\t"
    "s_nop 6\n\t"
    "v_permlane16_swap_b32 v12, v14\n\t"
    "v_permlane16_swap_b32 v13, v15\n\t"
    "v_permlane16_swap_b32 v16, v18\n\t"
    "v_permlane16_swap_b32 v17, v19\n\t"
    "v_add_f32 v12, v12, v14\n\t"
    "v_add_f32 v13, v13, v15\n\t"
    "v_add_f32 v16, v16, v18\n\t"
    "v_cvt_pk_bf16_f32 v12, v12, v13\n\t"
    "v_add_f32 v17, v17, v19\n\t"
    "ds_write_b32 v10, v12 offset:35840\n\t"
    "v_cvt_pk_bf16_f32 v16, v16, v17\n\t"
    "ds_write_b32 v10, v16 offset:36096\n\t"
    "s_waitcnt lgkmcnt(0)\n\t"
    "s_setprio 0\n\t"
    "s_barrier\n\t"
    "s_barrier\n\t"
    "s_waitcnt lgkmcnt(0)\n\t"
    "s_branch 9f\n\t"
    "1:\n\t"
    "s_add_u32 %[sp], %[sp], 1\n\t"
    "s_cmp_ge_u32 %[sp], %[cap]\n\t"
    "s_cbranch_scc1 7f\n\t"
    "global_load_dwordx4 v[36:39], v6, %[ag] sc1\n\t"
    "global_load_dwordx4 v[40:43], v6, %[ag] offset:1024 sc1\n\t"
    "s_waitcnt lgkmcnt(0)\n\t"
    "s_memtime %[tc]\n\t"
    "s_nop 7\n\t"
    "s_branch 3b\n\t"
    "7:\n\t"
    "s_mov_b32 %[cap], 0\n\t"
    "s_branch 2b\n\t"
    "9:"
    : [tc] "=&s"(tc), [sp] "=&s"(sp), [cap] "+s"(cap)
    : [ag] "s"(ag), [c44] "s"(0x4444u), "{v[64:95]}"(W.w0), "{v[96:127]}"(W.w1), "{v[128:159]}"(W.w2), "{v[160:191]}"(W.w3), "{v[192:223]}"(W.w4), "{v[224:255]}"(W.w5), "{v[56:63]}"(W.x)
    : "v6", "v7", "v8", "v9", "v10", "v11", "v12", "v13", "v14", "v15", "v16", "v17", "v18", "v19", "v20", "v21", "v22", "v23", "v24", "v25", "v26", "v27", "v28", "v29", "v30", "v31", "v32", "v33", "v34", "v35", "v36", "v37", "v38", "v39", "v40", "v41", "v42", "v43", "v44", "v45", "v46", "v47", "v48", "v49", "v50", "v51", "v52", "v53", "v54", "v55", "vcc", "scc", "memory");
}
template <> __device__ __forceinline__ void qk_step<7>(const QKW& W, const char* ag, const char* vs, const char* od, const char* kv, unsigned& cap, unsigned& sp,
                                                        unsigned long long& tc, unsigned long long* ts, unsigned& d0, unsigned& d1) {
  unsigned st, bg;
  asm volatile("v_mbcnt_lo_u32_b32 v52, -1, 0\n\t"
    "v_mbcnt_hi_u32_b32 v52, -1, v52\n\t"
    "v_lshlrev_b32 v6, 4, v52\n\t"
    "s_mov_b32 %[sp], 0\n\t"
    "global_load_dwordx4 v[36:39], v6, %[ag] sc1\n\t"
    "global_load_dwordx4 v[40:43], v6, %[ag] offset:1024 sc1\n\t"
    "s_memtime %[tc]\n\t"
    "v_add_u32 v7, 79872, v6\n\t"
    "v_add_u32 v8, 137216, v6\n\t"
    "ds_read_b128 v[20:23], v7 offset:8192\n\t"
    "ds_read_b128 v[24:27], v7 offset:9216\n\t"
    "ds_read_b128 v[28:31], v7 offset:0\n\t"
    "ds_read_b128 v[32:35], v7 offset:1024\n\t"
    "v_lshrrev_b32 v44, 4, v52\n\t"
    "v_and_b32 v45, 15, v52\n\t"
    "v_and_b32 v53, 4, v52\n\t"
    "v_mul_u32_u24 v53, 0x2aaa, v53\n\t"
    "v_lshrrev_b32 v9, 1, v52\n\t"
    "v_and_b32 v9, 2, v9\n\t"
    "v_or3_b32 v9, v9, v53, %[c44]\n\t"
    "v_lshlrev_b32 v53, 2, v44\n\t"
    "v_lshl_or_b32 v10, v45, 4, v53\n\t"
    "3:\n\t"
    "v_mov_b32 v12, 0\n\t"
    "v_mov_b32 v13, 0\n\t"
    "v_mov_b32 v14, 0\n\t"
    "v_mov_b32 v15, 0\n\t"
    "v_mov_b32 v16, 0\n\t"
    "v_mov_b32 v17, 0\n\t"
    "v_mov_b32 v18, 0\n\t"
    "v_mov_b32 v19, 0\n\t"
    "s_waitcnt vmcnt(0) lgkmcnt(0)\n\t"
    "14:\n\t"
    "ds_write_b128 v6, v[36:39] offset:14336\n\t"
    "ds_write_b128 v6, v[40:43] offset:15360\n\t"
    "v_smfmac_f32_16x16x64_bf16 v[12:15], v[36:39], v[224:231], v9\n\t"
    "v_max3_u32 v52, v36, v37, v38\n\t"
    "v_max3_u32 v53, v39, v40, v41\n\t"
    "v_smfmac_f32_16x16x64_bf16 v[16:19], v[36:39], v[232:239], v9\n\t"
    "v_max3_u32 v52, v52, v42, v43\n\t"
    "v_max_u32 v52, v52, v53\n\t"
    "v_cmp_eq_u32 vcc, -1, v52\n\t"
    "s_cbranch_vccnz 1f\n\t"
    "2:\n\t"
    "s_waitcnt lgkmcnt(0)\n\t"
    "s_barrier\n\t"
    "ds_read_b128 v[36:39], v6 offset:0\n\t"
    "ds_read_b128 v[44:47], v6 offset:4096\n\t"
    "v_smfmac_f32_16x16x64_bf16 v[12:15], v[40:43], v[240:247], v9\n\t"
    "ds_read_b128 v[48:51], v6 offset:5120\n\t"
    "ds_read_b128 v[52:55], v6 offset:6144\n\t"
    "v_smfmac_f32_16x16x64_bf16 v[16:19], v[40:43], v[248:255], v9\n\t"
    "ds_read_b128 v[40:43], v6 offset:1024\n\t"
    "s_sleep 0\n\t"
    "s_waitcnt lgkmcnt(4)\n\t"
    "v_smfmac_f32_16x16x64_bf16 v[12:15], v[36:39], v[56:63], v9\n\t"
    "v_smfmac_f32_16x16x64_bf16 v[16:19], v[36:39], v[28:35], v9\n\t"
    "ds_read_b128 v[28:31], v7 offset:16384\n\t"
    "ds_read_b128 v[32:35], v7 offset:17408\n\t"
    "ds_read_b128 v[36:39], v6 offset:7168\n\t"
    "s_waitcnt lgkmcnt(6)\n\t"
    "v_smfmac_f32_16x16x64_bf16 v[12:15], v[44:47], v[64:71], v9\n\t"
    "v_smfmac_f32_16x16x64_bf16 v[16:19], v[44:47], v[72:79], v9\n\t"
    "ds_read_b128 v[44:47], v6 offset:8192\n\t"
    "s_waitcnt lgkmcnt(6)\n\t"
    "v_smfmac_f32_16x16x64_bf16 v[12:15], v[48:51], v[80:87], v9\n\t"
    "v_smfmac_f32_16x16x64_bf16 v[16:19], v[48:51], v[88:95], v9\n\t"
    "ds_read_b128 v[48:51], v6 offset:9216\n\t"
    "s_waitcnt lgkmcnt(6)\n\t"
    "v_smfmac_f32_16x16x64_bf16 v[12:15], v[52:55], v[96:103], v9\n\t"
    "v_smfmac_f32_16x16x64_bf16 v[16:19], v[52:55], v[104:111], v9\n\t"
    "ds_read_b128 v[52:55], v6 offset:2048\n\t"
    "s_waitcnt lgkmcnt(6)\n\t"
    "v_smfmac_f32_16x16x64_bf16 v[12:15], v[40:43], v[20:27], v9\n\t"
    "ds_read_b128 v[20:23], v8 offset:0\n\t"
    "ds_read_b128 v[24:27], v8 offset:1024\n\t"
    "s_waitcnt lgkmcnt(6)\n\t"
    "v_smfmac_f32_16x16x64_bf16 v[16:19], v[40:43], v[28:35], v9\n\t"
    "ds_read_b128 v[28:31], v8 offset:8192\n\t"
    "ds_read_b128 v[32:35], v8 offset:9216\n\t"
    "ds_read_b128 v[40:43], v6 offset:10240\n\t"
    "s_waitcnt lgkmcnt(8)\n\t"
    "v_smfmac_f32_16x16x64_bf16 v[12:15], v[36:39], v[112:119], v9\n\t"
    "v_smfmac_f32_16x16x64_bf16 v[16:19], v[36:39], v[120:127], v9\n\t"
    "ds_read_b128 v[36:39], v6 offset:11264\n\t"
    "s_waitcnt lgkmcnt(8)\n\t"
    "v_smfmac_f32_16x16x64_bf16 v[12:15], v[44:47], v[128:135], v9\n\t"
    "v_smfmac_f32_16x16x64_bf16 v[16:19], v[44:47], v[136:143], v9\n\t"
    "ds_read_b128 v[44:47], v6 offset:12288\n\t"
    "s_barrier\n\t"
    "s_setprio 2\n\t"
    "s_waitcnt lgkmcnt(8)\n\t"
    "v_smfmac_f32_16x16x64_bf16 v[12:15], v[48:51], v[144:151], v9\n\t"
    "v_smfmac_f32_16x16x64_bf16 v[16:19], v[48:51], v[152:159], v9\n\t"
    "ds_read_b128 v[48:51], v6 offset:3072\n\t"
    "s_waitcnt lgkmcnt(6)\n\t"
    "v_smfmac_f32_16x16x64_bf16 v[12:15], v[52:55], v[20:27], v9\n\t"
    "ds_read_b128 v[20:23], v8 offset:16384\n\t"
    "ds_read_b128 v[24:27], v8 offset:17408\n\t"
    "s_waitcnt lgkmcnt(6)\n\t"
    "v_smfmac_f32_16x16x64_bf16 v[16:19], v[52:55], v[28:35], v9\n\t"
    "ds_read_b128 v[28:31], v8 offset:24576\n\t"
    "ds_read_b128 v[32:35], v8 offset:25600\n\t"
    "ds_read_b128 v[52:55], v6 offset:13312\n\t"
    "s_waitcnt lgkmcnt(8)\n\t"
    "v_smfmac_f32_16x16x64_bf16 v[12:15], v[40:43], v[160:167], v9\n\t"
    "v_smfmac_f32_16x16x64_bf16 v[16:19], v[40:43], v[168:175], v9\n\t"
    "s_waitcnt lgkmcnt(7)\n\t"
    "v_smfmac_f32_16x16x64_bf16 v[12:15], v[36:39], v[176:183], v9\n\t"
    "v_smfmac_f32_16x16x64_bf16 v[16:19], v[36:39], v[184:191], v9\n\t"
    "s_waitcnt lgkmcnt(6)\n\t"
    "v_smfmac_f32_16x16x64_bf16 v[12:15], v[44:47], v[192:199], v9\n\t"
    "v_smfmac_f32_16x16x64_bf16 v[16:19], v[44:47], v[200:207], v9\n\t"
    "s_waitcnt lgkmcnt(3)\n\t"
    "v_smfmac_f32_16x16x64_bf16 v[12:15], v[48:51], v[20:27], v9\n\t"
    "s_waitcnt lgkmcnt(1)\n\t"
    "v_smfmac_f32_16x16x64_bf16 v[16:19], v[48:51], v[28:35], v9\n\t"
    "s_waitcnt lgkmcnt(0)\n\t"
    "v_smfmac_f32_16x16x64_bf16 v[12:15], v[52:55], v[208:215], v9\n\t"
    "v_smfmac_f32_16x16x64_bf16 v[16:19], v[52:55], v[216:223], v9\n\t"
    "s_nop 6\n\t"
    "v_permlane16_swap_b32 v12, v14\n\t"
    "v_permlane16_swap_b32 v13, v15\n\t"
    "v_permlane16_swap_b32 v16, v18\n\t"
    "v_permlane16_swap_b32 v17, v19\n\t"
    "v_add_f32 v12, v12, v14\n\t"
    "v_add_f32 v13, v13, v15\n\t"
    "v_add_f32 v16, v16, v18\n\t"
    "v_cvt_pk_bf16_f32 v12, v12, v13\n\t"
    "v_add_f32 v17, v17, v19\n\t"
    "ds_write_b32 v10, v12 offset:36352\n\t"
    "v_cvt_pk_bf16_f32 v16, v16, v17\n\t"
    "ds_write_b32 v10, v16 offset:36608\n\t"
    "s_waitcnt lgkmcnt(0)\n\t"
    "s_setprio 0\n\t"
    "s_barrier\n\t"
    "s_barrier\n\t"
    "s_waitcnt lgkmcnt(0)\n\t"
    "s_branch 9f\n\t"
    "1:\n\t"
    "s_add_u32 %[sp], %[sp], 1\n\t"
    "s_cmp_ge_u32 %[sp], %[cap]\n\t"
    "s_cbranch_scc1 7f\n\t"
    "global_load_dwordx4 v[36:39], v6, %[ag] sc1\n\t"
    "global_load_dwordx4 v[40:43], v6, %[ag] offset:1024 sc1\n\t"
    "s_waitcnt lgkmcnt(0)\n\t"
    "s_memtime %[tc]\n\t"
    "s_nop 7\n\t"
    "s_branch 3b\n\t"
    "7:\n\t"
    "s_mov_b32 %[cap], 0\n\t"
    "s_branch 2b\n\t"
    "9:"
    : [tc] "=&s"(tc), [sp] "=&s"(sp), [cap] "+s"(cap)
    : [ag] "s"(ag), [c44] "s"(0x4444u), "{v[64:95]}"(W.w0), "{v[96:127]}"(W.w1), "{v[128:159]}"(W.w2), "{v[160:191]}"(W.w3), "{v[192:223]}"(W.w4), "{v[224:255]}"(W.w5), "{v[56:63]}"(W.x)
    : "v6", "v7", "v8", "v9", "v10", "v11", "v12", "v13", "v14", "v15", "v16", "v17", "v18", "v19", "v20", "v21", "v22", "v23", "v24", "v25", "v26", "v27", "v28", "v29", "v30", "v31", "v32", "v33", "v34", "v35", "v36", "v37", "v38", "v39", "v40", "v41", "v42", "v43", "v44", "v45", "v46", "v47", "v48", "v49", "v50", "v51", "v52", "v53", "v54", "v55", "vcc", "scc", "memory");
}
template <int I> __device__ __forceinline__ void qk_wload(u32x32& w, unsigned a0, unsigned a1);
template <> __device__ __forceinline__ void qk_wload<0>(u32x32& w, unsigned a0, unsigned a1) {
  asm volatile("ds_read_b64_tr_b16 v[64:65], %1 offset:0\n\t"
    "ds_read_b64_tr_b16 v[66:67], %1 offset:512\n\t"
    "ds_read_b64_tr_b16 v[68:69], %1 offset:4096\n\t"
    "ds_read_b64_tr_b16 v[70:71], %1 offset:4608\n\t"
    "ds_read_b64_tr_b16 v[72:73], %2 offset:0\n\t"
    "ds_read_b64_tr_b16 v[74:75], %2 offset:512\n\t"
    "ds_read_b64_tr_b16 v[76:77], %2 offset:4096\n\t"
    "ds_read_b64_tr_b16 v[78:79], %2 offset:4608\n\t"
    "s_waitcnt lgkmcnt(0)\n\t"
    "ds_read_b64_tr_b16 v[80:81], %1 offset:16384\n\t"
    "ds_read_b64_tr_b16 v[82:83], %1 offset:16896\n\t"
    "ds_read_b64_tr_b16 v[84:85], %1 offset:20480\n\t"
    "ds_read_b64_tr_b16 v[86:87], %1 offset:20992\n\t"
    "ds_read_b64_tr_b16 v[88:89], %2 offset:16384\n\t"
    "ds_read_b64_tr_b16 v[90:91], %2 offset:16896\n\t"
    "ds_read_b64_tr_b16 v[92:93], %2 offset:20480\n\t"
    "ds_read_b64_tr_b16 v[94:95], %2 offset:20992\n\t"
    "s_waitcnt lgkmcnt(0)"
    : "=&{v[64:95]}"(w) : "v"(a0), "v"(a1) : "memory");
}
template <> __device__ __forceinline__ void qk_wload<1>(u32x32& w, unsigned a0, unsigned a1) {
  asm volatile("ds_read_b64_tr_b16 v[96:97], %1 offset:0\n\t"
    "ds_read_b64_tr_b16 v[98:99], %1 offset:512\n\t"
    "ds_read_b64_tr_b16 v[100:101], %1 offset:4096\n\t"
    "ds_read_b64_tr_b16 v[102:103], %1 offset:4608\n\t"
    "ds_read_b64_tr_b16 v[104:105], %2 offset:0\n\t"
    "ds_read_b64_tr_b16 v[106:107], %2 offset:512\n\t"
    "ds_read_b64_tr_b16 v[108:109], %2 offset:4096\n\t"
    "ds_read_b64_tr_b16 v[110:111], %2 offset:4608\n\t"
    "s_waitcnt lgkmcnt(0)\n\t"
    "ds_read_b64_tr_b16 v[112:113], %1 offset:16384\n\t"
    "ds_read_b64_tr_b16 v[114:115], %1 offset:16896\n\t"
    "ds_read_b64_tr_b16 v[116:117], %1 offset:20480\n\t"
    "ds_read_b64_tr_b16 v[118:119], %1 offset:20992\n\t"
    "ds_read_b64_tr_b16 v[120:121], %2 offset:16384\n\t"
    "ds_read_b64_tr_b16 v[122:123], %2 offset:16896\n\t"
    "ds_read_b64_tr_b16 v[124:125], %2 offset:20480\n\t"
    "ds_read_b64_tr_b16 v[126:127], %2 offset:20992\n\t"
    "s_waitcnt lgkmcnt(0)"
    : "=&{v[96:127]}"(w) : "v"(a0), "v"(a1) : "memory");
}
template <> __device__ __forceinline__ void qk_wload<2>(u32x32& w, unsigned a0, unsigned a1) {
  asm volatile("ds_read_b64_tr_b16 v[128:129], %1 offset:0\n\t"
    "ds_read_b64_tr_b16 v[130:131], %1 offset:512\n\t"
    "ds_read_b64_tr_b16 v[132:133], %1 offset:4096\n\t"
    "ds_read_b64_tr_b16 v[134:135], %1 offset:4608\n\t"
    "ds_read_b64_tr_b16 v[136:137], %2 offset:0\n\t"
    "ds_read_b64_tr_b16 v[138:139], %2 offset:512\n\t"
    "ds_read_b64_tr_b16 v[140:141], %2 offset:4096\n\t"
    "ds_read_b64_tr_b16 v[142:143], %2 offset:4608\n\t"
    "s_waitcnt lgkmcnt(0)\n\t"
    "ds_read_b64_tr_b16 v[144:145], %1 offset:16384\n\t"
    "ds_read_b64_tr_b16 v[146:147], %1 offset:16896\n\t"
    "ds_read_b64_tr_b16 v[148:149], %1 offset:20480\n\t"
    "ds_read_b64_tr_b16 v[150:151], %1 offset:20992\n\t"
    "ds_read_b64_tr_b16 v[152:153], %2 offset:16384\n\t"
    "ds_read_b64_tr_b16 v[154:155], %2 offset:16896\n\t"
    "ds_read_b64_tr_b16 v[156:157], %2 offset:20480\n\t"
    "ds_read_b64_tr_b16 v[158:159], %2 offset:20992\n\t"
    "s_waitcnt lgkmcnt(0)"
    : "=&{v[128:159]}"(w) : "v"(a0), "v"(a1) : "memory");
}
template <> __device__ __forceinline__ void qk_wload<3>(u32x32& w, unsigned a0, unsigned a1) {
  asm volatile("ds_read_b64_tr_b16 v[160:161], %1 offset:0\n\t"
    "ds_read_b64_tr_b16 v[162:163], %1 offset:512\n\t"
    "ds_read_b64_tr_b16 v[164:165], %1 offset:4096\n\t"
    "ds_read_b64_tr_b16 v[166:167], %1 offset:4608\n\t"
    "ds_read_b64_tr_b16 v[168:169], %2 offset:0\n\t"
    "ds_read_b64_tr_b16 v[170:171], %2 offset:512\n\t"
    "ds_read_b64_tr_b16 v[172:173], %2 offset:4096\n\t"
    "ds_read_b64_tr_b16 v[174:175], %2 offset:4608\n\t"
    "s_waitcnt lgkmcnt(0)\n\t"
    "ds_read_b64_tr_b16 v[176:177], %1 offset:16384\n\t"
    "ds_read_b64_tr_b16 v[178:179], %1 offset:16896\n\t"
    "ds_read_b64_tr_b16 v[180:181], %1 offset:20480\n\t"
    "ds_read_b64_tr_b16 v[182:183], %1 offset:20992\n\t"
    "ds_read_b64_tr_b16 v[184:185], %2 offset:16384\n\t"
    "ds_read_b64_tr_b16 v[186:187], %2 offset:16896\n\t"
    "ds_read_b64_tr_b16 v[188:189], %2 offset:20480\n\t"
    "ds_read_b64_tr_b16 v[190:191], %2 offset:20992\n\t"
    "s_waitcnt lgkmcnt(0)"
    : "=&{v[160:191]}"(w) : "v"(a0), "v"(a1) : "memory");
}
template <> __device__ __forceinline__ void qk_wload<4>(u32x32& w, unsigned a0, unsigned a1) {
  asm volatile("ds_read_b64_tr_b16 v[192:193], %1 offset:0\n\t"
    "ds_read_b64_tr_b16 v[194:195], %1 offset:512\n\t"
    "ds_read_b64_tr_b16 v[196:197], %1 offset:4096\n\t"
    "ds_read_b64_tr_b16 v[198:199], %1 offset:4608\n\t"
    "ds_read_b64_tr_b16 v[200:201], %2 offset:0\n\t"
    "ds_read_b64_tr_b16 v[202:203], %2 offset:512\n\t"
    "ds_read_b64_tr_b16 v[204:205], %2 offset:4096\n\t"
    "ds_read_b64_tr_b16 v[206:207], %2 offset:4608\n\t"
    "s_waitcnt lgkmcnt(0)\n\t"
    "ds_read_b64_tr_b16 v[208:209], %1 offset:16384\n\t"
    "ds_read_b64_tr_b16 v[210:211], %1 offset:16896\n\t"
    "ds_read_b64_tr_b16 v[212:213], %1 offset:20480\n\t"
    "ds_read_b64_tr_b16 v[214:215], %1 offset:20992\n\t"
    "ds_read_b64_tr_b16 v[216:217], %2 offset:16384\n\t"
    "ds_read_b64_tr_b16 v[218:219], %2 offset:16896\n\t"
    "ds_read_b64_tr_b16 v[220:221], %2 offset:20480\n\t"
    "ds_read_b64_tr_b16 v[222:223], %2 offset:20992\n\t"
    "s_waitcnt lgkmcnt(0)"
    : "=&{v[192:223]}"(w) : "v"(a0), "v"(a1) : "memory");
}
template <> __device__ __forceinline__ void qk_wload<5>(u32x32& w, unsigned a0, unsigned a1) {
  asm volatile("ds_read_b64_tr_b16 v[224:225], %1 offset:0\n\t"
    "ds_read_b64_tr_b16 v[226:227], %1 offset:512\n\t"
    "ds_read_b64_tr_b16 v[228:229], %1 offset:4096\n\t"
    "ds_read_b64_tr_b16 v[230:231], %1 offset:4608\n\t"
    "ds_read_b64_tr_b16 v[232:233], %2 offset:0\n\t"
    "ds_read_b64_tr_b16 v[234:235], %2 offset:512\n\t"
    "ds_read_b64_tr_b16 v[236:237], %2 offset:4096\n\t"
    "ds_read_b64_tr_b16 v[238:239], %2 offset:4608\n\t"
    "s_waitcnt lgkmcnt(0)\n\t"
    "ds_read_b64_tr_b16 v[240:241], %1 offset:16384\n\t"
    "ds_read_b64_tr_b16 v[242:243], %1 offset:16896\n\t"
    "ds_read_b64_tr_b16 v[244:245], %1 offset:20480\n\t"
    "ds_read_b64_tr_b16 v[246:247], %1 offset:20992\n\t"
    "ds_read_b64_tr_b16 v[248:249], %2 offset:16384\n\t"
    "ds_read_b64_tr_b16 v[250:251], %2 offset:16896\n\t"
    "ds_read_b64_tr_b16 v[252:253], %2 offset:20480\n\t"
    "ds_read_b64_tr_b16 v[254:255], %2 offset:20992\n\t"
    "s_waitcnt lgkmcnt(0)"
    : "=&{v[224:255]}"(w) : "v"(a0), "v"(a1) : "memory");
}
#define QK_OWN 0
template <int I> __device__ __forceinline__ void qk_swap(u32x32& w, unsigned a);
template <> __device__ __forceinline__ void qk_swap<0>(u32x32& w, unsigned a) {
  asm volatile("ds_read_b128 v[8:11], %1 offset:0\n\t"
    "ds_read_b128 v[12:15], %1 offset:1024\n\t"
    "ds_read_b128 v[16:19], %1 offset:8192\n\t"
    "ds_read_b128 v[20:23], %1 offset:9216\n\t"
    "ds_read_b128 v[24:27], %1 offset:16384\n\t"
    "ds_read_b128 v[28:31], %1 offset:17408\n\t"
    "ds_read_b128 v[32:35], %1 offset:24576\n\t"
    "ds_read_b128 v[36:39], %1 offset:25600\n\t"
    "ds_write_b128 %1, v[64:67] offset:0\n\t"
    "ds_write_b128 %1, v[68:71] offset:1024\n\t"
    "ds_write_b128 %1, v[72:75] offset:8192\n\t"
    "ds_write_b128 %1, v[76:79] offset:9216\n\t"
    "ds_write_b128 %1, v[80:83] offset:16384\n\t"
    "ds_write_b128 %1, v[84:87] offset:17408\n\t"
    "ds_write_b128 %1, v[88:91] offset:24576\n\t"
    "ds_write_b128 %1, v[92:95] offset:25600\n\t"
    "s_waitcnt lgkmcnt(0)\n\t"
    "v_mov_b32 v64, v8\n\t"
    "v_mov_b32 v65, v9\n\t"
    "v_mov_b32 v66, v10\n\t"
    "v_mov_b32 v67, v11\n\t"
    "v_mov_b32 v68, v12\n\t"
    "v_mov_b32 v69, v13\n\t"
    "v_mov_b32 v70, v14\n\t"
    "v_mov_b32 v71, v15\n\t"
    "v_mov_b32 v72, v16\n\t"
    "v_mov_b32 v73, v17\n\t"
    "v_mov_b32 v74, v18\n\t"
    "v_mov_b32 v75, v19\n\t"
    "v_mov_b32 v76, v20\n\t"
    "v_mov_b32 v77, v21\n\t"
    "v_mov_b32 v78, v22\n\t"
    "v_mov_b32 v79, v23\n\t"
    "v_mov_b32 v80, v24\n\t"
    "v_mov_b32 v81, v25\n\t"
    "v_mov_b32 v82, v26\n\t"
    "v_mov_b32 v83, v27\n\t"
    "v_mov_b32 v84, v28\n\t"
    "v_mov_b32 v85, v29\n\t"
    "v_mov_b32 v86, v30\n\t"
    "v_mov_b32 v87, v31\n\t"
    "v_mov_b32 v88, v32\n\t"
    "v_mov_b32 v89, v33\n\t"
    "v_mov_b32 v90, v34\n\t"
    "v_mov_b32 v91, v35\n\t"
    "v_mov_b32 v92, v36\n\t"
    "v_mov_b32 v93, v37\n\t"
    "v_mov_b32 v94, v38\n\t"
    "v_mov_b32 v95, v39"
    :: "{v[64:95]}"(w), "v"(a) : "v8", "v9", "v10", "v11", "v12", "v13", "v14", "v15", "v16", "v17", "v18", "v19", "v20", "v21", "v22", "v23", "v24", "v25", "v26", "v27", "v28", "v29", "v30", "v31", "v32", "v33", "v34", "v35", "v36", "v37", "v38", "v39", "memory");
}
template <> __device__ __forceinline__ void qk_swap<1>(u32x32& w, unsigned a) {
  asm volatile("ds_read_b128 v[8:11], %1 offset:0\n\t"
    "ds_read_b128 v[12:15], %1 offset:1024\n\t"
    "ds_read_b128 v[16:19], %1 offset:8192\n\t"
    "ds_read_b128 v[20:23], %1 offset:9216\n\t"
    "ds_read_b128 v[24:27], %1 offset:16384\n\t"
    "ds_read_b128 v[28:31], %1 offset:17408\n\t"
    "ds_read_b128 v[32:35], %1 offset:24576\n\t"
    "ds_read_b128 v[36:39], %1 offset:25600\n\t"
    "ds_write_b128 %1, v[96:99] offset:0\n\t"
    "ds_write_b128 %1, v[100:103] offset:1024\n\t"
    "ds_write_b128 %1, v[104:107] offset:8192\n\t"
    "ds_write_b128 %1, v[108:111] offset:9216\n\t"
    "ds_write_b128 %1, v[112:115] offset:16384\n\t"
    "ds_write_b128 %1, v[116:119] offset:17408\n\t"
    "ds_write_b128 %1, v[120:123] offset:24576\n\t"
    "ds_write_b128 %1, v[124:127] offset:25600\n\t"
    "s_waitcnt lgkmcnt(0)\n\t"
    "v_mov_b32 v96, v8\n\t"
    "v_mov_b32 v97, v9\n\t"
    "v_mov_b32 v98, v10\n\t"
    "v_mov_b32 v99, v11\n\t"
    "v_mov_b32 v100, v12\n\t"
    "v_mov_b32 v101, v13\n\t"
    "v_mov_b32 v102, v14\n\t"
    "v_mov_b32 v103, v15\n\t"
    "v_mov_b32 v104, v16\n\t"
    "v_mov_b32 v105, v17\n\t"
    "v_mov_b32 v106, v18\n\t"
    "v_mov_b32 v107, v19\n\t"
    "v_mov_b32 v108, v20\n\t"
    "v_mov_b32 v109, v21\n\t"
    "v_mov_b32 v110, v22\n\t"
    "v_mov_b32 v111, v23\n\t"
    "v_mov_b32 v112, v24\n\t"
    "v_mov_b32 v113, v25\n\t"
    "v_mov_b32 v114, v26\n\t"
    "v_mov_b32 v115, v27\n\t"
    "v_mov_b32 v116, v28\n\t"
    "v_mov_b32 v117, v29\n\t"
    "v_mov_b32 v118, v30\n\t"
    "v_mov_b32 v119, v31\n\t"
    "v_mov_b32 v120, v32\n\t"
    "v_mov_b32 v121, v33\n\t"
    "v_mov_b32 v122, v34\n\t"
    "v_mov_b32 v123, v35\n\t"
    "v_mov_b32 v124, v36\n\t"
    "v_mov_b32 v125, v37\n\t"
    "v_mov_b32 v126, v38\n\t"
    "v_mov_b32 v127, v39"
    :: "{v[96:127]}"(w), "v"(a) : "v8", "v9", "v10", "v11", "v12", "v13", "v14", "v15", "v16", "v17", "v18", "v19", "v20", "v21", "v22", "v23", "v24", "v25", "v26", "v27", "v28", "v29", "v30", "v31", "v32", "v33", "v34", "v35", "v36", "v37", "v38", "v39", "memory");
}
template <> __device__ __forceinline__ void qk_swap<2>(u32x32& w, unsigned a) {
  asm volatile("ds_read_b128 v[8:11], %1 offset:0\n\t"
    "ds_read_b128 v[12:15], %1 offset:1024\n\t"
    "ds_read_b128 v[16:19], %1 offset:8192\n\t"
    "ds_read_b128 v[20:23], %1 offset:9216\n\t"
    "ds_read_b128 v[24:27], %1 offset:16384\n\t"
    "ds_read_b128 v[28:31], %1 offset:17408\n\t"
    "ds_read_b128 v[32:35], %1 offset:24576\n\t"
    "ds_read_b128 v[36:39], %1 offset:25600\n\t"
    "ds_write_b128 %1, v[128:131] offset:0\n\t"
    "ds_write_b128 %1, v[132:135] offset:1024\n\t"
    "ds_write_b128 %1, v[136:139] offset:8192\n\t"
    "ds_write_b128 %1, v[140:143] offset:9216\n\t"
    "ds_write_b128 %1, v[144:147] offset:16384\n\t"
    "ds_write_b128 %1, v[148:151] offset:17408\n\t"
    "ds_write_b128 %1, v[152:155] offset:24576\n\t"
    "ds_write_b128 %1, v[156:159] offset:25600\n\t"
    "s_waitcnt lgkmcnt(0)\n\t"
    "v_mov_b32 v128, v8\n\t"
    "v_mov_b32 v129, v9\n\t"
    "v_mov_b32 v130, v10\n\t"
    "v_mov_b32 v131, v11\n\t"
    "v_mov_b32 v132, v12\n\t"
    "v_mov_b32 v133, v13\n\t"
    "v_mov_b32 v134, v14\n\t"
    "v_mov_b32 v135, v15\n\t"
    "v_mov_b32 v136, v16\n\t"
    "v_mov_b32 v137, v17\n\t"
    "v_mov_b32 v138, v18\n\t"
    "v_mov_b32 v139, v19\n\t"
    "v_mov_b32 v140, v20\n\t"
    "v_mov_b32 v141, v21\n\t"
    "v_mov_b32 v142, v22\n\t"
    "v_mov_b32 v143, v23\n\t"
    "v_mov_b32 v144, v24\n\t"
    "v_mov_b32 v145, v25\n\t"
    "v_mov_b32 v146, v26\n\t"
    "v_mov_b32 v147, v27\n\t"
    "v_mov_b32 v148, v28\n\t"
    "v_mov_b32 v149, v29\n\t"
    "v_mov_b32 v150, v30\n\t"
    "v_mov_b32 v151, v31\n\t"
    "v_mov_b32 v152, v32\n\t"
    "v_mov_b32 v153, v33\n\t"
    "v_mov_b32 v154, v34\n\t"
    "v_mov_b32 v155, v35\n\t"
    "v_mov_b32 v156, v36\n\t"
    "v_mov_b32 v157, v37\n\t"
    "v_mov_b32 v158, v38\n\t"
    "v_mov_b32 v159, v39"
    :: "{v[128:159]}"(w), "v"(a) : "v8", "v9", "v10", "v11", "v12", "v13", "v14", "v15", "v16", "v17", "v18", "v19", "v20", "v21", "v22", "v23", "v24", "v25", "v26", "v27", "v28", "v29", "v30", "v31", "v32", "v33", "v34", "v35", "v36", "v37", "v38", "v39", "memory");
}
template <> __device__ __forceinline__ void qk_swap<3>(u32x32& w, unsigned a) {
  asm volatile("ds_read_b128 v[8:11], %1 offset:0\n\t"
    "ds_read_b128 v[12:15], %1 offset:1024\n\t"
    "ds_read_b128 v[16:19], %1 offset:8192\n\t"
    "ds_read_b128 v[20:23], %1 offset:9216\n\t"
    "ds_read_b128 v[24:27], %1 offset:16384\n\t"
    "ds_read_b128 v[28:31], %1 offset:17408\n\t"
    "ds_read_b128 v[32:35], %1 offset:24576\n\t"
    "ds_read_b128 v[36:39], %1 offset:25600\n\t"
    "ds_write_b128 %1, v[160:163] offset:0\n\t"
    "ds_write_b128 %1, v[164:167] offset:1024\n\t"
    "ds_write_b128 %1, v[168:171] offset:8192\n\t"
    "ds_write_b128 %1, v[172:175] offset:9216\n\t"
    "ds_write_b128 %1, v[176:179] offset:16384\n\t"
    "ds_write_b128 %1, v[180:183] offset:17408\n\t"
    "ds_write_b128 %1, v[184:187] offset:24576\n\t"
    "ds_write_b128 %1, v[188:191] offset:25600\n\t"
    "s_waitcnt lgkmcnt(0)\n\t"
    "v_mov_b32 v160, v8\n\t"
    "v_mov_b32 v161, v9\n\t"
    "v_mov_b32 v162, v10\n\t"
    "v_mov_b32 v163, v11\n\t"
    "v_mov_b32 v164, v12\n\t"
    "v_mov_b32 v165, v13\n\t"
    "v_mov_b32 v166, v14\n\t"
    "v_mov_b32 v167, v15\n\t"
    "v_mov_b32 v168, v16\n\t"
    "v_mov_b32 v169, v17\n\t"
    "v_mov_b32 v170, v18\n\t"
    "v_mov_b32 v171, v19\n\t"
    "v_mov_b32 v172, v20\n\t"
    "v_mov_b32 v173, v21\n\t"
    "v_mov_b32 v174, v22\n\t"
    "v_mov_b32 v175, v23\n\t"
    "v_mov_b32 v176, v24\n\t"
    "v_mov_b32 v177, v25\n\t"
    "v_mov_b32 v178, v26\n\t"
    "v_mov_b32 v179, v27\n\t"
    "v_mov_b32 v180, v28\n\t"
    "v_mov_b32 v181, v29\n\t"
    "v_mov_b32 v182, v30\n\t"
    "v_mov_b32 v183, v31\n\t"
    "v_mov_b32 v184, v32\n\t"
    "v_mov_b32 v185, v33\n\t"
    "v_mov_b32 v186, v34\n\t"
    "v_mov_b32 v187, v35\n\t"
    "v_mov_b32 v188, v36\n\t"
    "v_mov_b32 v189, v37\n\t"
    "v_mov_b32 v190, v38\n\t"
    "v_mov_b32 v191, v39"
    :: "{v[160:191]}"(w), "v"(a) : "v8", "v9", "v10", "v11", "v12", "v13", "v14", "v15", "v16", "v17", "v18", "v19", "v20", "v21", "v22", "v23", "v24", "v25", "v26", "v27", "v28", "v29", "v30", "v31", "v32", "v33", "v34", "v35", "v36", "v37", "v38", "v39", "memory");
}
template <> __device__ __forceinline__ void qk_swap<4>(u32x32& w, unsigned a) {
  asm volatile("ds_read_b128 v[8:11], %1 offset:0\n\t"
    "ds_read_b128 v[12:15], %1 offset:1024\n\t"
    "ds_read_b128 v[16:19], %1 offset:8192\n\t"
    "ds_read_b128 v[20:23], %1 offset:9216\n\t"
    "ds_read_b128 v[24:27], %1 offset:16384\n\t"
    "ds_read_b128 v[28:31], %1 offset:17408\n\t"
    "ds_read_b128 v[32:35], %1 offset:24576\n\t"
    "ds_read_b128 v[36:39], %1 offset:25600\n\t"
    "ds_write_b128 %1, v[192:195] offset:0\n\t"
    "ds_write_b128 %1, v[196:199] offset:1024\n\t"
    "ds_write_b128 %1, v[200:203] offset:8192\n\t"
    "ds_write_b128 %1, v[204:207] offset:9216\n\t"
    "ds_write_b128 %1, v[208:211] offset:16384\n\t"
    "ds_write_b128 %1, v[212:215] offset:17408\n\t"
    "ds_write_b128 %1, v[216:219] offset:24576\n\t"
    "ds_write_b128 %1, v[220:223] offset:25600\n\t"
    "s_waitcnt lgkmcnt(0)\n\t"
    "v_mov_b32 v192, v8\n\t"
    "v_mov_b32 v193, v9\n\t"
    "v_mov_b32 v194, v10\n\t"
    "v_mov_b32 v195, v11\n\t"
    "v_mov_b32 v196, v12\n\t"
    "v_mov_b32 v197, v13\n\t"
    "v_mov_b32 v198, v14\n\t"
    "v_mov_b32 v199, v15\n\t"
    "v_mov_b32 v200, v16\n\t"
    "v_mov_b32 v201, v17\n\t"
    "v_mov_b32 v202, v18\n\t"
    "v_mov_b32 v203, v19\n\t"
    "v_mov_b32 v204, v20\n\t"
    "v_mov_b32 v205, v21\n\t"
    "v_mov_b32 v206, v22\n\t"
    "v_mov_b32 v207, v23\n\t"
    "v_mov_b32 v208, v24\n\t"
    "v_mov_b32 v209, v25\n\t"
    "v_mov_b32 v210, v26\n\t"
    "v_mov_b32 v211, v27\n\t"
    "v_mov_b32 v212, v28\n\t"
    "v_mov_b32 v213, v29\n\t"
    "v_mov_b32 v214, v30\n\t"
    "v_mov_b32 v215, v31\n\t"
    "v_mov_b32 v216, v32\n\t"
    "v_mov_b32 v217, v33\n\t"
    "v_mov_b32 v218, v34\n\t"
    "v_mov_b32 v219, v35\n\t"
    "v_mov_b32 v220, v36\n\t"
    "v_mov_b32 v221, v37\n\t"
    "v_mov_b32 v222, v38\n\t"
    "v_mov_b32 v223, v39"
    :: "{v[192:223]}"(w), "v"(a) : "v8", "v9", "v10", "v11", "v12", "v13", "v14", "v15", "v16", "v17", "v18", "v19", "v20", "v21", "v22", "v23", "v24", "v25", "v26", "v27", "v28", "v29", "v30", "v31", "v32", "v33", "v34", "v35", "v36", "v37", "v38", "v39", "memory");
}
template <> __device__ __forceinline__ void qk_swap<5>(u32x32& w, unsigned a) {
  asm volatile("ds_read_b128 v[8:11], %1 offset:0\n\t"
    "ds_read_b128 v[12:15], %1 offset:1024\n\t"
    "ds_read_b128 v[16:19], %1 offset:8192\n\t"
    "ds_read_b128 v[20:23], %1 offset:9216\n\t"
    "ds_read_b128 v[24:27], %1 offset:16384\n\t"
    "ds_read_b128 v[28:31], %1 offset:17408\n\t"
    "ds_read_b128 v[32:35], %1 offset:24576\n\t"
    "ds_read_b128 v[36:39], %1 offset:25600\n\t"
    "ds_write_b128 %1, v[224:227] offset:0\n\t"
    "ds_write_b128 %1, v[228:231] offset:1024\n\t"
    "ds_write_b128 %1, v[232:235] offset:8192\n\t"
    "ds_write_b128 %1, v[236:239] offset:9216\n\t"
    "ds_write_b128 %1, v[240:243] offset:16384\n\t"
    "ds_write_b128 %1, v[244:247] offset:17408\n\t"
    "ds_write_b128 %1, v[248:251] offset:24576\n\t"
    "ds_write_b128 %1, v[252:255] offset:25600\n\t"
    "s_waitcnt lgkmcnt(0)\n\t"
    "v_mov_b32 v224, v8\n\t"
    "v_mov_b32 v225, v9\n\t"
    "v_mov_b32 v226, v10\n\t"
    "v_mov_b32 v227, v11\n\t"
    "v_mov_b32 v228, v12\n\t"
    "v_mov_b32 v229, v13\n\t"
    "v_mov_b32 v230, v14\n\t"
    "v_mov_b32 v231, v15\n\t"
    "v_mov_b32 v232, v16\n\t"
    "v_mov_b32 v233, v17\n\t"
    "v_mov_b32 v234, v18\n\t"
    "v_mov_b32 v235, v19\n\t"
    "v_mov_b32 v236, v20\n\t"
    "v_mov_b32 v237, v21\n\t"
    "v_mov_b32 v238, v22\n\t"
    "v_mov_b32 v239, v23\n\t"
    "v_mov_b32 v240, v24\n\t"
    "v_mov_b32 v241, v25\n\t"
    "v_mov_b32 v242, v26\n\t"
    "v_mov_b32 v243, v27\n\t"
    "v_mov_b32 v244, v28\n\t"
    "v_mov_b32 v245, v29\n\t"
    "v_mov_b32 v246, v30\n\t"
    "v_mov_b32 v247, v31\n\t"
    "v_mov_b32 v248, v32\n\t"
    "v_mov_b32 v249, v33\n\t"
    "v_mov_b32 v250, v34\n\t"
    "v_mov_b32 v251, v35\n\t"
    "v_mov_b32 v252, v36\n\t"
    "v_mov_b32 v253, v37\n\t"
    "v_mov_b32 v254, v38\n\t"
    "v_mov_b32 v255, v39"
    :: "{v[224:255]}"(w), "v"(a) : "v8", "v9", "v10", "v11", "v12", "v13", "v14", "v15", "v16", "v17", "v18", "v19", "v20", "v21", "v22", "v23", "v24", "v25", "v26", "v27", "v28", "v29", "v30", "v31", "v32", "v33", "v34", "v35", "v36", "v37", "v38", "v39", "memory");
}
template <int I> __device__ __forceinline__ void vr_wload(u32x32& w, unsigned a);
template <> __device__ __forceinline__ void vr_wload<0>(u32x32& w, unsigned a) {
  asm volatile("ds_read_b64_tr_b16 v[64:65], %1 offset:0\n\t"
    "ds_read_b64_tr_b16 v[66:67], %1 offset:256\n\t"
    "ds_read_b64_tr_b16 v[68:69], %1 offset:2048\n\t"
    "ds_read_b64_tr_b16 v[70:71], %1 offset:2304\n\t"
    "ds_read_b64_tr_b16 v[72:73], %1 offset:32\n\t"
    "ds_read_b64_tr_b16 v[74:75], %1 offset:288\n\t"
    "ds_read_b64_tr_b16 v[76:77], %1 offset:2080\n\t"
    "ds_read_b64_tr_b16 v[78:79], %1 offset:2336\n\t"
    "s_waitcnt lgkmcnt(0)\n\t"
    "ds_read_b64_tr_b16 v[80:81], %1 offset:64\n\t"
    "ds_read_b64_tr_b16 v[82:83], %1 offset:320\n\t"
    "ds_read_b64_tr_b16 v[84:85], %1 offset:2112\n\t"
    "ds_read_b64_tr_b16 v[86:87], %1 offset:2368\n\t"
    "ds_read_b64_tr_b16 v[88:89], %1 offset:96\n\t"
    "ds_read_b64_tr_b16 v[90:91], %1 offset:352\n\t"
    "ds_read_b64_tr_b16 v[92:93], %1 offset:2144\n\t"
    "ds_read_b64_tr_b16 v[94:95], %1 offset:2400\n\t"
    "s_waitcnt lgkmcnt(0)"
    : "=&{v[64:95]}"(w) : "v"(a) : "memory");
}
template <> __device__ __forceinline__ void vr_wload<1>(u32x32& w, unsigned a) {
  asm volatile("ds_read_b64_tr_b16 v[96:97], %1 offset:8192\n\t"
    "ds_read_b64_tr_b16 v[98:99], %1 offset:8448\n\t"
    "ds_read_b64_tr_b16 v[100:101], %1 offset:10240\n\t"
    "ds_read_b64_tr_b16 v[102:103], %1 offset:10496\n\t"
    "ds_read_b64_tr_b16 v[104:105], %1 offset:8224\n\t"
    "ds_read_b64_tr_b16 v[106:107], %1 offset:8480\n\t"
    "ds_read_b64_tr_b16 v[108:109], %1 offset:10272\n\t"
    "ds_read_b64_tr_b16 v[110:111], %1 offset:10528\n\t"
    "s_waitcnt lgkmcnt(0)\n\t"
    "ds_read_b64_tr_b16 v[112:113], %1 offset:8256\n\t"
    "ds_read_b64_tr_b16 v[114:115], %1 offset:8512\n\t"
    "ds_read_b64_tr_b16 v[116:117], %1 offset:10304\n\t"
    "ds_read_b64_tr_b16 v[118:119], %1 offset:10560\n\t"
    "ds_read_b64_tr_b16 v[120:121], %1 offset:8288\n\t"
    "ds_read_b64_tr_b16 v[122:123], %1 offset:8544\n\t"
    "ds_read_b64_tr_b16 v[124:125], %1 offset:10336\n\t"
    "ds_read_b64_tr_b16 v[126:127], %1 offset:10592\n\t"
    "s_waitcnt lgkmcnt(0)"
    : "=&{v[96:127]}"(w) : "v"(a) : "memory");
}
template <> __device__ __forceinline__ void vr_wload<2>(u32x32& w, unsigned a) {
  asm volatile("ds_read_b64_tr_b16 v[128:129], %1 offset:0\n\t"
    "ds_read_b64_tr_b16 v[130:131], %1 offset:128\n\t"
    "ds_read_b64_tr_b16 v[132:133], %1 offset:1024\n\t"
    "ds_read_b64_tr_b16 v[134:135], %1 offset:1152\n\t"
    "ds_read_b64_tr_b16 v[136:137], %1 offset:32\n\t"
    "ds_read_b64_tr_b16 v[138:139], %1 offset:160\n\t"
    "ds_read_b64_tr_b16 v[140:141], %1 offset:1056\n\t"
    "ds_read_b64_tr_b16 v[142:143], %1 offset:1184\n\t"
    "s_waitcnt lgkmcnt(0)\n\t"
    "ds_read_b64_tr_b16 v[144:145], %1 offset:4096\n\t"
    "ds_read_b64_tr_b16 v[146:147], %1 offset:4224\n\t"
    "ds_read_b64_tr_b16 v[148:149], %1 offset:5120\n\t"
    "ds_read_b64_tr_b16 v[150:151], %1 offset:5248\n\t"
    "ds_read_b64_tr_b16 v[152:153], %1 offset:4128\n\t"
    "ds_read_b64_tr_b16 v[154:155], %1 offset:4256\n\t"
    "ds_read_b64_tr_b16 v[156:157], %1 offset:5152\n\t"
    "ds_read_b64_tr_b16 v[158:159], %1 offset:5280\n\t"
    "s_waitcnt lgkmcnt(0)"
    : "=&{v[128:159]}"(w) : "v"(a) : "memory");
}
template <> __device__ __forceinline__ void vr_wload<3>(u32x32& w, unsigned a) {
  asm volatile("ds_read_b64_tr_b16 v[160:161], %1 offset:8192\n\t"
    "ds_read_b64_tr_b16 v[162:163], %1 offset:8320\n\t"
    "ds_read_b64_tr_b16 v[164:165], %1 offset:9216\n\t"
    "ds_read_b64_tr_b16 v[166:167], %1 offset:9344\n\t"
    "ds_read_b64_tr_b16 v[168:169], %1 offset:8224\n\t"
    "ds_read_b64_tr_b16 v[170:171], %1 offset:8352\n\t"
    "ds_read_b64_tr_b16 v[172:173], %1 offset:9248\n\t"
    "ds_read_b64_tr_b16 v[174:175], %1 offset:9376\n\t"
    "s_waitcnt lgkmcnt(0)\n\t"
    "ds_read_b64_tr_b16 v[176:177], %1 offset:12288\n\t"
    "ds_read_b64_tr_b16 v[178:179], %1 offset:12416\n\t"
    "ds_read_b64_tr_b16 v[180:181], %1 offset:13312\n\t"
    "ds_read_b64_tr_b16 v[182:183], %1 offset:13440\n\t"
    "ds_read_b64_tr_b16 v[184:185], %1 offset:12320\n\t"
    "ds_read_b64_tr_b16 v[186:187], %1 offset:12448\n\t"
    "ds_read_b64_tr_b16 v[188:189], %1 offset:13344\n\t"
    "ds_read_b64_tr_b16 v[190:191], %1 offset:13472\n\t"
    "s_waitcnt lgkmcnt(0)"
    : "=&{v[160:191]}"(w) : "v"(a) : "memory");
}
template <> __device__ __forceinline__ void vr_wload<4>(u32x32& w, unsigned a) {
  asm volatile("ds_read_b64_tr_b16 v[192:193], %1 offset:0\n\t"
    "ds_read_b64_tr_b16 v[194:195], %1 offset:128\n\t"
    "ds_read_b64_tr_b16 v[196:197], %1 offset:1024\n\t"
    "ds_read_b64_tr_b16 v[198:199], %1 offset:1152\n\t"
    "ds_read_b64_tr_b16 v[200:201], %1 offset:32\n\t"
    "ds_read_b64_tr_b16 v[202:203], %1 offset:160\n\t"
    "ds_read_b64_tr_b16 v[204:205], %1 offset:1056\n\t"
    "ds_read_b64_tr_b16 v[206:207], %1 offset:1184\n\t"
    "s_waitcnt lgkmcnt(0)\n\t"
    "ds_read_b64_tr_b16 v[208:209], %1 offset:4096\n\t"
    "ds_read_b64_tr_b16 v[210:211], %1 offset:4224\n\t"
    "ds_read_b64_tr_b16 v[212:213], %1 offset:5120\n\t"
    "ds_read_b64_tr_b16 v[214:215], %1 offset:5248\n\t"
    "ds_read_b64_tr_b16 v[216:217], %1 offset:4128\n\t"
    "ds_read_b64_tr_b16 v[218:219], %1 offset:4256\n\t"
    "ds_read_b64_tr_b16 v[220:221], %1 offset:5152\n\t"
    "ds_read_b64_tr_b16 v[222:223], %1 offset:5280\n\t"
    "s_waitcnt lgkmcnt(0)"
    : "=&{v[192:223]}"(w) : "v"(a) : "memory");
}
template <> __device__ __forceinline__ void vr_wload<5>(u32x32& w, unsigned a) {
  asm volatile("ds_read_b64_tr_b16 v[224:225], %1 offset:8192\n\t"
    "ds_read_b64_tr_b16 v[226:227], %1 offset:8320\n\t"
    "ds_read_b64_tr_b16 v[228:229], %1 offset:9216\n\t"
    "ds_read_b64_tr_b16 v[230:231], %1 offset:9344\n\t"
    "ds_read_b64_tr_b16 v[232:233], %1 offset:8224\n\t"
    "ds_read_b64_tr_b16 v[234:235], %1 offset:8352\n\t"
    "ds_read_b64_tr_b16 v[236:237], %1 offset:9248\n\t"
    "ds_read_b64_tr_b16 v[238:239], %1 offset:9376\n\t"
    "s_waitcnt lgkmcnt(0)\n\t"
    "ds_read_b64_tr_b16 v[240:241], %1 offset:12288\n\t"
    "ds_read_b64_tr_b16 v[242:243], %1 offset:12416\n\t"
    "ds_read_b64_tr_b16 v[244:245], %1 offset:13312\n\t"
    "ds_read_b64_tr_b16 v[246:247], %1 offset:13440\n\t"
    "ds_read_b64_tr_b16 v[248:249], %1 offset:12320\n\t"
    "ds_read_b64_tr_b16 v[250:251], %1 offset:12448\n\t"
    "ds_read_b64_tr_b16 v[252:253], %1 offset:13344\n\t"
    "ds_read_b64_tr_b16 v[254:255], %1 offset:13472\n\t"
    "s_waitcnt lgkmcnt(0)"
    : "=&{v[224:255]}"(w) : "v"(a) : "memory");
}
#define OREP 2
#define AREP 2
struct VRW { u32x32 v0, v1, o0, o1, o2, o3; };
__device__ __forceinline__ void vr_v(const VRW& W, const char* ag, const char* vd, unsigned wa, unsigned ra, unsigned& cap, unsigned& sp, unsigned long long& tc, unsigned long long* ts) {
  asm volatile("v_mbcnt_lo_u32_b32 v16, -1, 0\n\t"
    "v_mbcnt_hi_u32_b32 v16, -1, v16\n\t"
    "v_lshlrev_b32 v17, 4, v16\n\t"
    "s_mov_b32 %[sp], 0\n\t"
    "global_load_dwordx4 v[24:27], v17, %[ag] sc1\n\t"
    "global_load_dwordx4 v[28:31], v17, %[ag] offset:1024 sc1\n\t"
    "s_memtime %[tc]\n\t"
    "v_and_b32 v22, 4, v16\n\t"
    "v_mul_u32_u24 v22, 0x2aaa, v22\n\t"
    "v_lshrrev_b32 v18, 1, v16\n\t"
    "v_and_b32 v18, 2, v18\n\t"
    "v_or3_b32 v18, v18, v22, %[c44]\n\t"
    "v_lshl_add_u32 v19, v16, 3, %[wa]\n\t"
    "v_lshl_add_u32 v20, v16, 3, %[ra]\n\t"
    "v_and_b32 v22, 40, v16\n\t"
    "v_lshlrev_b32 v21, 3, v22\n\t"
    "v_and_b32 v22, 3, v16\n\t"
    "v_lshl_or_b32 v21, v22, 4, v21\n\t"
    "v_and_b32 v22, 4, v16\n\t"
    "v_lshl_or_b32 v21, v22, 1, v21\n\t"
    "v_lshrrev_b32 v22, 2, v16\n\t"
    "v_and_or_b32 v21, v22, 4, v21\n\t"
    "3:\n\t"
    "v_mov_b32 v32, 0\n\t"
    "v_mov_b32 v33, 0\n\t"
    "v_mov_b32 v34, 0\n\t"
    "v_mov_b32 v35, 0\n\t"
    "v_mov_b32 v36, 0\n\t"
    "v_mov_b32 v37, 0\n\t"
    "v_mov_b32 v38, 0\n\t"
    "v_mov_b32 v39, 0\n\t"
    "v_mov_b32 v40, 0\n\t"
    "v_mov_b32 v41, 0\n\t"
    "v_mov_b32 v42, 0\n\t"
    "v_mov_b32 v43, 0\n\t"
    "v_mov_b32 v44, 0\n\t"
    "v_mov_b32 v45, 0\n\t"
    "v_mov_b32 v46, 0\n\t"
    "v_mov_b32 v47, 0\n\t"
    "s_waitcnt vmcnt(0) lgkmcnt(0)\n\t"
    "v_smfmac_f32_16x16x64_bf16 v[32:35], v[24:27], v[64:71], v18\n\t"
    "v_smfmac_f32_16x16x64_bf16 v[32:35], v[28:31], v[96:103], v18\n\t"
    "v_max3_u32 v22, v24, v25, v26\n\t"
    "v_max3_u32 v23, v27, v28, v29\n\t"
    "v_smfmac_f32_16x16x64_bf16 v[36:39], v[24:27], v[72:79], v18\n\t"
    "v_max3_u32 v22, v22, v30, v31\n\t"
    "v_max_u32 v22, v22, v23\n\t"
    "v_smfmac_f32_16x16x64_bf16 v[36:39], v[28:31], v[104:111], v18\n\t"
    "v_cmp_eq_u32 vcc, -1, v22\n\t"
    "s_cbranch_vccnz 1f\n\t"
    "2:\n\t"
    "v_smfmac_f32_16x16x64_bf16 v[40:43], v[24:27], v[80:87], v18\n\t"
    "v_permlane16_swap_b32 v32, v34\n\t"
    "v_permlane16_swap_b32 v33, v35\n\t"
    "v_smfmac_f32_16x16x64_bf16 v[40:43], v[28:31], v[112:119], v18\n\t"
    "v_pk_add_f32 v[32:33], v[32:33], v[34:35]\n\t"
    "ds_write_b64 v19, v[32:33] offset:0\n\t"
    "v_smfmac_f32_16x16x64_bf16 v[44:47], v[24:27], v[88:95], v18\n\t"
    "s_nop 0\n\t"
    "v_permlane16_swap_b32 v36, v38\n\t"
    "v_permlane16_swap_b32 v37, v39\n\t"
    "v_smfmac_f32_16x16x64_bf16 v[44:47], v[28:31], v[120:127], v18\n\t"
    "v_pk_add_f32 v[36:37], v[36:37], v[38:39]\n\t"
    "ds_write_b64 v19, v[36:37] offset:512\n\t"
    "v_permlane16_swap_b32 v40, v42\n\t"
    "v_permlane16_swap_b32 v41, v43\n\t"
    "v_pk_add_f32 v[40:41], v[40:41], v[42:43]\n\t"
    "ds_write_b64 v19, v[40:41] offset:1024\n\t"
    "s_nop 1\n\t"
    "v_permlane16_swap_b32 v44, v46\n\t"
    "v_permlane16_swap_b32 v45, v47\n\t"
    "v_pk_add_f32 v[44:45], v[44:45], v[46:47]\n\t"
    "ds_write_b64 v19, v[44:45] offset:1536\n\t"
    "s_cmp_ge_u32 %[wa], 8192\n\t"
    "s_waitcnt lgkmcnt(0)\n\t"
    "s_barrier\n\t"
    "s_cbranch_scc1 9f\n\t"
    "ds_read_b64 v[48:49], v20 offset:0\n\t"
    "ds_read_b64 v[50:51], v20 offset:2048\n\t"
    "ds_read_b64 v[52:53], v20 offset:4096\n\t"
    "ds_read_b64 v[54:55], v20 offset:6144\n\t"
    "ds_read_b64 v[56:57], v20 offset:8192\n\t"
    "ds_read_b64 v[58:59], v20 offset:10240\n\t"
    "ds_read_b64 v[60:61], v20 offset:12288\n\t"
    "ds_read_b64 v[62:63], v20 offset:14336\n\t"
    "s_waitcnt lgkmcnt(6)\n\t"
    "v_pk_add_f32 v[48:49], v[48:49], v[50:51]\n\t"
    "s_waitcnt lgkmcnt(4)\n\t"
    "v_pk_add_f32 v[52:53], v[52:53], v[54:55]\n\t"
    "v_pk_add_f32 v[48:49], v[48:49], v[52:53]\n\t"
    "s_waitcnt lgkmcnt(2)\n\t"
    "v_pk_add_f32 v[56:57], v[56:57], v[58:59]\n\t"
    "s_waitcnt lgkmcnt(0)\n\t"
    "v_pk_add_f32 v[60:61], v[60:61], v[62:63]\n\t"
    "v_pk_add_f32 v[56:57], v[56:57], v[60:61]\n\t"
    "v_pk_add_f32 v[48:49], v[48:49], v[56:57]\n\t"
    "v_cvt_pk_bf16_f32 v22, v48, v49\n\t"
    "v_min_u32 v22, -2, v22\n\t"
    "global_store_dword v21, v22, %[vd] sc1\n\t"
    "s_waitcnt lgkmcnt(0)\n\t"
    "s_branch 9f\n\t"
    "1:\n\t"
    "s_add_u32 %[sp], %[sp], 1\n\t"
    "s_cmp_ge_u32 %[sp], %[cap]\n\t"
    "s_cbranch_scc1 7f\n\t"
    "global_load_dwordx4 v[24:27], v17, %[ag] sc1\n\t"
    "global_load_dwordx4 v[28:31], v17, %[ag] offset:1024 sc1\n\t"
    "s_waitcnt lgkmcnt(0)\n\t"
    "s_memtime %[tc]\n\t"
    "s_nop 7\n\t"
    "s_branch 3b\n\t"
    "7:\n\t"
    "s_mov_b32 %[cap], 0\n\t"
    "s_branch 2b\n\t"
    "9:"
    : [tc] "=&s"(tc), [sp] "=&s"(sp), [cap] "+s"(cap)
    : [ag] "s"(ag), [vd] "s"(vd), [wa] "s"(wa), [ra] "s"(ra), [c44] "s"(0x4444u), "{v[64:95]}"(W.v0), "{v[96:127]}"(W.v1), "{v[128:159]}"(W.o0), "{v[160:191]}"(W.o1), "{v[192:223]}"(W.o2), "{v[224:255]}"(W.o3)
    : "v16", "v17", "v18", "v19", "v20", "v21", "v22", "v23", "v24", "v25", "v26", "v27", "v28", "v29", "v30", "v31", "v32", "v33", "v34", "v35", "v36", "v37", "v38", "v39", "v40", "v41", "v42", "v43", "v44", "v45", "v46", "v47", "v48", "v49", "v50", "v51", "v52", "v53", "v54", "v55", "v56", "v57", "v58", "v59", "v60", "v61", "v62", "v63", "vcc", "scc", "memory");
}
__device__ __forceinline__ void vr_r(const VRW& W, const char* os, const char* ad, unsigned wa, unsigned jx, unsigned& cap, unsigned& sp, unsigned long long* ts) {
  asm volatile("v_mbcnt_lo_u32_b32 v16, -1, 0\n\t"
    "v_mbcnt_hi_u32_b32 v16, -1, v16\n\t"
    "v_lshl_or_b32 v17, v16, 4, %[jx]\n\t"
    "global_load_dwordx4 v[24:27], v17, %[os] sc1\n\t"
    "v_xor_b32 v18, 2048, v17\n\t"
    "global_load_dwordx4 v[28:31], v18, %[os] sc1\n\t"
    "v_xor_b32 v57, 4096, v17\n\t"
    "global_load_dwordx4 v[32:35], v57, %[os] sc1\n\t"
    "v_xor_b32 v58, 6144, v17\n\t"
    "global_load_dwordx4 v[36:39], v58, %[os] sc1\n\t"
    "s_mov_b32 %[sp], 0\n\t"
    "v_and_b32 v23, 4, v16\n\t"
    "v_mul_u32_u24 v23, 0x2aaa, v23\n\t"
    "v_lshrrev_b32 v19, 1, v16\n\t"
    "v_and_b32 v19, 2, v19\n\t"
    "v_or3_b32 v19, v19, v23, %[c44]\n\t"
    "v_and_b32 v23, 15, v16\n\t"
    "v_lshl_add_u32 v20, v23, 2, %[wa]\n\t"
    "v_and_b32 v23, 48, v16\n\t"
    "v_lshl_add_u32 v20, v23, 3, v20\n\t"
    "v_and_b32 v23, 32, v16\n\t"
    "v_and_b32 v21, 28, v16\n\t"
    "v_lshlrev_b32 v21, 4, v21\n\t"
    "v_or_b32 v21, v21, v23\n\t"
    "v_lshlrev_b32 v22, 3, v23\n\t"
    "v_and_b32 v23, 3, v16\n\t"
    "v_lshl_or_b32 v21, v23, 3, v21\n\t"
    "v_and_b32 v23, 31, v16\n\t"
    "v_lshl_or_b32 v22, v23, 2, v22\n\t"
    "3:\n\t"
    "v_mov_b32 v40, 0\n\t"
    "v_mov_b32 v41, 0\n\t"
    "v_mov_b32 v42, 0\n\t"
    "v_mov_b32 v43, 0\n\t"
    "v_mov_b32 v44, 0\n\t"
    "v_mov_b32 v45, 0\n\t"
    "v_mov_b32 v46, 0\n\t"
    "v_mov_b32 v47, 0\n\t"
    "v_mov_b32 v48, 0\n\t"
    "v_mov_b32 v49, 0\n\t"
    "v_mov_b32 v50, 0\n\t"
    "v_mov_b32 v51, 0\n\t"
    "v_mov_b32 v52, 0\n\t"
    "v_mov_b32 v53, 0\n\t"
    "v_mov_b32 v54, 0\n\t"
    "v_mov_b32 v55, 0\n\t"
    "v_mov_b32 v56, 0\n\t"
    "s_waitcnt vmcnt(3)\n\t"
    "v_smfmac_f32_16x16x64_bf16 v[44:47], v[24:27], v[144:151], v19\n\t"
    "v_max3_u32 v56, v56, v24, v25\n\t"
    "v_smfmac_f32_16x16x64_bf16 v[48:51], v[24:27], v[136:143], v19\n\t"
    "v_max3_u32 v56, v56, v26, v27\n\t"
    "v_smfmac_f32_16x16x64_bf16 v[40:43], v[24:27], v[128:135], v19\n\t"
    "v_smfmac_f32_16x16x64_bf16 v[52:55], v[24:27], v[152:159], v19\n\t"
    "s_waitcnt vmcnt(2)\n\t"
    "v_smfmac_f32_16x16x64_bf16 v[44:47], v[28:31], v[176:183], v19\n\t"
    "v_max3_u32 v56, v56, v28, v29\n\t"
    "v_smfmac_f32_16x16x64_bf16 v[48:51], v[28:31], v[168:175], v19\n\t"
    "v_max3_u32 v56, v56, v30, v31\n\t"
    "v_smfmac_f32_16x16x64_bf16 v[40:43], v[28:31], v[160:167], v19\n\t"
    "v_smfmac_f32_16x16x64_bf16 v[52:55], v[28:31], v[184:191], v19\n\t"
    "s_waitcnt vmcnt(1)\n\t"
    "v_smfmac_f32_16x16x64_bf16 v[44:47], v[32:35], v[208:215], v19\n\t"
    "v_max3_u32 v56, v56, v32, v33\n\t"
    "v_smfmac_f32_16x16x64_bf16 v[48:51], v[32:35], v[200:207], v19\n\t"
    "v_max3_u32 v56, v56, v34, v35\n\t"
    "v_smfmac_f32_16x16x64_bf16 v[40:43], v[32:35], v[192:199], v19\n\t"
    "v_smfmac_f32_16x16x64_bf16 v[52:55], v[32:35], v[216:223], v19\n\t"
    "s_waitcnt vmcnt(0)\n\t"
    "v_smfmac_f32_16x16x64_bf16 v[44:47], v[36:39], v[240:247], v19\n\t"
    "v_max3_u32 v56, v56, v36, v37\n\t"
    "v_smfmac_f32_16x16x64_bf16 v[48:51], v[36:39], v[232:239], v19\n\t"
    "v_max3_u32 v56, v56, v38, v39\n\t"
    "v_smfmac_f32_16x16x64_bf16 v[40:43], v[36:39], v[224:231], v19\n\t"
    "v_cmp_eq_u32 vcc, -1, v56\n\t"
    "v_smfmac_f32_16x16x64_bf16 v[52:55], v[36:39], v[248:255], v19\n\t"
    "s_cbranch_vccnz 4f\n\t"
    "5:\n\t"
    "s_nop 2\n\t"
    "v_permlane32_swap_b32 v44, v48\n\t"
    "v_permlane32_swap_b32 v45, v49\n\t"
    "v_permlane32_swap_b32 v46, v50\n\t"
    "v_permlane32_swap_b32 v47, v51\n\t"
    "s_mov_b32 exec_hi, 0\n\t"
    "v_pk_add_f32 v[40:41], v[40:41], v[48:49]\n\t"
    "v_pk_add_f32 v[42:43], v[42:43], v[50:51]\n\t"
    "s_mov_b64 exec, %[hi]\n\t"
    "v_pk_add_f32 v[40:41], v[44:45], v[52:53]\n\t"
    "v_pk_add_f32 v[42:43], v[46:47], v[54:55]\n\t"
    "s_mov_b32 exec_lo, -1\n\t"
    "s_nop 0\n\t"
    "v_permlane16_swap_b32 v40, v42\n\t"
    "v_permlane16_swap_b32 v41, v43\n\t"
    "v_add_f32 v40, v40, v42\n\t"
    "v_add_f32 v41, v41, v43\n\t"
    "ds_write_b32 v20, v40 offset:16384\n\t"
    "ds_write_b32 v20, v41 offset:16448\n\t"
    "s_cmp_ge_u32 %[wa], 1024\n\t"
    "s_waitcnt lgkmcnt(0)\n\t"
    "s_barrier\n\t"
    "s_cbranch_scc1 9f\n\t"
    "ds_read_b64 v[24:25], v21 offset:16384\n\t"
    "ds_read_b64 v[26:27], v21 offset:16896\n\t"
    "ds_read_b64 v[28:29], v21 offset:17408\n\t"
    "ds_read_b64 v[30:31], v21 offset:17920\n\t"
    "ds_read_b64 v[32:33], v21 offset:18432\n\t"
    "ds_read_b64 v[34:35], v21 offset:18944\n\t"
    "ds_read_b64 v[36:37], v21 offset:19456\n\t"
    "ds_read_b64 v[38:39], v21 offset:19968\n\t"
    "s_waitcnt lgkmcnt(6)\n\t"
    "v_pk_add_f32 v[24:25], v[24:25], v[26:27]\n\t"
    "s_waitcnt lgkmcnt(4)\n\t"
    "v_pk_add_f32 v[28:29], v[28:29], v[30:31]\n\t"
    "v_pk_add_f32 v[24:25], v[24:25], v[28:29]\n\t"
    "s_waitcnt lgkmcnt(2)\n\t"
    "v_pk_add_f32 v[32:33], v[32:33], v[34:35]\n\t"
    "s_waitcnt lgkmcnt(0)\n\t"
    "v_pk_add_f32 v[36:37], v[36:37], v[38:39]\n\t"
    "v_pk_add_f32 v[32:33], v[32:33], v[36:37]\n\t"
    "v_pk_add_f32 v[24:25], v[24:25], v[32:33]\n\t"
    "v_cvt_pk_bf16_f32 v23, v24, v25\n\t"
    "v_min_u32 v23, -2, v23\n\t"
    "global_store_dword v22, v23, %[ad] sc1\n\t"
    "s_waitcnt lgkmcnt(0)\n\t"
    "s_branch 9f\n\t"
    "4:\n\t"
    "s_add_u32 %[sp], %[sp], 1\n\t"
    "s_cmp_ge_u32 %[sp], %[cap]\n\t"
    "s_cbranch_scc1 7f\n\t"
    "global_load_dwordx4 v[24:27], v17, %[os] sc1\n\t"
    "global_load_dwordx4 v[28:31], v18, %[os] sc1\n\t"
    "global_load_dwordx4 v[32:35], v57, %[os] sc1\n\t"
    "global_load_dwordx4 v[36:39], v58, %[os] sc1\n\t"
    "s_nop 7\n\t"
    "s_branch 3b\n\t"
    "7:\n\t"
    "s_mov_b32 %[cap], 0\n\t"
    "s_branch 5b\n\t"
    "9:"
    : [sp] "=&s"(sp), [cap] "+s"(cap)
    : [os] "s"(os), [ad] "s"(ad), [wa] "s"(wa), [jx] "s"(jx), [c44] "s"(0x4444u), [hi] "s"(0xFFFFFFFF00000000ull), "{v[64:95]}"(W.v0), "{v[96:127]}"(W.v1), "{v[128:159]}"(W.o0), "{v[160:191]}"(W.o1), "{v[192:223]}"(W.o2), "{v[224:255]}"(W.o3)
    : "v16", "v17", "v18", "v19", "v20", "v21", "v22", "v23", "v24", "v25", "v26", "v27", "v28", "v29", "v30", "v31", "v32", "v33", "v34", "v35", "v36", "v37", "v38", "v39", "v40", "v41", "v42", "v43", "v44", "v45", "v46", "v47", "v48", "v49", "v50", "v51", "v52", "v53", "v54", "v55", "v56", "v57", "v58", "v59", "v60", "v61", "v62", "v63", "vcc", "scc", "memory");
}
__device__ __forceinline__ void qk_body(char* smem, int wave_id_, int xg, int h, int wg, const u16* __restrict__ Wq, const u16* __restrict__ Wk,
                                        const char* __restrict__ Kx, const char* __restrict__ V, char* ws, unsigned* tmo, u64 tag) {
  const int wave = WAVE_ID;
  const int ty = wave >> 2, w4 = wave & 3;
  const char* wq = uni((const char*)(Wq + h * DH)); const char* wk = uni((const char*)(Wk + h * DH));
  auto stage = [&](int u) {
    stage_proj((u & 1) ? wk + (size_t)(((u >> 1) + 2) & 7) * 128 * HD * 2 : wq + (size_t)(u >> 1) * 128 * HD * 2, ((u + 4) % 5) * 32768, wave * 64 + lane_id(), wave);
  };
  QKW W;
  if (wave == 0) STS(ws, wg, 7);
  stage(0); stage(1); stage(2); stage(3);
#define WLD(i, w) do { WAIT_V(12); WBAR(); stage(2 * (i) + 4); if (ty) { WAIT_V(12); WBAR(); stage(2 * (i) + 5); } \
    { const int l_ = lane_id(); const unsigned o_ = ty ? ((2 * (i) + 5) % 5) * 32768 : ((2 * (i) + 4) % 5) * 32768; qk_wload<i>(w, proj_base(2 * w4, l_) + o_, proj_base(2 * w4 + 1, l_) + o_); } \
    if (!ty) { WAIT_V(12); WBAR(); stage(2 * (i) + 5); } if (wave == 0) STS(ws, wg, 1 + (i)); } while (0)
  WLD(0, W.w0); WLD(1, W.w1); WLD(2, W.w2); WLD(3, W.w3); WLD(4, W.w4); WLD(5, W.w5);
#undef WLD
  unsigned in = 0;
#pragma unroll
  for (int i = 0; i < 2; ++i) {
    if (i == 0) WAIT_V(8); else WAIT_V(0);
    WBAR();
    if (i == 0 && wave == 1) init_issue(ws, Q_V);
    if (i == 1) in = init_get(ws, tag, smem + Q_V);
    const int lane = lane_id();
    const unsigned o = Q_RING + ty * 32768 + i * 65536, f0 = proj_base(2 * w4, lane) + o, f1 = proj_base(2 * w4 + 1, lane) + o;
    u32x8 x[4] = {proj_frag(smem, f0), proj_frag(smem, f1), proj_frag(smem, f0 + 16384), proj_frag(smem, f1 + 16384)};
    asm volatile("" : "+v"(x[0]), "+v"(x[1]), "+v"(x[2]), "+v"(x[3]));
    BAR();
    char* d = smem + Q_CB + i * 57344 + ty * (i ? 32768 : 24576) + w4 * 2048 + lane * 16;
#pragma unroll
    for (int j = 0; j < 4; ++j)
      if (i || j || !ty) { *(u32x4*)(d + j * 8192) = u32x4{x[j][0], x[j][1], x[j][2], x[j][3]}; *(u32x4*)(d + j * 8192 + 1024) = u32x4{x[j][4], x[j][5], x[j][6], x[j][7]}; }
    if (i == 0 && ty) { *(u32x4*)(smem + Q_MEM + wave * 2048 + lane * 16) = u32x4{x[0][0], x[0][1], x[0][2], x[0][3]}; *(u32x4*)(smem + Q_MEM + wave * 2048 + 1024 + lane * 16) = u32x4{x[0][4], x[0][5], x[0][6], x[0][7]}; }
  }
  if (wave == 4) *(volatile LAS u32x2*)(smem + Q_TM) = u32x2{0u, 0u};
  BAR();
  if (wave == 0) STS(ws, wg, 0);
  unsigned* cnt = cnt_of(ws, in);
  const char* xb = uni(xch_base(ws) + ((size_t)3 << 21) + (size_t)((in & 2u) << 12));
  const char* oh = uni(xch_base(ws) + (size_t)((in & 1u) << 13) + (size_t)(h >> 2) * 16384 + (h & 3) * 2048);
  auto rearm = [&](int ps) {
    unsigned z = SENT; asm volatile("" : "+v"(z));
    const unsigned l16 = lane_id() * 16;
#pragma unroll
    for (int r = 0; r < 2 * OREP; ++r) st16s(oh + (size_t)(ps * 4 + (r >> 1)) * 131072, (r & 1) * 1024u + l16, u32x4{z, z, z, z});
  };
  const char* dbw = xb + (size_t)48 * 16384 + 4096 + h * 128;
  unsigned avail = 0;
  auto stage_kv = [&](int s) {
    if (avail <= (unsigned)s) {
      for (int spins = 0;; ++spins) {
        const unsigned p = ld4_sc1(cnt + (PQKV + h) * 32 + (lane_id() & 15));
        avail = min(qkv_avail(p, 1), qkv_avail(p, 2));
        if (avail > (unsigned)s) break;
        __builtin_amdgcn_s_sleep(8);
        if (give_up(spins, tmo, 5)) break;
      }
    }
    const char* ks = uni(Kx + ((size_t)s * NH + h) * 8192); const char* vs = uni(V + ((size_t)s * NH + h) * 8192);
    const int lane = lane_id();
    head_to_lds(ks, lane, Q_K); head_to_lds(vs, lane, Q_V);
  };
  auto loop = [&](auto w_c) __attribute__((always_inline)) {
    constexpr int WV = decltype(w_c)::value;
    if (WV >= 4) asm volatile("ds_read_b128 v[56:59], %1\n\tds_read_b128 v[60:63], %1 offset:1024\n\ts_waitcnt lgkmcnt(0)" : "=&{v[56:63]}"(W.x) : "v"(Q_MEM + WV * 2048 + lane_id() * 16) : "memory");
    if (WV == 6) stage_kv(0);
    if (QK_OWN) {
      constexpr int PI = WV < 4 ? WV : WV - 2;
      qk_swap<PI>(PI == 0 ? W.w0 : PI == 1 ? W.w1 : PI == 2 ? W.w2 : PI == 3 ? W.w3 : PI == 4 ? W.w4 : W.w5, Q_CB + 57344 + (WV >> 2) * 32768 + (WV & 3) * 2048 + lane_id() * 16);
    }
    if (WV < 4) __builtin_amdgcn_s_setprio(2);
    unsigned t0 = 0, cap = 60000, d0 = 105, d1 = 200;
    int dq = D_Q;
    int sl = 0;
    for (int s = 0; s < NSEG; ++s) {
      TSDECL;
      const char* ag = xb + (size_t)((sl * 4 + (AREP == 4 ? xg : (AREP == 2 ? ARSEL : 0))) * 2 + (WV >> 2)) * 16384 + (WV & 3) * 2048;
      const char* vs = WV == 4 ? dbw : WV == 5 ? ws + OFF_KVM + ((size_t)s * NH + h) * 4096 : xb + (size_t)(24 + sl * 8 + (h >> 2)) * 16384 + (h & 3) * 2048;
      const char* od = WV == 4 ? (const char*)(size_t)((unsigned)(s + 1) << 24) : oh + (size_t)sl * 524288;
      if (s > 0) wait_rt(t0, dq);
      TS(0);
      unsigned long long tc; unsigned sp;
      qk_step<WV>(W, ag, vs, od, LAST && s == NSEG - 1 ? nullptr : ws + OFF_KVM + ((size_t)s * NH + h) * 4096 + 2048, cap, sp, tc, nullptr, d0, d1);
      dq = (sp & 0xFFFFu) ? min(dq + DQU, D_Q + DQM) : max(dq - DQD, D_Q);
      if ((WV < 4 || WV == 5) && !(LAST && s == NSEG - 1)) {
        const unsigned pv = (unsigned)(s + 1) << 24 | (rt_now() & 0xFFFFFFu);
        WAIT_V(0);
        if (lane_id() == 0) st4_sc1(cnt + (PROG + h) * 32 + WV, pv);
      }
      if (WV == 6 && s + 1 < NSEG - 1) stage_kv(s + 1);
      if (WV == 7 && s > 0 && s < NSEG - 1) rearm(sl == 0 ? 2 : sl - 1);
      if (WV == 7 && s >= 8 && s < 8 + NSEG - SDIR) {
        unsigned z = SENT; asm volatile("" : "+v"(z));
        const char* kd = ws + OFF_KVM + ((size_t)(s - 8 + SDIR) * NH + h) * 4096;
#pragma unroll
        for (int r = 0; r < 4; ++r) st16s(kd, r * 1024u + lane_id() * 16, u32x4{z, z, z, z});
      }
      if (s < NSEG - 1) {
        if (WV == 4) {
          delay64<E_B>();
          t0 = bell_time(xb + (size_t)(48 + xg) * 16384 + 4096, (lane_id() & 31) * 128, (unsigned)(s + 1), cap);
          *(volatile LAS u32x2*)(smem + Q_TM) = u32x2{(unsigned)(s + 1), t0};
        } else {
          for (;;) {
            const u32x2 v = *(volatile LAS u32x2*)(smem + Q_TM);
            t0 = __builtin_amdgcn_readfirstlane(v.y);
            if (__builtin_amdgcn_readfirstlane(v.x) == (unsigned)(s + 1)) break;
            __builtin_amdgcn_s_sleep(1);
          }
        }
      }
      sl = sl == 2 ? 0 : sl + 1;
    }
    if (cap == 0 && lane_id() == 0) st4_sc1(tmo, 1u);
  };
  switch (wave) {
    case 0: loop(std::integral_constant<int, 0>{}); break;
    case 1: loop(std::integral_constant<int, 1>{}); break;
    case 2: loop(std::integral_constant<int, 2>{}); break;
    case 3: loop(std::integral_constant<int, 3>{}); break;
    case 4: loop(std::integral_constant<int, 4>{}); break;
    case 5: loop(std::integral_constant<int, 5>{}); break;
    case 6: loop(std::integral_constant<int, 6>{}); break;
    default: loop(std::integral_constant<int, 7>{}); break;
  }
  __builtin_amdgcn_s_setprio(0);
}

__device__ __forceinline__ unsigned vr_body(char* smem, int wave_id_, int xg, int h, int c, int r, int wg, const u16* __restrict__ Wv, const u16* __restrict__ Wo,
                                        const u16* __restrict__ Wq, const u16* __restrict__ Wk, char* ws, unsigned* tmo, u64 tag, const char* code) {
  const int wave = WAVE_ID;
  const unsigned po = (unsigned)(r >> 5) * 16384u + ((r >> 2) & 7) * 1024 + (r & 1) * 512 + ((r >> 1) & 1) * 128;
  const unsigned vo_ = (unsigned)(24 + (h >> 2)) * 16384u + (h & 3) * 2048 + c * 1024;
  const int hg = (wave + ROTW * (r & 3)) & 7;
  const int jr = ROTH ? r & 3 : 0;
  VRW W;
  {
    const int lane = lane_id();
    const int g = lane >> 4, q4 = (lane & 15) >> 2, p4 = lane & 3, fr = 4 * g + (q4 & 1) + 32 * (q4 >> 1);
    const unsigned lw = wave * 16384;
    const char* sv_ = uni((const char*)(Wv + (size_t)(128 * wave) * HD + h * DH + 64 * c));
    const char* so = uni((const char*)(Wo + (size_t)(512 * hg) * DM + 64 * (r >> 2) + 16 * (r & 1)));
    const unsigned vo = (unsigned)((32 * (lane >> 5) + ((lane >> 2) & 7)) * DM * 2 + ((lane >> 1) & 1) * 64 + (lane & 1) * 16);
    auto issue = [&](int p) {
#pragma unroll
      for (int i = 0; i < 8; ++i) {
        if (p >= 2 && (NTM & 8)) gldsw(so + (size_t)(128 * ((p - 2) ^ jr) + 64 * (i & 1) + 8 * (i >> 1)) * DM * 2, vo, lw + (p & 1) * 8192 + i * 1024);
        else if (p >= 2) glds16s<0>(so + (size_t)(128 * ((p - 2) ^ jr) + 64 * (i & 1) + 8 * (i >> 1)) * DM * 2, vo, lw + (p & 1) * 8192 + i * 1024);
        else gldsw(sv_ + (size_t)(8 * p + i) * 8 * HD * 2, (unsigned)((lane >> 3) * HD * 2 + (lane & 7) * 16), lw + (p & 1) * 8192 + i * 1024);
      }
    };
#define VTS(p) do { } while (0)
    if (wave == 0) STS(ws, wg, 7);
    issue(0); issue(1);
    const unsigned av = lw + (unsigned)(fr * 128 + p4 * 8), ao = lw + (unsigned)(fr * 64 + p4 * 8);
    WAIT_V(8); VTS(0); vr_wload<0>(W.v0, av); issue(2);
    WAIT_V(8); VTS(1); vr_wload<1>(W.v1, av); issue(3);
    WAIT_V(8); VTS(2); vr_wload<2>(W.o0, ao); issue(4);
    WAIT_V(8); vr_wload<3>(W.o1, ao); issue(5);
    if (wave == 0) init_issue(ws, 131072);
    WAIT_V(8); vr_wload<4>(W.o2, ao);
    WAIT_V(0); VTS(3);
    if (VHN) {
      const int rl = r - 20 * xg, hn = ((xg == 1 ? rl : rl - (rl >= (xg ? 8 : 16) ? 4 : 0)) * 8 + wave) * (6 * VHN) + (lane >> 1);
      const int pm = hn / 384, hr = hn - pm * 384, p = 8 - VHN + (pm >> 1), hh = 12 * xg + 1 + 4 * (hr >> 7);
      const u16* hp = ((pm & 1) ? Wk : Wq) + (size_t)(((pm & 1) ? (p + 2) & 7 : p) * 128 + (hr & 127)) * HD + min(hh, 31) * DH + (lane & 1) * 64;
      if (((r >> 2) & 7) != 3 && lane < 12 * VHN && pm < 2 * VHN) asm volatile("s_mov_b32 m0, %0\n\ts_nop 0\n\tglobal_load_lds_dword %1, off" :: "s"(155648 + wave * 256), "v"(hp) : "memory", "m0");
    }
    vr_wload<5>(W.o3, ao);
#undef VTS
  }
  if (wave == 0) STS(ws, wg, 5);
  if (wave == 0) STS(ws, wg, 6);
  BAR();
  if (wave == 0) STS(ws, wg, 0);
  const unsigned in = init_get(ws, tag, smem + 131072);
  const char* xb = uni(xch_base(ws) + ((size_t)3 << 21) + (size_t)((in & 2u) << 12));
  const char* ob = uni(xch_base(ws) + (size_t)((in & 1u) << 13));
  auto rearm = [&](int ps, bool ag) {
    const int lane = lane_id();
    unsigned z = SENT; asm volatile("" : "+v"(z));
    const u32x4 sv = u32x4{z, z, z, z};
    if (ag && lane < 16 * AREP) st16s(xb, (unsigned)(ps * 8 + (lane >> 4) * 2) * 16384u + po + ((lane >> 3) & 1) * 256 + (lane & 7) * 16, sv);
    st16s(xb, (unsigned)ps * 131072u + vo_ + lane * 16, sv);
  };
  __builtin_amdgcn_s_setprio(2);
  unsigned cap = 60000;
  int sl = 0;
  for (int s = 0; s < NSEG; ++s) {
    TSDECL;
    const int sn = sl == 2 ? 0 : sl + 1;
    const char* ag = xb + (size_t)((sl * 4 + (AREP == 4 ? xg : (AREP == 2 ? ARSEL : 0))) * 2 + (wave >> 2)) * 16384 + (wave & 3) * 2048;
    const char* vd = (LAST && s == NSEG - 1 ? ws + OFF_KVM + ((size_t)s * NH + h) * 4096 + 2048 + c * 1024 : xb + (size_t)sl * 131072 + vo_) + (wave & 2) * 256 + (wave & 1) * 128;
    const char* os = ob + (size_t)((sl * 4 + (OREP == 4 ? xg : ORSEL)) * 8 + hg) * 16384 + ((r >> 1) & 1) * 1024;
    const char* ad = xb + (size_t)((sn * 4 + (wave & 3)) * 2) * 16384 + po;
    if (s > 0) delay64<E_AG>();
    TS(0);
    unsigned long long tc; unsigned sp;
    unsigned long long* tsp = nullptr;
    vr_v(W, ag, vd, wave * 2048, (wave & 3) * 512, cap, sp, tc, tsp);
    TN(12, sp + 1);
    if (s == NSEG - 1) break;
    if (wave == 7 && s > 0) rearm(sl == 0 ? 2 : sl - 1, true);
    if (wave == 6 && s == NSEG - 4 && code) touch(code, (unsigned)lane_id() * 128, 155648);
    wait_clk((unsigned)tc, E_O);
    TS(3);
    const unsigned tb = bell_time(xb + (size_t)(48 + xg) * 16384 + hg * 512, (lane_id() & 3) * 128, (unsigned)(s + 1), cap);
    wait_rt(tb, D_O);
    TS(4);
    vr_r(W, os, ad, wave * 512, jr * 2048, cap, sp, tsp);
    TN(14, (sp & 0xFFFF) + 1);
    if (wave == 1) TSFLUSH(0x6078);
    sl = sn;
  }
  __builtin_amdgcn_s_setprio(0);
  if (cap == 0 && lane_id() == 0) st4_sc1(tmo, 2u);
  return in;
}
struct ModIn { u32x4 w[8]; unsigned cv; };
__device__ __forceinline__ void mod_issue(ModIn& I, int wave_id_, int j, const u16* __restrict__ c, const u16* __restrict__ wmod) {
  const int tid = opaque_tid(), k0 = (j >> 5) * 512 + (tid >> 3);
  I.cv = c[k0 + 64 * (tid & 7)];
#pragma unroll
  for (int i = 0; i < 8; ++i) I.w[i] = ldnt<1>(wmod + (size_t)(k0 + 64 * i) * (6 * DM) + (j & 31) * 64 + (tid & 7) * 8);
}
__device__ __forceinline__ void mod_job(char* smem, int wave_id_, int j, u64 tag, const ModIn& I, char* ws) {
  const int tid = opaque_tid(), lane = tid & 63, wave = WAVE_ID, cg = tid & 7;
  const float cv = bflo(I.cv), sl = bfr(cv / (1.0f + __expf(-cv)));
  float acc[8] = {0, 0, 0, 0, 0, 0, 0, 0};
#pragma unroll
  for (int i = 0; i < 8; ++i) {
    const float sv = __shfl(sl, (lane & 56) + i);
#pragma unroll
    for (int e = 0; e < 4; ++e) { acc[2 * e] += sv * bflo(I.w[i][e]); acc[2 * e + 1] += sv * bfhi(I.w[i][e]); }
  }
#pragma unroll
  for (int e = 0; e < 8; ++e) acc[e] = red16_32_add(add_ror8(acc[e]));
  float* red = (float*)(smem + L_GB);
  if (lane < 8) { *(f32x4*)(red + wave * 64 + cg * 8) = f32x4{acc[0], acc[1], acc[2], acc[3]}; *(f32x4*)(red + wave * 64 + cg * 8 + 4) = f32x4{acc[4], acc[5], acc[6], acc[7]}; }
  __syncthreads();
  if (tid < 32) {
    f32x2 s = f32x2{0, 0};
#pragma unroll
    for (int r = 0; r < 8; ++r) s += *(const f32x2*)(red + r * 64 + tid * 2);
    st16_sc1(ws + OFF_MOD + (j * 32 + tid) * 16, u32x4{__float_as_uint(s[0]), (unsigned)tag, __float_as_uint(s[1]), tag2(tag)});
  }
  BAR();
}
#define NIMM 3
constexpr unsigned TK(int m, int hl, int part, int bo, int be) { return (unsigned)(m | hl << 2 | part << 5 | bo << 8 | be << 16); }
#define QX 30
#define CA 36
#define CB 42
#define C2 46
constexpr unsigned task_tab(int t, int it) {
  return t >= 8 && t < 13 ? (it == 0 ? TK(1, t - 8, 0, 0, 64) : it == 1 ? (t < 12 ? TK(2, t - 4, 1, C2, 64) : TK(1, 5, 1, C2, 64)) : 0)
       : it > 0 ? 0
       : t < 8 ? TK(0, t, 1, QX, 64)
       : t < 16 ? TK(1, t - 8, 0, 0, C2)
       : t < 24 ? TK(2, t - 16, 0, 0, t < 18 ? CA : (t < 20 ? CB : C2))
       : t < 26 ? TK(2, t - 24, 1, CA, 64)
       : t == 26 ? TK(1, 6, 1, C2, (C2 + 64) / 2) : t == 27 ? TK(1, 6, 2, (C2 + 64) / 2, 64)
       : t < 30 ? TK(2, t - 26, 1, CB, 64)
       : t == 30 ? TK(1, 7, 1, C2, (C2 + 64) / 2) : t == 31 ? TK(1, 7, 2, (C2 + 64) / 2, 64)
       : TK(0, t - 32, 0, 0, QX);
}
template <int IT, int I = 0> __device__ __forceinline__ unsigned task_sel(int t) {
  if constexpr (I == 40) return 0u;
  else { unsigned r = task_sel<IT, I + 1>(t); constexpr unsigned c = task_tab(I, IT); if (c) asm volatile("s_cmp_eq_u32 %1, %2\n\ts_cselect_b32 %0, %3, %0" : "+s"(r) : "s"(t), "i"(I), "i"(c) : "scc"); return r; }
}
__global__ void __launch_bounds__(512, 2) k_mega(const u16* __restrict__ Wq, const u16* __restrict__ Wk, const u16* __restrict__ Wv,
                                                 const u16* __restrict__ Wo, const u16* __restrict__ w1, const u16* __restrict__ w3,
                                                 const u16* __restrict__ w2, const u16* __restrict__ x, const u16* __restrict__ cvec,
                                                 const u16* __restrict__ wmod, const u16* __restrict__ mem0, char* ws, u16* out) {
  extern __shared__ __attribute__((aligned(16))) char smem[];
  int wave_id_ = __builtin_amdgcn_readfirstlane((int)threadIdx.x >> 6);
  unsigned bid = blockIdx.x;
  asm volatile("" : "+s"(wave_id_), "+s"(bid));
  unsigned* tmo = (unsigned*)(ws + OFF_TMO);
  const unsigned tst = (unsigned)__builtin_amdgcn_s_memrealtime();
  unsigned long long* mts = nullptr;
  const float* mod = (const float*)(ws + OFF_MOD);
  volatile LAS unsigned* tslot = (volatile LAS unsigned*)(smem + L_SLOT + 768);
#define CPF 131072
  u64 pc; asm volatile("s_getpc_b64 %0" : "=s"(pc));
  const u64 pc0 = pc & ~(u64)127;
  if (CPF && wave_id_ == 1) {
    const unsigned o = (((bid >> 3) & 31) * 64 + lane_id()) * 64;
    if (o < CPF) asm volatile("s_mov_b32 m0, %0\n\ts_nop 0\n\tglobal_load_lds_dword %1, %2" :: "s"(155648 + 1024), "v"(o), "s"(pc & ~(u64)127) : "memory", "m0");
  }
  unsigned xcc; asm volatile("s_getreg_b32 %0, hwreg(HW_REG_XCC_ID)" : "=s"(xcc));
  xcc &= 7;
  const unsigned pred = xcc * 32 + ((bid >> 3) & 31);
  ModIn mi;
  auto modj = [](unsigned sl) { return sl >= 128 && ((sl + 8) & 31) < 16 ? (int)(((sl >> 5) & 3) * 16 + (sl & 15)) : -1; };
  if (modj(pred) >= 0) mod_issue(mi, wave_id_, modj(pred), cvec, wmod);
  u64 tag;
  asm volatile("s_load_dwordx2 %0, %1, 0x0\n\ts_waitcnt lgkmcnt(0)" : "=s"(tag) : "s"(ws + OFF_TAG) : "memory");
  tag += 1;
  if (opaque_tid() == 0) {
    unsigned sl = pred;
    const unsigned s0 = sl;
    for (unsigned i = 1; i <= 256; ++i) {
      if (__hip_atomic_exchange((u64*)(ws + OFF_CLAIM + sl * 128), tag, __ATOMIC_RELAXED, __HIP_MEMORY_SCOPE_AGENT) != tag) break;
      sl = i == 256 ? 0xFFFFu : (((s0 + (i & ~31u)) & 0xE0u) | ((s0 + i) & 31u));
    }
    *tslot = sl;
  }
  BAR();
  const int slot = (int)__builtin_amdgcn_readfirstlane(*tslot);
  BAR();
  if (slot >= 256) return;
  if (modj(slot) >= 0) {
    if (slot != (int)pred) mod_issue(mi, wave_id_, modj(slot), cvec, wmod);
    mod_job(smem, wave_id_, modj(slot), tag, mi, ws);
  }
  unsigned* cnt = nullptr;
  const int xg = slot >> 5;
  int t = xg == 3 ? 24 + (slot & 7) : slot & 31;
  int hx = -1;
  if (xg < 3) {
    const int nq = xg == 2 ? 8 : 12;
    if (t < nq) { const int h = xg * 12 + t; qk_body(smem, wave_id_, xg, h, (h >> 3) * 32 + (h & 7), Wq, Wk, ws + OFF_K, ws + OFF_V, ws, tmo, tag); }
    else {
      const int u = t - nq, r = xg * 20 + u;
      cnt = cnt_of(ws, vr_body(smem, wave_id_, xg, r >> 1, r & 1, r, (r >> 4) * 32 + 8 + (r & 15), Wv, Wo, Wq, Wk, ws, tmo, tag, CPF && HB < NSEG && u < 16 ? (const char*)pc0 + u * 8192 : nullptr));
      if (HB < NSEG) hx = u < 8 ? xg * 8 + u : (u == 8 ? 24 + xg : (u == 9 && xg == 2 ? 27 : -1));
#define HPF 1
      if (HPF && HB < NSEG && u >= 10 && u < 22) {
        const int lane = lane_id(), wv = (u - 10) * 8 + wave_id_;
        if (wv >= 64) touch(w2 + (size_t)(3072 + 256 + (wv & 15) * 16) * DM + (wv >= 80 ? 3 : xg) * 256, (unsigned)((lane >> 2) * DM * 2 + (lane & 3) * 128), wave_id_ * 256);
        else {
#pragma unroll
          for (int j = 0; j < 2; ++j) touch(w2 + (size_t)(xg * 1024 + 256 + j * 128 + (wv & 32) * 16 + (wv & 31) * 4) * DM, (unsigned)lane * 128, wave_id_ * 256);
        }
        WAIT_V(0);
      }
    }
    if (hx < 0) return;
    t = 8;
  }
  const bool cx = xg == 3;
  const int xq = cx ? (slot >> 3) & 3 : xg & 3, wsl = xq * 32 + t;
#define GDEL 1200
#define GDX 1200
  if (cx || t < 8) l1_role(smem, wave_id_, cx, xq * 8 + (t & 7), x, mod, mem0, ws + OFF_H1, cnt, tag, tmo, mts);
#define KH 0
  if (KH && !cx && t >= 24) {
    const int it = (t & 7) * 8 + wave_id_, lane = lane_id();
    touch(((it & 16) ? Wk : Wv) + (size_t)(256 + (it & 8) * 64 + (it & 7) * 32) * HD + (8 * xq + 1 + 4 * (it >> 5)) * DH,
          (unsigned)((lane >> 1) * HD * 2 + (lane & 1) * 128), 155648 + wave_id_ * 256);
  }
  if (t < 8 || t >= 24) {
    while ((unsigned)__builtin_amdgcn_s_memrealtime() - tst < (cx ? GDX : GDEL)) __builtin_amdgcn_s_sleep(8);
    if (!cx && t >= 24) {
      cnt = cnt_of(ws, init_wait(ws, tag));
      mod2_job(smem, wave_id_, xq * 8 + t - 24, mod, cvec, wmod, cnt);
    }
  }
  auto u2_of = [&]() { return cx ? -1 : (t >= 13 && t < 16 ? t - 13 : (t >= 20 && t < 24 ? t - 17 : -1)); };
  auto f_of = [&]() {
    int fl = t < 8 ? t : (t < 18 ? t - 4 : (t < 20 ? t - 10 : t - 14));
    const int sa = (3 + 2 * xq) & 7, sb = sa + 8 < 14 ? sa + 8 : 13;
    if (fl == 12) fl = sa; else if (fl == sa) fl = 12;
    else if (fl == 13) fl = sb; else if (fl == sb) fl = 13;
    return (int)__builtin_amdgcn_readfirstlane(14 * xq + fl);
  };
  unsigned tk = task_sel<0>(hx >= 0 ? 99 : (cx ? t + 8 : t)), t2 = task_sel<1>(cx ? t + 8 : t), t3 = task_sel<2>(cx ? t + 8 : t);
  for (int it = 0; tk != 0; ++it, tk = t2, t2 = t3, t3 = 0) {
    const int m = tk & 3, hl = (tk >> 2) & 7, part = (tk >> 5) & 3, bo = (tk >> 8) & 255, ns = (int)(tk >> 16) - bo, h = 8 * xq + hl;
    const bool first = m != 0 && bo == 0, late = it > 0;
    const int lane = lane_id();
    const Dep d{nullptr, (unsigned)((lane < 32 ? PL1 : PL1B - 32) + lane) * 128, 8u, (unsigned)((lane < 32 ? 0 : 8) - bo)};
    if (wave_id_ == 0 && late) wait_l1(cnt + (PL1B - PL1) * 32, 7u, tmo);
    BAR();
    gemm7<R_QKV>(smem, wave_id_, (m == 0 ? Wq : (m == 1 ? Wk : Wv)) + h * DH, nullptr, HD, ws + OFF_H1 + (size_t)bo * 65536, 65536, d,
                    ws + (m == 0 ? OFF_Q : (m == 1 ? OFF_K : OFF_V)) + h * 8192 + (size_t)bo * 32 * 8192, 32 * 8192,
                    cnt, (PQKV + h) * 32 + 4 * part + m, tmo, wsl, mts, false, first ? NIMM : 0, ns, late ? ns : 0, bo * 257, tag);
  }
  __syncthreads();
  const int u2 = u2_of();
  const int f2 = hx >= 0 ? hx : (u2 < 0 ? -1 : (xq < 3 ? xq * 8 + u2 : (u2 < 3 ? u2 * 8 + 7 : 21 + u2)));
  const int fb = hx >= 0 ? HB : 0, fe = hx >= 0 ? NSEG : HB;
  if (cx || (f2 >= 0 && f2 < 24)) {
    const int ks = cx ? xq : f2 >> 3, cn = cx ? (t + 1) & 7 : f2 & 7;
    const int lane = lane_id();
    const Dep d{nullptr, (unsigned)(cx ? (PSA + 4 * xq + ((lane >> 3) & 3)) * 128 + (lane & 7) * 4 : (PF1 + 16 * ks + (lane & 15)) * 128), cx ? 4u : 1u, cx ? (unsigned)((lane >> 1) & 3) : 0u};
    gemm7<R_PART>(smem, wave_id_, (cx ? Wo : w2) + (size_t)ks * 1024 * DM + cn * 128, nullptr, DM, ws + (cx ? OFF_O : OFF_G) + ks * 16 * 4096, (cx ? 64 : 56) * 4096, d,
                  ws + (cx ? OFF_P : OFF_P2) + (ks * DM + cn * 128) * (TLP ? 64 : 2), (size_t)32 * PROW, cnt, ((cx ? PWO : PF2) + ks * 8 + cn) * 32 + (hx >= 0 ? 1 : 0), tmo,
                  hx >= 0 ? 280 + hx : 128 + xq * 32 + (cx ? cn : 22 + u2), mts, !cx, 0, cx ? NSEG : fe, 0, 0, 0, fb);
  } else if (f2 >= 0) {
    const int c4 = f2 - 24;
    const int lane = lane_id();
    const Dep d{nullptr, (unsigned)(PF1 + 48 + (lane & 7)) * 128, 1u, 0u};
    gemm7<R_PART2>(smem, wave_id_, w2 + (size_t)3072 * DM + c4 * 256, nullptr, DM, ws + OFF_G + 48 * 4096, 56 * 4096, d,
                   ws + OFF_P2 + (3 * DM + c4 * 256) * (TLP ? 64 : 2), (size_t)32 * PROW, cnt, (PF2 + 24 + c4) * 32 + (hx >= 0 ? 1 : 0), tmo, hx >= 0 ? 280 + hx : 128 + xq * 32 + 22 + u2, mts, true, 0, fe, 0, 0, 0, fb);
  } else if (t < 8 || (t >= 16 && t < 20) || t == 24 || t == 25) {
    int f = f_of();
    asm volatile("" : "+s"(f));
    const int lane = lane_id();
    const Dep d{nullptr, (unsigned)((PLN + (lane >> 3)) * 128 + (lane & 7) * 4), 1u, 0u};
    gemm7<R_F1>(smem, wave_id_, w1 + 64 * f, w3 + 64 * f, FF, ws + OFF_H2, 65536, d, ws + OFF_G + f * 4096, 56 * 4096, cnt, (PF1 + f) * 32, tmo, 128 + xq * 18 + 8 + f, mts, false);
  } else if (t >= 9 && t < 13) {
    fin_role(smem, wave_id_, xq * 4 + t - 9, mod, ws + OFF_P2, (const u16*)(ws + OFF_X1), out, cnt, tmo, mts);
    if (xq == (FL63 ? 0 : 3) && t == 12 && opaque_tid() == 0) {
      const u32x4 v = ld16_sc1(ws + OFF_TAG);
      const u64 tn = (((u64)v.y << 32) | v.x) + 1;
      st8_sc1(ws + OFF_TAG, u32x2{(unsigned)tn, (unsigned)(tn >> 32)});
    }
  } else if (t == 28 || t == 29) {
    row_role(smem, wave_id_, 8 * xq, t - 28, mod, ws + OFF_P, x, (u16*)(ws + OFF_X1), ws + OFF_H2, cnt, tmo, 256 + xq, mts);
  } else if (t >= 26) {
    sa_role(smem, wave_id_, 8 * xq + 2 * (t >= 30 ? t - 30 : t - 24), ws + OFF_Q, ws + OFF_K, ws + OFF_V, ws + OFF_KVM, ws + OFF_O, cnt, tmo, 128 + xq * 32 + 30 + (t & 1), mts);
  }
}

extern "C" void kernel_launch(void* const* d_in, const int* in_sizes, int n_in, void* d_out, int out_size, void* d_ws, size_t ws_size,
                              hipStream_t stream) {
  const u16* x    = (const u16*)d_in[0];
  const u16* mem0 = (const u16*)d_in[1];
  const u16* Wq   = (const u16*)d_in[2];
  const u16* Wk   = (const u16*)d_in[3];
  const u16* Wv   = (const u16*)d_in[4];
  const u16* Wo   = (const u16*)d_in[5];
  const u16* w1   = (const u16*)d_in[8];
  const u16* w3   = (const u16*)d_in[9];
  const u16* w2   = (const u16*)d_in[10];
  const u16* c    = (const u16*)d_in[11];
  const u16* wmod = (const u16*)d_in[12];
#define WSHIFT 0
  char* ws = (char*)d_ws + ((size_t)WSHIFT << 20);
  hipFuncSetAttribute((const void*)k_mega, hipFuncAttributeMaxDynamicSharedMemorySize, MEGA_LDS);
  k_mega<<<256, 512, MEGA_LDS, stream>>>(Wq, Wk, Wv, Wo, w1, w3, w2, x, c, wmod, mem0, ws, (u16*)d_out);
}
```

```cpp
#include <hip/hip_runtime.h>
#include <hip/hip_bf16.h>
#include <stdint.h>
#include <type_traits>
#pragma clang diagnostic ignored "-Winline-asm"
#pragma clang diagnostic ignored "-Wunused-value"

typedef unsigned short u16;
typedef short s16x4 __attribute__((ext_vector_type(4)));
typedef __bf16 bf16x8 __attribute__((ext_vector_type(8)));
typedef float f32x4 __attribute__((ext_vector_type(4)));
typedef float f32x2 __attribute__((ext_vector_type(2)));
typedef unsigned u32x4 __attribute__((ext_vector_type(4)));
typedef unsigned u32x2 __attribute__((ext_vector_type(2)));
#define LAS __attribute__((address_space(3)))

constexpr int SEQ = 2048, DM = 1024, NH = 32, DH = 128, HD = 4096, NMEM = 8, SEG = 32, NSEG = 64, FF = 3584;
constexpr unsigned SENT = 0xFFFFFFFFu;

__device__ __forceinline__ int lane_id() { int l; asm volatile("v_mbcnt_lo_u32_b32 %0, -1, 0\n\tv_mbcnt_hi_u32_b32 %0, -1, %0" : "=v"(l)); return l; }
#define WAVE_ID wave_id_
#define opaque_tid() (WAVE_ID * 64 + lane_id())
__device__ __forceinline__ float bf2f(u16 v) { return __builtin_bit_cast(float, (unsigned)v << 16); }
__device__ __forceinline__ float bflo(unsigned v) { return __builtin_bit_cast(float, v << 16); }
__device__ __forceinline__ float bfhi(unsigned v) { return __builtin_bit_cast(float, v & 0xffff0000u); }
__device__ __forceinline__ float bfr(float x) { return (float)(__bf16)x; }
__device__ __forceinline__ unsigned pack2(float a, float b) {
  typedef __bf16 bf16x2 __attribute__((ext_vector_type(2)));
  bf16x2 v; v[0] = (__bf16)a; v[1] = (__bf16)b; return __builtin_bit_cast(unsigned, v);
}
__device__ __forceinline__ unsigned nosent(unsigned v) { return v == SENT ? 0x7FC07FC0u : v; }
__device__ __forceinline__ bf16x8 asbf8(u32x4 v) { return __builtin_bit_cast(bf16x8, v); }
__device__ __forceinline__ s16x4 ass4(u32x2 v) { return __builtin_bit_cast(s16x4, v); }
__device__ __forceinline__ u32x2 trread(const char* lds_base, unsigned off) {
  s16x4 v = __builtin_amdgcn_ds_read_tr16_b64_v4i16((LAS s16x4*)(lds_base + off));
  return __builtin_bit_cast(u32x2, v);
}
__device__ __forceinline__ void glds16(const void* gsrc, unsigned lds_dst) {
  unsigned keep;
  asm volatile("s_mov_b32 %0, m0\n\ts_mov_b32 m0, %2\n\ts_nop 0\n\tglobal_load_lds_dwordx4 %1, off\n\ts_mov_b32 m0, %0"
               : "=&s"(keep) : "v"(gsrc), "s"(lds_dst) : "memory");
}
#define CNT_ " nt"
#define WNT_ " nt"
__device__ __forceinline__ void glds16_w(const void* gsrc, unsigned lds_dst) {
  unsigned keep;
  asm volatile("s_mov_b32 %0, m0\n\ts_mov_b32 m0, %2\n\ts_nop 0\n\tglobal_load_lds_dwordx4 %1, off" CNT_ "\n\ts_mov_b32 m0, %0"
               : "=&s"(keep) : "v"(gsrc), "s"(lds_dst) : "memory");
}
__device__ __forceinline__ void glds16_sc1(const void* gsrc, unsigned lds_dst) {
  unsigned keep;
  asm volatile("s_mov_b32 %0, m0\n\ts_mov_b32 m0, %2\n\ts_nop 0\n\tglobal_load_lds_dwordx4 %1, off sc1\n\ts_mov_b32 m0, %0"
               : "=&s"(keep) : "v"(gsrc), "s"(lds_dst) : "memory");
}
#define NTM 1
template <int B> __device__ __forceinline__ u32x4 ldnt(const void* p) { if (NTM & B) return __builtin_nontemporal_load((const u32x4*)p); else return *(const u32x4*)p; }
#define MFMA32(a, b, c) __builtin_amdgcn_mfma_f32_16x16x32_bf16((a), (b), (c), 0, 0, 0)

__device__ __forceinline__ void st16_sc1(void* p, u32x4 v) { asm volatile("global_store_dwordx4 %0, %1, off sc1\n\ts_nop 1" :: "v"(p), "v"(v) : "memory"); }
__device__ __forceinline__ void st8_sc1(void* p, u32x2 v) { asm volatile("global_store_dwordx2 %0, %1, off sc1\n\ts_nop 1" :: "v"(p), "v"(v) : "memory"); }
template <bool WT> __device__ __forceinline__ void st8(void* p, u32x2 v) { if (WT) st8_sc1(p, v); else *(u32x2*)p = v; }
template <bool WT> __device__ __forceinline__ void st16(void* p, u32x4 v) { if (WT) st16_sc1(p, v); else *(u32x4*)p = v; }
__device__ __forceinline__ u32x4 ld16_sc1(const void* p) {
  u32x4 r; asm volatile("global_load_dwordx4 %0, %1, off sc1\n\ts_waitcnt vmcnt(0)" : "=&v"(r) : "v"(p) : "memory"); return r;
}
__device__ __forceinline__ void ld16x4_sc1(const void* p, u32x4& a, u32x4& b, u32x4& c, u32x4& d) {
  asm volatile("global_load_dwordx4 %0, %4, off sc1\n\tglobal_load_dwordx4 %1, %4, off offset:1024 sc1\n\t"
               "global_load_dwordx4 %2, %4, off offset:2048 sc1\n\tglobal_load_dwordx4 %3, %4, off offset:3072 sc1\n\ts_waitcnt vmcnt(0)"
               : "=&v"(a), "=&v"(b), "=&v"(c), "=&v"(d) : "v"(p) : "memory");
}
typedef __amdgpu_buffer_rsrc_t rsrc_t;
__device__ __forceinline__ rsrc_t mkrs(const void* p) { return __builtin_amdgcn_make_buffer_rsrc((void*)p, 0, 0x7FFFFFFF, 0x00020000); }
__device__ __forceinline__ u32x4 bld16(rsrc_t r, unsigned off) { return __builtin_amdgcn_raw_buffer_load_b128(r, off, 0, 16); }
__device__ __forceinline__ u32x2 bld8(rsrc_t r, unsigned off) { return __builtin_amdgcn_raw_buffer_load_b64(r, off, 0, 16); }
__device__ __forceinline__ bool ok4(u32x4 v) { return v.x != SENT && v.y != SENT && v.z != SENT && v.w != SENT; }
constexpr int SPIN_CAP = 2000000;
__device__ __forceinline__ bool give_up(int spins, unsigned* tmo, unsigned code) {
  if ((spins & 4095) != 4095) return false;
  if (spins > SPIN_CAP) { __hip_atomic_store(tmo, code, __ATOMIC_RELAXED, __HIP_MEMORY_SCOPE_AGENT); return true; }
  return __hip_atomic_load(tmo, __ATOMIC_RELAXED, __HIP_MEMORY_SCOPE_AGENT) != 0u;
}
template <bool ACQ = true>
__device__ __forceinline__ void wait_cnt(unsigned* c, unsigned n, unsigned* tmo) {
  for (int spins = 0;; ++spins) {
    const unsigned v = __hip_atomic_load(c, __ATOMIC_RELAXED, __HIP_MEMORY_SCOPE_AGENT);
    if (v >= n) break;
    __builtin_amdgcn_s_sleep(8);
    if (give_up(spins, tmo, 5)) break;
  }
  if (ACQ) {
    __builtin_amdgcn_fence(__ATOMIC_ACQUIRE, "agent");
    asm volatile("s_waitcnt vmcnt(0)" ::: "memory");
  }
}

__device__ __forceinline__ const char* uni(const char* p) {
  const unsigned long long v = (unsigned long long)p;
  const unsigned lo = __builtin_amdgcn_readfirstlane((unsigned)v), hi = __builtin_amdgcn_readfirstlane((unsigned)(v >> 32));
  return (const char*)(((unsigned long long)hi << 32) | lo);
}
#define TSCLK "s_memrealtime"
#define TC(k) do { } while (0)
#define TS(k) do { } while (0)
#define TSDECL do { } while (0)
#define TSFLUSH(mask) do { } while (0)

constexpr size_t OFF_MOD  = 0;
constexpr size_t OFF_MOD2 = 65536;
constexpr size_t OFF_TMO  = 98304;
constexpr size_t OFF_TAG  = 131072;
constexpr size_t OFF_CLAIM = 135168;
constexpr size_t OFF_INIT = 196608;
constexpr size_t OFF_H1   = 262144;
constexpr size_t OFF_Q    = OFF_H1 + (size_t)SEQ * DM * 2;
constexpr size_t OFF_K    = OFF_Q + (size_t)SEQ * HD * 2;
constexpr size_t OFF_V    = OFF_K + (size_t)SEQ * HD * 2;
constexpr size_t OFF_O    = OFF_V + (size_t)SEQ * HD * 2;
constexpr size_t OFF_X1   = OFF_O + (size_t)SEQ * HD * 2;
constexpr size_t OFF_H2   = OFF_X1 + (size_t)SEQ * DM * 2;
constexpr size_t OFF_G    = OFF_H2 + (size_t)SEQ * DM * 2;
constexpr size_t OFF_KVM  = (size_t)92 << 20;
constexpr size_t OFF_XCH  = (size_t)100 << 20;
constexpr size_t OFF_P    = (size_t)112 << 20;
constexpr size_t OFF_P2   = (size_t)128 << 20;
constexpr size_t OFF_DUMP = (size_t)144 << 20;
constexpr int PROW = 4 * DM * 2;
#define TLQ 1
#define TLP 1
static_assert(OFF_G + (size_t)SEQ * FF * 2 <= OFF_KVM, "layout");
__device__ __forceinline__ char* xch_base(char* ws) { return (char*)(((size_t)(ws + OFF_XCH) + ((2u << 20) - 1)) & ~(size_t)((2u << 20) - 1)); }
#define XPAR 0
constexpr int PROG = 16;
constexpr int PQKV = 64;
constexpr int PL1 = 96;
constexpr int PL1B = 350;
constexpr int PSA = 128;
constexpr int PWO = 192;
constexpr int PLN = 224;
constexpr int PF2 = 320;
constexpr int PF1 = 448;
#define SDIR 40
#define SB 52
#define SC 60
#define MARG 20
#define MARL 15
#define MARF 8
#define LAST 52
#define HB 61
#define HC 62
#define MARX 10
#define MARH 35
#define SAW 0
constexpr int L_RED = 131072;
constexpr int L_SLOT = L_RED + 16384;
constexpr int L_GB = L_SLOT + 1024;
constexpr int MEGA_LDS = 163840;
static_assert(L_GB + 12288 + 2048 <= MEGA_LDS, "lds");
#define XPARC 0

__device__ __forceinline__ int bswz(int row) { return (((row >> 1) & 1) | (((row >> 3) & 1) << 1)) << 1; }
template <int OFF, bool SC1 = false>
__device__ __forceinline__ void glds16s(const char* sbase, unsigned voff, unsigned lds_base) {
  unsigned keep;
  if (SC1)
    asm volatile("s_mov_b32 %0, m0\n\ts_add_u32 m0, %3, %4\n\ts_nop 2\n\tglobal_load_lds_dwordx4 %1, %2 sc1\n\ts_mov_b32 m0, %0"
                 : "=&s"(keep) : "v"(voff), "s"(sbase), "s"(lds_base), "i"(OFF) : "memory", "scc");
  else
    asm volatile("s_mov_b32 %0, m0\n\ts_add_u32 m0, %3, %4\n\ts_nop 2\n\tglobal_load_lds_dwordx4 %1, %2\n\ts_mov_b32 m0, %0"
                 : "=&s"(keep) : "v"(voff), "s"(sbase), "s"(lds_base), "i"(OFF) : "memory", "scc");
}
template <int OFF>
__device__ __forceinline__ void glds16x2s(const char* sbase, unsigned voff, unsigned lds_base) {
  unsigned keep;
  asm volatile("s_mov_b32 %0, m0\n\ts_add_u32 m0, %3, %4\n\ts_nop 2\n\tglobal_load_lds_dwordx4 %1, %2 sc1\n\tglobal_load_lds_dwordx4 %1, %2 offset:1024 sc1\n\ts_mov_b32 m0, %0"
               : "=&s"(keep) : "v"(voff), "s"(sbase), "s"(lds_base), "i"(OFF) : "memory", "scc");
}
__device__ __forceinline__ void st16s(const char* sb, unsigned vo, u32x4 v) { asm volatile("global_store_dwordx4 %0, %1, %2 sc1\n\ts_nop 1" :: "v"(vo), "v"(v), "s"(sb) : "memory"); }
__device__ __forceinline__ void head_to_lds(const char* src, int lane, unsigned lds) {
  if (TLQ) {
    const int c = (lane & 15) ^ (lane >> 4);
    const unsigned v0 = (unsigned)((c >> 2) * 2048 + (lane >> 4) * 64 + (c & 3) * 16);
#pragma unroll
    for (int i = 0; i < 8; ++i) glds16s<0, true>(src + i * 256, v0 ^ ((i & 3) * 2048), lds + i * 1024);
  } else {
#pragma unroll
    for (int i = 0; i < 4; ++i) glds16x2s<0>(src + i * 2048, (unsigned)lane * 16, lds + i * 2048);
  }
}
#define TCHP ""
template <bool NT = false> __device__ __forceinline__ void touch(const void* sb, unsigned vo, unsigned lds) {
  if (NT) asm volatile("s_mov_b32 m0, %0\n\ts_nop 0\n\tglobal_load_lds_dword %1, %2 nt" :: "s"(lds), "v"(vo), "s"(sb) : "memory", "m0");
  else
  asm volatile("s_mov_b32 m0, %0\n\ts_nop 0\n\tglobal_load_lds_dword %1, %2" TCHP :: "s"(lds), "v"(vo), "s"(sb) : "memory", "m0");
}
#define WAIT_V(n) asm volatile("s_waitcnt vmcnt(" #n ")" ::: "memory")
#define BAR() do { asm volatile("s_waitcnt lgkmcnt(0)" ::: "memory"); __builtin_amdgcn_s_barrier(); asm volatile("" ::: "memory"); } while (0)
__device__ __forceinline__ void st4_sc1(void* p, unsigned v) { asm volatile("global_store_dword %0, %1, off sc1" :: "v"(p), "v"(v) : "memory"); }
__device__ __forceinline__ unsigned ld4_sc1(const void* p) {
  unsigned r; asm volatile("global_load_dword %0, %1, off sc1\n\ts_waitcnt vmcnt(0)" : "=&v"(r) : "v"(p) : "memory"); return r;
}
template <int CTRL> __device__ __forceinline__ unsigned dppu(unsigned x) { return (unsigned)__builtin_amdgcn_update_dpp(0, (int)x, CTRL, 0xf, 0xf, false); }
__device__ __forceinline__ unsigned rowmin16(unsigned v) {
  v = min(v, dppu<0x128>(v)); v = min(v, dppu<0x124>(v)); v = min(v, dppu<0x122>(v)); v = min(v, dppu<0x121>(v));
  return v;
}
__device__ __forceinline__ unsigned rdl(unsigned v, int l) { return (unsigned)__builtin_amdgcn_readlane((int)v, l); }
__device__ __forceinline__ unsigned qkv_avail(unsigned v, int m) {
  unsigned a = rdl(v, m);
#pragma unroll
  for (int j = 1; j < 4; ++j) { const unsigned p = rdl(v, 4 * j + m); if ((p >> 8) <= a) a = max(a, p & 255u); }
  return a;
}
constexpr float SCALE = 0.08838834764831845f;
constexpr float LOG2E = 1.4426950408889634f;
template <int N> __device__ __forceinline__ void delay64() {
  if (N >= 16) { __builtin_amdgcn_s_sleep(15); delay64<(N >= 16 ? N - 15 : 0)>(); } else if (N > 0) __builtin_amdgcn_s_sleep(N);
}
__device__ __forceinline__ void sw16(float x, float& a, float& b) {
  a = x; b = x; asm volatile("s_nop 1\n\tv_permlane16_swap_b32 %0, %1" : "+v"(a), "+v"(b));
}
__device__ __forceinline__ void sw32(float x, float& a, float& b) {
  a = x; b = x; asm volatile("s_nop 1\n\tv_permlane32_swap_b32 %0, %1" : "+v"(a), "+v"(b));
}
__device__ __forceinline__ float red16_32_max(float x) {
  float a, b; sw16(x, a, b); x = fmaxf(a, b); sw32(x, a, b); return fmaxf(a, b);
}
__device__ __forceinline__ float red16_32_add(float x) {
  float a, b; sw16(x, a, b); x = a + b; sw32(x, a, b); return a + b;
}
__device__ __forceinline__ unsigned cvtpk(float a, float b) { unsigned r; asm("v_cvt_pk_bf16_f32 %0, %1, %2" : "=v"(r) : "v"(a), "v"(b)); return r; }
__device__ __forceinline__ float max3(float a, float b, float c) { float r; asm("v_max3_f32 %0, %1, %2, %3" : "=v"(r) : "v"(a), "v"(b), "v"(c)); return r; }
__device__ __forceinline__ float max2(float a, float b) { float r; asm("v_max_f32 %0, %1, %2" : "=v"(r) : "v"(a), "v"(b)); return r; }
__device__ __forceinline__ unsigned ror8u(unsigned x) { return (unsigned)__builtin_amdgcn_update_dpp(0, (int)x, 0x128, 0xf, 0xf, false); }
__device__ __forceinline__ float ror8(float x) { return __builtin_bit_cast(float, ror8u(__builtin_bit_cast(unsigned, x))); }
__device__ __forceinline__ float add_ror8(float x) {
  return x + __builtin_bit_cast(float, __builtin_amdgcn_update_dpp(0, __builtin_bit_cast(int, x), 0x128, 0xf, 0xf, false));
}

__device__ __forceinline__ unsigned rt_now() { return (unsigned)__builtin_amdgcn_s_memrealtime(); }
__device__ __forceinline__ unsigned latest(unsigned v) {
  const unsigned now = rt_now() + 400u, age = rowmin16((now - v) & 0xFFFFFFu);
  return now - min(min(min(rdl(age, 0), rdl(age, 16)), min(rdl(age, 32), rdl(age, 48))), 900u);
}
__device__ __forceinline__ void wait_rt(unsigned t0, int d) {
  while ((int)(rt_now() - t0) < d) { }
}
__device__ __forceinline__ bool bells(bool ok, unsigned u, int b, int marg, int sb = SB) {
  if (b < sb || !__all(ok || (int)(u >> 24) > b)) return false;
  wait_rt(latest(!ok && (int)(u >> 24) == b + 1 ? u : rt_now() - 400u), marg);
  return true;
}
__device__ __forceinline__ void ld_fb(unsigned& v, unsigned& u, const void* sb, unsigned vo) {
  asm volatile("global_load_dword %0, %2, %3 sc1\n\tglobal_load_dword %1, %2, %3 offset:64 sc1\n\ts_waitcnt vmcnt(0)" : "=&v"(v), "=&v"(u) : "v"(vo), "s"(sb) : "memory");
}
__device__ __forceinline__ unsigned bell(int b, unsigned t) { return (unsigned)(b + 1) << 24 | (t & 0xFFFFFFu); }
typedef unsigned long long u64;
__device__ __forceinline__ unsigned tag2(u64 tag) { return (unsigned)(tag >> 32) ^ ((unsigned)tag * 0x9E3779B1u); }
__device__ __forceinline__ unsigned init_eval(u32x4 v, u64 tag) {
  if (!__all((v.x & ~63u) == (unsigned)tag << 6 && v.y == (unsigned)(tag >> 26))) return 0u;
  unsigned r = 8u;
#pragma unroll
  for (int c = 0; c < 3; ++c) r |= __popcll(__ballot(v.x & (1u << c))) > __popcll(__ballot(v.x & (8u << c))) ? 1u << c : 0u;
  return r ^ (XPAR * 3u + XPARC * 4u);
}
__device__ __forceinline__ unsigned init_wait(const char* ws, u64 tag) {
  for (int spins = 0; spins < SPIN_CAP; ++spins) {
    const unsigned r = init_eval(ld16_sc1(ws + OFF_INIT + (lane_id() & 31) * 128), tag);
    if (r) return r;
    __builtin_amdgcn_s_sleep(4);
  }
  return 8u;
}
__device__ __forceinline__ void init_issue(const char* ws, unsigned lds) { glds16s<0, true>(uni(ws + OFF_INIT), (unsigned)(lane_id() & 31) * 128u, lds); }
__device__ __forceinline__ unsigned init_get(const char* ws, u64 tag, const char* copy) {
  const unsigned r = init_eval(*(volatile LAS u32x4*)(copy + lane_id() * 16), tag);
  return r ? r : init_wait(ws, tag);
}
__device__ __forceinline__ unsigned* cnt_of(char* ws, unsigned in) { return (unsigned*)uni(xch_base(ws) + ((size_t)4 << 21) + (size_t)((in & 4u) << 11)); }
__device__ __forceinline__ void wait_l1(const unsigned* cnt, unsigned n, unsigned* tmo) {
  const int lane = lane_id();
  for (int spins = 0;; ++spins) {
    const unsigned v = ld4_sc1(cnt + (PL1 + (lane & 31)) * 32);
    if (__all(v >= n)) break;
    __builtin_amdgcn_s_sleep(8);
    if (give_up(spins, tmo, 12)) break;
  }
}
#define GKV 1
__device__ __forceinline__ void wait_k0(const unsigned* cnt, int h, unsigned* tmo) {
  for (int spins = 0;; ++spins) {
    const int l = lane_id();
    const unsigned v = ld4_sc1(cnt + (PQKV + h) * 32 + 1 + (l & 1));
    if (__all(v >= (unsigned)GKV)) break;
    __builtin_amdgcn_s_sleep(32);
    if (give_up(spins, tmo, 13)) break;
  }
}
#define WCLK(slot, b) do { } while (0)
#define WTM(slot, b, k) do { } while (0)
#define WTS(slot, b, k) do { } while (0)
#define WFB() do { } while (0)
#define STS(ws_, slot_, k_) do { } while (0)
typedef __bf16 bf16x2 __attribute__((ext_vector_type(2)));
__device__ __forceinline__ f32x2 pkfma(f32x2 a, f32x2 b, f32x2 c) { return __builtin_elementwise_fma(a, b, c); }
__device__ __forceinline__ float wavesum(float v) {
  v += __builtin_bit_cast(float, dppu<0x128>(__builtin_bit_cast(unsigned, v))); v += __builtin_bit_cast(float, dppu<0x124>(__builtin_bit_cast(unsigned, v)));
  v += __builtin_bit_cast(float, dppu<0x122>(__builtin_bit_cast(unsigned, v))); v += __builtin_bit_cast(float, dppu<0x121>(__builtin_bit_cast(unsigned, v)));
  return red16_32_add(v);
}
__device__ __forceinline__ float adds(unsigned v, unsigned sel, float c) { return __builtin_amdgcn_fdot2_f32_bf16(__builtin_bit_cast(bf16x2, v), __builtin_bit_cast(bf16x2, sel), c, false); }
__device__ __forceinline__ void sum4(const u32x4 (&p)[4], f32x2 (&a)[4]) {
  unsigned slo = 0x00003F80u, shi = 0x3F800000u;
  asm("" : "+s"(slo), "+s"(shi));
#pragma unroll
  for (int e = 0; e < 4; ++e) {
    float lo = bflo(p[0][e]), hi = bfhi(p[0][e]);
#pragma unroll
    for (int q = 1; q < 4; ++q) { lo = adds(p[q][e], slo, lo); hi = adds(p[q][e], shi, hi); }
    a[e] = f32x2{lo, hi};
  }
}
__device__ __forceinline__ u32x4 norm8(const f32x2 (&v)[4], const char* mt, float mb, float rden) {
  const f32x4 h0 = *(const f32x4*)(mt + 4096), h1 = *(const f32x4*)(mt + 4096 + 1024), c0 = *(const f32x4*)(mt + 8192), c1 = *(const f32x4*)(mt + 8192 + 1024);
  const f32x2 m2 = f32x2{mb, mb}, d2 = f32x2{rden, rden};
  const f32x2 r0 = pkfma(v[0] - m2, f32x2{c0[0], c0[1]} * d2, f32x2{h0[0], h0[1]}), r1 = pkfma(v[1] - m2, f32x2{c0[2], c0[3]} * d2, f32x2{h0[2], h0[3]});
  const f32x2 r2 = pkfma(v[2] - m2, f32x2{c1[0], c1[1]} * d2, f32x2{h1[0], h1[1]}), r3 = pkfma(v[3] - m2, f32x2{c1[2], c1[3]} * d2, f32x2{h1[2], h1[3]});
  return u32x4{pack2(r0[0], r0[1]), pack2(r1[0], r1[1]), pack2(r2[0], r2[1]), pack2(r3[0], r3[1])};
}
__device__ __forceinline__ void half_load(const char* P, const u16* res, int row, int hf, int lane, f32x2 (&a)[4], u32x4& rv) {
  const rsrc_t rp = mkrs(P), rr = mkrs(res);
  const unsigned po = TLP ? (unsigned)((row >> 5) * 32 * PROW + (hf * 16 + (lane >> 2)) * 2048 + (row & 31) * 64 + (lane & 3) * 16) : (unsigned)(row * PROW + hf * 1024 + lane * 16), ro = (unsigned)(row * DM * 2 + hf * 1024 + lane * 16);
  u32x4 p[4];
#pragma unroll
  for (int q = 0; q < 4; ++q) p[q] = bld16(rp, po + q * (TLP ? 65536 : 2048));
  if (res) rv = bld16(rr, ro);
  sum4(p, a);
}
__device__ __forceinline__ bool fin_half(bool fin, const char* P, const u16* res, const char* gbt, u16* __restrict__ o1, int row, int hf, int lane) {
  f32x2 a[4]; u32x4 rv;
  half_load(P, res, row, hf, lane, a, rv);
  const u32x4 gv = *(const u32x4*)(gbt + hf * 1024 + lane * 16);
  u32x4 o; f32x2 t = f32x2{0, 0};
#pragma unroll
  for (int e = 0; e < 4; ++e) { const f32x2 s = pkfma(f32x2{bflo(gv[e]), bfhi(gv[e])}, a[e], f32x2{bflo(rv[e]), bfhi(rv[e])}); o[e] = pack2(s[0], s[1]); t += s; }
  if (!fin && !__all(t[0] == t[0] && t[1] == t[1])) return false;
  st16_sc1(o1 + (size_t)row * DM + hf * 512 + lane * 8, o);
  return true;
}
#define LSL 2
#define FSL 2
#define GSL 2
#define XSL 2
#define DLN 10
#define LEB 64
#define MARE 25
#define LNE 1
template <int CTRL> __device__ __forceinline__ float dppf(float x) { return __builtin_bit_cast(float, dppu<CTRL>(__builtin_bit_cast(unsigned, x))); }
__device__ __forceinline__ bool ln_row(bool fin, int bb, unsigned* bl, const f32x2 (&a)[2][4], const f32x2 (&xf)[8], const f32x2 (&tg)[8], const f32x2 (&th)[8], const f32x2 (&tc)[8],
                                       const char* x1, const char* h2, unsigned xo, unsigned ho) {
  f32x2 v[8]; u32x4 o[2];
#pragma unroll
  for (int e = 0; e < 8; ++e) { const f32x2 t = pkfma(tg[e], a[e >> 2][e & 3], xf[e]); const unsigned u = pack2(t[0], t[1]); o[e >> 2][e & 3] = u; v[e] = f32x2{bflo(u), bfhi(u)}; }
  const float pv = __builtin_bit_cast(float, __builtin_amdgcn_readfirstlane(__float_as_int(v[0][0])));
  f32x2 d[8];
#pragma unroll
  for (int e = 0; e < 8; ++e) d[e] = v[e] - f32x2{pv, pv};
  const f32x2 s2 = ((d[0] + d[1]) + (d[2] + d[3])) + ((d[4] + d[5]) + (d[6] + d[7]));
  const f32x2 q2 = pkfma(d[3], d[3], pkfma(d[2], d[2], pkfma(d[1], d[1], d[0] * d[0]))) + pkfma(d[7], d[7], pkfma(d[6], d[6], pkfma(d[5], d[5], d[4] * d[4])));
  float s = s2[0] + s2[1], q = q2[0] + q2[1];
  if (!fin && !__all(s == s)) return false;
  if (LNE && bb >= SB && bl && lane_id() == 0) st4_sc1(bl, bell(bb, rt_now() + DLN + 30));
  s += dppf<0x128>(s); q += dppf<0x128>(q); s += dppf<0x124>(s); q += dppf<0x124>(q);
  s += dppf<0x122>(s); q += dppf<0x122>(q); s += dppf<0x121>(s); q += dppf<0x121>(q);
  asm volatile("s_nop 1\n\tv_permlane16_swap_b32 %0, %1" : "+v"(s), "+v"(q));
  s += q;
  { float x, y; sw32(s, x, y); s = x + y; }
  const float sh = __builtin_bit_cast(float, rdl(__builtin_bit_cast(unsigned, s), 0)) * (1.0f / DM), sq = __builtin_bit_cast(float, rdl(__builtin_bit_cast(unsigned, s), 16));
  const float mb = bfr(pv + sh), vb = bfr(fmaxf(sq * (1.0f / DM) - sh * sh, 0.0f));
  const float rden = __builtin_amdgcn_rcpf(bfr(__builtin_amdgcn_sqrtf(bfr(vb + 1e-5f))));
#pragma unroll
  for (int i = 0; i < 2; ++i) {
    u32x4 r;
#pragma unroll
    for (int e = 0; e < 4; ++e) { const f32x2 t = pkfma(v[4 * i + e] - f32x2{mb, mb}, tc[4 * i + e] * f32x2{rden, rden}, th[4 * i + e]); r[e] = pack2(t[0], t[1]); }
    st16s(h2, ho + i * 32768, r);
  }
  if (!LNE && bb >= SB && lane_id() == 0) st4_sc1(bl, bell(bb, rt_now() + DLN));
  st16s(x1, xo, o[0]); st16s(x1, xo + 1024, o[1]);
  return true;
}
__device__ __forceinline__ void row_table(char* tab, const float* mod, const unsigned* cnt, unsigned* tmo, int tid) {
  wait_l1(cnt + 1, 1u, tmo);
  const rsrc_t rm = mkrs((const char*)mod + OFF_MOD2);
#pragma unroll
  for (int i = 0; i < 2; ++i) {
    const int u = tid + 512 * i, a = u >> 8, c = u & 255;
    f32x4 v = __builtin_bit_cast(f32x4, bld16(rm, (unsigned)u * 16));
    v = f32x4{bfr(v[0]), bfr(v[1]), bfr(v[2]), bfr(v[3])};
    if (a == 2) v = f32x4{bfr(1.0f + v[0]), bfr(1.0f + v[1]), bfr(1.0f + v[2]), bfr(1.0f + v[3])};
    if (a < 3) *(f32x4*)(tab + a * 4096 + ((c >> 7) * 2 + (c & 1)) * 1024 + ((c >> 1) & 63) * 16) = v;
    else *(u32x2*)(tab + 12288 + c * 8) = u32x2{pack2(v[0], v[1]), pack2(v[2], v[3])};
  }
  __syncthreads();
}
__device__ __forceinline__ void row_role(char* smem, int wave_id_, int rbase, int par, const float* mod, const char* P, const u16* x, u16* x1, char* h2c,
                                         unsigned* cnt, unsigned* tmo, int slot, unsigned long long* mts) {
  const int tid = opaque_tid(), lane = tid & 63, wave = WAVE_ID;
  if (tid < 80) *(volatile LAS unsigned*)(smem + 16384 + tid * 4) = 0u;
  {
    static_assert(TLP && SB % 2 == 0, "");
    unsigned z = SENT; asm volatile("" : "+v"(z));
    const int r4 = rbase + 4 * par + (wave & 3);
    for (int bb = SB + (wave >> 2); bb < NSEG; bb += 2)
#pragma unroll
      for (int i = 0; i < 2; ++i) {
        st16s(uni((const char*)x1 + (size_t)bb * 65536), (unsigned)(r4 * DM * 2 + i * 1024 + lane * 16), u32x4{z, z, z, z});
        st16s(uni(h2c + (size_t)bb * 65536), (unsigned)(r4 * 128 + (lane & 7) * 16 + (lane >> 3) * 4096 + i * 32768), u32x4{z, z, z, z});
      }
    WAIT_V(0);
  }
  row_table(smem, mod, cnt, tmo, tid);
  const int rb = rbase + wave + 32 * par;
  const rsrc_t rp = mkrs(P), rr = mkrs(x);
  const unsigned l16 = lane * 16;
  const unsigned pwo = (unsigned)(PWO + (lane & 31)) * 128;
  unsigned* myp = cnt + (PLN + (rb >> 3)) * 32 + wave;
  f32x2 a[2][4];
  const int gp = wave >> 2, r4 = rbase + 4 * par + (wave & 3);
  volatile LAS unsigned* lwb = (volatile LAS unsigned*)(smem + 16384 + gp * 8);
  if (gp && lane == 0) st4_sc1(myp, 1u);
  f32x2 tg[8], th[8], tc[8];
#pragma unroll
  for (int i = 0; i < 4; ++i) {
    const char* mt = smem + lane * 16 + i * 1024;
    const f32x4 g_ = *(const f32x4*)mt, h_ = *(const f32x4*)(mt + 4096), c_ = *(const f32x4*)(mt + 8192);
    tg[2 * i] = f32x2{g_[0], g_[1]}; tg[2 * i + 1] = f32x2{g_[2], g_[3]}; th[2 * i] = f32x2{h_[0], h_[1]}; th[2 * i + 1] = f32x2{h_[2], h_[3]};
    tc[2 * i] = f32x2{c_[0], c_[1]}; tc[2 * i + 1] = f32x2{c_[2], c_[3]};
  }
  const unsigned xo = (unsigned)(r4 * DM * 2 + lane * 16), ho = (unsigned)(r4 * 128 + (((lane & 7) ^ (r4 & 7)) << 4) + (lane >> 3) * 4096);
  const unsigned po = (l16 >> 6) * 2048 + (l16 & 48);
  u32x4 rv[2];
#pragma unroll
  for (int i = 0; i < 2; ++i) rv[i] = __builtin_amdgcn_raw_buffer_load_b128(rr, xo + i * 1024, (unsigned)gp * 65536u, 0);
  unsigned wb = 0, d0 = 150, d1 = 150;
  for (int bb = gp; bb < NSEG; bb += 2) {
    f32x2 xf[8];
#pragma unroll
    for (int e = 0; e < 8; ++e) xf[e] = f32x2{bflo(rv[e >> 2][e & 3]), bfhi(rv[e >> 2][e & 3])};
    asm volatile("" : "+v"(xf[0]), "+v"(xf[1]), "+v"(xf[2]), "+v"(xf[3]), "+v"(xf[4]), "+v"(xf[5]), "+v"(xf[6]), "+v"(xf[7]));
    if (wb <= (unsigned)bb) {
      if ((wave & 3) == 0) {
        wb = (unsigned)(bb + 1);
        for (int spins = 0;; ++spins) {
          unsigned v, u;
          ld_fb(v, u, cnt, pwo);
          if (__all(v > (unsigned)bb + 2u)) { v = rowmin16(v); wb = min(rdl(v, 0), rdl(v, 16)); break; }
          if (__all(v > (unsigned)bb)) break;
          if (bb >= LEB && __all(v > (unsigned)bb || (u >> 24) > (unsigned)bb)) {
            const unsigned now = rt_now(), g0 = latest(v <= (unsigned)bb && (u >> 24) == (unsigned)bb + 1u ? u : now - 400u) + MARE, go = (int)(g0 - now) > 0 ? g0 : now;
            if (lane < 4) st4_sc1(myp + 16 + lane, bell(bb, go + max(d0, d1)));
            wait_rt(go, 0);
            wb |= 256u;
            break;
          }
          if (bells(v > (unsigned)bb, u, bb, MARL)) break;
          __builtin_amdgcn_s_sleep(LSL);
          if (give_up(spins, tmo, 11)) break;
        }
        lwb[0] = wb;
      } else {
        while (((wb = __builtin_amdgcn_readfirstlane(lwb[0])) & 255u) <= (unsigned)bb) __builtin_amdgcn_s_sleep(1);
      }
    }
    const bool rung = wb == (unsigned)bb + 257u;
    wb &= 255u;
    const unsigned t0 = rt_now();
    WTM(slot, bb, 0);
    bool fin = bb < SB;
    for (int spins = 0;; ++spins) {
      u32x4 pp[2][4];
#pragma unroll
      for (int i = 0; i < 2; ++i)
#pragma unroll
        for (int q = 0; q < 4; ++q) pp[i][q] = __builtin_amdgcn_raw_buffer_load_b128(rp, po + i * 32768, (unsigned)(bb * 32 * PROW + q * 65536 + r4 * 64), 16);
      sum4(pp[0], a[0]); sum4(pp[1], a[1]);
      if (ln_row(fin, bb, rung && spins == 0 ? nullptr : myp + 16, a, xf, tg, th, tc, uni((const char*)x1 + (size_t)bb * 65536), uni(h2c + (size_t)bb * 65536), xo, ho)) break;
      if (spins > 1) fin = __all(ld4_sc1((const char*)cnt + pwo) > (unsigned)bb) || give_up(spins, tmo, 11);
    }
    d1 = d0; d0 = min(rt_now() - t0 + 12u, 200u);
    WTM(slot, bb, 3);
#pragma unroll
    for (int i = 0; i < 2; ++i) rv[i] = __builtin_amdgcn_raw_buffer_load_b128(rr, xo + i * 1024, (unsigned)min(bb + 2, NSEG - 1) * 65536u, 0);
    WAIT_V(2);
    unsigned o = 0;
    if (lane == 0) o = __hip_atomic_fetch_add((LAS unsigned*)(smem + 16448 + bb * 4), 1u, __ATOMIC_RELAXED, __HIP_MEMORY_SCOPE_WORKGROUP);
    if (__builtin_amdgcn_readfirstlane(o) == 3u) {
      if (lane < 4) st4_sc1(myp - (wave & 3) + lane, (unsigned)(bb + 2));
      WTM(slot, bb, 2);
    }
  }
}
#define NCS 8
__device__ __forceinline__ void l1_role(char* smem, int wave_id_, bool fst, int c, const u16* __restrict__ x, const float* __restrict__ mod,
                                        const u16* __restrict__ mem0, char* __restrict__ h1c, unsigned*& cnt, u64 tag, unsigned* tmo, unsigned long long* mts) {
  const int tid = opaque_tid(), lane = tid & 63, wave = WAVE_ID;
  char* ws = (char*)mod - OFF_MOD;
  u32x4 xr[7][2];
  const int row0 = (fst ? 0 : 256) + 8 * c + wave;
  const u16* src = x + (size_t)row0 * DM + lane * 8;
  xr[0][0] = ldnt<2>(src); xr[0][1] = ldnt<2>(src + 512);
  volatile LAS unsigned* sg = (volatile LAS unsigned*)(smem + L_SLOT + 784);
  if (!fst) {
#pragma unroll
    for (int pp = 1; pp < 7; ++pp) { xr[pp][0] = ldnt<2>(src + (size_t)pp * 256 * DM); xr[pp][1] = ldnt<2>(src + (size_t)pp * 256 * DM + 512); }
  } else if ((wave & 3) != 3) {
    char* cal = xch_base(ws) + ((size_t)((wave & 3) == 0 ? 0 : (wave & 3) + 2) << 21) + (124 + (wave >> 2) * 2 + (c >> 4)) * 16384 + (c & 15) * 512;
    st16_sc1(cal + (lane >> 5) * 8192 + (lane & 31) * 16, u32x4{SENT, SENT, SENT, SENT});
    WAIT_V(0);
    unsigned best[2] = {1u << 30, 1u << 30};
    const char* sb = uni(cal);
#pragma unroll
    for (int r = 1; r < NCS; ++r) {
      unsigned long long t0, t1; unsigned v;
      asm volatile("s_memtime %0\n\ts_waitcnt lgkmcnt(0)\n\ts_load_dword %2, %3, %4 glc\n\ts_waitcnt lgkmcnt(0)\n\ts_memtime %1\n\ts_waitcnt lgkmcnt(0)"
                   : "=&s"(t0), "=&s"(t1), "=&s"(v) : "s"(sb), "i"((r & 1) * 8192 + (r >> 1) * 128) : "memory");
      if (r > 1) best[r & 1] = min(best[r & 1], (unsigned)(t1 - t0));
    }
    sg[2 * wave] = best[0]; sg[2 * wave + 1] = best[1];
  }
  BAR();
  unsigned votes = 0;
  if (fst && wave == 7) {
    unsigned xcc; asm volatile("s_getreg_b32 %0, hwreg(HW_REG_XCC_ID)" : "=s"(xcc));
#pragma unroll
    for (int i = 0; i < 3; ++i) {
      const unsigned b0 = min(sg[2 * i], sg[2 * i + 8]), b1 = min(sg[2 * i + 1], sg[2 * i + 9]), df = max(b0, b1) - min(b0, b1);
      votes |= df < 110u || df > 400u ? 0u : (((b1 < b0) != ((xcc & 4) != 0)) ? 1u : 8u) << i;
    }
  }
  if (!fst || wave >= 5) {
  } else if (wave < 3) {
    const int b = c + 32 * wave;
    if (b < 89) {
      const char* p = uni(xch_base(ws) + (b < 48 ? (size_t)((b >> 4) * 32 + (b & 15)) * 16384
                                        : b < 56 ? ((size_t)3 << 21) + (size_t)(8 + ((b - 48) >> 2) * 8 + (b & 3)) * 16384
                                        : b < 84 ? ((size_t)3 << 21) + (size_t)(b - 56 + 24) * 16384 : ((size_t)4 << 21) + (size_t)(b - 84) * 16384));
      const unsigned v = b < 80 ? SENT : 0u;
#pragma unroll
      for (int i = 0; i < 16; ++i) st16s(p, (unsigned)(i * 1024 + lane * 16), u32x4{v, v, v, v});
    } else if (b == 89 && lane == 0) st4_sc1(tmo, 0u);
    WAIT_V(0);
  } else {
    const int pc = (c & 7) * 2 + (wave & 1), g = lane >> 4, hf = (lane >> 2) & 1, row = ((lane >> 3) & 1) * 4 + (lane & 3);
    u32x4 v = *(const u32x4*)(mem0 + row * DM + 32 * (2 * pc + hf) + 8 * g);
    v.x = nosent(v.x); v.y = nosent(v.y); v.z = nosent(v.z); v.w = nosent(v.w);
    st16_sc1(xch_base(ws) + ((size_t)3 << 21) + (size_t)(((c >> 3) & 1) * 2 + (pc >> 3)) * 16384 + (c >> 4) * 8192 + (pc & 7) * 1024 + lane * 16, v);
    WAIT_V(0);
  }
  BAR();
  if (fst && tid == 448) st8_sc1(ws + OFF_INIT + c * 128, u32x2{(unsigned)tag << 6 | votes, (unsigned)(tag >> 26)});
  if (fst && wave == 0) STS(ws, 128 + (c >> 3) * 32 + 24 + (c & 7), 2);
  {
    f32x4 v;
    for (int spins = 0; spins < SPIN_CAP; ++spins) {
      u32x4 m[4];
      asm volatile("s_nop 4\n\tglobal_load_dwordx4 %0, %4, %6 sc1\n\tglobal_load_dwordx4 %1, %4, %6 offset:16 sc1\n\t"
                   "global_load_dwordx4 %2, %5, %6 sc1\n\tglobal_load_dwordx4 %3, %5, %6 offset:16 sc1\n\ts_waitcnt vmcnt(0)"
                   : "=&v"(m[0]), "=&v"(m[1]), "=&v"(m[2]), "=&v"(m[3]) : "v"((unsigned)tid * 32), "v"((unsigned)tid * 32 + 16384), "s"(mod) : "memory");
      bool ok = true;
#pragma unroll
      for (int i = 0; i < 4; ++i) ok = ok && m[i].y == (unsigned)tag && m[i].w == tag2(tag);
      v = f32x4{__uint_as_float(m[0].x) + __uint_as_float(m[2].x), __uint_as_float(m[0].z) + __uint_as_float(m[2].z),
                __uint_as_float(m[1].x) + __uint_as_float(m[3].x), __uint_as_float(m[1].z) + __uint_as_float(m[3].z)};
      if (__all(ok)) break;
      __builtin_amdgcn_s_sleep(4);
    }
    v = f32x4{bfr(v[0]), bfr(v[1]), bfr(v[2]), bfr(v[3])};
    const int a = tid >> 8, cc = tid & 255;
    if (a) v = f32x4{bfr(1.0f + v[0]), bfr(1.0f + v[1]), bfr(1.0f + v[2]), bfr(1.0f + v[3])};
    *(f32x4*)(smem + 4096 + a * 4096 + ((cc >> 7) * 2 + (cc & 1)) * 1024 + ((cc >> 1) & 63) * 16) = v;
  }
  BAR();
  if (fst && wave == 0) STS(ws, 128 + (c >> 3) * 32 + 24 + (c & 7), 3);
#pragma unroll
  for (int p = 0; p < 7; ++p) {
    if (p > 0 && fst) break;
    f32x2 v[2][4];
#pragma unroll
    for (int i = 0; i < 2; ++i)
#pragma unroll
      for (int e = 0; e < 4; ++e) v[i][e] = f32x2{bflo(xr[p][i][e]), bfhi(xr[p][i][e])};
    const f32x2 s2 = ((v[0][0] + v[0][1]) + (v[0][2] + v[0][3])) + ((v[1][0] + v[1][1]) + (v[1][2] + v[1][3]));
    const float mean = wavesum(s2[0] + s2[1]) * (1.0f / DM);
    f32x2 q2 = f32x2{0, 0};
#pragma unroll
    for (int i = 0; i < 8; ++i) { const f32x2 d = v[i >> 2][i & 3] - f32x2{mean, mean}; q2 = pkfma(d, d, q2); }
    const float q = wavesum(q2[0] + q2[1]);
    if (p == 0 && fst && wave == 0) STS((char*)tmo - OFF_TMO, 128 + (c >> 3) * 32 + 24 + (c & 7), 5);
    const float mb = bfr(mean), vb = bfr(q * (1.0f / DM));
    const float rden = 1.0f / bfr(sqrtf(bfr(vb + 1e-5f)));
    const int row = 256 * p + row0, rb = row & 31;
    char* dst = h1c + (size_t)(row >> 5) * 65536 + (lane >> 3) * 4096 + rb * 128 + (((lane & 7) ^ (rb & 7)) << 4);
    const char* ml = smem + lane * 16;
#pragma unroll
    for (int i = 0; i < 2; ++i) st16_sc1(dst + i * 32768, norm8(v[i], ml + i * 2048, mb, rden));
  }
  cnt = cnt_of(ws, init_wait(ws, tag));
  WAIT_V(0);
  __builtin_amdgcn_s_barrier();
  if (tid == 0) st4_sc1(cnt + ((fst ? PL1 : PL1B) + c) * 32, fst ? 8u : 7u);
  if (wave == 0) STS(ws, 128 + (c >> 3) * 32 + 24 + (c & 7), fst ? 6 : 7);
}
__device__ __forceinline__ void mod2_job(char* smem, int wave_id_, int c, const float* __restrict__ mod, const u16* __restrict__ cvec, const u16* __restrict__ wmod, unsigned* cnt) {
  const int tid = opaque_tid();
  {
    const float sva = bf2f(cvec[tid]), svb = bf2f(cvec[tid + 512]);
    float* sv = (float*)(smem + 12288);
    sv[tid] = bfr(sva / (1.0f + __expf(-sva))); sv[tid + 512] = bfr(svb / (1.0f + __expf(-svb)));
  }
  __syncthreads();
  {
    const int cg = tid & 15, r0 = tid >> 4;
    const u16* wp = wmod + (size_t)r0 * (6 * DM) + 2 * DM + 128 * c + cg * 8;
    const float* sv = (const float*)(smem + 12288) + r0;
    f32x2 acc[4] = {f32x2{0, 0}, f32x2{0, 0}, f32x2{0, 0}, f32x2{0, 0}};
#pragma unroll 1
    for (int o = 0; o < 4; ++o) {
      u32x4 w[8];
#pragma unroll
      for (int i = 0; i < 8; ++i) w[i] = ldnt<1>(wp + (size_t)(o * 8 + i) * 32 * (6 * DM));
#pragma unroll
      for (int i = 0; i < 8; ++i) {
        const float s = sv[(o * 8 + i) * 32];
#pragma unroll
        for (int e = 0; e < 4; ++e) acc[e] = pkfma(f32x2{s, s}, f32x2{bflo(w[i][e]), bfhi(w[i][e])}, acc[e]);
      }
    }
    float* red = (float*)(smem + 16384);
    *(f32x4*)(red + r0 * 128 + cg * 8) = f32x4{acc[0][0], acc[0][1], acc[1][0], acc[1][1]};
    *(f32x4*)(red + r0 * 128 + cg * 8 + 4) = f32x4{acc[2][0], acc[2][1], acc[3][0], acc[3][1]};
    __syncthreads();
    if (tid < 128) {
      float s = 0;
#pragma unroll
      for (int r = 0; r < 32; ++r) s += red[r * 128 + tid];
      st4_sc1((char*)mod + OFF_MOD2 + (128 * c + tid) * 4, __builtin_bit_cast(unsigned, s));
    }
    WAIT_V(0);
    __builtin_amdgcn_s_barrier();
    if (tid == 0) st4_sc1(cnt + (PL1 + c) * 32 + 1, 1u);
  }
}
enum { R_QKV = 0, R_PART = 1, R_F1 = 2, R_PART2 = 3 };
struct Dep {
  const char* base; unsigned voff; unsigned mul, add;
};
__device__ __forceinline__ unsigned dep_eval(unsigned v, const Dep& d) {
  v = rowmin16(v * d.mul + (d.add + 64u));
  return min(min(rdl(v, 0), rdl(v, 16)), min(rdl(v, 32), rdl(v, 48))) - 64u;
}
typedef unsigned u32x32 __attribute__((ext_vector_type(32)));
typedef unsigned u32x16 __attribute__((ext_vector_type(16)));
__device__ __forceinline__ void fast_ld0(const u32x32 (&W)[4], u32x16& pd, unsigned ea, unsigned da, int& b, int& lf, int cp, unsigned up, unsigned sa, unsigned mu, unsigned ad,
    unsigned rw, unsigned ab, const char* p1, const char* p0, unsigned l1, unsigned l0, const unsigned (&av)[8], int la) {
  asm volatile("s_setprio 0\n\t"
    "s_cmp_eq_u32 %[la], 3\n\t"
    "s_waitcnt vmcnt(12)\n\t"
    "s_cbranch_scc1 20f\n\t"
    "s_waitcnt vmcnt(4)\n\t"
    "20:\n\t"
    "s_barrier\n\t"
    "ds_read_b128 v[96:99], %[ea] offset:0\n\t"
    "ds_read_b128 v[100:103], %[ea] offset:2048\n\t"
    "ds_read_b128 v[104:107], %[da] offset:0\n\t"
    "ds_read_b128 v[108:111], %[da] offset:2048\n\t"
    "ds_read_b128 v[112:115], %[ea] offset:4096\n\t"
    "ds_read_b128 v[116:119], %[ea] offset:6144\n\t"
    "s_branch 2f\n\t"
    ".p2align 6\n\t"
    "1:\n\t"
    "s_cmp_eq_u32 %[la], 3\n\t"
    "s_waitcnt vmcnt(12)\n\t"
    "s_cbranch_scc1 21f\n\t"
    "s_waitcnt vmcnt(4)\n\t"
    "21:\n\t"
    "s_barrier\n\t"
    "2:\n\t"
    "s_waitcnt lgkmcnt(4)\n\t"
    "v_mfma_f32_16x16x32_bf16 v[64:67], v[128:131], v[96:99], 0\n\t"
    "ds_read_b128 v[120:123], %[da] offset:4096\n\t"
    "s_add_u32 m0, %[l1], 0\n\t"
    "ds_write_b128 %[rw], v[80:83] offset:0\n\t"
    "v_mfma_f32_16x16x32_bf16 v[68:71], v[132:135], v[96:99], 0\n\t"
    "ds_read_b128 v[124:127], %[da] offset:6144\n\t"
    "global_load_lds_dwordx4 %[av0], s[36:37] sc1\n\t"
    "ds_write_b128 %[rw], v[84:87] offset:1024\n\t"
    "v_mfma_f32_16x16x32_bf16 v[72:75], v[128:131], v[100:103], 0\n\t"
    "s_add_u32 m0, %[l1], 32768\n\t"
    "ds_write_b128 %[rw], v[88:91] offset:2048\n\t"
    "v_mfma_f32_16x16x32_bf16 v[76:79], v[132:135], v[100:103], 0\n\t"
    "global_load_lds_dwordx4 %[av1], s[36:37] sc1\n\t"
    "ds_write_b128 %[rw], v[92:95] offset:3072\n\t"
    "s_waitcnt lgkmcnt(8)\n\t"
    "v_mfma_f32_16x16x32_bf16 v[64:67], v[136:139], v[104:107], v[64:67]\n\t"
    "ds_read_b128 v[96:99], %[ea] offset:8192\n\t"
    "s_add_u32 m0, %[l1], 4096\n\t"
    "v_mfma_f32_16x16x32_bf16 v[68:71], v[140:143], v[104:107], v[68:71]\n\t"
    "ds_read_b128 v[100:103], %[ea] offset:10240\n\t"
    "global_load_lds_dwordx4 %[av2], s[36:37] sc1\n\t"
    "v_mfma_f32_16x16x32_bf16 v[72:75], v[136:139], v[108:111], v[72:75]\n\t"
    "s_add_u32 m0, %[l1], 36864\n\t"
    "v_mfma_f32_16x16x32_bf16 v[76:79], v[140:143], v[108:111], v[76:79]\n\t"
    "global_load_lds_dwordx4 %[av3], s[36:37] sc1\n\t"
    "s_waitcnt lgkmcnt(8)\n\t"
    "v_mfma_f32_16x16x32_bf16 v[64:67], v[144:147], v[112:115], v[64:67]\n\t"
    "ds_read_b128 v[104:107], %[da] offset:8192\n\t"
    "s_add_u32 m0, %[l1], 8192\n\t"
    "v_mfma_f32_16x16x32_bf16 v[68:71], v[148:151], v[112:115], v[68:71]\n\t"
    "ds_read_b128 v[108:111], %[da] offset:10240\n\t"
    "global_load_lds_dwordx4 %[av4], s[36:37] sc1\n\t"
    "v_mfma_f32_16x16x32_bf16 v[72:75], v[144:147], v[116:119], v[72:75]\n\t"
    "s_add_u32 m0, %[l1], 40960\n\t"
    "v_mfma_f32_16x16x32_bf16 v[76:79], v[148:151], v[116:119], v[76:79]\n\t"
    "global_load_lds_dwordx4 %[av5], s[36:37] sc1\n\t"
    "s_waitcnt lgkmcnt(7)\n\t"
    "v_mfma_f32_16x16x32_bf16 v[64:67], v[152:155], v[120:123], v[64:67]\n\t"
    "ds_read_b128 v[112:115], %[ea] offset:12288\n\t"
    "s_add_u32 m0, %[l1], 12288\n\t"
    "v_mfma_f32_16x16x32_bf16 v[68:71], v[156:159], v[120:123], v[68:71]\n\t"
    "ds_read_b128 v[116:119], %[ea] offset:14336\n\t"
    "global_load_lds_dwordx4 %[av6], s[36:37] sc1\n\t"
    "v_mfma_f32_16x16x32_bf16 v[72:75], v[152:155], v[124:127], v[72:75]\n\t"
    "s_add_u32 m0, %[l1], 45056\n\t"
    "v_mfma_f32_16x16x32_bf16 v[76:79], v[156:159], v[124:127], v[76:79]\n\t"
    "global_load_lds_dwordx4 %[av7], s[36:37] sc1\n\t"
    "s_waitcnt lgkmcnt(4)\n\t"
    "v_mfma_f32_16x16x32_bf16 v[64:67], v[160:163], v[96:99], v[64:67]\n\t"
    "ds_read_b128 v[120:123], %[da] offset:12288\n\t"
    "v_mfma_f32_16x16x32_bf16 v[68:71], v[164:167], v[96:99], v[68:71]\n\t"
    "ds_read_b128 v[124:127], %[da] offset:14336\n\t"
    "v_mfma_f32_16x16x32_bf16 v[72:75], v[160:163], v[100:103], v[72:75]\n\t"
    "v_mfma_f32_16x16x32_bf16 v[76:79], v[164:167], v[100:103], v[76:79]\n\t"
    "s_waitcnt lgkmcnt(4)\n\t"
    "v_mfma_f32_16x16x32_bf16 v[64:67], v[168:171], v[104:107], v[64:67]\n\t"
    "ds_read_b128 v[96:99], %[ea] offset:16384\n\t"
    "v_mfma_f32_16x16x32_bf16 v[68:71], v[172:175], v[104:107], v[68:71]\n\t"
    "ds_read_b128 v[100:103], %[ea] offset:18432\n\t"
    "v_mfma_f32_16x16x32_bf16 v[72:75], v[168:171], v[108:111], v[72:75]\n\t"
    "v_mfma_f32_16x16x32_bf16 v[76:79], v[172:175], v[108:111], v[76:79]\n\t"
    "s_waitcnt lgkmcnt(4)\n\t"
    "v_mfma_f32_16x16x32_bf16 v[64:67], v[176:179], v[112:115], v[64:67]\n\t"
    "ds_read_b128 v[104:107], %[da] offset:16384\n\t"
    "v_mfma_f32_16x16x32_bf16 v[68:71], v[180:183], v[112:115], v[68:71]\n\t"
    "ds_read_b128 v[108:111], %[da] offset:18432\n\t"
    "v_mfma_f32_16x16x32_bf16 v[72:75], v[176:179], v[116:119], v[72:75]\n\t"
    "v_mfma_f32_16x16x32_bf16 v[76:79], v[180:183], v[116:119], v[76:79]\n\t"
    "s_waitcnt lgkmcnt(4)\n\t"
    "v_mfma_f32_16x16x32_bf16 v[64:67], v[184:187], v[120:123], v[64:67]\n\t"
    "ds_read_b128 v[112:115], %[ea] offset:20480\n\t"
    "v_mfma_f32_16x16x32_bf16 v[68:71], v[188:191], v[120:123], v[68:71]\n\t"
    "ds_read_b128 v[116:119], %[ea] offset:22528\n\t"
    "v_mfma_f32_16x16x32_bf16 v[72:75], v[184:187], v[124:127], v[72:75]\n\t"
    "v_mfma_f32_16x16x32_bf16 v[76:79], v[188:191], v[124:127], v[76:79]\n\t"
    "s_cmp_eq_u32 %[la], 3\n\t"
    "s_waitcnt vmcnt(12)\n\t"
    "s_cbranch_scc1 22f\n\t"
    "s_waitcnt vmcnt(4)\n\t"
    "22:\n\t"
    "s_barrier\n\t"
    "s_waitcnt lgkmcnt(4)\n\t"
    "v_mfma_f32_16x16x32_bf16 v[64:67], v[192:195], v[96:99], v[64:67]\n\t"
    "ds_read_b128 v[120:123], %[da] offset:20480\n\t"
    "s_add_u32 m0, %[l0], 0\n\t"
    "v_mfma_f32_16x16x32_bf16 v[68:71], v[196:199], v[96:99], v[68:71]\n\t"
    "ds_read_b128 v[124:127], %[da] offset:22528\n\t"
    "global_load_lds_dwordx4 %[av0], s[38:39] sc1\n\t"
    "v_mfma_f32_16x16x32_bf16 v[72:75], v[192:195], v[100:103], v[72:75]\n\t"
    "s_add_u32 m0, %[l0], 32768\n\t"
    "v_mfma_f32_16x16x32_bf16 v[76:79], v[196:199], v[100:103], v[76:79]\n\t"
    "global_load_lds_dwordx4 %[av1], s[38:39] sc1\n\t"
    "s_waitcnt lgkmcnt(4)\n\t"
    "v_mfma_f32_16x16x32_bf16 v[64:67], v[200:203], v[104:107], v[64:67]\n\t"
    "ds_read_b128 v[96:99], %[ea] offset:24576\n\t"
    "s_add_u32 m0, %[l0], 4096\n\t"
    "v_mfma_f32_16x16x32_bf16 v[68:71], v[204:207], v[104:107], v[68:71]\n\t"
    "ds_read_b128 v[100:103], %[ea] offset:26624\n\t"
    "global_load_lds_dwordx4 %[av2], s[38:39] sc1\n\t"
    "v_mfma_f32_16x16x32_bf16 v[72:75], v[200:203], v[108:111], v[72:75]\n\t"
    "s_add_u32 m0, %[l0], 36864\n\t"
    "v_mfma_f32_16x16x32_bf16 v[76:79], v[204:207], v[108:111], v[76:79]\n\t"
    "global_load_lds_dwordx4 %[av3], s[38:39] sc1\n\t"
    "s_add_u32 s36, s36, %[ab]\n\t"
    "s_addc_u32 s37, s37, 0\n\t"
    "s_waitcnt lgkmcnt(4)\n\t"
    "v_mfma_f32_16x16x32_bf16 v[64:67], v[208:211], v[112:115], v[64:67]\n\t"
    "ds_read_b128 v[104:107], %[da] offset:24576\n\t"
    "s_add_u32 m0, %[l0], 8192\n\t"
    "v_mfma_f32_16x16x32_bf16 v[68:71], v[212:215], v[112:115], v[68:71]\n\t"
    "ds_read_b128 v[108:111], %[da] offset:26624\n\t"
    "global_load_lds_dwordx4 %[av4], s[38:39] sc1\n\t"
    "v_mfma_f32_16x16x32_bf16 v[72:75], v[208:211], v[116:119], v[72:75]\n\t"
    "ds_read_b32 v39, %[sa]\n\t"
    "s_add_u32 m0, %[l0], 40960\n\t"
    "v_mfma_f32_16x16x32_bf16 v[76:79], v[212:215], v[116:119], v[76:79]\n\t"
    "global_load_lds_dwordx4 %[av5], s[38:39] sc1\n\t"
    "s_xor_b32 %[l1], %[l1], 0x10000\n\t"
    "s_waitcnt lgkmcnt(5)\n\t"
    "v_mfma_f32_16x16x32_bf16 v[64:67], v[216:219], v[120:123], v[64:67]\n\t"
    "ds_read_b128 v[112:115], %[ea] offset:28672\n\t"
    "s_add_u32 m0, %[l0], 12288\n\t"
    "v_mfma_f32_16x16x32_bf16 v[68:71], v[220:223], v[120:123], v[68:71]\n\t"
    "ds_read_b128 v[116:119], %[ea] offset:30720\n\t"
    "global_load_lds_dwordx4 %[av6], s[38:39] sc1\n\t"
    "v_mfma_f32_16x16x32_bf16 v[72:75], v[216:219], v[124:127], v[72:75]\n\t"
    "v_xor_b32 %[ea], 0x10000, %[ea]\n\t"
    "s_add_u32 m0, %[l0], 45056\n\t"
    "v_mfma_f32_16x16x32_bf16 v[76:79], v[220:223], v[124:127], v[76:79]\n\t"
    "global_load_lds_dwordx4 %[av7], s[38:39] sc1\n\t"
    "s_waitcnt lgkmcnt(5)\n\t"
    "v_mfma_f32_16x16x32_bf16 v[64:67], v[224:227], v[96:99], v[64:67]\n\t"
    "ds_read_b128 v[120:123], %[da] offset:28672\n\t"
    "v_mfma_f32_16x16x32_bf16 v[68:71], v[228:231], v[96:99], v[68:71]\n\t"
    "ds_read_b128 v[124:127], %[da] offset:30720\n\t"
    "v_mfma_f32_16x16x32_bf16 v[72:75], v[224:227], v[100:103], v[72:75]\n\t"
    "v_xor_b32 %[da], 0x10000, %[da]\n\t"
    "v_mfma_f32_16x16x32_bf16 v[76:79], v[228:231], v[100:103], v[76:79]\n\t"
    "s_waitcnt lgkmcnt(5)\n\t"
    "v_mfma_f32_16x16x32_bf16 v[64:67], v[232:235], v[104:107], v[64:67]\n\t"
    "ds_read_b128 v[96:99], %[ea] offset:0\n\t"
    "v_mfma_f32_16x16x32_bf16 v[68:71], v[236:239], v[104:107], v[68:71]\n\t"
    "ds_read_b128 v[100:103], %[ea] offset:2048\n\t"
    "v_mfma_f32_16x16x32_bf16 v[72:75], v[232:235], v[108:111], v[72:75]\n\t"
    "v_mfma_f32_16x16x32_bf16 v[76:79], v[236:239], v[108:111], v[76:79]\n\t"
    "s_waitcnt lgkmcnt(4)\n\t"
    "v_mfma_f32_16x16x32_bf16 v[64:67], v[240:243], v[112:115], v[64:67]\n\t"
    "ds_read_b128 v[104:107], %[da] offset:0\n\t"
    "v_mad_u32_u24 v39, v39, %[mu], %[ad]\n\t"
    "v_mfma_f32_16x16x32_bf16 v[68:71], v[244:247], v[112:115], v[68:71]\n\t"
    "ds_read_b128 v[108:111], %[da] offset:2048\n\t"
    "v_cmp_ge_i32 vcc, %[lf], v39\n\t"
    "s_cmp_eq_u64 vcc, 0\n\t"
    "s_addc_u32 %[lf], %[lf], 0\n\t"
    "v_mfma_f32_16x16x32_bf16 v[72:75], v[240:243], v[116:119], v[72:75]\n\t"
    "v_mfma_f32_16x16x32_bf16 v[76:79], v[244:247], v[116:119], v[76:79]\n\t"
    "v_cmp_ge_i32 vcc, %[lf], v39\n\t"
    "s_cmp_eq_u64 vcc, 0\n\t"
    "s_addc_u32 %[lf], %[lf], 0\n\t"
    "s_waitcnt lgkmcnt(4)\n\t"
    "v_mfma_f32_16x16x32_bf16 v[80:83], v[248:251], v[120:123], v[64:67]\n\t"
    "ds_read_b128 v[112:115], %[ea] offset:4096\n\t"
    "s_min_i32 %[lf], %[lf], %[cp]\n\t"
    "s_add_u32 %[b], %[b], 1\n\t"
    "v_mfma_f32_16x16x32_bf16 v[84:87], v[252:255], v[120:123], v[68:71]\n\t"
    "ds_read_b128 v[116:119], %[ea] offset:6144\n\t"
    "v_mfma_f32_16x16x32_bf16 v[88:91], v[248:251], v[124:127], v[72:75]\n\t"
    "v_mfma_f32_16x16x32_bf16 v[92:95], v[252:255], v[124:127], v[76:79]\n\t"
    "s_add_u32 s38, s38, %[ab]\n\t"
    "s_addc_u32 s39, s39, 0\n\t"
    "s_xor_b32 %[l0], %[l0], 0x10000\n\t"
    "s_sub_u32 m0, %[lf], %[b]\n\t"
    "s_sub_u32 m0, m0, 1\n\t"
    "s_cmp_lt_u32 m0, %[up]\n\t"
    "s_cbranch_scc1 1b\n\t"
    "s_waitcnt lgkmcnt(0)\n\t"
    "s_nop 4"
    : "=&{v[80:95]}"(pd), [ea] "+&v"(ea), [da] "+&v"(da), [b] "+&s"(b), [lf] "+&s"(lf), "+{s[36:37]}"(p1), "+{s[38:39]}"(p0), [sa] "+&v"(sa), [l1] "+&s"(l1), [l0] "+&s"(l0)
    : [rw] "v"(rw), [ab] "s"(ab), [cp] "s"(cp), [mu] "s"(mu), [ad] "v"(ad), [up] "s"(up), "{v[128:159]}"(W[0]), "{v[160:191]}"(W[1]), "{v[192:223]}"(W[2]), "{v[224:255]}"(W[3]), [av0] "v"(av[0]), [av1] "v"(av[1]), [av2] "v"(av[2]), [av3] "v"(av[3]), [av4] "v"(av[4]), [av5] "v"(av[5]), [av6] "v"(av[6]), [av7] "v"(av[7]), [la] "s"(la)
    : "v39", "v64", "v65", "v66", "v67", "v68", "v69", "v70", "v71", "v72", "v73", "v74", "v75", "v76", "v77", "v78", "v79", "v96", "v97", "v98", "v99", "v100", "v101", "v102", "v103", "v104", "v105", "v106", "v107", "v108", "v109", "v110", "v111", "v112", "v113", "v114", "v115", "v116", "v117", "v118", "v119", "v120", "v121", "v122", "v123", "v124", "v125", "v126", "v127", "vcc", "scc", "m0", "memory");
}
__device__ __forceinline__ void fast_ld1(const u32x32 (&W)[4], u32x16& pd, unsigned ea, unsigned da, int& b, int& lf, int cp, unsigned up, unsigned sa, unsigned mu, unsigned ad,
    unsigned rw, unsigned ab, const char* p1, const char* p0, unsigned l1, unsigned l0, const unsigned (&av)[8], int la) {
  asm volatile("s_setprio 0\n\t"
    "s_cmp_eq_u32 %[la], 3\n\t"
    "s_waitcnt vmcnt(12)\n\t"
    "s_cbranch_scc1 20f\n\t"
    "s_waitcnt vmcnt(4)\n\t"
    "20:\n\t"
    "s_barrier\n\t"
    "ds_read_b128 v[96:99], %[ea] offset:0\n\t"
    "ds_read_b128 v[100:103], %[ea] offset:2048\n\t"
    "ds_read_b128 v[104:107], %[da] offset:0\n\t"
    "ds_read_b128 v[108:111], %[da] offset:2048\n\t"
    "ds_read_b128 v[112:115], %[ea] offset:4096\n\t"
    "ds_read_b128 v[116:119], %[ea] offset:6144\n\t"
    "s_branch 2f\n\t"
    ".p2align 6\n\t"
    "1:\n\t"
    "s_cmp_eq_u32 %[la], 3\n\t"
    "s_waitcnt vmcnt(12)\n\t"
    "s_cbranch_scc1 21f\n\t"
    "s_waitcnt vmcnt(4)\n\t"
    "21:\n\t"
    "s_barrier\n\t"
    "2:\n\t"
    "s_waitcnt lgkmcnt(4)\n\t"
    "v_mfma_f32_16x16x32_bf16 v[64:67], v[128:131], v[96:99], 0\n\t"
    "ds_read_b128 v[120:123], %[da] offset:4096\n\t"
    "s_add_u32 m0, %[l1], 0\n\t"
    "ds_write_b128 %[rw], v[80:83] offset:0\n\t"
    "v_mfma_f32_16x16x32_bf16 v[68:71], v[132:135], v[96:99], 0\n\t"
    "ds_read_b128 v[124:127], %[da] offset:6144\n\t"
    "global_load_lds_dwordx4 %[av0], s[36:37] sc1\n\t"
    "ds_write_b128 %[rw], v[84:87] offset:1024\n\t"
    "v_mfma_f32_16x16x32_bf16 v[72:75], v[128:131], v[100:103], 0\n\t"
    "s_add_u32 m0, %[l1], 32768\n\t"
    "ds_write_b128 %[rw], v[88:91] offset:2048\n\t"
    "v_mfma_f32_16x16x32_bf16 v[76:79], v[132:135], v[100:103], 0\n\t"
    "global_load_lds_dwordx4 %[av1], s[36:37] sc1\n\t"
    "ds_write_b128 %[rw], v[92:95] offset:3072\n\t"
    "s_waitcnt lgkmcnt(8)\n\t"
    "v_mfma_f32_16x16x32_bf16 v[64:67], v[136:139], v[104:107], v[64:67]\n\t"
    "ds_read_b128 v[96:99], %[ea] offset:8192\n\t"
    "s_add_u32 m0, %[l1], 4096\n\t"
    "v_mfma_f32_16x16x32_bf16 v[68:71], v[140:143], v[104:107], v[68:71]\n\t"
    "ds_read_b128 v[100:103], %[ea] offset:10240\n\t"
    "global_load_lds_dwordx4 %[av2], s[36:37] sc1\n\t"
    "v_mfma_f32_16x16x32_bf16 v[72:75], v[136:139], v[108:111], v[72:75]\n\t"
    "s_add_u32 m0, %[l1], 36864\n\t"
    "v_mfma_f32_16x16x32_bf16 v[76:79], v[140:143], v[108:111], v[76:79]\n\t"
    "global_load_lds_dwordx4 %[av3], s[36:37] sc1\n\t"
    "s_waitcnt lgkmcnt(8)\n\t"
    "v_mfma_f32_16x16x32_bf16 v[64:67], v[144:147], v[112:115], v[64:67]\n\t"
    "ds_read_b128 v[104:107], %[da] offset:8192\n\t"
    "s_add_u32 m0, %[l1], 8192\n\t"
    "v_mfma_f32_16x16x32_bf16 v[68:71], v[148:151], v[112:115], v[68:71]\n\t"
    "ds_read_b128 v[108:111], %[da] offset:10240\n\t"
    "global_load_lds_dwordx4 %[av4], s[36:37] sc1\n\t"
    "v_mfma_f32_16x16x32_bf16 v[72:75], v[144:147], v[116:119], v[72:75]\n\t"
    "s_add_u32 m0, %[l1], 40960\n\t"
    "v_mfma_f32_16x16x32_bf16 v[76:79], v[148:151], v[116:119], v[76:79]\n\t"
    "global_load_lds_dwordx4 %[av5], s[36:37] sc1\n\t"
    "s_waitcnt lgkmcnt(7)\n\t"
    "v_mfma_f32_16x16x32_bf16 v[64:67], v[152:155], v[120:123], v[64:67]\n\t"
    "ds_read_b128 v[112:115], %[ea] offset:12288\n\t"
    "s_add_u32 m0, %[l1], 12288\n\t"
    "v_mfma_f32_16x16x32_bf16 v[68:71], v[156:159], v[120:123], v[68:71]\n\t"
    "ds_read_b128 v[116:119], %[ea] offset:14336\n\t"
    "global_load_lds_dwordx4 %[av6], s[36:37] sc1\n\t"
    "v_mfma_f32_16x16x32_bf16 v[72:75], v[152:155], v[124:127], v[72:75]\n\t"
    "s_add_u32 m0, %[l1], 45056\n\t"
    "v_mfma_f32_16x16x32_bf16 v[76:79], v[156:159], v[124:127], v[76:79]\n\t"
    "global_load_lds_dwordx4 %[av7], s[36:37] sc1\n\t"
    "s_waitcnt lgkmcnt(4)\n\t"
    "v_mfma_f32_16x16x32_bf16 v[64:67], v[160:163], v[96:99], v[64:67]\n\t"
    "ds_read_b128 v[120:123], %[da] offset:12288\n\t"
    "v_mfma_f32_16x16x32_bf16 v[68:71], v[164:167], v[96:99], v[68:71]\n\t"
    "ds_read_b128 v[124:127], %[da] offset:14336\n\t"
    "v_mfma_f32_16x16x32_bf16 v[72:75], v[160:163], v[100:103], v[72:75]\n\t"
    "v_mfma_f32_16x16x32_bf16 v[76:79], v[164:167], v[100:103], v[76:79]\n\t"
    "s_waitcnt lgkmcnt(4)\n\t"
    "v_mfma_f32_16x16x32_bf16 v[64:67], v[168:171], v[104:107], v[64:67]\n\t"
    "ds_read_b128 v[96:99], %[ea] offset:16384\n\t"
    "v_mfma_f32_16x16x32_bf16 v[68:71], v[172:175], v[104:107], v[68:71]\n\t"
    "ds_read_b128 v[100:103], %[ea] offset:18432\n\t"
    "v_mfma_f32_16x16x32_bf16 v[72:75], v[168:171], v[108:111], v[72:75]\n\t"
    "v_mfma_f32_16x16x32_bf16 v[76:79], v[172:175], v[108:111], v[76:79]\n\t"
    "s_waitcnt lgkmcnt(4)\n\t"
    "v_mfma_f32_16x16x32_bf16 v[64:67], v[176:179], v[112:115], v[64:67]\n\t"
    "ds_read_b128 v[104:107], %[da] offset:16384\n\t"
    "v_mfma_f32_16x16x32_bf16 v[68:71], v[180:183], v[112:115], v[68:71]\n\t"
    "ds_read_b128 v[108:111], %[da] offset:18432\n\t"
    "v_mfma_f32_16x16x32_bf16 v[72:75], v[176:179], v[116:119], v[72:75]\n\t"
    "v_mfma_f32_16x16x32_bf16 v[76:79], v[180:183], v[116:119], v[76:79]\n\t"
    "s_waitcnt lgkmcnt(4)\n\t"
    "v_mfma_f32_16x16x32_bf16 v[64:67], v[184:187], v[120:123], v[64:67]\n\t"
    "ds_read_b128 v[112:115], %[ea] offset:20480\n\t"
    "v_mfma_f32_16x16x32_bf16 v[68:71], v[188:191], v[120:123], v[68:71]\n\t"
    "ds_read_b128 v[116:119], %[ea] offset:22528\n\t"
    "v_mfma_f32_16x16x32_bf16 v[72:75], v[184:187], v[124:127], v[72:75]\n\t"
    "v_mfma_f32_16x16x32_bf16 v[76:79], v[188:191], v[124:127], v[76:79]\n\t"
    "s_cmp_eq_u32 %[la], 3\n\t"
    "s_waitcnt vmcnt(12)\n\t"
    "s_cbranch_scc1 22f\n\t"
    "s_waitcnt vmcnt(4)\n\t"
    "22:\n\t"
    "s_barrier\n\t"
    "s_waitcnt lgkmcnt(4)\n\t"
    "v_mfma_f32_16x16x32_bf16 v[64:67], v[192:195], v[96:99], v[64:67]\n\t"
    "ds_read_b128 v[120:123], %[da] offset:20480\n\t"
    "s_add_u32 m0, %[l0], 0\n\t"
    "v_mfma_f32_16x16x32_bf16 v[68:71], v[196:199], v[96:99], v[68:71]\n\t"
    "ds_read_b128 v[124:127], %[da] offset:22528\n\t"
    "global_load_lds_dwordx4 %[av0], s[38:39] sc1\n\t"
    "v_mfma_f32_16x16x32_bf16 v[72:75], v[192:195], v[100:103], v[72:75]\n\t"
    "s_add_u32 m0, %[l0], 32768\n\t"
    "v_mfma_f32_16x16x32_bf16 v[76:79], v[196:199], v[100:103], v[76:79]\n\t"
    "global_load_lds_dwordx4 %[av1], s[38:39] sc1\n\t"
    "s_waitcnt lgkmcnt(4)\n\t"
    "v_mfma_f32_16x16x32_bf16 v[64:67], v[200:203], v[104:107], v[64:67]\n\t"
    "ds_read_b128 v[96:99], %[ea] offset:24576\n\t"
    "s_add_u32 m0, %[l0], 4096\n\t"
    "v_mfma_f32_16x16x32_bf16 v[68:71], v[204:207], v[104:107], v[68:71]\n\t"
    "ds_read_b128 v[100:103], %[ea] offset:26624\n\t"
    "global_load_lds_dwordx4 %[av2], s[38:39] sc1\n\t"
    "v_mfma_f32_16x16x32_bf16 v[72:75], v[200:203], v[108:111], v[72:75]\n\t"
    "s_add_u32 m0, %[l0], 36864\n\t"
    "v_mfma_f32_16x16x32_bf16 v[76:79], v[204:207], v[108:111], v[76:79]\n\t"
    "global_load_lds_dwordx4 %[av3], s[38:39] sc1\n\t"
    "s_add_u32 s36, s36, %[ab]\n\t"
    "s_addc_u32 s37, s37, 0\n\t"
    "s_waitcnt lgkmcnt(4)\n\t"
    "v_mfma_f32_16x16x32_bf16 v[64:67], v[208:211], v[112:115], v[64:67]\n\t"
    "ds_read_b128 v[104:107], %[da] offset:24576\n\t"
    "s_add_u32 m0, %[l0], 8192\n\t"
    "v_mfma_f32_16x16x32_bf16 v[68:71], v[212:215], v[112:115], v[68:71]\n\t"
    "ds_read_b128 v[108:111], %[da] offset:26624\n\t"
    "global_load_lds_dwordx4 %[av4], s[38:39] sc1\n\t"
    "v_mfma_f32_16x16x32_bf16 v[72:75], v[208:211], v[116:119], v[72:75]\n\t"
    "ds_read_b32 v39, %[sa]\n\t"
    "s_add_u32 m0, %[l0], 40960\n\t"
    "v_mfma_f32_16x16x32_bf16 v[76:79], v[212:215], v[116:119], v[76:79]\n\t"
    "global_load_lds_dwordx4 %[av5], s[38:39] sc1\n\t"
    "s_xor_b32 %[l1], %[l1], 0x10000\n\t"
    "s_waitcnt lgkmcnt(5)\n\t"
    "v_mfma_f32_16x16x32_bf16 v[64:67], v[216:219], v[120:123], v[64:67]\n\t"
    "ds_read_b128 v[112:115], %[ea] offset:28672\n\t"
    "s_add_u32 m0, %[l0], 12288\n\t"
    "v_mfma_f32_16x16x32_bf16 v[68:71], v[220:223], v[120:123], v[68:71]\n\t"
    "ds_read_b128 v[116:119], %[ea] offset:30720\n\t"
    "global_load_lds_dwordx4 %[av6], s[38:39] sc1\n\t"
    "v_mfma_f32_16x16x32_bf16 v[72:75], v[216:219], v[124:127], v[72:75]\n\t"
    "v_xor_b32 %[ea], 0x10000, %[ea]\n\t"
    "s_add_u32 m0, %[l0], 45056\n\t"
    "v_mfma_f32_16x16x32_bf16 v[76:79], v[220:223], v[124:127], v[76:79]\n\t"
    "global_load_lds_dwordx4 %[av7], s[38:39] sc1\n\t"
    "s_waitcnt lgkmcnt(5)\n\t"
    "v_mfma_f32_16x16x32_bf16 v[64:67], v[224:227], v[96:99], v[64:67]\n\t"
    "ds_read_b128 v[120:123], %[da] offset:28672\n\t"
    "v_mfma_f32_16x16x32_bf16 v[68:71], v[228:231], v[96:99], v[68:71]\n\t"
    "ds_read_b128 v[124:127], %[da] offset:30720\n\t"
    "v_mfma_f32_16x16x32_bf16 v[72:75], v[224:227], v[100:103], v[72:75]\n\t"
    "v_xor_b32 %[da], 0x10000, %[da]\n\t"
    "v_mfma_f32_16x16x32_bf16 v[76:79], v[228:231], v[100:103], v[76:79]\n\t"
    "s_waitcnt lgkmcnt(5)\n\t"
    "v_mfma_f32_16x16x32_bf16 v[64:67], v[232:235], v[104:107], v[64:67]\n\t"
    "ds_read_b128 v[96:99], %[ea] offset:0\n\t"
    "v_mfma_f32_16x16x32_bf16 v[68:71], v[236:239], v[104:107], v[68:71]\n\t"
    "ds_read_b128 v[100:103], %[ea] offset:2048\n\t"
    "v_mfma_f32_16x16x32_bf16 v[72:75], v[232:235], v[108:111], v[72:75]\n\t"
    "v_mfma_f32_16x16x32_bf16 v[76:79], v[236:239], v[108:111], v[76:79]\n\t"
    "s_waitcnt lgkmcnt(4)\n\t"
    "v_mfma_f32_16x16x32_bf16 v[64:67], v[240:243], v[112:115], v[64:67]\n\t"
    "ds_read_b128 v[104:107], %[da] offset:0\n\t"
    "v_mad_u32_u24 v39, v39, %[mu], %[ad]\n\t"
    "v_mfma_f32_16x16x32_bf16 v[68:71], v[244:247], v[112:115], v[68:71]\n\t"
    "ds_read_b128 v[108:111], %[da] offset:2048\n\t"
    "v_cmp_ge_i32 vcc, %[lf], v39\n\t"
    "s_cmp_eq_u64 vcc, 0\n\t"
    "s_addc_u32 %[lf], %[lf], 0\n\t"
    "v_mfma_f32_16x16x32_bf16 v[72:75], v[240:243], v[116:119], v[72:75]\n\t"
    "v_mfma_f32_16x16x32_bf16 v[76:79], v[244:247], v[116:119], v[76:79]\n\t"
    "v_cmp_ge_i32 vcc, %[lf], v39\n\t"
    "s_cmp_eq_u64 vcc, 0\n\t"
    "s_addc_u32 %[lf], %[lf], 0\n\t"
    "s_waitcnt lgkmcnt(4)\n\t"
    "v_mfma_f32_16x16x32_bf16 v[80:83], v[248:251], v[120:123], v[64:67]\n\t"
    "ds_read_b128 v[112:115], %[ea] offset:4096\n\t"
    "s_min_i32 %[lf], %[lf], %[cp]\n\t"
    "s_add_u32 %[b], %[b], 1\n\t"
    "v_mfma_f32_16x16x32_bf16 v[84:87], v[252:255], v[120:123], v[68:71]\n\t"
    "ds_read_b128 v[116:119], %[ea] offset:6144\n\t"
    "v_mfma_f32_16x16x32_bf16 v[88:91], v[248:251], v[124:127], v[72:75]\n\t"
    "v_mfma_f32_16x16x32_bf16 v[92:95], v[252:255], v[124:127], v[76:79]\n\t"
    "s_add_u32 s38, s38, %[ab]\n\t"
    "s_addc_u32 s39, s39, 0\n\t"
    "s_xor_b32 %[l0], %[l0], 0x10000\n\t"
    "s_sub_u32 m0, %[lf], %[b]\n\t"
    "s_sub_u32 m0, m0, 1\n\t"
    "s_cmp_lt_u32 m0, %[up]\n\t"
    "s_cbranch_scc1 1b\n\t"
    "s_waitcnt lgkmcnt(0)\n\t"
    "s_nop 4"
    : "=&{v[80:95]}"(pd), [ea] "+&v"(ea), [da] "+&v"(da), [b] "+&s"(b), [lf] "+&s"(lf), "+{s[36:37]}"(p1), "+{s[38:39]}"(p0), [sa] "+&v"(sa), [l1] "+&s"(l1), [l0] "+&s"(l0)
    : [rw] "v"(rw), [ab] "s"(ab), [cp] "s"(cp), [mu] "s"(mu), [ad] "v"(ad), [up] "s"(up), "{v[128:159]}"(W[0]), "{v[160:191]}"(W[1]), "{v[192:223]}"(W[2]), "{v[224:255]}"(W[3]), [av0] "v"(av[0]), [av1] "v"(av[1]), [av2] "v"(av[2]), [av3] "v"(av[3]), [av4] "v"(av[4]), [av5] "v"(av[5]), [av6] "v"(av[6]), [av7] "v"(av[7]), [la] "s"(la)
    : "v39", "v64", "v65", "v66", "v67", "v68", "v69", "v70", "v71", "v72", "v73", "v74", "v75", "v76", "v77", "v78", "v79", "v96", "v97", "v98", "v99", "v100", "v101", "v102", "v103", "v104", "v105", "v106", "v107", "v108", "v109", "v110", "v111", "v112", "v113", "v114", "v115", "v116", "v117", "v118", "v119", "v120", "v121", "v122", "v123", "v124", "v125", "v126", "v127", "vcc", "scc", "m0", "memory");
}
__device__ __forceinline__ void fast_ld2(const u32x32 (&W)[4], u32x16& pd, unsigned ea, unsigned da, int& b, int& lf, int cp, unsigned up, unsigned sa, unsigned mu, unsigned ad,
    unsigned rw, unsigned ab, const char* p1, const char* p0, unsigned l1, unsigned l0, const unsigned (&av)[8], int la) {
  asm volatile("s_setprio 0\n\t"
    "s_cmp_eq_u32 %[la], 3\n\t"
    "s_waitcnt vmcnt(6)\n\t"
    "s_cbranch_scc1 20f\n\t"
    "s_waitcnt vmcnt(2)\n\t"
    "20:\n\t"
    "s_barrier\n\t"
    "ds_read_b128 v[96:99], %[ea] offset:0\n\t"
    "ds_read_b128 v[100:103], %[ea] offset:2048\n\t"
    "ds_read_b128 v[104:107], %[da] offset:0\n\t"
    "ds_read_b128 v[108:111], %[da] offset:2048\n\t"
    "ds_read_b128 v[112:115], %[ea] offset:4096\n\t"
    "ds_read_b128 v[116:119], %[ea] offset:6144\n\t"
    "s_branch 2f\n\t"
    ".p2align 6\n\t"
    "1:\n\t"
    "s_cmp_eq_u32 %[la], 3\n\t"
    "s_waitcnt vmcnt(6)\n\t"
    "s_cbranch_scc1 21f\n\t"
    "s_waitcnt vmcnt(2)\n\t"
    "21:\n\t"
    "s_barrier\n\t"
    "2:\n\t"
    "s_waitcnt lgkmcnt(4)\n\t"
    "v_mfma_f32_16x16x32_bf16 v[64:67], v[128:131], v[96:99], 0\n\t"
    "ds_read_b128 v[120:123], %[da] offset:4096\n\t"
    "s_add_u32 m0, %[l1], 0\n\t"
    "ds_write_b128 %[rw], v[80:83] offset:0\n\t"
    "v_mfma_f32_16x16x32_bf16 v[68:71], v[132:135], v[96:99], 0\n\t"
    "ds_read_b128 v[124:127], %[da] offset:6144\n\t"
    "global_load_lds_dwordx4 %[av0], s[36:37] sc1\n\t"
    "ds_write_b128 %[rw], v[84:87] offset:1024\n\t"
    "v_mfma_f32_16x16x32_bf16 v[72:75], v[128:131], v[100:103], 0\n\t"
    "ds_write_b128 %[rw], v[88:91] offset:2048\n\t"
    "v_mfma_f32_16x16x32_bf16 v[76:79], v[132:135], v[100:103], 0\n\t"
    "ds_write_b128 %[rw], v[92:95] offset:3072\n\t"
    "s_waitcnt lgkmcnt(8)\n\t"
    "v_mfma_f32_16x16x32_bf16 v[64:67], v[136:139], v[104:107], v[64:67]\n\t"
    "ds_read_b128 v[96:99], %[ea] offset:8192\n\t"
    "s_add_u32 m0, %[l1], 4096\n\t"
    "v_mfma_f32_16x16x32_bf16 v[68:71], v[140:143], v[104:107], v[68:71]\n\t"
    "ds_read_b128 v[100:103], %[ea] offset:10240\n\t"
    "global_load_lds_dwordx4 %[av2], s[36:37] sc1\n\t"
    "v_mfma_f32_16x16x32_bf16 v[72:75], v[136:139], v[108:111], v[72:75]\n\t"
    "v_mfma_f32_16x16x32_bf16 v[76:79], v[140:143], v[108:111], v[76:79]\n\t"
    "s_waitcnt lgkmcnt(8)\n\t"
    "v_mfma_f32_16x16x32_bf16 v[64:67], v[144:147], v[112:115], v[64:67]\n\t"
    "ds_read_b128 v[104:107], %[da] offset:8192\n\t"
    "s_add_u32 m0, %[l1], 8192\n\t"
    "v_mfma_f32_16x16x32_bf16 v[68:71], v[148:151], v[112:115], v[68:71]\n\t"
    "ds_read_b128 v[108:111], %[da] offset:10240\n\t"
    "global_load_lds_dwordx4 %[av4], s[36:37] sc1\n\t"
    "v_mfma_f32_16x16x32_bf16 v[72:75], v[144:147], v[116:119], v[72:75]\n\t"
    "v_mfma_f32_16x16x32_bf16 v[76:79], v[148:151], v[116:119], v[76:79]\n\t"
    "s_waitcnt lgkmcnt(7)\n\t"
    "v_mfma_f32_16x16x32_bf16 v[64:67], v[152:155], v[120:123], v[64:67]\n\t"
    "ds_read_b128 v[112:115], %[ea] offset:12288\n\t"
    "s_add_u32 m0, %[l1], 12288\n\t"
    "v_mfma_f32_16x16x32_bf16 v[68:71], v[156:159], v[120:123], v[68:71]\n\t"
    "ds_read_b128 v[116:119], %[ea] offset:14336\n\t"
    "global_load_lds_dwordx4 %[av6], s[36:37] sc1\n\t"
    "v_mfma_f32_16x16x32_bf16 v[72:75], v[152:155], v[124:127], v[72:75]\n\t"
    "v_mfma_f32_16x16x32_bf16 v[76:79], v[156:159], v[124:127], v[76:79]\n\t"
    "s_waitcnt lgkmcnt(4)\n\t"
    "v_mfma_f32_16x16x32_bf16 v[64:67], v[160:163], v[96:99], v[64:67]\n\t"
    "ds_read_b128 v[120:123], %[da] offset:12288\n\t"
    "v_mfma_f32_16x16x32_bf16 v[68:71], v[164:167], v[96:99], v[68:71]\n\t"
    "ds_read_b128 v[124:127], %[da] offset:14336\n\t"
    "v_mfma_f32_16x16x32_bf16 v[72:75], v[160:163], v[100:103], v[72:75]\n\t"
    "v_mfma_f32_16x16x32_bf16 v[76:79], v[164:167], v[100:103], v[76:79]\n\t"
    "s_waitcnt lgkmcnt(4)\n\t"
    "v_mfma_f32_16x16x32_bf16 v[64:67], v[168:171], v[104:107], v[64:67]\n\t"
    "ds_read_b128 v[96:99], %[ea] offset:16384\n\t"
    "v_mfma_f32_16x16x32_bf16 v[68:71], v[172:175], v[104:107], v[68:71]\n\t"
    "ds_read_b128 v[100:103], %[ea] offset:18432\n\t"
    "v_mfma_f32_16x16x32_bf16 v[72:75], v[168:171], v[108:111], v[72:75]\n\t"
    "v_mfma_f32_16x16x32_bf16 v[76:79], v[172:175], v[108:111], v[76:79]\n\t"
    "s_waitcnt lgkmcnt(4)\n\t"
    "v_mfma_f32_16x16x32_bf16 v[64:67], v[176:179], v[112:115], v[64:67]\n\t"
    "ds_read_b128 v[104:107], %[da] offset:16384\n\t"
    "v_mfma_f32_16x16x32_bf16 v[68:71], v[180:183], v[112:115], v[68:71]\n\t"
    "ds_read_b128 v[108:111], %[da] offset:18432\n\t"
    "v_mfma_f32_16x16x32_bf16 v[72:75], v[176:179], v[116:119], v[72:75]\n\t"
    "v_mfma_f32_16x16x32_bf16 v[76:79], v[180:183], v[116:119], v[76:79]\n\t"
    "s_waitcnt lgkmcnt(4)\n\t"
    "v_mfma_f32_16x16x32_bf16 v[64:67], v[184:187], v[120:123], v[64:67]\n\t"
    "ds_read_b128 v[112:115], %[ea] offset:20480\n\t"
    "v_mfma_f32_16x16x32_bf16 v[68:71], v[188:191], v[120:123], v[68:71]\n\t"
    "ds_read_b128 v[116:119], %[ea] offset:22528\n\t"
    "v_mfma_f32_16x16x32_bf16 v[72:75], v[184:187], v[124:127], v[72:75]\n\t"
    "v_mfma_f32_16x16x32_bf16 v[76:79], v[188:191], v[124:127], v[76:79]\n\t"
    "s_cmp_eq_u32 %[la], 3\n\t"
    "s_waitcnt vmcnt(6)\n\t"
    "s_cbranch_scc1 22f\n\t"
    "s_waitcnt vmcnt(2)\n\t"
    "22:\n\t"
    "s_barrier\n\t"
    "s_waitcnt lgkmcnt(4)\n\t"
    "v_mfma_f32_16x16x32_bf16 v[64:67], v[192:195], v[96:99], v[64:67]\n\t"
    "ds_read_b128 v[120:123], %[da] offset:20480\n\t"
    "s_add_u32 m0, %[l0], 0\n\t"
    "v_mfma_f32_16x16x32_bf16 v[68:71], v[196:199], v[96:99], v[68:71]\n\t"
    "ds_read_b128 v[124:127], %[da] offset:22528\n\t"
    "global_load_lds_dwordx4 %[av0], s[38:39] sc1\n\t"
    "v_mfma_f32_16x16x32_bf16 v[72:75], v[192:195], v[100:103], v[72:75]\n\t"
    "v_mfma_f32_16x16x32_bf16 v[76:79], v[196:199], v[100:103], v[76:79]\n\t"
    "s_waitcnt lgkmcnt(4)\n\t"
    "v_mfma_f32_16x16x32_bf16 v[64:67], v[200:203], v[104:107], v[64:67]\n\t"
    "ds_read_b128 v[96:99], %[ea] offset:24576\n\t"
    "s_add_u32 m0, %[l0], 4096\n\t"
    "v_mfma_f32_16x16x32_bf16 v[68:71], v[204:207], v[104:107], v[68:71]\n\t"
    "ds_read_b128 v[100:103], %[ea] offset:26624\n\t"
    "global_load_lds_dwordx4 %[av2], s[38:39] sc1\n\t"
    "v_mfma_f32_16x16x32_bf16 v[72:75], v[200:203], v[108:111], v[72:75]\n\t"
    "v_mfma_f32_16x16x32_bf16 v[76:79], v[204:207], v[108:111], v[76:79]\n\t"
    "s_add_u32 s36, s36, %[ab]\n\t"
    "s_addc_u32 s37, s37, 0\n\t"
    "s_waitcnt lgkmcnt(4)\n\t"
    "v_mfma_f32_16x16x32_bf16 v[64:67], v[208:211], v[112:115], v[64:67]\n\t"
    "ds_read_b128 v[104:107], %[da] offset:24576\n\t"
    "s_add_u32 m0, %[l0], 8192\n\t"
    "v_mfma_f32_16x16x32_bf16 v[68:71], v[212:215], v[112:115], v[68:71]\n\t"
    "ds_read_b128 v[108:111], %[da] offset:26624\n\t"
    "global_load_lds_dwordx4 %[av4], s[38:39] sc1\n\t"
    "v_mfma_f32_16x16x32_bf16 v[72:75], v[208:211], v[116:119], v[72:75]\n\t"
    "ds_read_b32 v39, %[sa]\n\t"
    "v_mfma_f32_16x16x32_bf16 v[76:79], v[212:215], v[116:119], v[76:79]\n\t"
    "s_xor_b32 %[l1], %[l1], 0x10000\n\t"
    "s_waitcnt lgkmcnt(5)\n\t"
    "v_mfma_f32_16x16x32_bf16 v[64:67], v[216:219], v[120:123], v[64:67]\n\t"
    "ds_read_b128 v[112:115], %[ea] offset:28672\n\t"
    "s_add_u32 m0, %[l0], 12288\n\t"
    "v_mfma_f32_16x16x32_bf16 v[68:71], v[220:223], v[120:123], v[68:71]\n\t"
    "ds_read_b128 v[116:119], %[ea] offset:30720\n\t"
    "global_load_lds_dwordx4 %[av6], s[38:39] sc1\n\t"
    "v_mfma_f32_16x16x32_bf16 v[72:75], v[216:219], v[124:127], v[72:75]\n\t"
    "v_xor_b32 %[ea], 0x10000, %[ea]\n\t"
    "v_mfma_f32_16x16x32_bf16 v[76:79], v[220:223], v[124:127], v[76:79]\n\t"
    "s_waitcnt lgkmcnt(5)\n\t"
    "v_mfma_f32_16x16x32_bf16 v[64:67], v[224:227], v[96:99], v[64:67]\n\t"
    "ds_read_b128 v[120:123], %[da] offset:28672\n\t"
    "v_mfma_f32_16x16x32_bf16 v[68:71], v[228:231], v[96:99], v[68:71]\n\t"
    "ds_read_b128 v[124:127], %[da] offset:30720\n\t"
    "v_mfma_f32_16x16x32_bf16 v[72:75], v[224:227], v[100:103], v[72:75]\n\t"
    "v_xor_b32 %[da], 0x10000, %[da]\n\t"
    "v_mfma_f32_16x16x32_bf16 v[76:79], v[228:231], v[100:103], v[76:79]\n\t"
    "s_waitcnt lgkmcnt(5)\n\t"
    "v_mfma_f32_16x16x32_bf16 v[64:67], v[232:235], v[104:107], v[64:67]\n\t"
    "ds_read_b128 v[96:99], %[ea] offset:0\n\t"
    "v_mfma_f32_16x16x32_bf16 v[68:71], v[236:239], v[104:107], v[68:71]\n\t"
    "ds_read_b128 v[100:103], %[ea] offset:2048\n\t"
    "v_mfma_f32_16x16x32_bf16 v[72:75], v[232:235], v[108:111], v[72:75]\n\t"
    "v_mfma_f32_16x16x32_bf16 v[76:79], v[236:239], v[108:111], v[76:79]\n\t"
    "s_waitcnt lgkmcnt(4)\n\t"
    "v_mfma_f32_16x16x32_bf16 v[64:67], v[240:243], v[112:115], v[64:67]\n\t"
    "ds_read_b128 v[104:107], %[da] offset:0\n\t"
    "v_mad_u32_u24 v39, v39, %[mu], %[ad]\n\t"
    "v_mfma_f32_16x16x32_bf16 v[68:71], v[244:247], v[112:115], v[68:71]\n\t"
    "ds_read_b128 v[108:111], %[da] offset:2048\n\t"
    "v_cmp_ge_i32 vcc, %[lf], v39\n\t"
    "s_cmp_eq_u64 vcc, 0\n\t"
    "s_addc_u32 %[lf], %[lf], 0\n\t"
    "v_mfma_f32_16x16x32_bf16 v[72:75], v[240:243], v[116:119], v[72:75]\n\t"
    "v_mfma_f32_16x16x32_bf16 v[76:79], v[244:247], v[116:119], v[76:79]\n\t"
    "v_cmp_ge_i32 vcc, %[lf], v39\n\t"
    "s_cmp_eq_u64 vcc, 0\n\t"
    "s_addc_u32 %[lf], %[lf], 0\n\t"
    "s_waitcnt lgkmcnt(4)\n\t"
    "v_mfma_f32_16x16x32_bf16 v[80:83], v[248:251], v[120:123], v[64:67]\n\t"
    "ds_read_b128 v[112:115], %[ea] offset:4096\n\t"
    "s_min_i32 %[lf], %[lf], %[cp]\n\t"
    "s_add_u32 %[b], %[b], 1\n\t"
    "v_mfma_f32_16x16x32_bf16 v[84:87], v[252:255], v[120:123], v[68:71]\n\t"
    "ds_read_b128 v[116:119], %[ea] offset:6144\n\t"
    "v_mfma_f32_16x16x32_bf16 v[88:91], v[248:251], v[124:127], v[72:75]\n\t"
    "v_mfma_f32_16x16x32_bf16 v[92:95], v[252:255], v[124:127], v[76:79]\n\t"
    "s_add_u32 s38, s38, %[ab]\n\t"
    "s_addc_u32 s39, s39, 0\n\t"
    "s_xor_b32 %[l0], %[l0], 0x10000\n\t"
    "s_sub_u32 m0, %[lf], %[b]\n\t"
    "s_sub_u32 m0, m0, 1\n\t"
    "s_cmp_lt_u32 m0, %[up]\n\t"
    "s_cbranch_scc1 1b\n\t"
    "s_waitcnt lgkmcnt(0)\n\t"
    "s_nop 4"
    : "=&{v[80:95]}"(pd), [ea] "+&v"(ea), [da] "+&v"(da), [b] "+&s"(b), [lf] "+&s"(lf), "+{s[36:37]}"(p1), "+{s[38:39]}"(p0), [sa] "+&v"(sa), [l1] "+&s"(l1), [l0] "+&s"(l0)
    : [rw] "v"(rw), [ab] "s"(ab), [cp] "s"(cp), [mu] "s"(mu), [ad] "v"(ad), [up] "s"(up), "{v[128:159]}"(W[0]), "{v[160:191]}"(W[1]), "{v[192:223]}"(W[2]), "{v[224:255]}"(W[3]), [av0] "v"(av[0]), [av1] "v"(av[1]), [av2] "v"(av[2]), [av3] "v"(av[3]), [av4] "v"(av[4]), [av5] "v"(av[5]), [av6] "v"(av[6]), [av7] "v"(av[7]), [la] "s"(la)
    : "v39", "v64", "v65", "v66", "v67", "v68", "v69", "v70", "v71", "v72", "v73", "v74", "v75", "v76", "v77", "v78", "v79", "v96", "v97", "v98", "v99", "v100", "v101", "v102", "v103", "v104", "v105", "v106", "v107", "v108", "v109", "v110", "v111", "v112", "v113", "v114", "v115", "v116", "v117", "v118", "v119", "v120", "v121", "v122", "v123", "v124", "v125", "v126", "v127", "vcc", "scc", "m0", "memory");
}
__device__ __forceinline__ void fast_st0(const u32x32 (&W)[4], u32x16& pd, unsigned ea, unsigned da, int& b, int& lf, int cp, unsigned up, unsigned sa, unsigned mu, unsigned ad,
    unsigned rw, unsigned ab, const char* p1, const char* p0, unsigned co0, unsigned co1, int po, int pf, unsigned w0, unsigned sl, unsigned dv, const char* db, const char* pg) {
  unsigned st;
  asm volatile("s_barrier\n\t"
    "ds_read_b128 v[96:99], %[ea] offset:0\n\t"
    "ds_read_b128 v[100:103], %[ea] offset:2048\n\t"
    "ds_read_b128 v[104:107], %[da] offset:0\n\t"
    "ds_read_b128 v[108:111], %[da] offset:2048\n\t"
    "ds_read_b128 v[112:115], %[ea] offset:4096\n\t"
    "ds_read_b128 v[116:119], %[ea] offset:6144\n\t"
    "s_branch 2f\n\t"
    ".p2align 6\n\t"
    "1:\n\t"
    "s_barrier\n\t"
    "2:\n\t"
    "s_waitcnt lgkmcnt(4)\n\t"
    "v_mfma_f32_16x16x32_bf16 v[64:67], v[128:131], v[96:99], 0\n\t"
    "ds_read_b128 v[120:123], %[da] offset:4096\n\t"
    "v_mfma_f32_16x16x32_bf16 v[68:71], v[132:135], v[96:99], 0\n\t"
    "ds_read_b128 v[124:127], %[da] offset:6144\n\t"
    "v_mfma_f32_16x16x32_bf16 v[72:75], v[128:131], v[100:103], 0\n\t"
    "v_mfma_f32_16x16x32_bf16 v[76:79], v[132:135], v[100:103], 0\n\t"
    "s_waitcnt lgkmcnt(4)\n\t"
    "v_mfma_f32_16x16x32_bf16 v[64:67], v[136:139], v[104:107], v[64:67]\n\t"
    "ds_read_b128 v[96:99], %[ea] offset:8192\n\t"
    "v_mfma_f32_16x16x32_bf16 v[68:71], v[140:143], v[104:107], v[68:71]\n\t"
    "ds_read_b128 v[100:103], %[ea] offset:10240\n\t"
    "v_mfma_f32_16x16x32_bf16 v[72:75], v[136:139], v[108:111], v[72:75]\n\t"
    "v_mfma_f32_16x16x32_bf16 v[76:79], v[140:143], v[108:111], v[76:79]\n\t"
    "s_cmp_eq_u32 %[w0], 1\n\t"
    "s_cbranch_scc1 7f\n\t"
    "8:\n\t"
    "s_waitcnt lgkmcnt(4)\n\t"
    "v_mfma_f32_16x16x32_bf16 v[64:67], v[144:147], v[112:115], v[64:67]\n\t"
    "ds_read_b128 v[104:107], %[da] offset:8192\n\t"
    "v_mfma_f32_16x16x32_bf16 v[68:71], v[148:151], v[112:115], v[68:71]\n\t"
    "ds_read_b128 v[108:111], %[da] offset:10240\n\t"
    "v_mfma_f32_16x16x32_bf16 v[72:75], v[144:147], v[116:119], v[72:75]\n\t"
    "v_mfma_f32_16x16x32_bf16 v[76:79], v[148:151], v[116:119], v[76:79]\n\t"
    "s_waitcnt lgkmcnt(4)\n\t"
    "v_mfma_f32_16x16x32_bf16 v[64:67], v[152:155], v[120:123], v[64:67]\n\t"
    "ds_read_b128 v[112:115], %[ea] offset:12288\n\t"
    "v_mfma_f32_16x16x32_bf16 v[68:71], v[156:159], v[120:123], v[68:71]\n\t"
    "ds_read_b128 v[116:119], %[ea] offset:14336\n\t"
    "v_mfma_f32_16x16x32_bf16 v[72:75], v[152:155], v[124:127], v[72:75]\n\t"
    "v_mfma_f32_16x16x32_bf16 v[76:79], v[156:159], v[124:127], v[76:79]\n\t"
    "s_waitcnt lgkmcnt(4)\n\t"
    "v_mfma_f32_16x16x32_bf16 v[64:67], v[160:163], v[96:99], v[64:67]\n\t"
    "ds_read_b128 v[120:123], %[da] offset:12288\n\t"
    "v_mfma_f32_16x16x32_bf16 v[68:71], v[164:167], v[96:99], v[68:71]\n\t"
    "ds_read_b128 v[124:127], %[da] offset:14336\n\t"
    "v_mfma_f32_16x16x32_bf16 v[72:75], v[160:163], v[100:103], v[72:75]\n\t"
    "v_mfma_f32_16x16x32_bf16 v[76:79], v[164:167], v[100:103], v[76:79]\n\t"
    "s_waitcnt lgkmcnt(4)\n\t"
    "v_mfma_f32_16x16x32_bf16 v[64:67], v[168:171], v[104:107], v[64:67]\n\t"
    "ds_read_b128 v[96:99], %[ea] offset:16384\n\t"
    "v_mfma_f32_16x16x32_bf16 v[68:71], v[172:175], v[104:107], v[68:71]\n\t"
    "ds_read_b128 v[100:103], %[ea] offset:18432\n\t"
    "v_mfma_f32_16x16x32_bf16 v[72:75], v[168:171], v[108:111], v[72:75]\n\t"
    "v_mfma_f32_16x16x32_bf16 v[76:79], v[172:175], v[108:111], v[76:79]\n\t"
    "s_waitcnt lgkmcnt(4)\n\t"
    "v_mfma_f32_16x16x32_bf16 v[64:67], v[176:179], v[112:115], v[64:67]\n\t"
    "ds_read_b128 v[104:107], %[da] offset:16384\n\t"
    "v_mfma_f32_16x16x32_bf16 v[68:71], v[180:183], v[112:115], v[68:71]\n\t"
    "ds_read_b128 v[108:111], %[da] offset:18432\n\t"
    "v_mfma_f32_16x16x32_bf16 v[72:75], v[176:179], v[116:119], v[72:75]\n\t"
    "v_mfma_f32_16x16x32_bf16 v[76:79], v[180:183], v[116:119], v[76:79]\n\t"
    "s_waitcnt lgkmcnt(4)\n\t"
    "v_mfma_f32_16x16x32_bf16 v[64:67], v[184:187], v[120:123], v[64:67]\n\t"
    "ds_read_b128 v[112:115], %[ea] offset:20480\n\t"
    "v_mfma_f32_16x16x32_bf16 v[68:71], v[188:191], v[120:123], v[68:71]\n\t"
    "ds_read_b128 v[116:119], %[ea] offset:22528\n\t"
    "v_mfma_f32_16x16x32_bf16 v[72:75], v[184:187], v[124:127], v[72:75]\n\t"
    "v_mfma_f32_16x16x32_bf16 v[76:79], v[188:191], v[124:127], v[76:79]\n\t"
    "s_waitcnt vmcnt(0)\n\t"
    "s_barrier\n\t"
    "s_waitcnt lgkmcnt(4)\n\t"
    "v_mfma_f32_16x16x32_bf16 v[64:67], v[192:195], v[96:99], v[64:67]\n\t"
    "ds_read_b128 v[120:123], %[da] offset:20480\n\t"
    "v_mfma_f32_16x16x32_bf16 v[68:71], v[196:199], v[96:99], v[68:71]\n\t"
    "ds_read_b128 v[124:127], %[da] offset:22528\n\t"
    "v_mfma_f32_16x16x32_bf16 v[72:75], v[192:195], v[100:103], v[72:75]\n\t"
    "ds_read_b128 v[48:51], %[rw] offset:0\n\t"
    "v_mfma_f32_16x16x32_bf16 v[76:79], v[196:199], v[100:103], v[76:79]\n\t"
    "ds_read_b128 v[52:55], %[rw] offset:1024\n\t"
    "s_cmp_eq_u32 %[w0], 0\n\t"
    "s_cbranch_scc1 5f\n\t"
    "6:\n\t"
    "s_waitcnt lgkmcnt(6)\n\t"
    "v_mfma_f32_16x16x32_bf16 v[64:67], v[200:203], v[104:107], v[64:67]\n\t"
    "ds_read_b128 v[96:99], %[ea] offset:24576\n\t"
    "v_mfma_f32_16x16x32_bf16 v[68:71], v[204:207], v[104:107], v[68:71]\n\t"
    "ds_read_b128 v[100:103], %[ea] offset:26624\n\t"
    "v_mfma_f32_16x16x32_bf16 v[72:75], v[200:203], v[108:111], v[72:75]\n\t"
    "ds_read_b128 v[56:59], %[rw] offset:2048\n\t"
    "v_mfma_f32_16x16x32_bf16 v[76:79], v[204:207], v[108:111], v[76:79]\n\t"
    "ds_read_b128 v[60:63], %[rw] offset:3072\n\t"
    "s_waitcnt lgkmcnt(0)\n\t"
    "v_mfma_f32_16x16x32_bf16 v[64:67], v[208:211], v[112:115], v[64:67]\n\t"
    "ds_read_b128 v[104:107], %[da] offset:24576\n\t"
    "v_add_f32 v48, v48, v80\n\t"
    "v_add_f32 v49, v49, v81\n\t"
    "v_mfma_f32_16x16x32_bf16 v[68:71], v[212:215], v[112:115], v[68:71]\n\t"
    "ds_read_b128 v[108:111], %[da] offset:26624\n\t"
    "v_add_f32 v50, v50, v82\n\t"
    "v_add_f32 v51, v51, v83\n\t"
    "v_mfma_f32_16x16x32_bf16 v[72:75], v[208:211], v[116:119], v[72:75]\n\t"
    "ds_read_b32 v39, %[sa]\n\t"
    "v_add_f32 v52, v52, v84\n\t"
    "v_mfma_f32_16x16x32_bf16 v[76:79], v[212:215], v[116:119], v[76:79]\n\t"
    "v_add_f32 v53, v53, v85\n\t"
    "v_add_f32 v54, v54, v86\n\t"
    "v_mfma_f32_16x16x32_bf16 v[64:67], v[216:219], v[120:123], v[64:67]\n\t"
    "ds_read_b128 v[112:115], %[ea] offset:28672\n\t"
    "v_add_f32 v55, v55, v87\n\t"
    "v_add_f32 v56, v56, v88\n\t"
    "v_mfma_f32_16x16x32_bf16 v[68:71], v[220:223], v[120:123], v[68:71]\n\t"
    "ds_read_b128 v[116:119], %[ea] offset:30720\n\t"
    "v_add_f32 v57, v57, v89\n\t"
    "v_mfma_f32_16x16x32_bf16 v[72:75], v[216:219], v[124:127], v[72:75]\n\t"
    "v_xor_b32 %[ea], 0x10000, %[ea]\n\t"
    "v_add_f32 v58, v58, v90\n\t"
    "v_add_f32 v59, v59, v91\n\t"
    "v_mfma_f32_16x16x32_bf16 v[76:79], v[220:223], v[124:127], v[76:79]\n\t"
    "v_add_f32 v60, v60, v92\n\t"
    "v_mfma_f32_16x16x32_bf16 v[64:67], v[224:227], v[96:99], v[64:67]\n\t"
    "ds_read_b128 v[120:123], %[da] offset:28672\n\t"
    "v_add_f32 v61, v61, v93\n\t"
    "v_add_f32 v62, v62, v94\n\t"
    "v_mfma_f32_16x16x32_bf16 v[68:71], v[228:231], v[96:99], v[68:71]\n\t"
    "ds_read_b128 v[124:127], %[da] offset:30720\n\t"
    "v_add_f32 v63, v63, v95\n\t"
    "v_cvt_pk_bf16_f32 v40, v48, v49\n\t"
    "v_mfma_f32_16x16x32_bf16 v[72:75], v[224:227], v[100:103], v[72:75]\n\t"
    "v_xor_b32 %[da], 0x10000, %[da]\n\t"
    "v_cvt_pk_bf16_f32 v41, v50, v51\n\t"
    "v_mfma_f32_16x16x32_bf16 v[76:79], v[228:231], v[100:103], v[76:79]\n\t"
    "v_cvt_pk_bf16_f32 v42, v52, v53\n\t"
    "v_cvt_pk_bf16_f32 v43, v54, v55\n\t"
    "s_waitcnt lgkmcnt(5)\n\t"
    "v_mfma_f32_16x16x32_bf16 v[64:67], v[232:235], v[104:107], v[64:67]\n\t"
    "ds_read_b128 v[96:99], %[ea] offset:0\n\t"
    "global_store_dwordx4 %[co0], v[40:43], s[36:37] sc1\n\t"
    "v_cvt_pk_bf16_f32 v44, v56, v57\n\t"
    "v_mfma_f32_16x16x32_bf16 v[68:71], v[236:239], v[104:107], v[68:71]\n\t"
    "ds_read_b128 v[100:103], %[ea] offset:2048\n\t"
    "v_cvt_pk_bf16_f32 v45, v58, v59\n\t"
    "v_mfma_f32_16x16x32_bf16 v[72:75], v[232:235], v[108:111], v[72:75]\n\t"
    "v_cvt_pk_bf16_f32 v46, v60, v61\n\t"
    "v_cvt_pk_bf16_f32 v47, v62, v63\n\t"
    "v_mfma_f32_16x16x32_bf16 v[76:79], v[236:239], v[108:111], v[76:79]\n\t"
    "global_store_dwordx4 %[co1], v[44:47], s[36:37] sc1\n\t"
    "s_waitcnt lgkmcnt(4)\n\t"
    "v_mfma_f32_16x16x32_bf16 v[64:67], v[240:243], v[112:115], v[64:67]\n\t"
    "ds_read_b128 v[104:107], %[da] offset:0\n\t"
    "v_mad_u32_u24 v39, v39, %[mu], %[ad]\n\t"
    "v_mfma_f32_16x16x32_bf16 v[68:71], v[244:247], v[112:115], v[68:71]\n\t"
    "ds_read_b128 v[108:111], %[da] offset:2048\n\t"
    "v_cmp_ge_i32 vcc, %[lf], v39\n\t"
    "s_cmp_eq_u64 vcc, 0\n\t"
    "s_addc_u32 %[lf], %[lf], 0\n\t"
    "v_mfma_f32_16x16x32_bf16 v[72:75], v[240:243], v[116:119], v[72:75]\n\t"
    "v_mfma_f32_16x16x32_bf16 v[76:79], v[244:247], v[116:119], v[76:79]\n\t"
    "v_cmp_ge_i32 vcc, %[lf], v39\n\t"
    "s_cmp_eq_u64 vcc, 0\n\t"
    "s_addc_u32 %[lf], %[lf], 0\n\t"
    "s_waitcnt lgkmcnt(4)\n\t"
    "v_mfma_f32_16x16x32_bf16 v[80:83], v[248:251], v[120:123], v[64:67]\n\t"
    "ds_read_b128 v[112:115], %[ea] offset:4096\n\t"
    "s_min_i32 %[lf], %[lf], %[cp]\n\t"
    "s_add_u32 %[b], %[b], 1\n\t"
    "v_mfma_f32_16x16x32_bf16 v[84:87], v[252:255], v[120:123], v[68:71]\n\t"
    "ds_read_b128 v[116:119], %[ea] offset:6144\n\t"
    "v_mfma_f32_16x16x32_bf16 v[88:91], v[248:251], v[124:127], v[72:75]\n\t"
    "v_mfma_f32_16x16x32_bf16 v[92:95], v[252:255], v[124:127], v[76:79]\n\t"
    "s_mov_b64 s[36:37], s[38:39]\n\t"
    "s_add_u32 s38, s38, %[ab]\n\t"
    "s_addc_u32 s39, s39, 0\n\t"
    "s_sub_u32 m0, %[lf], %[b]\n\t"
    "s_sub_u32 m0, m0, 1\n\t"
    "s_cmp_lt_u32 m0, %[up]\n\t"
    "s_cbranch_scc1 1b\n\t"
    "s_waitcnt lgkmcnt(0)\n\t"
    "s_nop 4\n\t"
    "s_branch 9f\n\t"
    "5:\n\t"
    "s_add_u32 %[st], %[b], %[po]\n\t"
    "s_max_i32 %[st], %[st], %[p0]\n\t"
    "v_mov_b32 v39, %[st]\n\t"
    "v_mov_b32 v38, 0\n\t"
    "s_mov_b64 exec, 1\n\t"
    "global_store_dword v38, v39, %[pg] sc1\n\t"
    "s_mov_b64 exec, -1\n\t"
    "s_mov_b32 %[p0], %[p1]\n\t"
    "s_branch 6b\n\t"
    "7:\n\t"
    "s_cmp_ge_i32 %[lf], %[cp]\n\t"
    "s_cbranch_scc1 8b\n\t"
    "s_mov_b32 m0, %[sl]\n\t"
    "s_nop 0\n\t"
    "global_load_lds_dword %[dv], %[db] sc1\n\t"
    "s_branch 8b\n\t"
    "9:"
    : "=&{v[80:95]}"(pd), [ea] "+&v"(ea), [da] "+&v"(da), [b] "+&s"(b), [lf] "+&s"(lf), "+{s[36:37]}"(p1), "+{s[38:39]}"(p0), [sa] "+&v"(sa), [st] "=&s"(st), [sl] "+&s"(sl), [p0] "+&s"(pf)
    : [rw] "v"(rw), [ab] "s"(ab), [cp] "s"(cp), [mu] "s"(mu), [ad] "v"(ad), [up] "s"(up), "{v[128:159]}"(W[0]), "{v[160:191]}"(W[1]), "{v[192:223]}"(W[2]), "{v[224:255]}"(W[3]), [co0] "v"(co0), [w0] "s"(w0), [dv] "v"(dv), [db] "s"(db), [pg] "s"(pg), [po] "s"(po - 1), [p1] "s"(b + po), [co1] "v"(co1)
    : "v38", "v39", "v40", "v41", "v42", "v43", "v44", "v45", "v46", "v47", "v48", "v49", "v50", "v51", "v52", "v53", "v54", "v55", "v56", "v57", "v58", "v59", "v60", "v61", "v62", "v63", "v64", "v65", "v66", "v67", "v68", "v69", "v70", "v71", "v72", "v73", "v74", "v75", "v76", "v77", "v78", "v79", "v96", "v97", "v98", "v99", "v100", "v101", "v102", "v103", "v104", "v105", "v106", "v107", "v108", "v109", "v110", "v111", "v112", "v113", "v114", "v115", "v116", "v117", "v118", "v119", "v120", "v121", "v122", "v123", "v124", "v125", "v126", "v127", "vcc", "scc", "m0", "memory");
}
__device__ __forceinline__ void fast_st1(const u32x32 (&W)[4], u32x16& pd, unsigned ea, unsigned da, int& b, int& lf, int cp, unsigned up, unsigned sa, unsigned mu, unsigned ad,
    unsigned rw, unsigned ab, const char* p1, const char* p0, unsigned co0, unsigned co1, int po, int pf, unsigned w0, unsigned sl, unsigned dv, const char* db, const char* pg) {
  unsigned st;
  asm volatile("s_barrier\n\t"
    "ds_read_b128 v[96:99], %[ea] offset:0\n\t"
    "ds_read_b128 v[100:103], %[ea] offset:2048\n\t"
    "ds_read_b128 v[104:107], %[da] offset:0\n\t"
    "ds_read_b128 v[108:111], %[da] offset:2048\n\t"
    "ds_read_b128 v[112:115], %[ea] offset:4096\n\t"
    "ds_read_b128 v[116:119], %[ea] offset:6144\n\t"
    "s_branch 2f\n\t"
    ".p2align 6\n\t"
    "1:\n\t"
    "s_barrier\n\t"
    "2:\n\t"
    "s_waitcnt lgkmcnt(4)\n\t"
    "v_mfma_f32_16x16x32_bf16 v[64:67], v[128:131], v[96:99], 0\n\t"
    "ds_read_b128 v[120:123], %[da] offset:4096\n\t"
    "v_mfma_f32_16x16x32_bf16 v[68:71], v[132:135], v[96:99], 0\n\t"
    "ds_read_b128 v[124:127], %[da] offset:6144\n\t"
    "v_mfma_f32_16x16x32_bf16 v[72:75], v[128:131], v[100:103], 0\n\t"
    "v_mfma_f32_16x16x32_bf16 v[76:79], v[132:135], v[100:103], 0\n\t"
    "s_waitcnt lgkmcnt(4)\n\t"
    "v_mfma_f32_16x16x32_bf16 v[64:67], v[136:139], v[104:107], v[64:67]\n\t"
    "ds_read_b128 v[96:99], %[ea] offset:8192\n\t"
    "v_mfma_f32_16x16x32_bf16 v[68:71], v[140:143], v[104:107], v[68:71]\n\t"
    "ds_read_b128 v[100:103], %[ea] offset:10240\n\t"
    "v_mfma_f32_16x16x32_bf16 v[72:75], v[136:139], v[108:111], v[72:75]\n\t"
    "v_mfma_f32_16x16x32_bf16 v[76:79], v[140:143], v[108:111], v[76:79]\n\t"
    "s_cmp_eq_u32 %[w0], 1\n\t"
    "s_cbranch_scc1 7f\n\t"
    "8:\n\t"
    "s_waitcnt lgkmcnt(4)\n\t"
    "v_mfma_f32_16x16x32_bf16 v[64:67], v[144:147], v[112:115], v[64:67]\n\t"
    "ds_read_b128 v[104:107], %[da] offset:8192\n\t"
    "v_mfma_f32_16x16x32_bf16 v[68:71], v[148:151], v[112:115], v[68:71]\n\t"
    "ds_read_b128 v[108:111], %[da] offset:10240\n\t"
    "v_mfma_f32_16x16x32_bf16 v[72:75], v[144:147], v[116:119], v[72:75]\n\t"
    "v_mfma_f32_16x16x32_bf16 v[76:79], v[148:151], v[116:119], v[76:79]\n\t"
    "s_waitcnt lgkmcnt(4)\n\t"
    "v_mfma_f32_16x16x32_bf16 v[64:67], v[152:155], v[120:123], v[64:67]\n\t"
    "ds_read_b128 v[112:115], %[ea] offset:12288\n\t"
    "v_mfma_f32_16x16x32_bf16 v[68:71], v[156:159], v[120:123], v[68:71]\n\t"
    "ds_read_b128 v[116:119], %[ea] offset:14336\n\t"
    "v_mfma_f32_16x16x32_bf16 v[72:75], v[152:155], v[124:127], v[72:75]\n\t"
    "v_mfma_f32_16x16x32_bf16 v[76:79], v[156:159], v[124:127], v[76:79]\n\t"
    "s_waitcnt lgkmcnt(4)\n\t"
    "v_mfma_f32_16x16x32_bf16 v[64:67], v[160:163], v[96:99], v[64:67]\n\t"
    "ds_read_b128 v[120:123], %[da] offset:12288\n\t"
    "v_mfma_f32_16x16x32_bf16 v[68:71], v[164:167], v[96:99], v[68:71]\n\t"
    "ds_read_b128 v[124:127], %[da] offset:14336\n\t"
    "v_mfma_f32_16x16x32_bf16 v[72:75], v[160:163], v[100:103], v[72:75]\n\t"
    "v_mfma_f32_16x16x32_bf16 v[76:79], v[164:167], v[100:103], v[76:79]\n\t"
    "s_waitcnt lgkmcnt(4)\n\t"
    "v_mfma_f32_16x16x32_bf16 v[64:67], v[168:171], v[104:107], v[64:67]\n\t"
    "ds_read_b128 v[96:99], %[ea] offset:16384\n\t"
    "v_mfma_f32_16x16x32_bf16 v[68:71], v[172:175], v[104:107], v[68:71]\n\t"
    "ds_read_b128 v[100:103], %[ea] offset:18432\n\t"
    "v_mfma_f32_16x16x32_bf16 v[72:75], v[168:171], v[108:111], v[72:75]\n\t"
    "v_mfma_f32_16x16x32_bf16 v[76:79], v[172:175], v[108:111], v[76:79]\n\t"
    "s_waitcnt lgkmcnt(4)\n\t"
    "v_mfma_f32_16x16x32_bf16 v[64:67], v[176:179], v[112:115], v[64:67]\n\t"
    "ds_read_b128 v[104:107], %[da] offset:16384\n\t"
    "v_mfma_f32_16x16x32_bf16 v[68:71], v[180:183], v[112:115], v[68:71]\n\t"
    "ds_read_b128 v[108:111], %[da] offset:18432\n\t"
    "v_mfma_f32_16x16x32_bf16 v[72:75], v[176:179], v[116:119], v[72:75]\n\t"
    "v_mfma_f32_16x16x32_bf16 v[76:79], v[180:183], v[116:119], v[76:79]\n\t"
    "s_waitcnt lgkmcnt(4)\n\t"
    "v_mfma_f32_16x16x32_bf16 v[64:67], v[184:187], v[120:123], v[64:67]\n\t"
    "ds_read_b128 v[112:115], %[ea] offset:20480\n\t"
    "v_mfma_f32_16x16x32_bf16 v[68:71], v[188:191], v[120:123], v[68:71]\n\t"
    "ds_read_b128 v[116:119], %[ea] offset:22528\n\t"
    "v_mfma_f32_16x16x32_bf16 v[72:75], v[184:187], v[124:127], v[72:75]\n\t"
    "v_mfma_f32_16x16x32_bf16 v[76:79], v[188:191], v[124:127], v[76:79]\n\t"
    "s_waitcnt vmcnt(0)\n\t"
    "s_barrier\n\t"
    "s_waitcnt lgkmcnt(4)\n\t"
    "v_mfma_f32_16x16x32_bf16 v[64:67], v[192:195], v[96:99], v[64:67]\n\t"
    "ds_read_b128 v[120:123], %[da] offset:20480\n\t"
    "v_mfma_f32_16x16x32_bf16 v[68:71], v[196:199], v[96:99], v[68:71]\n\t"
    "ds_read_b128 v[124:127], %[da] offset:22528\n\t"
    "v_mfma_f32_16x16x32_bf16 v[72:75], v[192:195], v[100:103], v[72:75]\n\t"
    "ds_read_b128 v[48:51], %[rw] offset:0\n\t"
    "v_mfma_f32_16x16x32_bf16 v[76:79], v[196:199], v[100:103], v[76:79]\n\t"
    "ds_read_b128 v[52:55], %[rw] offset:1024\n\t"
    "s_cmp_eq_u32 %[w0], 0\n\t"
    "s_cbranch_scc1 5f\n\t"
    "6:\n\t"
    "s_waitcnt lgkmcnt(6)\n\t"
    "v_mfma_f32_16x16x32_bf16 v[64:67], v[200:203], v[104:107], v[64:67]\n\t"
    "ds_read_b128 v[96:99], %[ea] offset:24576\n\t"
    "v_mfma_f32_16x16x32_bf16 v[68:71], v[204:207], v[104:107], v[68:71]\n\t"
    "ds_read_b128 v[100:103], %[ea] offset:26624\n\t"
    "v_mfma_f32_16x16x32_bf16 v[72:75], v[200:203], v[108:111], v[72:75]\n\t"
    "ds_read_b128 v[56:59], %[rw] offset:2048\n\t"
    "v_mfma_f32_16x16x32_bf16 v[76:79], v[204:207], v[108:111], v[76:79]\n\t"
    "ds_read_b128 v[60:63], %[rw] offset:3072\n\t"
    "s_waitcnt lgkmcnt(0)\n\t"
    "v_mfma_f32_16x16x32_bf16 v[64:67], v[208:211], v[112:115], v[64:67]\n\t"
    "ds_read_b128 v[104:107], %[da] offset:24576\n\t"
    "v_add_f32 v48, v48, v80\n\t"
    "v_add_f32 v49, v49, v81\n\t"
    "v_add_f32 v50, v50, v82\n\t"
    "v_mfma_f32_16x16x32_bf16 v[68:71], v[212:215], v[112:115], v[68:71]\n\t"
    "ds_read_b128 v[108:111], %[da] offset:26624\n\t"
    "v_add_f32 v51, v51, v83\n\t"
    "v_add_f32 v52, v52, v84\n\t"
    "v_add_f32 v53, v53, v85\n\t"
    "v_mfma_f32_16x16x32_bf16 v[72:75], v[208:211], v[116:119], v[72:75]\n\t"
    "ds_read_b32 v39, %[sa]\n\t"
    "v_add_f32 v54, v54, v86\n\t"
    "v_add_f32 v55, v55, v87\n\t"
    "v_add_f32 v56, v56, v88\n\t"
    "v_mfma_f32_16x16x32_bf16 v[76:79], v[212:215], v[116:119], v[76:79]\n\t"
    "v_add_f32 v57, v57, v89\n\t"
    "v_add_f32 v58, v58, v90\n\t"
    "v_add_f32 v59, v59, v91\n\t"
    "v_mfma_f32_16x16x32_bf16 v[64:67], v[216:219], v[120:123], v[64:67]\n\t"
    "ds_read_b128 v[112:115], %[ea] offset:28672\n\t"
    "v_add_f32 v60, v60, v92\n\t"
    "v_add_f32 v61, v61, v93\n\t"
    "v_add_f32 v62, v62, v94\n\t"
    "v_mfma_f32_16x16x32_bf16 v[68:71], v[220:223], v[120:123], v[68:71]\n\t"
    "ds_read_b128 v[116:119], %[ea] offset:30720\n\t"
    "v_add_f32 v63, v63, v95\n\t"
    "v_mul_f32 v40, %[nl], v48\n\t"
    "v_mul_f32 v41, %[nl], v49\n\t"
    "v_mfma_f32_16x16x32_bf16 v[72:75], v[216:219], v[124:127], v[72:75]\n\t"
    "v_xor_b32 %[ea], 0x10000, %[ea]\n\t"
    "v_mul_f32 v42, %[nl], v50\n\t"
    "v_mul_f32 v43, %[nl], v51\n\t"
    "v_mul_f32 v44, %[nl], v56\n\t"
    "v_mfma_f32_16x16x32_bf16 v[76:79], v[220:223], v[124:127], v[76:79]\n\t"
    "v_mul_f32 v45, %[nl], v57\n\t"
    "v_mul_f32 v46, %[nl], v58\n\t"
    "v_mul_f32 v47, %[nl], v59\n\t"
    "v_mfma_f32_16x16x32_bf16 v[64:67], v[224:227], v[96:99], v[64:67]\n\t"
    "ds_read_b128 v[120:123], %[da] offset:28672\n\t"
    "v_exp_f32 v40, v40\n\t"
    "v_exp_f32 v41, v41\n\t"
    "v_exp_f32 v42, v42\n\t"
    "v_mfma_f32_16x16x32_bf16 v[68:71], v[228:231], v[96:99], v[68:71]\n\t"
    "ds_read_b128 v[124:127], %[da] offset:30720\n\t"
    "v_exp_f32 v43, v43\n\t"
    "v_exp_f32 v44, v44\n\t"
    "v_exp_f32 v45, v45\n\t"
    "v_mfma_f32_16x16x32_bf16 v[72:75], v[224:227], v[100:103], v[72:75]\n\t"
    "v_xor_b32 %[da], 0x10000, %[da]\n\t"
    "v_exp_f32 v46, v46\n\t"
    "v_exp_f32 v47, v47\n\t"
    "v_add_f32 v40, 1.0, v40\n\t"
    "v_mfma_f32_16x16x32_bf16 v[76:79], v[228:231], v[100:103], v[76:79]\n\t"
    "v_add_f32 v41, 1.0, v41\n\t"
    "v_add_f32 v42, 1.0, v42\n\t"
    "v_add_f32 v43, 1.0, v43\n\t"
    "s_waitcnt lgkmcnt(5)\n\t"
    "v_mfma_f32_16x16x32_bf16 v[64:67], v[232:235], v[104:107], v[64:67]\n\t"
    "ds_read_b128 v[96:99], %[ea] offset:0\n\t"
    "v_add_f32 v44, 1.0, v44\n\t"
    "v_add_f32 v45, 1.0, v45\n\t"
    "v_add_f32 v46, 1.0, v46\n\t"
    "v_mfma_f32_16x16x32_bf16 v[68:71], v[236:239], v[104:107], v[68:71]\n\t"
    "ds_read_b128 v[100:103], %[ea] offset:2048\n\t"
    "v_add_f32 v47, 1.0, v47\n\t"
    "v_rcp_f32 v40, v40\n\t"
    "v_rcp_f32 v41, v41\n\t"
    "v_mfma_f32_16x16x32_bf16 v[72:75], v[232:235], v[108:111], v[72:75]\n\t"
    "v_rcp_f32 v42, v42\n\t"
    "v_rcp_f32 v43, v43\n\t"
    "v_rcp_f32 v44, v44\n\t"
    "v_mfma_f32_16x16x32_bf16 v[76:79], v[236:239], v[108:111], v[76:79]\n\t"
    "v_rcp_f32 v45, v45\n\t"
    "v_rcp_f32 v46, v46\n\t"
    "v_rcp_f32 v47, v47\n\t"
    "s_waitcnt lgkmcnt(4)\n\t"
    "v_mfma_f32_16x16x32_bf16 v[64:67], v[240:243], v[112:115], v[64:67]\n\t"
    "ds_read_b128 v[104:107], %[da] offset:0\n\t"
    "v_mad_u32_u24 v39, v39, %[mu], %[ad]\n\t"
    "v_mul_f32 v40, v48, v40\n\t"
    "v_mul_f32 v41, v49, v41\n\t"
    "v_mul_f32 v42, v50, v42\n\t"
    "v_mfma_f32_16x16x32_bf16 v[68:71], v[244:247], v[112:115], v[68:71]\n\t"
    "ds_read_b128 v[108:111], %[da] offset:2048\n\t"
    "v_cmp_ge_i32 vcc, %[lf], v39\n\t"
    "s_cmp_eq_u64 vcc, 0\n\t"
    "s_addc_u32 %[lf], %[lf], 0\n\t"
    "v_mul_f32 v43, v51, v43\n\t"
    "v_mul_f32 v44, v56, v44\n\t"
    "v_mul_f32 v45, v57, v45\n\t"
    "v_mfma_f32_16x16x32_bf16 v[72:75], v[240:243], v[116:119], v[72:75]\n\t"
    "v_mul_f32 v46, v58, v46\n\t"
    "v_mul_f32 v47, v59, v47\n\t"
    "v_mul_f32 v40, v40, v52\n\t"
    "v_mfma_f32_16x16x32_bf16 v[76:79], v[244:247], v[116:119], v[76:79]\n\t"
    "v_cmp_ge_i32 vcc, %[lf], v39\n\t"
    "s_cmp_eq_u64 vcc, 0\n\t"
    "s_addc_u32 %[lf], %[lf], 0\n\t"
    "v_mul_f32 v41, v41, v53\n\t"
    "v_mul_f32 v42, v42, v54\n\t"
    "v_mul_f32 v43, v43, v55\n\t"
    "s_waitcnt lgkmcnt(4)\n\t"
    "v_mfma_f32_16x16x32_bf16 v[80:83], v[248:251], v[120:123], v[64:67]\n\t"
    "ds_read_b128 v[112:115], %[ea] offset:4096\n\t"
    "s_min_i32 %[lf], %[lf], %[cp]\n\t"
    "s_add_u32 %[b], %[b], 1\n\t"
    "v_mul_f32 v44, v44, v60\n\t"
    "v_mul_f32 v45, v45, v61\n\t"
    "v_mul_f32 v46, v46, v62\n\t"
    "v_mfma_f32_16x16x32_bf16 v[84:87], v[252:255], v[120:123], v[68:71]\n\t"
    "ds_read_b128 v[116:119], %[ea] offset:6144\n\t"
    "v_mul_f32 v47, v47, v63\n\t"
    "v_cvt_pk_bf16_f32 v48, v40, v41\n\t"
    "v_cvt_pk_bf16_f32 v49, v42, v43\n\t"
    "v_mfma_f32_16x16x32_bf16 v[88:91], v[248:251], v[124:127], v[72:75]\n\t"
    "v_cvt_pk_bf16_f32 v50, v44, v45\n\t"
    "v_cvt_pk_bf16_f32 v51, v46, v47\n\t"
    "s_nop 1\n\t"
    "v_mfma_f32_16x16x32_bf16 v[92:95], v[252:255], v[124:127], v[76:79]\n\t"
    "v_permlane16_swap_b32 v48, v50\n\t"
    "v_permlane16_swap_b32 v49, v51\n\t"
    "global_store_dwordx4 %[co0], v[48:51], s[36:37] sc1\n\t"
    "s_mov_b64 s[36:37], s[38:39]\n\t"
    "s_add_u32 s38, s38, %[ab]\n\t"
    "s_addc_u32 s39, s39, 0\n\t"
    "s_sub_u32 m0, %[lf], %[b]\n\t"
    "s_sub_u32 m0, m0, 1\n\t"
    "s_cmp_lt_u32 m0, %[up]\n\t"
    "s_cbranch_scc1 1b\n\t"
    "s_waitcnt lgkmcnt(0)\n\t"
    "s_nop 4\n\t"
    "s_branch 9f\n\t"
    "5:\n\t"
    "s_add_u32 %[st], %[b], %[po]\n\t"
    "s_max_i32 %[st], %[st], %[p0]\n\t"
    "v_mov_b32 v39, %[st]\n\t"
    "v_mov_b32 v38, 0\n\t"
    "s_mov_b64 exec, 1\n\t"
    "global_store_dword v38, v39, %[pg] sc1\n\t"
    "s_mov_b64 exec, -1\n\t"
    "s_mov_b32 %[p0], %[p1]\n\t"
    "s_branch 6b\n\t"
    "7:\n\t"
    "s_cmp_ge_i32 %[lf], %[cp]\n\t"
    "s_cbranch_scc1 8b\n\t"
    "s_mov_b32 m0, %[sl]\n\t"
    "s_nop 0\n\t"
    "global_load_lds_dword %[dv], %[db] sc1\n\t"
    "s_branch 8b\n\t"
    "9:"
    : "=&{v[80:95]}"(pd), [ea] "+&v"(ea), [da] "+&v"(da), [b] "+&s"(b), [lf] "+&s"(lf), "+{s[36:37]}"(p1), "+{s[38:39]}"(p0), [sa] "+&v"(sa), [st] "=&s"(st), [sl] "+&s"(sl), [p0] "+&s"(pf)
    : [rw] "v"(rw), [ab] "s"(ab), [cp] "s"(cp), [mu] "s"(mu), [ad] "v"(ad), [up] "s"(up), "{v[128:159]}"(W[0]), "{v[160:191]}"(W[1]), "{v[192:223]}"(W[2]), "{v[224:255]}"(W[3]), [co0] "v"(co0), [w0] "s"(w0), [dv] "v"(dv), [db] "s"(db), [pg] "s"(pg), [po] "s"(po - 1), [p1] "s"(b + po), [nl] "s"(-LOG2E)
    : "v38", "v39", "v40", "v41", "v42", "v43", "v44", "v45", "v46", "v47", "v48", "v49", "v50", "v51", "v52", "v53", "v54", "v55", "v56", "v57", "v58", "v59", "v60", "v61", "v62", "v63", "v64", "v65", "v66", "v67", "v68", "v69", "v70", "v71", "v72", "v73", "v74", "v75", "v76", "v77", "v78", "v79", "v96", "v97", "v98", "v99", "v100", "v101", "v102", "v103", "v104", "v105", "v106", "v107", "v108", "v109", "v110", "v111", "v112", "v113", "v114", "v115", "v116", "v117", "v118", "v119", "v120", "v121", "v122", "v123", "v124", "v125", "v126", "v127", "vcc", "scc", "m0", "memory");
}
__device__ __forceinline__ void fast_st2(const u32x32 (&W)[4], u32x16& pd, unsigned ea, unsigned da, int& b, int& lf, int cp, unsigned up, unsigned sa, unsigned mu, unsigned ad,
    unsigned rw, unsigned ab, const char* p1, const char* p0, unsigned co0, unsigned co1, int po, int pf, unsigned w0, unsigned sl, unsigned dv, const char* db, const char* pg) {
  unsigned st;
  asm volatile("s_barrier\n\t"
    "ds_read_b128 v[96:99], %[ea] offset:0\n\t"
    "ds_read_b128 v[100:103], %[ea] offset:2048\n\t"
    "ds_read_b128 v[104:107], %[da] offset:0\n\t"
    "ds_read_b128 v[108:111], %[da] offset:2048\n\t"
    "ds_read_b128 v[112:115], %[ea] offset:4096\n\t"
    "ds_read_b128 v[116:119], %[ea] offset:6144\n\t"
    "s_branch 2f\n\t"
    ".p2align 6\n\t"
    "1:\n\t"
    "s_barrier\n\t"
    "2:\n\t"
    "s_waitcnt lgkmcnt(4)\n\t"
    "v_mfma_f32_16x16x32_bf16 v[64:67], v[128:131], v[96:99], 0\n\t"
    "ds_read_b128 v[120:123], %[da] offset:4096\n\t"
    "v_mfma_f32_16x16x32_bf16 v[68:71], v[132:135], v[96:99], 0\n\t"
    "ds_read_b128 v[124:127], %[da] offset:6144\n\t"
    "v_mfma_f32_16x16x32_bf16 v[72:75], v[128:131], v[100:103], 0\n\t"
    "v_mfma_f32_16x16x32_bf16 v[76:79], v[132:135], v[100:103], 0\n\t"
    "s_waitcnt lgkmcnt(4)\n\t"
    "v_mfma_f32_16x16x32_bf16 v[64:67], v[136:139], v[104:107], v[64:67]\n\t"
    "ds_read_b128 v[96:99], %[ea] offset:8192\n\t"
    "v_mfma_f32_16x16x32_bf16 v[68:71], v[140:143], v[104:107], v[68:71]\n\t"
    "ds_read_b128 v[100:103], %[ea] offset:10240\n\t"
    "v_mfma_f32_16x16x32_bf16 v[72:75], v[136:139], v[108:111], v[72:75]\n\t"
    "v_mfma_f32_16x16x32_bf16 v[76:79], v[140:143], v[108:111], v[76:79]\n\t"
    "s_cmp_eq_u32 %[w0], 1\n\t"
    "s_cbranch_scc1 7f\n\t"
    "8:\n\t"
    "s_waitcnt lgkmcnt(4)\n\t"
    "v_mfma_f32_16x16x32_bf16 v[64:67], v[144:147], v[112:115], v[64:67]\n\t"
    "ds_read_b128 v[104:107], %[da] offset:8192\n\t"
    "v_mfma_f32_16x16x32_bf16 v[68:71], v[148:151], v[112:115], v[68:71]\n\t"
    "ds_read_b128 v[108:111], %[da] offset:10240\n\t"
    "v_mfma_f32_16x16x32_bf16 v[72:75], v[144:147], v[116:119], v[72:75]\n\t"
    "v_mfma_f32_16x16x32_bf16 v[76:79], v[148:151], v[116:119], v[76:79]\n\t"
    "s_waitcnt lgkmcnt(4)\n\t"
    "v_mfma_f32_16x16x32_bf16 v[64:67], v[152:155], v[120:123], v[64:67]\n\t"
    "ds_read_b128 v[112:115], %[ea] offset:12288\n\t"
    "v_mfma_f32_16x16x32_bf16 v[68:71], v[156:159], v[120:123], v[68:71]\n\t"
    "ds_read_b128 v[116:119], %[ea] offset:14336\n\t"
    "v_mfma_f32_16x16x32_bf16 v[72:75], v[152:155], v[124:127], v[72:75]\n\t"
    "v_mfma_f32_16x16x32_bf16 v[76:79], v[156:159], v[124:127], v[76:79]\n\t"
    "s_waitcnt lgkmcnt(4)\n\t"
    "v_mfma_f32_16x16x32_bf16 v[64:67], v[160:163], v[96:99], v[64:67]\n\t"
    "ds_read_b128 v[120:123], %[da] offset:12288\n\t"
    "v_mfma_f32_16x16x32_bf16 v[68:71], v[164:167], v[96:99], v[68:71]\n\t"
    "ds_read_b128 v[124:127], %[da] offset:14336\n\t"
    "v_mfma_f32_16x16x32_bf16 v[72:75], v[160:163], v[100:103], v[72:75]\n\t"
    "v_mfma_f32_16x16x32_bf16 v[76:79], v[164:167], v[100:103], v[76:79]\n\t"
    "s_waitcnt lgkmcnt(4)\n\t"
    "v_mfma_f32_16x16x32_bf16 v[64:67], v[168:171], v[104:107], v[64:67]\n\t"
    "ds_read_b128 v[96:99], %[ea] offset:16384\n\t"
    "v_mfma_f32_16x16x32_bf16 v[68:71], v[172:175], v[104:107], v[68:71]\n\t"
    "ds_read_b128 v[100:103], %[ea] offset:18432\n\t"
    "v_mfma_f32_16x16x32_bf16 v[72:75], v[168:171], v[108:111], v[72:75]\n\t"
    "v_mfma_f32_16x16x32_bf16 v[76:79], v[172:175], v[108:111], v[76:79]\n\t"
    "s_waitcnt lgkmcnt(4)\n\t"
    "v_mfma_f32_16x16x32_bf16 v[64:67], v[176:179], v[112:115], v[64:67]\n\t"
    "ds_read_b128 v[104:107], %[da] offset:16384\n\t"
    "v_mfma_f32_16x16x32_bf16 v[68:71], v[180:183], v[112:115], v[68:71]\n\t"
    "ds_read_b128 v[108:111], %[da] offset:18432\n\t"
    "v_mfma_f32_16x16x32_bf16 v[72:75], v[176:179], v[116:119], v[72:75]\n\t"
    "v_mfma_f32_16x16x32_bf16 v[76:79], v[180:183], v[116:119], v[76:79]\n\t"
    "s_waitcnt lgkmcnt(4)\n\t"
    "v_mfma_f32_16x16x32_bf16 v[64:67], v[184:187], v[120:123], v[64:67]\n\t"
    "ds_read_b128 v[112:115], %[ea] offset:20480\n\t"
    "v_mfma_f32_16x16x32_bf16 v[68:71], v[188:191], v[120:123], v[68:71]\n\t"
    "ds_read_b128 v[116:119], %[ea] offset:22528\n\t"
    "v_mfma_f32_16x16x32_bf16 v[72:75], v[184:187], v[124:127], v[72:75]\n\t"
    "v_mfma_f32_16x16x32_bf16 v[76:79], v[188:191], v[124:127], v[76:79]\n\t"
    "s_waitcnt vmcnt(0)\n\t"
    "s_barrier\n\t"
    "s_waitcnt lgkmcnt(4)\n\t"
    "v_mfma_f32_16x16x32_bf16 v[64:67], v[192:195], v[96:99], v[64:67]\n\t"
    "ds_read_b128 v[120:123], %[da] offset:20480\n\t"
    "v_mfma_f32_16x16x32_bf16 v[68:71], v[196:199], v[96:99], v[68:71]\n\t"
    "ds_read_b128 v[124:127], %[da] offset:22528\n\t"
    "v_mfma_f32_16x16x32_bf16 v[72:75], v[192:195], v[100:103], v[72:75]\n\t"
    "ds_read_b128 v[48:51], %[rw] offset:0\n\t"
    "v_mfma_f32_16x16x32_bf16 v[76:79], v[196:199], v[100:103], v[76:79]\n\t"
    "ds_read_b128 v[52:55], %[rw] offset:1024\n\t"
    "s_cmp_eq_u32 %[w0], 0\n\t"
    "s_cbranch_scc1 5f\n\t"
    "6:\n\t"
    "s_waitcnt lgkmcnt(6)\n\t"
    "v_mfma_f32_16x16x32_bf16 v[64:67], v[200:203], v[104:107], v[64:67]\n\t"
    "ds_read_b128 v[96:99], %[ea] offset:24576\n\t"
    "v_mfma_f32_16x16x32_bf16 v[68:71], v[204:207], v[104:107], v[68:71]\n\t"
    "ds_read_b128 v[100:103], %[ea] offset:26624\n\t"
    "v_mfma_f32_16x16x32_bf16 v[72:75], v[200:203], v[108:111], v[72:75]\n\t"
    "ds_read_b128 v[56:59], %[rw] offset:2048\n\t"
    "v_mfma_f32_16x16x32_bf16 v[76:79], v[204:207], v[108:111], v[76:79]\n\t"
    "ds_read_b128 v[60:63], %[rw] offset:3072\n\t"
    "s_waitcnt lgkmcnt(0)\n\t"
    "v_mfma_f32_16x16x32_bf16 v[64:67], v[208:211], v[112:115], v[64:67]\n\t"
    "ds_read_b128 v[104:107], %[da] offset:24576\n\t"
    "v_cvt_pk_bf16_f32 v40, v80, v81\n\t"
    "v_cvt_pk_bf16_f32 v41, v82, v83\n\t"
    "v_mfma_f32_16x16x32_bf16 v[68:71], v[212:215], v[112:115], v[68:71]\n\t"
    "ds_read_b128 v[108:111], %[da] offset:26624\n\t"
    "v_cvt_pk_bf16_f32 v42, v84, v85\n\t"
    "v_mfma_f32_16x16x32_bf16 v[72:75], v[208:211], v[116:119], v[72:75]\n\t"
    "ds_read_b32 v39, %[sa]\n\t"
    "v_cvt_pk_bf16_f32 v43, v86, v87\n\t"
    "v_mfma_f32_16x16x32_bf16 v[76:79], v[212:215], v[116:119], v[76:79]\n\t"
    "global_store_dwordx4 %[co0], v[40:43], s[36:37] sc1\n\t"
    "v_mfma_f32_16x16x32_bf16 v[64:67], v[216:219], v[120:123], v[64:67]\n\t"
    "ds_read_b128 v[112:115], %[ea] offset:28672\n\t"
    "v_cvt_pk_bf16_f32 v44, v88, v89\n\t"
    "v_cvt_pk_bf16_f32 v45, v90, v91\n\t"
    "v_mfma_f32_16x16x32_bf16 v[68:71], v[220:223], v[120:123], v[68:71]\n\t"
    "ds_read_b128 v[116:119], %[ea] offset:30720\n\t"
    "v_cvt_pk_bf16_f32 v46, v92, v93\n\t"
    "v_mfma_f32_16x16x32_bf16 v[72:75], v[216:219], v[124:127], v[72:75]\n\t"
    "v_xor_b32 %[ea], 0x10000, %[ea]\n\t"
    "v_cvt_pk_bf16_f32 v47, v94, v95\n\t"
    "v_mfma_f32_16x16x32_bf16 v[76:79], v[220:223], v[124:127], v[76:79]\n\t"
    "global_store_dwordx4 %[co1], v[44:47], s[36:37] sc1\n\t"
    "v_mfma_f32_16x16x32_bf16 v[64:67], v[224:227], v[96:99], v[64:67]\n\t"
    "ds_read_b128 v[120:123], %[da] offset:28672\n\t"
    "v_cvt_pk_bf16_f32 v48, v48, v49\n\t"
    "v_cvt_pk_bf16_f32 v49, v50, v51\n\t"
    "v_mfma_f32_16x16x32_bf16 v[68:71], v[228:231], v[96:99], v[68:71]\n\t"
    "ds_read_b128 v[124:127], %[da] offset:30720\n\t"
    "v_cvt_pk_bf16_f32 v50, v52, v53\n\t"
    "v_mfma_f32_16x16x32_bf16 v[72:75], v[224:227], v[100:103], v[72:75]\n\t"
    "v_xor_b32 %[da], 0x10000, %[da]\n\t"
    "v_cvt_pk_bf16_f32 v51, v54, v55\n\t"
    "v_mfma_f32_16x16x32_bf16 v[76:79], v[228:231], v[100:103], v[76:79]\n\t"
    "global_store_dwordx4 %[co0], v[48:51], s[36:37] offset:2048 sc1\n\t"
    "s_waitcnt lgkmcnt(5)\n\t"
    "v_mfma_f32_16x16x32_bf16 v[64:67], v[232:235], v[104:107], v[64:67]\n\t"
    "ds_read_b128 v[96:99], %[ea] offset:0\n\t"
    "v_cvt_pk_bf16_f32 v56, v56, v57\n\t"
    "v_cvt_pk_bf16_f32 v57, v58, v59\n\t"
    "v_mfma_f32_16x16x32_bf16 v[68:71], v[236:239], v[104:107], v[68:71]\n\t"
    "ds_read_b128 v[100:103], %[ea] offset:2048\n\t"
    "v_cvt_pk_bf16_f32 v58, v60, v61\n\t"
    "v_mfma_f32_16x16x32_bf16 v[72:75], v[232:235], v[108:111], v[72:75]\n\t"
    "v_cvt_pk_bf16_f32 v59, v62, v63\n\t"
    "v_mfma_f32_16x16x32_bf16 v[76:79], v[236:239], v[108:111], v[76:79]\n\t"
    "global_store_dwordx4 %[co1], v[56:59], s[36:37] offset:2048 sc1\n\t"
    "s_waitcnt lgkmcnt(4)\n\t"
    "v_mfma_f32_16x16x32_bf16 v[64:67], v[240:243], v[112:115], v[64:67]\n\t"
    "ds_read_b128 v[104:107], %[da] offset:0\n\t"
    "v_mad_u32_u24 v39, v39, %[mu], %[ad]\n\t"
    "v_mfma_f32_16x16x32_bf16 v[68:71], v[244:247], v[112:115], v[68:71]\n\t"
    "ds_read_b128 v[108:111], %[da] offset:2048\n\t"
    "v_cmp_ge_i32 vcc, %[lf], v39\n\t"
    "s_cmp_eq_u64 vcc, 0\n\t"
    "s_addc_u32 %[lf], %[lf], 0\n\t"
    "v_mfma_f32_16x16x32_bf16 v[72:75], v[240:243], v[116:119], v[72:75]\n\t"
    "v_mfma_f32_16x16x32_bf16 v[76:79], v[244:247], v[116:119], v[76:79]\n\t"
    "v_cmp_ge_i32 vcc, %[lf], v39\n\t"
    "s_cmp_eq_u64 vcc, 0\n\t"
    "s_addc_u32 %[lf], %[lf], 0\n\t"
    "s_waitcnt lgkmcnt(4)\n\t"
    "v_mfma_f32_16x16x32_bf16 v[80:83], v[248:251], v[120:123], v[64:67]\n\t"
    "ds_read_b128 v[112:115], %[ea] offset:4096\n\t"
    "s_min_i32 %[lf], %[lf], %[cp]\n\t"
    "s_add_u32 %[b], %[b], 1\n\t"
    "v_mfma_f32_16x16x32_bf16 v[84:87], v[252:255], v[120:123], v[68:71]\n\t"
    "ds_read_b128 v[116:119], %[ea] offset:6144\n\t"
    "v_mfma_f32_16x16x32_bf16 v[88:91], v[248:251], v[124:127], v[72:75]\n\t"
    "v_mfma_f32_16x16x32_bf16 v[92:95], v[252:255], v[124:127], v[76:79]\n\t"
    "s_mov_b64 s[36:37], s[38:39]\n\t"
    "s_add_u32 s38, s38, %[ab]\n\t"
    "s_addc_u32 s39, s39, 0\n\t"
    "s_sub_u32 m0, %[lf], %[b]\n\t"
    "s_sub_u32 m0, m0, 1\n\t"
    "s_cmp_lt_u32 m0, %[up]\n\t"
    "s_cbranch_scc1 1b\n\t"
    "s_waitcnt lgkmcnt(0)\n\t"
    "s_nop 4\n\t"
    "s_branch 9f\n\t"
    "5:\n\t"
    "s_add_u32 %[st], %[b], %[po]\n\t"
    "s_max_i32 %[st], %[st], %[p0]\n\t"
    "v_mov_b32 v39, %[st]\n\t"
    "v_mov_b32 v38, 0\n\t"
    "s_mov_b64 exec, 1\n\t"
    "global_store_dword v38, v39, %[pg] sc1\n\t"
    "s_mov_b64 exec, -1\n\t"
    "s_mov_b32 %[p0], %[p1]\n\t"
    "s_branch 6b\n\t"
    "7:\n\t"
    "s_cmp_ge_i32 %[lf], %[cp]\n\t"
    "s_cbranch_scc1 8b\n\t"
    "s_mov_b32 m0, %[sl]\n\t"
    "s_nop 0\n\t"
    "global_load_lds_dword %[dv], %[db] sc1\n\t"
    "s_branch 8b\n\t"
    "9:"
    : "=&{v[80:95]}"(pd), [ea] "+&v"(ea), [da] "+&v"(da), [b] "+&s"(b), [lf] "+&s"(lf), "+{s[36:37]}"(p1), "+{s[38:39]}"(p0), [sa] "+&v"(sa), [st] "=&s"(st), [sl] "+&s"(sl), [p0] "+&s"(pf)
    : [rw] "v"(rw), [ab] "s"(ab), [cp] "s"(cp), [mu] "s"(mu), [ad] "v"(ad), [up] "s"(up), "{v[128:159]}"(W[0]), "{v[160:191]}"(W[1]), "{v[192:223]}"(W[2]), "{v[224:255]}"(W[3]), [co0] "v"(co0), [w0] "s"(w0), [dv] "v"(dv), [db] "s"(db), [pg] "s"(pg), [po] "s"(po - 1), [p1] "s"(b + po), [co1] "v"(co1)
    : "v38", "v39", "v40", "v41", "v42", "v43", "v44", "v45", "v46", "v47", "v48", "v49", "v50", "v51", "v52", "v53", "v54", "v55", "v56", "v57", "v58", "v59", "v60", "v61", "v62", "v63", "v64", "v65", "v66", "v67", "v68", "v69", "v70", "v71", "v72", "v73", "v74", "v75", "v76", "v77", "v78", "v79", "v96", "v97", "v98", "v99", "v100", "v101", "v102", "v103", "v104", "v105", "v106", "v107", "v108", "v109", "v110", "v111", "v112", "v113", "v114", "v115", "v116", "v117", "v118", "v119", "v120", "v121", "v122", "v123", "v124", "v125", "v126", "v127", "vcc", "scc", "m0", "memory");
}
__device__ __forceinline__ void lean_ld0(const u32x32 (&W)[4], unsigned ea, unsigned da, unsigned x, unsigned lb, const char* s0, const char* s1, const unsigned (&av)[8], unsigned ps, unsigned dv, const char* db, unsigned rw, unsigned fa, unsigned fv,
    unsigned ga, unsigned mu, unsigned ad, int th, int tb, unsigned& sv, unsigned& su, unsigned& got) {
  asm volatile("s_setprio 2\n\t"
    "v_mov_b32 v76, %[ga]\n\t"
    "ds_read_b32 v76, v76 offset:24\n\t"
    "ds_read_b128 v[96:99], %[ea] offset:0\n\t"
    "ds_read_b128 v[100:103], %[ea] offset:2048\n\t"
    "ds_read_b128 v[104:107], %[da] offset:0\n\t"
    "ds_read_b128 v[108:111], %[da] offset:2048\n\t"
    "ds_read_b128 v[112:115], %[ea] offset:4096\n\t"
    "ds_read_b128 v[116:119], %[ea] offset:6144\n\t"
    "s_waitcnt lgkmcnt(6)\n\t"
    "v_cmp_ne_u32 vcc, 0, v76\n\t"
    "s_mov_b32 %[got], -1\n\t"
    "s_cbranch_vccnz 12f\n\t"
    "s_waitcnt lgkmcnt(4)\n\t"
    "v_mfma_f32_16x16x32_bf16 v[80:83], v[128:131], v[96:99], 0\n\t"
    "ds_read_b128 v[120:123], %[da] offset:4096\n\t"
    "v_mfma_f32_16x16x32_bf16 v[84:87], v[132:135], v[96:99], 0\n\t"
    "ds_read_b128 v[124:127], %[da] offset:6144\n\t"
    "v_mfma_f32_16x16x32_bf16 v[88:91], v[128:131], v[100:103], 0\n\t"
    "v_mfma_f32_16x16x32_bf16 v[92:95], v[132:135], v[100:103], 0\n\t"
    "s_waitcnt vmcnt(24)\n\t"
    "s_cmp_lt_u32 %[x], 2\n\t"
    "s_cbranch_scc0 41f\n\t"
    "s_waitcnt vmcnt(8)\n\t"
    "41:\n\t"
    "s_barrier\n\t"
    "s_waitcnt lgkmcnt(4)\n\t"
    "v_mfma_f32_16x16x32_bf16 v[80:83], v[136:139], v[104:107], v[80:83]\n\t"
    "ds_read_b128 v[96:99], %[ea] offset:8192\n\t"
    "v_mfma_f32_16x16x32_bf16 v[84:87], v[140:143], v[104:107], v[84:87]\n\t"
    "ds_read_b128 v[100:103], %[ea] offset:10240\n\t"
    "v_mfma_f32_16x16x32_bf16 v[88:91], v[136:139], v[108:111], v[88:91]\n\t"
    "v_mfma_f32_16x16x32_bf16 v[92:95], v[140:143], v[108:111], v[92:95]\n\t"
    "s_waitcnt lgkmcnt(4)\n\t"
    "v_mfma_f32_16x16x32_bf16 v[80:83], v[144:147], v[112:115], v[80:83]\n\t"
    "ds_read_b128 v[104:107], %[da] offset:8192\n\t"
    "v_mfma_f32_16x16x32_bf16 v[84:87], v[148:151], v[112:115], v[84:87]\n\t"
    "ds_read_b128 v[108:111], %[da] offset:10240\n\t"
    "v_mfma_f32_16x16x32_bf16 v[88:91], v[144:147], v[116:119], v[88:91]\n\t"
    "v_mfma_f32_16x16x32_bf16 v[92:95], v[148:151], v[116:119], v[92:95]\n\t"
    "s_waitcnt lgkmcnt(4)\n\t"
    "v_mfma_f32_16x16x32_bf16 v[80:83], v[152:155], v[120:123], v[80:83]\n\t"
    "ds_read_b128 v[112:115], %[ea] offset:12288\n\t"
    "v_mfma_f32_16x16x32_bf16 v[84:87], v[156:159], v[120:123], v[84:87]\n\t"
    "ds_read_b128 v[116:119], %[ea] offset:14336\n\t"
    "v_mfma_f32_16x16x32_bf16 v[88:91], v[152:155], v[124:127], v[88:91]\n\t"
    "v_mfma_f32_16x16x32_bf16 v[92:95], v[156:159], v[124:127], v[92:95]\n\t"
    "s_waitcnt lgkmcnt(4)\n\t"
    "v_mfma_f32_16x16x32_bf16 v[80:83], v[160:163], v[96:99], v[80:83]\n\t"
    "ds_read_b128 v[120:123], %[da] offset:12288\n\t"
    "v_mfma_f32_16x16x32_bf16 v[84:87], v[164:167], v[96:99], v[84:87]\n\t"
    "ds_read_b128 v[124:127], %[da] offset:14336\n\t"
    "v_mfma_f32_16x16x32_bf16 v[88:91], v[160:163], v[100:103], v[88:91]\n\t"
    "v_mfma_f32_16x16x32_bf16 v[92:95], v[164:167], v[100:103], v[92:95]\n\t"
    "s_waitcnt vmcnt(20)\n\t"
    "s_cmp_lt_u32 %[x], 2\n\t"
    "s_cbranch_scc0 45f\n\t"
    "s_waitcnt vmcnt(4)\n\t"
    "45:\n\t"
    "s_barrier\n\t"
    "s_waitcnt lgkmcnt(4)\n\t"
    "v_mfma_f32_16x16x32_bf16 v[80:83], v[168:171], v[104:107], v[80:83]\n\t"
    "ds_read_b128 v[96:99], %[ea] offset:16384\n\t"
    "v_mfma_f32_16x16x32_bf16 v[84:87], v[172:175], v[104:107], v[84:87]\n\t"
    "ds_read_b128 v[100:103], %[ea] offset:18432\n\t"
    "v_mfma_f32_16x16x32_bf16 v[88:91], v[168:171], v[108:111], v[88:91]\n\t"
    "v_mfma_f32_16x16x32_bf16 v[92:95], v[172:175], v[108:111], v[92:95]\n\t"
    "s_waitcnt lgkmcnt(4)\n\t"
    "v_mfma_f32_16x16x32_bf16 v[80:83], v[176:179], v[112:115], v[80:83]\n\t"
    "ds_read_b128 v[104:107], %[da] offset:16384\n\t"
    "v_mfma_f32_16x16x32_bf16 v[84:87], v[180:183], v[112:115], v[84:87]\n\t"
    "ds_read_b128 v[108:111], %[da] offset:18432\n\t"
    "v_mfma_f32_16x16x32_bf16 v[88:91], v[176:179], v[116:119], v[88:91]\n\t"
    "v_mfma_f32_16x16x32_bf16 v[92:95], v[180:183], v[116:119], v[92:95]\n\t"
    "s_waitcnt lgkmcnt(4)\n\t"
    "v_mfma_f32_16x16x32_bf16 v[80:83], v[184:187], v[120:123], v[80:83]\n\t"
    "ds_read_b128 v[112:115], %[ea] offset:20480\n\t"
    "v_mfma_f32_16x16x32_bf16 v[84:87], v[188:191], v[120:123], v[84:87]\n\t"
    "ds_read_b128 v[116:119], %[ea] offset:22528\n\t"
    "v_mfma_f32_16x16x32_bf16 v[88:91], v[184:187], v[124:127], v[88:91]\n\t"
    "v_mfma_f32_16x16x32_bf16 v[92:95], v[188:191], v[124:127], v[92:95]\n\t"
    "s_waitcnt lgkmcnt(4)\n\t"
    "v_mfma_f32_16x16x32_bf16 v[80:83], v[192:195], v[96:99], v[80:83]\n\t"
    "ds_read_b128 v[120:123], %[da] offset:20480\n\t"
    "v_mfma_f32_16x16x32_bf16 v[84:87], v[196:199], v[96:99], v[84:87]\n\t"
    "ds_read_b128 v[124:127], %[da] offset:22528\n\t"
    "v_mfma_f32_16x16x32_bf16 v[88:91], v[192:195], v[100:103], v[88:91]\n\t"
    "v_mfma_f32_16x16x32_bf16 v[92:95], v[196:199], v[100:103], v[92:95]\n\t"
    "s_waitcnt vmcnt(16)\n\t"
    "s_cmp_lt_u32 %[x], 2\n\t"
    "s_cbranch_scc0 49f\n\t"
    "s_waitcnt vmcnt(0)\n\t"
    "49:\n\t"
    "s_barrier\n\t"
    "s_waitcnt lgkmcnt(4)\n\t"
    "v_mfma_f32_16x16x32_bf16 v[80:83], v[200:203], v[104:107], v[80:83]\n\t"
    "ds_read_b128 v[96:99], %[ea] offset:24576\n\t"
    "v_mfma_f32_16x16x32_bf16 v[84:87], v[204:207], v[104:107], v[84:87]\n\t"
    "ds_read_b128 v[100:103], %[ea] offset:26624\n\t"
    "v_mfma_f32_16x16x32_bf16 v[88:91], v[200:203], v[108:111], v[88:91]\n\t"
    "v_mfma_f32_16x16x32_bf16 v[92:95], v[204:207], v[108:111], v[92:95]\n\t"
    "s_waitcnt lgkmcnt(4)\n\t"
    "v_mfma_f32_16x16x32_bf16 v[80:83], v[208:211], v[112:115], v[80:83]\n\t"
    "ds_read_b128 v[104:107], %[da] offset:24576\n\t"
    "v_mfma_f32_16x16x32_bf16 v[84:87], v[212:215], v[112:115], v[84:87]\n\t"
    "ds_read_b128 v[108:111], %[da] offset:26624\n\t"
    "v_mfma_f32_16x16x32_bf16 v[88:91], v[208:211], v[116:119], v[88:91]\n\t"
    "v_mfma_f32_16x16x32_bf16 v[92:95], v[212:215], v[116:119], v[92:95]\n\t"
    "s_cmp_eq_u32 %[ps], 0\n\t"
    "s_cbranch_scc1 10f\n\t"
    "global_load_dword %[sv], %[dv], %[db] sc1\n\t"
    "global_load_dword %[su], %[dv], %[db] offset:64 sc1\n\t"
    "10:\n\t"
    "s_waitcnt lgkmcnt(4)\n\t"
    "v_mfma_f32_16x16x32_bf16 v[80:83], v[216:219], v[120:123], v[80:83]\n\t"
    "ds_read_b128 v[112:115], %[ea] offset:28672\n\t"
    "v_mfma_f32_16x16x32_bf16 v[84:87], v[220:223], v[120:123], v[84:87]\n\t"
    "ds_read_b128 v[116:119], %[ea] offset:30720\n\t"
    "v_mfma_f32_16x16x32_bf16 v[88:91], v[216:219], v[124:127], v[88:91]\n\t"
    "v_mfma_f32_16x16x32_bf16 v[92:95], v[220:223], v[124:127], v[92:95]\n\t"
    "s_waitcnt lgkmcnt(4)\n\t"
    "v_mfma_f32_16x16x32_bf16 v[80:83], v[224:227], v[96:99], v[80:83]\n\t"
    "ds_read_b128 v[120:123], %[da] offset:28672\n\t"
    "v_mfma_f32_16x16x32_bf16 v[84:87], v[228:231], v[96:99], v[84:87]\n\t"
    "ds_read_b128 v[124:127], %[da] offset:30720\n\t"
    "v_mfma_f32_16x16x32_bf16 v[88:91], v[224:227], v[100:103], v[88:91]\n\t"
    "v_mfma_f32_16x16x32_bf16 v[92:95], v[228:231], v[100:103], v[92:95]\n\t"
    "s_waitcnt lgkmcnt(4)\n\t"
    "v_mfma_f32_16x16x32_bf16 v[80:83], v[232:235], v[104:107], v[80:83]\n\t"
    "v_mfma_f32_16x16x32_bf16 v[84:87], v[236:239], v[104:107], v[84:87]\n\t"
    "v_mfma_f32_16x16x32_bf16 v[88:91], v[232:235], v[108:111], v[88:91]\n\t"
    "v_mfma_f32_16x16x32_bf16 v[92:95], v[236:239], v[108:111], v[92:95]\n\t"
    "s_waitcnt lgkmcnt(2)\n\t"
    "v_mfma_f32_16x16x32_bf16 v[80:83], v[240:243], v[112:115], v[80:83]\n\t"
    "v_mfma_f32_16x16x32_bf16 v[84:87], v[244:247], v[112:115], v[84:87]\n\t"
    "v_mfma_f32_16x16x32_bf16 v[88:91], v[240:243], v[116:119], v[88:91]\n\t"
    "v_mfma_f32_16x16x32_bf16 v[92:95], v[244:247], v[116:119], v[92:95]\n\t"
    "s_waitcnt lgkmcnt(0)\n\t"
    "v_mfma_f32_16x16x32_bf16 v[80:83], v[248:251], v[120:123], v[80:83]\n\t"
    "s_memrealtime s[36:37]\n\t"
    "v_mfma_f32_16x16x32_bf16 v[84:87], v[252:255], v[120:123], v[84:87]\n\t"
    "v_mfma_f32_16x16x32_bf16 v[88:91], v[248:251], v[124:127], v[88:91]\n\t"
    "v_mfma_f32_16x16x32_bf16 v[92:95], v[252:255], v[124:127], v[92:95]\n\t"
    "s_mov_b32 %[got], 0\n\t"
    "v_mov_b32 v76, %[fa]\n\t"
    "v_mov_b32 v77, %[fv]\n\t"
    "v_add_u32 v78, 1, v77\n\t"
    "v_mov_b32 v79, %[ga]\n\t"
    "s_waitcnt lgkmcnt(0)\n\t"
    "ds_write_b128 %[rw], v[80:83] offset:0\n\t"
    "ds_write_b128 %[rw], v[84:87] offset:1024\n\t"
    "ds_write_b128 %[rw], v[88:91] offset:2048\n\t"
    "ds_write_b128 %[rw], v[92:95] offset:3072\n\t"
    "ds_write_b32 v76, v77\n\t"
    "s_cmp_eq_u32 %[ps], 0\n\t"
    "s_cbranch_scc1 9f\n\t"
    "s_waitcnt vmcnt(0)\n\t"
    "v_mad_u32_u24 v76, %[sv], %[mu], %[ad]\n\t"
    "v_cmp_ge_i32 vcc, %[th], v76\n\t"
    "s_cbranch_vccz 11f\n\t"
    "s_sub_u32 s36, s36, 20\n\t"
    "v_sub_u32 v76, s36, %[su]\n\t"
    "v_lshrrev_b32 v77, 24, %[su]\n\t"
    "v_lshlrev_b32 v76, 8, v76\n\t"
    "v_ashrrev_i32 v76, 31, v76\n\t"
    "v_add_u32 v77, v77, v76\n\t"
    "v_cmp_lt_i32 s[36:37], %[tb], v77\n\t"
    "s_andn2_b64 vcc, vcc, s[36:37]\n\t"
    "s_cbranch_scc1 9f\n\t"
    "11:\n\t"
    "ds_write2_b32 v79, v78, v78 offset1:1\n\t"
    "s_add_u32 m0, %[lb], 0\n\t"
    "s_nop 0\n\t"
    "global_load_lds_dwordx4 %[av0], %[s0] sc1\n\t"
    "s_add_u32 m0, %[lb], 32768\n\t"
    "s_nop 0\n\t"
    "global_load_lds_dwordx4 %[av1], %[s0] sc1\n\t"
    "s_add_u32 m0, %[lb], 4096\n\t"
    "s_nop 0\n\t"
    "global_load_lds_dwordx4 %[av2], %[s0] sc1\n\t"
    "s_add_u32 m0, %[lb], 36864\n\t"
    "s_nop 0\n\t"
    "global_load_lds_dwordx4 %[av3], %[s0] sc1\n\t"
    "s_add_u32 m0, %[lb], 8192\n\t"
    "s_nop 0\n\t"
    "global_load_lds_dwordx4 %[av4], %[s0] sc1\n\t"
    "s_add_u32 m0, %[lb], 40960\n\t"
    "s_nop 0\n\t"
    "global_load_lds_dwordx4 %[av5], %[s0] sc1\n\t"
    "s_add_u32 m0, %[lb], 12288\n\t"
    "s_nop 0\n\t"
    "global_load_lds_dwordx4 %[av6], %[s0] sc1\n\t"
    "s_add_u32 m0, %[lb], 45056\n\t"
    "s_nop 0\n\t"
    "global_load_lds_dwordx4 %[av7], %[s0] sc1\n\t"
    "s_add_u32 m0, %[lb], 16384\n\t"
    "s_nop 0\n\t"
    "global_load_lds_dwordx4 %[av0], %[s1] sc1\n\t"
    "s_add_u32 m0, %[lb], 49152\n\t"
    "s_nop 0\n\t"
    "global_load_lds_dwordx4 %[av1], %[s1] sc1\n\t"
    "s_add_u32 m0, %[lb], 20480\n\t"
    "s_nop 0\n\t"
    "global_load_lds_dwordx4 %[av2], %[s1] sc1\n\t"
    "s_add_u32 m0, %[lb], 53248\n\t"
    "s_nop 0\n\t"
    "global_load_lds_dwordx4 %[av3], %[s1] sc1\n\t"
    "s_add_u32 m0, %[lb], 24576\n\t"
    "s_nop 0\n\t"
    "global_load_lds_dwordx4 %[av4], %[s1] sc1\n\t"
    "s_add_u32 m0, %[lb], 57344\n\t"
    "s_nop 0\n\t"
    "global_load_lds_dwordx4 %[av5], %[s1] sc1\n\t"
    "s_add_u32 m0, %[lb], 28672\n\t"
    "s_nop 0\n\t"
    "global_load_lds_dwordx4 %[av6], %[s1] sc1\n\t"
    "s_add_u32 m0, %[lb], 61440\n\t"
    "s_nop 0\n\t"
    "global_load_lds_dwordx4 %[av7], %[s1] sc1\n\t"
    "s_mov_b32 %[got], 1\n\t"
    "s_branch 9f\n\t"
    "12:\n\t"
    "s_waitcnt lgkmcnt(0)\n\t"
    "9:"
    : [sv] "+&v"(sv), [su] "+&v"(su), [got] "=&s"(got)
    : [ea] "v"(ea), [da] "v"(da), [x] "s"(x), "{v[128:159]}"(W[0]), "{v[160:191]}"(W[1]), "{v[192:223]}"(W[2]), "{v[224:255]}"(W[3]), [lb] "s"(lb), [s0] "s"(s0), [s1] "s"(s1), [av0] "v"(av[0]), [av1] "v"(av[1]), [av2] "v"(av[2]), [av3] "v"(av[3]), [av4] "v"(av[4]), [av5] "v"(av[5]), [av6] "v"(av[6]), [av7] "v"(av[7]), [ps] "s"(ps), [dv] "v"(dv), [db] "s"(db), [rw] "v"(rw), [fa] "s"(fa), [fv] "s"(fv), [ga] "s"(ga), [mu] "s"(mu), [ad] "v"(ad), [th] "s"(th), [tb] "s"(tb)
    : "v96", "v97", "v98", "v99", "v100", "v101", "v102", "v103", "v104", "v105", "v106", "v107", "v108", "v109", "v110", "v111", "v112", "v113", "v114", "v115", "v116", "v117", "v118", "v119", "v120", "v121", "v122", "v123", "v124", "v125", "v126", "v127", "v76", "v77", "v78", "v79", "v80", "v81", "v82", "v83", "v84", "v85", "v86", "v87", "v88", "v89", "v90", "v91", "v92", "v93", "v94", "v95", "vcc", "scc", "m0", "memory", "s36", "s37", "s38", "s39");
}
__device__ __forceinline__ void lean_dm0(const u32x32 (&W)[4], unsigned ea, unsigned da, unsigned x, unsigned lb, const char* s0, const char* s1, const unsigned (&av)[8], unsigned ps, unsigned dv, const char* db, unsigned rw, unsigned fa, unsigned fv,
    unsigned ga, unsigned mu, unsigned ad, int th, int tb, unsigned& sv, unsigned& su, unsigned& got) {
  asm volatile("s_setprio 2\n\t"
    "v_mov_b32 v76, %[ga]\n\t"
    "ds_read_b32 v76, v76 offset:24\n\t"
    "ds_read_b128 v[96:99], %[ea] offset:0\n\t"
    "ds_read_b128 v[100:103], %[ea] offset:2048\n\t"
    "ds_read_b128 v[104:107], %[da] offset:0\n\t"
    "ds_read_b128 v[108:111], %[da] offset:2048\n\t"
    "ds_read_b128 v[112:115], %[ea] offset:4096\n\t"
    "ds_read_b128 v[116:119], %[ea] offset:6144\n\t"
    "s_waitcnt lgkmcnt(6)\n\t"
    "v_cmp_ne_u32 vcc, 0, v76\n\t"
    "s_mov_b32 %[got], -1\n\t"
    "s_cbranch_vccnz 12f\n\t"
    "s_waitcnt lgkmcnt(4)\n\t"
    "v_mfma_f32_16x16x32_bf16 v[80:83], v[128:131], v[96:99], 0\n\t"
    "ds_read_b128 v[120:123], %[da] offset:4096\n\t"
    "s_add_u32 m0, %[lb], 0\n\t"
    "v_mfma_f32_16x16x32_bf16 v[84:87], v[132:135], v[96:99], 0\n\t"
    "ds_read_b128 v[124:127], %[da] offset:6144\n\t"
    "global_load_lds_dwordx4 %[av0], %[s0] sc1\n\t"
    "v_mfma_f32_16x16x32_bf16 v[88:91], v[128:131], v[100:103], 0\n\t"
    "s_add_u32 m0, %[lb], 32768\n\t"
    "v_mfma_f32_16x16x32_bf16 v[92:95], v[132:135], v[100:103], 0\n\t"
    "global_load_lds_dwordx4 %[av1], %[s0] sc1\n\t"
    "s_waitcnt vmcnt(10)\n\t"
    "s_barrier\n\t"
    "s_waitcnt lgkmcnt(4)\n\t"
    "v_mfma_f32_16x16x32_bf16 v[80:83], v[136:139], v[104:107], v[80:83]\n\t"
    "ds_read_b128 v[96:99], %[ea] offset:8192\n\t"
    "s_add_u32 m0, %[lb], 4096\n\t"
    "v_mfma_f32_16x16x32_bf16 v[84:87], v[140:143], v[104:107], v[84:87]\n\t"
    "ds_read_b128 v[100:103], %[ea] offset:10240\n\t"
    "global_load_lds_dwordx4 %[av2], %[s0] sc1\n\t"
    "v_mfma_f32_16x16x32_bf16 v[88:91], v[136:139], v[108:111], v[88:91]\n\t"
    "s_add_u32 m0, %[lb], 36864\n\t"
    "v_mfma_f32_16x16x32_bf16 v[92:95], v[140:143], v[108:111], v[92:95]\n\t"
    "global_load_lds_dwordx4 %[av3], %[s0] sc1\n\t"
    "s_waitcnt lgkmcnt(4)\n\t"
    "v_mfma_f32_16x16x32_bf16 v[80:83], v[144:147], v[112:115], v[80:83]\n\t"
    "ds_read_b128 v[104:107], %[da] offset:8192\n\t"
    "s_add_u32 m0, %[lb], 8192\n\t"
    "v_mfma_f32_16x16x32_bf16 v[84:87], v[148:151], v[112:115], v[84:87]\n\t"
    "ds_read_b128 v[108:111], %[da] offset:10240\n\t"
    "global_load_lds_dwordx4 %[av4], %[s0] sc1\n\t"
    "v_mfma_f32_16x16x32_bf16 v[88:91], v[144:147], v[116:119], v[88:91]\n\t"
    "s_add_u32 m0, %[lb], 40960\n\t"
    "v_mfma_f32_16x16x32_bf16 v[92:95], v[148:151], v[116:119], v[92:95]\n\t"
    "global_load_lds_dwordx4 %[av5], %[s0] sc1\n\t"
    "s_waitcnt lgkmcnt(4)\n\t"
    "v_mfma_f32_16x16x32_bf16 v[80:83], v[152:155], v[120:123], v[80:83]\n\t"
    "ds_read_b128 v[112:115], %[ea] offset:12288\n\t"
    "s_add_u32 m0, %[lb], 12288\n\t"
    "v_mfma_f32_16x16x32_bf16 v[84:87], v[156:159], v[120:123], v[84:87]\n\t"
    "ds_read_b128 v[116:119], %[ea] offset:14336\n\t"
    "global_load_lds_dwordx4 %[av6], %[s0] sc1\n\t"
    "v_mfma_f32_16x16x32_bf16 v[88:91], v[152:155], v[124:127], v[88:91]\n\t"
    "s_add_u32 m0, %[lb], 45056\n\t"
    "v_mfma_f32_16x16x32_bf16 v[92:95], v[156:159], v[124:127], v[92:95]\n\t"
    "global_load_lds_dwordx4 %[av7], %[s0] sc1\n\t"
    "s_waitcnt lgkmcnt(4)\n\t"
    "v_mfma_f32_16x16x32_bf16 v[80:83], v[160:163], v[96:99], v[80:83]\n\t"
    "ds_read_b128 v[120:123], %[da] offset:12288\n\t"
    "s_add_u32 m0, %[lb], 16384\n\t"
    "v_mfma_f32_16x16x32_bf16 v[84:87], v[164:167], v[96:99], v[84:87]\n\t"
    "ds_read_b128 v[124:127], %[da] offset:14336\n\t"
    "global_load_lds_dwordx4 %[av0], %[s1] sc1\n\t"
    "v_mfma_f32_16x16x32_bf16 v[88:91], v[160:163], v[100:103], v[88:91]\n\t"
    "s_add_u32 m0, %[lb], 49152\n\t"
    "v_mfma_f32_16x16x32_bf16 v[92:95], v[164:167], v[100:103], v[92:95]\n\t"
    "global_load_lds_dwordx4 %[av1], %[s1] sc1\n\t"
    "s_waitcnt vmcnt(14)\n\t"
    "s_barrier\n\t"
    "s_waitcnt lgkmcnt(4)\n\t"
    "v_mfma_f32_16x16x32_bf16 v[80:83], v[168:171], v[104:107], v[80:83]\n\t"
    "ds_read_b128 v[96:99], %[ea] offset:16384\n\t"
    "s_add_u32 m0, %[lb], 20480\n\t"
    "v_mfma_f32_16x16x32_bf16 v[84:87], v[172:175], v[104:107], v[84:87]\n\t"
    "ds_read_b128 v[100:103], %[ea] offset:18432\n\t"
    "global_load_lds_dwordx4 %[av2], %[s1] sc1\n\t"
    "v_mfma_f32_16x16x32_bf16 v[88:91], v[168:171], v[108:111], v[88:91]\n\t"
    "s_add_u32 m0, %[lb], 53248\n\t"
    "v_mfma_f32_16x16x32_bf16 v[92:95], v[172:175], v[108:111], v[92:95]\n\t"
    "global_load_lds_dwordx4 %[av3], %[s1] sc1\n\t"
    "s_waitcnt lgkmcnt(4)\n\t"
    "v_mfma_f32_16x16x32_bf16 v[80:83], v[176:179], v[112:115], v[80:83]\n\t"
    "ds_read_b128 v[104:107], %[da] offset:16384\n\t"
    "s_add_u32 m0, %[lb], 24576\n\t"
    "v_mfma_f32_16x16x32_bf16 v[84:87], v[180:183], v[112:115], v[84:87]\n\t"
    "ds_read_b128 v[108:111], %[da] offset:18432\n\t"
    "global_load_lds_dwordx4 %[av4], %[s1] sc1\n\t"
    "v_mfma_f32_16x16x32_bf16 v[88:91], v[176:179], v[116:119], v[88:91]\n\t"
    "s_add_u32 m0, %[lb], 57344\n\t"
    "v_mfma_f32_16x16x32_bf16 v[92:95], v[180:183], v[116:119], v[92:95]\n\t"
    "global_load_lds_dwordx4 %[av5], %[s1] sc1\n\t"
    "s_waitcnt lgkmcnt(4)\n\t"
    "v_mfma_f32_16x16x32_bf16 v[80:83], v[184:187], v[120:123], v[80:83]\n\t"
    "ds_read_b128 v[112:115], %[ea] offset:20480\n\t"
    "s_add_u32 m0, %[lb], 28672\n\t"
    "v_mfma_f32_16x16x32_bf16 v[84:87], v[188:191], v[120:123], v[84:87]\n\t"
    "ds_read_b128 v[116:119], %[ea] offset:22528\n\t"
    "global_load_lds_dwordx4 %[av6], %[s1] sc1\n\t"
    "v_mfma_f32_16x16x32_bf16 v[88:91], v[184:187], v[124:127], v[88:91]\n\t"
    "s_add_u32 m0, %[lb], 61440\n\t"
    "v_mfma_f32_16x16x32_bf16 v[92:95], v[188:191], v[124:127], v[92:95]\n\t"
    "global_load_lds_dwordx4 %[av7], %[s1] sc1\n\t"
    "s_waitcnt lgkmcnt(4)\n\t"
    "v_mfma_f32_16x16x32_bf16 v[80:83], v[192:195], v[96:99], v[80:83]\n\t"
    "ds_read_b128 v[120:123], %[da] offset:20480\n\t"
    "v_mfma_f32_16x16x32_bf16 v[84:87], v[196:199], v[96:99], v[84:87]\n\t"
    "ds_read_b128 v[124:127], %[da] offset:22528\n\t"
    "v_mfma_f32_16x16x32_bf16 v[88:91], v[192:195], v[100:103], v[88:91]\n\t"
    "v_mfma_f32_16x16x32_bf16 v[92:95], v[196:199], v[100:103], v[92:95]\n\t"
    "s_waitcnt vmcnt(16)\n\t"
    "s_barrier\n\t"
    "s_waitcnt lgkmcnt(4)\n\t"
    "v_mfma_f32_16x16x32_bf16 v[80:83], v[200:203], v[104:107], v[80:83]\n\t"
    "ds_read_b128 v[96:99], %[ea] offset:24576\n\t"
    "v_mfma_f32_16x16x32_bf16 v[84:87], v[204:207], v[104:107], v[84:87]\n\t"
    "ds_read_b128 v[100:103], %[ea] offset:26624\n\t"
    "v_mfma_f32_16x16x32_bf16 v[88:91], v[200:203], v[108:111], v[88:91]\n\t"
    "v_mfma_f32_16x16x32_bf16 v[92:95], v[204:207], v[108:111], v[92:95]\n\t"
    "s_waitcnt lgkmcnt(4)\n\t"
    "v_mfma_f32_16x16x32_bf16 v[80:83], v[208:211], v[112:115], v[80:83]\n\t"
    "ds_read_b128 v[104:107], %[da] offset:24576\n\t"
    "v_mfma_f32_16x16x32_bf16 v[84:87], v[212:215], v[112:115], v[84:87]\n\t"
    "ds_read_b128 v[108:111], %[da] offset:26624\n\t"
    "v_mfma_f32_16x16x32_bf16 v[88:91], v[208:211], v[116:119], v[88:91]\n\t"
    "v_mfma_f32_16x16x32_bf16 v[92:95], v[212:215], v[116:119], v[92:95]\n\t"
    "s_waitcnt lgkmcnt(4)\n\t"
    "v_mfma_f32_16x16x32_bf16 v[80:83], v[216:219], v[120:123], v[80:83]\n\t"
    "ds_read_b128 v[112:115], %[ea] offset:28672\n\t"
    "v_mfma_f32_16x16x32_bf16 v[84:87], v[220:223], v[120:123], v[84:87]\n\t"
    "ds_read_b128 v[116:119], %[ea] offset:30720\n\t"
    "v_mfma_f32_16x16x32_bf16 v[88:91], v[216:219], v[124:127], v[88:91]\n\t"
    "v_mfma_f32_16x16x32_bf16 v[92:95], v[220:223], v[124:127], v[92:95]\n\t"
    "s_waitcnt lgkmcnt(4)\n\t"
    "v_mfma_f32_16x16x32_bf16 v[80:83], v[224:227], v[96:99], v[80:83]\n\t"
    "ds_read_b128 v[120:123], %[da] offset:28672\n\t"
    "v_mfma_f32_16x16x32_bf16 v[84:87], v[228:231], v[96:99], v[84:87]\n\t"
    "ds_read_b128 v[124:127], %[da] offset:30720\n\t"
    "v_mfma_f32_16x16x32_bf16 v[88:91], v[224:227], v[100:103], v[88:91]\n\t"
    "v_mfma_f32_16x16x32_bf16 v[92:95], v[228:231], v[100:103], v[92:95]\n\t"
    "s_waitcnt lgkmcnt(4)\n\t"
    "v_mfma_f32_16x16x32_bf16 v[80:83], v[232:235], v[104:107], v[80:83]\n\t"
    "v_mfma_f32_16x16x32_bf16 v[84:87], v[236:239], v[104:107], v[84:87]\n\t"
    "v_mfma_f32_16x16x32_bf16 v[88:91], v[232:235], v[108:111], v[88:91]\n\t"
    "v_mfma_f32_16x16x32_bf16 v[92:95], v[236:239], v[108:111], v[92:95]\n\t"
    "s_waitcnt lgkmcnt(2)\n\t"
    "v_mfma_f32_16x16x32_bf16 v[80:83], v[240:243], v[112:115], v[80:83]\n\t"
    "v_mfma_f32_16x16x32_bf16 v[84:87], v[244:247], v[112:115], v[84:87]\n\t"
    "v_mfma_f32_16x16x32_bf16 v[88:91], v[240:243], v[116:119], v[88:91]\n\t"
    "v_mfma_f32_16x16x32_bf16 v[92:95], v[244:247], v[116:119], v[92:95]\n\t"
    "s_waitcnt lgkmcnt(0)\n\t"
    "v_mfma_f32_16x16x32_bf16 v[80:83], v[248:251], v[120:123], v[80:83]\n\t"
    "v_mfma_f32_16x16x32_bf16 v[84:87], v[252:255], v[120:123], v[84:87]\n\t"
    "v_mfma_f32_16x16x32_bf16 v[88:91], v[248:251], v[124:127], v[88:91]\n\t"
    "v_mfma_f32_16x16x32_bf16 v[92:95], v[252:255], v[124:127], v[92:95]\n\t"
    "s_cmp_eq_u32 %[ps], 0\n\t"
    "s_cbranch_scc1 10f\n\t"
    "global_load_dword %[sv], %[dv], %[db] sc1\n\t"
    "global_load_dword %[su], %[dv], %[db] offset:64 sc1\n\t"
    "10:\n\t"
    "s_mov_b32 %[got], 0\n\t"
    "v_mov_b32 v76, %[fa]\n\t"
    "v_mov_b32 v77, %[fv]\n\t"
    "v_add_u32 v78, 1, v77\n\t"
    "v_mov_b32 v79, %[ga]\n\t"
    "ds_write_b128 %[rw], v[80:83] offset:0\n\t"
    "ds_write_b128 %[rw], v[84:87] offset:1024\n\t"
    "ds_write_b128 %[rw], v[88:91] offset:2048\n\t"
    "ds_write_b128 %[rw], v[92:95] offset:3072\n\t"
    "ds_write_b32 v76, v77\n\t"
    "s_cmp_eq_u32 %[ps], 0\n\t"
    "s_cbranch_scc1 9f\n\t"
    "s_waitcnt vmcnt(0)\n\t"
    "s_branch 9f\n\t"
    "12:\n\t"
    "s_waitcnt lgkmcnt(0)\n\t"
    "9:"
    : [sv] "+&v"(sv), [su] "+&v"(su), [got] "=&s"(got)
    : [ea] "v"(ea), [da] "v"(da), [x] "s"(x), "{v[128:159]}"(W[0]), "{v[160:191]}"(W[1]), "{v[192:223]}"(W[2]), "{v[224:255]}"(W[3]), [lb] "s"(lb), [s0] "s"(s0), [s1] "s"(s1), [av0] "v"(av[0]), [av1] "v"(av[1]), [av2] "v"(av[2]), [av3] "v"(av[3]), [av4] "v"(av[4]), [av5] "v"(av[5]), [av6] "v"(av[6]), [av7] "v"(av[7]), [ps] "s"(ps), [dv] "v"(dv), [db] "s"(db), [rw] "v"(rw), [fa] "s"(fa), [fv] "s"(fv), [ga] "s"(ga), [mu] "s"(mu), [ad] "v"(ad), [th] "s"(th), [tb] "s"(tb)
    : "v96", "v97", "v98", "v99", "v100", "v101", "v102", "v103", "v104", "v105", "v106", "v107", "v108", "v109", "v110", "v111", "v112", "v113", "v114", "v115", "v116", "v117", "v118", "v119", "v120", "v121", "v122", "v123", "v124", "v125", "v126", "v127", "v76", "v77", "v78", "v79", "v80", "v81", "v82", "v83", "v84", "v85", "v86", "v87", "v88", "v89", "v90", "v91", "v92", "v93", "v94", "v95", "vcc", "scc", "m0", "memory");
}
__device__ __forceinline__ void lean_ld1(const u32x32 (&W)[4], unsigned ea, unsigned da, unsigned x, unsigned lb, const char* s0, const char* s1, const unsigned (&av)[8], unsigned ps, unsigned dv, const char* db, unsigned rw, unsigned fa, unsigned fv,
    unsigned ga, unsigned mu, unsigned ad, int th, int tb, unsigned& sv, unsigned& su, unsigned& got) {
  asm volatile("s_setprio 2\n\t"
    "v_mov_b32 v76, %[ga]\n\t"
    "ds_read_b32 v76, v76 offset:24\n\t"
    "ds_read_b128 v[96:99], %[ea] offset:0\n\t"
    "ds_read_b128 v[100:103], %[ea] offset:2048\n\t"
    "ds_read_b128 v[104:107], %[da] offset:0\n\t"
    "ds_read_b128 v[108:111], %[da] offset:2048\n\t"
    "ds_read_b128 v[112:115], %[ea] offset:4096\n\t"
    "ds_read_b128 v[116:119], %[ea] offset:6144\n\t"
    "s_waitcnt lgkmcnt(6)\n\t"
    "v_cmp_ne_u32 vcc, 0, v76\n\t"
    "s_mov_b32 %[got], -1\n\t"
    "s_cbranch_vccnz 12f\n\t"
    "s_waitcnt lgkmcnt(4)\n\t"
    "v_mfma_f32_16x16x32_bf16 v[80:83], v[128:131], v[96:99], 0\n\t"
    "ds_read_b128 v[120:123], %[da] offset:4096\n\t"
    "v_mfma_f32_16x16x32_bf16 v[84:87], v[132:135], v[96:99], 0\n\t"
    "ds_read_b128 v[124:127], %[da] offset:6144\n\t"
    "v_mfma_f32_16x16x32_bf16 v[88:91], v[128:131], v[100:103], 0\n\t"
    "v_mfma_f32_16x16x32_bf16 v[92:95], v[132:135], v[100:103], 0\n\t"
    "s_waitcnt vmcnt(12)\n\t"
    "s_cmp_lt_u32 %[x], 2\n\t"
    "s_cbranch_scc0 41f\n\t"
    "s_waitcnt vmcnt(4)\n\t"
    "41:\n\t"
    "s_barrier\n\t"
    "s_waitcnt lgkmcnt(4)\n\t"
    "v_mfma_f32_16x16x32_bf16 v[80:83], v[136:139], v[104:107], v[80:83]\n\t"
    "ds_read_b128 v[96:99], %[ea] offset:8192\n\t"
    "v_mfma_f32_16x16x32_bf16 v[84:87], v[140:143], v[104:107], v[84:87]\n\t"
    "ds_read_b128 v[100:103], %[ea] offset:10240\n\t"
    "v_mfma_f32_16x16x32_bf16 v[88:91], v[136:139], v[108:111], v[88:91]\n\t"
    "v_mfma_f32_16x16x32_bf16 v[92:95], v[140:143], v[108:111], v[92:95]\n\t"
    "s_waitcnt lgkmcnt(4)\n\t"
    "v_mfma_f32_16x16x32_bf16 v[80:83], v[144:147], v[112:115], v[80:83]\n\t"
    "ds_read_b128 v[104:107], %[da] offset:8192\n\t"
    "v_mfma_f32_16x16x32_bf16 v[84:87], v[148:151], v[112:115], v[84:87]\n\t"
    "ds_read_b128 v[108:111], %[da] offset:10240\n\t"
    "v_mfma_f32_16x16x32_bf16 v[88:91], v[144:147], v[116:119], v[88:91]\n\t"
    "v_mfma_f32_16x16x32_bf16 v[92:95], v[148:151], v[116:119], v[92:95]\n\t"
    "s_waitcnt lgkmcnt(4)\n\t"
    "v_mfma_f32_16x16x32_bf16 v[80:83], v[152:155], v[120:123], v[80:83]\n\t"
    "ds_read_b128 v[112:115], %[ea] offset:12288\n\t"
    "v_mfma_f32_16x16x32_bf16 v[84:87], v[156:159], v[120:123], v[84:87]\n\t"
    "ds_read_b128 v[116:119], %[ea] offset:14336\n\t"
    "v_mfma_f32_16x16x32_bf16 v[88:91], v[152:155], v[124:127], v[88:91]\n\t"
    "v_mfma_f32_16x16x32_bf16 v[92:95], v[156:159], v[124:127], v[92:95]\n\t"
    "s_waitcnt lgkmcnt(4)\n\t"
    "v_mfma_f32_16x16x32_bf16 v[80:83], v[160:163], v[96:99], v[80:83]\n\t"
    "ds_read_b128 v[120:123], %[da] offset:12288\n\t"
    "v_mfma_f32_16x16x32_bf16 v[84:87], v[164:167], v[96:99], v[84:87]\n\t"
    "ds_read_b128 v[124:127], %[da] offset:14336\n\t"
    "v_mfma_f32_16x16x32_bf16 v[88:91], v[160:163], v[100:103], v[88:91]\n\t"
    "v_mfma_f32_16x16x32_bf16 v[92:95], v[164:167], v[100:103], v[92:95]\n\t"
    "s_waitcnt vmcnt(10)\n\t"
    "s_cmp_lt_u32 %[x], 2\n\t"
    "s_cbranch_scc0 45f\n\t"
    "s_waitcnt vmcnt(2)\n\t"
    "45:\n\t"
    "s_barrier\n\t"
    "s_waitcnt lgkmcnt(4)\n\t"
    "v_mfma_f32_16x16x32_bf16 v[80:83], v[168:171], v[104:107], v[80:83]\n\t"
    "ds_read_b128 v[96:99], %[ea] offset:16384\n\t"
    "v_mfma_f32_16x16x32_bf16 v[84:87], v[172:175], v[104:107], v[84:87]\n\t"
    "ds_read_b128 v[100:103], %[ea] offset:18432\n\t"
    "v_mfma_f32_16x16x32_bf16 v[88:91], v[168:171], v[108:111], v[88:91]\n\t"
    "v_mfma_f32_16x16x32_bf16 v[92:95], v[172:175], v[108:111], v[92:95]\n\t"
    "s_waitcnt lgkmcnt(4)\n\t"
    "v_mfma_f32_16x16x32_bf16 v[80:83], v[176:179], v[112:115], v[80:83]\n\t"
    "ds_read_b128 v[104:107], %[da] offset:16384\n\t"
    "v_mfma_f32_16x16x32_bf16 v[84:87], v[180:183], v[112:115], v[84:87]\n\t"
    "ds_read_b128 v[108:111], %[da] offset:18432\n\t"
    "v_mfma_f32_16x16x32_bf16 v[88:91], v[176:179], v[116:119], v[88:91]\n\t"
    "v_mfma_f32_16x16x32_bf16 v[92:95], v[180:183], v[116:119], v[92:95]\n\t"
    "s_waitcnt lgkmcnt(4)\n\t"
    "v_mfma_f32_16x16x32_bf16 v[80:83], v[184:187], v[120:123], v[80:83]\n\t"
    "ds_read_b128 v[112:115], %[ea] offset:20480\n\t"
    "v_mfma_f32_16x16x32_bf16 v[84:87], v[188:191], v[120:123], v[84:87]\n\t"
    "ds_read_b128 v[116:119], %[ea] offset:22528\n\t"
    "v_mfma_f32_16x16x32_bf16 v[88:91], v[184:187], v[124:127], v[88:91]\n\t"
    "v_mfma_f32_16x16x32_bf16 v[92:95], v[188:191], v[124:127], v[92:95]\n\t"
    "s_waitcnt lgkmcnt(4)\n\t"
    "v_mfma_f32_16x16x32_bf16 v[80:83], v[192:195], v[96:99], v[80:83]\n\t"
    "ds_read_b128 v[120:123], %[da] offset:20480\n\t"
    "v_mfma_f32_16x16x32_bf16 v[84:87], v[196:199], v[96:99], v[84:87]\n\t"
    "ds_read_b128 v[124:127], %[da] offset:22528\n\t"
    "v_mfma_f32_16x16x32_bf16 v[88:91], v[192:195], v[100:103], v[88:91]\n\t"
    "v_mfma_f32_16x16x32_bf16 v[92:95], v[196:199], v[100:103], v[92:95]\n\t"
    "s_waitcnt vmcnt(8)\n\t"
    "s_cmp_lt_u32 %[x], 2\n\t"
    "s_cbranch_scc0 49f\n\t"
    "s_waitcnt vmcnt(0)\n\t"
    "49:\n\t"
    "s_barrier\n\t"
    "s_waitcnt lgkmcnt(4)\n\t"
    "v_mfma_f32_16x16x32_bf16 v[80:83], v[200:203], v[104:107], v[80:83]\n\t"
    "ds_read_b128 v[96:99], %[ea] offset:24576\n\t"
    "v_mfma_f32_16x16x32_bf16 v[84:87], v[204:207], v[104:107], v[84:87]\n\t"
    "ds_read_b128 v[100:103], %[ea] offset:26624\n\t"
    "v_mfma_f32_16x16x32_bf16 v[88:91], v[200:203], v[108:111], v[88:91]\n\t"
    "v_mfma_f32_16x16x32_bf16 v[92:95], v[204:207], v[108:111], v[92:95]\n\t"
    "s_waitcnt lgkmcnt(4)\n\t"
    "v_mfma_f32_16x16x32_bf16 v[80:83], v[208:211], v[112:115], v[80:83]\n\t"
    "ds_read_b128 v[104:107], %[da] offset:24576\n\t"
    "v_mfma_f32_16x16x32_bf16 v[84:87], v[212:215], v[112:115], v[84:87]\n\t"
    "ds_read_b128 v[108:111], %[da] offset:26624\n\t"
    "v_mfma_f32_16x16x32_bf16 v[88:91], v[208:211], v[116:119], v[88:91]\n\t"
    "v_mfma_f32_16x16x32_bf16 v[92:95], v[212:215], v[116:119], v[92:95]\n\t"
    "s_cmp_eq_u32 %[ps], 0\n\t"
    "s_cbranch_scc1 10f\n\t"
    "global_load_dword %[sv], %[dv], %[db] sc1\n\t"
    "global_load_dword %[su], %[dv], %[db] offset:64 sc1\n\t"
    "10:\n\t"
    "s_waitcnt lgkmcnt(4)\n\t"
    "v_mfma_f32_16x16x32_bf16 v[80:83], v[216:219], v[120:123], v[80:83]\n\t"
    "ds_read_b128 v[112:115], %[ea] offset:28672\n\t"
    "v_mfma_f32_16x16x32_bf16 v[84:87], v[220:223], v[120:123], v[84:87]\n\t"
    "ds_read_b128 v[116:119], %[ea] offset:30720\n\t"
    "v_mfma_f32_16x16x32_bf16 v[88:91], v[216:219], v[124:127], v[88:91]\n\t"
    "v_mfma_f32_16x16x32_bf16 v[92:95], v[220:223], v[124:127], v[92:95]\n\t"
    "s_waitcnt lgkmcnt(4)\n\t"
    "v_mfma_f32_16x16x32_bf16 v[80:83], v[224:227], v[96:99], v[80:83]\n\t"
    "ds_read_b128 v[120:123], %[da] offset:28672\n\t"
    "v_mfma_f32_16x16x32_bf16 v[84:87], v[228:231], v[96:99], v[84:87]\n\t"
    "ds_read_b128 v[124:127], %[da] offset:30720\n\t"
    "v_mfma_f32_16x16x32_bf16 v[88:91], v[224:227], v[100:103], v[88:91]\n\t"
    "v_mfma_f32_16x16x32_bf16 v[92:95], v[228:231], v[100:103], v[92:95]\n\t"
    "s_waitcnt lgkmcnt(4)\n\t"
    "v_mfma_f32_16x16x32_bf16 v[80:83], v[232:235], v[104:107], v[80:83]\n\t"
    "v_mfma_f32_16x16x32_bf16 v[84:87], v[236:239], v[104:107], v[84:87]\n\t"
    "v_mfma_f32_16x16x32_bf16 v[88:91], v[232:235], v[108:111], v[88:91]\n\t"
    "v_mfma_f32_16x16x32_bf16 v[92:95], v[236:239], v[108:111], v[92:95]\n\t"
    "s_waitcnt lgkmcnt(2)\n\t"
    "v_mfma_f32_16x16x32_bf16 v[80:83], v[240:243], v[112:115], v[80:83]\n\t"
    "v_mfma_f32_16x16x32_bf16 v[84:87], v[244:247], v[112:115], v[84:87]\n\t"
    "v_mfma_f32_16x16x32_bf16 v[88:91], v[240:243], v[116:119], v[88:91]\n\t"
    "v_mfma_f32_16x16x32_bf16 v[92:95], v[244:247], v[116:119], v[92:95]\n\t"
    "s_waitcnt lgkmcnt(0)\n\t"
    "v_mfma_f32_16x16x32_bf16 v[80:83], v[248:251], v[120:123], v[80:83]\n\t"
    "s_memrealtime s[36:37]\n\t"
    "v_mfma_f32_16x16x32_bf16 v[84:87], v[252:255], v[120:123], v[84:87]\n\t"
    "v_mfma_f32_16x16x32_bf16 v[88:91], v[248:251], v[124:127], v[88:91]\n\t"
    "v_mfma_f32_16x16x32_bf16 v[92:95], v[252:255], v[124:127], v[92:95]\n\t"
    "s_mov_b32 %[got], 0\n\t"
    "v_mov_b32 v76, %[fa]\n\t"
    "v_mov_b32 v77, %[fv]\n\t"
    "v_add_u32 v78, 1, v77\n\t"
    "v_mov_b32 v79, %[ga]\n\t"
    "s_waitcnt lgkmcnt(0)\n\t"
    "ds_write_b128 %[rw], v[80:83] offset:0\n\t"
    "ds_write_b128 %[rw], v[84:87] offset:1024\n\t"
    "ds_write_b128 %[rw], v[88:91] offset:2048\n\t"
    "ds_write_b128 %[rw], v[92:95] offset:3072\n\t"
    "ds_write_b32 v76, v77\n\t"
    "s_cmp_eq_u32 %[ps], 0\n\t"
    "s_cbranch_scc1 9f\n\t"
    "s_waitcnt vmcnt(0)\n\t"
    "v_mad_u32_u24 v76, %[sv], %[mu], %[ad]\n\t"
    "v_cmp_ge_i32 vcc, %[th], v76\n\t"
    "s_cbranch_vccz 11f\n\t"
    "s_sub_u32 s36, s36, 20\n\t"
    "v_sub_u32 v76, s36, %[su]\n\t"
    "v_lshrrev_b32 v77, 24, %[su]\n\t"
    "v_lshlrev_b32 v76, 8, v76\n\t"
    "v_ashrrev_i32 v76, 31, v76\n\t"
    "v_add_u32 v77, v77, v76\n\t"
    "v_cmp_lt_i32 s[36:37], %[tb], v77\n\t"
    "s_andn2_b64 vcc, vcc, s[36:37]\n\t"
    "s_cbranch_scc1 9f\n\t"
    "11:\n\t"
    "ds_write2_b32 v79, v78, v78 offset1:1\n\t"
    "s_add_u32 m0, %[lb], 0\n\t"
    "s_nop 0\n\t"
    "global_load_lds_dwordx4 %[av0], %[s0] sc1\n\t"
    "s_add_u32 m0, %[lb], 4096\n\t"
    "s_nop 0\n\t"
    "global_load_lds_dwordx4 %[av2], %[s0] sc1\n\t"
    "s_add_u32 m0, %[lb], 8192\n\t"
    "s_nop 0\n\t"
    "global_load_lds_dwordx4 %[av4], %[s0] sc1\n\t"
    "s_add_u32 m0, %[lb], 12288\n\t"
    "s_nop 0\n\t"
    "global_load_lds_dwordx4 %[av6], %[s0] sc1\n\t"
    "s_add_u32 m0, %[lb], 16384\n\t"
    "s_nop 0\n\t"
    "global_load_lds_dwordx4 %[av0], %[s1] sc1\n\t"
    "s_add_u32 m0, %[lb], 20480\n\t"
    "s_nop 0\n\t"
    "global_load_lds_dwordx4 %[av2], %[s1] sc1\n\t"
    "s_add_u32 m0, %[lb], 24576\n\t"
    "s_nop 0\n\t"
    "global_load_lds_dwordx4 %[av4], %[s1] sc1\n\t"
    "s_add_u32 m0, %[lb], 28672\n\t"
    "s_nop 0\n\t"
    "global_load_lds_dwordx4 %[av6], %[s1] sc1\n\t"
    "s_mov_b32 %[got], 1\n\t"
    "s_branch 9f\n\t"
    "12:\n\t"
    "s_waitcnt lgkmcnt(0)\n\t"
    "9:"
    : [sv] "+&v"(sv), [su] "+&v"(su), [got] "=&s"(got)
    : [ea] "v"(ea), [da] "v"(da), [x] "s"(x), "{v[128:159]}"(W[0]), "{v[160:191]}"(W[1]), "{v[192:223]}"(W[2]), "{v[224:255]}"(W[3]), [lb] "s"(lb), [s0] "s"(s0), [s1] "s"(s1), [av0] "v"(av[0]), [av1] "v"(av[1]), [av2] "v"(av[2]), [av3] "v"(av[3]), [av4] "v"(av[4]), [av5] "v"(av[5]), [av6] "v"(av[6]), [av7] "v"(av[7]), [ps] "s"(ps), [dv] "v"(dv), [db] "s"(db), [rw] "v"(rw), [fa] "s"(fa), [fv] "s"(fv), [ga] "s"(ga), [mu] "s"(mu), [ad] "v"(ad), [th] "s"(th), [tb] "s"(tb)
    : "v96", "v97", "v98", "v99", "v100", "v101", "v102", "v103", "v104", "v105", "v106", "v107", "v108", "v109", "v110", "v111", "v112", "v113", "v114", "v115", "v116", "v117", "v118", "v119", "v120", "v121", "v122", "v123", "v124", "v125", "v126", "v127", "v76", "v77", "v78", "v79", "v80", "v81", "v82", "v83", "v84", "v85", "v86", "v87", "v88", "v89", "v90", "v91", "v92", "v93", "v94", "v95", "vcc", "scc", "m0", "memory", "s36", "s37", "s38", "s39");
}
__device__ __forceinline__ void lean_dm1(const u32x32 (&W)[4], unsigned ea, unsigned da, unsigned x, unsigned lb, const char* s0, const char* s1, const unsigned (&av)[8], unsigned ps, unsigned dv, const char* db, unsigned rw, unsigned fa, unsigned fv,
    unsigned ga, unsigned mu, unsigned ad, int th, int tb, unsigned& sv, unsigned& su, unsigned& got) {
  asm volatile("s_setprio 2\n\t"
    "v_mov_b32 v76, %[ga]\n\t"
    "ds_read_b32 v76, v76 offset:24\n\t"
    "ds_read_b128 v[96:99], %[ea] offset:0\n\t"
    "ds_read_b128 v[100:103], %[ea] offset:2048\n\t"
    "ds_read_b128 v[104:107], %[da] offset:0\n\t"
    "ds_read_b128 v[108:111], %[da] offset:2048\n\t"
    "ds_read_b128 v[112:115], %[ea] offset:4096\n\t"
    "ds_read_b128 v[116:119], %[ea] offset:6144\n\t"
    "s_waitcnt lgkmcnt(6)\n\t"
    "v_cmp_ne_u32 vcc, 0, v76\n\t"
    "s_mov_b32 %[got], -1\n\t"
    "s_cbranch_vccnz 12f\n\t"
    "s_waitcnt lgkmcnt(4)\n\t"
    "v_mfma_f32_16x16x32_bf16 v[80:83], v[128:131], v[96:99], 0\n\t"
    "ds_read_b128 v[120:123], %[da] offset:4096\n\t"
    "s_add_u32 m0, %[lb], 0\n\t"
    "v_mfma_f32_16x16x32_bf16 v[84:87], v[132:135], v[96:99], 0\n\t"
    "ds_read_b128 v[124:127], %[da] offset:6144\n\t"
    "global_load_lds_dwordx4 %[av0], %[s0] sc1\n\t"
    "v_mfma_f32_16x16x32_bf16 v[88:91], v[128:131], v[100:103], 0\n\t"
    "v_mfma_f32_16x16x32_bf16 v[92:95], v[132:135], v[100:103], 0\n\t"
    "s_waitcnt vmcnt(5)\n\t"
    "s_barrier\n\t"
    "s_waitcnt lgkmcnt(4)\n\t"
    "v_mfma_f32_16x16x32_bf16 v[80:83], v[136:139], v[104:107], v[80:83]\n\t"
    "ds_read_b128 v[96:99], %[ea] offset:8192\n\t"
    "s_add_u32 m0, %[lb], 4096\n\t"
    "v_mfma_f32_16x16x32_bf16 v[84:87], v[140:143], v[104:107], v[84:87]\n\t"
    "ds_read_b128 v[100:103], %[ea] offset:10240\n\t"
    "global_load_lds_dwordx4 %[av2], %[s0] sc1\n\t"
    "v_mfma_f32_16x16x32_bf16 v[88:91], v[136:139], v[108:111], v[88:91]\n\t"
    "v_mfma_f32_16x16x32_bf16 v[92:95], v[140:143], v[108:111], v[92:95]\n\t"
    "s_waitcnt lgkmcnt(4)\n\t"
    "v_mfma_f32_16x16x32_bf16 v[80:83], v[144:147], v[112:115], v[80:83]\n\t"
    "ds_read_b128 v[104:107], %[da] offset:8192\n\t"
    "s_add_u32 m0, %[lb], 8192\n\t"
    "v_mfma_f32_16x16x32_bf16 v[84:87], v[148:151], v[112:115], v[84:87]\n\t"
    "ds_read_b128 v[108:111], %[da] offset:10240\n\t"
    "global_load_lds_dwordx4 %[av4], %[s0] sc1\n\t"
    "v_mfma_f32_16x16x32_bf16 v[88:91], v[144:147], v[116:119], v[88:91]\n\t"
    "v_mfma_f32_16x16x32_bf16 v[92:95], v[148:151], v[116:119], v[92:95]\n\t"
    "s_waitcnt lgkmcnt(4)\n\t"
    "v_mfma_f32_16x16x32_bf16 v[80:83], v[152:155], v[120:123], v[80:83]\n\t"
    "ds_read_b128 v[112:115], %[ea] offset:12288\n\t"
    "s_add_u32 m0, %[lb], 12288\n\t"
    "v_mfma_f32_16x16x32_bf16 v[84:87], v[156:159], v[120:123], v[84:87]\n\t"
    "ds_read_b128 v[116:119], %[ea] offset:14336\n\t"
    "global_load_lds_dwordx4 %[av6], %[s0] sc1\n\t"
    "v_mfma_f32_16x16x32_bf16 v[88:91], v[152:155], v[124:127], v[88:91]\n\t"
    "v_mfma_f32_16x16x32_bf16 v[92:95], v[156:159], v[124:127], v[92:95]\n\t"
    "s_waitcnt lgkmcnt(4)\n\t"
    "v_mfma_f32_16x16x32_bf16 v[80:83], v[160:163], v[96:99], v[80:83]\n\t"
    "ds_read_b128 v[120:123], %[da] offset:12288\n\t"
    "s_add_u32 m0, %[lb], 16384\n\t"
    "v_mfma_f32_16x16x32_bf16 v[84:87], v[164:167], v[96:99], v[84:87]\n\t"
    "ds_read_b128 v[124:127], %[da] offset:14336\n\t"
    "global_load_lds_dwordx4 %[av0], %[s1] sc1\n\t"
    "v_mfma_f32_16x16x32_bf16 v[88:91], v[160:163], v[100:103], v[88:91]\n\t"
    "v_mfma_f32_16x16x32_bf16 v[92:95], v[164:167], v[100:103], v[92:95]\n\t"
    "s_waitcnt vmcnt(7)\n\t"
    "s_barrier\n\t"
    "s_waitcnt lgkmcnt(4)\n\t"
    "v_mfma_f32_16x16x32_bf16 v[80:83], v[168:171], v[104:107], v[80:83]\n\t"
    "ds_read_b128 v[96:99], %[ea] offset:16384\n\t"
    "s_add_u32 m0, %[lb], 20480\n\t"
    "v_mfma_f32_16x16x32_bf16 v[84:87], v[172:175], v[104:107], v[84:87]\n\t"
    "ds_read_b128 v[100:103], %[ea] offset:18432\n\t"
    "global_load_lds_dwordx4 %[av2], %[s1] sc1\n\t"
    "v_mfma_f32_16x16x32_bf16 v[88:91], v[168:171], v[108:111], v[88:91]\n\t"
    "v_mfma_f32_16x16x32_bf16 v[92:95], v[172:175], v[108:111], v[92:95]\n\t"
    "s_waitcnt lgkmcnt(4)\n\t"
    "v_mfma_f32_16x16x32_bf16 v[80:83], v[176:179], v[112:115], v[80:83]\n\t"
    "ds_read_b128 v[104:107], %[da] offset:16384\n\t"
    "s_add_u32 m0, %[lb], 24576\n\t"
    "v_mfma_f32_16x16x32_bf16 v[84:87], v[180:183], v[112:115], v[84:87]\n\t"
    "ds_read_b128 v[108:111], %[da] offset:18432\n\t"
    "global_load_lds_dwordx4 %[av4], %[s1] sc1\n\t"
    "v_mfma_f32_16x16x32_bf16 v[88:91], v[176:179], v[116:119], v[88:91]\n\t"
    "v_mfma_f32_16x16x32_bf16 v[92:95], v[180:183], v[116:119], v[92:95]\n\t"
    "s_waitcnt lgkmcnt(4)\n\t"
    "v_mfma_f32_16x16x32_bf16 v[80:83], v[184:187], v[120:123], v[80:83]\n\t"
    "ds_read_b128 v[112:115], %[ea] offset:20480\n\t"
    "s_add_u32 m0, %[lb], 28672\n\t"
    "v_mfma_f32_16x16x32_bf16 v[84:87], v[188:191], v[120:123], v[84:87]\n\t"
    "ds_read_b128 v[116:119], %[ea] offset:22528\n\t"
    "global_load_lds_dwordx4 %[av6], %[s1] sc1\n\t"
    "v_mfma_f32_16x16x32_bf16 v[88:91], v[184:187], v[124:127], v[88:91]\n\t"
    "v_mfma_f32_16x16x32_bf16 v[92:95], v[188:191], v[124:127], v[92:95]\n\t"
    "s_waitcnt lgkmcnt(4)\n\t"
    "v_mfma_f32_16x16x32_bf16 v[80:83], v[192:195], v[96:99], v[80:83]\n\t"
    "ds_read_b128 v[120:123], %[da] offset:20480\n\t"
    "v_mfma_f32_16x16x32_bf16 v[84:87], v[196:199], v[96:99], v[84:87]\n\t"
    "ds_read_b128 v[124:127], %[da] offset:22528\n\t"
    "v_mfma_f32_16x16x32_bf16 v[88:91], v[192:195], v[100:103], v[88:91]\n\t"
    "v_mfma_f32_16x16x32_bf16 v[92:95], v[196:199], v[100:103], v[92:95]\n\t"
    "s_waitcnt vmcnt(8)\n\t"
    "s_barrier\n\t"
    "s_waitcnt lgkmcnt(4)\n\t"
    "v_mfma_f32_16x16x32_bf16 v[80:83], v[200:203], v[104:107], v[80:83]\n\t"
    "ds_read_b128 v[96:99], %[ea] offset:24576\n\t"
    "v_mfma_f32_16x16x32_bf16 v[84:87], v[204:207], v[104:107], v[84:87]\n\t"
    "ds_read_b128 v[100:103], %[ea] offset:26624\n\t"
    "v_mfma_f32_16x16x32_bf16 v[88:91], v[200:203], v[108:111], v[88:91]\n\t"
    "v_mfma_f32_16x16x32_bf16 v[92:95], v[204:207], v[108:111], v[92:95]\n\t"
    "s_waitcnt lgkmcnt(4)\n\t"
    "v_mfma_f32_16x16x32_bf16 v[80:83], v[208:211], v[112:115], v[80:83]\n\t"
    "ds_read_b128 v[104:107], %[da] offset:24576\n\t"
    "v_mfma_f32_16x16x32_bf16 v[84:87], v[212:215], v[112:115], v[84:87]\n\t"
    "ds_read_b128 v[108:111], %[da] offset:26624\n\t"
    "v_mfma_f32_16x16x32_bf16 v[88:91], v[208:211], v[116:119], v[88:91]\n\t"
    "v_mfma_f32_16x16x32_bf16 v[92:95], v[212:215], v[116:119], v[92:95]\n\t"
    "s_waitcnt lgkmcnt(4)\n\t"
    "v_mfma_f32_16x16x32_bf16 v[80:83], v[216:219], v[120:123], v[80:83]\n\t"
    "ds_read_b128 v[112:115], %[ea] offset:28672\n\t"
    "v_mfma_f32_16x16x32_bf16 v[84:87], v[220:223], v[120:123], v[84:87]\n\t"
    "ds_read_b128 v[116:119], %[ea] offset:30720\n\t"
    "v_mfma_f32_16x16x32_bf16 v[88:91], v[216:219], v[124:127], v[88:91]\n\t"
    "v_mfma_f32_16x16x32_bf16 v[92:95], v[220:223], v[124:127], v[92:95]\n\t"
    "s_waitcnt lgkmcnt(4)\n\t"
    "v_mfma_f32_16x16x32_bf16 v[80:83], v[224:227], v[96:99], v[80:83]\n\t"
    "ds_read_b128 v[120:123], %[da] offset:28672\n\t"
    "v_mfma_f32_16x16x32_bf16 v[84:87], v[228:231], v[96:99], v[84:87]\n\t"
    "ds_read_b128 v[124:127], %[da] offset:30720\n\t"
    "v_mfma_f32_16x16x32_bf16 v[88:91], v[224:227], v[100:103], v[88:91]\n\t"
    "v_mfma_f32_16x16x32_bf16 v[92:95], v[228:231], v[100:103], v[92:95]\n\t"
    "s_waitcnt lgkmcnt(4)\n\t"
    "v_mfma_f32_16x16x32_bf16 v[80:83], v[232:235], v[104:107], v[80:83]\n\t"
    "v_mfma_f32_16x16x32_bf16 v[84:87], v[236:239], v[104:107], v[84:87]\n\t"
    "v_mfma_f32_16x16x32_bf16 v[88:91], v[232:235], v[108:111], v[88:91]\n\t"
    "v_mfma_f32_16x16x32_bf16 v[92:95], v[236:239], v[108:111], v[92:95]\n\t"
    "s_waitcnt lgkmcnt(2)\n\t"
    "v_mfma_f32_16x16x32_bf16 v[80:83], v[240:243], v[112:115], v[80:83]\n\t"
    "v_mfma_f32_16x16x32_bf16 v[84:87], v[244:247], v[112:115], v[84:87]\n\t"
    "v_mfma_f32_16x16x32_bf16 v[88:91], v[240:243], v[116:119], v[88:91]\n\t"
    "v_mfma_f32_16x16x32_bf16 v[92:95], v[244:247], v[116:119], v[92:95]\n\t"
    "s_waitcnt lgkmcnt(0)\n\t"
    "v_mfma_f32_16x16x32_bf16 v[80:83], v[248:251], v[120:123], v[80:83]\n\t"
    "v_mfma_f32_16x16x32_bf16 v[84:87], v[252:255], v[120:123], v[84:87]\n\t"
    "v_mfma_f32_16x16x32_bf16 v[88:91], v[248:251], v[124:127], v[88:91]\n\t"
    "v_mfma_f32_16x16x32_bf16 v[92:95], v[252:255], v[124:127], v[92:95]\n\t"
    "s_cmp_eq_u32 %[ps], 0\n\t"
    "s_cbranch_scc1 10f\n\t"
    "global_load_dword %[sv], %[dv], %[db] sc1\n\t"
    "global_load_dword %[su], %[dv], %[db] offset:64 sc1\n\t"
    "10:\n\t"
    "s_mov_b32 %[got], 0\n\t"
    "v_mov_b32 v76, %[fa]\n\t"
    "v_mov_b32 v77, %[fv]\n\t"
    "v_add_u32 v78, 1, v77\n\t"
    "v_mov_b32 v79, %[ga]\n\t"
    "ds_write_b128 %[rw], v[80:83] offset:0\n\t"
    "ds_write_b128 %[rw], v[84:87] offset:1024\n\t"
    "ds_write_b128 %[rw], v[88:91] offset:2048\n\t"
    "ds_write_b128 %[rw], v[92:95] offset:3072\n\t"
    "ds_write_b32 v76, v77\n\t"
    "s_cmp_eq_u32 %[ps], 0\n\t"
    "s_cbranch_scc1 9f\n\t"
    "s_waitcnt vmcnt(0)\n\t"
    "s_branch 9f\n\t"
    "12:\n\t"
    "s_waitcnt lgkmcnt(0)\n\t"
    "9:"
    : [sv] "+&v"(sv), [su] "+&v"(su), [got] "=&s"(got)
    : [ea] "v"(ea), [da] "v"(da), [x] "s"(x), "{v[128:159]}"(W[0]), "{v[160:191]}"(W[1]), "{v[192:223]}"(W[2]), "{v[224:255]}"(W[3]), [lb] "s"(lb), [s0] "s"(s0), [s1] "s"(s1), [av0] "v"(av[0]), [av1] "v"(av[1]), [av2] "v"(av[2]), [av3] "v"(av[3]), [av4] "v"(av[4]), [av5] "v"(av[5]), [av6] "v"(av[6]), [av7] "v"(av[7]), [ps] "s"(ps), [dv] "v"(dv), [db] "s"(db), [rw] "v"(rw), [fa] "s"(fa), [fv] "s"(fv), [ga] "s"(ga), [mu] "s"(mu), [ad] "v"(ad), [th] "s"(th), [tb] "s"(tb)
    : "v96", "v97", "v98", "v99", "v100", "v101", "v102", "v103", "v104", "v105", "v106", "v107", "v108", "v109", "v110", "v111", "v112", "v113", "v114", "v115", "v116", "v117", "v118", "v119", "v120", "v121", "v122", "v123", "v124", "v125", "v126", "v127", "v76", "v77", "v78", "v79", "v80", "v81", "v82", "v83", "v84", "v85", "v86", "v87", "v88", "v89", "v90", "v91", "v92", "v93", "v94", "v95", "vcc", "scc", "m0", "memory");
}
__device__ __forceinline__ void lean_st0(const u32x32 (&W)[4], unsigned ea, unsigned da, unsigned x, u32x16& pd, unsigned pv, unsigned cn, const char* pg, unsigned bl, unsigned& tk, unsigned& st) {
  asm volatile("v_mov_b32 v76, %[cn]\n\t"
    "ds_read_b32 v76, v76 offset:28\n\t"
    "ds_read_b128 v[96:99], %[ea] offset:0\n\t"
    "ds_read_b128 v[100:103], %[ea] offset:2048\n\t"
    "ds_read_b128 v[104:107], %[da] offset:0\n\t"
    "ds_read_b128 v[108:111], %[da] offset:2048\n\t"
    "ds_read_b128 v[112:115], %[ea] offset:4096\n\t"
    "ds_read_b128 v[116:119], %[ea] offset:6144\n\t"
    "s_waitcnt lgkmcnt(6)\n\t"
    "v_cmp_ne_u32 vcc, 0, v76\n\t"
    "s_mov_b32 %[st], -1\n\t"
    "s_cbranch_vccnz 12f\n\t"
    "s_waitcnt lgkmcnt(4)\n\t"
    "v_mfma_f32_16x16x32_bf16 v[80:83], v[128:131], v[96:99], 0\n\t"
    "ds_read_b128 v[120:123], %[da] offset:4096\n\t"
    "v_mfma_f32_16x16x32_bf16 v[84:87], v[132:135], v[96:99], 0\n\t"
    "ds_read_b128 v[124:127], %[da] offset:6144\n\t"
    "v_mfma_f32_16x16x32_bf16 v[88:91], v[128:131], v[100:103], 0\n\t"
    "v_mfma_f32_16x16x32_bf16 v[92:95], v[132:135], v[100:103], 0\n\t"
    "s_barrier\n\t"
    "s_bitcmp1_b32 %[x], 2\n\t"
    "s_cbranch_scc1 7f\n\t"
    "8:\n\t"
    "s_bitcmp1_b32 %[x], 3\n\t"
    "s_cbranch_scc1 13f\n\t"
    "14:\n\t"
    "s_waitcnt lgkmcnt(4)\n\t"
    "v_mfma_f32_16x16x32_bf16 v[80:83], v[136:139], v[104:107], v[80:83]\n\t"
    "ds_read_b128 v[96:99], %[ea] offset:8192\n\t"
    "v_mfma_f32_16x16x32_bf16 v[84:87], v[140:143], v[104:107], v[84:87]\n\t"
    "ds_read_b128 v[100:103], %[ea] offset:10240\n\t"
    "v_mfma_f32_16x16x32_bf16 v[88:91], v[136:139], v[108:111], v[88:91]\n\t"
    "v_mfma_f32_16x16x32_bf16 v[92:95], v[140:143], v[108:111], v[92:95]\n\t"
    "s_waitcnt lgkmcnt(4)\n\t"
    "v_mfma_f32_16x16x32_bf16 v[80:83], v[144:147], v[112:115], v[80:83]\n\t"
    "ds_read_b128 v[104:107], %[da] offset:8192\n\t"
    "v_mfma_f32_16x16x32_bf16 v[84:87], v[148:151], v[112:115], v[84:87]\n\t"
    "ds_read_b128 v[108:111], %[da] offset:10240\n\t"
    "v_mfma_f32_16x16x32_bf16 v[88:91], v[144:147], v[116:119], v[88:91]\n\t"
    "v_mfma_f32_16x16x32_bf16 v[92:95], v[148:151], v[116:119], v[92:95]\n\t"
    "s_waitcnt lgkmcnt(4)\n\t"
    "v_mfma_f32_16x16x32_bf16 v[80:83], v[152:155], v[120:123], v[80:83]\n\t"
    "ds_read_b128 v[112:115], %[ea] offset:12288\n\t"
    "v_mfma_f32_16x16x32_bf16 v[84:87], v[156:159], v[120:123], v[84:87]\n\t"
    "ds_read_b128 v[116:119], %[ea] offset:14336\n\t"
    "v_mfma_f32_16x16x32_bf16 v[88:91], v[152:155], v[124:127], v[88:91]\n\t"
    "v_mfma_f32_16x16x32_bf16 v[92:95], v[156:159], v[124:127], v[92:95]\n\t"
    "s_waitcnt lgkmcnt(4)\n\t"
    "v_mfma_f32_16x16x32_bf16 v[80:83], v[160:163], v[96:99], v[80:83]\n\t"
    "ds_read_b128 v[120:123], %[da] offset:12288\n\t"
    "v_mfma_f32_16x16x32_bf16 v[84:87], v[164:167], v[96:99], v[84:87]\n\t"
    "ds_read_b128 v[124:127], %[da] offset:14336\n\t"
    "v_mfma_f32_16x16x32_bf16 v[88:91], v[160:163], v[100:103], v[88:91]\n\t"
    "v_mfma_f32_16x16x32_bf16 v[92:95], v[164:167], v[100:103], v[92:95]\n\t"
    "s_barrier\n\t"
    "s_waitcnt lgkmcnt(4)\n\t"
    "v_mfma_f32_16x16x32_bf16 v[80:83], v[168:171], v[104:107], v[80:83]\n\t"
    "ds_read_b128 v[96:99], %[ea] offset:16384\n\t"
    "v_mfma_f32_16x16x32_bf16 v[84:87], v[172:175], v[104:107], v[84:87]\n\t"
    "ds_read_b128 v[100:103], %[ea] offset:18432\n\t"
    "v_mfma_f32_16x16x32_bf16 v[88:91], v[168:171], v[108:111], v[88:91]\n\t"
    "v_mfma_f32_16x16x32_bf16 v[92:95], v[172:175], v[108:111], v[92:95]\n\t"
    "s_waitcnt lgkmcnt(4)\n\t"
    "v_mfma_f32_16x16x32_bf16 v[80:83], v[176:179], v[112:115], v[80:83]\n\t"
    "ds_read_b128 v[104:107], %[da] offset:16384\n\t"
    "v_mfma_f32_16x16x32_bf16 v[84:87], v[180:183], v[112:115], v[84:87]\n\t"
    "ds_read_b128 v[108:111], %[da] offset:18432\n\t"
    "v_mfma_f32_16x16x32_bf16 v[88:91], v[176:179], v[116:119], v[88:91]\n\t"
    "v_mfma_f32_16x16x32_bf16 v[92:95], v[180:183], v[116:119], v[92:95]\n\t"
    "s_waitcnt lgkmcnt(4)\n\t"
    "v_mfma_f32_16x16x32_bf16 v[80:83], v[184:187], v[120:123], v[80:83]\n\t"
    "ds_read_b128 v[112:115], %[ea] offset:20480\n\t"
    "v_mfma_f32_16x16x32_bf16 v[84:87], v[188:191], v[120:123], v[84:87]\n\t"
    "ds_read_b128 v[116:119], %[ea] offset:22528\n\t"
    "v_mfma_f32_16x16x32_bf16 v[88:91], v[184:187], v[124:127], v[88:91]\n\t"
    "v_mfma_f32_16x16x32_bf16 v[92:95], v[188:191], v[124:127], v[92:95]\n\t"
    "s_waitcnt lgkmcnt(4)\n\t"
    "v_mfma_f32_16x16x32_bf16 v[80:83], v[192:195], v[96:99], v[80:83]\n\t"
    "ds_read_b128 v[120:123], %[da] offset:20480\n\t"
    "v_mfma_f32_16x16x32_bf16 v[84:87], v[196:199], v[96:99], v[84:87]\n\t"
    "ds_read_b128 v[124:127], %[da] offset:22528\n\t"
    "v_mfma_f32_16x16x32_bf16 v[88:91], v[192:195], v[100:103], v[88:91]\n\t"
    "v_mfma_f32_16x16x32_bf16 v[92:95], v[196:199], v[100:103], v[92:95]\n\t"
    "s_barrier\n\t"
    "s_and_b32 %[st], %[x], 3\n\t"
    "s_cbranch_scc1 3f\n\t"
    "4:\n\t"
    "s_waitcnt lgkmcnt(4)\n\t"
    "v_mfma_f32_16x16x32_bf16 v[80:83], v[200:203], v[104:107], v[80:83]\n\t"
    "ds_read_b128 v[96:99], %[ea] offset:24576\n\t"
    "v_mfma_f32_16x16x32_bf16 v[84:87], v[204:207], v[104:107], v[84:87]\n\t"
    "ds_read_b128 v[100:103], %[ea] offset:26624\n\t"
    "v_mfma_f32_16x16x32_bf16 v[88:91], v[200:203], v[108:111], v[88:91]\n\t"
    "v_mfma_f32_16x16x32_bf16 v[92:95], v[204:207], v[108:111], v[92:95]\n\t"
    "s_waitcnt lgkmcnt(4)\n\t"
    "v_mfma_f32_16x16x32_bf16 v[80:83], v[208:211], v[112:115], v[80:83]\n\t"
    "ds_read_b128 v[104:107], %[da] offset:24576\n\t"
    "v_mfma_f32_16x16x32_bf16 v[84:87], v[212:215], v[112:115], v[84:87]\n\t"
    "ds_read_b128 v[108:111], %[da] offset:26624\n\t"
    "v_mfma_f32_16x16x32_bf16 v[88:91], v[208:211], v[116:119], v[88:91]\n\t"
    "v_mfma_f32_16x16x32_bf16 v[92:95], v[212:215], v[116:119], v[92:95]\n\t"
    "s_waitcnt lgkmcnt(4)\n\t"
    "v_mfma_f32_16x16x32_bf16 v[80:83], v[216:219], v[120:123], v[80:83]\n\t"
    "ds_read_b128 v[112:115], %[ea] offset:28672\n\t"
    "v_mfma_f32_16x16x32_bf16 v[84:87], v[220:223], v[120:123], v[84:87]\n\t"
    "ds_read_b128 v[116:119], %[ea] offset:30720\n\t"
    "v_mfma_f32_16x16x32_bf16 v[88:91], v[216:219], v[124:127], v[88:91]\n\t"
    "v_mfma_f32_16x16x32_bf16 v[92:95], v[220:223], v[124:127], v[92:95]\n\t"
    "s_waitcnt lgkmcnt(4)\n\t"
    "v_mfma_f32_16x16x32_bf16 v[80:83], v[224:227], v[96:99], v[80:83]\n\t"
    "ds_read_b128 v[120:123], %[da] offset:28672\n\t"
    "v_mfma_f32_16x16x32_bf16 v[84:87], v[228:231], v[96:99], v[84:87]\n\t"
    "ds_read_b128 v[124:127], %[da] offset:30720\n\t"
    "v_mfma_f32_16x16x32_bf16 v[88:91], v[224:227], v[100:103], v[88:91]\n\t"
    "v_mfma_f32_16x16x32_bf16 v[92:95], v[228:231], v[100:103], v[92:95]\n\t"
    "s_waitcnt lgkmcnt(4)\n\t"
    "v_mfma_f32_16x16x32_bf16 v[80:83], v[232:235], v[104:107], v[80:83]\n\t"
    "v_mfma_f32_16x16x32_bf16 v[84:87], v[236:239], v[104:107], v[84:87]\n\t"
    "v_mfma_f32_16x16x32_bf16 v[88:91], v[232:235], v[108:111], v[88:91]\n\t"
    "v_mfma_f32_16x16x32_bf16 v[92:95], v[236:239], v[108:111], v[92:95]\n\t"
    "s_waitcnt lgkmcnt(2)\n\t"
    "v_mfma_f32_16x16x32_bf16 v[80:83], v[240:243], v[112:115], v[80:83]\n\t"
    "v_mfma_f32_16x16x32_bf16 v[84:87], v[244:247], v[112:115], v[84:87]\n\t"
    "v_mfma_f32_16x16x32_bf16 v[88:91], v[240:243], v[116:119], v[88:91]\n\t"
    "v_mfma_f32_16x16x32_bf16 v[92:95], v[244:247], v[116:119], v[92:95]\n\t"
    "s_waitcnt lgkmcnt(0)\n\t"
    "v_mfma_f32_16x16x32_bf16 v[80:83], v[248:251], v[120:123], v[80:83]\n\t"
    "v_mfma_f32_16x16x32_bf16 v[84:87], v[252:255], v[120:123], v[84:87]\n\t"
    "v_mfma_f32_16x16x32_bf16 v[88:91], v[248:251], v[124:127], v[88:91]\n\t"
    "v_mfma_f32_16x16x32_bf16 v[92:95], v[252:255], v[124:127], v[92:95]\n\t"
    "s_mov_b32 %[st], 0\n\t"
    "s_nop 5\n\t"
    "s_branch 9f\n\t"
    "13:\n\t"
    "s_memrealtime s[36:37]\n\t"
    "v_mov_b32 v79, 0\n\t"
    "s_waitcnt lgkmcnt(0)\n\t"
    "s_mov_b32 %[tk], s36\n\t"
    "s_add_u32 s36, s36, %[bl]\n\t"
    "s_and_b32 s36, s36, 0xffffff\n\t"
    "s_and_b32 s37, %[bl], 0xff000000\n\t"
    "s_or_b32 s36, s36, s37\n\t"
    "v_mov_b32 v78, s36\n\t"
    "s_mov_b64 exec, 1\n\t"
    "global_store_dword v79, v78, %[pg] offset:64 sc1\n\t"
    "s_mov_b64 exec, -1\n\t"
    "s_branch 14b\n\t"
    "3:\n\t"
    "7:\n\t"
    "s_and_b32 %[st], %[pv], 1\n\t"
    "s_mul_i32 %[st], %[st], 36\n\t"
    "s_add_u32 %[st], %[st], %[cn]\n\t"
    "v_mov_b32 v76, %[st]\n\t"
    "v_mov_b32 v77, 1\n\t"
    "v_mov_b32 v78, %[pv]\n\t"
    "v_mov_b32 v79, 0\n\t"
    "s_waitcnt vmcnt(0)\n\t"
    "s_mov_b64 exec, 1\n\t"
    "ds_add_rtn_u32 v77, v76, v77\n\t"
    "s_waitcnt lgkmcnt(0)\n\t"
    "v_readfirstlane_b32 %[st], v77\n\t"
    "s_and_b32 %[st], %[st], 3\n\t"
    "s_cmp_lg_u32 %[st], 3\n\t"
    "s_cbranch_scc1 6f\n\t"
    "global_store_dword v79, v78, %[pg] sc1\n\t"
    "6:\n\t"
    "s_mov_b64 exec, -1\n\t"
    "s_bitcmp1_b32 %[x], 2\n\t"
    "s_cbranch_scc1 8b\n\t"
    "s_branch 4b\n\t"
    "12:\n\t"
    "s_waitcnt lgkmcnt(0)\n\t"
    "9:"
    : "=&{v[80:95]}"(pd), [st] "=&s"(st), [tk] "+&s"(tk)
    : [ea] "v"(ea), [da] "v"(da), [x] "s"(x), "{v[128:159]}"(W[0]), "{v[160:191]}"(W[1]), "{v[192:223]}"(W[2]), "{v[224:255]}"(W[3]), [pv] "s"(pv), [cn] "s"(cn), [pg] "s"(pg), [bl] "s"(bl)
    : "v96", "v97", "v98", "v99", "v100", "v101", "v102", "v103", "v104", "v105", "v106", "v107", "v108", "v109", "v110", "v111", "v112", "v113", "v114", "v115", "v116", "v117", "v118", "v119", "v120", "v121", "v122", "v123", "v124", "v125", "v126", "v127", "v76", "v77", "v78", "v79", "vcc", "scc", "m0", "memory", "s36", "s37", "s38", "s39");
}
__device__ __forceinline__ void lean_ep0(const u32x32 (&W)[4], unsigned ea, unsigned da, unsigned x, unsigned pv, unsigned cn, const char* pg, unsigned bl, unsigned& tk, unsigned& st,
    unsigned rw, unsigned fa, unsigned fv, const char* cb, unsigned co0, unsigned co1, unsigned& bad, unsigned& pk) {
  asm volatile("v_mov_b32 v76, %[cn]\n\t"
    "ds_read_b32 v76, v76 offset:28\n\t"
    "ds_read_b128 v[96:99], %[ea] offset:0\n\t"
    "ds_read_b128 v[100:103], %[ea] offset:2048\n\t"
    "ds_read_b128 v[104:107], %[da] offset:0\n\t"
    "ds_read_b128 v[108:111], %[da] offset:2048\n\t"
    "ds_read_b128 v[112:115], %[ea] offset:4096\n\t"
    "ds_read_b128 v[116:119], %[ea] offset:6144\n\t"
    "s_waitcnt lgkmcnt(6)\n\t"
    "v_cmp_ne_u32 vcc, 0, v76\n\t"
    "s_mov_b32 %[st], -1\n\t"
    "s_cbranch_vccnz 12f\n\t"
    "s_waitcnt lgkmcnt(4)\n\t"
    "v_mfma_f32_16x16x32_bf16 v[80:83], v[128:131], v[96:99], 0\n\t"
    "ds_read_b128 v[120:123], %[da] offset:4096\n\t"
    "v_mfma_f32_16x16x32_bf16 v[84:87], v[132:135], v[96:99], 0\n\t"
    "ds_read_b128 v[124:127], %[da] offset:6144\n\t"
    "v_mfma_f32_16x16x32_bf16 v[88:91], v[128:131], v[100:103], 0\n\t"
    "v_mfma_f32_16x16x32_bf16 v[92:95], v[132:135], v[100:103], 0\n\t"
    "s_barrier\n\t"
    "s_bitcmp1_b32 %[x], 2\n\t"
    "s_cbranch_scc1 7f\n\t"
    "8:\n\t"
    "s_bitcmp1_b32 %[x], 3\n\t"
    "s_cbranch_scc1 13f\n\t"
    "14:\n\t"
    "s_waitcnt lgkmcnt(4)\n\t"
    "v_mfma_f32_16x16x32_bf16 v[80:83], v[136:139], v[104:107], v[80:83]\n\t"
    "ds_read_b128 v[96:99], %[ea] offset:8192\n\t"
    "v_mfma_f32_16x16x32_bf16 v[84:87], v[140:143], v[104:107], v[84:87]\n\t"
    "ds_read_b128 v[100:103], %[ea] offset:10240\n\t"
    "v_mfma_f32_16x16x32_bf16 v[88:91], v[136:139], v[108:111], v[88:91]\n\t"
    "v_mfma_f32_16x16x32_bf16 v[92:95], v[140:143], v[108:111], v[92:95]\n\t"
    "s_waitcnt lgkmcnt(4)\n\t"
    "v_mfma_f32_16x16x32_bf16 v[80:83], v[144:147], v[112:115], v[80:83]\n\t"
    "ds_read_b128 v[104:107], %[da] offset:8192\n\t"
    "v_mfma_f32_16x16x32_bf16 v[84:87], v[148:151], v[112:115], v[84:87]\n\t"
    "ds_read_b128 v[108:111], %[da] offset:10240\n\t"
    "v_mfma_f32_16x16x32_bf16 v[88:91], v[144:147], v[116:119], v[88:91]\n\t"
    "v_mfma_f32_16x16x32_bf16 v[92:95], v[148:151], v[116:119], v[92:95]\n\t"
    "s_waitcnt lgkmcnt(4)\n\t"
    "v_mfma_f32_16x16x32_bf16 v[80:83], v[152:155], v[120:123], v[80:83]\n\t"
    "ds_read_b128 v[112:115], %[ea] offset:12288\n\t"
    "v_mfma_f32_16x16x32_bf16 v[84:87], v[156:159], v[120:123], v[84:87]\n\t"
    "ds_read_b128 v[116:119], %[ea] offset:14336\n\t"
    "v_mfma_f32_16x16x32_bf16 v[88:91], v[152:155], v[124:127], v[88:91]\n\t"
    "v_mfma_f32_16x16x32_bf16 v[92:95], v[156:159], v[124:127], v[92:95]\n\t"
    "s_waitcnt lgkmcnt(4)\n\t"
    "v_mfma_f32_16x16x32_bf16 v[80:83], v[160:163], v[96:99], v[80:83]\n\t"
    "ds_read_b128 v[120:123], %[da] offset:12288\n\t"
    "v_mfma_f32_16x16x32_bf16 v[84:87], v[164:167], v[96:99], v[84:87]\n\t"
    "ds_read_b128 v[124:127], %[da] offset:14336\n\t"
    "v_mfma_f32_16x16x32_bf16 v[88:91], v[160:163], v[100:103], v[88:91]\n\t"
    "v_mfma_f32_16x16x32_bf16 v[92:95], v[164:167], v[100:103], v[92:95]\n\t"
    "s_barrier\n\t"
    "s_waitcnt lgkmcnt(4)\n\t"
    "v_mfma_f32_16x16x32_bf16 v[80:83], v[168:171], v[104:107], v[80:83]\n\t"
    "ds_read_b128 v[96:99], %[ea] offset:16384\n\t"
    "v_mfma_f32_16x16x32_bf16 v[84:87], v[172:175], v[104:107], v[84:87]\n\t"
    "ds_read_b128 v[100:103], %[ea] offset:18432\n\t"
    "v_mfma_f32_16x16x32_bf16 v[88:91], v[168:171], v[108:111], v[88:91]\n\t"
    "v_mfma_f32_16x16x32_bf16 v[92:95], v[172:175], v[108:111], v[92:95]\n\t"
    "s_waitcnt lgkmcnt(4)\n\t"
    "v_mfma_f32_16x16x32_bf16 v[80:83], v[176:179], v[112:115], v[80:83]\n\t"
    "ds_read_b128 v[104:107], %[da] offset:16384\n\t"
    "v_mfma_f32_16x16x32_bf16 v[84:87], v[180:183], v[112:115], v[84:87]\n\t"
    "ds_read_b128 v[108:111], %[da] offset:18432\n\t"
    "v_mfma_f32_16x16x32_bf16 v[88:91], v[176:179], v[116:119], v[88:91]\n\t"
    "v_mfma_f32_16x16x32_bf16 v[92:95], v[180:183], v[116:119], v[92:95]\n\t"
    "s_waitcnt lgkmcnt(4)\n\t"
    "v_mfma_f32_16x16x32_bf16 v[80:83], v[184:187], v[120:123], v[80:83]\n\t"
    "ds_read_b128 v[112:115], %[ea] offset:20480\n\t"
    "v_mfma_f32_16x16x32_bf16 v[84:87], v[188:191], v[120:123], v[84:87]\n\t"
    "ds_read_b128 v[116:119], %[ea] offset:22528\n\t"
    "v_mfma_f32_16x16x32_bf16 v[88:91], v[184:187], v[124:127], v[88:91]\n\t"
    "v_mfma_f32_16x16x32_bf16 v[92:95], v[188:191], v[124:127], v[92:95]\n\t"
    "s_waitcnt lgkmcnt(4)\n\t"
    "v_mfma_f32_16x16x32_bf16 v[80:83], v[192:195], v[96:99], v[80:83]\n\t"
    "ds_read_b128 v[120:123], %[da] offset:20480\n\t"
    "v_mfma_f32_16x16x32_bf16 v[84:87], v[196:199], v[96:99], v[84:87]\n\t"
    "ds_read_b128 v[124:127], %[da] offset:22528\n\t"
    "v_mfma_f32_16x16x32_bf16 v[88:91], v[192:195], v[100:103], v[88:91]\n\t"
    "v_mfma_f32_16x16x32_bf16 v[92:95], v[196:199], v[100:103], v[92:95]\n\t"
    "s_barrier\n\t"
    "s_and_b32 %[st], %[x], 3\n\t"
    "s_cbranch_scc1 3f\n\t"
    "4:\n\t"
    "s_waitcnt lgkmcnt(4)\n\t"
    "v_mfma_f32_16x16x32_bf16 v[80:83], v[200:203], v[104:107], v[80:83]\n\t"
    "ds_read_b128 v[96:99], %[ea] offset:24576\n\t"
    "v_mfma_f32_16x16x32_bf16 v[84:87], v[204:207], v[104:107], v[84:87]\n\t"
    "ds_read_b128 v[100:103], %[ea] offset:26624\n\t"
    "v_mfma_f32_16x16x32_bf16 v[88:91], v[200:203], v[108:111], v[88:91]\n\t"
    "v_mfma_f32_16x16x32_bf16 v[92:95], v[204:207], v[108:111], v[92:95]\n\t"
    "s_waitcnt lgkmcnt(4)\n\t"
    "v_mfma_f32_16x16x32_bf16 v[80:83], v[208:211], v[112:115], v[80:83]\n\t"
    "ds_read_b128 v[104:107], %[da] offset:24576\n\t"
    "v_mfma_f32_16x16x32_bf16 v[84:87], v[212:215], v[112:115], v[84:87]\n\t"
    "ds_read_b128 v[108:111], %[da] offset:26624\n\t"
    "v_mfma_f32_16x16x32_bf16 v[88:91], v[208:211], v[116:119], v[88:91]\n\t"
    "v_mfma_f32_16x16x32_bf16 v[92:95], v[212:215], v[116:119], v[92:95]\n\t"
    "s_waitcnt lgkmcnt(4)\n\t"
    "v_mfma_f32_16x16x32_bf16 v[80:83], v[216:219], v[120:123], v[80:83]\n\t"
    "ds_read_b128 v[112:115], %[ea] offset:28672\n\t"
    "v_mfma_f32_16x16x32_bf16 v[84:87], v[220:223], v[120:123], v[84:87]\n\t"
    "ds_read_b128 v[116:119], %[ea] offset:30720\n\t"
    "v_mfma_f32_16x16x32_bf16 v[88:91], v[216:219], v[124:127], v[88:91]\n\t"
    "v_mfma_f32_16x16x32_bf16 v[92:95], v[220:223], v[124:127], v[92:95]\n\t"
    "s_waitcnt lgkmcnt(4)\n\t"
    "v_mfma_f32_16x16x32_bf16 v[80:83], v[224:227], v[96:99], v[80:83]\n\t"
    "ds_read_b128 v[120:123], %[da] offset:28672\n\t"
    "v_mfma_f32_16x16x32_bf16 v[84:87], v[228:231], v[96:99], v[84:87]\n\t"
    "ds_read_b128 v[124:127], %[da] offset:30720\n\t"
    "v_mfma_f32_16x16x32_bf16 v[88:91], v[224:227], v[100:103], v[88:91]\n\t"
    "v_mfma_f32_16x16x32_bf16 v[92:95], v[228:231], v[100:103], v[92:95]\n\t"
    "s_waitcnt lgkmcnt(4)\n\t"
    "v_mfma_f32_16x16x32_bf16 v[80:83], v[232:235], v[104:107], v[80:83]\n\t"
    "v_mfma_f32_16x16x32_bf16 v[84:87], v[236:239], v[104:107], v[84:87]\n\t"
    "v_mfma_f32_16x16x32_bf16 v[88:91], v[232:235], v[108:111], v[88:91]\n\t"
    "v_mfma_f32_16x16x32_bf16 v[92:95], v[236:239], v[108:111], v[92:95]\n\t"
    "s_waitcnt lgkmcnt(2)\n\t"
    "v_mfma_f32_16x16x32_bf16 v[80:83], v[240:243], v[112:115], v[80:83]\n\t"
    "ds_read_b32 v39, %[fa]\n\t"
    "ds_read_b128 v[48:51], %[rw] offset:0\n\t"
    "v_mfma_f32_16x16x32_bf16 v[84:87], v[244:247], v[112:115], v[84:87]\n\t"
    "ds_read_b128 v[52:55], %[rw] offset:1024\n\t"
    "v_mfma_f32_16x16x32_bf16 v[88:91], v[240:243], v[116:119], v[88:91]\n\t"
    "ds_read_b128 v[56:59], %[rw] offset:2048\n\t"
    "v_mfma_f32_16x16x32_bf16 v[92:95], v[244:247], v[116:119], v[92:95]\n\t"
    "ds_read_b128 v[60:63], %[rw] offset:3072\n\t"
    "s_waitcnt lgkmcnt(5)\n\t"
    "v_mfma_f32_16x16x32_bf16 v[80:83], v[248:251], v[120:123], v[80:83]\n\t"
    "v_mfma_f32_16x16x32_bf16 v[84:87], v[252:255], v[120:123], v[84:87]\n\t"
    "v_mfma_f32_16x16x32_bf16 v[88:91], v[248:251], v[124:127], v[88:91]\n\t"
    "v_mfma_f32_16x16x32_bf16 v[92:95], v[252:255], v[124:127], v[92:95]\n\t"
    "s_mov_b32 %[st], 0\n\t"
    "21:\n\t"
    "s_waitcnt lgkmcnt(0)\n\t"
    "v_cmp_ne_u32 vcc, %[fv], v39\n\t"
    "s_cbranch_vccnz 20f\n\t"
    "v_mov_b32 v39, %[cn]\n\t"
    "s_nop 0\n\t"
    "v_pk_add_f32 v[48:49], v[48:49], v[80:81]\n\t"
    "v_pk_add_f32 v[50:51], v[50:51], v[82:83]\n\t"
    "v_pk_add_f32 v[52:53], v[52:53], v[84:85]\n\t"
    "v_pk_add_f32 v[54:55], v[54:55], v[86:87]\n\t"
    "v_pk_add_f32 v[56:57], v[56:57], v[88:89]\n\t"
    "v_pk_add_f32 v[58:59], v[58:59], v[90:91]\n\t"
    "v_pk_add_f32 v[60:61], v[60:61], v[92:93]\n\t"
    "v_pk_add_f32 v[62:63], v[62:63], v[94:95]\n\t"
    "v_cvt_pk_bf16_f32 v40, v48, v49\n\t"
    "v_cvt_pk_bf16_f32 v41, v50, v51\n\t"
    "v_cvt_pk_bf16_f32 v42, v52, v53\n\t"
    "v_cvt_pk_bf16_f32 v43, v54, v55\n\t"
    "global_store_dwordx4 %[co0], v[40:43], %[cb] sc1\n\t"
    "ds_read_b32 v38, v39 offset:4\n\t"
    "v_cvt_pk_bf16_f32 v44, v56, v57\n\t"
    "v_cvt_pk_bf16_f32 v45, v58, v59\n\t"
    "v_cvt_pk_bf16_f32 v46, v60, v61\n\t"
    "v_cvt_pk_bf16_f32 v47, v62, v63\n\t"
    "global_store_dwordx4 %[co1], v[44:47], %[cb] sc1\n\t"
    "s_memrealtime s[36:37]\n\t"
    "v_cmp_u_f32 vcc, v48, v56\n\t"
    "s_waitcnt lgkmcnt(0)\n\t"
    "s_or_b32 %[bad], vcc_lo, vcc_hi\n\t"
    "v_readfirstlane_b32 %[pk], v38\n\t"
    "s_sub_u32 %[tk], s36, %[tk]\n\t"
    "s_branch 9f\n\t"
    "20:\n\t"
    "ds_read_b32 v39, %[fa]\n\t"
    "ds_read_b128 v[48:51], %[rw] offset:0\n\t"
    "ds_read_b128 v[52:55], %[rw] offset:1024\n\t"
    "ds_read_b128 v[56:59], %[rw] offset:2048\n\t"
    "ds_read_b128 v[60:63], %[rw] offset:3072\n\t"
    "s_branch 21b\n\t"
    "13:\n\t"
    "s_memrealtime s[36:37]\n\t"
    "v_mov_b32 v79, 0\n\t"
    "s_waitcnt lgkmcnt(0)\n\t"
    "s_mov_b32 %[tk], s36\n\t"
    "s_add_u32 s36, s36, %[bl]\n\t"
    "s_and_b32 s36, s36, 0xffffff\n\t"
    "s_and_b32 s37, %[bl], 0xff000000\n\t"
    "s_or_b32 s36, s36, s37\n\t"
    "v_mov_b32 v78, s36\n\t"
    "s_mov_b64 exec, 1\n\t"
    "global_store_dword v79, v78, %[pg] offset:64 sc1\n\t"
    "s_mov_b64 exec, -1\n\t"
    "s_branch 14b\n\t"
    "3:\n\t"
    "7:\n\t"
    "s_and_b32 %[st], %[pv], 1\n\t"
    "s_mul_i32 %[st], %[st], 36\n\t"
    "s_add_u32 %[st], %[st], %[cn]\n\t"
    "v_mov_b32 v76, %[st]\n\t"
    "v_mov_b32 v77, 1\n\t"
    "v_mov_b32 v78, %[pv]\n\t"
    "v_mov_b32 v79, 0\n\t"
    "s_waitcnt vmcnt(0)\n\t"
    "s_mov_b64 exec, 1\n\t"
    "ds_add_rtn_u32 v77, v76, v77\n\t"
    "s_waitcnt lgkmcnt(0)\n\t"
    "v_readfirstlane_b32 %[st], v77\n\t"
    "s_and_b32 %[st], %[st], 3\n\t"
    "s_cmp_lg_u32 %[st], 3\n\t"
    "s_cbranch_scc1 6f\n\t"
    "global_store_dword v79, v78, %[pg] sc1\n\t"
    "6:\n\t"
    "s_mov_b64 exec, -1\n\t"
    "s_bitcmp1_b32 %[x], 2\n\t"
    "s_cbranch_scc1 8b\n\t"
    "s_branch 4b\n\t"
    "12:\n\t"
    "s_waitcnt lgkmcnt(0)\n\t"
    "9:"
    : [st] "=&s"(st), [tk] "+&s"(tk), [bad] "=&s"(bad), [pk] "=&s"(pk)
    : [ea] "v"(ea), [da] "v"(da), [x] "s"(x), "{v[128:159]}"(W[0]), "{v[160:191]}"(W[1]), "{v[192:223]}"(W[2]), "{v[224:255]}"(W[3]), [pv] "s"(pv), [cn] "s"(cn), [pg] "s"(pg), [bl] "s"(bl), [rw] "v"(rw), [fa] "v"(fa), [fv] "s"(fv), [cb] "s"(cb), [co0] "v"(co0), [co1] "v"(co1)
    : "v96", "v97", "v98", "v99", "v100", "v101", "v102", "v103", "v104", "v105", "v106", "v107", "v108", "v109", "v110", "v111", "v112", "v113", "v114", "v115", "v116", "v117", "v118", "v119", "v120", "v121", "v122", "v123", "v124", "v125", "v126", "v127", "v76", "v77", "v78", "v79", "v38", "v39", "v40", "v41", "v42", "v43", "v44", "v45", "v46", "v47", "v48", "v49", "v50", "v51", "v52", "v53", "v54", "v55", "v56", "v57", "v58", "v59", "v60", "v61", "v62", "v63", "v80", "v81", "v82", "v83", "v84", "v85", "v86", "v87", "v88", "v89", "v90", "v91", "v92", "v93", "v94", "v95", "vcc", "scc", "m0", "memory", "s36", "s37", "s38", "s39");
}
__device__ __forceinline__ void lean_ep1(const u32x32 (&W)[4], unsigned ea, unsigned da, unsigned x, unsigned pv, unsigned cn, const char* pg, unsigned bl, unsigned& tk, unsigned& st,
    unsigned rw, unsigned fa, unsigned fv, const char* cb, unsigned co0, unsigned co1, unsigned& bad, unsigned& pk) {
  asm volatile("v_mov_b32 v76, %[cn]\n\t"
    "ds_read_b32 v76, v76 offset:28\n\t"
    "ds_read_b128 v[96:99], %[ea] offset:0\n\t"
    "ds_read_b128 v[100:103], %[ea] offset:2048\n\t"
    "ds_read_b128 v[104:107], %[da] offset:0\n\t"
    "ds_read_b128 v[108:111], %[da] offset:2048\n\t"
    "ds_read_b128 v[112:115], %[ea] offset:4096\n\t"
    "ds_read_b128 v[116:119], %[ea] offset:6144\n\t"
    "s_waitcnt lgkmcnt(6)\n\t"
    "v_cmp_ne_u32 vcc, 0, v76\n\t"
    "s_mov_b32 %[st], -1\n\t"
    "s_cbranch_vccnz 12f\n\t"
    "s_waitcnt lgkmcnt(4)\n\t"
    "v_mfma_f32_16x16x32_bf16 v[80:83], v[128:131], v[96:99], 0\n\t"
    "ds_read_b128 v[120:123], %[da] offset:4096\n\t"
    "v_mfma_f32_16x16x32_bf16 v[84:87], v[132:135], v[96:99], 0\n\t"
    "ds_read_b128 v[124:127], %[da] offset:6144\n\t"
    "v_mfma_f32_16x16x32_bf16 v[88:91], v[128:131], v[100:103], 0\n\t"
    "v_mfma_f32_16x16x32_bf16 v[92:95], v[132:135], v[100:103], 0\n\t"
    "s_barrier\n\t"
    "s_bitcmp1_b32 %[x], 2\n\t"
    "s_cbranch_scc1 7f\n\t"
    "8:\n\t"
    "s_bitcmp1_b32 %[x], 3\n\t"
    "s_cbranch_scc1 13f\n\t"
    "14:\n\t"
    "s_waitcnt lgkmcnt(4)\n\t"
    "v_mfma_f32_16x16x32_bf16 v[80:83], v[136:139], v[104:107], v[80:83]\n\t"
    "ds_read_b128 v[96:99], %[ea] offset:8192\n\t"
    "v_mfma_f32_16x16x32_bf16 v[84:87], v[140:143], v[104:107], v[84:87]\n\t"
    "ds_read_b128 v[100:103], %[ea] offset:10240\n\t"
    "v_mfma_f32_16x16x32_bf16 v[88:91], v[136:139], v[108:111], v[88:91]\n\t"
    "v_mfma_f32_16x16x32_bf16 v[92:95], v[140:143], v[108:111], v[92:95]\n\t"
    "s_waitcnt lgkmcnt(4)\n\t"
    "v_mfma_f32_16x16x32_bf16 v[80:83], v[144:147], v[112:115], v[80:83]\n\t"
    "ds_read_b128 v[104:107], %[da] offset:8192\n\t"
    "v_mfma_f32_16x16x32_bf16 v[84:87], v[148:151], v[112:115], v[84:87]\n\t"
    "ds_read_b128 v[108:111], %[da] offset:10240\n\t"
    "v_mfma_f32_16x16x32_bf16 v[88:91], v[144:147], v[116:119], v[88:91]\n\t"
    "v_mfma_f32_16x16x32_bf16 v[92:95], v[148:151], v[116:119], v[92:95]\n\t"
    "s_waitcnt lgkmcnt(4)\n\t"
    "v_mfma_f32_16x16x32_bf16 v[80:83], v[152:155], v[120:123], v[80:83]\n\t"
    "ds_read_b128 v[112:115], %[ea] offset:12288\n\t"
    "v_mfma_f32_16x16x32_bf16 v[84:87], v[156:159], v[120:123], v[84:87]\n\t"
    "ds_read_b128 v[116:119], %[ea] offset:14336\n\t"
    "v_mfma_f32_16x16x32_bf16 v[88:91], v[152:155], v[124:127], v[88:91]\n\t"
    "v_mfma_f32_16x16x32_bf16 v[92:95], v[156:159], v[124:127], v[92:95]\n\t"
    "s_waitcnt lgkmcnt(4)\n\t"
    "v_mfma_f32_16x16x32_bf16 v[80:83], v[160:163], v[96:99], v[80:83]\n\t"
    "ds_read_b128 v[120:123], %[da] offset:12288\n\t"
    "v_mfma_f32_16x16x32_bf16 v[84:87], v[164:167], v[96:99], v[84:87]\n\t"
    "ds_read_b128 v[124:127], %[da] offset:14336\n\t"
    "v_mfma_f32_16x16x32_bf16 v[88:91], v[160:163], v[100:103], v[88:91]\n\t"
    "v_mfma_f32_16x16x32_bf16 v[92:95], v[164:167], v[100:103], v[92:95]\n\t"
    "s_barrier\n\t"
    "s_waitcnt lgkmcnt(4)\n\t"
    "v_mfma_f32_16x16x32_bf16 v[80:83], v[168:171], v[104:107], v[80:83]\n\t"
    "ds_read_b128 v[96:99], %[ea] offset:16384\n\t"
    "v_mfma_f32_16x16x32_bf16 v[84:87], v[172:175], v[104:107], v[84:87]\n\t"
    "ds_read_b128 v[100:103], %[ea] offset:18432\n\t"
    "v_mfma_f32_16x16x32_bf16 v[88:91], v[168:171], v[108:111], v[88:91]\n\t"
    "v_mfma_f32_16x16x32_bf16 v[92:95], v[172:175], v[108:111], v[92:95]\n\t"
    "s_waitcnt lgkmcnt(4)\n\t"
    "v_mfma_f32_16x16x32_bf16 v[80:83], v[176:179], v[112:115], v[80:83]\n\t"
    "ds_read_b128 v[104:107], %[da] offset:16384\n\t"
    "v_mfma_f32_16x16x32_bf16 v[84:87], v[180:183], v[112:115], v[84:87]\n\t"
    "ds_read_b128 v[108:111], %[da] offset:18432\n\t"
    "v_mfma_f32_16x16x32_bf16 v[88:91], v[176:179], v[116:119], v[88:91]\n\t"
    "v_mfma_f32_16x16x32_bf16 v[92:95], v[180:183], v[116:119], v[92:95]\n\t"
    "s_waitcnt lgkmcnt(4)\n\t"
    "v_mfma_f32_16x16x32_bf16 v[80:83], v[184:187], v[120:123], v[80:83]\n\t"
    "ds_read_b128 v[112:115], %[ea] offset:20480\n\t"
    "v_mfma_f32_16x16x32_bf16 v[84:87], v[188:191], v[120:123], v[84:87]\n\t"
    "ds_read_b128 v[116:119], %[ea] offset:22528\n\t"
    "v_mfma_f32_16x16x32_bf16 v[88:91], v[184:187], v[124:127], v[88:91]\n\t"
    "v_mfma_f32_16x16x32_bf16 v[92:95], v[188:191], v[124:127], v[92:95]\n\t"
    "s_waitcnt lgkmcnt(4)\n\t"
    "v_mfma_f32_16x16x32_bf16 v[80:83], v[192:195], v[96:99], v[80:83]\n\t"
    "ds_read_b128 v[120:123], %[da] offset:20480\n\t"
    "v_mfma_f32_16x16x32_bf16 v[84:87], v[196:199], v[96:99], v[84:87]\n\t"
    "ds_read_b128 v[124:127], %[da] offset:22528\n\t"
    "v_mfma_f32_16x16x32_bf16 v[88:91], v[192:195], v[100:103], v[88:91]\n\t"
    "v_mfma_f32_16x16x32_bf16 v[92:95], v[196:199], v[100:103], v[92:95]\n\t"
    "s_barrier\n\t"
    "s_and_b32 %[st], %[x], 3\n\t"
    "s_cbranch_scc1 3f\n\t"
    "4:\n\t"
    "s_waitcnt lgkmcnt(4)\n\t"
    "v_mfma_f32_16x16x32_bf16 v[80:83], v[200:203], v[104:107], v[80:83]\n\t"
    "ds_read_b128 v[96:99], %[ea] offset:24576\n\t"
    "v_mfma_f32_16x16x32_bf16 v[84:87], v[204:207], v[104:107], v[84:87]\n\t"
    "ds_read_b128 v[100:103], %[ea] offset:26624\n\t"
    "v_mfma_f32_16x16x32_bf16 v[88:91], v[200:203], v[108:111], v[88:91]\n\t"
    "v_mfma_f32_16x16x32_bf16 v[92:95], v[204:207], v[108:111], v[92:95]\n\t"
    "s_waitcnt lgkmcnt(4)\n\t"
    "v_mfma_f32_16x16x32_bf16 v[80:83], v[208:211], v[112:115], v[80:83]\n\t"
    "ds_read_b128 v[104:107], %[da] offset:24576\n\t"
    "v_mfma_f32_16x16x32_bf16 v[84:87], v[212:215], v[112:115], v[84:87]\n\t"
    "ds_read_b128 v[108:111], %[da] offset:26624\n\t"
    "v_mfma_f32_16x16x32_bf16 v[88:91], v[208:211], v[116:119], v[88:91]\n\t"
    "v_mfma_f32_16x16x32_bf16 v[92:95], v[212:215], v[116:119], v[92:95]\n\t"
    "s_waitcnt lgkmcnt(4)\n\t"
    "v_mfma_f32_16x16x32_bf16 v[80:83], v[216:219], v[120:123], v[80:83]\n\t"
    "ds_read_b128 v[112:115], %[ea] offset:28672\n\t"
    "v_mfma_f32_16x16x32_bf16 v[84:87], v[220:223], v[120:123], v[84:87]\n\t"
    "ds_read_b128 v[116:119], %[ea] offset:30720\n\t"
    "v_mfma_f32_16x16x32_bf16 v[88:91], v[216:219], v[124:127], v[88:91]\n\t"
    "v_mfma_f32_16x16x32_bf16 v[92:95], v[220:223], v[124:127], v[92:95]\n\t"
    "s_waitcnt lgkmcnt(4)\n\t"
    "v_mfma_f32_16x16x32_bf16 v[80:83], v[224:227], v[96:99], v[80:83]\n\t"
    "ds_read_b128 v[120:123], %[da] offset:28672\n\t"
    "v_mfma_f32_16x16x32_bf16 v[84:87], v[228:231], v[96:99], v[84:87]\n\t"
    "ds_read_b128 v[124:127], %[da] offset:30720\n\t"
    "v_mfma_f32_16x16x32_bf16 v[88:91], v[224:227], v[100:103], v[88:91]\n\t"
    "v_mfma_f32_16x16x32_bf16 v[92:95], v[228:231], v[100:103], v[92:95]\n\t"
    "s_waitcnt lgkmcnt(4)\n\t"
    "v_mfma_f32_16x16x32_bf16 v[80:83], v[232:235], v[104:107], v[80:83]\n\t"
    "v_mfma_f32_16x16x32_bf16 v[84:87], v[236:239], v[104:107], v[84:87]\n\t"
    "v_mfma_f32_16x16x32_bf16 v[88:91], v[232:235], v[108:111], v[88:91]\n\t"
    "v_mfma_f32_16x16x32_bf16 v[92:95], v[236:239], v[108:111], v[92:95]\n\t"
    "s_waitcnt lgkmcnt(2)\n\t"
    "v_mfma_f32_16x16x32_bf16 v[80:83], v[240:243], v[112:115], v[80:83]\n\t"
    "ds_read_b32 v39, %[fa]\n\t"
    "ds_read_b128 v[48:51], %[rw] offset:0\n\t"
    "v_mfma_f32_16x16x32_bf16 v[84:87], v[244:247], v[112:115], v[84:87]\n\t"
    "ds_read_b128 v[52:55], %[rw] offset:1024\n\t"
    "v_mfma_f32_16x16x32_bf16 v[88:91], v[240:243], v[116:119], v[88:91]\n\t"
    "ds_read_b128 v[56:59], %[rw] offset:2048\n\t"
    "v_mfma_f32_16x16x32_bf16 v[92:95], v[244:247], v[116:119], v[92:95]\n\t"
    "ds_read_b128 v[60:63], %[rw] offset:3072\n\t"
    "s_waitcnt lgkmcnt(5)\n\t"
    "v_mfma_f32_16x16x32_bf16 v[80:83], v[248:251], v[120:123], v[80:83]\n\t"
    "v_mfma_f32_16x16x32_bf16 v[84:87], v[252:255], v[120:123], v[84:87]\n\t"
    "v_mfma_f32_16x16x32_bf16 v[88:91], v[248:251], v[124:127], v[88:91]\n\t"
    "v_mfma_f32_16x16x32_bf16 v[92:95], v[252:255], v[124:127], v[92:95]\n\t"
    "s_mov_b32 %[st], 0\n\t"
    "21:\n\t"
    "s_waitcnt lgkmcnt(0)\n\t"
    "v_cmp_ne_u32 vcc, %[fv], v39\n\t"
    "s_cbranch_vccnz 20f\n\t"
    "v_mov_b32 v39, %[cn]\n\t"
    "s_nop 0\n\t"
    "v_pk_add_f32 v[48:49], v[48:49], v[80:81]\n\t"
    "v_pk_add_f32 v[50:51], v[50:51], v[82:83]\n\t"
    "v_pk_add_f32 v[52:53], v[52:53], v[84:85]\n\t"
    "v_pk_add_f32 v[54:55], v[54:55], v[86:87]\n\t"
    "v_pk_add_f32 v[56:57], v[56:57], v[88:89]\n\t"
    "v_pk_add_f32 v[58:59], v[58:59], v[90:91]\n\t"
    "v_pk_add_f32 v[60:61], v[60:61], v[92:93]\n\t"
    "v_pk_add_f32 v[62:63], v[62:63], v[94:95]\n\t"
    "v_mul_f32 v40, %[nl], v48\n\t"
    "v_mul_f32 v41, %[nl], v49\n\t"
    "v_mul_f32 v42, %[nl], v50\n\t"
    "v_mul_f32 v43, %[nl], v51\n\t"
    "v_mul_f32 v44, %[nl], v56\n\t"
    "v_mul_f32 v45, %[nl], v57\n\t"
    "v_mul_f32 v46, %[nl], v58\n\t"
    "v_mul_f32 v47, %[nl], v59\n\t"
    "v_exp_f32 v40, v40\n\t"
    "v_exp_f32 v41, v41\n\t"
    "v_exp_f32 v42, v42\n\t"
    "v_exp_f32 v43, v43\n\t"
    "v_exp_f32 v44, v44\n\t"
    "v_exp_f32 v45, v45\n\t"
    "v_exp_f32 v46, v46\n\t"
    "v_exp_f32 v47, v47\n\t"
    "v_pk_add_f32 v[40:41], v[40:41], 1.0 op_sel_hi:[1,0]\n\t"
    "v_pk_add_f32 v[42:43], v[42:43], 1.0 op_sel_hi:[1,0]\n\t"
    "v_pk_add_f32 v[44:45], v[44:45], 1.0 op_sel_hi:[1,0]\n\t"
    "v_pk_add_f32 v[46:47], v[46:47], 1.0 op_sel_hi:[1,0]\n\t"
    "v_rcp_f32 v40, v40\n\t"
    "v_rcp_f32 v41, v41\n\t"
    "v_rcp_f32 v42, v42\n\t"
    "v_rcp_f32 v43, v43\n\t"
    "v_rcp_f32 v44, v44\n\t"
    "v_rcp_f32 v45, v45\n\t"
    "v_rcp_f32 v46, v46\n\t"
    "v_rcp_f32 v47, v47\n\t"
    "v_pk_mul_f32 v[40:41], v[48:49], v[40:41]\n\t"
    "v_pk_mul_f32 v[42:43], v[50:51], v[42:43]\n\t"
    "v_pk_mul_f32 v[44:45], v[56:57], v[44:45]\n\t"
    "v_pk_mul_f32 v[46:47], v[58:59], v[46:47]\n\t"
    "v_pk_mul_f32 v[40:41], v[40:41], v[52:53]\n\t"
    "v_pk_mul_f32 v[42:43], v[42:43], v[54:55]\n\t"
    "v_pk_mul_f32 v[44:45], v[44:45], v[60:61]\n\t"
    "v_pk_mul_f32 v[46:47], v[46:47], v[62:63]\n\t"
    "v_cvt_pk_bf16_f32 v48, v40, v41\n\t"
    "v_cvt_pk_bf16_f32 v49, v42, v43\n\t"
    "v_cvt_pk_bf16_f32 v50, v44, v45\n\t"
    "ds_read_b32 v38, v39 offset:4\n\t"
    "v_cvt_pk_bf16_f32 v51, v46, v47\n\t"
    "s_nop 1\n\t"
    "v_permlane16_swap_b32 v48, v50\n\t"
    "v_permlane16_swap_b32 v49, v51\n\t"
    "global_store_dwordx4 %[co0], v[48:51], %[cb] sc1\n\t"
    "s_memrealtime s[36:37]\n\t"
    "v_cmp_u_f32 vcc, v52, v60\n\t"
    "s_waitcnt lgkmcnt(0)\n\t"
    "s_or_b32 %[bad], vcc_lo, vcc_hi\n\t"
    "v_readfirstlane_b32 %[pk], v38\n\t"
    "s_sub_u32 %[tk], s36, %[tk]\n\t"
    "s_branch 9f\n\t"
    "20:\n\t"
    "ds_read_b32 v39, %[fa]\n\t"
    "ds_read_b128 v[48:51], %[rw] offset:0\n\t"
    "ds_read_b128 v[52:55], %[rw] offset:1024\n\t"
    "ds_read_b128 v[56:59], %[rw] offset:2048\n\t"
    "ds_read_b128 v[60:63], %[rw] offset:3072\n\t"
    "s_branch 21b\n\t"
    "13:\n\t"
    "s_memrealtime s[36:37]\n\t"
    "v_mov_b32 v79, 0\n\t"
    "s_waitcnt lgkmcnt(0)\n\t"
    "s_mov_b32 %[tk], s36\n\t"
    "s_add_u32 s36, s36, %[bl]\n\t"
    "s_and_b32 s36, s36, 0xffffff\n\t"
    "s_and_b32 s37, %[bl], 0xff000000\n\t"
    "s_or_b32 s36, s36, s37\n\t"
    "v_mov_b32 v78, s36\n\t"
    "s_mov_b64 exec, 1\n\t"
    "global_store_dword v79, v78, %[pg] offset:64 sc1\n\t"
    "s_mov_b64 exec, -1\n\t"
    "s_branch 14b\n\t"
    "3:\n\t"
    "7:\n\t"
    "s_and_b32 %[st], %[pv], 1\n\t"
    "s_mul_i32 %[st], %[st], 36\n\t"
    "s_add_u32 %[st], %[st], %[cn]\n\t"
    "v_mov_b32 v76, %[st]\n\t"
    "v_mov_b32 v77, 1\n\t"
    "v_mov_b32 v78, %[pv]\n\t"
    "v_mov_b32 v79, 0\n\t"
    "s_waitcnt vmcnt(0)\n\t"
    "s_mov_b64 exec, 1\n\t"
    "ds_add_rtn_u32 v77, v76, v77\n\t"
    "s_waitcnt lgkmcnt(0)\n\t"
    "v_readfirstlane_b32 %[st], v77\n\t"
    "s_and_b32 %[st], %[st], 3\n\t"
    "s_cmp_lg_u32 %[st], 3\n\t"
    "s_cbranch_scc1 6f\n\t"
    "global_store_dword v79, v78, %[pg] sc1\n\t"
    "6:\n\t"
    "s_mov_b64 exec, -1\n\t"
    "s_bitcmp1_b32 %[x], 2\n\t"
    "s_cbranch_scc1 8b\n\t"
    "s_branch 4b\n\t"
    "12:\n\t"
    "s_waitcnt lgkmcnt(0)\n\t"
    "9:"
    : [st] "=&s"(st), [tk] "+&s"(tk), [bad] "=&s"(bad), [pk] "=&s"(pk)
    : [ea] "v"(ea), [da] "v"(da), [x] "s"(x), "{v[128:159]}"(W[0]), "{v[160:191]}"(W[1]), "{v[192:223]}"(W[2]), "{v[224:255]}"(W[3]), [pv] "s"(pv), [cn] "s"(cn), [pg] "s"(pg), [bl] "s"(bl), [rw] "v"(rw), [fa] "v"(fa), [fv] "s"(fv), [cb] "s"(cb), [co0] "v"(co0), [nl] "s"(-LOG2E)
    : "v96", "v97", "v98", "v99", "v100", "v101", "v102", "v103", "v104", "v105", "v106", "v107", "v108", "v109", "v110", "v111", "v112", "v113", "v114", "v115", "v116", "v117", "v118", "v119", "v120", "v121", "v122", "v123", "v124", "v125", "v126", "v127", "v76", "v77", "v78", "v79", "v38", "v39", "v40", "v41", "v42", "v43", "v44", "v45", "v46", "v47", "v48", "v49", "v50", "v51", "v52", "v53", "v54", "v55", "v56", "v57", "v58", "v59", "v60", "v61", "v62", "v63", "v80", "v81", "v82", "v83", "v84", "v85", "v86", "v87", "v88", "v89", "v90", "v91", "v92", "v93", "v94", "v95", "vcc", "scc", "m0", "memory", "s36", "s37", "s38", "s39");
}
__device__ __forceinline__ void lean_ep2(const u32x32 (&W)[4], unsigned ea, unsigned da, unsigned x, unsigned pv, unsigned cn, const char* pg, unsigned bl, unsigned& tk, unsigned& st,
    unsigned rw, unsigned fa, unsigned fv, const char* cb, unsigned co0, unsigned co1, unsigned& bad, unsigned& pk) {
  asm volatile("v_mov_b32 v76, %[cn]\n\t"
    "ds_read_b32 v76, v76 offset:28\n\t"
    "ds_read_b128 v[96:99], %[ea] offset:0\n\t"
    "ds_read_b128 v[100:103], %[ea] offset:2048\n\t"
    "ds_read_b128 v[104:107], %[da] offset:0\n\t"
    "ds_read_b128 v[108:111], %[da] offset:2048\n\t"
    "ds_read_b128 v[112:115], %[ea] offset:4096\n\t"
    "ds_read_b128 v[116:119], %[ea] offset:6144\n\t"
    "s_waitcnt lgkmcnt(6)\n\t"
    "v_cmp_ne_u32 vcc, 0, v76\n\t"
    "s_mov_b32 %[st], -1\n\t"
    "s_cbranch_vccnz 12f\n\t"
    "s_waitcnt lgkmcnt(4)\n\t"
    "v_mfma_f32_16x16x32_bf16 v[80:83], v[128:131], v[96:99], 0\n\t"
    "ds_read_b128 v[120:123], %[da] offset:4096\n\t"
    "v_mfma_f32_16x16x32_bf16 v[84:87], v[132:135], v[96:99], 0\n\t"
    "ds_read_b128 v[124:127], %[da] offset:6144\n\t"
    "v_mfma_f32_16x16x32_bf16 v[88:91], v[128:131], v[100:103], 0\n\t"
    "v_mfma_f32_16x16x32_bf16 v[92:95], v[132:135], v[100:103], 0\n\t"
    "s_barrier\n\t"
    "s_bitcmp1_b32 %[x], 2\n\t"
    "s_cbranch_scc1 7f\n\t"
    "8:\n\t"
    "s_bitcmp1_b32 %[x], 3\n\t"
    "s_cbranch_scc1 13f\n\t"
    "14:\n\t"
    "s_waitcnt lgkmcnt(4)\n\t"
    "v_mfma_f32_16x16x32_bf16 v[80:83], v[136:139], v[104:107], v[80:83]\n\t"
    "ds_read_b128 v[96:99], %[ea] offset:8192\n\t"
    "v_mfma_f32_16x16x32_bf16 v[84:87], v[140:143], v[104:107], v[84:87]\n\t"
    "ds_read_b128 v[100:103], %[ea] offset:10240\n\t"
    "v_mfma_f32_16x16x32_bf16 v[88:91], v[136:139], v[108:111], v[88:91]\n\t"
    "v_mfma_f32_16x16x32_bf16 v[92:95], v[140:143], v[108:111], v[92:95]\n\t"
    "s_waitcnt lgkmcnt(4)\n\t"
    "v_mfma_f32_16x16x32_bf16 v[80:83], v[144:147], v[112:115], v[80:83]\n\t"
    "ds_read_b128 v[104:107], %[da] offset:8192\n\t"
    "v_mfma_f32_16x16x32_bf16 v[84:87], v[148:151], v[112:115], v[84:87]\n\t"
    "ds_read_b128 v[108:111], %[da] offset:10240\n\t"
    "v_mfma_f32_16x16x32_bf16 v[88:91], v[144:147], v[116:119], v[88:91]\n\t"
    "v_mfma_f32_16x16x32_bf16 v[92:95], v[148:151], v[116:119], v[92:95]\n\t"
    "s_waitcnt lgkmcnt(4)\n\t"
    "v_mfma_f32_16x16x32_bf16 v[80:83], v[152:155], v[120:123], v[80:83]\n\t"
    "ds_read_b128 v[112:115], %[ea] offset:12288\n\t"
    "v_mfma_f32_16x16x32_bf16 v[84:87], v[156:159], v[120:123], v[84:87]\n\t"
    "ds_read_b128 v[116:119], %[ea] offset:14336\n\t"
    "v_mfma_f32_16x16x32_bf16 v[88:91], v[152:155], v[124:127], v[88:91]\n\t"
    "v_mfma_f32_16x16x32_bf16 v[92:95], v[156:159], v[124:127], v[92:95]\n\t"
    "s_waitcnt lgkmcnt(4)\n\t"
    "v_mfma_f32_16x16x32_bf16 v[80:83], v[160:163], v[96:99], v[80:83]\n\t"
    "ds_read_b128 v[120:123], %[da] offset:12288\n\t"
    "v_mfma_f32_16x16x32_bf16 v[84:87], v[164:167], v[96:99], v[84:87]\n\t"
    "ds_read_b128 v[124:127], %[da] offset:14336\n\t"
    "v_mfma_f32_16x16x32_bf16 v[88:91], v[160:163], v[100:103], v[88:91]\n\t"
    "v_mfma_f32_16x16x32_bf16 v[92:95], v[164:167], v[100:103], v[92:95]\n\t"
    "s_barrier\n\t"
    "s_waitcnt lgkmcnt(4)\n\t"
    "v_mfma_f32_16x16x32_bf16 v[80:83], v[168:171], v[104:107], v[80:83]\n\t"
    "ds_read_b128 v[96:99], %[ea] offset:16384\n\t"
    "v_mfma_f32_16x16x32_bf16 v[84:87], v[172:175], v[104:107], v[84:87]\n\t"
    "ds_read_b128 v[100:103], %[ea] offset:18432\n\t"
    "v_mfma_f32_16x16x32_bf16 v[88:91], v[168:171], v[108:111], v[88:91]\n\t"
    "v_mfma_f32_16x16x32_bf16 v[92:95], v[172:175], v[108:111], v[92:95]\n\t"
    "s_waitcnt lgkmcnt(4)\n\t"
    "v_mfma_f32_16x16x32_bf16 v[80:83], v[176:179], v[112:115], v[80:83]\n\t"
    "ds_read_b128 v[104:107], %[da] offset:16384\n\t"
    "v_mfma_f32_16x16x32_bf16 v[84:87], v[180:183], v[112:115], v[84:87]\n\t"
    "ds_read_b128 v[108:111], %[da] offset:18432\n\t"
    "v_mfma_f32_16x16x32_bf16 v[88:91], v[176:179], v[116:119], v[88:91]\n\t"
    "v_mfma_f32_16x16x32_bf16 v[92:95], v[180:183], v[116:119], v[92:95]\n\t"
    "s_waitcnt lgkmcnt(4)\n\t"
    "v_mfma_f32_16x16x32_bf16 v[80:83], v[184:187], v[120:123], v[80:83]\n\t"
    "ds_read_b128 v[112:115], %[ea] offset:20480\n\t"
    "v_mfma_f32_16x16x32_bf16 v[84:87], v[188:191], v[120:123], v[84:87]\n\t"
    "ds_read_b128 v[116:119], %[ea] offset:22528\n\t"
    "v_mfma_f32_16x16x32_bf16 v[88:91], v[184:187], v[124:127], v[88:91]\n\t"
    "v_mfma_f32_16x16x32_bf16 v[92:95], v[188:191], v[124:127], v[92:95]\n\t"
    "s_waitcnt lgkmcnt(4)\n\t"
    "v_mfma_f32_16x16x32_bf16 v[80:83], v[192:195], v[96:99], v[80:83]\n\t"
    "ds_read_b128 v[120:123], %[da] offset:20480\n\t"
    "v_mfma_f32_16x16x32_bf16 v[84:87], v[196:199], v[96:99], v[84:87]\n\t"
    "ds_read_b128 v[124:127], %[da] offset:22528\n\t"
    "v_mfma_f32_16x16x32_bf16 v[88:91], v[192:195], v[100:103], v[88:91]\n\t"
    "v_mfma_f32_16x16x32_bf16 v[92:95], v[196:199], v[100:103], v[92:95]\n\t"
    "s_barrier\n\t"
    "s_and_b32 %[st], %[x], 3\n\t"
    "s_cbranch_scc1 3f\n\t"
    "4:\n\t"
    "s_waitcnt lgkmcnt(4)\n\t"
    "v_mfma_f32_16x16x32_bf16 v[80:83], v[200:203], v[104:107], v[80:83]\n\t"
    "ds_read_b128 v[96:99], %[ea] offset:24576\n\t"
    "v_mfma_f32_16x16x32_bf16 v[84:87], v[204:207], v[104:107], v[84:87]\n\t"
    "ds_read_b128 v[100:103], %[ea] offset:26624\n\t"
    "v_mfma_f32_16x16x32_bf16 v[88:91], v[200:203], v[108:111], v[88:91]\n\t"
    "v_mfma_f32_16x16x32_bf16 v[92:95], v[204:207], v[108:111], v[92:95]\n\t"
    "s_waitcnt lgkmcnt(4)\n\t"
    "v_mfma_f32_16x16x32_bf16 v[80:83], v[208:211], v[112:115], v[80:83]\n\t"
    "ds_read_b128 v[104:107], %[da] offset:24576\n\t"
    "v_mfma_f32_16x16x32_bf16 v[84:87], v[212:215], v[112:115], v[84:87]\n\t"
    "ds_read_b128 v[108:111], %[da] offset:26624\n\t"
    "v_mfma_f32_16x16x32_bf16 v[88:91], v[208:211], v[116:119], v[88:91]\n\t"
    "v_mfma_f32_16x16x32_bf16 v[92:95], v[212:215], v[116:119], v[92:95]\n\t"
    "s_waitcnt lgkmcnt(4)\n\t"
    "v_mfma_f32_16x16x32_bf16 v[80:83], v[216:219], v[120:123], v[80:83]\n\t"
    "ds_read_b128 v[112:115], %[ea] offset:28672\n\t"
    "v_mfma_f32_16x16x32_bf16 v[84:87], v[220:223], v[120:123], v[84:87]\n\t"
    "ds_read_b128 v[116:119], %[ea] offset:30720\n\t"
    "v_mfma_f32_16x16x32_bf16 v[88:91], v[216:219], v[124:127], v[88:91]\n\t"
    "v_mfma_f32_16x16x32_bf16 v[92:95], v[220:223], v[124:127], v[92:95]\n\t"
    "s_waitcnt lgkmcnt(4)\n\t"
    "v_mfma_f32_16x16x32_bf16 v[80:83], v[224:227], v[96:99], v[80:83]\n\t"
    "ds_read_b128 v[120:123], %[da] offset:28672\n\t"
    "v_mfma_f32_16x16x32_bf16 v[84:87], v[228:231], v[96:99], v[84:87]\n\t"
    "ds_read_b128 v[124:127], %[da] offset:30720\n\t"
    "v_mfma_f32_16x16x32_bf16 v[88:91], v[224:227], v[100:103], v[88:91]\n\t"
    "v_mfma_f32_16x16x32_bf16 v[92:95], v[228:231], v[100:103], v[92:95]\n\t"
    "s_waitcnt lgkmcnt(4)\n\t"
    "v_mfma_f32_16x16x32_bf16 v[80:83], v[232:235], v[104:107], v[80:83]\n\t"
    "v_mfma_f32_16x16x32_bf16 v[84:87], v[236:239], v[104:107], v[84:87]\n\t"
    "v_mfma_f32_16x16x32_bf16 v[88:91], v[232:235], v[108:111], v[88:91]\n\t"
    "v_mfma_f32_16x16x32_bf16 v[92:95], v[236:239], v[108:111], v[92:95]\n\t"
    "s_waitcnt lgkmcnt(2)\n\t"
    "v_mfma_f32_16x16x32_bf16 v[80:83], v[240:243], v[112:115], v[80:83]\n\t"
    "ds_read_b32 v39, %[fa]\n\t"
    "ds_read_b128 v[48:51], %[rw] offset:0\n\t"
    "v_mfma_f32_16x16x32_bf16 v[84:87], v[244:247], v[112:115], v[84:87]\n\t"
    "ds_read_b128 v[52:55], %[rw] offset:1024\n\t"
    "v_mfma_f32_16x16x32_bf16 v[88:91], v[240:243], v[116:119], v[88:91]\n\t"
    "ds_read_b128 v[56:59], %[rw] offset:2048\n\t"
    "v_mfma_f32_16x16x32_bf16 v[92:95], v[244:247], v[116:119], v[92:95]\n\t"
    "ds_read_b128 v[60:63], %[rw] offset:3072\n\t"
    "s_waitcnt lgkmcnt(5)\n\t"
    "v_mfma_f32_16x16x32_bf16 v[80:83], v[248:251], v[120:123], v[80:83]\n\t"
    "v_mfma_f32_16x16x32_bf16 v[84:87], v[252:255], v[120:123], v[84:87]\n\t"
    "v_mfma_f32_16x16x32_bf16 v[88:91], v[248:251], v[124:127], v[88:91]\n\t"
    "v_mfma_f32_16x16x32_bf16 v[92:95], v[252:255], v[124:127], v[92:95]\n\t"
    "s_mov_b32 %[st], 0\n\t"
    "21:\n\t"
    "s_waitcnt lgkmcnt(0)\n\t"
    "v_cmp_ne_u32 vcc, %[fv], v39\n\t"
    "s_cbranch_vccnz 20f\n\t"
    "v_mov_b32 v39, %[cn]\n\t"
    "s_nop 0\n\t"
    "v_cvt_pk_bf16_f32 v40, v80, v81\n\t"
    "v_cvt_pk_bf16_f32 v41, v82, v83\n\t"
    "v_cvt_pk_bf16_f32 v42, v84, v85\n\t"
    "v_cvt_pk_bf16_f32 v43, v86, v87\n\t"
    "global_store_dwordx4 %[co0], v[40:43], %[cb] sc1\n\t"
    "v_cvt_pk_bf16_f32 v44, v88, v89\n\t"
    "v_cvt_pk_bf16_f32 v45, v90, v91\n\t"
    "v_cvt_pk_bf16_f32 v46, v92, v93\n\t"
    "v_cvt_pk_bf16_f32 v47, v94, v95\n\t"
    "global_store_dwordx4 %[co1], v[44:47], %[cb] sc1\n\t"
    "v_cvt_pk_bf16_f32 v48, v48, v49\n\t"
    "v_cvt_pk_bf16_f32 v49, v50, v51\n\t"
    "v_cvt_pk_bf16_f32 v50, v52, v53\n\t"
    "v_cvt_pk_bf16_f32 v51, v54, v55\n\t"
    "global_store_dwordx4 %[co0], v[48:51], %[cb] offset:2048 sc1\n\t"
    "ds_read_b32 v38, v39 offset:4\n\t"
    "v_cvt_pk_bf16_f32 v56, v56, v57\n\t"
    "v_cvt_pk_bf16_f32 v57, v58, v59\n\t"
    "v_cvt_pk_bf16_f32 v58, v60, v61\n\t"
    "v_cvt_pk_bf16_f32 v59, v62, v63\n\t"
    "global_store_dwordx4 %[co1], v[56:59], %[cb] offset:2048 sc1\n\t"
    "s_memrealtime s[36:37]\n\t"
    "v_cmp_u_f32 vcc, v80, v88\n\t"
    "s_waitcnt lgkmcnt(0)\n\t"
    "s_or_b32 %[bad], vcc_lo, vcc_hi\n\t"
    "v_readfirstlane_b32 %[pk], v38\n\t"
    "s_sub_u32 %[tk], s36, %[tk]\n\t"
    "s_branch 9f\n\t"
    "20:\n\t"
    "ds_read_b32 v39, %[fa]\n\t"
    "ds_read_b128 v[48:51], %[rw] offset:0\n\t"
    "ds_read_b128 v[52:55], %[rw] offset:1024\n\t"
    "ds_read_b128 v[56:59], %[rw] offset:2048\n\t"
    "ds_read_b128 v[60:63], %[rw] offset:3072\n\t"
    "s_branch 21b\n\t"
    "13:\n\t"
    "s_memrealtime s[36:37]\n\t"
    "v_mov_b32 v79, 0\n\t"
    "s_waitcnt lgkmcnt(0)\n\t"
    "s_mov_b32 %[tk], s36\n\t"
    "s_add_u32 s36, s36, %[bl]\n\t"
    "s_and_b32 s36, s36, 0xffffff\n\t"
    "s_and_b32 s37, %[bl], 0xff000000\n\t"
    "s_or_b32 s36, s36, s37\n\t"
    "v_mov_b32 v78, s36\n\t"
    "s_mov_b64 exec, 1\n\t"
    "global_store_dword v79, v78, %[pg] offset:64 sc1\n\t"
    "s_mov_b64 exec, -1\n\t"
    "s_branch 14b\n\t"
    "3:\n\t"
    "7:\n\t"
    "s_and_b32 %[st], %[pv], 1\n\t"
    "s_mul_i32 %[st], %[st], 36\n\t"
    "s_add_u32 %[st], %[st], %[cn]\n\t"
    "v_mov_b32 v76, %[st]\n\t"
    "v_mov_b32 v77, 1\n\t"
    "v_mov_b32 v78, %[pv]\n\t"
    "v_mov_b32 v79, 0\n\t"
    "s_waitcnt vmcnt(0)\n\t"
    "s_mov_b64 exec, 1\n\t"
    "ds_add_rtn_u32 v77, v76, v77\n\t"
    "s_waitcnt lgkmcnt(0)\n\t"
    "v_readfirstlane_b32 %[st], v77\n\t"
    "s_and_b32 %[st], %[st], 3\n\t"
    "s_cmp_lg_u32 %[st], 3\n\t"
    "s_cbranch_scc1 6f\n\t"
    "global_store_dword v79, v78, %[pg] sc1\n\t"
    "6:\n\t"
    "s_mov_b64 exec, -1\n\t"
    "s_bitcmp1_b32 %[x], 2\n\t"
    "s_cbranch_scc1 8b\n\t"
    "s_branch 4b\n\t"
    "12:\n\t"
    "s_waitcnt lgkmcnt(0)\n\t"
    "9:"
    : [st] "=&s"(st), [tk] "+&s"(tk), [bad] "=&s"(bad), [pk] "=&s"(pk)
    : [ea] "v"(ea), [da] "v"(da), [x] "s"(x), "{v[128:159]}"(W[0]), "{v[160:191]}"(W[1]), "{v[192:223]}"(W[2]), "{v[224:255]}"(W[3]), [pv] "s"(pv), [cn] "s"(cn), [pg] "s"(pg), [bl] "s"(bl), [rw] "v"(rw), [fa] "v"(fa), [fv] "s"(fv), [cb] "s"(cb), [co0] "v"(co0), [co1] "v"(co1)
    : "v96", "v97", "v98", "v99", "v100", "v101", "v102", "v103", "v104", "v105", "v106", "v107", "v108", "v109", "v110", "v111", "v112", "v113", "v114", "v115", "v116", "v117", "v118", "v119", "v120", "v121", "v122", "v123", "v124", "v125", "v126", "v127", "v76", "v77", "v78", "v79", "v38", "v39", "v40", "v41", "v42", "v43", "v44", "v45", "v46", "v47", "v48", "v49", "v50", "v51", "v52", "v53", "v54", "v55", "v56", "v57", "v58", "v59", "v60", "v61", "v62", "v63", "v80", "v81", "v82", "v83", "v84", "v85", "v86", "v87", "v88", "v89", "v90", "v91", "v92", "v93", "v94", "v95", "vcc", "scc", "m0", "memory", "s36", "s37", "s38", "s39");
}
#define LEAN_QB 1
#define PREK 10
#define NEARLY 16
__device__ __forceinline__ void fin_ld(const u32x16& pd, unsigned rw, unsigned fa, unsigned fv) {
  asm volatile("ds_write_b128 %[rw], v[80:83] offset:0\n\t"
    "ds_write_b128 %[rw], v[84:87] offset:1024\n\t"
    "ds_write_b128 %[rw], v[88:91] offset:2048\n\t"
    "ds_write_b128 %[rw], v[92:95] offset:3072\n\t"
    "ds_write_b32 %[fa], %[fv]"
    :
    : [rw] "v"(rw), "{v[80:95]}"(pd), [fa] "v"(fa), [fv] "v"(fv)
    : "memory");
}
__device__ __forceinline__ void fin_st0(const u32x16& pd, unsigned rw, unsigned fa, unsigned fv, const char* cb, unsigned co0, unsigned co1, unsigned pa, unsigned& bad, unsigned& pk) {
  asm volatile("1:\n\t"
    "ds_read_b32 v39, %[fa]\n\t"
    "ds_read_b128 v[48:51], %[rw] offset:0\n\t"
    "ds_read_b128 v[52:55], %[rw] offset:1024\n\t"
    "ds_read_b128 v[56:59], %[rw] offset:2048\n\t"
    "ds_read_b128 v[60:63], %[rw] offset:3072\n\t"
    "s_waitcnt lgkmcnt(2)\n\t"
    "v_cmp_ne_u32 vcc, v39, %[fv]\n\t"
    "s_cbranch_vccz 2f\n\t"
    "s_waitcnt lgkmcnt(0)\n\t"
    "s_branch 1b\n\t"
    "2:\n\t"
    "v_pk_add_f32 v[48:49], v[48:49], v[80:81]\n\t"
    "v_pk_add_f32 v[50:51], v[50:51], v[82:83]\n\t"
    "v_pk_add_f32 v[52:53], v[52:53], v[84:85]\n\t"
    "v_pk_add_f32 v[54:55], v[54:55], v[86:87]\n\t"
    "v_cvt_pk_bf16_f32 v40, v48, v49\n\t"
    "v_cvt_pk_bf16_f32 v41, v50, v51\n\t"
    "v_cvt_pk_bf16_f32 v42, v52, v53\n\t"
    "v_cvt_pk_bf16_f32 v43, v54, v55\n\t"
    "global_store_dwordx4 %[co0], v[40:43], %[cb] sc1\n\t"
    "s_waitcnt lgkmcnt(0)\n\t"
    "v_mov_b32 v39, %[pa]\n\t"
    "ds_read_b32 v39, v39\n\t"
    "v_pk_add_f32 v[56:57], v[56:57], v[88:89]\n\t"
    "v_pk_add_f32 v[58:59], v[58:59], v[90:91]\n\t"
    "v_pk_add_f32 v[60:61], v[60:61], v[92:93]\n\t"
    "v_pk_add_f32 v[62:63], v[62:63], v[94:95]\n\t"
    "v_cvt_pk_bf16_f32 v44, v56, v57\n\t"
    "v_cvt_pk_bf16_f32 v45, v58, v59\n\t"
    "v_cvt_pk_bf16_f32 v46, v60, v61\n\t"
    "v_cvt_pk_bf16_f32 v47, v62, v63\n\t"
    "global_store_dwordx4 %[co1], v[44:47], %[cb] sc1\n\t"
    "v_cmp_u_f32 vcc, v48, v56\n\t"
    "s_waitcnt lgkmcnt(0)\n\t"
    "s_or_b32 %[bad], vcc_lo, vcc_hi\n\t"
    "v_readfirstlane_b32 %[pk], v39"
    : [bad] "=&s"(bad), [pk] "=&s"(pk)
    : [rw] "v"(rw), "{v[80:95]}"(pd), [fa] "v"(fa), [fv] "v"(fv), [cb] "s"(cb), [co0] "v"(co0), [co1] "v"(co1), [pa] "s"(pa)
    : "v39", "v40", "v41", "v42", "v43", "v44", "v45", "v46", "v47", "v48", "v49", "v50", "v51", "v52", "v53", "v54", "v55", "v56", "v57", "v58", "v59", "v60", "v61", "v62", "v63", "vcc", "scc", "memory");
}
__device__ __forceinline__ void fin_st1(const u32x16& pd, unsigned rw, unsigned fa, unsigned fv, const char* cb, unsigned co0, unsigned co1, unsigned pa, unsigned& bad, unsigned& pk) {
  asm volatile("1:\n\t"
    "ds_read_b32 v39, %[fa]\n\t"
    "ds_read_b128 v[48:51], %[rw] offset:0\n\t"
    "ds_read_b128 v[52:55], %[rw] offset:1024\n\t"
    "ds_read_b128 v[56:59], %[rw] offset:2048\n\t"
    "ds_read_b128 v[60:63], %[rw] offset:3072\n\t"
    "s_waitcnt lgkmcnt(2)\n\t"
    "v_cmp_ne_u32 vcc, v39, %[fv]\n\t"
    "s_cbranch_vccz 2f\n\t"
    "s_waitcnt lgkmcnt(0)\n\t"
    "s_branch 1b\n\t"
    "2:\n\t"
    "v_pk_add_f32 v[48:49], v[48:49], v[80:81]\n\t"
    "v_pk_add_f32 v[50:51], v[50:51], v[82:83]\n\t"
    "v_pk_add_f32 v[52:53], v[52:53], v[84:85]\n\t"
    "v_pk_add_f32 v[54:55], v[54:55], v[86:87]\n\t"
    "s_waitcnt lgkmcnt(0)\n\t"
    "v_mov_b32 v39, %[pa]\n\t"
    "ds_read_b32 v39, v39\n\t"
    "v_pk_add_f32 v[56:57], v[56:57], v[88:89]\n\t"
    "v_pk_add_f32 v[58:59], v[58:59], v[90:91]\n\t"
    "v_pk_add_f32 v[60:61], v[60:61], v[92:93]\n\t"
    "v_pk_add_f32 v[62:63], v[62:63], v[94:95]\n\t"
    "v_mul_f32 v40, %[nl], v48\n\t"
    "v_mul_f32 v41, %[nl], v49\n\t"
    "v_mul_f32 v42, %[nl], v50\n\t"
    "v_mul_f32 v43, %[nl], v51\n\t"
    "v_mul_f32 v44, %[nl], v56\n\t"
    "v_mul_f32 v45, %[nl], v57\n\t"
    "v_mul_f32 v46, %[nl], v58\n\t"
    "v_mul_f32 v47, %[nl], v59\n\t"
    "v_exp_f32 v40, v40\n\t"
    "v_exp_f32 v41, v41\n\t"
    "v_exp_f32 v42, v42\n\t"
    "v_exp_f32 v43, v43\n\t"
    "v_exp_f32 v44, v44\n\t"
    "v_exp_f32 v45, v45\n\t"
    "v_exp_f32 v46, v46\n\t"
    "v_exp_f32 v47, v47\n\t"
    "v_pk_add_f32 v[40:41], v[40:41], 1.0 op_sel_hi:[1,0]\n\t"
    "v_pk_add_f32 v[42:43], v[42:43], 1.0 op_sel_hi:[1,0]\n\t"
    "v_pk_add_f32 v[44:45], v[44:45], 1.0 op_sel_hi:[1,0]\n\t"
    "v_pk_add_f32 v[46:47], v[46:47], 1.0 op_sel_hi:[1,0]\n\t"
    "v_rcp_f32 v40, v40\n\t"
    "v_rcp_f32 v41, v41\n\t"
    "v_rcp_f32 v42, v42\n\t"
    "v_rcp_f32 v43, v43\n\t"
    "v_rcp_f32 v44, v44\n\t"
    "v_rcp_f32 v45, v45\n\t"
    "v_rcp_f32 v46, v46\n\t"
    "v_rcp_f32 v47, v47\n\t"
    "v_pk_mul_f32 v[40:41], v[48:49], v[40:41]\n\t"
    "v_pk_mul_f32 v[42:43], v[50:51], v[42:43]\n\t"
    "v_pk_mul_f32 v[44:45], v[56:57], v[44:45]\n\t"
    "v_pk_mul_f32 v[46:47], v[58:59], v[46:47]\n\t"
    "v_pk_mul_f32 v[40:41], v[40:41], v[52:53]\n\t"
    "v_pk_mul_f32 v[42:43], v[42:43], v[54:55]\n\t"
    "v_pk_mul_f32 v[44:45], v[44:45], v[60:61]\n\t"
    "v_pk_mul_f32 v[46:47], v[46:47], v[62:63]\n\t"
    "v_cvt_pk_bf16_f32 v48, v40, v41\n\t"
    "v_cvt_pk_bf16_f32 v49, v42, v43\n\t"
    "v_cvt_pk_bf16_f32 v50, v44, v45\n\t"
    "v_cvt_pk_bf16_f32 v51, v46, v47\n\t"
    "s_nop 1\n\t"
    "v_permlane16_swap_b32 v48, v50\n\t"
    "v_permlane16_swap_b32 v49, v51\n\t"
    "global_store_dwordx4 %[co0], v[48:51], %[cb] sc1\n\t"
    "v_cmp_u_f32 vcc, v52, v60\n\t"
    "s_waitcnt lgkmcnt(0)\n\t"
    "s_or_b32 %[bad], vcc_lo, vcc_hi\n\t"
    "v_readfirstlane_b32 %[pk], v39"
    : [bad] "=&s"(bad), [pk] "=&s"(pk)
    : [rw] "v"(rw), "{v[80:95]}"(pd), [fa] "v"(fa), [fv] "v"(fv), [cb] "s"(cb), [co0] "v"(co0), [nl] "s"(-LOG2E), [pa] "s"(pa)
    : "v39", "v40", "v41", "v42", "v43", "v44", "v45", "v46", "v47", "v48", "v49", "v50", "v51", "v52", "v53", "v54", "v55", "v56", "v57", "v58", "v59", "v60", "v61", "v62", "v63", "vcc", "scc", "memory");
}
__device__ __forceinline__ void fin_st2(const u32x16& pd, unsigned rw, unsigned fa, unsigned fv, const char* cb, unsigned co0, unsigned co1, unsigned pa, unsigned& bad, unsigned& pk) {
  asm volatile("s_nop 1\n\t"
    "v_cvt_pk_bf16_f32 v40, v80, v81\n\t"
    "v_cvt_pk_bf16_f32 v41, v82, v83\n\t"
    "v_cvt_pk_bf16_f32 v42, v84, v85\n\t"
    "v_cvt_pk_bf16_f32 v43, v86, v87\n\t"
    "global_store_dwordx4 %[co0], v[40:43], %[cb] sc1\n\t"
    "v_cvt_pk_bf16_f32 v44, v88, v89\n\t"
    "v_cvt_pk_bf16_f32 v45, v90, v91\n\t"
    "v_cvt_pk_bf16_f32 v46, v92, v93\n\t"
    "v_cvt_pk_bf16_f32 v47, v94, v95\n\t"
    "global_store_dwordx4 %[co1], v[44:47], %[cb] sc1\n\t"
    "1:\n\t"
    "ds_read_b32 v39, %[fa]\n\t"
    "ds_read_b128 v[48:51], %[rw] offset:0\n\t"
    "ds_read_b128 v[52:55], %[rw] offset:1024\n\t"
    "ds_read_b128 v[56:59], %[rw] offset:2048\n\t"
    "ds_read_b128 v[60:63], %[rw] offset:3072\n\t"
    "s_waitcnt lgkmcnt(2)\n\t"
    "v_cmp_ne_u32 vcc, v39, %[fv]\n\t"
    "s_cbranch_vccz 2f\n\t"
    "s_waitcnt lgkmcnt(0)\n\t"
    "s_branch 1b\n\t"
    "2:\n\t"
    "v_cvt_pk_bf16_f32 v48, v48, v49\n\t"
    "v_cvt_pk_bf16_f32 v49, v50, v51\n\t"
    "v_cvt_pk_bf16_f32 v50, v52, v53\n\t"
    "v_cvt_pk_bf16_f32 v51, v54, v55\n\t"
    "global_store_dwordx4 %[co0], v[48:51], %[cb] offset:2048 sc1\n\t"
    "s_waitcnt lgkmcnt(0)\n\t"
    "v_mov_b32 v39, %[pa]\n\t"
    "ds_read_b32 v39, v39\n\t"
    "v_cvt_pk_bf16_f32 v56, v56, v57\n\t"
    "v_cvt_pk_bf16_f32 v57, v58, v59\n\t"
    "v_cvt_pk_bf16_f32 v58, v60, v61\n\t"
    "v_cvt_pk_bf16_f32 v59, v62, v63\n\t"
    "global_store_dwordx4 %[co1], v[56:59], %[cb] offset:2048 sc1\n\t"
    "v_cmp_u_f32 vcc, v80, v88\n\t"
    "s_waitcnt lgkmcnt(0)\n\t"
    "s_or_b32 %[bad], vcc_lo, vcc_hi\n\t"
    "v_readfirstlane_b32 %[pk], v39"
    : [bad] "=&s"(bad), [pk] "=&s"(pk)
    : [rw] "v"(rw), "{v[80:95]}"(pd), [fa] "v"(fa), [fv] "v"(fv), [cb] "s"(cb), [co0] "v"(co0), [co1] "v"(co1), [pa] "s"(pa)
    : "v39", "v40", "v41", "v42", "v43", "v44", "v45", "v46", "v47", "v48", "v49", "v50", "v51", "v52", "v53", "v54", "v55", "v56", "v57", "v58", "v59", "v60", "v61", "v62", "v63", "vcc", "scc", "memory");
}
template <int R> __device__ __forceinline__ void gw_load(u32x32& w, unsigned a0, unsigned a1);
template <> __device__ __forceinline__ void gw_load<0>(u32x32& w, unsigned a0, unsigned a1) {
  asm volatile("ds_read_b64_tr_b16 v[128:129], %1 offset:0\n\t"
    "ds_read_b64_tr_b16 v[130:131], %1 offset:512\n\t"
    "ds_read_b64_tr_b16 v[132:133], %2 offset:0\n\t"
    "ds_read_b64_tr_b16 v[134:135], %2 offset:512\n\t"
    "ds_read_b64_tr_b16 v[136:137], %1 offset:4096\n\t"
    "ds_read_b64_tr_b16 v[138:139], %1 offset:4608\n\t"
    "ds_read_b64_tr_b16 v[140:141], %2 offset:4096\n\t"
    "ds_read_b64_tr_b16 v[142:143], %2 offset:4608\n\t"
    "s_waitcnt lgkmcnt(0)\n\t"
    "ds_read_b64_tr_b16 v[144:145], %1 offset:16384\n\t"
    "ds_read_b64_tr_b16 v[146:147], %1 offset:16896\n\t"
    "ds_read_b64_tr_b16 v[148:149], %2 offset:16384\n\t"
    "ds_read_b64_tr_b16 v[150:151], %2 offset:16896\n\t"
    "ds_read_b64_tr_b16 v[152:153], %1 offset:20480\n\t"
    "ds_read_b64_tr_b16 v[154:155], %1 offset:20992\n\t"
    "ds_read_b64_tr_b16 v[156:157], %2 offset:20480\n\t"
    "ds_read_b64_tr_b16 v[158:159], %2 offset:20992\n\t"
    "s_waitcnt lgkmcnt(0)"
    : "=&{v[128:159]}"(w) : "v"(a0), "v"(a1) : "memory");
}
template <> __device__ __forceinline__ void gw_load<1>(u32x32& w, unsigned a0, unsigned a1) {
  asm volatile("ds_read_b64_tr_b16 v[160:161], %1 offset:0\n\t"
    "ds_read_b64_tr_b16 v[162:163], %1 offset:512\n\t"
    "ds_read_b64_tr_b16 v[164:165], %2 offset:0\n\t"
    "ds_read_b64_tr_b16 v[166:167], %2 offset:512\n\t"
    "ds_read_b64_tr_b16 v[168:169], %1 offset:4096\n\t"
    "ds_read_b64_tr_b16 v[170:171], %1 offset:4608\n\t"
    "ds_read_b64_tr_b16 v[172:173], %2 offset:4096\n\t"
    "ds_read_b64_tr_b16 v[174:175], %2 offset:4608\n\t"
    "s_waitcnt lgkmcnt(0)\n\t"
    "ds_read_b64_tr_b16 v[176:177], %1 offset:16384\n\t"
    "ds_read_b64_tr_b16 v[178:179], %1 offset:16896\n\t"
    "ds_read_b64_tr_b16 v[180:181], %2 offset:16384\n\t"
    "ds_read_b64_tr_b16 v[182:183], %2 offset:16896\n\t"
    "ds_read_b64_tr_b16 v[184:185], %1 offset:20480\n\t"
    "ds_read_b64_tr_b16 v[186:187], %1 offset:20992\n\t"
    "ds_read_b64_tr_b16 v[188:189], %2 offset:20480\n\t"
    "ds_read_b64_tr_b16 v[190:191], %2 offset:20992\n\t"
    "s_waitcnt lgkmcnt(0)"
    : "=&{v[160:191]}"(w) : "v"(a0), "v"(a1) : "memory");
}
template <> __device__ __forceinline__ void gw_load<2>(u32x32& w, unsigned a0, unsigned a1) {
  asm volatile("ds_read_b64_tr_b16 v[192:193], %1 offset:0\n\t"
    "ds_read_b64_tr_b16 v[194:195], %1 offset:512\n\t"
    "ds_read_b64_tr_b16 v[196:197], %2 offset:0\n\t"
    "ds_read_b64_tr_b16 v[198:199], %2 offset:512\n\t"
    "ds_read_b64_tr_b16 v[200:201], %1 offset:4096\n\t"
    "ds_read_b64_tr_b16 v[202:203], %1 offset:4608\n\t"
    "ds_read_b64_tr_b16 v[204:205], %2 offset:4096\n\t"
    "ds_read_b64_tr_b16 v[206:207], %2 offset:4608\n\t"
    "s_waitcnt lgkmcnt(0)\n\t"
    "ds_read_b64_tr_b16 v[208:209], %1 offset:16384\n\t"
    "ds_read_b64_tr_b16 v[210:211], %1 offset:16896\n\t"
    "ds_read_b64_tr_b16 v[212:213], %2 offset:16384\n\t"
    "ds_read_b64_tr_b16 v[214:215], %2 offset:16896\n\t"
    "ds_read_b64_tr_b16 v[216:217], %1 offset:20480\n\t"
    "ds_read_b64_tr_b16 v[218:219], %1 offset:20992\n\t"
    "ds_read_b64_tr_b16 v[220:221], %2 offset:20480\n\t"
    "ds_read_b64_tr_b16 v[222:223], %2 offset:20992\n\t"
    "s_waitcnt lgkmcnt(0)"
    : "=&{v[192:223]}"(w) : "v"(a0), "v"(a1) : "memory");
}
template <> __device__ __forceinline__ void gw_load<3>(u32x32& w, unsigned a0, unsigned a1) {
  asm volatile("ds_read_b64_tr_b16 v[224:225], %1 offset:0\n\t"
    "ds_read_b64_tr_b16 v[226:227], %1 offset:512\n\t"
    "ds_read_b64_tr_b16 v[228:229], %2 offset:0\n\t"
    "ds_read_b64_tr_b16 v[230:231], %2 offset:512\n\t"
    "ds_read_b64_tr_b16 v[232:233], %1 offset:4096\n\t"
    "ds_read_b64_tr_b16 v[234:235], %1 offset:4608\n\t"
    "ds_read_b64_tr_b16 v[236:237], %2 offset:4096\n\t"
    "ds_read_b64_tr_b16 v[238:239], %2 offset:4608\n\t"
    "s_waitcnt lgkmcnt(0)\n\t"
    "ds_read_b64_tr_b16 v[240:241], %1 offset:16384\n\t"
    "ds_read_b64_tr_b16 v[242:243], %1 offset:16896\n\t"
    "ds_read_b64_tr_b16 v[244:245], %2 offset:16384\n\t"
    "ds_read_b64_tr_b16 v[246:247], %2 offset:16896\n\t"
    "ds_read_b64_tr_b16 v[248:249], %1 offset:20480\n\t"
    "ds_read_b64_tr_b16 v[250:251], %1 offset:20992\n\t"
    "ds_read_b64_tr_b16 v[252:253], %2 offset:20480\n\t"
    "ds_read_b64_tr_b16 v[254:255], %2 offset:20992\n\t"
    "s_waitcnt lgkmcnt(0)"
    : "=&{v[224:255]}"(w) : "v"(a0), "v"(a1) : "memory");
}
template <int I, int E, class F> __device__ __forceinline__ void sfor(F&& f) { if constexpr (I < E) { f(std::integral_constant<int, I>{}); sfor<I + 1, E>(f); } }
#define W2C 0
#define FL63 0
template <int ROLE, int KQ, class F>
__device__ __forceinline__ void wload(char* smem, int wave_id_, const u16* W0, const u16* W1, int ldb, u32x32 (&bw)[4], int rot, F&& hook) {
  constexpr int ABUF = 65536;
  const int tid = opaque_tid(), lane = tid & 63, wave = WAVE_ID;
  const int kq = KQ == 2 ? wave >> 2 : 0, nq = KQ == 2 ? wave & 3 : wave;
  const int g = lane >> 4, r16 = lane & 15, q4 = r16 >> 2, p4 = lane & 3;
  const unsigned ldsb = __builtin_amdgcn_readfirstlane((unsigned)(size_t)(LAS char*)smem);
  {
    const int srow = 8 * wave + (lane >> 3), ch = (lane & 7) ^ bswz(srow);
    const unsigned voff = (unsigned)(srow * ldb + ch * 8) * 2;
    const char* w0 = uni((const char*)W0); const char* w1 = uni((const char*)(ROLE == R_F1 ? W1 : W0 + 64));
    const size_t kb = (size_t)64 * ldb * 2;
    auto wstage = [&](int r) {
      const unsigned ld = ldsb + (r & 1) * ABUF + wave * 1024;
#pragma unroll
      for (int i = 0; i < 8; ++i) {
        const int kbi = KQ == 2 ? (ROLE == R_PART2 ? 0 : (i >> 2) * 8) + ((2 * r + ((i >> 1) & 1)) ^ rot) : 2 * r + (i >> 2);
        const char* src = KQ == 2 ? ((i & 1) ? w1 : w0) + kbi * kb + (ROLE == R_PART2 ? (i >> 2) * 256 : 0) : w0 + kbi * kb + (i & 3) * 128;
        unsigned keep;
        if (W2C && (ROLE == R_PART || ROLE == R_PART2)) glds16s<0>(src, voff, ld + i * 8192);
        else
        asm volatile("s_mov_b32 %0, m0\n\ts_mov_b32 m0, %3\n\ts_nop 2\n\tglobal_load_lds_dwordx4 %1, %2" WNT_ "\n\ts_mov_b32 m0, %0"
                     : "=&s"(keep) : "v"(voff), "s"(src), "s"(ld + i * 8192) : "memory");
      }
    };
    const int brow = 8 * g + q4, bs = bswz(brow);
    unsigned la[2];
#pragma unroll
    for (int j = 0; j < 2; ++j) {
      int blk, c16, sub;
      if (ROLE == R_F1) { blk = j; c16 = nq * 2 + (p4 >> 1); sub = (p4 & 1) * 8; }
      else { blk = nq >> 1; c16 = (nq & 1) * 4 + p4; sub = j * 8; }
      int set = kq;
      if (ROLE == R_PART2 && TLP) { set = nq >> 1; blk = nq & 1; c16 = kq * 4 + p4; }
      la[j] = (unsigned)((KQ == 2 ? set * 4 + blk : blk) * 8192 + brow * 128 + ((c16 ^ bs) << 4) + sub);
    }
    hook(4);
    wstage(0); wstage(1);
    sfor<0, 4>([&](auto r_c) __attribute__((always_inline)) {
      constexpr int r = decltype(r_c)::value;
      if (r < 3) WAIT_V(8); else WAIT_V(0);
      __builtin_amdgcn_s_barrier(); asm volatile("" ::: "memory");
      gw_load<r>(bw[r], ldsb + (r & 1) * ABUF + la[0], ldsb + (r & 1) * ABUF + la[1]);
      BAR();
      if (r + 2 < 4) wstage(r + 2);
      hook(r);
    });
  }
}
#define DSR2(d0, d1, a0_, a1_, off) asm volatile("ds_read_b128 %0, %2 offset:%4\n\tds_read_b128 %1, %3 offset:%4" : "=&v"(d0), "=&v"(d1) : "v"(a0_), "v"(a1_), "i"(off))
template <int N> __device__ __forceinline__ void waitl(u32x4& a, u32x4& b) { asm volatile("s_waitcnt lgkmcnt(%2)" : "+v"(a), "+v"(b) : "i"(N)); }
__device__ __forceinline__ void glds1(const char* sbase, unsigned voff, unsigned lds) {
  unsigned keep;
  asm volatile("s_mov_b32 %0, m0\n\ts_mov_b32 m0, %3\n\ts_nop 2\n\tglobal_load_lds_dwordx4 %1, %2 sc1\n\ts_mov_b32 m0, %0"
               : "=&s"(keep) : "v"(voff), "s"(sbase), "s"(lds) : "memory");
}
#define XS(i) do { } while (0)
#define XFL(b_) do { } while (0)
#define XA_ 0
#define XF_ 0
#define XE_ 0
#define XP_ 0
#define PUBK 9
#define LATE 0
#define NOWP 0
#define TB 48
#define TDM 1
#define TDA 15
#define TPUB 1
#define DUR0 160
#define PUP 4
#define PDQ 5
#define PDM 2
#define PDEEP 5
template <int ROLE>
__device__ __forceinline__ void gemm7(char* smem, int wave_id_, const u16* W0, const u16* W1, int ldb, const char* A_, size_t a_blk, Dep dep,
                                      char* C_, size_t c_blk, unsigned*& cnt, int myw, unsigned* tmo, int slot, unsigned long long* mts,
                                      bool gl, int nimm = 0, int nseg = NSEG, int wm0 = 0, int pofs = 0, u64 tag = 0, int b0 = 0) {
  dep.base = (const char*)cnt;
  unsigned* myprog = cnt + myw;
  volatile LAS unsigned* wsl = (volatile LAS unsigned*)(smem + L_SLOT + 512);
  if (opaque_tid() < 144) *(volatile LAS unsigned*)(smem + L_SLOT + opaque_tid() * 4) = 0u;
  const int tid = opaque_tid(), lane = tid & 63, wave = WAVE_ID;
  const int kq = wave >> 2, nq = wave & 3;
  const unsigned ldsb = __builtin_amdgcn_readfirstlane((unsigned)(size_t)(LAS char*)smem);
  constexpr int rot = 0;
  const char* A = uni(A_) + nq * 1024;
  const char* Cu = uni(C_);
  constexpr int PD = PDM;
  constexpr bool P2C = ROLE == R_PART2;
  int pw = 0;
  int wm = wm0, nh = 2 * b0;
  constexpr bool SPEC = ROLE != R_QKV;
  const int sbi = gl ? SC : SB, sbo = ROLE == R_F1 || gl ? SC : SB;
  auto lgo = [&](int b, unsigned& v, unsigned& u, bool pre, bool force) {
    if (wave == 4) {
      for (int spins = 0; __builtin_amdgcn_readfirstlane(wsl[2]) != (unsigned)(b + 1); ++spins) {
        if (!pre) ld_fb(v, u, dep.base, dep.voff);
        const bool ok = (int)(v * dep.mul + dep.add) > b;
        if (__all(ok)) break;
        if (SPEC && !force && bells(ok, u, b, b0 ? MARH : MARG, sbi)) break;
        if (!pre) { if (gl) __builtin_amdgcn_s_sleep(GSL); else __builtin_amdgcn_s_sleep(XSL); }
        pre = false;
        if (give_up(spins, tmo, 9)) { v = 255u; break; }
      }
      wsl[2] = (unsigned)(b + 1);
    } else {
      while (__builtin_amdgcn_readfirstlane(wsl[2]) != (unsigned)(b + 1)) __builtin_amdgcn_s_sleep(1);
    }
  };
  auto lwm = [&](int b, unsigned v) {
    unsigned w;
    if (wave == 4) {
      const int w2 = (int)dep_eval(v, dep);
      w = (unsigned)min(max(max(w2, wm), b + 1), 255);
      wsl[0] = ((unsigned)(b + 1) << 8) | w;
    } else {
      for (;;) {
        w = __builtin_amdgcn_readfirstlane(wsl[0]);
        if ((w >> 8) == (unsigned)(b + 1)) break;
        __builtin_amdgcn_s_sleep(1);
      }
      w &= 255u;
    }
    return (int)w;
  };
  auto pubw = [&](int n) {
    WAIT_V(0);
    if (lane == 0) {
      const unsigned o = __hip_atomic_fetch_add((LAS unsigned*)(smem + L_SLOT + 516 + ((n + pofs) & 1) * 36), 1u, __ATOMIC_RELAXED, __HIP_MEMORY_SCOPE_WORKGROUP);
      if ((o & 3u) == 3u) {
        st4_sc1(myprog, (unsigned)(n + pofs));
      }
    }
    asm volatile("s_waitcnt lgkmcnt(0)" ::: "memory");
  };
  auto hsrc = [&](int hh) { return A + (size_t)(hh >> 1) * a_blk + ((hh ^ (rot >> 2)) & 1) * 16384; };
  auto hlds = [&](int hh) { return ldsb + (unsigned)(((hh >> 1) & 1) * 65536 + (hh & 1) * 16384 + nq * 1024); };
#define POFF(i_) (((i_) & 1) * 32768 + ((i_) >> 1) * 4096)
#define PIECE(s_, l_, i_) asm volatile("s_add_u32 m0, %2, %3\n\ts_nop 0\n\tglobal_load_lds_dwordx4 %0, %1 sc1" :: "v"(av[i_]), "s"(s_), "s"(l_), "i"(POFF(i_)) : "memory", "scc", "m0")
  int na = max(sbo, b0);
  auto arm = [&](int bb) {
    unsigned z = SENT; asm volatile("" : "+v"(z));
    constexpr int N = ROLE == R_F1 ? 1 : (P2C ? 4 : 2);
    const unsigned o = nq * N * 1024 + lane_id() * 16;
    const char* p = Cu + (size_t)bb * c_blk;
#pragma unroll
    for (int i = 0; i < N; ++i) st16s(p, o + i * 1024, u32x4{z, z, z, z});
  };
  u32x32 BW[4];
  wload<ROLE, 2>(smem, wave_id_, W0, W1, ldb, BW, rot, [&](int r) {
    if (ROLE == R_QKV && nimm > 0 && wave == 0) STS((char*)tmo - OFF_TMO, 128 + slot, 3 + r);
    if (SPEC && r == 2 && (ROLE != R_PART || gl)) { for (na += kq; na < nseg; na += 2) arm(na); na = nseg + 1; }
    if (ROLE == R_QKV && nimm > 0 && r == 0 && wave == 4) init_issue((char*)tmo - OFF_TMO, ldsb + L_RED);
    if (ROLE == R_QKV && nimm > 0 && r == 2) {
      cnt = cnt_of((char*)tmo - OFF_TMO, init_get((char*)tmo - OFF_TMO, tag, smem + L_RED));
      dep.base = (const char*)cnt; myprog = cnt + myw;
      if (kq) {
        unsigned v0 = 0, u0;
        lgo(0, v0, u0, false, true);
        wm = lwm(0, v0);
        const unsigned l16 = lane_id() * 16;
#pragma unroll
        for (int i = 0; i < 16; ++i) glds1(hsrc(i >> 3) + (i & 1) * 32768 + ((((i & 7) >> 1) ^ rot) & 3) * 4096, l16, hlds(i >> 3) + POFF(i & 7));
      }
      nh = 2;
    }
  });
  if (ROLE == R_QKV && nimm > 0 && !kq) wm = (int)(__builtin_amdgcn_readfirstlane(wsl[0]) & 255u);
  if (ROLE == R_QKV && nimm > 0 && opaque_tid() == 0) STS((char*)tmo - OFF_TMO, 128 + slot, 0);
  if (wave == 0) WFB();
  auto body = [&](auto ldr_c) __attribute__((always_inline)) {
    constexpr bool LDR = decltype(ldr_c)::value;
    if (XP_ == 0 ? !LDR : (XP_ == 1 ? LDR : false)) __builtin_amdgcn_s_setprio(1);
    unsigned av[8];
#pragma unroll
    for (int i = 0; i < 8; ++i) av[i] = lane * 16 + (P2C ? 0 : (i & 1) * 32768) + (((i >> 1) ^ rot) & 3) * 4096;
    auto issue_all = [&](int hh) {
      if (LDR) {
        const char* s_ = hsrc(hh); const unsigned l_ = hlds(hh);
#pragma unroll
        for (int i = 0; i < 8; ++i) if (!(P2C && (i & 1))) PIECE(s_, l_, i);
      }
    };
    int b = b0;
    bool pre = false;
    unsigned smu = 0, tk = 0;
    unsigned smp = 0, got = 0;
    while (b < nseg) {
      const bool fast = (wm < nseg ? wm : nseg) >= b + PD && b >= nimm && nh <= 2 * b + 2;
      if (SPEC && b >= TB && !fast) break;
      const bool pl = wm <= b || pre, cold = nh < 2 * b + 2, nh3 = nh == 2 * b + 3;
      if (!fast) {
        XS(0);
        if (LDR && wm <= b && !got) lgo(b, smp, smu, false, true);
        XS(1);
        if (got) nh += NEARLY / 8;
        { const int upto = nh3 ? 2 * b + 4 : 2 * b + 2; for (; nh < upto; ++nh) issue_all(nh); }
        if (LDR && pl) wm = lwm(b, smp);
        XS(2);
      }
      const int ln = lane_id(), g = ln >> 4, r16 = ln & 15;
      const unsigned e0 = ldsb + (unsigned)((P2C ? 0 : kq * 32768) + (b & 1) * 65536 + (gl ? (g >> 1) * 512 + r16 * 32 + (g & 1) * 16 : r16 * 128 + ((g ^ (r16 & 7)) << 4)));
      const unsigned d0 = e0 ^ (gl ? 1024u : 64u);
      const unsigned rw = ldsb + L_RED + nq * 4096 + ln * 16;
      const unsigned co0 = ROLE == R_F1 ? (unsigned)((g & 1) * 2048 + nq * 512 + r16 * 32 + (g >> 1) * 16)
                         : ((ROLE == R_QKV ? TLQ : TLP) ? (unsigned)(nq * (P2C ? 4096 : 2048) + r16 * 64 + g * 16)
                         : (ROLE == R_QKV ? (unsigned)(r16 * 256 + (((4 * nq + g) ^ r16) << 4)) : (unsigned)(r16 * PROW + (32 * nq + 8 * g) * 2)));
      const unsigned co1 = co0 + ((ROLE == R_QKV ? TLQ : TLP) ? 1024u : (ROLE == R_QKV ? 4096u : 16u * PROW));
      u32x16 pv_;
      bool pr2 = false;

      if (fast) {
        __builtin_amdgcn_s_barrier(); asm volatile("" ::: "memory");
        const int LA = (wm < nseg ? wm : nseg) >= b + (ROLE == R_QKV ? PDQ : PDEEP) ? 3 : 2;
        for (; nh < 2 * b + LA; ++nh) issue_all(nh);
        int lf = (wm < nseg ? wm : nseg) - (LA - 1), cp = nseg - (LA - 1);
        const unsigned sa = ldsb + L_SLOT + ln * 4, ad = dep.add - (unsigned)(LA - 1);
        const unsigned up = LA == 3 ? 1000u : (unsigned)PUP;
#define FARGS BW, pv_, e0, d0, b, lf, cp, up, sa, dep.mul, ad, rw, (unsigned)a_blk, hsrc(2 * b + LA), hsrc(2 * b + LA + 1), hlds(2 * b + LA), hlds(2 * b + LA + 1), av
        if constexpr (LDR) (P2C ? fast_ld2 : (ROLE == R_F1 ? fast_ld1 : fast_ld0))(FARGS, LA);
#undef FARGS
        else {
          const char* c0_ = uni((const char*)tmo - OFF_TMO + OFF_DUMP); const char* c1_ = Cu + (size_t)b * c_blk;
          const int sf = b + pofs;
          const char* pg = uni((const char*)myprog);
          (ROLE == R_F1 ? fast_st1 : (P2C ? fast_st2 : fast_st0))(BW, pv_, e0, d0, b, lf, cp, up, sa, dep.mul, ad, rw, (unsigned)c_blk, c0_, c1_, co0, co1, pofs, sf, (unsigned)wave,
                                                                   ldsb + L_SLOT, dep.voff, dep.base, pg);
        }
        wm = lf + LA - 1 > wm ? lf + LA - 1 : wm;
        nh = 2 * b + 2;
        --b;
        __builtin_amdgcn_s_barrier(); asm volatile("" ::: "memory");
      } else {
        if (LDR) { if (nh >= 2 * b + 4) { if (P2C) WAIT_V(14); else WAIT_V(28); } else { if (P2C) WAIT_V(6); else WAIT_V(12); } }
        BAR();
        XS(3);
        if (ROLE == R_QKV && nimm > 0 && b < 2 && opaque_tid() == 0) STS((char*)tmo - OFF_TMO, 128 + slot, 1 + b);
        if constexpr (!LDR) {
          const unsigned wv = wsl[0];
          unsigned st;
          lean_st0(BW, e0, d0, __builtin_amdgcn_readfirstlane(pw > 0 ? (cold ? 4u : 1u) : 0u), pv_, (unsigned)(pw + pofs), ldsb + L_SLOT + 516, uni((const char*)myprog), 0u, tk, st);
          if (pl) wm = (int)(__builtin_amdgcn_readfirstlane(wv) & 255u);
        }
        const bool dm = nh == 2 * b + 2 && wm > b + 1 && b + 1 < nseg;
        pr2 = b + 1 < nseg && wm < nseg;
        if constexpr (LDR) {
          const bool er = nh == 2 * b + 2 && !dm;
          const unsigned ps = pr2 && (wave == 4 || er) ? (unsigned)(b + 1) : 0u;
#define LARGS BW, e0, d0, (unsigned)(nh - 2 * b - 2), hlds(nh), hsrc(nh), hsrc(nh + 1), av, ps, dep.voff, dep.base, \
              rw, ldsb + L_SLOT + 528 + nq * 4, (unsigned)(b + 1), ldsb + L_SLOT + 520, dep.mul, dep.add, er ? b + 1 : 0x7FFFFFFF, 0x7FFFFFFF, smp, smu, got
          if (dm) (P2C ? lean_dm1 : lean_dm0)(LARGS); else (P2C ? lean_ld1 : lean_ld0)(LARGS);
#undef LARGS
          got = __builtin_amdgcn_readfirstlane(got);
        }
        if (dm) nh += 2;
        XS(5);
      }
      unsigned bad = 0, pk = 0;
      if constexpr (LDR) { if (fast) fin_ld(pv_, rw, ldsb + L_SLOT + 528 + nq * 4, (unsigned)(b + 1)); }
      else (ROLE == R_F1 ? fin_st1 : (P2C ? fin_st2 : fin_st0))(pv_, rw, ldsb + L_SLOT + 528 + nq * 4, (unsigned)(b + 1), Cu + (size_t)b * c_blk, co0, co1, ldsb + L_SLOT + 524, bad, pk);
      XS(6);
      pre = pr2;
      bool now = (nh < 2 * b + 4 && wm < b + 1 + PD) || b + 1 >= nseg || b < nimm;
      if (!LDR && now) {
        if (NOWP == 0 && b >= nimm && pk == (unsigned)(b + 2)) now = false;
        else pubw(b + 1);
      }
      XS(7);
      XFL(b);
      pw = now ? 0 : b + 1;
      if (SPEC && !LDR && !fast && b >= 16 && na < nseg && na > b + 1) { arm(na); arm(na + 1); na += 2; }
      ++b;
    }
    if (SPEC && b < nseg) {
      static_assert(!SPEC || (NEARLY == 16 && TLP), "");
      const int ln = lane_id(), g = ln >> 4, r16 = ln & 15;
      const unsigned e0 = ldsb + (unsigned)((P2C ? 0 : kq * 32768) + (gl ? (g >> 1) * 512 + r16 * 32 + (g & 1) * 16 : r16 * 128 + ((g ^ (r16 & 7)) << 4)));
      const unsigned rw = ldsb + L_RED + nq * 4096 + ln * 16;
      const unsigned co0 = ROLE == R_F1 ? (unsigned)((g & 1) * 2048 + nq * 512 + r16 * 32 + (g >> 1) * 16) : (unsigned)(nq * (P2C ? 4096 : 2048) + r16 * 64 + g * 16);
      const char* pg = uni((const char*)myprog);
      unsigned fa = ldsb + L_SLOT + 528 + nq * 4; asm volatile("" : "+v"(fa));
      bool force = false, dmx = false;
      unsigned dur = DUR0, dp = 0, x8 = __builtin_amdgcn_readfirstlane(wave == 0 ? 8u : 0u);
      if (nh >= 2 * b + 2) got = 1;
      pre = false;
      for (;;) {
        unsigned rd;
        XS(0);
        if (LDR && b < nseg) {
          if (!got) { lgo(b, smp, smu, pre, force); issue_all(2 * b); issue_all(2 * b + 1); }
          XS(2);
          if (P2C) WAIT_V(6); else WAIT_V(12);
        }
        if (!LDR && b >= sbo && na <= nseg) { for (na = max(na, b); na < nseg; ++na) arm(na); WAIT_V(0); ++na; }
        const unsigned ea = e0 + (b & 1) * 65536;
        BAR();
        XS(3);
        if (b >= nseg) rd = __builtin_amdgcn_readfirstlane(wsl[8]);
        else if constexpr (LDR) {
          const int th = b + 1 < nseg ? b + 1 : 0;
#define LARGS BW, ea, ea ^ (gl ? 1024u : 64u), 0u, hlds(2 * b + 2), hsrc(2 * b + 2), hsrc(2 * b + 3), av, (unsigned)th, dep.voff, dep.base, \
              rw, ldsb + L_SLOT + 528 + nq * 4, (unsigned)(b + 1), ldsb + L_SLOT + 520, dep.mul, dep.add, th, th >= sbi ? th : 0x7FFFFFFF, smp, smu, got
          if (dmx) {
            *(volatile LAS u32x2*)(wsl + 2) = u32x2{(unsigned)(b + 2), (unsigned)(b + 2)};
            (P2C ? lean_dm1 : lean_dm0)(LARGS);
          } else (P2C ? lean_ld1 : lean_ld0)(LARGS);
#undef LARGS
          got = __builtin_amdgcn_readfirstlane(got) | (dmx ? 1u : 0u);
          rd = got >> 1;
          dmx = TDM && got == 1u && b + 2 < nseg
             && __all((int)(smp * dep.mul + dep.add) > b + 2 || (b + 2 >= sbi && (int)(smu >> 24) + ((int)((rt_now() + (dmx ? TDA : 0) - MARG - smu) << 8) >> 31) > b + 2));
          pre = true;
        } else {
          unsigned bad, pk;
          (ROLE == R_F1 ? lean_ep1 : (P2C ? lean_ep2 : lean_ep0))(BW, ea, ea ^ (gl ? 1024u : 64u), (unsigned)min(pw, 1) | (b >= sbo - 2 ? x8 : 0u), (unsigned)pw, ldsb + L_SLOT + 516, pg,
              (unsigned)(b + 1) << 24 | dur, tk, rd, rw, fa, (unsigned)(b + 1), Cu + (size_t)b * c_blk, co0, co0 + 1024u, bad, pk);
          XS(6);
          if (!rd) {
            pw = b + 1;
            { const unsigned d = min(tk, 300u); dur = max(d, dp); dp = d; }
            if (bad && !force && b >= sbi) wsl[8] = 1u;
            else if ((ROLE == R_PART && !gl && b == sbo - 1) || pk != (unsigned)(b + 2)) {
              pubw(b + 1);
              pw = 0;
            }
            if (na < nseg && na > b + 1) { arm(na); arm(na + 1); na += 2; }
          }
        }
        XS(7);
        if (rd) {
          WAIT_V(0);
          BAR();
          if (wave == 4 && ln < 11) wsl[ln] = 0u;
          BAR();
          --b; pw = 0; pre = false; got = 0; force = true;
          continue;
        }
        if (b >= nseg) break;
        force = false;
        ++b;
      }
    }
    __builtin_amdgcn_s_setprio(0);
  };
  if (kq) body(std::true_type{}); else body(std::false_type{});
#undef PIECE
#undef POFF
  WAIT_V(0);
  BAR();
}
__device__ __forceinline__ void fin_role(char* smem, int wave_id_, int fidx, const float* mod, const char* P2, const u16* x1, u16* out, unsigned* cnt, unsigned* tmo,
                                         unsigned long long* mts) {
  const int tid = opaque_tid(), lane = tid & 63, wave = WAVE_ID;
  row_table(smem + L_GB, mod, cnt, tmo, tid);
  volatile LAS unsigned* wsl = (volatile LAS unsigned*)(smem + L_SLOT + 512);
  int wf = 0, par = 0;
  const unsigned pf2 = (unsigned)(PF2 + ((lane & 31) < 28 ? (lane & 31) : 0)) * 128, pln = (unsigned)((PLN + (lane >> 3)) * 128 + (lane & 7) * 4);
  for (int nf = fidx >> 3; nf < NSEG; nf += !FL63 || nf < NSEG - 3 ? 2 : (nf == NSEG - 2 ? 1 : 3)) {
    if (wf <= nf) {
      if (wave == 0) {
        unsigned w;
        for (int spins = 0;; ++spins) {
          unsigned v, u;
          ld_fb(v, u, cnt, pf2 + (nf >= HC ? 4 : (nf >= HB ? 8 : 0)));
          if (!__all(v > (unsigned)nf) && bells(v > (unsigned)nf, u, nf, nf >= HB ? MARX : MARF, SC)) { w = 0; break; }
          v = rowmin16(v);
          w = min(rdl(v, 0), rdl(v, 16));
          if ((int)w > nf) break;
          __builtin_amdgcn_s_sleep(FSL);
          if (give_up(spins, tmo, 9)) { w = NSEG; break; }
        }
        if (lane == 0) wsl[par] = w;
      }
      BAR();
      wf = (int)__builtin_amdgcn_readfirstlane(wsl[par]);
      par ^= 1;
    }
    int ln = lane; asm volatile("" : "+v"(ln));
    if (wave == 0) WTS(256 + 8 + fidx, nf / 2, 0);
    bool fin = nf < SB;
    for (int spins = 0; !fin_half(fin, P2, x1, smem + L_GB + 12288, out, nf * 32 + 4 * (fidx & 7) + (wave >> 1), wave & 1, ln); ++spins)
      if (spins > 1) fin = __all(ld4_sc1((const char*)cnt + pf2 + (nf >= HC ? 4 : (nf >= HB ? 8 : 0))) > (unsigned)nf && ld4_sc1((const char*)cnt + pln) > (unsigned)nf) || give_up(spins, tmo, 9);
    if (wave == 0) WTS(256 + 8 + fidx, nf / 2, 2);
  }
}
#define LD8(r, vo, sb) asm volatile("global_load_dwordx4 %0, %8, %12 sc1\n\tglobal_load_dwordx4 %1, %9, %12 sc1\n\t" \
    "global_load_dwordx4 %2, %10, %12 sc1\n\tglobal_load_dwordx4 %3, %11, %12 sc1\n\t" \
    "global_load_dwordx4 %4, %8, %13 sc1\n\tglobal_load_dwordx4 %5, %9, %13 sc1\n\t" \
    "global_load_dwordx4 %6, %10, %13 sc1\n\tglobal_load_dwordx4 %7, %11, %13 sc1" \
    : "=&v"(r[0][0]), "=&v"(r[0][1]), "=&v"(r[0][2]), "=&v"(r[0][3]), "=&v"(r[1][0]), "=&v"(r[1][1]), "=&v"(r[1][2]), "=&v"(r[1][3]) \
    : "v"(vo), "v"(vo ^ (TLQ ? 2048u : 64u)), "v"(vo ^ (TLQ ? 4096u : 128u)), "v"(vo ^ (TLQ ? 6144u : 192u)), "s"(sb), "s"((sb) + (TLQ ? 1024 : 4096)) : "memory")
#define TIE8(r) "+v"(r[0][0]), "+v"(r[0][1]), "+v"(r[0][2]), "+v"(r[0][3]), "+v"(r[1][0]), "+v"(r[1][1]), "+v"(r[1][2]), "+v"(r[1][3])
#define SADL 255
#define SAE 110
#define DSA 20
#define SASL 8
__device__ __forceinline__ void sa_role(char* smem, int wave_id_, int hbase, const char* __restrict__ Q, const char* __restrict__ Kx, const char* __restrict__ V,
                                        const char* kvm, char* __restrict__ O, unsigned* cnt, unsigned* tmo, int slot, unsigned long long* mts) {
  constexpr float LS = LOG2E * SCALE;
  const int tid = opaque_tid(), wave = WAVE_ID;
  const int h = hbase + (wave & 1), s0 = wave >> 1;
  char* vl = smem + wave * 17408;
  const unsigned ldsw = __builtin_amdgcn_readfirstlane((unsigned)(size_t)(LAS char*)vl);
  unsigned avail = 0;
  {
    static_assert(SB % 4 == 0, "");
    unsigned z = SENT; asm volatile("" : "+v"(z));
    for (int s = SB + s0; s < NSEG; s += 4)
#pragma unroll
      for (int i = 0; i < 8; ++i) st16s(uni(O + ((size_t)s * 64 + 2 * h) * 4096), (unsigned)(i * 1024 + (tid & 63) * 16), u32x4{z, z, z, z});
  }
  for (int s = s0, it = 1; s < NSEG; s += 4, ++it) {
    u32x4 qv[2][4], kv[2][4];
    int lane = tid & 63; asm volatile("" : "+v"(lane));
    const int g = lane >> 4, r16 = lane & 15, q4 = r16 >> 2, p4 = lane & 3;
    auto request = [&](int sr) {
      const size_t o = ((size_t)sr * NH + h) * 8192;
      const char* qs = uni(Q + o); const char* ks = uni(Kx + o); const char* vs = uni(V + o);
      const unsigned fo = TLQ ? (unsigned)(r16 * 64 + g * 16) : (unsigned)(r16 * 256 + ((g ^ r16) << 4));
      LD8(qv, fo, qs); LD8(kv, fo, ks);
      head_to_lds(vs, lane, ldsw);
    };
    {
      for (int spins = 0; avail <= (unsigned)s; ++spins) {
        const unsigned p = ld4_sc1(cnt + (PQKV + h) * 32 + (lane & 15));
        avail = min(min(qkv_avail(p, 0), qkv_avail(p, 1)), qkv_avail(p, 2));
        if (avail > (unsigned)s) break;
        __builtin_amdgcn_s_sleep(32);
        if (give_up(spins, tmo, 10)) break;
      }
      request(s);
      asm volatile("s_waitcnt vmcnt(0)" : TIE8(qv), TIE8(kv) :: "memory");
    }
    WTM(slot, s, 0);
    f32x4 s00 = f32x4{0, 0, 0, 0}, s01 = s00, s11 = s00;
#pragma unroll
    for (int ks = 0; ks < 4; ++ks) {
      s00 = MFMA32(asbf8(kv[0][ks]), asbf8(qv[0][ks]), s00);
      s01 = MFMA32(asbf8(kv[0][ks]), asbf8(qv[1][ks]), s01);
      s11 = MFMA32(asbf8(kv[1][ks]), asbf8(qv[1][ks]), s11);
    }
    float mx[2], sum[2];
    u32x4 pseg[2];
    {
#pragma unroll
      for (int r = 0; r < 4; ++r) { const bool ok = 4 * g + r <= r16; s00[r] = ok ? s00[r] : -3.0e38f; s11[r] = ok ? s11[r] : -3.0e38f; }
      mx[0] = red16_32_max(max3(max3(s00[0], s00[1], s00[2]), s00[3], -1.0e38f));
      mx[1] = red16_32_max(max3(max3(fmaxf(fmaxf(s01[0], s01[1]), fmaxf(s01[2], s01[3])), s11[0], -1.0e38f), max3(s11[1], s11[2], s11[3]), -1.0e38f));
      const float n0 = -mx[0] * LS, n1 = -mx[1] * LS;
#pragma unroll
      for (int r = 0; r < 4; ++r) {
        s00[r] = __builtin_amdgcn_exp2f(s00[r] * LS + n0); s01[r] = __builtin_amdgcn_exp2f(s01[r] * LS + n1); s11[r] = __builtin_amdgcn_exp2f(s11[r] * LS + n1);
      }
      sum[0] = (s00[0] + s00[1]) + (s00[2] + s00[3]);
      sum[1] = ((s01[0] + s01[1]) + (s01[2] + s01[3])) + ((s11[0] + s11[1]) + (s11[2] + s11[3]));
      pseg[0] = u32x4{pack2(s00[0], s00[1]), pack2(s00[2], s00[3]), 0u, 0u};
      pseg[1] = u32x4{pack2(s01[0], s01[1]), pack2(s01[2], s01[3]), pack2(s11[0], s11[1]), pack2(s11[2], s11[3])};
    }
    f32x4 o[8][2];
#pragma unroll
    for (int dt = 0; dt < 8; ++dt) {
      const int row0 = 4 * g + q4;
      const unsigned xo = (unsigned)(row0 * 256 + (((4 * (dt >> 1) + p4) ^ row0) << 4) + (dt & 1) * 8);
      const u32x2 lo = trread(vl, xo), hi = trread(vl, xo + 4096);
      o[dt][0] = MFMA32(asbf8(u32x4{lo.x, lo.y, 0u, 0u}), asbf8(pseg[0]), (f32x4{0, 0, 0, 0}));
      o[dt][1] = MFMA32(asbf8(u32x4{lo.x, lo.y, hi.x, hi.y}), asbf8(pseg[1]), (f32x4{0, 0, 0, 0}));
    }
    float n0[2]; f32x4 sm[2];
#pragma unroll
    for (int b = 0; b < 2; ++b) { const float t = red16_32_add(sum[b]); sm[b] = f32x4{t, t, t, t}; n0[b] = -mx[b] * LS; }
    unsigned oa = (unsigned)(r16 * 128 + ((g ^ (r16 & 7)) << 4)), ka = (unsigned)(g * 256 + (r16 & 7) * 16), va = (unsigned)lane * 16;
    unsigned one = g < 2 ? 0x3F803F80u : 0u;
    u32x4 kmv[4], vmv[4];
#pragma unroll
    for (int i = 0; i < 4; ++i) vmv[i] = u32x4{0u, 0u, 0u, 0u};
#pragma unroll
    for (int dt = 0; dt < 8; ++dt) asm volatile("" : "+v"(o[dt][0]), "+v"(o[dt][1]), "+v"(vmv[dt >> 1]));
    asm volatile("s_waitcnt lgkmcnt(0)" : "+v"(sm[0]), "+v"(sm[1]), "+v"(n0[0]), "+v"(n0[1]), "+v"(oa), "+v"(ka), "+v"(va), "+v"(one) :: "memory");
    unsigned pg;
    for (int spins = 0;; ++spins) {
      pg = ld4_sc1(cnt + (PROG + h) * 32 + ((lane & 4) ? 5 : (lane & 3)));
      if (__all((pg >> 24) + (s >= SDIR ? 1u : 0u) > (unsigned)s)) break;
      __builtin_amdgcn_s_sleep(SASL);
      if (give_up(spins, tmo, 6)) break;
    }
    if (__all((pg >> 24) == (unsigned)s)) wait_rt(latest(pg), s == NSEG - 1 ? SADL - LAST : SADL);
    const char* mk = uni(kvm + ((size_t)s * NH + h) * 4096);
    for (int spins = 0;; ++spins) {
      asm volatile("global_load_dwordx4 %0, %8, %10 sc1\n\tglobal_load_dwordx4 %1, %8, %10 offset:128 sc1\n\t"
                   "global_load_dwordx4 %2, %8, %10 offset:1024 sc1\n\tglobal_load_dwordx4 %3, %8, %10 offset:1152 sc1\n\ts_mov_b32 exec_hi, 0\n\t"
                   "global_load_dwordx4 %4, %9, %10 offset:2048 sc1\n\tglobal_load_dwordx4 %5, %9, %10 offset:2560 sc1\n\t"
                   "global_load_dwordx4 %6, %9, %10 offset:3072 sc1\n\tglobal_load_dwordx4 %7, %9, %10 offset:3584 sc1\n\ts_mov_b32 exec_hi, -1\n\ts_waitcnt vmcnt(0)"
                   : "=&v"(kmv[0]), "=&v"(kmv[1]), "=&v"(kmv[2]), "=&v"(kmv[3]), "+v"(vmv[0]), "+v"(vmv[1]), "+v"(vmv[2]), "+v"(vmv[3])
                   : "v"(ka), "v"(va), "s"(mk) : "memory");
      unsigned mx_ = 0;
#pragma unroll
      for (int i = 0; i < 4; ++i) mx_ = max(max(max(max(mx_, kmv[i].x), kmv[i].y), max(kmv[i].z, kmv[i].w)), max(max(vmv[i].x, vmv[i].y), max(vmv[i].z, vmv[i].w)));
      if (__all(mx_ != SENT)) break;
      __builtin_amdgcn_s_sleep(2);
      if (give_up(spins, tmo, 6)) break;
    }
    if (SAE && s >= SB && lane == 0) st4_sc1(cnt + (PSA + (hbase >> 1)) * 32 + 16 + wave, bell(s, rt_now() + SAE));
    WTM(slot, s, 1);
    f32x4 tt[2] = {f32x4{0, 0, 0, 0}, f32x4{0, 0, 0, 0}};
#pragma unroll
    for (int ks = 0; ks < 4; ++ks) { tt[0] = MFMA32(asbf8(kmv[ks]), asbf8(qv[0][ks]), tt[0]); tt[1] = MFMA32(asbf8(kmv[ks]), asbf8(qv[1][ks]), tt[1]); }
#pragma unroll
    for (int b = 0; b < 2; ++b)
#pragma unroll
      for (int r = 0; r < 4; ++r) tt[b][r] = tt[b][r] * LS + n0[b];
    if (!__all(max3(max3(tt[0][0], tt[0][1], tt[0][2]), max3(tt[0][3], tt[1][0], tt[1][1]), max2(tt[1][2], tt[1][3])) <= 40.0f)) {
#pragma unroll
      for (int b = 0; b < 2; ++b) {
        float m = max3(max3(tt[b][0], tt[b][1], tt[b][2]), tt[b][3], 0.0f);
        { float x, y; sw16(m, x, y); m = max2(x, y); }
        const float f = __builtin_amdgcn_exp2f(-m);
        tt[b] -= m; sm[b] *= f;
#pragma unroll
        for (int dt = 0; dt < 8; ++dt) o[dt][b] *= f;
      }
    }
    u32x2 pm[2]; float inv[2];
#pragma unroll
    for (int b = 0; b < 2; ++b) {
      pm[b] = u32x2{pack2(__builtin_amdgcn_exp2f(tt[b][0]), __builtin_amdgcn_exp2f(tt[b][1])), pack2(__builtin_amdgcn_exp2f(tt[b][2]), __builtin_amdgcn_exp2f(tt[b][3]))};
      sm[b] = __builtin_amdgcn_mfma_f32_16x16x16bf16_1k(ass4(u32x2{one, one}), ass4(pm[b]), sm[b], 0, 0, 0);
    }
#pragma unroll
    for (int dt = 0; dt < 8; ++dt)
#pragma unroll
      for (int b = 0; b < 2; ++b) o[dt][b] = __builtin_amdgcn_mfma_f32_16x16x16bf16_1k(ass4((dt & 1) ? u32x2{vmv[dt >> 1].z, vmv[dt >> 1].w} : u32x2{vmv[dt >> 1].x, vmv[dt >> 1].y}), ass4(pm[b]), o[dt][b], 0, 0, 0);
    inv[0] = __builtin_amdgcn_rcpf(sm[0][0]); inv[1] = __builtin_amdgcn_rcpf(sm[1][0]);
    const char* od = uni(O + ((size_t)s * 64 + 2 * h) * 4096);
#pragma unroll
    for (int j = 0; j < 4; ++j) {
#pragma unroll
      for (int b = 0; b < 2; ++b) {
        const f32x4 r0 = o[2 * j][b] * inv[b], r1 = o[2 * j + 1][b] * inv[b];
        st16s(od + (j >> 1) * 4096 + b * 2048, oa ^ ((j & 1) * 64u), u32x4{pack2(r0[0], r0[1]), pack2(r0[2], r0[3]), pack2(r1[0], r1[1]), pack2(r1[2], r1[3])});
      }
    }
    if (!SAE && s >= SB && lane == 0) st4_sc1(cnt + (PSA + (hbase >> 1)) * 32 + 16 + wave, bell(s, rt_now() + DSA));
    WTM(slot, s, 3);
    WAIT_V(0);
    if (lane == 0) st4_sc1(cnt + (PSA + (hbase >> 1)) * 32 + wave, (unsigned)it);
    WTM(slot, s, 2);
  }
}
#undef LD8
#undef TIE8

constexpr int Q_MEM = 0;
constexpr int Q_K   = 16384;
constexpr int Q_V   = 24576;
constexpr int Q_QK  = 32768;
constexpr int Q_TM  = 36864;
constexpr int Q_CB  = 40960;
constexpr int Q_RING = 32768;
constexpr int V_P1  = 0;
constexpr int V_P2  = 16384;
constexpr int NCHAIN = 96;
#define VHN 2
#define ROTH 0
#define NQQ 6
#define NQK 4
#define E_AG 9
#define RDYI -2
#define ROTW 2
#define DQU 6
#define DQM 18
#define DQD 1
#define ARSEL (xg >> 1)
#define ORSEL (xg >> 1)
#define D_Q 105
#define D_O 4
#define E_B 6
#define E_O 1900
#define KDEL 5
#define VPOS 0

typedef __bf16 bf16x16 __attribute__((ext_vector_type(16)));
typedef unsigned u32x8 __attribute__((ext_vector_type(8)));
__device__ __forceinline__ f32x4 SMF(u32x4 a, u32x8 b, f32x4 c, int idx) {
  return __builtin_amdgcn_smfmac_f32_16x16x64_bf16(__builtin_bit_cast(bf16x8, a), __builtin_bit_cast(bf16x16, b), c, idx, 0, 0);
}
__device__ __forceinline__ float fold32(float a, float b) {
  const auto p = __builtin_amdgcn_permlane32_swap(__float_as_uint(a), __float_as_uint(b), false, false);
  return __uint_as_float(p[0]) + __uint_as_float(p[1]);
}
__device__ __forceinline__ f32x2 rowsum(f32x4 x) { return f32x2{fold32(x[0], x[2]), fold32(x[1], x[3])}; }
__device__ __forceinline__ unsigned nosent1(unsigned v) { return v < 0xFFFFFFFEu ? v : 0xFFFFFFFEu; }
__device__ __forceinline__ void st4s(const char* sb, unsigned vo, unsigned v) { asm volatile("global_store_dword %0, %1, %2 sc1\n\ts_nop 0" :: "v"(vo), "v"(v), "s"(sb) : "memory"); }
__device__ __forceinline__ void st8s(const char* sb, unsigned vo, u32x2 v) { asm volatile("global_store_dwordx2 %0, %1, %2 sc1\n\ts_nop 1" :: "v"(vo), "v"(v), "s"(sb) : "memory"); }
__device__ __forceinline__ void gldsw(const char* sbase, unsigned voff, unsigned lds) {
  unsigned keep;
  asm volatile("s_mov_b32 %0, m0\n\ts_mov_b32 m0, %3\n\ts_nop 2\n\tglobal_load_lds_dwordx4 %1, %2" CNT_ "\n\ts_mov_b32 m0, %0"
               : "=&s"(keep) : "v"(voff), "s"(sbase), "s"(lds) : "memory");
}
__device__ __forceinline__ void wait_clk(unsigned t0, unsigned d) {
  while ((unsigned)__builtin_amdgcn_s_memtime() - t0 < d) __builtin_amdgcn_s_sleep(1);
}
__device__ __forceinline__ unsigned bell_time(const char* a, unsigned vo, unsigned tag, unsigned cap) {
  unsigned v;
  for (unsigned spins = 0;; ++spins) {
    asm volatile("global_load_dword %0, %1, %2 sc1\n\ts_waitcnt vmcnt(0)" : "=&v"(v) : "v"(vo), "s"(a) : "memory");
    if (__all((v >> 24) >= tag) || spins >= cap) break;
  }
  const unsigned now = (unsigned)__builtin_amdgcn_s_memrealtime() + 250u;
  const unsigned age = rowmin16((now - v) & 0xFFFFFFu);
  return now - min(min(rdl(age, 0), rdl(age, 16)), 500u);
}
#define WBAR() do { __builtin_amdgcn_s_barrier(); asm volatile("" ::: "memory"); } while (0)
#define TN(k, v) do { } while (0)
__device__ __forceinline__ void stage_proj(const char* src, unsigned lds, int tid, int wave) {
  const int row = tid >> 4, f = 2 * ((row & 1) | (((row >> 2) & 1) << 1));
#pragma unroll
  for (int j = 0; j < 4; ++j)
    gldsw(src + (size_t)j * 32 * HD * 2, (unsigned)(row * HD * 2 + (((tid & 15) ^ f ^ ((j & 1) << 3)) << 4)), lds + (wave * 64 + 512 * j) * 16);
}
__device__ __forceinline__ unsigned proj_base(int T, int lane) {
  const int g = lane >> 4, q4 = (lane & 15) >> 2, p4 = lane & 3, fs = 2 * ((q4 & 1) | ((g & 1) << 1) | ((q4 >> 1) << 2));
  return (unsigned)((4 * g + (q4 & 1) + 32 * (q4 >> 1)) * 256 + (((2 * T) ^ fs) << 4) + p4 * 8);
}
__device__ __forceinline__ u32x8 proj_frag(const char* smem, unsigned a) {
  const u32x2 t0 = trread(smem, a), t1 = trread(smem, a + 512), t2 = trread(smem, a + 4096), t3 = trread(smem, a + 4608);
  return u32x8{t0.x, t0.y, t1.x, t1.y, t2.x, t2.y, t3.x, t3.y};
}
__device__ __forceinline__ void flag_frags(unsigned fa, unsigned a, unsigned want, bf16x8 (&f)[4]) {
  u32x2 t[8];
  for (;;) {
    u32x4 fl;
    asm volatile("ds_read_b128 %0, %9\n\t"
                 "ds_read_b64_tr_b16 %1, %10\n\tds_read_b64_tr_b16 %2, %10 offset:64\n\t"
                 "ds_read_b64_tr_b16 %3, %10 offset:512\n\tds_read_b64_tr_b16 %4, %10 offset:576\n\t"
                 "ds_read_b64_tr_b16 %5, %10 offset:1024\n\tds_read_b64_tr_b16 %6, %10 offset:1088\n\t"
                 "ds_read_b64_tr_b16 %7, %10 offset:1536\n\tds_read_b64_tr_b16 %8, %10 offset:1600\n\ts_waitcnt lgkmcnt(0)"
                 : "=&v"(fl), "=&v"(t[0]), "=&v"(t[1]), "=&v"(t[2]), "=&v"(t[3]), "=&v"(t[4]), "=&v"(t[5]), "=&v"(t[6]), "=&v"(t[7])
                 : "v"(fa), "v"(a) : "memory");
    if (__builtin_amdgcn_readfirstlane(min(min(fl.x, fl.y), min(fl.z, fl.w))) >= want) break;
  }
#pragma unroll
  for (int ks = 0; ks < 4; ++ks) f[ks] = asbf8(u32x4{t[2 * ks].x, t[2 * ks].y, t[2 * ks + 1].x, t[2 * ks + 1].y});
}
__device__ __forceinline__ void wait_flags(const char* p, unsigned want) {
  for (;;) {
    const u32x4 fl = *(volatile LAS u32x4*)p;
    if (__builtin_amdgcn_readfirstlane(min(min(fl.x, fl.y), min(fl.z, fl.w))) >= want) break;
  }
}
__device__ __forceinline__ f32x4 zero4() { float z = 0.0f; asm volatile("" : "+v"(z)); return f32x4{z, z, z, z}; }
__device__ __forceinline__ unsigned lds_word(const char* p) { return __builtin_amdgcn_readfirstlane(*(volatile LAS unsigned*)p); }

#define MFMA16(a, b, c) __builtin_amdgcn_mfma_f32_16x16x16bf16_1k((a), (b), (c), 0, 0, 0)
#define UF(x) __uint_as_float(x)
__device__ __forceinline__ unsigned umax3(unsigned a, unsigned b, unsigned c) { return max(max(a, b), c); }
__device__ __forceinline__ f32x2 logit2(float a, float b) {
  return f32x2{a, b};
}
constexpr float LSC = LOG2E * SCALE;

struct QKW { u32x32 w0, w1, w2, w3, w4, w5; u32x8 x; };
template <int W_> __device__ __forceinline__ void qk_step(const QKW& W, const char* ag, const char* vs, const char* od, const char* kv, unsigned& cap, unsigned& sp,
                                                          unsigned long long& tc, unsigned long long* ts, unsigned& d0, unsigned& d1);
template <> __device__ __forceinline__ void qk_step<0>(const QKW& W, const char* ag, const char* vs, const char* od, const char* kv, unsigned& cap, unsigned& sp,
                                                        unsigned long long& tc, unsigned long long* ts, unsigned& d0, unsigned& d1) {
  unsigned st, bg;
  asm volatile("v_mbcnt_lo_u32_b32 v60, -1, 0\n\t"
    "v_mbcnt_hi_u32_b32 v60, -1, v60\n\t"
    "v_lshlrev_b32 v6, 4, v60\n\t"
    "s_mov_b32 %[sp], 0\n\t"
    "s_setprio 3\n\t"
    "global_load_dwordx4 v[40:43], v6, %[ag] sc1\n\t"
    "global_load_dwordx4 v[44:47], v6, %[ag] offset:1024 sc1\n\t"
    "s_memtime %[tc]\n\t"
    "v_add_u32 v7, 40960, v6\n\t"
    "v_add_u32 v8, 98304, v6\n\t"
    "ds_read_b128 v[24:27], v7 offset:0\n\t"
    "ds_read_b128 v[28:31], v7 offset:1024\n\t"
    "ds_read_b128 v[32:35], v7 offset:8192\n\t"
    "ds_read_b128 v[36:39], v7 offset:9216\n\t"
    "v_lshrrev_b32 v48, 4, v60\n\t"
    "v_and_b32 v49, 15, v60\n\t"
    "v_and_b32 v61, 4, v60\n\t"
    "v_mul_u32_u24 v61, 0x2aaa, v61\n\t"
    "v_lshrrev_b32 v9, 1, v60\n\t"
    "v_and_b32 v9, 2, v9\n\t"
    "v_or3_b32 v9, v9, v61, %[c44]\n\t"
    "v_add_u32 v14, 0, v6\n\t"
    "v_lshlrev_b32 v61, 2, v48\n\t"
    "v_lshl_or_b32 v10, v49, 4, v61\n\t"
    "v_lshrrev_b32 v50, 2, v49\n\t"
    "v_and_b32 v51, 3, v60\n\t"
    "v_and_b32 v61, 1, v60\n\t"
    "v_lshlrev_b32 v61, 3, v61\n\t"
    "v_lshl_or_b32 v11, v50, 4, v61\n\t"
    "v_lshl_or_b32 v11, v48, 7, v11\n\t"
    "v_xor_b32 v12, v48, v49\n\t"
    "v_lshlrev_b32 v12, 4, v12\n\t"
    "v_lshl_or_b32 v12, v49, 8, v12\n\t"
    "v_lshl_or_b32 v50, v48, 2, v50\n\t"
    "v_or_b32 v51, 0, v51\n\t"
    "v_xor_b32 v51, v51, v50\n\t"
    "v_lshlrev_b32 v13, 4, v51\n\t"
    "v_lshl_or_b32 v13, v50, 8, v13\n\t"
    "v_and_b32 v61, 3, v60\n\t"
    "v_lshlrev_b32 v61, 4, v61\n\t"
    "v_lshl_or_b32 v15, v48, 6, v61\n\t"
    "v_and_b32 v61, 8, v60\n\t"
    "v_lshl_or_b32 v15, v61, 14, v15\n\t"
    "v_and_b32 v61, 4, v60\n\t"
    "v_lshl_or_b32 v15, v61, 8, v15\n\t"
    "v_add_u32 v15, 0, v15\n\t"
    "3:\n\t"
    "v_mov_b32 v16, 0\n\t"
    "v_mov_b32 v17, 0\n\t"
    "v_mov_b32 v18, 0\n\t"
    "v_mov_b32 v19, 0\n\t"
    "v_mov_b32 v20, 0\n\t"
    "v_mov_b32 v21, 0\n\t"
    "v_mov_b32 v22, 0\n\t"
    "v_mov_b32 v23, 0\n\t"
    "s_waitcnt vmcnt(0) lgkmcnt(0)\n\t"
    "14:\n\t"
    "ds_write_b128 v6, v[40:43] offset:0\n\t"
    "ds_write_b128 v6, v[44:47] offset:1024\n\t"
    "v_smfmac_f32_16x16x64_bf16 v[16:19], v[40:43], v[64:71], v9\n\t"
    "v_max3_u32 v60, v40, v41, v42\n\t"
    "v_max3_u32 v61, v43, v44, v45\n\t"
    "v_smfmac_f32_16x16x64_bf16 v[20:23], v[40:43], v[72:79], v9\n\t"
    "v_max3_u32 v60, v60, v46, v47\n\t"
    "v_max_u32 v60, v60, v61\n\t"
    "v_cmp_eq_u32 vcc, -1, v60\n\t"
    "s_cbranch_vccnz 1f\n\t"
    "2:\n\t"
    "s_waitcnt lgkmcnt(0)\n\t"
    "s_cmp_eq_u64 %[kv], 0\n\t"
    "s_barrier\n\t"
    "s_cbranch_scc1 20f\n\t"
    "ds_read_b128 v[40:43], v6 offset:12288\n\t"
    "ds_read_b128 v[48:51], v6 offset:2048\n\t"
    "v_smfmac_f32_16x16x64_bf16 v[16:19], v[44:47], v[80:87], v9\n\t"
    "ds_read_b128 v[52:55], v6 offset:3072\n\t"
    "ds_read_b128 v[56:59], v6 offset:4096\n\t"
    "v_smfmac_f32_16x16x64_bf16 v[20:23], v[44:47], v[88:95], v9\n\t"
    "ds_read_b128 v[60:63], v6 offset:13312\n\t"
    "ds_read_b128 v[44:47], v6 offset:5120\n\t"
    "s_waitcnt lgkmcnt(5)\n\t"
    "v_smfmac_f32_16x16x64_bf16 v[16:19], v[40:43], v[24:31], v9\n\t"
    "ds_read_b128 v[24:27], v7 offset:16384\n\t"
    "ds_read_b128 v[28:31], v7 offset:17408\n\t"
    "v_smfmac_f32_16x16x64_bf16 v[20:23], v[40:43], v[32:39], v9\n\t"
    "ds_read_b128 v[32:35], v7 offset:24576\n\t"
    "ds_read_b128 v[36:39], v7 offset:25600\n\t"
    "ds_read_b128 v[40:43], v6 offset:6144\n\t"
    "s_waitcnt lgkmcnt(9)\n\t"
    "v_smfmac_f32_16x16x64_bf16 v[16:19], v[48:51], v[96:103], v9\n\t"
    "v_smfmac_f32_16x16x64_bf16 v[20:23], v[48:51], v[104:111], v9\n\t"
    "ds_read_b128 v[48:51], v6 offset:7168\n\t"
    "s_waitcnt lgkmcnt(9)\n\t"
    "v_smfmac_f32_16x16x64_bf16 v[16:19], v[52:55], v[112:119], v9\n\t"
    "v_smfmac_f32_16x16x64_bf16 v[20:23], v[52:55], v[120:127], v9\n\t"
    "ds_read_b128 v[52:55], v6 offset:14336\n\t"
    "s_waitcnt lgkmcnt(9)\n\t"
    "v_smfmac_f32_16x16x64_bf16 v[16:19], v[56:59], v[128:135], v9\n\t"
    "v_smfmac_f32_16x16x64_bf16 v[20:23], v[56:59], v[136:143], v9\n\t"
    "ds_read_b128 v[56:59], v6 offset:8192\n\t"
    "s_waitcnt lgkmcnt(6)\n\t"
    "v_smfmac_f32_16x16x64_bf16 v[16:19], v[60:63], v[24:31], v9\n\t"
    "ds_read_b128 v[24:27], v8 offset:0\n\t"
    "ds_read_b128 v[28:31], v8 offset:1024\n\t"
    "s_waitcnt lgkmcnt(6)\n\t"
    "v_smfmac_f32_16x16x64_bf16 v[20:23], v[60:63], v[32:39], v9\n\t"
    "ds_read_b128 v[32:35], v8 offset:8192\n\t"
    "ds_read_b128 v[36:39], v8 offset:9216\n\t"
    "ds_read_b128 v[60:63], v6 offset:9216\n\t"
    "v_smfmac_f32_16x16x64_bf16 v[16:19], v[44:47], v[144:151], v9\n\t"
    "v_smfmac_f32_16x16x64_bf16 v[20:23], v[44:47], v[152:159], v9\n\t"
    "ds_read_b128 v[44:47], v6 offset:10240\n\t"
    "s_waitcnt lgkmcnt(9)\n\t"
    "v_smfmac_f32_16x16x64_bf16 v[16:19], v[40:43], v[160:167], v9\n\t"
    "v_smfmac_f32_16x16x64_bf16 v[20:23], v[40:43], v[168:175], v9\n\t"
    "ds_read_b128 v[40:43], v6 offset:15360\n\t"
    "s_waitcnt lgkmcnt(9)\n\t"
    "v_smfmac_f32_16x16x64_bf16 v[16:19], v[48:51], v[176:183], v9\n\t"
    "v_smfmac_f32_16x16x64_bf16 v[20:23], v[48:51], v[184:191], v9\n\t"
    "ds_read_b128 v[48:51], v6 offset:11264\n\t"
    "s_waitcnt lgkmcnt(6)\n\t"
    "v_smfmac_f32_16x16x64_bf16 v[16:19], v[52:55], v[24:31], v9\n\t"
    "ds_read_b128 v[24:27], v8 offset:16384\n\t"
    "ds_read_b128 v[28:31], v8 offset:17408\n\t"
    "s_waitcnt lgkmcnt(6)\n\t"
    "v_smfmac_f32_16x16x64_bf16 v[20:23], v[52:55], v[32:39], v9\n\t"
    "ds_read_b128 v[32:35], v8 offset:24576\n\t"
    "ds_read_b128 v[36:39], v8 offset:25600\n\t"
    "v_smfmac_f32_16x16x64_bf16 v[16:19], v[56:59], v[192:199], v9\n\t"
    "v_smfmac_f32_16x16x64_bf16 v[20:23], v[56:59], v[200:207], v9\n\t"
    "s_waitcnt lgkmcnt(7)\n\t"
    "v_smfmac_f32_16x16x64_bf16 v[16:19], v[60:63], v[208:215], v9\n\t"
    "v_smfmac_f32_16x16x64_bf16 v[20:23], v[60:63], v[216:223], v9\n\t"
    "s_waitcnt lgkmcnt(6)\n\t"
    "v_smfmac_f32_16x16x64_bf16 v[16:19], v[44:47], v[224:231], v9\n\t"
    "v_smfmac_f32_16x16x64_bf16 v[20:23], v[44:47], v[232:239], v9\n\t"
    "s_waitcnt lgkmcnt(2)\n\t"
    "v_smfmac_f32_16x16x64_bf16 v[16:19], v[40:43], v[24:31], v9\n\t"
    "s_waitcnt lgkmcnt(0)\n\t"
    "v_smfmac_f32_16x16x64_bf16 v[20:23], v[40:43], v[32:39], v9\n\t"
    "v_smfmac_f32_16x16x64_bf16 v[16:19], v[48:51], v[240:247], v9\n\t"
    "v_smfmac_f32_16x16x64_bf16 v[20:23], v[48:51], v[248:255], v9\n\t"
    "v_xor_b32 v7, 64, v12\n\t"
    "v_xor_b32 v8, 0x80, v12\n\t"
    "v_xor_b32 v6, 0xc0, v12\n\t"
    "ds_read_b128 v[24:27], v12 offset:16384\n\t"
    "ds_read_b128 v[28:31], v12 offset:20480\n\t"
    "ds_read_b128 v[32:35], v7 offset:16384\n\t"
    "ds_read_b128 v[36:39], v7 offset:20480\n\t"
    "v_permlane16_swap_b32 v16, v18\n\t"
    "v_permlane16_swap_b32 v17, v19\n\t"
    "v_permlane16_swap_b32 v20, v22\n\t"
    "v_permlane16_swap_b32 v21, v23\n\t"
    "v_add_f32 v16, v16, v18\n\t"
    "v_add_f32 v17, v17, v19\n\t"
    "v_add_f32 v20, v20, v22\n\t"
    "v_mul_f32 v16, %[lsc], v16\n\t"
    "v_mul_f32 v17, %[lsc], v17\n\t"
    "v_cvt_pk_bf16_f32 v16, v16, v17\n\t"
    "v_add_f32 v21, v21, v23\n\t"
    "ds_write_b32 v10, v16 offset:32768\n\t"
    "v_mul_f32 v20, %[lsc], v20\n\t"
    "v_mul_f32 v21, %[lsc], v21\n\t"
    "v_cvt_pk_bf16_f32 v20, v20, v21\n\t"
    "ds_write_b32 v10, v20 offset:33024\n\t"
    "ds_read_b128 v[40:43], v8 offset:16384\n\t"
    "ds_read_b128 v[44:47], v8 offset:20480\n\t"
    "ds_read_b128 v[48:51], v6 offset:16384\n\t"
    "ds_read_b128 v[52:55], v6 offset:20480\n\t"
    "s_waitcnt lgkmcnt(4)\n\t"
    "s_barrier\n\t"
    "ds_read_b64_tr_b16 v[16:17], v11 offset:32768\n\t"
    "ds_read_b64_tr_b16 v[18:19], v11 offset:32832\n\t"
    "ds_read_b64_tr_b16 v[20:21], v11 offset:33280\n\t"
    "ds_read_b64_tr_b16 v[22:23], v11 offset:33344\n\t"
    "ds_read_b64_tr_b16 v[56:57], v11 offset:33792\n\t"
    "ds_read_b64_tr_b16 v[58:59], v11 offset:33856\n\t"
    "ds_read_b64_tr_b16 v[60:61], v11 offset:34304\n\t"
    "ds_read_b64_tr_b16 v[62:63], v11 offset:34368\n\t"
    "v_mov_b32 v6, 0\n\t"
    "v_mov_b32 v7, 0\n\t"
    "v_mov_b32 v8, 0\n\t"
    "v_mov_b32 v9, 0\n\t"
    "s_mov_b32 exec_hi, 0\n\t"
    "global_load_dwordx4 v[6:9], v14, %[vs] sc1\n\t"
    "s_mov_b32 exec_hi, -1\n\t"
    "s_waitcnt lgkmcnt(6)\n\t"
    "v_mfma_f32_16x16x32_bf16 v[24:27], v[24:27], v[16:19], 0\n\t"
    "v_mfma_f32_16x16x32_bf16 v[28:31], v[28:31], v[16:19], 0\n\t"
    "s_waitcnt lgkmcnt(4)\n\t"
    "v_mfma_f32_16x16x32_bf16 v[24:27], v[32:35], v[20:23], v[24:27]\n\t"
    "v_mfma_f32_16x16x32_bf16 v[28:31], v[36:39], v[20:23], v[28:31]\n\t"
    "ds_read_b64_tr_b16 v[32:33], v13 offset:24576\n\t"
    "ds_read_b64_tr_b16 v[34:35], v13 offset:28672\n\t"
    "ds_read_b64_tr_b16 v[36:37], v13 offset:24584\n\t"
    "ds_read_b64_tr_b16 v[38:39], v13 offset:28680\n\t"
    "s_waitcnt lgkmcnt(6)\n\t"
    "v_mfma_f32_16x16x32_bf16 v[24:27], v[40:43], v[56:59], v[24:27]\n\t"
    "v_mfma_f32_16x16x32_bf16 v[28:31], v[44:47], v[56:59], v[28:31]\n\t"
    "s_waitcnt lgkmcnt(4)\n\t"
    "v_mfma_f32_16x16x32_bf16 v[24:27], v[48:51], v[60:63], v[24:27]\n\t"
    "v_mfma_f32_16x16x32_bf16 v[28:31], v[52:55], v[60:63], v[28:31]\n\t"
    "s_setprio 1\n\t"
    "v_mov_b32 v52, %[one]\n\t"
    "v_mov_b32 v53, %[one]\n\t"
    "v_mov_b32 v54, %[one]\n\t"
    "v_mov_b32 v55, %[one]\n\t"
    "s_nop 2\n\t"
    "v_max3_f32 v10, |v24|, |v25|, |v26|\n\t"
    "v_max3_f32 v40, |v27|, |v28|, |v29|\n\t"
    "v_max3_f32 v10, v10, |v30|, |v31|\n\t"
    "v_max_f32 v10, v10, v40\n\t"
    "v_cmp_nge_f32 vcc, %[big], v10\n\t"
    "s_mov_b32 %[bg], %[big]\n\t"
    "v_mov_b32 v10, 0\n\t"
    "s_cbranch_vccnz 12f\n\t"
    "13:\n\t"
    "v_exp_f32 v24, v24\n\t"
    "v_exp_f32 v25, v25\n\t"
    "v_exp_f32 v26, v26\n\t"
    "v_exp_f32 v27, v27\n\t"
    "v_exp_f32 v28, v28\n\t"
    "v_exp_f32 v29, v29\n\t"
    "v_exp_f32 v30, v30\n\t"
    "v_exp_f32 v31, v31\n\t"
    "v_cvt_pk_bf16_f32 v40, v24, v25\n\t"
    "v_cvt_pk_bf16_f32 v41, v26, v27\n\t"
    "v_cvt_pk_bf16_f32 v42, v28, v29\n\t"
    "v_cvt_pk_bf16_f32 v43, v30, v31\n\t"
    "s_setprio 3\n\t"
    "s_waitcnt lgkmcnt(0)\n\t"
    "s_nop 0\n\t"
    "v_mfma_f32_16x16x32_bf16 v[44:47], v[32:35], v[40:43], 0\n\t"
    "v_mfma_f32_16x16x32_bf16 v[48:51], v[36:39], v[40:43], 0\n\t"
    "v_mfma_f32_16x16x32_bf16 v[52:55], v[52:55], v[40:43], 0\n\t"
    "v_mov_b32 v12, 0\n\t"
    "v_mov_b32 v13, 0\n\t"
    "s_mov_b32 exec_hi, 0\n\t"
    "v_mov_b32 v12, %[one]\n\t"
    "v_mov_b32 v13, %[one]\n\t"
    "s_mov_b32 exec_hi, -1\n\t"
    "s_barrier\n\t"
    "ds_read_b64_tr_b16 v[24:25], v11 offset:34816\n\t"
    "ds_read_b64_tr_b16 v[26:27], v11 offset:34880\n\t"
    "ds_read_b64_tr_b16 v[28:29], v11 offset:35328\n\t"
    "ds_read_b64_tr_b16 v[30:31], v11 offset:35392\n\t"
    "ds_read_b64_tr_b16 v[32:33], v11 offset:35840\n\t"
    "ds_read_b64_tr_b16 v[34:35], v11 offset:35904\n\t"
    "ds_read_b64_tr_b16 v[36:37], v11 offset:36352\n\t"
    "ds_read_b64_tr_b16 v[38:39], v11 offset:36416\n\t"
    "s_waitcnt lgkmcnt(6)\n\t"
    "v_mfma_f32_16x16x32_bf16 v[40:43], v[24:27], v[16:19], 0\n\t"
    "s_waitcnt lgkmcnt(4)\n\t"
    "v_mfma_f32_16x16x32_bf16 v[40:43], v[28:31], v[20:23], v[40:43]\n\t"
    "s_waitcnt lgkmcnt(2)\n\t"
    "v_mfma_f32_16x16x32_bf16 v[40:43], v[32:35], v[56:59], v[40:43]\n\t"
    "s_waitcnt lgkmcnt(0)\n\t"
    "v_mfma_f32_16x16x32_bf16 v[40:43], v[36:39], v[60:63], v[40:43]\n\t"
    "5:\n\t"
    "s_waitcnt vmcnt(0)\n\t"
    "v_max3_u32 v11, v6, v7, v8\n\t"
    "v_max_u32 v11, v11, v9\n\t"
    "v_cmp_eq_u32 vcc, -1, v11\n\t"
    "s_cbranch_vccnz 4f\n\t"
    "6:\n\t"
    "s_nop 2\n\t"
    "v_exp_f32 v28, v40\n\t"
    "v_max3_f32 v11, v40, v41, v42\n\t"
    "v_exp_f32 v29, v41\n\t"
    "v_max_f32 v11, v11, v43\n\t"
    "v_exp_f32 v30, v42\n\t"
    "v_cmp_nge_f32 vcc, %[bg], v11\n\t"
    "v_exp_f32 v31, v43\n\t"
    "s_cbranch_vccnz 10f\n\t"
    "11:\n\t"
    "v_cvt_pk_bf16_f32 v24, v28, v29\n\t"
    "v_cvt_pk_bf16_f32 v25, v30, v31\n\t"
    "s_nop 1\n\t"
    "v_mfma_f32_16x16x16_bf16 v[52:55], v[12:13], v[24:25], v[52:55]\n\t"
    "v_mfma_f32_16x16x16_bf16 v[44:47], v[6:7], v[24:25], v[44:47]\n\t"
    "v_mfma_f32_16x16x16_bf16 v[48:51], v[8:9], v[24:25], v[48:51]\n\t"
    "s_nop 5\n\t"
    "v_rcp_f32 v26, v52\n\t"
    "s_nop 0\n\t"
    "v_pk_mul_f32 v[44:45], v[44:45], v[26:27] op_sel_hi:[1,0]\n\t"
    "v_pk_mul_f32 v[46:47], v[46:47], v[26:27] op_sel_hi:[1,0]\n\t"
    "v_pk_mul_f32 v[48:49], v[48:49], v[26:27] op_sel_hi:[1,0]\n\t"
    "v_pk_mul_f32 v[50:51], v[50:51], v[26:27] op_sel_hi:[1,0]\n\t"
    "v_cvt_pk_bf16_f32 v36, v44, v45\n\t"
    "v_cvt_pk_bf16_f32 v37, v46, v47\n\t"
    "v_cvt_pk_bf16_f32 v38, v48, v49\n\t"
    "v_cvt_pk_bf16_f32 v39, v50, v51\n\t"
    "v_min_u32 v36, -2, v36\n\t"
    "v_min_u32 v37, -2, v37\n\t"
    "v_min_u32 v38, -2, v38\n\t"
    "v_min_u32 v39, -2, v39\n\t"
    "global_store_dwordx4 v15, v[36:39], %[od] sc1\n\t"
    "s_barrier\n\t"
    "s_mov_b32 exec_hi, 0\n\t"
    "global_store_dwordx4 v14, v[6:9], %[kv] sc1\n\t"
    "s_mov_b32 exec_hi, -1\n\t"
    "s_nop 0\n\t"
    "s_waitcnt lgkmcnt(0)\n\t"
    "s_branch 9f\n\t"
    "1:\n\t"
    "s_add_u32 %[sp], %[sp], 1\n\t"
    "s_cmp_ge_u32 %[sp], %[cap]\n\t"
    "s_cbranch_scc1 7f\n\t"
    "global_load_dwordx4 v[40:43], v6, %[ag] sc1\n\t"
    "global_load_dwordx4 v[44:47], v6, %[ag] offset:1024 sc1\n\t"
    "s_waitcnt lgkmcnt(0)\n\t"
    "s_memtime %[tc]\n\t"
    "s_nop 7\n\t"
    "s_branch 3b\n\t"
    "7:\n\t"
    "s_mov_b32 %[cap], 0\n\t"
    "s_branch 2b\n\t"
    "20:\n\t"
    "s_barrier\n\t"
    "s_barrier\n\t"
    "s_barrier\n\t"
    "s_branch 9f\n\t"
    "4:\n\t"
    "s_add_u32 %[sp], %[sp], 0x10000\n\t"
    "s_lshr_b32 %[st], %[sp], 16\n\t"
    "s_cmp_ge_u32 %[st], %[cap]\n\t"
    "s_cbranch_scc1 8f\n\t"
    "s_mov_b32 exec_hi, 0\n\t"
    "global_load_dwordx4 v[6:9], v14, %[vs] sc1\n\t"
    "s_mov_b32 exec_hi, -1\n\t"
    "s_branch 5b\n\t"
    "8:\n\t"
    "s_mov_b32 %[cap], 0\n\t"
    "s_branch 6b\n\t"
    "10:\n\t"
    "v_max_f32 v11, v11, v10\n\t"
    "v_mov_b32 v26, v11\n\t"
    "s_nop 1\n\t"
    "v_permlane16_swap_b32 v11, v26\n\t"
    "v_max_f32 v11, v11, v26\n\t"
    "v_sub_f32 v28, v40, v11\n\t"
    "v_sub_f32 v29, v41, v11\n\t"
    "v_sub_f32 v30, v42, v11\n\t"
    "v_sub_f32 v31, v43, v11\n\t"
    "v_sub_f32 v11, v10, v11\n\t"
    "v_exp_f32 v11, v11\n\t"
    "v_exp_f32 v28, v28\n\t"
    "v_exp_f32 v29, v29\n\t"
    "v_exp_f32 v30, v30\n\t"
    "v_exp_f32 v31, v31\n\t"
    "v_mul_f32 v44, v44, v11\n\t"
    "v_mul_f32 v45, v45, v11\n\t"
    "v_mul_f32 v46, v46, v11\n\t"
    "v_mul_f32 v47, v47, v11\n\t"
    "v_mul_f32 v48, v48, v11\n\t"
    "v_mul_f32 v49, v49, v11\n\t"
    "v_mul_f32 v50, v50, v11\n\t"
    "v_mul_f32 v51, v51, v11\n\t"
    "v_mul_f32 v52, v52, v11\n\t"
    "v_mul_f32 v53, v53, v11\n\t"
    "v_mul_f32 v54, v54, v11\n\t"
    "v_mul_f32 v55, v55, v11\n\t"
    "s_branch 11b\n\t"
    "12:\n\t"
    "v_max3_f32 v10, v24, v25, v26\n\t"
    "v_max3_f32 v40, v27, v28, v29\n\t"
    "v_max3_f32 v10, v10, v30, v31\n\t"
    "v_max3_f32 v10, v10, v40, %[neg]\n\t"
    "v_mov_b32 v40, v10\n\t"
    "s_nop 1\n\t"
    "v_permlane16_swap_b32 v10, v40\n\t"
    "v_max_f32 v10, v10, v40\n\t"
    "v_mov_b32 v40, v10\n\t"
    "s_nop 1\n\t"
    "v_permlane32_swap_b32 v10, v40\n\t"
    "v_max_f32 v10, v10, v40\n\t"
    "v_sub_f32 v24, v24, v10\n\t"
    "v_sub_f32 v25, v25, v10\n\t"
    "v_sub_f32 v26, v26, v10\n\t"
    "v_sub_f32 v27, v27, v10\n\t"
    "v_sub_f32 v28, v28, v10\n\t"
    "v_sub_f32 v29, v29, v10\n\t"
    "v_sub_f32 v30, v30, v10\n\t"
    "v_sub_f32 v31, v31, v10\n\t"
    "s_mov_b32 %[bg], 0x7fc00000\n\t"
    "s_branch 13b\n\t"
    "9:"
    : [tc] "=&s"(tc), [sp] "=&s"(sp), [cap] "+s"(cap), [st] "=&s"(st), [bg] "=&s"(bg)
    : [ag] "s"(ag), [c44] "s"(0x4444u), [kv] "s"(kv), [vs] "s"(vs), [od] "s"(od), [od2] "s"(od + 262144), [neg] "s"(-1.0e38f), [lsc] "s"(LSC), [one] "s"(0x3F803F80u), [big] "s"(24.0f), "{v[64:95]}"(W.w0), "{v[96:127]}"(W.w1), "{v[128:159]}"(W.w2), "{v[160:191]}"(W.w3), "{v[192:223]}"(W.w4), "{v[224:255]}"(W.w5)
    : "v6", "v7", "v8", "v9", "v10", "v11", "v12", "v13", "v14", "v15", "v16", "v17", "v18", "v19", "v20", "v21", "v22", "v23", "v24", "v25", "v26", "v27", "v28", "v29", "v30", "v31", "v32", "v33", "v34", "v35", "v36", "v37", "v38", "v39", "v40", "v41", "v42", "v43", "v44", "v45", "v46", "v47", "v48", "v49", "v50", "v51", "v52", "v53", "v54", "v55", "v56", "v57", "v58", "v59", "v60", "v61", "v62", "v63", "vcc", "scc", "memory");
}
template <> __device__ __forceinline__ void qk_step<1>(const QKW& W, const char* ag, const char* vs, const char* od, const char* kv, unsigned& cap, unsigned& sp,
                                                        unsigned long long& tc, unsigned long long* ts, unsigned& d0, unsigned& d1) {
  unsigned st, bg;
  asm volatile("v_mbcnt_lo_u32_b32 v60, -1, 0\n\t"
    "v_mbcnt_hi_u32_b32 v60, -1, v60\n\t"
    "v_lshlrev_b32 v6, 4, v60\n\t"
    "s_mov_b32 %[sp], 0\n\t"
    "s_setprio 3\n\t"
    "global_load_dwordx4 v[40:43], v6, %[ag] sc1\n\t"
    "global_load_dwordx4 v[44:47], v6, %[ag] offset:1024 sc1\n\t"
    "s_memtime %[tc]\n\t"
    "v_add_u32 v7, 43008, v6\n\t"
    "v_add_u32 v8, 100352, v6\n\t"
    "ds_read_b128 v[24:27], v7 offset:0\n\t"
    "ds_read_b128 v[28:31], v7 offset:1024\n\t"
    "ds_read_b128 v[32:35], v7 offset:8192\n\t"
    "ds_read_b128 v[36:39], v7 offset:9216\n\t"
    "v_lshrrev_b32 v48, 4, v60\n\t"
    "v_and_b32 v49, 15, v60\n\t"
    "v_and_b32 v61, 4, v60\n\t"
    "v_mul_u32_u24 v61, 0x2aaa, v61\n\t"
    "v_lshrrev_b32 v9, 1, v60\n\t"
    "v_and_b32 v9, 2, v9\n\t"
    "v_or3_b32 v9, v9, v61, %[c44]\n\t"
    "v_add_u32 v14, 512, v6\n\t"
    "v_lshlrev_b32 v61, 2, v48\n\t"
    "v_lshl_or_b32 v10, v49, 4, v61\n\t"
    "v_lshrrev_b32 v50, 2, v49\n\t"
    "v_and_b32 v51, 3, v60\n\t"
    "v_and_b32 v61, 1, v60\n\t"
    "v_lshlrev_b32 v61, 3, v61\n\t"
    "v_lshl_or_b32 v11, v50, 4, v61\n\t"
    "v_lshl_or_b32 v11, v48, 7, v11\n\t"
    "v_xor_b32 v12, v48, v49\n\t"
    "v_lshlrev_b32 v12, 4, v12\n\t"
    "v_lshl_or_b32 v12, v49, 8, v12\n\t"
    "v_lshl_or_b32 v50, v48, 2, v50\n\t"
    "v_or_b32 v51, 4, v51\n\t"
    "v_xor_b32 v51, v51, v50\n\t"
    "v_lshlrev_b32 v13, 4, v51\n\t"
    "v_lshl_or_b32 v13, v50, 8, v13\n\t"
    "v_and_b32 v61, 3, v60\n\t"
    "v_lshlrev_b32 v61, 4, v61\n\t"
    "v_lshl_or_b32 v15, v48, 6, v61\n\t"
    "v_and_b32 v61, 8, v60\n\t"
    "v_lshl_or_b32 v15, v61, 14, v15\n\t"
    "v_and_b32 v61, 4, v60\n\t"
    "v_lshl_or_b32 v15, v61, 8, v15\n\t"
    "v_add_u32 v15, 256, v15\n\t"
    "3:\n\t"
    "v_mov_b32 v16, 0\n\t"
    "v_mov_b32 v17, 0\n\t"
    "v_mov_b32 v18, 0\n\t"
    "v_mov_b32 v19, 0\n\t"
    "v_mov_b32 v20, 0\n\t"
    "v_mov_b32 v21, 0\n\t"
    "v_mov_b32 v22, 0\n\t"
    "v_mov_b32 v23, 0\n\t"
    "s_waitcnt vmcnt(0) lgkmcnt(0)\n\t"
    "14:\n\t"
    "ds_write_b128 v6, v[40:43] offset:2048\n\t"
    "ds_write_b128 v6, v[44:47] offset:3072\n\t"
    "v_smfmac_f32_16x16x64_bf16 v[16:19], v[40:43], v[96:103], v9\n\t"
    "v_max3_u32 v60, v40, v41, v42\n\t"
    "v_max3_u32 v61, v43, v44, v45\n\t"
    "v_smfmac_f32_16x16x64_bf16 v[20:23], v[40:43], v[104:111], v9\n\t"
    "v_max3_u32 v60, v60, v46, v47\n\t"
    "v_max_u32 v60, v60, v61\n\t"
    "v_cmp_eq_u32 vcc, -1, v60\n\t"
    "s_cbranch_vccnz 1f\n\t"
    "2:\n\t"
    "s_waitcnt lgkmcnt(0)\n\t"
    "s_cmp_eq_u64 %[kv], 0\n\t"
    "s_barrier\n\t"
    "s_cbranch_scc1 20f\n\t"
    "ds_read_b128 v[40:43], v6 offset:12288\n\t"
    "ds_read_b128 v[48:51], v6 offset:0\n\t"
    "v_smfmac_f32_16x16x64_bf16 v[16:19], v[44:47], v[112:119], v9\n\t"
    "ds_read_b128 v[52:55], v6 offset:1024\n\t"
    "ds_read_b128 v[56:59], v6 offset:4096\n\t"
    "v_smfmac_f32_16x16x64_bf16 v[20:23], v[44:47], v[120:127], v9\n\t"
    "ds_read_b128 v[60:63], v6 offset:13312\n\t"
    "ds_read_b128 v[44:47], v6 offset:5120\n\t"
    "s_waitcnt lgkmcnt(5)\n\t"
    "v_smfmac_f32_16x16x64_bf16 v[16:19], v[40:43], v[24:31], v9\n\t"
    "ds_read_b128 v[24:27], v7 offset:16384\n\t"
    "ds_read_b128 v[28:31], v7 offset:17408\n\t"
    "v_smfmac_f32_16x16x64_bf16 v[20:23], v[40:43], v[32:39], v9\n\t"
    "ds_read_b128 v[32:35], v7 offset:24576\n\t"
    "ds_read_b128 v[36:39], v7 offset:25600\n\t"
    "ds_read_b128 v[40:43], v6 offset:6144\n\t"
    "s_waitcnt lgkmcnt(9)\n\t"
    "v_smfmac_f32_16x16x64_bf16 v[16:19], v[48:51], v[64:71], v9\n\t"
    "v_smfmac_f32_16x16x64_bf16 v[20:23], v[48:51], v[72:79], v9\n\t"
    "ds_read_b128 v[48:51], v6 offset:7168\n\t"
    "s_waitcnt lgkmcnt(9)\n\t"
    "v_smfmac_f32_16x16x64_bf16 v[16:19], v[52:55], v[80:87], v9\n\t"
    "v_smfmac_f32_16x16x64_bf16 v[20:23], v[52:55], v[88:95], v9\n\t"
    "ds_read_b128 v[52:55], v6 offset:14336\n\t"
    "s_waitcnt lgkmcnt(9)\n\t"
    "v_smfmac_f32_16x16x64_bf16 v[16:19], v[56:59], v[128:135], v9\n\t"
    "v_smfmac_f32_16x16x64_bf16 v[20:23], v[56:59], v[136:143], v9\n\t"
    "ds_read_b128 v[56:59], v6 offset:8192\n\t"
    "s_waitcnt lgkmcnt(6)\n\t"
    "v_smfmac_f32_16x16x64_bf16 v[16:19], v[60:63], v[24:31], v9\n\t"
    "ds_read_b128 v[24:27], v8 offset:0\n\t"
    "ds_read_b128 v[28:31], v8 offset:1024\n\t"
    "s_waitcnt lgkmcnt(6)\n\t"
    "v_smfmac_f32_16x16x64_bf16 v[20:23], v[60:63], v[32:39], v9\n\t"
    "ds_read_b128 v[32:35], v8 offset:8192\n\t"
    "ds_read_b128 v[36:39], v8 offset:9216\n\t"
    "ds_read_b128 v[60:63], v6 offset:9216\n\t"
    "v_smfmac_f32_16x16x64_bf16 v[16:19], v[44:47], v[144:151], v9\n\t"
    "v_smfmac_f32_16x16x64_bf16 v[20:23], v[44:47], v[152:159], v9\n\t"
    "ds_read_b128 v[44:47], v6 offset:10240\n\t"
    "s_waitcnt lgkmcnt(9)\n\t"
    "v_smfmac_f32_16x16x64_bf16 v[16:19], v[40:43], v[160:167], v9\n\t"
    "v_smfmac_f32_16x16x64_bf16 v[20:23], v[40:43], v[168:175], v9\n\t"
    "ds_read_b128 v[40:43], v6 offset:15360\n\t"
    "s_waitcnt lgkmcnt(9)\n\t"
    "v_smfmac_f32_16x16x64_bf16 v[16:19], v[48:51], v[176:183], v9\n\t"
    "v_smfmac_f32_16x16x64_bf16 v[20:23], v[48:51], v[184:191], v9\n\t"
    "ds_read_b128 v[48:51], v6 offset:11264\n\t"
    "s_waitcnt lgkmcnt(6)\n\t"
    "v_smfmac_f32_16x16x64_bf16 v[16:19], v[52:55], v[24:31], v9\n\t"
    "ds_read_b128 v[24:27], v8 offset:16384\n\t"
    "ds_read_b128 v[28:31], v8 offset:17408\n\t"
    "s_waitcnt lgkmcnt(6)\n\t"
    "v_smfmac_f32_16x16x64_bf16 v[20:23], v[52:55], v[32:39], v9\n\t"
    "ds_read_b128 v[32:35], v8 offset:24576\n\t"
    "ds_read_b128 v[36:39], v8 offset:25600\n\t"
    "v_smfmac_f32_16x16x64_bf16 v[16:19], v[56:59], v[192:199], v9\n\t"
    "v_smfmac_f32_16x16x64_bf16 v[20:23], v[56:59], v[200:207], v9\n\t"
    "s_waitcnt lgkmcnt(7)\n\t"
    "v_smfmac_f32_16x16x64_bf16 v[16:19], v[60:63], v[208:215], v9\n\t"
    "v_smfmac_f32_16x16x64_bf16 v[20:23], v[60:63], v[216:223], v9\n\t"
    "s_waitcnt lgkmcnt(6)\n\t"
    "v_smfmac_f32_16x16x64_bf16 v[16:19], v[44:47], v[224:231], v9\n\t"
    "v_smfmac_f32_16x16x64_bf16 v[20:23], v[44:47], v[232:239], v9\n\t"
    "s_waitcnt lgkmcnt(2)\n\t"
    "v_smfmac_f32_16x16x64_bf16 v[16:19], v[40:43], v[24:31], v9\n\t"
    "s_waitcnt lgkmcnt(0)\n\t"
    "v_smfmac_f32_16x16x64_bf16 v[20:23], v[40:43], v[32:39], v9\n\t"
    "v_smfmac_f32_16x16x64_bf16 v[16:19], v[48:51], v[240:247], v9\n\t"
    "v_smfmac_f32_16x16x64_bf16 v[20:23], v[48:51], v[248:255], v9\n\t"
    "v_xor_b32 v7, 64, v12\n\t"
    "v_xor_b32 v8, 0x80, v12\n\t"
    "v_xor_b32 v6, 0xc0, v12\n\t"
    "ds_read_b128 v[24:27], v12 offset:16384\n\t"
    "ds_read_b128 v[28:31], v12 offset:20480\n\t"
    "ds_read_b128 v[32:35], v7 offset:16384\n\t"
    "ds_read_b128 v[36:39], v7 offset:20480\n\t"
    "v_permlane16_swap_b32 v16, v18\n\t"
    "v_permlane16_swap_b32 v17, v19\n\t"
    "v_permlane16_swap_b32 v20, v22\n\t"
    "v_permlane16_swap_b32 v21, v23\n\t"
    "v_add_f32 v16, v16, v18\n\t"
    "v_add_f32 v17, v17, v19\n\t"
    "v_add_f32 v20, v20, v22\n\t"
    "v_mul_f32 v16, %[lsc], v16\n\t"
    "v_mul_f32 v17, %[lsc], v17\n\t"
    "v_cvt_pk_bf16_f32 v16, v16, v17\n\t"
    "v_add_f32 v21, v21, v23\n\t"
    "ds_write_b32 v10, v16 offset:33280\n\t"
    "v_mul_f32 v20, %[lsc], v20\n\t"
    "v_mul_f32 v21, %[lsc], v21\n\t"
    "v_cvt_pk_bf16_f32 v20, v20, v21\n\t"
    "ds_write_b32 v10, v20 offset:33536\n\t"
    "ds_read_b128 v[40:43], v8 offset:16384\n\t"
    "ds_read_b128 v[44:47], v8 offset:20480\n\t"
    "ds_read_b128 v[48:51], v6 offset:16384\n\t"
    "ds_read_b128 v[52:55], v6 offset:20480\n\t"
    "s_waitcnt lgkmcnt(4)\n\t"
    "s_barrier\n\t"
    "ds_read_b64_tr_b16 v[16:17], v11 offset:32768\n\t"
    "ds_read_b64_tr_b16 v[18:19], v11 offset:32832\n\t"
    "ds_read_b64_tr_b16 v[20:21], v11 offset:33280\n\t"
    "ds_read_b64_tr_b16 v[22:23], v11 offset:33344\n\t"
    "ds_read_b64_tr_b16 v[56:57], v11 offset:33792\n\t"
    "ds_read_b64_tr_b16 v[58:59], v11 offset:33856\n\t"
    "ds_read_b64_tr_b16 v[60:61], v11 offset:34304\n\t"
    "ds_read_b64_tr_b16 v[62:63], v11 offset:34368\n\t"
    "v_mov_b32 v6, 0\n\t"
    "v_mov_b32 v7, 0\n\t"
    "v_mov_b32 v8, 0\n\t"
    "v_mov_b32 v9, 0\n\t"
    "s_mov_b32 exec_hi, 0\n\t"
    "global_load_dwordx4 v[6:9], v14, %[vs] sc1\n\t"
    "s_mov_b32 exec_hi, -1\n\t"
    "s_waitcnt lgkmcnt(6)\n\t"
    "v_mfma_f32_16x16x32_bf16 v[24:27], v[24:27], v[16:19], 0\n\t"
    "v_mfma_f32_16x16x32_bf16 v[28:31], v[28:31], v[16:19], 0\n\t"
    "s_waitcnt lgkmcnt(4)\n\t"
    "v_mfma_f32_16x16x32_bf16 v[24:27], v[32:35], v[20:23], v[24:27]\n\t"
    "v_mfma_f32_16x16x32_bf16 v[28:31], v[36:39], v[20:23], v[28:31]\n\t"
    "ds_read_b64_tr_b16 v[32:33], v13 offset:24576\n\t"
    "ds_read_b64_tr_b16 v[34:35], v13 offset:28672\n\t"
    "ds_read_b64_tr_b16 v[36:37], v13 offset:24584\n\t"
    "ds_read_b64_tr_b16 v[38:39], v13 offset:28680\n\t"
    "s_waitcnt lgkmcnt(6)\n\t"
    "v_mfma_f32_16x16x32_bf16 v[24:27], v[40:43], v[56:59], v[24:27]\n\t"
    "v_mfma_f32_16x16x32_bf16 v[28:31], v[44:47], v[56:59], v[28:31]\n\t"
    "s_waitcnt lgkmcnt(4)\n\t"
    "v_mfma_f32_16x16x32_bf16 v[24:27], v[48:51], v[60:63], v[24:27]\n\t"
    "v_mfma_f32_16x16x32_bf16 v[28:31], v[52:55], v[60:63], v[28:31]\n\t"
    "s_setprio 1\n\t"
    "v_mov_b32 v52, %[one]\n\t"
    "v_mov_b32 v53, %[one]\n\t"
    "v_mov_b32 v54, %[one]\n\t"
    "v_mov_b32 v55, %[one]\n\t"
    "s_nop 2\n\t"
    "v_max3_f32 v10, |v24|, |v25|, |v26|\n\t"
    "v_max3_f32 v40, |v27|, |v28|, |v29|\n\t"
    "v_max3_f32 v10, v10, |v30|, |v31|\n\t"
    "v_max_f32 v10, v10, v40\n\t"
    "v_cmp_nge_f32 vcc, %[big], v10\n\t"
    "s_mov_b32 %[bg], %[big]\n\t"
    "v_mov_b32 v10, 0\n\t"
    "s_cbranch_vccnz 12f\n\t"
    "13:\n\t"
    "v_exp_f32 v24, v24\n\t"
    "v_exp_f32 v25, v25\n\t"
    "v_exp_f32 v26, v26\n\t"
    "v_exp_f32 v27, v27\n\t"
    "v_exp_f32 v28, v28\n\t"
    "v_exp_f32 v29, v29\n\t"
    "v_exp_f32 v30, v30\n\t"
    "v_exp_f32 v31, v31\n\t"
    "v_cvt_pk_bf16_f32 v40, v24, v25\n\t"
    "v_cvt_pk_bf16_f32 v41, v26, v27\n\t"
    "v_cvt_pk_bf16_f32 v42, v28, v29\n\t"
    "v_cvt_pk_bf16_f32 v43, v30, v31\n\t"
    "s_setprio 3\n\t"
    "s_waitcnt lgkmcnt(0)\n\t"
    "s_nop 0\n\t"
    "v_mfma_f32_16x16x32_bf16 v[44:47], v[32:35], v[40:43], 0\n\t"
    "v_mfma_f32_16x16x32_bf16 v[48:51], v[36:39], v[40:43], 0\n\t"
    "v_mfma_f32_16x16x32_bf16 v[52:55], v[52:55], v[40:43], 0\n\t"
    "v_mov_b32 v12, 0\n\t"
    "v_mov_b32 v13, 0\n\t"
    "s_mov_b32 exec_hi, 0\n\t"
    "v_mov_b32 v12, %[one]\n\t"
    "v_mov_b32 v13, %[one]\n\t"
    "s_mov_b32 exec_hi, -1\n\t"
    "s_barrier\n\t"
    "ds_read_b64_tr_b16 v[24:25], v11 offset:34816\n\t"
    "ds_read_b64_tr_b16 v[26:27], v11 offset:34880\n\t"
    "ds_read_b64_tr_b16 v[28:29], v11 offset:35328\n\t"
    "ds_read_b64_tr_b16 v[30:31], v11 offset:35392\n\t"
    "ds_read_b64_tr_b16 v[32:33], v11 offset:35840\n\t"
    "ds_read_b64_tr_b16 v[34:35], v11 offset:35904\n\t"
    "ds_read_b64_tr_b16 v[36:37], v11 offset:36352\n\t"
    "ds_read_b64_tr_b16 v[38:39], v11 offset:36416\n\t"
    "s_waitcnt lgkmcnt(6)\n\t"
    "v_mfma_f32_16x16x32_bf16 v[40:43], v[24:27], v[16:19], 0\n\t"
    "s_waitcnt lgkmcnt(4)\n\t"
    "v_mfma_f32_16x16x32_bf16 v[40:43], v[28:31], v[20:23], v[40:43]\n\t"
    "s_waitcnt lgkmcnt(2)\n\t"
    "v_mfma_f32_16x16x32_bf16 v[40:43], v[32:35], v[56:59], v[40:43]\n\t"
    "s_waitcnt lgkmcnt(0)\n\t"
    "v_mfma_f32_16x16x32_bf16 v[40:43], v[36:39], v[60:63], v[40:43]\n\t"
    "5:\n\t"
    "s_waitcnt vmcnt(0)\n\t"
    "v_max3_u32 v11, v6, v7, v8\n\t"
    "v_max_u32 v11, v11, v9\n\t"
    "v_cmp_eq_u32 vcc, -1, v11\n\t"
    "s_cbranch_vccnz 4f\n\t"
    "6:\n\t"
    "s_nop 2\n\t"
    "v_exp_f32 v28, v40\n\t"
    "v_max3_f32 v11, v40, v41, v42\n\t"
    "v_exp_f32 v29, v41\n\t"
    "v_max_f32 v11, v11, v43\n\t"
    "v_exp_f32 v30, v42\n\t"
    "v_cmp_nge_f32 vcc, %[bg], v11\n\t"
    "v_exp_f32 v31, v43\n\t"
    "s_cbranch_vccnz 10f\n\t"
    "11:\n\t"
    "v_cvt_pk_bf16_f32 v24, v28, v29\n\t"
    "v_cvt_pk_bf16_f32 v25, v30, v31\n\t"
    "s_nop 1\n\t"
    "v_mfma_f32_16x16x16_bf16 v[52:55], v[12:13], v[24:25], v[52:55]\n\t"
    "v_mfma_f32_16x16x16_bf16 v[44:47], v[6:7], v[24:25], v[44:47]\n\t"
    "v_mfma_f32_16x16x16_bf16 v[48:51], v[8:9], v[24:25], v[48:51]\n\t"
    "s_nop 5\n\t"
    "v_rcp_f32 v26, v52\n\t"
    "s_nop 0\n\t"
    "v_pk_mul_f32 v[44:45], v[44:45], v[26:27] op_sel_hi:[1,0]\n\t"
    "v_pk_mul_f32 v[46:47], v[46:47], v[26:27] op_sel_hi:[1,0]\n\t"
    "v_pk_mul_f32 v[48:49], v[48:49], v[26:27] op_sel_hi:[1,0]\n\t"
    "v_pk_mul_f32 v[50:51], v[50:51], v[26:27] op_sel_hi:[1,0]\n\t"
    "v_cvt_pk_bf16_f32 v36, v44, v45\n\t"
    "v_cvt_pk_bf16_f32 v37, v46, v47\n\t"
    "v_cvt_pk_bf16_f32 v38, v48, v49\n\t"
    "v_cvt_pk_bf16_f32 v39, v50, v51\n\t"
    "v_min_u32 v36, -2, v36\n\t"
    "v_min_u32 v37, -2, v37\n\t"
    "v_min_u32 v38, -2, v38\n\t"
    "v_min_u32 v39, -2, v39\n\t"
    "global_store_dwordx4 v15, v[36:39], %[od] sc1\n\t"
    "s_barrier\n\t"
    "s_mov_b32 exec_hi, 0\n\t"
    "global_store_dwordx4 v14, v[6:9], %[kv] sc1\n\t"
    "s_mov_b32 exec_hi, -1\n\t"
    "s_nop 0\n\t"
    "s_waitcnt lgkmcnt(0)\n\t"
    "s_branch 9f\n\t"
    "1:\n\t"
    "s_add_u32 %[sp], %[sp], 1\n\t"
    "s_cmp_ge_u32 %[sp], %[cap]\n\t"
    "s_cbranch_scc1 7f\n\t"
    "global_load_dwordx4 v[40:43], v6, %[ag] sc1\n\t"
    "global_load_dwordx4 v[44:47], v6, %[ag] offset:1024 sc1\n\t"
    "s_waitcnt lgkmcnt(0)\n\t"
    "s_memtime %[tc]\n\t"
    "s_nop 7\n\t"
    "s_branch 3b\n\t"
    "7:\n\t"
    "s_mov_b32 %[cap], 0\n\t"
    "s_branch 2b\n\t"
    "20:\n\t"
    "s_barrier\n\t"
    "s_barrier\n\t"
    "s_barrier\n\t"
    "s_branch 9f\n\t"
    "4:\n\t"
    "s_add_u32 %[sp], %[sp], 0x10000\n\t"
    "s_lshr_b32 %[st], %[sp], 16\n\t"
    "s_cmp_ge_u32 %[st], %[cap]\n\t"
    "s_cbranch_scc1 8f\n\t"
    "s_mov_b32 exec_hi, 0\n\t"
    "global_load_dwordx4 v[6:9], v14, %[vs] sc1\n\t"
    "s_mov_b32 exec_hi, -1\n\t"
    "s_branch 5b\n\t"
    "8:\n\t"
    "s_mov_b32 %[cap], 0\n\t"
    "s_branch 6b\n\t"
    "10:\n\t"
    "v_max_f32 v11, v11, v10\n\t"
    "v_mov_b32 v26, v11\n\t"
    "s_nop 1\n\t"
    "v_permlane16_swap_b32 v11, v26\n\t"
    "v_max_f32 v11, v11, v26\n\t"
    "v_sub_f32 v28, v40, v11\n\t"
    "v_sub_f32 v29, v41, v11\n\t"
    "v_sub_f32 v30, v42, v11\n\t"
    "v_sub_f32 v31, v43, v11\n\t"
    "v_sub_f32 v11, v10, v11\n\t"
    "v_exp_f32 v11, v11\n\t"
    "v_exp_f32 v28, v28\n\t"
    "v_exp_f32 v29, v29\n\t"
    "v_exp_f32 v30, v30\n\t"
    "v_exp_f32 v31, v31\n\t"
    "v_mul_f32 v44, v44, v11\n\t"
    "v_mul_f32 v45, v45, v11\n\t"
    "v_mul_f32 v46, v46, v11\n\t"
    "v_mul_f32 v47, v47, v11\n\t"
    "v_mul_f32 v48, v48, v11\n\t"
    "v_mul_f32 v49, v49, v11\n\t"
    "v_mul_f32 v50, v50, v11\n\t"
    "v_mul_f32 v51, v51, v11\n\t"
    "v_mul_f32 v52, v52, v11\n\t"
    "v_mul_f32 v53, v53, v11\n\t"
    "v_mul_f32 v54, v54, v11\n\t"
    "v_mul_f32 v55, v55, v11\n\t"
    "s_branch 11b\n\t"
    "12:\n\t"
    "v_max3_f32 v10, v24, v25, v26\n\t"
    "v_max3_f32 v40, v27, v28, v29\n\t"
    "v_max3_f32 v10, v10, v30, v31\n\t"
    "v_max3_f32 v10, v10, v40, %[neg]\n\t"
    "v_mov_b32 v40, v10\n\t"
    "s_nop 1\n\t"
    "v_permlane16_swap_b32 v10, v40\n\t"
    "v_max_f32 v10, v10, v40\n\t"
    "v_mov_b32 v40, v10\n\t"
    "s_nop 1\n\t"
    "v_permlane32_swap_b32 v10, v40\n\t"
    "v_max_f32 v10, v10, v40\n\t"
    "v_sub_f32 v24, v24, v10\n\t"
    "v_sub_f32 v25, v25, v10\n\t"
    "v_sub_f32 v26, v26, v10\n\t"
    "v_sub_f32 v27, v27, v10\n\t"
    "v_sub_f32 v28, v28, v10\n\t"
    "v_sub_f32 v29, v29, v10\n\t"
    "v_sub_f32 v30, v30, v10\n\t"
    "v_sub_f32 v31, v31, v10\n\t"
    "s_mov_b32 %[bg], 0x7fc00000\n\t"
    "s_branch 13b\n\t"
    "9:"
    : [tc] "=&s"(tc), [sp] "=&s"(sp), [cap] "+s"(cap), [st] "=&s"(st), [bg] "=&s"(bg)
    : [ag] "s"(ag), [c44] "s"(0x4444u), [kv] "s"(kv), [vs] "s"(vs), [od] "s"(od), [od2] "s"(od + 262144), [neg] "s"(-1.0e38f), [lsc] "s"(LSC), [one] "s"(0x3F803F80u), [big] "s"(24.0f), "{v[64:95]}"(W.w0), "{v[96:127]}"(W.w1), "{v[128:159]}"(W.w2), "{v[160:191]}"(W.w3), "{v[192:223]}"(W.w4), "{v[224:255]}"(W.w5)
    : "v6", "v7", "v8", "v9", "v10", "v11", "v12", "v13", "v14", "v15", "v16", "v17", "v18", "v19", "v20", "v21", "v22", "v23", "v24", "v25", "v26", "v27", "v28", "v29", "v30", "v31", "v32", "v33", "v34", "v35", "v36", "v37", "v38", "v39", "v40", "v41", "v42", "v43", "v44", "v45", "v46", "v47", "v48", "v49", "v50", "v51", "v52", "v53", "v54", "v55", "v56", "v57", "v58", "v59", "v60", "v61", "v62", "v63", "vcc", "scc", "memory");
}
template <> __device__ __forceinline__ void qk_step<2>(const QKW& W, const char* ag, const char* vs, const char* od, const char* kv, unsigned& cap, unsigned& sp,
                                                        unsigned long long& tc, unsigned long long* ts, unsigned& d0, unsigned& d1) {
  unsigned st, bg;
  asm volatile("v_mbcnt_lo_u32_b32 v60, -1, 0\n\t"
    "v_mbcnt_hi_u32_b32 v60, -1, v60\n\t"
    "v_lshlrev_b32 v6, 4, v60\n\t"
    "s_mov_b32 %[sp], 0\n\t"
    "s_setprio 3\n\t"
    "global_load_dwordx4 v[40:43], v6, %[ag] sc1\n\t"
    "global_load_dwordx4 v[44:47], v6, %[ag] offset:1024 sc1\n\t"
    "s_memtime %[tc]\n\t"
    "v_add_u32 v7, 45056, v6\n\t"
    "v_add_u32 v8, 102400, v6\n\t"
    "ds_read_b128 v[24:27], v7 offset:0\n\t"
    "ds_read_b128 v[28:31], v7 offset:1024\n\t"
    "ds_read_b128 v[32:35], v7 offset:8192\n\t"
    "ds_read_b128 v[36:39], v7 offset:9216\n\t"
    "v_lshrrev_b32 v48, 4, v60\n\t"
    "v_and_b32 v49, 15, v60\n\t"
    "v_and_b32 v61, 4, v60\n\t"
    "v_mul_u32_u24 v61, 0x2aaa, v61\n\t"
    "v_lshrrev_b32 v9, 1, v60\n\t"
    "v_and_b32 v9, 2, v9\n\t"
    "v_or3_b32 v9, v9, v61, %[c44]\n\t"
    "v_add_u32 v14, 1024, v6\n\t"
    "v_lshlrev_b32 v61, 2, v48\n\t"
    "v_lshl_or_b32 v10, v49, 4, v61\n\t"
    "v_lshrrev_b32 v50, 2, v49\n\t"
    "v_and_b32 v51, 3, v60\n\t"
    "v_and_b32 v61, 1, v60\n\t"
    "v_lshlrev_b32 v61, 3, v61\n\t"
    "v_lshl_or_b32 v11, v50, 4, v61\n\t"
    "v_lshl_or_b32 v11, v48, 7, v11\n\t"
    "v_xor_b32 v12, v48, v49\n\t"
    "v_lshlrev_b32 v12, 4, v12\n\t"
    "v_lshl_or_b32 v12, v49, 8, v12\n\t"
    "v_lshl_or_b32 v50, v48, 2, v50\n\t"
    "v_or_b32 v51, 8, v51\n\t"
    "v_xor_b32 v51, v51, v50\n\t"
    "v_lshlrev_b32 v13, 4, v51\n\t"
    "v_lshl_or_b32 v13, v50, 8, v13\n\t"
    "v_and_b32 v61, 3, v60\n\t"
    "v_lshlrev_b32 v61, 4, v61\n\t"
    "v_lshl_or_b32 v15, v48, 6, v61\n\t"
    "v_and_b32 v61, 8, v60\n\t"
    "v_lshl_or_b32 v15, v61, 14, v15\n\t"
    "v_and_b32 v61, 4, v60\n\t"
    "v_lshl_or_b32 v15, v61, 8, v15\n\t"
    "v_add_u32 v15, 512, v15\n\t"
    "3:\n\t"
    "v_mov_b32 v16, 0\n\t"
    "v_mov_b32 v17, 0\n\t"
    "v_mov_b32 v18, 0\n\t"
    "v_mov_b32 v19, 0\n\t"
    "v_mov_b32 v20, 0\n\t"
    "v_mov_b32 v21, 0\n\t"
    "v_mov_b32 v22, 0\n\t"
    "v_mov_b32 v23, 0\n\t"
    "s_waitcnt vmcnt(0) lgkmcnt(0)\n\t"
    "14:\n\t"
    "ds_write_b128 v6, v[40:43] offset:4096\n\t"
    "ds_write_b128 v6, v[44:47] offset:5120\n\t"
    "v_smfmac_f32_16x16x64_bf16 v[16:19], v[40:43], v[128:135], v9\n\t"
    "v_max3_u32 v60, v40, v41, v42\n\t"
    "v_max3_u32 v61, v43, v44, v45\n\t"
    "v_smfmac_f32_16x16x64_bf16 v[20:23], v[40:43], v[136:143], v9\n\t"
    "v_max3_u32 v60, v60, v46, v47\n\t"
    "v_max_u32 v60, v60, v61\n\t"
    "v_cmp_eq_u32 vcc, -1, v60\n\t"
    "s_cbranch_vccnz 1f\n\t"
    "2:\n\t"
    "s_waitcnt lgkmcnt(0)\n\t"
    "s_cmp_eq_u64 %[kv], 0\n\t"
    "s_barrier\n\t"
    "s_cbranch_scc1 20f\n\t"
    "ds_read_b128 v[40:43], v6 offset:12288\n\t"
    "ds_read_b128 v[48:51], v6 offset:0\n\t"
    "v_smfmac_f32_16x16x64_bf16 v[16:19], v[44:47], v[144:151], v9\n\t"
    "ds_read_b128 v[52:55], v6 offset:1024\n\t"
    "ds_read_b128 v[56:59], v6 offset:2048\n\t"
    "v_smfmac_f32_16x16x64_bf16 v[20:23], v[44:47], v[152:159], v9\n\t"
    "ds_read_b128 v[60:63], v6 offset:13312\n\t"
    "ds_read_b128 v[44:47], v6 offset:3072\n\t"
    "s_waitcnt lgkmcnt(5)\n\t"
    "v_smfmac_f32_16x16x64_bf16 v[16:19], v[40:43], v[24:31], v9\n\t"
    "ds_read_b128 v[24:27], v7 offset:16384\n\t"
    "ds_read_b128 v[28:31], v7 offset:17408\n\t"
    "v_smfmac_f32_16x16x64_bf16 v[20:23], v[40:43], v[32:39], v9\n\t"
    "ds_read_b128 v[32:35], v7 offset:24576\n\t"
    "ds_read_b128 v[36:39], v7 offset:25600\n\t"
    "ds_read_b128 v[40:43], v6 offset:6144\n\t"
    "s_waitcnt lgkmcnt(9)\n\t"
    "v_smfmac_f32_16x16x64_bf16 v[16:19], v[48:51], v[64:71], v9\n\t"
    "v_smfmac_f32_16x16x64_bf16 v[20:23], v[48:51], v[72:79], v9\n\t"
    "ds_read_b128 v[48:51], v6 offset:7168\n\t"
    "s_waitcnt lgkmcnt(9)\n\t"
    "v_smfmac_f32_16x16x64_bf16 v[16:19], v[52:55], v[80:87], v9\n\t"
    "v_smfmac_f32_16x16x64_bf16 v[20:23], v[52:55], v[88:95], v9\n\t"
    "ds_read_b128 v[52:55], v6 offset:14336\n\t"
    "s_waitcnt lgkmcnt(9)\n\t"
    "v_smfmac_f32_16x16x64_bf16 v[16:19], v[56:59], v[96:103], v9\n\t"
    "v_smfmac_f32_16x16x64_bf16 v[20:23], v[56:59], v[104:111], v9\n\t"
    "ds_read_b128 v[56:59], v6 offset:8192\n\t"
    "s_waitcnt lgkmcnt(6)\n\t"
    "v_smfmac_f32_16x16x64_bf16 v[16:19], v[60:63], v[24:31], v9\n\t"
    "ds_read_b128 v[24:27], v8 offset:0\n\t"
    "ds_read_b128 v[28:31], v8 offset:1024\n\t"
    "s_waitcnt lgkmcnt(6)\n\t"
    "v_smfmac_f32_16x16x64_bf16 v[20:23], v[60:63], v[32:39], v9\n\t"
    "ds_read_b128 v[32:35], v8 offset:8192\n\t"
    "ds_read_b128 v[36:39], v8 offset:9216\n\t"
    "ds_read_b128 v[60:63], v6 offset:9216\n\t"
    "v_smfmac_f32_16x16x64_bf16 v[16:19], v[44:47], v[112:119], v9\n\t"
    "v_smfmac_f32_16x16x64_bf16 v[20:23], v[44:47], v[120:127], v9\n\t"
    "ds_read_b128 v[44:47], v6 offset:10240\n\t"
    "s_waitcnt lgkmcnt(9)\n\t"
    "v_smfmac_f32_16x16x64_bf16 v[16:19], v[40:43], v[160:167], v9\n\t"
    "v_smfmac_f32_16x16x64_bf16 v[20:23], v[40:43], v[168:175], v9\n\t"
    "ds_read_b128 v[40:43], v6 offset:15360\n\t"
    "s_waitcnt lgkmcnt(9)\n\t"
    "v_smfmac_f32_16x16x64_bf16 v[16:19], v[48:51], v[176:183], v9\n\t"
    "v_smfmac_f32_16x16x64_bf16 v[20:23], v[48:51], v[184:191], v9\n\t"
    "ds_read_b128 v[48:51], v6 offset:11264\n\t"
    "s_waitcnt lgkmcnt(6)\n\t"
    "v_smfmac_f32_16x16x64_bf16 v[16:19], v[52:55], v[24:31], v9\n\t"
    "ds_read_b128 v[24:27], v8 offset:16384\n\t"
    "ds_read_b128 v[28:31], v8 offset:17408\n\t"
    "s_waitcnt lgkmcnt(6)\n\t"
    "v_smfmac_f32_16x16x64_bf16 v[20:23], v[52:55], v[32:39], v9\n\t"
    "ds_read_b128 v[32:35], v8 offset:24576\n\t"
    "ds_read_b128 v[36:39], v8 offset:25600\n\t"
    "v_smfmac_f32_16x16x64_bf16 v[16:19], v[56:59], v[192:199], v9\n\t"
    "v_smfmac_f32_16x16x64_bf16 v[20:23], v[56:59], v[200:207], v9\n\t"
    "s_waitcnt lgkmcnt(7)\n\t"
    "v_smfmac_f32_16x16x64_bf16 v[16:19], v[60:63], v[208:215], v9\n\t"
    "v_smfmac_f32_16x16x64_bf16 v[20:23], v[60:63], v[216:223], v9\n\t"
    "s_waitcnt lgkmcnt(6)\n\t"
    "v_smfmac_f32_16x16x64_bf16 v[16:19], v[44:47], v[224:231], v9\n\t"
    "v_smfmac_f32_16x16x64_bf16 v[20:23], v[44:47], v[232:239], v9\n\t"
    "s_waitcnt lgkmcnt(2)\n\t"
    "v_smfmac_f32_16x16x64_bf16 v[16:19], v[40:43], v[24:31], v9\n\t"
    "s_waitcnt lgkmcnt(0)\n\t"
    "v_smfmac_f32_16x16x64_bf16 v[20:23], v[40:43], v[32:39], v9\n\t"
    "v_smfmac_f32_16x16x64_bf16 v[16:19], v[48:51], v[240:247], v9\n\t"
    "v_smfmac_f32_16x16x64_bf16 v[20:23], v[48:51], v[248:255], v9\n\t"
    "v_xor_b32 v7, 64, v12\n\t"
    "v_xor_b32 v8, 0x80, v12\n\t"
    "v_xor_b32 v6, 0xc0, v12\n\t"
    "ds_read_b128 v[24:27], v12 offset:16384\n\t"
    "ds_read_b128 v[28:31], v12 offset:20480\n\t"
    "ds_read_b128 v[32:35], v7 offset:16384\n\t"
    "ds_read_b128 v[36:39], v7 offset:20480\n\t"
    "v_permlane16_swap_b32 v16, v18\n\t"
    "v_permlane16_swap_b32 v17, v19\n\t"
    "v_permlane16_swap_b32 v20, v22\n\t"
    "v_permlane16_swap_b32 v21, v23\n\t"
    "v_add_f32 v16, v16, v18\n\t"
    "v_add_f32 v17, v17, v19\n\t"
    "v_add_f32 v20, v20, v22\n\t"
    "v_mul_f32 v16, %[lsc], v16\n\t"
    "v_mul_f32 v17, %[lsc], v17\n\t"
    "v_cvt_pk_bf16_f32 v16, v16, v17\n\t"
    "v_add_f32 v21, v21, v23\n\t"
    "ds_write_b32 v10, v16 offset:33792\n\t"
    "v_mul_f32 v20, %[lsc], v20\n\t"
    "v_mul_f32 v21, %[lsc], v21\n\t"
    "v_cvt_pk_bf16_f32 v20, v20, v21\n\t"
    "ds_write_b32 v10, v20 offset:34048\n\t"
    "ds_read_b128 v[40:43], v8 offset:16384\n\t"
    "ds_read_b128 v[44:47], v8 offset:20480\n\t"
    "ds_read_b128 v[48:51], v6 offset:16384\n\t"
    "ds_read_b128 v[52:55], v6 offset:20480\n\t"
    "s_waitcnt lgkmcnt(4)\n\t"
    "s_barrier\n\t"
    "ds_read_b64_tr_b16 v[16:17], v11 offset:32768\n\t"
    "ds_read_b64_tr_b16 v[18:19], v11 offset:32832\n\t"
    "ds_read_b64_tr_b16 v[20:21], v11 offset:33280\n\t"
    "ds_read_b64_tr_b16 v[22:23], v11 offset:33344\n\t"
    "ds_read_b64_tr_b16 v[56:57], v11 offset:33792\n\t"
    "ds_read_b64_tr_b16 v[58:59], v11 offset:33856\n\t"
    "ds_read_b64_tr_b16 v[60:61], v11 offset:34304\n\t"
    "ds_read_b64_tr_b16 v[62:63], v11 offset:34368\n\t"
    "v_mov_b32 v6, 0\n\t"
    "v_mov_b32 v7, 0\n\t"
    "v_mov_b32 v8, 0\n\t"
    "v_mov_b32 v9, 0\n\t"
    "s_mov_b32 exec_hi, 0\n\t"
    "global_load_dwordx4 v[6:9], v14, %[vs] sc1\n\t"
    "s_mov_b32 exec_hi, -1\n\t"
    "s_waitcnt lgkmcnt(6)\n\t"
    "v_mfma_f32_16x16x32_bf16 v[24:27], v[24:27], v[16:19], 0\n\t"
    "v_mfma_f32_16x16x32_bf16 v[28:31], v[28:31], v[16:19], 0\n\t"
    "s_waitcnt lgkmcnt(4)\n\t"
    "v_mfma_f32_16x16x32_bf16 v[24:27], v[32:35], v[20:23], v[24:27]\n\t"
    "v_mfma_f32_16x16x32_bf16 v[28:31], v[36:39], v[20:23], v[28:31]\n\t"
    "ds_read_b64_tr_b16 v[32:33], v13 offset:24576\n\t"
    "ds_read_b64_tr_b16 v[34:35], v13 offset:28672\n\t"
    "ds_read_b64_tr_b16 v[36:37], v13 offset:24584\n\t"
    "ds_read_b64_tr_b16 v[38:39], v13 offset:28680\n\t"
    "s_waitcnt lgkmcnt(6)\n\t"
    "v_mfma_f32_16x16x32_bf16 v[24:27], v[40:43], v[56:59], v[24:27]\n\t"
    "v_mfma_f32_16x16x32_bf16 v[28:31], v[44:47], v[56:59], v[28:31]\n\t"
    "s_waitcnt lgkmcnt(4)\n\t"
    "v_mfma_f32_16x16x32_bf16 v[24:27], v[48:51], v[60:63], v[24:27]\n\t"
    "v_mfma_f32_16x16x32_bf16 v[28:31], v[52:55], v[60:63], v[28:31]\n\t"
    "s_setprio 1\n\t"
    "v_mov_b32 v52, %[one]\n\t"
    "v_mov_b32 v53, %[one]\n\t"
    "v_mov_b32 v54, %[one]\n\t"
    "v_mov_b32 v55, %[one]\n\t"
    "s_nop 2\n\t"
    "v_max3_f32 v10, |v24|, |v25|, |v26|\n\t"
    "v_max3_f32 v40, |v27|, |v28|, |v29|\n\t"
    "v_max3_f32 v10, v10, |v30|, |v31|\n\t"
    "v_max_f32 v10, v10, v40\n\t"
    "v_cmp_nge_f32 vcc, %[big], v10\n\t"
    "s_mov_b32 %[bg], %[big]\n\t"
    "v_mov_b32 v10, 0\n\t"
    "s_cbranch_vccnz 12f\n\t"
    "13:\n\t"
    "v_exp_f32 v24, v24\n\t"
    "v_exp_f32 v25, v25\n\t"
    "v_exp_f32 v26, v26\n\t"
    "v_exp_f32 v27, v27\n\t"
    "v_exp_f32 v28, v28\n\t"
    "v_exp_f32 v29, v29\n\t"
    "v_exp_f32 v30, v30\n\t"
    "v_exp_f32 v31, v31\n\t"
    "v_cvt_pk_bf16_f32 v40, v24, v25\n\t"
    "v_cvt_pk_bf16_f32 v41, v26, v27\n\t"
    "v_cvt_pk_bf16_f32 v42, v28, v29\n\t"
    "v_cvt_pk_bf16_f32 v43, v30, v31\n\t"
    "s_setprio 3\n\t"
    "s_waitcnt lgkmcnt(0)\n\t"
    "s_nop 0\n\t"
    "v_mfma_f32_16x16x32_bf16 v[44:47], v[32:35], v[40:43], 0\n\t"
    "v_mfma_f32_16x16x32_bf16 v[48:51], v[36:39], v[40:43], 0\n\t"
    "v_mfma_f32_16x16x32_bf16 v[52:55], v[52:55], v[40:43], 0\n\t"
    "v_mov_b32 v12, 0\n\t"
    "v_mov_b32 v13, 0\n\t"
    "s_mov_b32 exec_hi, 0\n\t"
    "v_mov_b32 v12, %[one]\n\t"
    "v_mov_b32 v13, %[one]\n\t"
    "s_mov_b32 exec_hi, -1\n\t"
    "s_barrier\n\t"
    "ds_read_b64_tr_b16 v[24:25], v11 offset:34816\n\t"
    "ds_read_b64_tr_b16 v[26:27], v11 offset:34880\n\t"
    "ds_read_b64_tr_b16 v[28:29], v11 offset:35328\n\t"
    "ds_read_b64_tr_b16 v[30:31], v11 offset:35392\n\t"
    "ds_read_b64_tr_b16 v[32:33], v11 offset:35840\n\t"
    "ds_read_b64_tr_b16 v[34:35], v11 offset:35904\n\t"
    "ds_read_b64_tr_b16 v[36:37], v11 offset:36352\n\t"
    "ds_read_b64_tr_b16 v[38:39], v11 offset:36416\n\t"
    "s_waitcnt lgkmcnt(6)\n\t"
    "v_mfma_f32_16x16x32_bf16 v[40:43], v[24:27], v[16:19], 0\n\t"
    "s_waitcnt lgkmcnt(4)\n\t"
    "v_mfma_f32_16x16x32_bf16 v[40:43], v[28:31], v[20:23], v[40:43]\n\t"
    "s_waitcnt lgkmcnt(2)\n\t"
    "v_mfma_f32_16x16x32_bf16 v[40:43], v[32:35], v[56:59], v[40:43]\n\t"
    "s_waitcnt lgkmcnt(0)\n\t"
    "v_mfma_f32_16x16x32_bf16 v[40:43], v[36:39], v[60:63], v[40:43]\n\t"
    "5:\n\t"
    "s_waitcnt vmcnt(0)\n\t"
    "v_max3_u32 v11, v6, v7, v8\n\t"
    "v_max_u32 v11, v11, v9\n\t"
    "v_cmp_eq_u32 vcc, -1, v11\n\t"
    "s_cbranch_vccnz 4f\n\t"
    "6:\n\t"
    "s_nop 2\n\t"
    "v_exp_f32 v28, v40\n\t"
    "v_max3_f32 v11, v40, v41, v42\n\t"
    "v_exp_f32 v29, v41\n\t"
    "v_max_f32 v11, v11, v43\n\t"
    "v_exp_f32 v30, v42\n\t"
    "v_cmp_nge_f32 vcc, %[bg], v11\n\t"
    "v_exp_f32 v31, v43\n\t"
    "s_cbranch_vccnz 10f\n\t"
    "11:\n\t"
    "v_cvt_pk_bf16_f32 v24, v28, v29\n\t"
    "v_cvt_pk_bf16_f32 v25, v30, v31\n\t"
    "s_nop 1\n\t"
    "v_mfma_f32_16x16x16_bf16 v[52:55], v[12:13], v[24:25], v[52:55]\n\t"
    "v_mfma_f32_16x16x16_bf16 v[44:47], v[6:7], v[24:25], v[44:47]\n\t"
    "v_mfma_f32_16x16x16_bf16 v[48:51], v[8:9], v[24:25], v[48:51]\n\t"
    "s_nop 5\n\t"
    "v_rcp_f32 v26, v52\n\t"
    "s_nop 0\n\t"
    "v_pk_mul_f32 v[44:45], v[44:45], v[26:27] op_sel_hi:[1,0]\n\t"
    "v_pk_mul_f32 v[46:47], v[46:47], v[26:27] op_sel_hi:[1,0]\n\t"
    "v_pk_mul_f32 v[48:49], v[48:49], v[26:27] op_sel_hi:[1,0]\n\t"
    "v_pk_mul_f32 v[50:51], v[50:51], v[26:27] op_sel_hi:[1,0]\n\t"
    "v_cvt_pk_bf16_f32 v36, v44, v45\n\t"
    "v_cvt_pk_bf16_f32 v37, v46, v47\n\t"
    "v_cvt_pk_bf16_f32 v38, v48, v49\n\t"
    "v_cvt_pk_bf16_f32 v39, v50, v51\n\t"
    "v_min_u32 v36, -2, v36\n\t"
    "v_min_u32 v37, -2, v37\n\t"
    "v_min_u32 v38, -2, v38\n\t"
    "v_min_u32 v39, -2, v39\n\t"
    "global_store_dwordx4 v15, v[36:39], %[od] sc1\n\t"
    "s_barrier\n\t"
    "s_mov_b32 exec_hi, 0\n\t"
    "global_store_dwordx4 v14, v[6:9], %[kv] sc1\n\t"
    "s_mov_b32 exec_hi, -1\n\t"
    "s_nop 0\n\t"
    "s_waitcnt lgkmcnt(0)\n\t"
    "s_branch 9f\n\t"
    "1:\n\t"
    "s_add_u32 %[sp], %[sp], 1\n\t"
    "s_cmp_ge_u32 %[sp], %[cap]\n\t"
    "s_cbranch_scc1 7f\n\t"
    "global_load_dwordx4 v[40:43], v6, %[ag] sc1\n\t"
    "global_load_dwordx4 v[44:47], v6, %[ag] offset:1024 sc1\n\t"
    "s_waitcnt lgkmcnt(0)\n\t"
    "s_memtime %[tc]\n\t"
    "s_nop 7\n\t"
    "s_branch 3b\n\t"
    "7:\n\t"
    "s_mov_b32 %[cap], 0\n\t"
    "s_branch 2b\n\t"
    "20:\n\t"
    "s_barrier\n\t"
    "s_barrier\n\t"
    "s_barrier\n\t"
    "s_branch 9f\n\t"
    "4:\n\t"
    "s_add_u32 %[sp], %[sp], 0x10000\n\t"
    "s_lshr_b32 %[st], %[sp], 16\n\t"
    "s_cmp_ge_u32 %[st], %[cap]\n\t"
    "s_cbranch_scc1 8f\n\t"
    "s_mov_b32 exec_hi, 0\n\t"
    "global_load_dwordx4 v[6:9], v14, %[vs] sc1\n\t"
    "s_mov_b32 exec_hi, -1\n\t"
    "s_branch 5b\n\t"
    "8:\n\t"
    "s_mov_b32 %[cap], 0\n\t"
    "s_branch 6b\n\t"
    "10:\n\t"
    "v_max_f32 v11, v11, v10\n\t"
    "v_mov_b32 v26, v11\n\t"
    "s_nop 1\n\t"
    "v_permlane16_swap_b32 v11, v26\n\t"
    "v_max_f32 v11, v11, v26\n\t"
    "v_sub_f32 v28, v40, v11\n\t"
    "v_sub_f32 v29, v41, v11\n\t"
    "v_sub_f32 v30, v42, v11\n\t"
    "v_sub_f32 v31, v43, v11\n\t"
    "v_sub_f32 v11, v10, v11\n\t"
    "v_exp_f32 v11, v11\n\t"
    "v_exp_f32 v28, v28\n\t"
    "v_exp_f32 v29, v29\n\t"
    "v_exp_f32 v30, v30\n\t"
    "v_exp_f32 v31, v31\n\t"
    "v_mul_f32 v44, v44, v11\n\t"
    "v_mul_f32 v45, v45, v11\n\t"
    "v_mul_f32 v46, v46, v11\n\t"
    "v_mul_f32 v47, v47, v11\n\t"
    "v_mul_f32 v48, v48, v11\n\t"
    "v_mul_f32 v49, v49, v11\n\t"
    "v_mul_f32 v50, v50, v11\n\t"
    "v_mul_f32 v51, v51, v11\n\t"
    "v_mul_f32 v52, v52, v11\n\t"
    "v_mul_f32 v53, v53, v11\n\t"
    "v_mul_f32 v54, v54, v11\n\t"
    "v_mul_f32 v55, v55, v11\n\t"
    "s_branch 11b\n\t"
    "12:\n\t"
    "v_max3_f32 v10, v24, v25, v26\n\t"
    "v_max3_f32 v40, v27, v28, v29\n\t"
    "v_max3_f32 v10, v10, v30, v31\n\t"
    "v_max3_f32 v10, v10, v40, %[neg]\n\t"
    "v_mov_b32 v40, v10\n\t"
    "s_nop 1\n\t"
    "v_permlane16_swap_b32 v10, v40\n\t"
    "v_max_f32 v10, v10, v40\n\t"
    "v_mov_b32 v40, v10\n\t"
    "s_nop 1\n\t"
    "v_permlane32_swap_b32 v10, v40\n\t"
    "v_max_f32 v10, v10, v40\n\t"
    "v_sub_f32 v24, v24, v10\n\t"
    "v_sub_f32 v25, v25, v10\n\t"
    "v_sub_f32 v26, v26, v10\n\t"
    "v_sub_f32 v27, v27, v10\n\t"
    "v_sub_f32 v28, v28, v10\n\t"
    "v_sub_f32 v29, v29, v10\n\t"
    "v_sub_f32 v30, v30, v10\n\t"
    "v_sub_f32 v31, v31, v10\n\t"
    "s_mov_b32 %[bg], 0x7fc00000\n\t"
    "s_branch 13b\n\t"
    "9:"
    : [tc] "=&s"(tc), [sp] "=&s"(sp), [cap] "+s"(cap), [st] "=&s"(st), [bg] "=&s"(bg)
    : [ag] "s"(ag), [c44] "s"(0x4444u), [kv] "s"(kv), [vs] "s"(vs), [od] "s"(od), [od2] "s"(od + 262144), [neg] "s"(-1.0e38f), [lsc] "s"(LSC), [one] "s"(0x3F803F80u), [big] "s"(24.0f), "{v[64:95]}"(W.w0), "{v[96:127]}"(W.w1), "{v[128:159]}"(W.w2), "{v[160:191]}"(W.w3), "{v[192:223]}"(W.w4), "{v[224:255]}"(W.w5)
    : "v6", "v7", "v8", "v9", "v10", "v11", "v12", "v13", "v14", "v15", "v16", "v17", "v18", "v19", "v20", "v21", "v22", "v23", "v24", "v25", "v26", "v27", "v28", "v29", "v30", "v31", "v32", "v33", "v34", "v35", "v36", "v37", "v38", "v39", "v40", "v41", "v42", "v43", "v44", "v45", "v46", "v47", "v48", "v49", "v50", "v51", "v52", "v53", "v54", "v55", "v56", "v57", "v58", "v59", "v60", "v61", "v62", "v63", "vcc", "scc", "memory");
}
template <> __device__ __forceinline__ void qk_step<3>(const QKW& W, const char* ag, const char* vs, const char* od, const char* kv, unsigned& cap, unsigned& sp,
                                                        unsigned long long& tc, unsigned long long* ts, unsigned& d0, unsigned& d1) {
  unsigned st, bg;
  asm volatile("v_mbcnt_lo_u32_b32 v60, -1, 0\n\t"
    "v_mbcnt_hi_u32_b32 v60, -1, v60\n\t"
    "v_lshlrev_b32 v6, 4, v60\n\t"
    "s_mov_b32 %[sp], 0\n\t"
    "s_setprio 3\n\t"
    "global_load_dwordx4 v[40:43], v6, %[ag] sc1\n\t"
    "global_load_dwordx4 v[44:47], v6, %[ag] offset:1024 sc1\n\t"
    "s_memtime %[tc]\n\t"
    "v_add_u32 v7, 47104, v6\n\t"
    "v_add_u32 v8, 104448, v6\n\t"
    "ds_read_b128 v[24:27], v7 offset:0\n\t"
    "ds_read_b128 v[28:31], v7 offset:1024\n\t"
    "ds_read_b128 v[32:35], v7 offset:8192\n\t"
    "ds_read_b128 v[36:39], v7 offset:9216\n\t"
    "v_lshrrev_b32 v48, 4, v60\n\t"
    "v_and_b32 v49, 15, v60\n\t"
    "v_and_b32 v61, 4, v60\n\t"
    "v_mul_u32_u24 v61, 0x2aaa, v61\n\t"
    "v_lshrrev_b32 v9, 1, v60\n\t"
    "v_and_b32 v9, 2, v9\n\t"
    "v_or3_b32 v9, v9, v61, %[c44]\n\t"
    "v_add_u32 v14, 1536, v6\n\t"
    "v_lshlrev_b32 v61, 2, v48\n\t"
    "v_lshl_or_b32 v10, v49, 4, v61\n\t"
    "v_lshrrev_b32 v50, 2, v49\n\t"
    "v_and_b32 v51, 3, v60\n\t"
    "v_and_b32 v61, 1, v60\n\t"
    "v_lshlrev_b32 v61, 3, v61\n\t"
    "v_lshl_or_b32 v11, v50, 4, v61\n\t"
    "v_lshl_or_b32 v11, v48, 7, v11\n\t"
    "v_xor_b32 v12, v48, v49\n\t"
    "v_lshlrev_b32 v12, 4, v12\n\t"
    "v_lshl_or_b32 v12, v49, 8, v12\n\t"
    "v_lshl_or_b32 v50, v48, 2, v50\n\t"
    "v_or_b32 v51, 12, v51\n\t"
    "v_xor_b32 v51, v51, v50\n\t"
    "v_lshlrev_b32 v13, 4, v51\n\t"
    "v_lshl_or_b32 v13, v50, 8, v13\n\t"
    "v_and_b32 v61, 3, v60\n\t"
    "v_lshlrev_b32 v61, 4, v61\n\t"
    "v_lshl_or_b32 v15, v48, 6, v61\n\t"
    "v_and_b32 v61, 8, v60\n\t"
    "v_lshl_or_b32 v15, v61, 14, v15\n\t"
    "v_and_b32 v61, 4, v60\n\t"
    "v_lshl_or_b32 v15, v61, 8, v15\n\t"
    "v_add_u32 v15, 768, v15\n\t"
    "3:\n\t"
    "v_mov_b32 v16, 0\n\t"
    "v_mov_b32 v17, 0\n\t"
    "v_mov_b32 v18, 0\n\t"
    "v_mov_b32 v19, 0\n\t"
    "v_mov_b32 v20, 0\n\t"
    "v_mov_b32 v21, 0\n\t"
    "v_mov_b32 v22, 0\n\t"
    "v_mov_b32 v23, 0\n\t"
    "s_waitcnt vmcnt(0) lgkmcnt(0)\n\t"
    "14:\n\t"
    "ds_write_b128 v6, v[40:43] offset:6144\n\t"
    "ds_write_b128 v6, v[44:47] offset:7168\n\t"
    "v_smfmac_f32_16x16x64_bf16 v[16:19], v[40:43], v[160:167], v9\n\t"
    "v_max3_u32 v60, v40, v41, v42\n\t"
    "v_max3_u32 v61, v43, v44, v45\n\t"
    "v_smfmac_f32_16x16x64_bf16 v[20:23], v[40:43], v[168:175], v9\n\t"
    "v_max3_u32 v60, v60, v46, v47\n\t"
    "v_max_u32 v60, v60, v61\n\t"
    "v_cmp_eq_u32 vcc, -1, v60\n\t"
    "s_cbranch_vccnz 1f\n\t"
    "2:\n\t"
    "s_waitcnt lgkmcnt(0)\n\t"
    "s_cmp_eq_u64 %[kv], 0\n\t"
    "s_barrier\n\t"
    "s_cbranch_scc1 20f\n\t"
    "ds_read_b128 v[40:43], v6 offset:12288\n\t"
    "ds_read_b128 v[48:51], v6 offset:0\n\t"
    "v_smfmac_f32_16x16x64_bf16 v[16:19], v[44:47], v[176:183], v9\n\t"
    "ds_read_b128 v[52:55], v6 offset:1024\n\t"
    "ds_read_b128 v[56:59], v6 offset:2048\n\t"
    "v_smfmac_f32_16x16x64_bf16 v[20:23], v[44:47], v[184:191], v9\n\t"
    "ds_read_b128 v[60:63], v6 offset:13312\n\t"
    "ds_read_b128 v[44:47], v6 offset:3072\n\t"
    "s_waitcnt lgkmcnt(5)\n\t"
    "v_smfmac_f32_16x16x64_bf16 v[16:19], v[40:43], v[24:31], v9\n\t"
    "ds_read_b128 v[24:27], v7 offset:16384\n\t"
    "ds_read_b128 v[28:31], v7 offset:17408\n\t"
    "v_smfmac_f32_16x16x64_bf16 v[20:23], v[40:43], v[32:39], v9\n\t"
    "ds_read_b128 v[32:35], v7 offset:24576\n\t"
    "ds_read_b128 v[36:39], v7 offset:25600\n\t"
    "ds_read_b128 v[40:43], v6 offset:4096\n\t"
    "s_waitcnt lgkmcnt(9)\n\t"
    "v_smfmac_f32_16x16x64_bf16 v[16:19], v[48:51], v[64:71], v9\n\t"
    "v_smfmac_f32_16x16x64_bf16 v[20:23], v[48:51], v[72:79], v9\n\t"
    "ds_read_b128 v[48:51], v6 offset:5120\n\t"
    "s_waitcnt lgkmcnt(9)\n\t"
    "v_smfmac_f32_16x16x64_bf16 v[16:19], v[52:55], v[80:87], v9\n\t"
    "v_smfmac_f32_16x16x64_bf16 v[20:23], v[52:55], v[88:95], v9\n\t"
    "ds_read_b128 v[52:55], v6 offset:14336\n\t"
    "s_waitcnt lgkmcnt(9)\n\t"
    "v_smfmac_f32_16x16x64_bf16 v[16:19], v[56:59], v[96:103], v9\n\t"
    "v_smfmac_f32_16x16x64_bf16 v[20:23], v[56:59], v[104:111], v9\n\t"
    "ds_read_b128 v[56:59], v6 offset:8192\n\t"
    "s_waitcnt lgkmcnt(6)\n\t"
    "v_smfmac_f32_16x16x64_bf16 v[16:19], v[60:63], v[24:31], v9\n\t"
    "ds_read_b128 v[24:27], v8 offset:0\n\t"
    "ds_read_b128 v[28:31], v8 offset:1024\n\t"
    "s_waitcnt lgkmcnt(6)\n\t"
    "v_smfmac_f32_16x16x64_bf16 v[20:23], v[60:63], v[32:39], v9\n\t"
    "ds_read_b128 v[32:35], v8 offset:8192\n\t"
    "ds_read_b128 v[36:39], v8 offset:9216\n\t"
    "ds_read_b128 v[60:63], v6 offset:9216\n\t"
    "v_smfmac_f32_16x16x64_bf16 v[16:19], v[44:47], v[112:119], v9\n\t"
    "v_smfmac_f32_16x16x64_bf16 v[20:23], v[44:47], v[120:127], v9\n\t"
    "ds_read_b128 v[44:47], v6 offset:10240\n\t"
    "s_waitcnt lgkmcnt(9)\n\t"
    "v_smfmac_f32_16x16x64_bf16 v[16:19], v[40:43], v[128:135], v9\n\t"
    "v_smfmac_f32_16x16x64_bf16 v[20:23], v[40:43], v[136:143], v9\n\t"
    "ds_read_b128 v[40:43], v6 offset:15360\n\t"
    "s_waitcnt lgkmcnt(9)\n\t"
    "v_smfmac_f32_16x16x64_bf16 v[16:19], v[48:51], v[144:151], v9\n\t"
    "v_smfmac_f32_16x16x64_bf16 v[20:23], v[48:51], v[152:159], v9\n\t"
    "ds_read_b128 v[48:51], v6 offset:11264\n\t"
    "s_waitcnt lgkmcnt(6)\n\t"
    "v_smfmac_f32_16x16x64_bf16 v[16:19], v[52:55], v[24:31], v9\n\t"
    "ds_read_b128 v[24:27], v8 offset:16384\n\t"
    "ds_read_b128 v[28:31], v8 offset:17408\n\t"
    "s_waitcnt lgkmcnt(6)\n\t"
    "v_smfmac_f32_16x16x64_bf16 v[20:23], v[52:55], v[32:39], v9\n\t"
    "ds_read_b128 v[32:35], v8 offset:24576\n\t"
    "ds_read_b128 v[36:39], v8 offset:25600\n\t"
    "v_smfmac_f32_16x16x64_bf16 v[16:19], v[56:59], v[192:199], v9\n\t"
    "v_smfmac_f32_16x16x64_bf16 v[20:23], v[56:59], v[200:207], v9\n\t"
    "s_waitcnt lgkmcnt(7)\n\t"
    "v_smfmac_f32_16x16x64_bf16 v[16:19], v[60:63], v[208:215], v9\n\t"
    "v_smfmac_f32_16x16x64_bf16 v[20:23], v[60:63], v[216:223], v9\n\t"
    "s_waitcnt lgkmcnt(6)\n\t"
    "v_smfmac_f32_16x16x64_bf16 v[16:19], v[44:47], v[224:231], v9\n\t"
    "v_smfmac_f32_16x16x64_bf16 v[20:23], v[44:47], v[232:239], v9\n\t"
    "s_waitcnt lgkmcnt(2)\n\t"
    "v_smfmac_f32_16x16x64_bf16 v[16:19], v[40:43], v[24:31], v9\n\t"
    "s_waitcnt lgkmcnt(0)\n\t"
    "v_smfmac_f32_16x16x64_bf16 v[20:23], v[40:43], v[32:39], v9\n\t"
    "v_smfmac_f32_16x16x64_bf16 v[16:19], v[48:51], v[240:247], v9\n\t"
    "v_smfmac_f32_16x16x64_bf16 v[20:23], v[48:51], v[248:255], v9\n\t"
    "v_xor_b32 v7, 64, v12\n\t"
    "v_xor_b32 v8, 0x80, v12\n\t"
    "v_xor_b32 v6, 0xc0, v12\n\t"
    "ds_read_b128 v[24:27], v12 offset:16384\n\t"
    "ds_read_b128 v[28:31], v12 offset:20480\n\t"
    "ds_read_b128 v[32:35], v7 offset:16384\n\t"
    "ds_read_b128 v[36:39], v7 offset:20480\n\t"
    "v_permlane16_swap_b32 v16, v18\n\t"
    "v_permlane16_swap_b32 v17, v19\n\t"
    "v_permlane16_swap_b32 v20, v22\n\t"
    "v_permlane16_swap_b32 v21, v23\n\t"
    "v_add_f32 v16, v16, v18\n\t"
    "v_add_f32 v17, v17, v19\n\t"
    "v_add_f32 v20, v20, v22\n\t"
    "v_mul_f32 v16, %[lsc], v16\n\t"
    "v_mul_f32 v17, %[lsc], v17\n\t"
    "v_cvt_pk_bf16_f32 v16, v16, v17\n\t"
    "v_add_f32 v21, v21, v23\n\t"
    "ds_write_b32 v10, v16 offset:34304\n\t"
    "v_mul_f32 v20, %[lsc], v20\n\t"
    "v_mul_f32 v21, %[lsc], v21\n\t"
    "v_cvt_pk_bf16_f32 v20, v20, v21\n\t"
    "ds_write_b32 v10, v20 offset:34560\n\t"
    "ds_read_b128 v[40:43], v8 offset:16384\n\t"
    "ds_read_b128 v[44:47], v8 offset:20480\n\t"
    "ds_read_b128 v[48:51], v6 offset:16384\n\t"
    "ds_read_b128 v[52:55], v6 offset:20480\n\t"
    "s_waitcnt lgkmcnt(4)\n\t"
    "s_barrier\n\t"
    "ds_read_b64_tr_b16 v[16:17], v11 offset:32768\n\t"
    "ds_read_b64_tr_b16 v[18:19], v11 offset:32832\n\t"
    "ds_read_b64_tr_b16 v[20:21], v11 offset:33280\n\t"
    "ds_read_b64_tr_b16 v[22:23], v11 offset:33344\n\t"
    "ds_read_b64_tr_b16 v[56:57], v11 offset:33792\n\t"
    "ds_read_b64_tr_b16 v[58:59], v11 offset:33856\n\t"
    "ds_read_b64_tr_b16 v[60:61], v11 offset:34304\n\t"
    "ds_read_b64_tr_b16 v[62:63], v11 offset:34368\n\t"
    "v_mov_b32 v6, 0\n\t"
    "v_mov_b32 v7, 0\n\t"
    "v_mov_b32 v8, 0\n\t"
    "v_mov_b32 v9, 0\n\t"
    "s_mov_b32 exec_hi, 0\n\t"
    "global_load_dwordx4 v[6:9], v14, %[vs] sc1\n\t"
    "s_mov_b32 exec_hi, -1\n\t"
    "s_waitcnt lgkmcnt(6)\n\t"
    "v_mfma_f32_16x16x32_bf16 v[24:27], v[24:27], v[16:19], 0\n\t"
    "v_mfma_f32_16x16x32_bf16 v[28:31], v[28:31], v[16:19], 0\n\t"
    "s_waitcnt lgkmcnt(4)\n\t"
    "v_mfma_f32_16x16x32_bf16 v[24:27], v[32:35], v[20:23], v[24:27]\n\t"
    "v_mfma_f32_16x16x32_bf16 v[28:31], v[36:39], v[20:23], v[28:31]\n\t"
    "ds_read_b64_tr_b16 v[32:33], v13 offset:24576\n\t"
    "ds_read_b64_tr_b16 v[34:35], v13 offset:28672\n\t"
    "ds_read_b64_tr_b16 v[36:37], v13 offset:24584\n\t"
    "ds_read_b64_tr_b16 v[38:39], v13 offset:28680\n\t"
    "s_waitcnt lgkmcnt(6)\n\t"
    "v_mfma_f32_16x16x32_bf16 v[24:27], v[40:43], v[56:59], v[24:27]\n\t"
    "v_mfma_f32_16x16x32_bf16 v[28:31], v[44:47], v[56:59], v[28:31]\n\t"
    "s_waitcnt lgkmcnt(4)\n\t"
    "v_mfma_f32_16x16x32_bf16 v[24:27], v[48:51], v[60:63], v[24:27]\n\t"
    "v_mfma_f32_16x16x32_bf16 v[28:31], v[52:55], v[60:63], v[28:31]\n\t"
    "s_setprio 1\n\t"
    "v_mov_b32 v52, %[one]\n\t"
    "v_mov_b32 v53, %[one]\n\t"
    "v_mov_b32 v54, %[one]\n\t"
    "v_mov_b32 v55, %[one]\n\t"
    "s_nop 2\n\t"
    "v_max3_f32 v10, |v24|, |v25|, |v26|\n\t"
    "v_max3_f32 v40, |v27|, |v28|, |v29|\n\t"
    "v_max3_f32 v10, v10, |v30|, |v31|\n\t"
    "v_max_f32 v10, v10, v40\n\t"
    "v_cmp_nge_f32 vcc, %[big], v10\n\t"
    "s_mov_b32 %[bg], %[big]\n\t"
    "v_mov_b32 v10, 0\n\t"
    "s_cbranch_vccnz 12f\n\t"
    "13:\n\t"
    "v_exp_f32 v24, v24\n\t"
    "v_exp_f32 v25, v25\n\t"
    "v_exp_f32 v26, v26\n\t"
    "v_exp_f32 v27, v27\n\t"
    "v_exp_f32 v28, v28\n\t"
    "v_exp_f32 v29, v29\n\t"
    "v_exp_f32 v30, v30\n\t"
    "v_exp_f32 v31, v31\n\t"
    "v_cvt_pk_bf16_f32 v40, v24, v25\n\t"
    "v_cvt_pk_bf16_f32 v41, v26, v27\n\t"
    "v_cvt_pk_bf16_f32 v42, v28, v29\n\t"
    "v_cvt_pk_bf16_f32 v43, v30, v31\n\t"
    "s_setprio 3\n\t"
    "s_waitcnt lgkmcnt(0)\n\t"
    "s_nop 0\n\t"
    "v_mfma_f32_16x16x32_bf16 v[44:47], v[32:35], v[40:43], 0\n\t"
    "v_mfma_f32_16x16x32_bf16 v[48:51], v[36:39], v[40:43], 0\n\t"
    "v_mfma_f32_16x16x32_bf16 v[52:55], v[52:55], v[40:43], 0\n\t"
    "v_mov_b32 v12, 0\n\t"
    "v_mov_b32 v13, 0\n\t"
    "s_mov_b32 exec_hi, 0\n\t"
    "v_mov_b32 v12, %[one]\n\t"
    "v_mov_b32 v13, %[one]\n\t"
    "s_mov_b32 exec_hi, -1\n\t"
    "s_barrier\n\t"
    "ds_read_b64_tr_b16 v[24:25], v11 offset:34816\n\t"
    "ds_read_b64_tr_b16 v[26:27], v11 offset:34880\n\t"
    "ds_read_b64_tr_b16 v[28:29], v11 offset:35328\n\t"
    "ds_read_b64_tr_b16 v[30:31], v11 offset:35392\n\t"
    "ds_read_b64_tr_b16 v[32:33], v11 offset:35840\n\t"
    "ds_read_b64_tr_b16 v[34:35], v11 offset:35904\n\t"
    "ds_read_b64_tr_b16 v[36:37], v11 offset:36352\n\t"
    "ds_read_b64_tr_b16 v[38:39], v11 offset:36416\n\t"
    "s_waitcnt lgkmcnt(6)\n\t"
    "v_mfma_f32_16x16x32_bf16 v[40:43], v[24:27], v[16:19], 0\n\t"
    "s_waitcnt lgkmcnt(4)\n\t"
    "v_mfma_f32_16x16x32_bf16 v[40:43], v[28:31], v[20:23], v[40:43]\n\t"
    "s_waitcnt lgkmcnt(2)\n\t"
    "v_mfma_f32_16x16x32_bf16 v[40:43], v[32:35], v[56:59], v[40:43]\n\t"
    "s_waitcnt lgkmcnt(0)\n\t"
    "v_mfma_f32_16x16x32_bf16 v[40:43], v[36:39], v[60:63], v[40:43]\n\t"
    "5:\n\t"
    "s_waitcnt vmcnt(0)\n\t"
    "v_max3_u32 v11, v6, v7, v8\n\t"
    "v_max_u32 v11, v11, v9\n\t"
    "v_cmp_eq_u32 vcc, -1, v11\n\t"
    "s_cbranch_vccnz 4f\n\t"
    "6:\n\t"
    "s_nop 2\n\t"
    "v_exp_f32 v28, v40\n\t"
    "v_max3_f32 v11, v40, v41, v42\n\t"
    "v_exp_f32 v29, v41\n\t"
    "v_max_f32 v11, v11, v43\n\t"
    "v_exp_f32 v30, v42\n\t"
    "v_cmp_nge_f32 vcc, %[bg], v11\n\t"
    "v_exp_f32 v31, v43\n\t"
    "s_cbranch_vccnz 10f\n\t"
    "11:\n\t"
    "v_cvt_pk_bf16_f32 v24, v28, v29\n\t"
    "v_cvt_pk_bf16_f32 v25, v30, v31\n\t"
    "s_nop 1\n\t"
    "v_mfma_f32_16x16x16_bf16 v[52:55], v[12:13], v[24:25], v[52:55]\n\t"
    "v_mfma_f32_16x16x16_bf16 v[44:47], v[6:7], v[24:25], v[44:47]\n\t"
    "v_mfma_f32_16x16x16_bf16 v[48:51], v[8:9], v[24:25], v[48:51]\n\t"
    "s_nop 5\n\t"
    "v_rcp_f32 v26, v52\n\t"
    "s_nop 0\n\t"
    "v_pk_mul_f32 v[44:45], v[44:45], v[26:27] op_sel_hi:[1,0]\n\t"
    "v_pk_mul_f32 v[46:47], v[46:47], v[26:27] op_sel_hi:[1,0]\n\t"
    "v_pk_mul_f32 v[48:49], v[48:49], v[26:27] op_sel_hi:[1,0]\n\t"
    "v_pk_mul_f32 v[50:51], v[50:51], v[26:27] op_sel_hi:[1,0]\n\t"
    "v_cvt_pk_bf16_f32 v36, v44, v45\n\t"
    "v_cvt_pk_bf16_f32 v37, v46, v47\n\t"
    "v_cvt_pk_bf16_f32 v38, v48, v49\n\t"
    "v_cvt_pk_bf16_f32 v39, v50, v51\n\t"
    "v_min_u32 v36, -2, v36\n\t"
    "v_min_u32 v37, -2, v37\n\t"
    "v_min_u32 v38, -2, v38\n\t"
    "v_min_u32 v39, -2, v39\n\t"
    "global_store_dwordx4 v15, v[36:39], %[od] sc1\n\t"
    "s_barrier\n\t"
    "s_mov_b32 exec_hi, 0\n\t"
    "global_store_dwordx4 v14, v[6:9], %[kv] sc1\n\t"
    "s_mov_b32 exec_hi, -1\n\t"
    "s_nop 0\n\t"
    "s_waitcnt lgkmcnt(0)\n\t"
    "s_branch 9f\n\t"
    "1:\n\t"
    "s_add_u32 %[sp], %[sp], 1\n\t"
    "s_cmp_ge_u32 %[sp], %[cap]\n\t"
    "s_cbranch_scc1 7f\n\t"
    "global_load_dwordx4 v[40:43], v6, %[ag] sc1\n\t"
    "global_load_dwordx4 v[44:47], v6, %[ag] offset:1024 sc1\n\t"
    "s_waitcnt lgkmcnt(0)\n\t"
    "s_memtime %[tc]\n\t"
    "s_nop 7\n\t"
    "s_branch 3b\n\t"
    "7:\n\t"
    "s_mov_b32 %[cap], 0\n\t"
    "s_branch 2b\n\t"
    "20:\n\t"
    "s_barrier\n\t"
    "s_barrier\n\t"
    "s_barrier\n\t"
    "s_branch 9f\n\t"
    "4:\n\t"
    "s_add_u32 %[sp], %[sp], 0x10000\n\t"
    "s_lshr_b32 %[st], %[sp], 16\n\t"
    "s_cmp_ge_u32 %[st], %[cap]\n\t"
    "s_cbranch_scc1 8f\n\t"
    "s_mov_b32 exec_hi, 0\n\t"
    "global_load_dwordx4 v[6:9], v14, %[vs] sc1\n\t"
    "s_mov_b32 exec_hi, -1\n\t"
    "s_branch 5b\n\t"
    "8:\n\t"
    "s_mov_b32 %[cap], 0\n\t"
    "s_branch 6b\n\t"
    "10:\n\t"
    "v_max_f32 v11, v11, v10\n\t"
    "v_mov_b32 v26, v11\n\t"
    "s_nop 1\n\t"
    "v_permlane16_swap_b32 v11, v26\n\t"
    "v_max_f32 v11, v11, v26\n\t"
    "v_sub_f32 v28, v40, v11\n\t"
    "v_sub_f32 v29, v41, v11\n\t"
    "v_sub_f32 v30, v42, v11\n\t"
    "v_sub_f32 v31, v43, v11\n\t"
    "v_sub_f32 v11, v10, v11\n\t"
    "v_exp_f32 v11, v11\n\t"
    "v_exp_f32 v28, v28\n\t"
    "v_exp_f32 v29, v29\n\t"
    "v_exp_f32 v30, v30\n\t"
    "v_exp_f32 v31, v31\n\t"
    "v_mul_f32 v44, v44, v11\n\t"
    "v_mul_f32 v45, v45, v11\n\t"
    "v_mul_f32 v46, v46, v11\n\t"
    "v_mul_f32 v47, v47, v11\n\t"
    "v_mul_f32 v48, v48, v11\n\t"
    "v_mul_f32 v49, v49, v11\n\t"
    "v_mul_f32 v50, v50, v11\n\t"
    "v_mul_f32 v51, v51, v11\n\t"
    "v_mul_f32 v52, v52, v11\n\t"
    "v_mul_f32 v53, v53, v11\n\t"
    "v_mul_f32 v54, v54, v11\n\t"
    "v_mul_f32 v55, v55, v11\n\t"
    "s_branch 11b\n\t"
    "12:\n\t"
    "v_max3_f32 v10, v24, v25, v26\n\t"
    "v_max3_f32 v40, v27, v28, v29\n\t"
    "v_max3_f32 v10, v10, v30, v31\n\t"
    "v_max3_f32 v10, v10, v40, %[neg]\n\t"
    "v_mov_b32 v40, v10\n\t"
    "s_nop 1\n\t"
    "v_permlane16_swap_b32 v10, v40\n\t"
    "v_max_f32 v10, v10, v40\n\t"
    "v_mov_b32 v40, v10\n\t"
    "s_nop 1\n\t"
    "v_permlane32_swap_b32 v10, v40\n\t"
    "v_max_f32 v10, v10, v40\n\t"
    "v_sub_f32 v24, v24, v10\n\t"
    "v_sub_f32 v25, v25, v10\n\t"
    "v_sub_f32 v26, v26, v10\n\t"
    "v_sub_f32 v27, v27, v10\n\t"
    "v_sub_f32 v28, v28, v10\n\t"
    "v_sub_f32 v29, v29, v10\n\t"
    "v_sub_f32 v30, v30, v10\n\t"
    "v_sub_f32 v31, v31, v10\n\t"
    "s_mov_b32 %[bg], 0x7fc00000\n\t"
    "s_branch 13b\n\t"
    "9:"
    : [tc] "=&s"(tc), [sp] "=&s"(sp), [cap] "+s"(cap), [st] "=&s"(st), [bg] "=&s"(bg)
    : [ag] "s"(ag), [c44] "s"(0x4444u), [kv] "s"(kv), [vs] "s"(vs), [od] "s"(od), [od2] "s"(od + 262144), [neg] "s"(-1.0e38f), [lsc] "s"(LSC), [one] "s"(0x3F803F80u), [big] "s"(24.0f), "{v[64:95]}"(W.w0), "{v[96:127]}"(W.w1), "{v[128:159]}"(W.w2), "{v[160:191]}"(W.w3), "{v[192:223]}"(W.w4), "{v[224:255]}"(W.w5)
    : "v6", "v7", "v8", "v9", "v10", "v11", "v12", "v13", "v14", "v15", "v16", "v17", "v18", "v19", "v20", "v21", "v22", "v23", "v24", "v25", "v26", "v27", "v28", "v29", "v30", "v31", "v32", "v33", "v34", "v35", "v36", "v37", "v38", "v39", "v40", "v41", "v42", "v43", "v44", "v45", "v46", "v47", "v48", "v49", "v50", "v51", "v52", "v53", "v54", "v55", "v56", "v57", "v58", "v59", "v60", "v61", "v62", "v63", "vcc", "scc", "memory");
}
template <> __device__ __forceinline__ void qk_step<4>(const QKW& W, const char* ag, const char* vs, const char* od, const char* kv, unsigned& cap, unsigned& sp,
                                                        unsigned long long& tc, unsigned long long* ts, unsigned& d0, unsigned& d1) {
  unsigned st, bg;
  asm volatile("v_mbcnt_lo_u32_b32 v52, -1, 0\n\t"
    "v_mbcnt_hi_u32_b32 v52, -1, v52\n\t"
    "v_lshlrev_b32 v6, 4, v52\n\t"
    "s_mov_b32 %[sp], 0\n\t"
    "global_load_dwordx4 v[36:39], v6, %[ag] sc1\n\t"
    "global_load_dwordx4 v[40:43], v6, %[ag] offset:1024 sc1\n\t"
    "s_memtime %[tc]\n\t"
    "v_add_u32 v7, 73728, v6\n\t"
    "v_add_u32 v8, 131072, v6\n\t"
    "ds_read_b128 v[20:23], v7 offset:8192\n\t"
    "ds_read_b128 v[24:27], v7 offset:9216\n\t"
    "ds_read_b128 v[28:31], v7 offset:0\n\t"
    "ds_read_b128 v[32:35], v7 offset:1024\n\t"
    "v_lshrrev_b32 v44, 4, v52\n\t"
    "v_and_b32 v45, 15, v52\n\t"
    "v_and_b32 v53, 4, v52\n\t"
    "v_mul_u32_u24 v53, 0x2aaa, v53\n\t"
    "v_lshrrev_b32 v9, 1, v52\n\t"
    "v_and_b32 v9, 2, v9\n\t"
    "v_or3_b32 v9, v9, v53, %[c44]\n\t"
    "v_lshlrev_b32 v53, 2, v44\n\t"
    "v_lshl_or_b32 v10, v45, 4, v53\n\t"
    "3:\n\t"
    "v_mov_b32 v12, 0\n\t"
    "v_mov_b32 v13, 0\n\t"
    "v_mov_b32 v14, 0\n\t"
    "v_mov_b32 v15, 0\n\t"
    "v_mov_b32 v16, 0\n\t"
    "v_mov_b32 v17, 0\n\t"
    "v_mov_b32 v18, 0\n\t"
    "v_mov_b32 v19, 0\n\t"
    "s_waitcnt vmcnt(0) lgkmcnt(0)\n\t"
    "14:\n\t"
    "ds_write_b128 v6, v[36:39] offset:8192\n\t"
    "ds_write_b128 v6, v[40:43] offset:9216\n\t"
    "v_smfmac_f32_16x16x64_bf16 v[12:15], v[36:39], v[128:135], v9\n\t"
    "v_max3_u32 v52, v36, v37, v38\n\t"
    "v_max3_u32 v53, v39, v40, v41\n\t"
    "v_smfmac_f32_16x16x64_bf16 v[16:19], v[36:39], v[136:143], v9\n\t"
    "v_max3_u32 v52, v52, v42, v43\n\t"
    "v_max_u32 v52, v52, v53\n\t"
    "v_cmp_eq_u32 vcc, -1, v52\n\t"
    "s_cbranch_vccnz 1f\n\t"
    "2:\n\t"
    "s_waitcnt lgkmcnt(0)\n\t"
    "s_barrier\n\t"
    "s_memrealtime s[36:37]\n\t"
    "ds_read_b128 v[36:39], v6 offset:0\n\t"
    "ds_read_b128 v[44:47], v6 offset:4096\n\t"
    "v_smfmac_f32_16x16x64_bf16 v[12:15], v[40:43], v[144:151], v9\n\t"
    "ds_read_b128 v[48:51], v6 offset:5120\n\t"
    "ds_read_b128 v[52:55], v6 offset:6144\n\t"
    "v_smfmac_f32_16x16x64_bf16 v[16:19], v[40:43], v[152:159], v9\n\t"
    "ds_read_b128 v[40:43], v6 offset:1024\n\t"
    "s_waitcnt lgkmcnt(0)\n\t"
    "s_mov_b32 s38, s36\n\t"
    "s_min_u32 s37, %[d0], %[d1]\n\t"
    "s_add_u32 s36, s36, s37\n\t"
    "s_and_b32 s36, s36, 0xffffff\n\t"
    "s_or_b32 s36, s36, %[sn]\n\t"
    "v_lshlrev_b32 v11, 10, v6\n\t"
    "v_mov_b32 v5, s36\n\t"
    "s_mov_b64 exec, 15\n\t"
    "global_store_dword v11, v5, %[vs] offset:-4096 sc1\n\t"
    "s_mov_b64 exec, -1\n\t"
    "s_sleep 0\n\t"
    "v_smfmac_f32_16x16x64_bf16 v[12:15], v[36:39], v[56:63], v9\n\t"
    "v_smfmac_f32_16x16x64_bf16 v[16:19], v[36:39], v[28:35], v9\n\t"
    "ds_read_b128 v[28:31], v7 offset:16384\n\t"
    "ds_read_b128 v[32:35], v7 offset:17408\n\t"
    "ds_read_b128 v[36:39], v6 offset:7168\n\t"
    "v_smfmac_f32_16x16x64_bf16 v[12:15], v[44:47], v[64:71], v9\n\t"
    "v_smfmac_f32_16x16x64_bf16 v[16:19], v[44:47], v[72:79], v9\n\t"
    "ds_read_b128 v[44:47], v6 offset:10240\n\t"
    "v_smfmac_f32_16x16x64_bf16 v[12:15], v[48:51], v[80:87], v9\n\t"
    "v_smfmac_f32_16x16x64_bf16 v[16:19], v[48:51], v[88:95], v9\n\t"
    "ds_read_b128 v[48:51], v6 offset:11264\n\t"
    "v_smfmac_f32_16x16x64_bf16 v[12:15], v[52:55], v[96:103], v9\n\t"
    "v_smfmac_f32_16x16x64_bf16 v[16:19], v[52:55], v[104:111], v9\n\t"
    "ds_read_b128 v[52:55], v6 offset:2048\n\t"
    "v_smfmac_f32_16x16x64_bf16 v[12:15], v[40:43], v[20:27], v9\n\t"
    "ds_read_b128 v[20:23], v8 offset:0\n\t"
    "ds_read_b128 v[24:27], v8 offset:1024\n\t"
    "s_waitcnt lgkmcnt(6)\n\t"
    "v_smfmac_f32_16x16x64_bf16 v[16:19], v[40:43], v[28:35], v9\n\t"
    "ds_read_b128 v[28:31], v8 offset:8192\n\t"
    "ds_read_b128 v[32:35], v8 offset:9216\n\t"
    "ds_read_b128 v[40:43], v6 offset:12288\n\t"
    "s_waitcnt lgkmcnt(8)\n\t"
    "v_smfmac_f32_16x16x64_bf16 v[12:15], v[36:39], v[112:119], v9\n\t"
    "v_smfmac_f32_16x16x64_bf16 v[16:19], v[36:39], v[120:127], v9\n\t"
    "ds_read_b128 v[36:39], v6 offset:13312\n\t"
    "s_waitcnt lgkmcnt(8)\n\t"
    "v_smfmac_f32_16x16x64_bf16 v[12:15], v[44:47], v[160:167], v9\n\t"
    "v_smfmac_f32_16x16x64_bf16 v[16:19], v[44:47], v[168:175], v9\n\t"
    "ds_read_b128 v[44:47], v6 offset:14336\n\t"
    "s_barrier\n\t"
    "s_setprio 2\n\t"
    "s_waitcnt lgkmcnt(8)\n\t"
    "v_smfmac_f32_16x16x64_bf16 v[12:15], v[48:51], v[176:183], v9\n\t"
    "v_smfmac_f32_16x16x64_bf16 v[16:19], v[48:51], v[184:191], v9\n\t"
    "ds_read_b128 v[48:51], v6 offset:3072\n\t"
    "s_waitcnt lgkmcnt(6)\n\t"
    "v_smfmac_f32_16x16x64_bf16 v[12:15], v[52:55], v[20:27], v9\n\t"
    "ds_read_b128 v[20:23], v8 offset:16384\n\t"
    "ds_read_b128 v[24:27], v8 offset:17408\n\t"
    "s_waitcnt lgkmcnt(6)\n\t"
    "v_smfmac_f32_16x16x64_bf16 v[16:19], v[52:55], v[28:35], v9\n\t"
    "ds_read_b128 v[28:31], v8 offset:24576\n\t"
    "ds_read_b128 v[32:35], v8 offset:25600\n\t"
    "ds_read_b128 v[52:55], v6 offset:15360\n\t"
    "s_waitcnt lgkmcnt(8)\n\t"
    "v_smfmac_f32_16x16x64_bf16 v[12:15], v[40:43], v[192:199], v9\n\t"
    "v_smfmac_f32_16x16x64_bf16 v[16:19], v[40:43], v[200:207], v9\n\t"
    "s_waitcnt lgkmcnt(7)\n\t"
    "v_smfmac_f32_16x16x64_bf16 v[12:15], v[36:39], v[208:215], v9\n\t"
    "v_smfmac_f32_16x16x64_bf16 v[16:19], v[36:39], v[216:223], v9\n\t"
    "s_waitcnt lgkmcnt(6)\n\t"
    "v_smfmac_f32_16x16x64_bf16 v[12:15], v[44:47], v[224:231], v9\n\t"
    "v_smfmac_f32_16x16x64_bf16 v[16:19], v[44:47], v[232:239], v9\n\t"
    "s_waitcnt lgkmcnt(3)\n\t"
    "v_smfmac_f32_16x16x64_bf16 v[12:15], v[48:51], v[20:27], v9\n\t"
    "s_waitcnt lgkmcnt(1)\n\t"
    "v_smfmac_f32_16x16x64_bf16 v[16:19], v[48:51], v[28:35], v9\n\t"
    "s_waitcnt lgkmcnt(0)\n\t"
    "v_smfmac_f32_16x16x64_bf16 v[12:15], v[52:55], v[240:247], v9\n\t"
    "v_smfmac_f32_16x16x64_bf16 v[16:19], v[52:55], v[248:255], v9\n\t"
    "s_nop 6\n\t"
    "v_permlane16_swap_b32 v12, v14\n\t"
    "v_permlane16_swap_b32 v13, v15\n\t"
    "v_permlane16_swap_b32 v16, v18\n\t"
    "v_permlane16_swap_b32 v17, v19\n\t"
    "v_add_f32 v12, v12, v14\n\t"
    "v_add_f32 v13, v13, v15\n\t"
    "v_add_f32 v16, v16, v18\n\t"
    "v_cvt_pk_bf16_f32 v12, v12, v13\n\t"
    "v_add_f32 v17, v17, v19\n\t"
    "ds_write_b32 v10, v12 offset:34816\n\t"
    "v_cvt_pk_bf16_f32 v16, v16, v17\n\t"
    "ds_write_b32 v10, v16 offset:35072\n\t"
    "s_waitcnt lgkmcnt(0)\n\t"
    "s_setprio 0\n\t"
    "s_barrier\n\t"
    "s_memrealtime s[36:37]\n\t"
    "s_waitcnt lgkmcnt(0)\n\t"
    "s_and_b32 s36, s36, 0xffffff\n\t"
    "s_or_b32 s36, s36, %[sn]\n\t"
    "v_lshlrev_b32 v7, 10, v6\n\t"
    "v_mov_b32 v8, s36\n\t"
    "s_mov_b64 exec, 15\n\t"
    "global_store_dword v7, v8, %[vs] offset:0 sc1\n\t"
    "s_mov_b64 exec, -1\n\t"
    "s_barrier\n\t"
    "s_memrealtime s[36:37]\n\t"
    "s_mov_b32 %[d1], %[d0]\n\t"
    "s_waitcnt lgkmcnt(0)\n\t"
    "s_sub_u32 %[d0], s36, s38\n\t"
    "s_min_u32 %[d0], %[d0], 200\n\t"
    "s_waitcnt lgkmcnt(0)\n\t"
    "s_branch 9f\n\t"
    "1:\n\t"
    "s_add_u32 %[sp], %[sp], 1\n\t"
    "s_cmp_ge_u32 %[sp], %[cap]\n\t"
    "s_cbranch_scc1 7f\n\t"
    "global_load_dwordx4 v[36:39], v6, %[ag] sc1\n\t"
    "global_load_dwordx4 v[40:43], v6, %[ag] offset:1024 sc1\n\t"
    "s_waitcnt lgkmcnt(0)\n\t"
    "s_memtime %[tc]\n\t"
    "s_nop 7\n\t"
    "s_branch 3b\n\t"
    "7:\n\t"
    "s_mov_b32 %[cap], 0\n\t"
    "s_branch 2b\n\t"
    "9:"
    : [tc] "=&s"(tc), [sp] "=&s"(sp), [cap] "+s"(cap), [d0] "+s"(d0), [d1] "+s"(d1)
    : [ag] "s"(ag), [c44] "s"(0x4444u), [vs] "s"(vs), [sn] "s"((unsigned)(size_t)od), "{v[64:95]}"(W.w0), "{v[96:127]}"(W.w1), "{v[128:159]}"(W.w2), "{v[160:191]}"(W.w3), "{v[192:223]}"(W.w4), "{v[224:255]}"(W.w5), "{v[56:63]}"(W.x)
    : "v5", "v6", "v7", "v8", "v9", "v10", "v11", "v12", "v13", "v14", "v15", "v16", "v17", "v18", "v19", "v20", "v21", "v22", "v23", "v24", "v25", "v26", "v27", "v28", "v29", "v30", "v31", "v32", "v33", "v34", "v35", "v36", "v37", "v38", "v39", "v40", "v41", "v42", "v43", "v44", "v45", "v46", "v47", "v48", "v49", "v50", "v51", "v52", "v53", "v54", "v55", "vcc", "scc", "memory", "s36", "s37", "s38");
}
template <> __device__ __forceinline__ void qk_step<5>(const QKW& W, const char* ag, const char* vs, const char* od, const char* kv, unsigned& cap, unsigned& sp,
                                                        unsigned long long& tc, unsigned long long* ts, unsigned& d0, unsigned& d1) {
  unsigned st, bg;
  asm volatile("v_mbcnt_lo_u32_b32 v52, -1, 0\n\t"
    "v_mbcnt_hi_u32_b32 v52, -1, v52\n\t"
    "v_lshlrev_b32 v6, 4, v52\n\t"
    "s_mov_b32 %[sp], 0\n\t"
    "global_load_dwordx4 v[36:39], v6, %[ag] sc1\n\t"
    "global_load_dwordx4 v[40:43], v6, %[ag] offset:1024 sc1\n\t"
    "s_memtime %[tc]\n\t"
    "v_add_u32 v7, 75776, v6\n\t"
    "v_add_u32 v8, 133120, v6\n\t"
    "ds_read_b128 v[20:23], v7 offset:8192\n\t"
    "ds_read_b128 v[24:27], v7 offset:9216\n\t"
    "ds_read_b128 v[28:31], v7 offset:0\n\t"
    "ds_read_b128 v[32:35], v7 offset:1024\n\t"
    "v_lshrrev_b32 v44, 4, v52\n\t"
    "v_and_b32 v45, 15, v52\n\t"
    "v_and_b32 v53, 4, v52\n\t"
    "v_mul_u32_u24 v53, 0x2aaa, v53\n\t"
    "v_lshrrev_b32 v9, 1, v52\n\t"
    "v_and_b32 v9, 2, v9\n\t"
    "v_or3_b32 v9, v9, v53, %[c44]\n\t"
    "v_lshlrev_b32 v53, 2, v44\n\t"
    "v_lshl_or_b32 v10, v45, 4, v53\n\t"
    "v_and_b32 v53, 1, v52\n\t"
    "v_lshlrev_b32 v53, 3, v53\n\t"
    "v_lshl_or_b32 v11, v44, 7, v53\n\t"
    "v_and_b32 v53, 12, v52\n\t"
    "v_lshl_or_b32 v11, v53, 2, v11\n\t"
    "v_and_b32 v53, 2, v52\n\t"
    "v_lshl_or_b32 v11, v53, 8, v11\n\t"
    "3:\n\t"
    "v_mov_b32 v12, 0\n\t"
    "v_mov_b32 v13, 0\n\t"
    "v_mov_b32 v14, 0\n\t"
    "v_mov_b32 v15, 0\n\t"
    "v_mov_b32 v16, 0\n\t"
    "v_mov_b32 v17, 0\n\t"
    "v_mov_b32 v18, 0\n\t"
    "v_mov_b32 v19, 0\n\t"
    "s_waitcnt vmcnt(0) lgkmcnt(0)\n\t"
    "14:\n\t"
    "ds_write_b128 v6, v[36:39] offset:10240\n\t"
    "ds_write_b128 v6, v[40:43] offset:11264\n\t"
    "v_smfmac_f32_16x16x64_bf16 v[12:15], v[36:39], v[160:167], v9\n\t"
    "v_max3_u32 v52, v36, v37, v38\n\t"
    "v_max3_u32 v53, v39, v40, v41\n\t"
    "v_smfmac_f32_16x16x64_bf16 v[16:19], v[36:39], v[168:175], v9\n\t"
    "v_max3_u32 v52, v52, v42, v43\n\t"
    "v_max_u32 v52, v52, v53\n\t"
    "v_cmp_eq_u32 vcc, -1, v52\n\t"
    "s_cbranch_vccnz 1f\n\t"
    "2:\n\t"
    "s_waitcnt lgkmcnt(0)\n\t"
    "s_barrier\n\t"
    "ds_read_b128 v[36:39], v6 offset:0\n\t"
    "ds_read_b128 v[44:47], v6 offset:4096\n\t"
    "v_smfmac_f32_16x16x64_bf16 v[12:15], v[40:43], v[176:183], v9\n\t"
    "ds_read_b128 v[48:51], v6 offset:5120\n\t"
    "ds_read_b128 v[52:55], v6 offset:6144\n\t"
    "v_smfmac_f32_16x16x64_bf16 v[16:19], v[40:43], v[184:191], v9\n\t"
    "ds_read_b128 v[40:43], v6 offset:1024\n\t"
    "s_sleep 0\n\t"
    "s_waitcnt lgkmcnt(4)\n\t"
    "v_smfmac_f32_16x16x64_bf16 v[12:15], v[36:39], v[56:63], v9\n\t"
    "v_smfmac_f32_16x16x64_bf16 v[16:19], v[36:39], v[28:35], v9\n\t"
    "ds_read_b128 v[28:31], v7 offset:16384\n\t"
    "ds_read_b128 v[32:35], v7 offset:17408\n\t"
    "ds_read_b128 v[36:39], v6 offset:7168\n\t"
    "s_waitcnt lgkmcnt(6)\n\t"
    "v_smfmac_f32_16x16x64_bf16 v[12:15], v[44:47], v[64:71], v9\n\t"
    "v_smfmac_f32_16x16x64_bf16 v[16:19], v[44:47], v[72:79], v9\n\t"
    "ds_read_b128 v[44:47], v6 offset:8192\n\t"
    "s_waitcnt lgkmcnt(6)\n\t"
    "v_smfmac_f32_16x16x64_bf16 v[12:15], v[48:51], v[80:87], v9\n\t"
    "v_smfmac_f32_16x16x64_bf16 v[16:19], v[48:51], v[88:95], v9\n\t"
    "ds_read_b128 v[48:51], v6 offset:9216\n\t"
    "s_waitcnt lgkmcnt(6)\n\t"
    "v_smfmac_f32_16x16x64_bf16 v[12:15], v[52:55], v[96:103], v9\n\t"
    "v_smfmac_f32_16x16x64_bf16 v[16:19], v[52:55], v[104:111], v9\n\t"
    "ds_read_b128 v[52:55], v6 offset:2048\n\t"
    "s_waitcnt lgkmcnt(6)\n\t"
    "v_smfmac_f32_16x16x64_bf16 v[12:15], v[40:43], v[20:27], v9\n\t"
    "ds_read_b128 v[20:23], v8 offset:0\n\t"
    "ds_read_b128 v[24:27], v8 offset:1024\n\t"
    "s_waitcnt lgkmcnt(6)\n\t"
    "v_smfmac_f32_16x16x64_bf16 v[16:19], v[40:43], v[28:35], v9\n\t"
    "ds_read_b128 v[28:31], v8 offset:8192\n\t"
    "ds_read_b128 v[32:35], v8 offset:9216\n\t"
    "ds_read_b128 v[40:43], v6 offset:12288\n\t"
    "s_waitcnt lgkmcnt(8)\n\t"
    "v_smfmac_f32_16x16x64_bf16 v[12:15], v[36:39], v[112:119], v9\n\t"
    "v_smfmac_f32_16x16x64_bf16 v[16:19], v[36:39], v[120:127], v9\n\t"
    "ds_read_b128 v[36:39], v6 offset:13312\n\t"
    "s_waitcnt lgkmcnt(8)\n\t"
    "v_smfmac_f32_16x16x64_bf16 v[12:15], v[44:47], v[128:135], v9\n\t"
    "v_smfmac_f32_16x16x64_bf16 v[16:19], v[44:47], v[136:143], v9\n\t"
    "ds_read_b128 v[44:47], v6 offset:14336\n\t"
    "s_barrier\n\t"
    "s_setprio 2\n\t"
    "s_waitcnt lgkmcnt(8)\n\t"
    "v_smfmac_f32_16x16x64_bf16 v[12:15], v[48:51], v[144:151], v9\n\t"
    "v_smfmac_f32_16x16x64_bf16 v[16:19], v[48:51], v[152:159], v9\n\t"
    "ds_read_b128 v[48:51], v6 offset:3072\n\t"
    "s_waitcnt lgkmcnt(6)\n\t"
    "v_smfmac_f32_16x16x64_bf16 v[12:15], v[52:55], v[20:27], v9\n\t"
    "ds_read_b128 v[20:23], v8 offset:16384\n\t"
    "ds_read_b128 v[24:27], v8 offset:17408\n\t"
    "s_waitcnt lgkmcnt(6)\n\t"
    "v_smfmac_f32_16x16x64_bf16 v[16:19], v[52:55], v[28:35], v9\n\t"
    "ds_read_b128 v[28:31], v8 offset:24576\n\t"
    "ds_read_b128 v[32:35], v8 offset:25600\n\t"
    "ds_read_b128 v[52:55], v6 offset:15360\n\t"
    "s_waitcnt lgkmcnt(8)\n\t"
    "v_smfmac_f32_16x16x64_bf16 v[12:15], v[40:43], v[192:199], v9\n\t"
    "v_smfmac_f32_16x16x64_bf16 v[16:19], v[40:43], v[200:207], v9\n\t"
    "s_waitcnt lgkmcnt(7)\n\t"
    "v_smfmac_f32_16x16x64_bf16 v[12:15], v[36:39], v[208:215], v9\n\t"
    "v_smfmac_f32_16x16x64_bf16 v[16:19], v[36:39], v[216:223], v9\n\t"
    "s_waitcnt lgkmcnt(6)\n\t"
    "v_smfmac_f32_16x16x64_bf16 v[12:15], v[44:47], v[224:231], v9\n\t"
    "v_smfmac_f32_16x16x64_bf16 v[16:19], v[44:47], v[232:239], v9\n\t"
    "s_waitcnt lgkmcnt(3)\n\t"
    "v_smfmac_f32_16x16x64_bf16 v[12:15], v[48:51], v[20:27], v9\n\t"
    "s_waitcnt lgkmcnt(1)\n\t"
    "v_smfmac_f32_16x16x64_bf16 v[16:19], v[48:51], v[28:35], v9\n\t"
    "s_waitcnt lgkmcnt(0)\n\t"
    "v_smfmac_f32_16x16x64_bf16 v[12:15], v[52:55], v[240:247], v9\n\t"
    "v_smfmac_f32_16x16x64_bf16 v[16:19], v[52:55], v[248:255], v9\n\t"
    "s_nop 6\n\t"
    "v_permlane16_swap_b32 v12, v14\n\t"
    "v_permlane16_swap_b32 v13, v15\n\t"
    "v_permlane16_swap_b32 v16, v18\n\t"
    "v_permlane16_swap_b32 v17, v19\n\t"
    "v_add_f32 v12, v12, v14\n\t"
    "v_add_f32 v13, v13, v15\n\t"
    "v_add_f32 v16, v16, v18\n\t"
    "v_cvt_pk_bf16_f32 v12, v12, v13\n\t"
    "v_add_f32 v17, v17, v19\n\t"
    "ds_write_b32 v10, v12 offset:35328\n\t"
    "v_cvt_pk_bf16_f32 v16, v16, v17\n\t"
    "ds_write_b32 v10, v16 offset:35584\n\t"
    "s_waitcnt lgkmcnt(0)\n\t"
    "s_setprio 0\n\t"
    "s_barrier\n\t"
    "ds_read_b64_tr_b16 v[36:37], v11 offset:34816\n\t"
    "ds_read_b64_tr_b16 v[38:39], v11 offset:34880\n\t"
    "ds_read_b64_tr_b16 v[40:41], v11 offset:35840\n\t"
    "ds_read_b64_tr_b16 v[42:43], v11 offset:35904\n\t"
    "s_waitcnt lgkmcnt(0)\n\t"
    "v_min_u32 v36, -2, v36\n\t"
    "v_min_u32 v37, -2, v37\n\t"
    "v_min_u32 v38, -2, v38\n\t"
    "v_min_u32 v39, -2, v39\n\t"
    "v_min_u32 v40, -2, v40\n\t"
    "v_min_u32 v41, -2, v41\n\t"
    "v_min_u32 v42, -2, v42\n\t"
    "v_min_u32 v43, -2, v43\n\t"
    "global_store_dwordx4 v6, v[36:39], %[vs] sc1\n\t"
    "global_store_dwordx4 v6, v[40:43], %[vs] offset:1024 sc1\n\t"
    "s_nop 1\n\t"
    "s_barrier\n\t"
    "s_waitcnt lgkmcnt(0)\n\t"
    "s_branch 9f\n\t"
    "1:\n\t"
    "s_add_u32 %[sp], %[sp], 1\n\t"
    "s_cmp_ge_u32 %[sp], %[cap]\n\t"
    "s_cbranch_scc1 7f\n\t"
    "global_load_dwordx4 v[36:39], v6, %[ag] sc1\n\t"
    "global_load_dwordx4 v[40:43], v6, %[ag] offset:1024 sc1\n\t"
    "s_waitcnt lgkmcnt(0)\n\t"
    "s_memtime %[tc]\n\t"
    "s_nop 7\n\t"
    "s_branch 3b\n\t"
    "7:\n\t"
    "s_mov_b32 %[cap], 0\n\t"
    "s_branch 2b\n\t"
    "9:"
    : [tc] "=&s"(tc), [sp] "=&s"(sp), [cap] "+s"(cap)
    : [ag] "s"(ag), [c44] "s"(0x4444u), [vs] "s"(vs), "{v[64:95]}"(W.w0), "{v[96:127]}"(W.w1), "{v[128:159]}"(W.w2), "{v[160:191]}"(W.w3), "{v[192:223]}"(W.w4), "{v[224:255]}"(W.w5), "{v[56:63]}"(W.x)
    : "v6", "v7", "v8", "v9", "v10", "v11", "v12", "v13", "v14", "v15", "v16", "v17", "v18", "v19", "v20", "v21", "v22", "v23", "v24", "v25", "v26", "v27", "v28", "v29", "v30", "v31", "v32", "v33", "v34", "v35", "v36", "v37", "v38", "v39", "v40", "v41", "v42", "v43", "v44", "v45", "v46", "v47", "v48", "v49", "v50", "v51", "v52", "v53", "v54", "v55", "vcc", "scc", "memory");
}
template <> __device__ __forceinline__ void qk_step<6>(const QKW& W, const char* ag, const char* vs, const char* od, const char* kv, unsigned& cap, unsigned& sp,
                                                        unsigned long long& tc, unsigned long long* ts, unsigned& d0, unsigned& d1) {
  unsigned st, bg;
  asm volatile("v_mbcnt_lo_u32_b32 v52, -1, 0\n\t"
    "v_mbcnt_hi_u32_b32 v52, -1, v52\n\t"
    "v_lshlrev_b32 v6, 4, v52\n\t"
    "s_mov_b32 %[sp], 0\n\t"
    "global_load_dwordx4 v[36:39], v6, %[ag] sc1\n\t"
    "global_load_dwordx4 v[40:43], v6, %[ag] offset:1024 sc1\n\t"
    "s_memtime %[tc]\n\t"
    "v_add_u32 v7, 77824, v6\n\t"
    "v_add_u32 v8, 135168, v6\n\t"
    "ds_read_b128 v[20:23], v7 offset:8192\n\t"
    "ds_read_b128 v[24:27], v7 offset:9216\n\t"
    "ds_read_b128 v[28:31], v7 offset:0\n\t"
    "ds_read_b128 v[32:35], v7 offset:1024\n\t"
    "v_lshrrev_b32 v44, 4, v52\n\t"
    "v_and_b32 v45, 15, v52\n\t"
    "v_and_b32 v53, 4, v52\n\t"
    "v_mul_u32_u24 v53, 0x2aaa, v53\n\t"
    "v_lshrrev_b32 v9, 1, v52\n\t"
    "v_and_b32 v9, 2, v9\n\t"
    "v_or3_b32 v9, v9, v53, %[c44]\n\t"
    "v_lshlrev_b32 v53, 2, v44\n\t"
    "v_lshl_or_b32 v10, v45, 4, v53\n\t"
    "3:\n\t"
    "v_mov_b32 v12, 0\n\t"
    "v_mov_b32 v13, 0\n\t"
    "v_mov_b32 v14, 0\n\t"
    "v_mov_b32 v15, 0\n\t"
    "v_mov_b32 v16, 0\n\t"
    "v_mov_b32 v17, 0\n\t"
    "v_mov_b32 v18, 0\n\t"
    "v_mov_b32 v19, 0\n\t"
    "s_waitcnt vmcnt(0) lgkmcnt(0)\n\t"
    "14:\n\t"
    "ds_write_b128 v6, v[36:39] offset:12288\n\t"
    "ds_write_b128 v6, v[40:43] offset:13312\n\t"
    "v_smfmac_f32_16x16x64_bf16 v[12:15], v[36:39], v[192:199], v9\n\t"
    "v_max3_u32 v52, v36, v37, v38\n\t"
    "v_max3_u32 v53, v39, v40, v41\n\t"
    "v_smfmac_f32_16x16x64_bf16 v[16:19], v[36:39], v[200:207], v9\n\t"
    "v_max3_u32 v52, v52, v42, v43\n\t"
    "v_max_u32 v52, v52, v53\n\t"
    "v_cmp_eq_u32 vcc, -1, v52\n\t"
    "s_cbranch_vccnz 1f\n\t"
    "2:\n\t"
    "s_waitcnt lgkmcnt(0)\n\t"
    "s_barrier\n\t"
    "ds_read_b128 v[36:39], v6 offset:0\n\t"
    "ds_read_b128 v[44:47], v6 offset:4096\n\t"
    "v_smfmac_f32_16x16x64_bf16 v[12:15], v[40:43], v[208:215], v9\n\t"
    "ds_read_b128 v[48:51], v6 offset:5120\n\t"
    "ds_read_b128 v[52:55], v6 offset:6144\n\t"
    "v_smfmac_f32_16x16x64_bf16 v[16:19], v[40:43], v[216:223], v9\n\t"
    "ds_read_b128 v[40:43], v6 offset:1024\n\t"
    "s_sleep 0\n\t"
    "s_waitcnt lgkmcnt(4)\n\t"
    "v_smfmac_f32_16x16x64_bf16 v[12:15], v[36:39], v[56:63], v9\n\t"
    "v_smfmac_f32_16x16x64_bf16 v[16:19], v[36:39], v[28:35], v9\n\t"
    "ds_read_b128 v[28:31], v7 offset:16384\n\t"
    "ds_read_b128 v[32:35], v7 offset:17408\n\t"
    "ds_read_b128 v[36:39], v6 offset:7168\n\t"
    "s_waitcnt lgkmcnt(6)\n\t"
    "v_smfmac_f32_16x16x64_bf16 v[12:15], v[44:47], v[64:71], v9\n\t"
    "v_smfmac_f32_16x16x64_bf16 v[16:19], v[44:47], v[72:79], v9\n\t"
    "ds_read_b128 v[44:47], v6 offset:8192\n\t"
    "s_waitcnt lgkmcnt(6)\n\t"
    "v_smfmac_f32_16x16x64_bf16 v[12:15], v[48:51], v[80:87], v9\n\t"
    "v_smfmac_f32_16x16x64_bf16 v[16:19], v[48:51], v[88:95], v9\n\t"
    "ds_read_b128 v[48:51], v6 offset:9216\n\t"
    "s_waitcnt lgkmcnt(6)\n\t"
    "v_smfmac_f32_16x16x64_bf16 v[12:15], v[52:55], v[96:103], v9\n\t"
    "v_smfmac_f32_16x16x64_bf16 v[16:19], v[52:55], v[104:111], v9\n\t"
    "ds_read_b128 v[52:55], v6 offset:2048\n\t"
    "s_waitcnt lgkmcnt(6)\n\t"
    "v_smfmac_f32_16x16x64_bf16 v[12:15], v[40:43], v[20:27], v9\n\t"
    "ds_read_b128 v[20:23], v8 offset:0\n\t"
    "ds_read_b128 v[24:27], v8 offset:1024\n\t"
    "s_waitcnt lgkmcnt(6)\n\t"
    "v_smfmac_f32_16x16x64_bf16 v[16:19], v[40:43], v[28:35], v9\n\t"
    "ds_read_b128 v[28:31], v8 offset:8192\n\t"
    "ds_read_b128 v[32:35], v8 offset:9216\n\t"
    "ds_read_b128 v[40:43], v6 offset:10240\n\t"
    "s_waitcnt lgkmcnt(8)\n\t"
    "v_smfmac_f32_16x16x64_bf16 v[12:15], v[36:39], v[112:119], v9\n\t"
    "v_smfmac_f32_16x16x64_bf16 v[16:19], v[36:39], v[120:127], v9\n\t"
    "ds_read_b128 v[36:39], v6 offset:11264\n\t"
    "s_waitcnt lgkmcnt(8)\n\t"
    "v_smfmac_f32_16x16x64_bf16 v[12:15], v[44:47], v[128:135], v9\n\t"
    "v_smfmac_f32_16x16x64_bf16 v[16:19], v[44:47], v[136:143], v9\n\t"
    "ds_read_b128 v[44:47], v6 offset:14336\n\t"
    "s_barrier\n\t"
    "s_setprio 2\n\t"
    "s_waitcnt lgkmcnt(8)\n\t"
    "v_smfmac_f32_16x16x64_bf16 v[12:15], v[48:51], v[144:151], v9\n\t"
    "v_smfmac_f32_16x16x64_bf16 v[16:19], v[48:51], v[152:159], v9\n\t"
    "ds_read_b128 v[48:51], v6 offset:3072\n\t"
    "s_waitcnt lgkmcnt(6)\n\t"
    "v_smfmac_f32_16x16x64_bf16 v[12:15], v[52:55], v[20:27], v9\n\t"
    "ds_read_b128 v[20:23], v8 offset:16384\n\t"
    "ds_read_b128 v[24:27], v8 offset:17408\n\t"
    "s_waitcnt lgkmcnt(6)\n\t"
    "v_smfmac_f32_16x16x64_bf16 v[16:19], v[52:55], v[28:35], v9\n\t"
    "ds_read_b128 v[28:31], v8 offset:24576\n\t"
    "ds_read_b128 v[32:35], v8 offset:25600\n\t"
    "ds_read_b128 v[52:55], v6 offset:15360\n\t"
    "s_waitcnt lgkmcnt(8)\n\t"
    "v_smfmac_f32_16x16x64_bf16 v[12:15], v[40:43], v[160:167], v9\n\t"
    "v_smfmac_f32_16x16x64_bf16 v[16:19], v[40:43], v[168:175], v9\n\t"
    "s_waitcnt lgkmcnt(7)\n\t"
    "v_smfmac_f32_16x16x64_bf16 v[12:15], v[36:39], v[176:183], v9\n\t"
    "v_smfmac_f32_16x16x64_bf16 v[16:19], v[36:39], v[184:191], v9\n\t"
    "s_waitcnt lgkmcnt(6)\n\t"
    "v_smfmac_f32_16x16x64_bf16 v[12:15], v[44:47], v[224:231], v9\n\t"
    "v_smfmac_f32_16x16x64_bf16 v[16:19], v[44:47], v[232:239], v9\n\t"
    "s_waitcnt lgkmcnt(3)\n\t"
    "v_smfmac_f32_16x16x64_bf16 v[12:15], v[48:51], v[20:27], v9\n\t"
    "s_waitcnt lgkmcnt(1)\n\t"
    "v_smfmac_f32_16x16x64_bf16 v[16:19], v[48:51], v[28:35], v9\n\t"
    "s_waitcnt lgkmcnt(0)\n\t"
    "v_smfmac_f32_16x16x64_bf16 v[12:15], v[52:55], v[240:247], v9\n\t"
    "v_smfmac_f32_16x16x64_bf16 v[16:19], v[52:55], v[248:255], v9\n\t"
    "s_nop 6\n\t"
    "v_permlane16_swap_b32 v12, v14\n\t"
    "v_permlane16_swap_b32 v13, v15\n\t"
    "v_permlane16_swap_b32 v16, v18\n\t"
    "v_permlane16_swap_b32 v17, v19\n\t"
    "v_add_f32 v12, v12, v14\n\t"
    "v_add_f32 v13, v13, v15\n\t"
    "v_add_f32 v16, v16, v18\n\t"
    "v_cvt_pk_bf16_f32 v12, v12, v13\n\t"
    "v_add_f32 v17, v17, v19\n\t"
    "ds_write_b32 v10, v12 offset:35840\n\t"
    "v_cvt_pk_bf16_f32 v16, v16, v17\n\t"
    "ds_write_b32 v10, v16 offset:36096\n\t"
    "s_waitcnt lgkmcnt(0)\n\t"
    "s_setprio 0\n\t"
    "s_barrier\n\t"
    "s_barrier\n\t"
    "s_waitcnt lgkmcnt(0)\n\t"
    "s_branch 9f\n\t"
    "1:\n\t"
    "s_add_u32 %[sp], %[sp], 1\n\t"
    "s_cmp_ge_u32 %[sp], %[cap]\n\t"
    "s_cbranch_scc1 7f\n\t"
    "global_load_dwordx4 v[36:39], v6, %[ag] sc1\n\t"
    "global_load_dwordx4 v[40:43], v6, %[ag] offset:1024 sc1\n\t"
    "s_waitcnt lgkmcnt(0)\n\t"
    "s_memtime %[tc]\n\t"
    "s_nop 7\n\t"
    "s_branch 3b\n\t"
    "7:\n\t"
    "s_mov_b32 %[cap], 0\n\t"
    "s_branch 2b\n\t"
    "9:"
    : [tc] "=&s"(tc), [sp] "=&s"(sp), [cap] "+s"(cap)
    : [ag] "s"(ag), [c44] "s"(0x4444u), "{v[64:95]}"(W.w0), "{v[96:127]}"(W.w1), "{v[128:159]}"(W.w2), "{v[160:191]}"(W.w3), "{v[192:223]}"(W.w4), "{v[224:255]}"(W.w5), "{v[56:63]}"(W.x)
    : "v6", "v7", "v8", "v9", "v10", "v11", "v12", "v13", "v14", "v15", "v16", "v17", "v18", "v19", "v20", "v21", "v22", "v23", "v24", "v25", "v26", "v27", "v28", "v29", "v30", "v31", "v32", "v33", "v34", "v35", "v36", "v37", "v38", "v39", "v40", "v41", "v42", "v43", "v44", "v45", "v46", "v47", "v48", "v49", "v50", "v51", "v52", "v53", "v54", "v55", "vcc", "scc", "memory");
}
template <> __device__ __forceinline__ void qk_step<7>(const QKW& W, const char* ag, const char* vs, const char* od, const char* kv, unsigned& cap, unsigned& sp,
                                                        unsigned long long& tc, unsigned long long* ts, unsigned& d0, unsigned& d1) {
  unsigned st, bg;
  asm volatile("v_mbcnt_lo_u32_b32 v52, -1, 0\n\t"
    "v_mbcnt_hi_u32_b32 v52, -1, v52\n\t"
    "v_lshlrev_b32 v6, 4, v52\n\t"
    "s_mov_b32 %[sp], 0\n\t"
    "global_load_dwordx4 v[36:39], v6, %[ag] sc1\n\t"
    "global_load_dwordx4 v[40:43], v6, %[ag] offset:1024 sc1\n\t"
    "s_memtime %[tc]\n\t"
    "v_add_u32 v7, 79872, v6\n\t"
    "v_add_u32 v8, 137216, v6\n\t"
    "ds_read_b128 v[20:23], v7 offset:8192\n\t"
    "ds_read_b128 v[24:27], v7 offset:9216\n\t"
    "ds_read_b128 v[28:31], v7 offset:0\n\t"
    "ds_read_b128 v[32:35], v7 offset:1024\n\t"
    "v_lshrrev_b32 v44, 4, v52\n\t"
    "v_and_b32 v45, 15, v52\n\t"
    "v_and_b32 v53, 4, v52\n\t"
    "v_mul_u32_u24 v53, 0x2aaa, v53\n\t"
    "v_lshrrev_b32 v9, 1, v52\n\t"
    "v_and_b32 v9, 2, v9\n\t"
    "v_or3_b32 v9, v9, v53, %[c44]\n\t"
    "v_lshlrev_b32 v53, 2, v44\n\t"
    "v_lshl_or_b32 v10, v45, 4, v53\n\t"
    "3:\n\t"
    "v_mov_b32 v12, 0\n\t"
    "v_mov_b32 v13, 0\n\t"
    "v_mov_b32 v14, 0\n\t"
    "v_mov_b32 v15, 0\n\t"
    "v_mov_b32 v16, 0\n\t"
    "v_mov_b32 v17, 0\n\t"
    "v_mov_b32 v18, 0\n\t"
    "v_mov_b32 v19, 0\n\t"
    "s_waitcnt vmcnt(0) lgkmcnt(0)\n\t"
    "14:\n\t"
    "ds_write_b128 v6, v[36:39] offset:14336\n\t"
    "ds_write_b128 v6, v[40:43] offset:15360\n\t"
    "v_smfmac_f32_16x16x64_bf16 v[12:15], v[36:39], v[224:231], v9\n\t"
    "v_max3_u32 v52, v36, v37, v38\n\t"
    "v_max3_u32 v53, v39, v40, v41\n\t"
    "v_smfmac_f32_16x16x64_bf16 v[16:19], v[36:39], v[232:239], v9\n\t"
    "v_max3_u32 v52, v52, v42, v43\n\t"
    "v_max_u32 v52, v52, v53\n\t"
    "v_cmp_eq_u32 vcc, -1, v52\n\t"
    "s_cbranch_vccnz 1f\n\t"
    "2:\n\t"
    "s_waitcnt lgkmcnt(0)\n\t"
    "s_barrier\n\t"
    "ds_read_b128 v[36:39], v6 offset:0\n\t"
    "ds_read_b128 v[44:47], v6 offset:4096\n\t"
    "v_smfmac_f32_16x16x64_bf16 v[12:15], v[40:43], v[240:247], v9\n\t"
    "ds_read_b128 v[48:51], v6 offset:5120\n\t"
    "ds_read_b128 v[52:55], v6 offset:6144\n\t"
    "v_smfmac_f32_16x16x64_bf16 v[16:19], v[40:43], v[248:255], v9\n\t"
    "ds_read_b128 v[40:43], v6 offset:1024\n\t"
    "s_sleep 0\n\t"
    "s_waitcnt lgkmcnt(4)\n\t"
    "v_smfmac_f32_16x16x64_bf16 v[12:15], v[36:39], v[56:63], v9\n\t"
    "v_smfmac_f32_16x16x64_bf16 v[16:19], v[36:39], v[28:35], v9\n\t"
    "ds_read_b128 v[28:31], v7 offset:16384\n\t"
    "ds_read_b128 v[32:35], v7 offset:17408\n\t"
    "ds_read_b128 v[36:39], v6 offset:7168\n\t"
    "s_waitcnt lgkmcnt(6)\n\t"
    "v_smfmac_f32_16x16x64_bf16 v[12:15], v[44:47], v[64:71], v9\n\t"
    "v_smfmac_f32_16x16x64_bf16 v[16:19], v[44:47], v[72:79], v9\n\t"
    "ds_read_b128 v[44:47], v6 offset:8192\n\t"
    "s_waitcnt lgkmcnt(6)\n\t"
    "v_smfmac_f32_16x16x64_bf16 v[12:15], v[48:51], v[80:87], v9\n\t"
    "v_smfmac_f32_16x16x64_bf16 v[16:19], v[48:51], v[88:95], v9\n\t"
    "ds_read_b128 v[48:51], v6 offset:9216\n\t"
    "s_waitcnt lgkmcnt(6)\n\t"
    "v_smfmac_f32_16x16x64_bf16 v[12:15], v[52:55], v[96:103], v9\n\t"
    "v_smfmac_f32_16x16x64_bf16 v[16:19], v[52:55], v[104:111], v9\n\t"
    "ds_read_b128 v[52:55], v6 offset:2048\n\t"
    "s_waitcnt lgkmcnt(6)\n\t"
    "v_smfmac_f32_16x16x64_bf16 v[12:15], v[40:43], v[20:27], v9\n\t"
    "ds_read_b128 v[20:23], v8 offset:0\n\t"
    "ds_read_b128 v[24:27], v8 offset:1024\n\t"
    "s_waitcnt lgkmcnt(6)\n\t"
    "v_smfmac_f32_16x16x64_bf16 v[16:19], v[40:43], v[28:35], v9\n\t"
    "ds_read_b128 v[28:31], v8 offset:8192\n\t"
    "ds_read_b128 v[32:35], v8 offset:9216\n\t"
    "ds_read_b128 v[40:43], v6 offset:10240\n\t"
    "s_waitcnt lgkmcnt(8)\n\t"
    "v_smfmac_f32_16x16x64_bf16 v[12:15], v[36:39], v[112:119], v9\n\t"
    "v_smfmac_f32_16x16x64_bf16 v[16:19], v[36:39], v[120:127], v9\n\t"
    "ds_read_b128 v[36:39], v6 offset:11264\n\t"
    "s_waitcnt lgkmcnt(8)\n\t"
    "v_smfmac_f32_16x16x64_bf16 v[12:15], v[44:47], v[128:135], v9\n\t"
    "v_smfmac_f32_16x16x64_bf16 v[16:19], v[44:47], v[136:143], v9\n\t"
    "ds_read_b128 v[44:47], v6 offset:12288\n\t"
    "s_barrier\n\t"
    "s_setprio 2\n\t"
    "s_waitcnt lgkmcnt(8)\n\t"
    "v_smfmac_f32_16x16x64_bf16 v[12:15], v[48:51], v[144:151], v9\n\t"
    "v_smfmac_f32_16x16x64_bf16 v[16:19], v[48:51], v[152:159], v9\n\t"
    "ds_read_b128 v[48:51], v6 offset:3072\n\t"
    "s_waitcnt lgkmcnt(6)\n\t"
    "v_smfmac_f32_16x16x64_bf16 v[12:15], v[52:55], v[20:27], v9\n\t"
    "ds_read_b128 v[20:23], v8 offset:16384\n\t"
    "ds_read_b128 v[24:27], v8 offset:17408\n\t"
    "s_waitcnt lgkmcnt(6)\n\t"
    "v_smfmac_f32_16x16x64_bf16 v[16:19], v[52:55], v[28:35], v9\n\t"
    "ds_read_b128 v[28:31], v8 offset:24576\n\t"
    "ds_read_b128 v[32:35], v8 offset:25600\n\t"
    "ds_read_b128 v[52:55], v6 offset:13312\n\t"
    "s_waitcnt lgkmcnt(8)\n\t"
    "v_smfmac_f32_16x16x64_bf16 v[12:15], v[40:43], v[160:167], v9\n\t"
    "v_smfmac_f32_16x16x64_bf16 v[16:19], v[40:43], v[168:175], v9\n\t"
    "s_waitcnt lgkmcnt(7)\n\t"
    "v_smfmac_f32_16x16x64_bf16 v[12:15], v[36:39], v[176:183], v9\n\t"
    "v_smfmac_f32_16x16x64_bf16 v[16:19], v[36:39], v[184:191], v9\n\t"
    "s_waitcnt lgkmcnt(6)\n\t"
    "v_smfmac_f32_16x16x64_bf16 v[12:15], v[44:47], v[192:199], v9\n\t"
    "v_smfmac_f32_16x16x64_bf16 v[16:19], v[44:47], v[200:207], v9\n\t"
    "s_waitcnt lgkmcnt(3)\n\t"
    "v_smfmac_f32_16x16x64_bf16 v[12:15], v[48:51], v[20:27], v9\n\t"
    "s_waitcnt lgkmcnt(1)\n\t"
    "v_smfmac_f32_16x16x64_bf16 v[16:19], v[48:51], v[28:35], v9\n\t"
    "s_waitcnt lgkmcnt(0)\n\t"
    "v_smfmac_f32_16x16x64_bf16 v[12:15], v[52:55], v[208:215], v9\n\t"
    "v_smfmac_f32_16x16x64_bf16 v[16:19], v[52:55], v[216:223], v9\n\t"
    "s_nop 6\n\t"
    "v_permlane16_swap_b32 v12, v14\n\t"
    "v_permlane16_swap_b32 v13, v15\n\t"
    "v_permlane16_swap_b32 v16, v18\n\t"
    "v_permlane16_swap_b32 v17, v19\n\t"
    "v_add_f32 v12, v12, v14\n\t"
    "v_add_f32 v13, v13, v15\n\t"
    "v_add_f32 v16, v16, v18\n\t"
    "v_cvt_pk_bf16_f32 v12, v12, v13\n\t"
    "v_add_f32 v17, v17, v19\n\t"
    "ds_write_b32 v10, v12 offset:36352\n\t"
    "v_cvt_pk_bf16_f32 v16, v16, v17\n\t"
    "ds_write_b32 v10, v16 offset:36608\n\t"
    "s_waitcnt lgkmcnt(0)\n\t"
    "s_setprio 0\n\t"
    "s_barrier\n\t"
    "s_barrier\n\t"
    "s_waitcnt lgkmcnt(0)\n\t"
    "s_branch 9f\n\t"
    "1:\n\t"
    "s_add_u32 %[sp], %[sp], 1\n\t"
    "s_cmp_ge_u32 %[sp], %[cap]\n\t"
    "s_cbranch_scc1 7f\n\t"
    "global_load_dwordx4 v[36:39], v6, %[ag] sc1\n\t"
    "global_load_dwordx4 v[40:43], v6, %[ag] offset:1024 sc1\n\t"
    "s_waitcnt lgkmcnt(0)\n\t"
    "s_memtime %[tc]\n\t"
    "s_nop 7\n\t"
    "s_branch 3b\n\t"
    "7:\n\t"
    "s_mov_b32 %[cap], 0\n\t"
    "s_branch 2b\n\t"
    "9:"
    : [tc] "=&s"(tc), [sp] "=&s"(sp), [cap] "+s"(cap)
    : [ag] "s"(ag), [c44] "s"(0x4444u), "{v[64:95]}"(W.w0), "{v[96:127]}"(W.w1), "{v[128:159]}"(W.w2), "{v[160:191]}"(W.w3), "{v[192:223]}"(W.w4), "{v[224:255]}"(W.w5), "{v[56:63]}"(W.x)
    : "v6", "v7", "v8", "v9", "v10", "v11", "v12", "v13", "v14", "v15", "v16", "v17", "v18", "v19", "v20", "v21", "v22", "v23", "v24", "v25", "v26", "v27", "v28", "v29", "v30", "v31", "v32", "v33", "v34", "v35", "v36", "v37", "v38", "v39", "v40", "v41", "v42", "v43", "v44", "v45", "v46", "v47", "v48", "v49", "v50", "v51", "v52", "v53", "v54", "v55", "vcc", "scc", "memory");
}
template <int I> __device__ __forceinline__ void qk_wload(u32x32& w, unsigned a0, unsigned a1);
template <> __device__ __forceinline__ void qk_wload<0>(u32x32& w, unsigned a0, unsigned a1) {
  asm volatile("ds_read_b64_tr_b16 v[64:65], %1 offset:0\n\t"
    "ds_read_b64_tr_b16 v[66:67], %1 offset:512\n\t"
    "ds_read_b64_tr_b16 v[68:69], %1 offset:4096\n\t"
    "ds_read_b64_tr_b16 v[70:71], %1 offset:4608\n\t"
    "ds_read_b64_tr_b16 v[72:73], %2 offset:0\n\t"
    "ds_read_b64_tr_b16 v[74:75], %2 offset:512\n\t"
    "ds_read_b64_tr_b16 v[76:77], %2 offset:4096\n\t"
    "ds_read_b64_tr_b16 v[78:79], %2 offset:4608\n\t"
    "s_waitcnt lgkmcnt(0)\n\t"
    "ds_read_b64_tr_b16 v[80:81], %1 offset:16384\n\t"
    "ds_read_b64_tr_b16 v[82:83], %1 offset:16896\n\t"
    "ds_read_b64_tr_b16 v[84:85], %1 offset:20480\n\t"
    "ds_read_b64_tr_b16 v[86:87], %1 offset:20992\n\t"
    "ds_read_b64_tr_b16 v[88:89], %2 offset:16384\n\t"
    "ds_read_b64_tr_b16 v[90:91], %2 offset:16896\n\t"
    "ds_read_b64_tr_b16 v[92:93], %2 offset:20480\n\t"
    "ds_read_b64_tr_b16 v[94:95], %2 offset:20992\n\t"
    "s_waitcnt lgkmcnt(0)"
    : "=&{v[64:95]}"(w) : "v"(a0), "v"(a1) : "memory");
}
template <> __device__ __forceinline__ void qk_wload<1>(u32x32& w, unsigned a0, unsigned a1) {
  asm volatile("ds_read_b64_tr_b16 v[96:97], %1 offset:0\n\t"
    "ds_read_b64_tr_b16 v[98:99], %1 offset:512\n\t"
    "ds_read_b64_tr_b16 v[100:101], %1 offset:4096\n\t"
    "ds_read_b64_tr_b16 v[102:103], %1 offset:4608\n\t"
    "ds_read_b64_tr_b16 v[104:105], %2 offset:0\n\t"
    "ds_read_b64_tr_b16 v[106:107], %2 offset:512\n\t"
    "ds_read_b64_tr_b16 v[108:109], %2 offset:4096\n\t"
    "ds_read_b64_tr_b16 v[110:111], %2 offset:4608\n\t"
    "s_waitcnt lgkmcnt(0)\n\t"
    "ds_read_b64_tr_b16 v[112:113], %1 offset:16384\n\t"
    "ds_read_b64_tr_b16 v[114:115], %1 offset:16896\n\t"
    "ds_read_b64_tr_b16 v[116:117], %1 offset:20480\n\t"
    "ds_read_b64_tr_b16 v[118:119], %1 offset:20992\n\t"
    "ds_read_b64_tr_b16 v[120:121], %2 offset:16384\n\t"
    "ds_read_b64_tr_b16 v[122:123], %2 offset:16896\n\t"
    "ds_read_b64_tr_b16 v[124:125], %2 offset:20480\n\t"
    "ds_read_b64_tr_b16 v[126:127], %2 offset:20992\n\t"
    "s_waitcnt lgkmcnt(0)"
    : "=&{v[96:127]}"(w) : "v"(a0), "v"(a1) : "memory");
}
template <> __device__ __forceinline__ void qk_wload<2>(u32x32& w, unsigned a0, unsigned a1) {
  asm volatile("ds_read_b64_tr_b16 v[128:129], %1 offset:0\n\t"
    "ds_read_b64_tr_b16 v[130:131], %1 offset:512\n\t"
    "ds_read_b64_tr_b16 v[132:133], %1 offset:4096\n\t"
    "ds_read_b64_tr_b16 v[134:135], %1 offset:4608\n\t"
    "ds_read_b64_tr_b16 v[136:137], %2 offset:0\n\t"
    "ds_read_b64_tr_b16 v[138:139], %2 offset:512\n\t"
    "ds_read_b64_tr_b16 v[140:141], %2 offset:4096\n\t"
    "ds_read_b64_tr_b16 v[142:143], %2 offset:4608\n\t"
    "s_waitcnt lgkmcnt(0)\n\t"
    "ds_read_b64_tr_b16 v[144:145], %1 offset:16384\n\t"
    "ds_read_b64_tr_b16 v[146:147], %1 offset:16896\n\t"
    "ds_read_b64_tr_b16 v[148:149], %1 offset:20480\n\t"
    "ds_read_b64_tr_b16 v[150:151], %1 offset:20992\n\t"
    "ds_read_b64_tr_b16 v[152:153], %2 offset:16384\n\t"
    "ds_read_b64_tr_b16 v[154:155], %2 offset:16896\n\t"
    "ds_read_b64_tr_b16 v[156:157], %2 offset:20480\n\t"
    "ds_read_b64_tr_b16 v[158:159], %2 offset:20992\n\t"
    "s_waitcnt lgkmcnt(0)"
    : "=&{v[128:159]}"(w) : "v"(a0), "v"(a1) : "memory");
}
template <> __device__ __forceinline__ void qk_wload<3>(u32x32& w, unsigned a0, unsigned a1) {
  asm volatile("ds_read_b64_tr_b16 v[160:161], %1 offset:0\n\t"
    "ds_read_b64_tr_b16 v[162:163], %1 offset:512\n\t"
    "ds_read_b64_tr_b16 v[164:165], %1 offset:4096\n\t"
    "ds_read_b64_tr_b16 v[166:167], %1 offset:4608\n\t"
    "ds_read_b64_tr_b16 v[168:169], %2 offset:0\n\t"
    "ds_read_b64_tr_b16 v[170:171], %2 offset:512\n\t"
    "ds_read_b64_tr_b16 v[172:173], %2 offset:4096\n\t"
    "ds_read_b64_tr_b16 v[174:175], %2 offset:4608\n\t"
    "s_waitcnt lgkmcnt(0)\n\t"
    "ds_read_b64_tr_b16 v[176:177], %1 offset:16384\n\t"
    "ds_read_b64_tr_b16 v[178:179], %1 offset:16896\n\t"
    "ds_read_b64_tr_b16 v[180:181], %1 offset:20480\n\t"
    "ds_read_b64_tr_b16 v[182:183], %1 offset:20992\n\t"
    "ds_read_b64_tr_b16 v[184:185], %2 offset:16384\n\t"
    "ds_read_b64_tr_b16 v[186:187], %2 offset:16896\n\t"
    "ds_read_b64_tr_b16 v[188:189], %2 offset:20480\n\t"
    "ds_read_b64_tr_b16 v[190:191], %2 offset:20992\n\t"
    "s_waitcnt lgkmcnt(0)"
    : "=&{v[160:191]}"(w) : "v"(a0), "v"(a1) : "memory");
}
template <> __device__ __forceinline__ void qk_wload<4>(u32x32& w, unsigned a0, unsigned a1) {
  asm volatile("ds_read_b64_tr_b16 v[192:193], %1 offset:0\n\t"
    "ds_read_b64_tr_b16 v[194:195], %1 offset:512\n\t"
    "ds_read_b64_tr_b16 v[196:197], %1 offset:4096\n\t"
    "ds_read_b64_tr_b16 v[198:199], %1 offset:4608\n\t"
    "ds_read_b64_tr_b16 v[200:201], %2 offset:0\n\t"
    "ds_read_b64_tr_b16 v[202:203], %2 offset:512\n\t"
    "ds_read_b64_tr_b16 v[204:205], %2 offset:4096\n\t"
    "ds_read_b64_tr_b16 v[206:207], %2 offset:4608\n\t"
    "s_waitcnt lgkmcnt(0)\n\t"
    "ds_read_b64_tr_b16 v[208:209], %1 offset:16384\n\t"
    "ds_read_b64_tr_b16 v[210:211], %1 offset:16896\n\t"
    "ds_read_b64_tr_b16 v[212:213], %1 offset:20480\n\t"
    "ds_read_b64_tr_b16 v[214:215], %1 offset:20992\n\t"
    "ds_read_b64_tr_b16 v[216:217], %2 offset:16384\n\t"
    "ds_read_b64_tr_b16 v[218:219], %2 offset:16896\n\t"
    "ds_read_b64_tr_b16 v[220:221], %2 offset:20480\n\t"
    "ds_read_b64_tr_b16 v[222:223], %2 offset:20992\n\t"
    "s_waitcnt lgkmcnt(0)"
    : "=&{v[192:223]}"(w) : "v"(a0), "v"(a1) : "memory");
}
template <> __device__ __forceinline__ void qk_wload<5>(u32x32& w, unsigned a0, unsigned a1) {
  asm volatile("ds_read_b64_tr_b16 v[224:225], %1 offset:0\n\t"
    "ds_read_b64_tr_b16 v[226:227], %1 offset:512\n\t"
    "ds_read_b64_tr_b16 v[228:229], %1 offset:4096\n\t"
    "ds_read_b64_tr_b16 v[230:231], %1 offset:4608\n\t"
    "ds_read_b64_tr_b16 v[232:233], %2 offset:0\n\t"
    "ds_read_b64_tr_b16 v[234:235], %2 offset:512\n\t"
    "ds_read_b64_tr_b16 v[236:237], %2 offset:4096\n\t"
    "ds_read_b64_tr_b16 v[238:239], %2 offset:4608\n\t"
    "s_waitcnt lgkmcnt(0)\n\t"
    "ds_read_b64_tr_b16 v[240:241], %1 offset:16384\n\t"
    "ds_read_b64_tr_b16 v[242:243], %1 offset:16896\n\t"
    "ds_read_b64_tr_b16 v[244:245], %1 offset:20480\n\t"
    "ds_read_b64_tr_b16 v[246:247], %1 offset:20992\n\t"
    "ds_read_b64_tr_b16 v[248:249], %2 offset:16384\n\t"
    "ds_read_b64_tr_b16 v[250:251], %2 offset:16896\n\t"
    "ds_read_b64_tr_b16 v[252:253], %2 offset:20480\n\t"
    "ds_read_b64_tr_b16 v[254:255], %2 offset:20992\n\t"
    "s_waitcnt lgkmcnt(0)"
    : "=&{v[224:255]}"(w) : "v"(a0), "v"(a1) : "memory");
}
#define QK_OWN 0
template <int I> __device__ __forceinline__ void qk_swap(u32x32& w, unsigned a);
template <> __device__ __forceinline__ void qk_swap<0>(u32x32& w, unsigned a) {
  asm volatile("ds_read_b128 v[8:11], %1 offset:0\n\t"
    "ds_read_b128 v[12:15], %1 offset:1024\n\t"
    "ds_read_b128 v[16:19], %1 offset:8192\n\t"
    "ds_read_b128 v[20:23], %1 offset:9216\n\t"
    "ds_read_b128 v[24:27], %1 offset:16384\n\t"
    "ds_read_b128 v[28:31], %1 offset:17408\n\t"
    "ds_read_b128 v[32:35], %1 offset:24576\n\t"
    "ds_read_b128 v[36:39], %1 offset:25600\n\t"
    "ds_write_b128 %1, v[64:67] offset:0\n\t"
    "ds_write_b128 %1, v[68:71] offset:1024\n\t"
    "ds_write_b128 %1, v[72:75] offset:8192\n\t"
    "ds_write_b128 %1, v[76:79] offset:9216\n\t"
    "ds_write_b128 %1, v[80:83] offset:16384\n\t"
    "ds_write_b128 %1, v[84:87] offset:17408\n\t"
    "ds_write_b128 %1, v[88:91] offset:24576\n\t"
    "ds_write_b128 %1, v[92:95] offset:25600\n\t"
    "s_waitcnt lgkmcnt(0)\n\t"
    "v_mov_b32 v64, v8\n\t"
    "v_mov_b32 v65, v9\n\t"
    "v_mov_b32 v66, v10\n\t"
    "v_mov_b32 v67, v11\n\t"
    "v_mov_b32 v68, v12\n\t"
    "v_mov_b32 v69, v13\n\t"
    "v_mov_b32 v70, v14\n\t"
    "v_mov_b32 v71, v15\n\t"
    "v_mov_b32 v72, v16\n\t"
    "v_mov_b32 v73, v17\n\t"
    "v_mov_b32 v74, v18\n\t"
    "v_mov_b32 v75, v19\n\t"
    "v_mov_b32 v76, v20\n\t"
    "v_mov_b32 v77, v21\n\t"
    "v_mov_b32 v78, v22\n\t"
    "v_mov_b32 v79, v23\n\t"
    "v_mov_b32 v80, v24\n\t"
    "v_mov_b32 v81, v25\n\t"
    "v_mov_b32 v82, v26\n\t"
    "v_mov_b32 v83, v27\n\t"
    "v_mov_b32 v84, v28\n\t"
    "v_mov_b32 v85, v29\n\t"
    "v_mov_b32 v86, v30\n\t"
    "v_mov_b32 v87, v31\n\t"
    "v_mov_b32 v88, v32\n\t"
    "v_mov_b32 v89, v33\n\t"
    "v_mov_b32 v90, v34\n\t"
    "v_mov_b32 v91, v35\n\t"
    "v_mov_b32 v92, v36\n\t"
    "v_mov_b32 v93, v37\n\t"
    "v_mov_b32 v94, v38\n\t"
    "v_mov_b32 v95, v39"
    :: "{v[64:95]}"(w), "v"(a) : "v8", "v9", "v10", "v11", "v12", "v13", "v14", "v15", "v16", "v17", "v18", "v19", "v20", "v21", "v22", "v23", "v24", "v25", "v26", "v27", "v28", "v29", "v30", "v31", "v32", "v33", "v34", "v35", "v36", "v37", "v38", "v39", "memory");
}
template <> __device__ __forceinline__ void qk_swap<1>(u32x32& w, unsigned a) {
  asm volatile("ds_read_b128 v[8:11], %1 offset:0\n\t"
    "ds_read_b128 v[12:15], %1 offset:1024\n\t"
    "ds_read_b128 v[16:19], %1 offset:8192\n\t"
    "ds_read_b128 v[20:23], %1 offset:9216\n\t"
    "ds_read_b128 v[24:27], %1 offset:16384\n\t"
    "ds_read_b128 v[28:31], %1 offset:17408\n\t"
    "ds_read_b128 v[32:35], %1 offset:24576\n\t"
    "ds_read_b128 v[36:39], %1 offset:25600\n\t"
    "ds_write_b128 %1, v[96:99] offset:0\n\t"
    "ds_write_b128 %1, v[100:103] offset:1024\n\t"
    "ds_write_b128 %1, v[104:107] offset:8192\n\t"
    "ds_write_b128 %1, v[108:111] offset:9216\n\t"
    "ds_write_b128 %1, v[112:115] offset:16384\n\t"
    "ds_write_b128 %1, v[116:119] offset:17408\n\t"
    "ds_write_b128 %1, v[120:123] offset:24576\n\t"
    "ds_write_b128 %1, v[124:127] offset:25600\n\t"
    "s_waitcnt lgkmcnt(0)\n\t"
    "v_mov_b32 v96, v8\n\t"
    "v_mov_b32 v97, v9\n\t"
    "v_mov_b32 v98, v10\n\t"
    "v_mov_b32 v99, v11\n\t"
    "v_mov_b32 v100, v12\n\t"
    "v_mov_b32 v101, v13\n\t"
    "v_mov_b32 v102, v14\n\t"
    "v_mov_b32 v103, v15\n\t"
    "v_mov_b32 v104, v16\n\t"
    "v_mov_b32 v105, v17\n\t"
    "v_mov_b32 v106, v18\n\t"
    "v_mov_b32 v107, v19\n\t"
    "v_mov_b32 v108, v20\n\t"
    "v_mov_b32 v109, v21\n\t"
    "v_mov_b32 v110, v22\n\t"
    "v_mov_b32 v111, v23\n\t"
    "v_mov_b32 v112, v24\n\t"
    "v_mov_b32 v113, v25\n\t"
    "v_mov_b32 v114, v26\n\t"
    "v_mov_b32 v115, v27\n\t"
    "v_mov_b32 v116, v28\n\t"
    "v_mov_b32 v117, v29\n\t"
    "v_mov_b32 v118, v30\n\t"
    "v_mov_b32 v119, v31\n\t"
    "v_mov_b32 v120, v32\n\t"
    "v_mov_b32 v121, v33\n\t"
    "v_mov_b32 v122, v34\n\t"
    "v_mov_b32 v123, v35\n\t"
    "v_mov_b32 v124, v36\n\t"
    "v_mov_b32 v125, v37\n\t"
    "v_mov_b32 v126, v38\n\t"
    "v_mov_b32 v127, v39"
    :: "{v[96:127]}"(w), "v"(a) : "v8", "v9", "v10", "v11", "v12", "v13", "v14", "v15", "v16", "v17", "v18", "v19", "v20", "v21", "v22", "v23", "v24", "v25", "v26", "v27", "v28", "v29", "v30", "v31", "v32", "v33", "v34", "v35", "v36", "v37", "v38", "v39", "memory");
}
template <> __device__ __forceinline__ void qk_swap<2>(u32x32& w, unsigned a) {
  asm volatile("ds_read_b128 v[8:11], %1 offset:0\n\t"
    "ds_read_b128 v[12:15], %1 offset:1024\n\t"
    "ds_read_b128 v[16:19], %1 offset:8192\n\t"
    "ds_read_b128 v[20:23], %1 offset:9216\n\t"
    "ds_read_b128 v[24:27], %1 offset:16384\n\t"
    "ds_read_b128 v[28:31], %1 offset:17408\n\t"
    "ds_read_b128 v[32:35], %1 offset:24576\n\t"
    "ds_read_b128 v[36:39], %1 offset:25600\n\t"
    "ds_write_b128 %1, v[128:131] offset:0\n\t"
    "ds_write_b128 %1, v[132:135] offset:1024\n\t"
    "ds_write_b128 %1, v[136:139] offset:8192\n\t"
    "ds_write_b128 %1, v[140:143] offset:9216\n\t"
    "ds_write_b128 %1, v[144:147] offset:16384\n\t"
    "ds_write_b128 %1, v[148:151] offset:17408\n\t"
    "ds_write_b128 %1, v[152:155] offset:24576\n\t"
    "ds_write_b128 %1, v[156:159] offset:25600\n\t"
    "s_waitcnt lgkmcnt(0)\n\t"
    "v_mov_b32 v128, v8\n\t"
    "v_mov_b32 v129, v9\n\t"
    "v_mov_b32 v130, v10\n\t"
    "v_mov_b32 v131, v11\n\t"
    "v_mov_b32 v132, v12\n\t"
    "v_mov_b32 v133, v13\n\t"
    "v_mov_b32 v134, v14\n\t"
    "v_mov_b32 v135, v15\n\t"
    "v_mov_b32 v136, v16\n\t"
    "v_mov_b32 v137, v17\n\t"
    "v_mov_b32 v138, v18\n\t"
    "v_mov_b32 v139, v19\n\t"
    "v_mov_b32 v140, v20\n\t"
    "v_mov_b32 v141, v21\n\t"
    "v_mov_b32 v142, v22\n\t"
    "v_mov_b32 v143, v23\n\t"
    "v_mov_b32 v144, v24\n\t"
    "v_mov_b32 v145, v25\n\t"
    "v_mov_b32 v146, v26\n\t"
    "v_mov_b32 v147, v27\n\t"
    "v_mov_b32 v148, v28\n\t"
    "v_mov_b32 v149, v29\n\t"
    "v_mov_b32 v150, v30\n\t"
    "v_mov_b32 v151, v31\n\t"
    "v_mov_b32 v152, v32\n\t"
    "v_mov_b32 v153, v33\n\t"
    "v_mov_b32 v154, v34\n\t"
    "v_mov_b32 v155, v35\n\t"
    "v_mov_b32 v156, v36\n\t"
    "v_mov_b32 v157, v37\n\t"
    "v_mov_b32 v158, v38\n\t"
    "v_mov_b32 v159, v39"
    :: "{v[128:159]}"(w), "v"(a) : "v8", "v9", "v10", "v11", "v12", "v13", "v14", "v15", "v16", "v17", "v18", "v19", "v20", "v21", "v22", "v23", "v24", "v25", "v26", "v27", "v28", "v29", "v30", "v31", "v32", "v33", "v34", "v35", "v36", "v37", "v38", "v39", "memory");
}
template <> __device__ __forceinline__ void qk_swap<3>(u32x32& w, unsigned a) {
  asm volatile("ds_read_b128 v[8:11], %1 offset:0\n\t"
    "ds_read_b128 v[12:15], %1 offset:1024\n\t"
    "ds_read_b128 v[16:19], %1 offset:8192\n\t"
    "ds_read_b128 v[20:23], %1 offset:9216\n\t"
    "ds_read_b128 v[24:27], %1 offset:16384\n\t"
    "ds_read_b128 v[28:31], %1 offset:17408\n\t"
    "ds_read_b128 v[32:35], %1 offset:24576\n\t"
    "ds_read_b128 v[36:39], %1 offset:25600\n\t"
    "ds_write_b128 %1, v[160:163] offset:0\n\t"
    "ds_write_b128 %1, v[164:167] offset:1024\n\t"
    "ds_write_b128 %1, v[168:171] offset:8192\n\t"
    "ds_write_b128 %1, v[172:175] offset:9216\n\t"
    "ds_write_b128 %1, v[176:179] offset:16384\n\t"
    "ds_write_b128 %1, v[180:183] offset:17408\n\t"
    "ds_write_b128 %1, v[184:187] offset:24576\n\t"
    "ds_write_b128 %1, v[188:191] offset:25600\n\t"
    "s_waitcnt lgkmcnt(0)\n\t"
    "v_mov_b32 v160, v8\n\t"
    "v_mov_b32 v161, v9\n\t"
    "v_mov_b32 v162, v10\n\t"
    "v_mov_b32 v163, v11\n\t"
    "v_mov_b32 v164, v12\n\t"
    "v_mov_b32 v165, v13\n\t"
    "v_mov_b32 v166, v14\n\t"
    "v_mov_b32 v167, v15\n\t"
    "v_mov_b32 v168, v16\n\t"
    "v_mov_b32 v169, v17\n\t"
    "v_mov_b32 v170, v18\n\t"
    "v_mov_b32 v171, v19\n\t"
    "v_mov_b32 v172, v20\n\t"
    "v_mov_b32 v173, v21\n\t"
    "v_mov_b32 v174, v22\n\t"
    "v_mov_b32 v175, v23\n\t"
    "v_mov_b32 v176, v24\n\t"
    "v_mov_b32 v177, v25\n\t"
    "v_mov_b32 v178, v26\n\t"
    "v_mov_b32 v179, v27\n\t"
    "v_mov_b32 v180, v28\n\t"
    "v_mov_b32 v181, v29\n\t"
    "v_mov_b32 v182, v30\n\t"
    "v_mov_b32 v183, v31\n\t"
    "v_mov_b32 v184, v32\n\t"
    "v_mov_b32 v185, v33\n\t"
    "v_mov_b32 v186, v34\n\t"
    "v_mov_b32 v187, v35\n\t"
    "v_mov_b32 v188, v36\n\t"
    "v_mov_b32 v189, v37\n\t"
    "v_mov_b32 v190, v38\n\t"
    "v_mov_b32 v191, v39"
    :: "{v[160:191]}"(w), "v"(a) : "v8", "v9", "v10", "v11", "v12", "v13", "v14", "v15", "v16", "v17", "v18", "v19", "v20", "v21", "v22", "v23", "v24", "v25", "v26", "v27", "v28", "v29", "v30", "v31", "v32", "v33", "v34", "v35", "v36", "v37", "v38", "v39", "memory");
}
template <> __device__ __forceinline__ void qk_swap<4>(u32x32& w, unsigned a) {
  asm volatile("ds_read_b128 v[8:11], %1 offset:0\n\t"
    "ds_read_b128 v[12:15], %1 offset:1024\n\t"
    "ds_read_b128 v[16:19], %1 offset:8192\n\t"
    "ds_read_b128 v[20:23], %1 offset:9216\n\t"
    "ds_read_b128 v[24:27], %1 offset:16384\n\t"
    "ds_read_b128 v[28:31], %1 offset:17408\n\t"
    "ds_read_b128 v[32:35], %1 offset:24576\n\t"
    "ds_read_b128 v[36:39], %1 offset:25600\n\t"
    "ds_write_b128 %1, v[192:195] offset:0\n\t"
    "ds_write_b128 %1, v[196:199] offset:1024\n\t"
    "ds_write_b128 %1, v[200:203] offset:8192\n\t"
    "ds_write_b128 %1, v[204:207] offset:9216\n\t"
    "ds_write_b128 %1, v[208:211] offset:16384\n\t"
    "ds_write_b128 %1, v[212:215] offset:17408\n\t"
    "ds_write_b128 %1, v[216:219] offset:24576\n\t"
    "ds_write_b128 %1, v[220:223] offset:25600\n\t"
    "s_waitcnt lgkmcnt(0)\n\t"
    "v_mov_b32 v192, v8\n\t"
    "v_mov_b32 v193, v9\n\t"
    "v_mov_b32 v194, v10\n\t"
    "v_mov_b32 v195, v11\n\t"
    "v_mov_b32 v196, v12\n\t"
    "v_mov_b32 v197, v13\n\t"
    "v_mov_b32 v198, v14\n\t"
    "v_mov_b32 v199, v15\n\t"
    "v_mov_b32 v200, v16\n\t"
    "v_mov_b32 v201, v17\n\t"
    "v_mov_b32 v202, v18\n\t"
    "v_mov_b32 v203, v19\n\t"
    "v_mov_b32 v204, v20\n\t"
    "v_mov_b32 v205, v21\n\t"
    "v_mov_b32 v206, v22\n\t"
    "v_mov_b32 v207, v23\n\t"
    "v_mov_b32 v208, v24\n\t"
    "v_mov_b32 v209, v25\n\t"
    "v_mov_b32 v210, v26\n\t"
    "v_mov_b32 v211, v27\n\t"
    "v_mov_b32 v212, v28\n\t"
    "v_mov_b32 v213, v29\n\t"
    "v_mov_b32 v214, v30\n\t"
    "v_mov_b32 v215, v31\n\t"
    "v_mov_b32 v216, v32\n\t"
    "v_mov_b32 v217, v33\n\t"
    "v_mov_b32 v218, v34\n\t"
    "v_mov_b32 v219, v35\n\t"
    "v_mov_b32 v220, v36\n\t"
    "v_mov_b32 v221, v37\n\t"
    "v_mov_b32 v222, v38\n\t"
    "v_mov_b32 v223, v39"
    :: "{v[192:223]}"(w), "v"(a) : "v8", "v9", "v10", "v11", "v12", "v13", "v14", "v15", "v16", "v17", "v18", "v19", "v20", "v21", "v22", "v23", "v24", "v25", "v26", "v27", "v28", "v29", "v30", "v31", "v32", "v33", "v34", "v35", "v36", "v37", "v38", "v39", "memory");
}
template <> __device__ __forceinline__ void qk_swap<5>(u32x32& w, unsigned a) {
  asm volatile("ds_read_b128 v[8:11], %1 offset:0\n\t"
    "ds_read_b128 v[12:15], %1 offset:1024\n\t"
    "ds_read_b128 v[16:19], %1 offset:8192\n\t"
    "ds_read_b128 v[20:23], %1 offset:9216\n\t"
    "ds_read_b128 v[24:27], %1 offset:16384\n\t"
    "ds_read_b128 v[28:31], %1 offset:17408\n\t"
    "ds_read_b128 v[32:35], %1 offset:24576\n\t"
    "ds_read_b128 v[36:39], %1 offset:25600\n\t"
    "ds_write_b128 %1, v[224:227] offset:0\n\t"
    "ds_write_b128 %1, v[228:231] offset:1024\n\t"
    "ds_write_b128 %1, v[232:235] offset:8192\n\t"
    "ds_write_b128 %1, v[236:239] offset:9216\n\t"
    "ds_write_b128 %1, v[240:243] offset:16384\n\t"
    "ds_write_b128 %1, v[244:247] offset:17408\n\t"
    "ds_write_b128 %1, v[248:251] offset:24576\n\t"
    "ds_write_b128 %1, v[252:255] offset:25600\n\t"
    "s_waitcnt lgkmcnt(0)\n\t"
    "v_mov_b32 v224, v8\n\t"
    "v_mov_b32 v225, v9\n\t"
    "v_mov_b32 v226, v10\n\t"
    "v_mov_b32 v227, v11\n\t"
    "v_mov_b32 v228, v12\n\t"
    "v_mov_b32 v229, v13\n\t"
    "v_mov_b32 v230, v14\n\t"
    "v_mov_b32 v231, v15\n\t"
    "v_mov_b32 v232, v16\n\t"
    "v_mov_b32 v233, v17\n\t"
    "v_mov_b32 v234, v18\n\t"
    "v_mov_b32 v235, v19\n\t"
    "v_mov_b32 v236, v20\n\t"
    "v_mov_b32 v237, v21\n\t"
    "v_mov_b32 v238, v22\n\t"
    "v_mov_b32 v239, v23\n\t"
    "v_mov_b32 v240, v24\n\t"
    "v_mov_b32 v241, v25\n\t"
    "v_mov_b32 v242, v26\n\t"
    "v_mov_b32 v243, v27\n\t"
    "v_mov_b32 v244, v28\n\t"
    "v_mov_b32 v245, v29\n\t"
    "v_mov_b32 v246, v30\n\t"
    "v_mov_b32 v247, v31\n\t"
    "v_mov_b32 v248, v32\n\t"
    "v_mov_b32 v249, v33\n\t"
    "v_mov_b32 v250, v34\n\t"
    "v_mov_b32 v251, v35\n\t"
    "v_mov_b32 v252, v36\n\t"
    "v_mov_b32 v253, v37\n\t"
    "v_mov_b32 v254, v38\n\t"
    "v_mov_b32 v255, v39"
    :: "{v[224:255]}"(w), "v"(a) : "v8", "v9", "v10", "v11", "v12", "v13", "v14", "v15", "v16", "v17", "v18", "v19", "v20", "v21", "v22", "v23", "v24", "v25", "v26", "v27", "v28", "v29", "v30", "v31", "v32", "v33", "v34", "v35", "v36", "v37", "v38", "v39", "memory");
}
template <int I> __device__ __forceinline__ void qk_swapk(u32x32& w, u32x8& x, unsigned a);
template <> __device__ __forceinline__ void qk_swapk<2>(u32x32& w, u32x8& x, unsigned a) {
  asm volatile("ds_read_b128 v[16:19], %2 offset:0\n\t"
    "ds_read_b128 v[20:23], %2 offset:1024\n\t"
    "ds_read_b128 v[24:27], %2 offset:8192\n\t"
    "ds_read_b128 v[28:31], %2 offset:9216\n\t"
    "ds_read_b128 v[32:35], %2 offset:16384\n\t"
    "ds_read_b128 v[36:39], %2 offset:17408\n\t"
    "ds_write_b128 %2, v[136:139] offset:0\n\t"
    "ds_write_b128 %2, v[140:143] offset:1024\n\t"
    "ds_write_b128 %2, v[144:147] offset:8192\n\t"
    "ds_write_b128 %2, v[148:151] offset:9216\n\t"
    "ds_write_b128 %2, v[152:155] offset:16384\n\t"
    "ds_write_b128 %2, v[156:159] offset:17408\n\t"
    "v_swap_b32 v128, v56\n\t"
    "v_swap_b32 v129, v57\n\t"
    "v_swap_b32 v130, v58\n\t"
    "v_swap_b32 v131, v59\n\t"
    "v_swap_b32 v132, v60\n\t"
    "v_swap_b32 v133, v61\n\t"
    "v_swap_b32 v134, v62\n\t"
    "v_swap_b32 v135, v63\n\t"
    "s_waitcnt lgkmcnt(0)\n\t"
    "v_mov_b32 v136, v16\n\t"
    "v_mov_b32 v137, v17\n\t"
    "v_mov_b32 v138, v18\n\t"
    "v_mov_b32 v139, v19\n\t"
    "v_mov_b32 v140, v20\n\t"
    "v_mov_b32 v141, v21\n\t"
    "v_mov_b32 v142, v22\n\t"
    "v_mov_b32 v143, v23\n\t"
    "v_mov_b32 v144, v24\n\t"
    "v_mov_b32 v145, v25\n\t"
    "v_mov_b32 v146, v26\n\t"
    "v_mov_b32 v147, v27\n\t"
    "v_mov_b32 v148, v28\n\t"
    "v_mov_b32 v149, v29\n\t"
    "v_mov_b32 v150, v30\n\t"
    "v_mov_b32 v151, v31\n\t"
    "v_mov_b32 v152, v32\n\t"
    "v_mov_b32 v153, v33\n\t"
    "v_mov_b32 v154, v34\n\t"
    "v_mov_b32 v155, v35\n\t"
    "v_mov_b32 v156, v36\n\t"
    "v_mov_b32 v157, v37\n\t"
    "v_mov_b32 v158, v38\n\t"
    "v_mov_b32 v159, v39"
    :: "{v[128:159]}"(w), "{v[56:63]}"(x), "v"(a) : "v16", "v17", "v18", "v19", "v20", "v21", "v22", "v23", "v24", "v25", "v26", "v27", "v28", "v29", "v30", "v31", "v32", "v33", "v34", "v35", "v36", "v37", "v38", "v39", "memory");
}
template <> __device__ __forceinline__ void qk_swapk<3>(u32x32& w, u32x8& x, unsigned a) {
  asm volatile("ds_read_b128 v[16:19], %2 offset:0\n\t"
    "ds_read_b128 v[20:23], %2 offset:1024\n\t"
    "ds_read_b128 v[24:27], %2 offset:8192\n\t"
    "ds_read_b128 v[28:31], %2 offset:9216\n\t"
    "ds_read_b128 v[32:35], %2 offset:16384\n\t"
    "ds_read_b128 v[36:39], %2 offset:17408\n\t"
    "ds_write_b128 %2, v[168:171] offset:0\n\t"
    "ds_write_b128 %2, v[172:175] offset:1024\n\t"
    "ds_write_b128 %2, v[176:179] offset:8192\n\t"
    "ds_write_b128 %2, v[180:183] offset:9216\n\t"
    "ds_write_b128 %2, v[184:187] offset:16384\n\t"
    "ds_write_b128 %2, v[188:191] offset:17408\n\t"
    "v_swap_b32 v160, v56\n\t"
    "v_swap_b32 v161, v57\n\t"
    "v_swap_b32 v162, v58\n\t"
    "v_swap_b32 v163, v59\n\t"
    "v_swap_b32 v164, v60\n\t"
    "v_swap_b32 v165, v61\n\t"
    "v_swap_b32 v166, v62\n\t"
    "v_swap_b32 v167, v63\n\t"
    "s_waitcnt lgkmcnt(0)\n\t"
    "v_mov_b32 v168, v16\n\t"
    "v_mov_b32 v169, v17\n\t"
    "v_mov_b32 v170, v18\n\t"
    "v_mov_b32 v171, v19\n\t"
    "v_mov_b32 v172, v20\n\t"
    "v_mov_b32 v173, v21\n\t"
    "v_mov_b32 v174, v22\n\t"
    "v_mov_b32 v175, v23\n\t"
    "v_mov_b32 v176, v24\n\t"
    "v_mov_b32 v177, v25\n\t"
    "v_mov_b32 v178, v26\n\t"
    "v_mov_b32 v179, v27\n\t"
    "v_mov_b32 v180, v28\n\t"
    "v_mov_b32 v181, v29\n\t"
    "v_mov_b32 v182, v30\n\t"
    "v_mov_b32 v183, v31\n\t"
    "v_mov_b32 v184, v32\n\t"
    "v_mov_b32 v185, v33\n\t"
    "v_mov_b32 v186, v34\n\t"
    "v_mov_b32 v187, v35\n\t"
    "v_mov_b32 v188, v36\n\t"
    "v_mov_b32 v189, v37\n\t"
    "v_mov_b32 v190, v38\n\t"
    "v_mov_b32 v191, v39"
    :: "{v[160:191]}"(w), "{v[56:63]}"(x), "v"(a) : "v16", "v17", "v18", "v19", "v20", "v21", "v22", "v23", "v24", "v25", "v26", "v27", "v28", "v29", "v30", "v31", "v32", "v33", "v34", "v35", "v36", "v37", "v38", "v39", "memory");
}
template <> __device__ __forceinline__ void qk_swapk<4>(u32x32& w, u32x8& x, unsigned a) {
  asm volatile("ds_read_b128 v[16:19], %2 offset:0\n\t"
    "ds_read_b128 v[20:23], %2 offset:1024\n\t"
    "ds_read_b128 v[24:27], %2 offset:8192\n\t"
    "ds_read_b128 v[28:31], %2 offset:9216\n\t"
    "ds_read_b128 v[32:35], %2 offset:16384\n\t"
    "ds_read_b128 v[36:39], %2 offset:17408\n\t"
    "ds_write_b128 %2, v[200:203] offset:0\n\t"
    "ds_write_b128 %2, v[204:207] offset:1024\n\t"
    "ds_write_b128 %2, v[208:211] offset:8192\n\t"
    "ds_write_b128 %2, v[212:215] offset:9216\n\t"
    "ds_write_b128 %2, v[216:219] offset:16384\n\t"
    "ds_write_b128 %2, v[220:223] offset:17408\n\t"
    "v_swap_b32 v192, v56\n\t"
    "v_swap_b32 v193, v57\n\t"
    "v_swap_b32 v194, v58\n\t"
    "v_swap_b32 v195, v59\n\t"
    "v_swap_b32 v196, v60\n\t"
    "v_swap_b32 v197, v61\n\t"
    "v_swap_b32 v198, v62\n\t"
    "v_swap_b32 v199, v63\n\t"
    "s_waitcnt lgkmcnt(0)\n\t"
    "v_mov_b32 v200, v16\n\t"
    "v_mov_b32 v201, v17\n\t"
    "v_mov_b32 v202, v18\n\t"
    "v_mov_b32 v203, v19\n\t"
    "v_mov_b32 v204, v20\n\t"
    "v_mov_b32 v205, v21\n\t"
    "v_mov_b32 v206, v22\n\t"
    "v_mov_b32 v207, v23\n\t"
    "v_mov_b32 v208, v24\n\t"
    "v_mov_b32 v209, v25\n\t"
    "v_mov_b32 v210, v26\n\t"
    "v_mov_b32 v211, v27\n\t"
    "v_mov_b32 v212, v28\n\t"
    "v_mov_b32 v213, v29\n\t"
    "v_mov_b32 v214, v30\n\t"
    "v_mov_b32 v215, v31\n\t"
    "v_mov_b32 v216, v32\n\t"
    "v_mov_b32 v217, v33\n\t"
    "v_mov_b32 v218, v34\n\t"
    "v_mov_b32 v219, v35\n\t"
    "v_mov_b32 v220, v36\n\t"
    "v_mov_b32 v221, v37\n\t"
    "v_mov_b32 v222, v38\n\t"
    "v_mov_b32 v223, v39"
    :: "{v[192:223]}"(w), "{v[56:63]}"(x), "v"(a) : "v16", "v17", "v18", "v19", "v20", "v21", "v22", "v23", "v24", "v25", "v26", "v27", "v28", "v29", "v30", "v31", "v32", "v33", "v34", "v35", "v36", "v37", "v38", "v39", "memory");
}
template <> __device__ __forceinline__ void qk_swapk<5>(u32x32& w, u32x8& x, unsigned a) {
  asm volatile("ds_read_b128 v[16:19], %2 offset:0\n\t"
    "ds_read_b128 v[20:23], %2 offset:1024\n\t"
    "ds_read_b128 v[24:27], %2 offset:8192\n\t"
    "ds_read_b128 v[28:31], %2 offset:9216\n\t"
    "ds_read_b128 v[32:35], %2 offset:16384\n\t"
    "ds_read_b128 v[36:39], %2 offset:17408\n\t"
    "ds_write_b128 %2, v[232:235] offset:0\n\t"
    "ds_write_b128 %2, v[236:239] offset:1024\n\t"
    "ds_write_b128 %2, v[240:243] offset:8192\n\t"
    "ds_write_b128 %2, v[244:247] offset:9216\n\t"
    "ds_write_b128 %2, v[248:251] offset:16384\n\t"
    "ds_write_b128 %2, v[252:255] offset:17408\n\t"
    "v_swap_b32 v224, v56\n\t"
    "v_swap_b32 v225, v57\n\t"
    "v_swap_b32 v226, v58\n\t"
    "v_swap_b32 v227, v59\n\t"
    "v_swap_b32 v228, v60\n\t"
    "v_swap_b32 v229, v61\n\t"
    "v_swap_b32 v230, v62\n\t"
    "v_swap_b32 v231, v63\n\t"
    "s_waitcnt lgkmcnt(0)\n\t"
    "v_mov_b32 v232, v16\n\t"
    "v_mov_b32 v233, v17\n\t"
    "v_mov_b32 v234, v18\n\t"
    "v_mov_b32 v235, v19\n\t"
    "v_mov_b32 v236, v20\n\t"
    "v_mov_b32 v237, v21\n\t"
    "v_mov_b32 v238, v22\n\t"
    "v_mov_b32 v239, v23\n\t"
    "v_mov_b32 v240, v24\n\t"
    "v_mov_b32 v241, v25\n\t"
    "v_mov_b32 v242, v26\n\t"
    "v_mov_b32 v243, v27\n\t"
    "v_mov_b32 v244, v28\n\t"
    "v_mov_b32 v245, v29\n\t"
    "v_mov_b32 v246, v30\n\t"
    "v_mov_b32 v247, v31\n\t"
    "v_mov_b32 v248, v32\n\t"
    "v_mov_b32 v249, v33\n\t"
    "v_mov_b32 v250, v34\n\t"
    "v_mov_b32 v251, v35\n\t"
    "v_mov_b32 v252, v36\n\t"
    "v_mov_b32 v253, v37\n\t"
    "v_mov_b32 v254, v38\n\t"
    "v_mov_b32 v255, v39"
    :: "{v[224:255]}"(w), "{v[56:63]}"(x), "v"(a) : "v16", "v17", "v18", "v19", "v20", "v21", "v22", "v23", "v24", "v25", "v26", "v27", "v28", "v29", "v30", "v31", "v32", "v33", "v34", "v35", "v36", "v37", "v38", "v39", "memory");
}
template <int I> __device__ __forceinline__ void vr_wload(u32x32& w, unsigned a);
template <> __device__ __forceinline__ void vr_wload<0>(u32x32& w, unsigned a) {
  asm volatile("ds_read_b64_tr_b16 v[64:65], %1 offset:0\n\t"
    "ds_read_b64_tr_b16 v[66:67], %1 offset:256\n\t"
    "ds_read_b64_tr_b16 v[68:69], %1 offset:2048\n\t"
    "ds_read_b64_tr_b16 v[70:71], %1 offset:2304\n\t"
    "ds_read_b64_tr_b16 v[72:73], %1 offset:32\n\t"
    "ds_read_b64_tr_b16 v[74:75], %1 offset:288\n\t"
    "ds_read_b64_tr_b16 v[76:77], %1 offset:2080\n\t"
    "ds_read_b64_tr_b16 v[78:79], %1 offset:2336\n\t"
    "s_waitcnt lgkmcnt(0)\n\t"
    "ds_read_b64_tr_b16 v[80:81], %1 offset:64\n\t"
    "ds_read_b64_tr_b16 v[82:83], %1 offset:320\n\t"
    "ds_read_b64_tr_b16 v[84:85], %1 offset:2112\n\t"
    "ds_read_b64_tr_b16 v[86:87], %1 offset:2368\n\t"
    "ds_read_b64_tr_b16 v[88:89], %1 offset:96\n\t"
    "ds_read_b64_tr_b16 v[90:91], %1 offset:352\n\t"
    "ds_read_b64_tr_b16 v[92:93], %1 offset:2144\n\t"
    "ds_read_b64_tr_b16 v[94:95], %1 offset:2400\n\t"
    "s_waitcnt lgkmcnt(0)"
    : "=&{v[64:95]}"(w) : "v"(a) : "memory");
}
template <> __device__ __forceinline__ void vr_wload<1>(u32x32& w, unsigned a) {
  asm volatile("ds_read_b64_tr_b16 v[96:97], %1 offset:8192\n\t"
    "ds_read_b64_tr_b16 v[98:99], %1 offset:8448\n\t"
    "ds_read_b64_tr_b16 v[100:101], %1 offset:10240\n\t"
    "ds_read_b64_tr_b16 v[102:103], %1 offset:10496\n\t"
    "ds_read_b64_tr_b16 v[104:105], %1 offset:8224\n\t"
    "ds_read_b64_tr_b16 v[106:107], %1 offset:8480\n\t"
    "ds_read_b64_tr_b16 v[108:109], %1 offset:10272\n\t"
    "ds_read_b64_tr_b16 v[110:111], %1 offset:10528\n\t"
    "s_waitcnt lgkmcnt(0)\n\t"
    "ds_read_b64_tr_b16 v[112:113], %1 offset:8256\n\t"
    "ds_read_b64_tr_b16 v[114:115], %1 offset:8512\n\t"
    "ds_read_b64_tr_b16 v[116:117], %1 offset:10304\n\t"
    "ds_read_b64_tr_b16 v[118:119], %1 offset:10560\n\t"
    "ds_read_b64_tr_b16 v[120:121], %1 offset:8288\n\t"
    "ds_read_b64_tr_b16 v[122:123], %1 offset:8544\n\t"
    "ds_read_b64_tr_b16 v[124:125], %1 offset:10336\n\t"
    "ds_read_b64_tr_b16 v[126:127], %1 offset:10592\n\t"
    "s_waitcnt lgkmcnt(0)"
    : "=&{v[96:127]}"(w) : "v"(a) : "memory");
}
template <> __device__ __forceinline__ void vr_wload<2>(u32x32& w, unsigned a) {
  asm volatile("ds_read_b64_tr_b16 v[128:129], %1 offset:0\n\t"
    "ds_read_b64_tr_b16 v[130:131], %1 offset:128\n\t"
    "ds_read_b64_tr_b16 v[132:133], %1 offset:1024\n\t"
    "ds_read_b64_tr_b16 v[134:135], %1 offset:1152\n\t"
    "ds_read_b64_tr_b16 v[136:137], %1 offset:32\n\t"
    "ds_read_b64_tr_b16 v[138:139], %1 offset:160\n\t"
    "ds_read_b64_tr_b16 v[140:141], %1 offset:1056\n\t"
    "ds_read_b64_tr_b16 v[142:143], %1 offset:1184\n\t"
    "s_waitcnt lgkmcnt(0)\n\t"
    "ds_read_b64_tr_b16 v[144:145], %1 offset:4096\n\t"
    "ds_read_b64_tr_b16 v[146:147], %1 offset:4224\n\t"
    "ds_read_b64_tr_b16 v[148:149], %1 offset:5120\n\t"
    "ds_read_b64_tr_b16 v[150:151], %1 offset:5248\n\t"
    "ds_read_b64_tr_b16 v[152:153], %1 offset:4128\n\t"
    "ds_read_b64_tr_b16 v[154:155], %1 offset:4256\n\t"
    "ds_read_b64_tr_b16 v[156:157], %1 offset:5152\n\t"
    "ds_read_b64_tr_b16 v[158:159], %1 offset:5280\n\t"
    "s_waitcnt lgkmcnt(0)"
    : "=&{v[128:159]}"(w) : "v"(a) : "memory");
}
template <> __device__ __forceinline__ void vr_wload<3>(u32x32& w, unsigned a) {
  asm volatile("ds_read_b64_tr_b16 v[160:161], %1 offset:8192\n\t"
    "ds_read_b64_tr_b16 v[162:163], %1 offset:8320\n\t"
    "ds_read_b64_tr_b16 v[164:165], %1 offset:9216\n\t"
    "ds_read_b64_tr_b16 v[166:167], %1 offset:9344\n\t"
    "ds_read_b64_tr_b16 v[168:169], %1 offset:8224\n\t"
    "ds_read_b64_tr_b16 v[170:171], %1 offset:8352\n\t"
    "ds_read_b64_tr_b16 v[172:173], %1 offset:9248\n\t"
    "ds_read_b64_tr_b16 v[174:175], %1 offset:9376\n\t"
    "s_waitcnt lgkmcnt(0)\n\t"
    "ds_read_b64_tr_b16 v[176:177], %1 offset:12288\n\t"
    "ds_read_b64_tr_b16 v[178:179], %1 offset:12416\n\t"
    "ds_read_b64_tr_b16 v[180:181], %1 offset:13312\n\t"
    "ds_read_b64_tr_b16 v[182:183], %1 offset:13440\n\t"
    "ds_read_b64_tr_b16 v[184:185], %1 offset:12320\n\t"
    "ds_read_b64_tr_b16 v[186:187], %1 offset:12448\n\t"
    "ds_read_b64_tr_b16 v[188:189], %1 offset:13344\n\t"
    "ds_read_b64_tr_b16 v[190:191], %1 offset:13472\n\t"
    "s_waitcnt lgkmcnt(0)"
    : "=&{v[160:191]}"(w) : "v"(a) : "memory");
}
template <> __device__ __forceinline__ void vr_wload<4>(u32x32& w, unsigned a) {
  asm volatile("ds_read_b64_tr_b16 v[192:193], %1 offset:0\n\t"
    "ds_read_b64_tr_b16 v[194:195], %1 offset:128\n\t"
    "ds_read_b64_tr_b16 v[196:197], %1 offset:1024\n\t"
    "ds_read_b64_tr_b16 v[198:199], %1 offset:1152\n\t"
    "ds_read_b64_tr_b16 v[200:201], %1 offset:32\n\t"
    "ds_read_b64_tr_b16 v[202:203], %1 offset:160\n\t"
    "ds_read_b64_tr_b16 v[204:205], %1 offset:1056\n\t"
    "ds_read_b64_tr_b16 v[206:207], %1 offset:1184\n\t"
    "s_waitcnt lgkmcnt(0)\n\t"
    "ds_read_b64_tr_b16 v[208:209], %1 offset:4096\n\t"
    "ds_read_b64_tr_b16 v[210:211], %1 offset:4224\n\t"
    "ds_read_b64_tr_b16 v[212:213], %1 offset:5120\n\t"
    "ds_read_b64_tr_b16 v[214:215], %1 offset:5248\n\t"
    "ds_read_b64_tr_b16 v[216:217], %1 offset:4128\n\t"
    "ds_read_b64_tr_b16 v[218:219], %1 offset:4256\n\t"
    "ds_read_b64_tr_b16 v[220:221], %1 offset:5152\n\t"
    "ds_read_b64_tr_b16 v[222:223], %1 offset:5280\n\t"
    "s_waitcnt lgkmcnt(0)"
    : "=&{v[192:223]}"(w) : "v"(a) : "memory");
}
template <> __device__ __forceinline__ void vr_wload<5>(u32x32& w, unsigned a) {
  asm volatile("ds_read_b64_tr_b16 v[224:225], %1 offset:8192\n\t"
    "ds_read_b64_tr_b16 v[226:227], %1 offset:8320\n\t"
    "ds_read_b64_tr_b16 v[228:229], %1 offset:9216\n\t"
    "ds_read_b64_tr_b16 v[230:231], %1 offset:9344\n\t"
    "ds_read_b64_tr_b16 v[232:233], %1 offset:8224\n\t"
    "ds_read_b64_tr_b16 v[234:235], %1 offset:8352\n\t"
    "ds_read_b64_tr_b16 v[236:237], %1 offset:9248\n\t"
    "ds_read_b64_tr_b16 v[238:239], %1 offset:9376\n\t"
    "s_waitcnt lgkmcnt(0)\n\t"
    "ds_read_b64_tr_b16 v[240:241], %1 offset:12288\n\t"
    "ds_read_b64_tr_b16 v[242:243], %1 offset:12416\n\t"
    "ds_read_b64_tr_b16 v[244:245], %1 offset:13312\n\t"
    "ds_read_b64_tr_b16 v[246:247], %1 offset:13440\n\t"
    "ds_read_b64_tr_b16 v[248:249], %1 offset:12320\n\t"
    "ds_read_b64_tr_b16 v[250:251], %1 offset:12448\n\t"
    "ds_read_b64_tr_b16 v[252:253], %1 offset:13344\n\t"
    "ds_read_b64_tr_b16 v[254:255], %1 offset:13472\n\t"
    "s_waitcnt lgkmcnt(0)"
    : "=&{v[224:255]}"(w) : "v"(a) : "memory");
}
#define OREP 2
#define AREP 2
struct VRW { u32x32 v0, v1, o0, o1, o2, o3; };
__device__ __forceinline__ void vr_v(const VRW& W, const char* ag, const char* vd, unsigned wa, unsigned ra, unsigned& cap, unsigned& sp, unsigned long long& tc, unsigned long long* ts) {
  asm volatile("v_mbcnt_lo_u32_b32 v16, -1, 0\n\t"
    "v_mbcnt_hi_u32_b32 v16, -1, v16\n\t"
    "v_lshlrev_b32 v17, 4, v16\n\t"
    "s_mov_b32 %[sp], 0\n\t"
    "global_load_dwordx4 v[24:27], v17, %[ag] sc1\n\t"
    "global_load_dwordx4 v[28:31], v17, %[ag] offset:1024 sc1\n\t"
    "s_memtime %[tc]\n\t"
    "v_and_b32 v22, 4, v16\n\t"
    "v_mul_u32_u24 v22, 0x2aaa, v22\n\t"
    "v_lshrrev_b32 v18, 1, v16\n\t"
    "v_and_b32 v18, 2, v18\n\t"
    "v_or3_b32 v18, v18, v22, %[c44]\n\t"
    "v_lshl_add_u32 v19, v16, 3, %[wa]\n\t"
    "v_lshl_add_u32 v20, v16, 3, %[ra]\n\t"
    "v_and_b32 v22, 40, v16\n\t"
    "v_lshlrev_b32 v21, 3, v22\n\t"
    "v_and_b32 v22, 3, v16\n\t"
    "v_lshl_or_b32 v21, v22, 4, v21\n\t"
    "v_and_b32 v22, 4, v16\n\t"
    "v_lshl_or_b32 v21, v22, 1, v21\n\t"
    "v_lshrrev_b32 v22, 2, v16\n\t"
    "v_and_or_b32 v21, v22, 4, v21\n\t"
    "3:\n\t"
    "v_mov_b32 v32, 0\n\t"
    "v_mov_b32 v33, 0\n\t"
    "v_mov_b32 v34, 0\n\t"
    "v_mov_b32 v35, 0\n\t"
    "v_mov_b32 v36, 0\n\t"
    "v_mov_b32 v37, 0\n\t"
    "v_mov_b32 v38, 0\n\t"
    "v_mov_b32 v39, 0\n\t"
    "v_mov_b32 v40, 0\n\t"
    "v_mov_b32 v41, 0\n\t"
    "v_mov_b32 v42, 0\n\t"
    "v_mov_b32 v43, 0\n\t"
    "v_mov_b32 v44, 0\n\t"
    "v_mov_b32 v45, 0\n\t"
    "v_mov_b32 v46, 0\n\t"
    "v_mov_b32 v47, 0\n\t"
    "s_waitcnt vmcnt(0) lgkmcnt(0)\n\t"
    "v_smfmac_f32_16x16x64_bf16 v[32:35], v[24:27], v[64:71], v18\n\t"
    "v_smfmac_f32_16x16x64_bf16 v[32:35], v[28:31], v[96:103], v18\n\t"
    "v_max3_u32 v22, v24, v25, v26\n\t"
    "v_max3_u32 v23, v27, v28, v29\n\t"
    "v_smfmac_f32_16x16x64_bf16 v[36:39], v[24:27], v[72:79], v18\n\t"
    "v_max3_u32 v22, v22, v30, v31\n\t"
    "v_max_u32 v22, v22, v23\n\t"
    "v_smfmac_f32_16x16x64_bf16 v[36:39], v[28:31], v[104:111], v18\n\t"
    "v_cmp_eq_u32 vcc, -1, v22\n\t"
    "s_cbranch_vccnz 1f\n\t"
    "2:\n\t"
    "v_smfmac_f32_16x16x64_bf16 v[40:43], v[24:27], v[80:87], v18\n\t"
    "v_permlane16_swap_b32 v32, v34\n\t"
    "v_permlane16_swap_b32 v33, v35\n\t"
    "v_smfmac_f32_16x16x64_bf16 v[40:43], v[28:31], v[112:119], v18\n\t"
    "v_pk_add_f32 v[32:33], v[32:33], v[34:35]\n\t"
    "ds_write_b64 v19, v[32:33] offset:0\n\t"
    "v_smfmac_f32_16x16x64_bf16 v[44:47], v[24:27], v[88:95], v18\n\t"
    "s_nop 0\n\t"
    "v_permlane16_swap_b32 v36, v38\n\t"
    "v_permlane16_swap_b32 v37, v39\n\t"
    "v_smfmac_f32_16x16x64_bf16 v[44:47], v[28:31], v[120:127], v18\n\t"
    "v_pk_add_f32 v[36:37], v[36:37], v[38:39]\n\t"
    "ds_write_b64 v19, v[36:37] offset:512\n\t"
    "v_permlane16_swap_b32 v40, v42\n\t"
    "v_permlane16_swap_b32 v41, v43\n\t"
    "v_pk_add_f32 v[40:41], v[40:41], v[42:43]\n\t"
    "ds_write_b64 v19, v[40:41] offset:1024\n\t"
    "s_nop 1\n\t"
    "v_permlane16_swap_b32 v44, v46\n\t"
    "v_permlane16_swap_b32 v45, v47\n\t"
    "v_pk_add_f32 v[44:45], v[44:45], v[46:47]\n\t"
    "ds_write_b64 v19, v[44:45] offset:1536\n\t"
    "s_cmp_ge_u32 %[wa], 8192\n\t"
    "s_waitcnt lgkmcnt(0)\n\t"
    "s_barrier\n\t"
    "s_cbranch_scc1 9f\n\t"
    "ds_read_b64 v[48:49], v20 offset:0\n\t"
    "ds_read_b64 v[50:51], v20 offset:2048\n\t"
    "ds_read_b64 v[52:53], v20 offset:4096\n\t"
    "ds_read_b64 v[54:55], v20 offset:6144\n\t"
    "ds_read_b64 v[56:57], v20 offset:8192\n\t"
    "ds_read_b64 v[58:59], v20 offset:10240\n\t"
    "ds_read_b64 v[60:61], v20 offset:12288\n\t"
    "ds_read_b64 v[62:63], v20 offset:14336\n\t"
    "s_waitcnt lgkmcnt(6)\n\t"
    "v_pk_add_f32 v[48:49], v[48:49], v[50:51]\n\t"
    "s_waitcnt lgkmcnt(4)\n\t"
    "v_pk_add_f32 v[52:53], v[52:53], v[54:55]\n\t"
    "v_pk_add_f32 v[48:49], v[48:49], v[52:53]\n\t"
    "s_waitcnt lgkmcnt(2)\n\t"
    "v_pk_add_f32 v[56:57], v[56:57], v[58:59]\n\t"
    "s_waitcnt lgkmcnt(0)\n\t"
    "v_pk_add_f32 v[60:61], v[60:61], v[62:63]\n\t"
    "v_pk_add_f32 v[56:57], v[56:57], v[60:61]\n\t"
    "v_pk_add_f32 v[48:49], v[48:49], v[56:57]\n\t"
    "v_cvt_pk_bf16_f32 v22, v48, v49\n\t"
    "v_min_u32 v22, -2, v22\n\t"
    "global_store_dword v21, v22, %[vd] sc1\n\t"
    "s_waitcnt lgkmcnt(0)\n\t"
    "s_branch 9f\n\t"
    "1:\n\t"
    "s_add_u32 %[sp], %[sp], 1\n\t"
    "s_cmp_ge_u32 %[sp], %[cap]\n\t"
    "s_cbranch_scc1 7f\n\t"
    "global_load_dwordx4 v[24:27], v17, %[ag] sc1\n\t"
    "global_load_dwordx4 v[28:31], v17, %[ag] offset:1024 sc1\n\t"
    "s_waitcnt lgkmcnt(0)\n\t"
    "s_memtime %[tc]\n\t"
    "s_nop 7\n\t"
    "s_branch 3b\n\t"
    "7:\n\t"
    "s_mov_b32 %[cap], 0\n\t"
    "s_branch 2b\n\t"
    "9:"
    : [tc] "=&s"(tc), [sp] "=&s"(sp), [cap] "+s"(cap)
    : [ag] "s"(ag), [vd] "s"(vd), [wa] "s"(wa), [ra] "s"(ra), [c44] "s"(0x4444u), "{v[64:95]}"(W.v0), "{v[96:127]}"(W.v1), "{v[128:159]}"(W.o0), "{v[160:191]}"(W.o1), "{v[192:223]}"(W.o2), "{v[224:255]}"(W.o3)
    : "v16", "v17", "v18", "v19", "v20", "v21", "v22", "v23", "v24", "v25", "v26", "v27", "v28", "v29", "v30", "v31", "v32", "v33", "v34", "v35", "v36", "v37", "v38", "v39", "v40", "v41", "v42", "v43", "v44", "v45", "v46", "v47", "v48", "v49", "v50", "v51", "v52", "v53", "v54", "v55", "v56", "v57", "v58", "v59", "v60", "v61", "v62", "v63", "vcc", "scc", "memory");
}
__device__ __forceinline__ void vr_r(const VRW& W, const char* os, const char* ad, unsigned wa, unsigned jx, unsigned& cap, unsigned& sp, unsigned long long* ts) {
  asm volatile("v_mbcnt_lo_u32_b32 v16, -1, 0\n\t"
    "v_mbcnt_hi_u32_b32 v16, -1, v16\n\t"
    "v_lshl_or_b32 v17, v16, 4, %[jx]\n\t"
    "global_load_dwordx4 v[24:27], v17, %[os] sc1\n\t"
    "v_xor_b32 v18, 2048, v17\n\t"
    "global_load_dwordx4 v[28:31], v18, %[os] sc1\n\t"
    "v_xor_b32 v57, 4096, v17\n\t"
    "global_load_dwordx4 v[32:35], v57, %[os] sc1\n\t"
    "v_xor_b32 v58, 6144, v17\n\t"
    "global_load_dwordx4 v[36:39], v58, %[os] sc1\n\t"
    "s_mov_b32 %[sp], 0\n\t"
    "v_and_b32 v23, 4, v16\n\t"
    "v_mul_u32_u24 v23, 0x2aaa, v23\n\t"
    "v_lshrrev_b32 v19, 1, v16\n\t"
    "v_and_b32 v19, 2, v19\n\t"
    "v_or3_b32 v19, v19, v23, %[c44]\n\t"
    "v_and_b32 v23, 15, v16\n\t"
    "v_lshl_add_u32 v20, v23, 2, %[wa]\n\t"
    "v_and_b32 v23, 48, v16\n\t"
    "v_lshl_add_u32 v20, v23, 3, v20\n\t"
    "v_and_b32 v23, 32, v16\n\t"
    "v_and_b32 v21, 28, v16\n\t"
    "v_lshlrev_b32 v21, 4, v21\n\t"
    "v_or_b32 v21, v21, v23\n\t"
    "v_lshlrev_b32 v22, 3, v23\n\t"
    "v_and_b32 v23, 3, v16\n\t"
    "v_lshl_or_b32 v21, v23, 3, v21\n\t"
    "v_and_b32 v23, 31, v16\n\t"
    "v_lshl_or_b32 v22, v23, 2, v22\n\t"
    "3:\n\t"
    "v_mov_b32 v40, 0\n\t"
    "v_mov_b32 v41, 0\n\t"
    "v_mov_b32 v42, 0\n\t"
    "v_mov_b32 v43, 0\n\t"
    "v_mov_b32 v44, 0\n\t"
    "v_mov_b32 v45, 0\n\t"
    "v_mov_b32 v46, 0\n\t"
    "v_mov_b32 v47, 0\n\t"
    "v_mov_b32 v48, 0\n\t"
    "v_mov_b32 v49, 0\n\t"
    "v_mov_b32 v50, 0\n\t"
    "v_mov_b32 v51, 0\n\t"
    "v_mov_b32 v52, 0\n\t"
    "v_mov_b32 v53, 0\n\t"
    "v_mov_b32 v54, 0\n\t"
    "v_mov_b32 v55, 0\n\t"
    "v_mov_b32 v56, 0\n\t"
    "s_waitcnt vmcnt(3)\n\t"
    "v_smfmac_f32_16x16x64_bf16 v[44:47], v[24:27], v[144:151], v19\n\t"
    "v_max3_u32 v56, v56, v24, v25\n\t"
    "v_smfmac_f32_16x16x64_bf16 v[48:51], v[24:27], v[136:143], v19\n\t"
    "v_max3_u32 v56, v56, v26, v27\n\t"
    "v_smfmac_f32_16x16x64_bf16 v[40:43], v[24:27], v[128:135], v19\n\t"
    "v_smfmac_f32_16x16x64_bf16 v[52:55], v[24:27], v[152:159], v19\n\t"
    "s_waitcnt vmcnt(2)\n\t"
    "v_smfmac_f32_16x16x64_bf16 v[44:47], v[28:31], v[176:183], v19\n\t"
    "v_max3_u32 v56, v56, v28, v29\n\t"
    "v_smfmac_f32_16x16x64_bf16 v[48:51], v[28:31], v[168:175], v19\n\t"
    "v_max3_u32 v56, v56, v30, v31\n\t"
    "v_smfmac_f32_16x16x64_bf16 v[40:43], v[28:31], v[160:167], v19\n\t"
    "v_smfmac_f32_16x16x64_bf16 v[52:55], v[28:31], v[184:191], v19\n\t"
    "s_waitcnt vmcnt(1)\n\t"
    "v_smfmac_f32_16x16x64_bf16 v[44:47], v[32:35], v[208:215], v19\n\t"
    "v_max3_u32 v56, v56, v32, v33\n\t"
    "v_smfmac_f32_16x16x64_bf16 v[48:51], v[32:35], v[200:207], v19\n\t"
    "v_max3_u32 v56, v56, v34, v35\n\t"
    "v_smfmac_f32_16x16x64_bf16 v[40:43], v[32:35], v[192:199], v19\n\t"
    "v_smfmac_f32_16x16x64_bf16 v[52:55], v[32:35], v[216:223], v19\n\t"
    "s_waitcnt vmcnt(0)\n\t"
    "v_smfmac_f32_16x16x64_bf16 v[44:47], v[36:39], v[240:247], v19\n\t"
    "v_max3_u32 v56, v56, v36, v37\n\t"
    "v_smfmac_f32_16x16x64_bf16 v[48:51], v[36:39], v[232:239], v19\n\t"
    "v_max3_u32 v56, v56, v38, v39\n\t"
    "v_smfmac_f32_16x16x64_bf16 v[40:43], v[36:39], v[224:231], v19\n\t"
    "v_cmp_eq_u32 vcc, -1, v56\n\t"
    "v_smfmac_f32_16x16x64_bf16 v[52:55], v[36:39], v[248:255], v19\n\t"
    "s_cbranch_vccnz 4f\n\t"
    "5:\n\t"
    "s_nop 2\n\t"
    "v_permlane32_swap_b32 v44, v48\n\t"
    "v_permlane32_swap_b32 v45, v49\n\t"
    "v_permlane32_swap_b32 v46, v50\n\t"
    "v_permlane32_swap_b32 v47, v51\n\t"
    "s_mov_b32 exec_hi, 0\n\t"
    "v_pk_add_f32 v[40:41], v[40:41], v[48:49]\n\t"
    "v_pk_add_f32 v[42:43], v[42:43], v[50:51]\n\t"
    "s_mov_b64 exec, %[hi]\n\t"
    "v_pk_add_f32 v[40:41], v[44:45], v[52:53]\n\t"
    "v_pk_add_f32 v[42:43], v[46:47], v[54:55]\n\t"
    "s_mov_b32 exec_lo, -1\n\t"
    "s_nop 0\n\t"
    "v_permlane16_swap_b32 v40, v42\n\t"
    "v_permlane16_swap_b32 v41, v43\n\t"
    "v_add_f32 v40, v40, v42\n\t"
    "v_add_f32 v41, v41, v43\n\t"
    "ds_write_b32 v20, v40 offset:16384\n\t"
    "ds_write_b32 v20, v41 offset:16448\n\t"
    "s_cmp_ge_u32 %[wa], 1024\n\t"
    "s_waitcnt lgkmcnt(0)\n\t"
    "s_barrier\n\t"
    "s_cbranch_scc1 9f\n\t"
    "ds_read_b64 v[24:25], v21 offset:16384\n\t"
    "ds_read_b64 v[26:27], v21 offset:16896\n\t"
    "ds_read_b64 v[28:29], v21 offset:17408\n\t"
    "ds_read_b64 v[30:31], v21 offset:17920\n\t"
    "ds_read_b64 v[32:33], v21 offset:18432\n\t"
    "ds_read_b64 v[34:35], v21 offset:18944\n\t"
    "ds_read_b64 v[36:37], v21 offset:19456\n\t"
    "ds_read_b64 v[38:39], v21 offset:19968\n\t"
    "s_waitcnt lgkmcnt(6)\n\t"
    "v_pk_add_f32 v[24:25], v[24:25], v[26:27]\n\t"
    "s_waitcnt lgkmcnt(4)\n\t"
    "v_pk_add_f32 v[28:29], v[28:29], v[30:31]\n\t"
    "v_pk_add_f32 v[24:25], v[24:25], v[28:29]\n\t"
    "s_waitcnt lgkmcnt(2)\n\t"
    "v_pk_add_f32 v[32:33], v[32:33], v[34:35]\n\t"
    "s_waitcnt lgkmcnt(0)\n\t"
    "v_pk_add_f32 v[36:37], v[36:37], v[38:39]\n\t"
    "v_pk_add_f32 v[32:33], v[32:33], v[36:37]\n\t"
    "v_pk_add_f32 v[24:25], v[24:25], v[32:33]\n\t"
    "v_cvt_pk_bf16_f32 v23, v24, v25\n\t"
    "v_min_u32 v23, -2, v23\n\t"
    "global_store_dword v22, v23, %[ad] sc1\n\t"
    "s_waitcnt lgkmcnt(0)\n\t"
    "s_branch 9f\n\t"
    "4:\n\t"
    "s_add_u32 %[sp], %[sp], 1\n\t"
    "s_cmp_ge_u32 %[sp], %[cap]\n\t"
    "s_cbranch_scc1 7f\n\t"
    "global_load_dwordx4 v[24:27], v17, %[os] sc1\n\t"
    "global_load_dwordx4 v[28:31], v18, %[os] sc1\n\t"
    "global_load_dwordx4 v[32:35], v57, %[os] sc1\n\t"
    "global_load_dwordx4 v[36:39], v58, %[os] sc1\n\t"
    "s_nop 7\n\t"
    "s_branch 3b\n\t"
    "7:\n\t"
    "s_mov_b32 %[cap], 0\n\t"
    "s_branch 5b\n\t"
    "9:"
    : [sp] "=&s"(sp), [cap] "+s"(cap)
    : [os] "s"(os), [ad] "s"(ad), [wa] "s"(wa), [jx] "s"(jx), [c44] "s"(0x4444u), [hi] "s"(0xFFFFFFFF00000000ull), "{v[64:95]}"(W.v0), "{v[96:127]}"(W.v1), "{v[128:159]}"(W.o0), "{v[160:191]}"(W.o1), "{v[192:223]}"(W.o2), "{v[224:255]}"(W.o3)
    : "v16", "v17", "v18", "v19", "v20", "v21", "v22", "v23", "v24", "v25", "v26", "v27", "v28", "v29", "v30", "v31", "v32", "v33", "v34", "v35", "v36", "v37", "v38", "v39", "v40", "v41", "v42", "v43", "v44", "v45", "v46", "v47", "v48", "v49", "v50", "v51", "v52", "v53", "v54", "v55", "v56", "v57", "v58", "v59", "v60", "v61", "v62", "v63", "vcc", "scc", "memory");
}
__device__ __forceinline__ void qk_body(char* smem, int wave_id_, int xg, int h, int wg, const u16* __restrict__ Wq, const u16* __restrict__ Wk,
                                        const char* __restrict__ Kx, const char* __restrict__ V, char* ws, unsigned* tmo, u64 tag) {
  const int wave = WAVE_ID;
  const int ty = wave >> 2, w4 = wave & 3;
  const char* wq = uni((const char*)(Wq + h * DH)); const char* wk = uni((const char*)(Wk + h * DH));
  auto stage = [&](int u) {
    stage_proj((u & 1) ? wk + (size_t)(((u >> 1) + 2) & 7) * 128 * HD * 2 : wq + (size_t)(u >> 1) * 128 * HD * 2, ((u + 4) % 5) * 32768, wave * 64 + lane_id(), wave);
  };
  QKW W;
  if (wave == 0) STS(ws, wg, 7);
  stage(0); stage(1); stage(2); stage(3);
#define WLD(i, w) do { WAIT_V(12); WBAR(); stage(2 * (i) + 4); if (ty) { WAIT_V(12); WBAR(); stage(2 * (i) + 5); } \
    { const int l_ = lane_id(); const unsigned o_ = ty ? ((2 * (i) + 5) % 5) * 32768 : ((2 * (i) + 4) % 5) * 32768; qk_wload<i>(w, proj_base(2 * w4, l_) + o_, proj_base(2 * w4 + 1, l_) + o_); } \
    if (!ty) { WAIT_V(12); WBAR(); stage(2 * (i) + 5); } if (wave == 0) STS(ws, wg, 1 + (i)); } while (0)
  WLD(0, W.w0); WLD(1, W.w1); WLD(2, W.w2); WLD(3, W.w3); WLD(4, W.w4); WLD(5, W.w5);
#undef WLD
  unsigned in = 0;
#pragma unroll
  for (int i = 0; i < 2; ++i) {
    if (i == 0) WAIT_V(8); else WAIT_V(0);
    WBAR();
    if (i == 0 && wave == 1) init_issue(ws, Q_V);
    if (i == 1) in = init_get(ws, tag, smem + Q_V);
    const int lane = lane_id();
    const unsigned o = Q_RING + ty * 32768 + i * 65536, f0 = proj_base(2 * w4, lane) + o, f1 = proj_base(2 * w4 + 1, lane) + o;
    u32x8 x[4] = {proj_frag(smem, f0), proj_frag(smem, f1), proj_frag(smem, f0 + 16384), proj_frag(smem, f1 + 16384)};
    asm volatile("" : "+v"(x[0]), "+v"(x[1]), "+v"(x[2]), "+v"(x[3]));
    BAR();
    char* d = smem + Q_CB + i * 57344 + ty * (i ? 32768 : 24576) + w4 * 2048 + lane * 16;
#pragma unroll
    for (int j = 0; j < 4; ++j)
      if (i || j || !ty) { *(u32x4*)(d + j * 8192) = u32x4{x[j][0], x[j][1], x[j][2], x[j][3]}; *(u32x4*)(d + j * 8192 + 1024) = u32x4{x[j][4], x[j][5], x[j][6], x[j][7]}; }
    if (i == 0 && ty) { *(u32x4*)(smem + Q_MEM + wave * 2048 + lane * 16) = u32x4{x[0][0], x[0][1], x[0][2], x[0][3]}; *(u32x4*)(smem + Q_MEM + wave * 2048 + 1024 + lane * 16) = u32x4{x[0][4], x[0][5], x[0][6], x[0][7]}; }
  }
  if (wave == 4) *(volatile LAS u32x2*)(smem + Q_TM) = u32x2{0u, 0u};
  BAR();
  if (wave == 0) STS(ws, wg, 0);
  unsigned* cnt = cnt_of(ws, in);
  const char* xb = uni(xch_base(ws) + ((size_t)3 << 21) + (size_t)((in & 2u) << 12));
  const char* oh = uni(xch_base(ws) + (size_t)((in & 1u) << 13) + (size_t)(h >> 2) * 16384 + (h & 3) * 2048);
  auto rearm = [&](int ps) {
    unsigned z = SENT; asm volatile("" : "+v"(z));
    const unsigned l16 = lane_id() * 16;
#pragma unroll
    for (int r = 0; r < 2 * OREP; ++r) st16s(oh + (size_t)(ps * 4 + (r >> 1)) * 131072, (r & 1) * 1024u + l16, u32x4{z, z, z, z});
  };
  const char* dbw = xb + (size_t)48 * 16384 + 4096 + h * 128;
  unsigned avail = 0;
  auto stage_kv = [&](int s) {
    if (avail <= (unsigned)s) {
      for (int spins = 0;; ++spins) {
        const unsigned p = ld4_sc1(cnt + (PQKV + h) * 32 + (lane_id() & 15));
        avail = min(qkv_avail(p, 1), qkv_avail(p, 2));
        if (avail > (unsigned)s) break;
        __builtin_amdgcn_s_sleep(8);
        if (give_up(spins, tmo, 5)) break;
      }
    }
    const char* ks = uni(Kx + ((size_t)s * NH + h) * 8192); const char* vs = uni(V + ((size_t)s * NH + h) * 8192);
    const int lane = lane_id();
    head_to_lds(ks, lane, Q_K); head_to_lds(vs, lane, Q_V);
  };
  auto loop = [&](auto w_c) __attribute__((always_inline)) {
    constexpr int WV = decltype(w_c)::value;
    if (WV >= 4) asm volatile("ds_read_b128 v[56:59], %1\n\tds_read_b128 v[60:63], %1 offset:1024\n\ts_waitcnt lgkmcnt(0)" : "=&{v[56:63]}"(W.x) : "v"(Q_MEM + WV * 2048 + lane_id() * 16) : "memory");
    if (WV == 6) stage_kv(0);
    if (QK_OWN) {
      constexpr int PI = WV < 4 ? WV : WV - 2;
      if constexpr (QK_OWN == 3 && WV >= 4) qk_swapk<PI>(PI == 2 ? W.w2 : PI == 3 ? W.w3 : PI == 4 ? W.w4 : W.w5, W.x, Q_CB + 32768 + (WV & 3) * 2048 + lane_id() * 16);
      else qk_swap<PI>(PI == 0 ? W.w0 : PI == 1 ? W.w1 : PI == 2 ? W.w2 : PI == 3 ? W.w3 : PI == 4 ? W.w4 : W.w5, Q_CB + 57344 + (WV >> 2) * 32768 + (WV & 3) * 2048 + lane_id() * 16);
    }
    if (WV < 4) __builtin_amdgcn_s_setprio(2);
    unsigned t0 = 0, cap = 60000, d0 = 105, d1 = 200;
    int dq = D_Q;
    int sl = 0;
    for (int s = 0; s < NSEG; ++s) {
      TSDECL;
      const char* ag = xb + (size_t)((sl * 4 + (AREP == 4 ? xg : (AREP == 2 ? ARSEL : 0))) * 2 + (WV >> 2)) * 16384 + (WV & 3) * 2048;
      const char* vs = WV == 4 ? dbw : WV == 5 ? ws + OFF_KVM + ((size_t)s * NH + h) * 4096 : xb + (size_t)(24 + sl * 8 + (h >> 2)) * 16384 + (h & 3) * 2048;
      const char* od = WV == 4 ? (const char*)(size_t)((unsigned)(s + 1) << 24) : oh + (size_t)sl * 524288;
      if (s > 0) wait_rt(t0, dq);
      TS(0);
      unsigned long long tc; unsigned sp;
      qk_step<WV>(W, ag, vs, od, LAST && s == NSEG - 1 ? nullptr : ws + OFF_KVM + ((size_t)s * NH + h) * 4096 + 2048, cap, sp, tc, nullptr, d0, d1);
      dq = (sp & 0xFFFFu) ? min(dq + DQU, D_Q + DQM) : max(dq - DQD, D_Q);
      if ((WV < 4 || WV == 5) && !(LAST && s == NSEG - 1)) {
        const unsigned pv = (unsigned)(s + 1) << 24 | (rt_now() & 0xFFFFFFu);
        WAIT_V(0);
        if (lane_id() == 0) st4_sc1(cnt + (PROG + h) * 32 + WV, pv);
      }
      if (WV == 6 && s + 1 < NSEG - 1) stage_kv(s + 1);
      if (WV == 7 && s > 0 && s < NSEG - 1) rearm(sl == 0 ? 2 : sl - 1);
      if (WV == 7 && s >= 8 && s < 8 + NSEG - SDIR) {
        unsigned z = SENT; asm volatile("" : "+v"(z));
        const char* kd = ws + OFF_KVM + ((size_t)(s - 8 + SDIR) * NH + h) * 4096;
#pragma unroll
        for (int r = 0; r < 4; ++r) st16s(kd, r * 1024u + lane_id() * 16, u32x4{z, z, z, z});
      }
      if (s < NSEG - 1) {
        if (WV == 4) {
          delay64<E_B>();
          t0 = bell_time(xb + (size_t)(48 + xg) * 16384 + 4096, (lane_id() & 31) * 128, (unsigned)(s + 1), cap);
          *(volatile LAS u32x2*)(smem + Q_TM) = u32x2{(unsigned)(s + 1), t0};
        } else {
          for (;;) {
            const u32x2 v = *(volatile LAS u32x2*)(smem + Q_TM);
            t0 = __builtin_amdgcn_readfirstlane(v.y);
            if (__builtin_amdgcn_readfirstlane(v.x) == (unsigned)(s + 1)) break;
            __builtin_amdgcn_s_sleep(1);
          }
        }
      }
      sl = sl == 2 ? 0 : sl + 1;
    }
    if (cap == 0 && lane_id() == 0) st4_sc1(tmo, 1u);
  };
  switch (wave) {
    case 0: loop(std::integral_constant<int, 0>{}); break;
    case 1: loop(std::integral_constant<int, 1>{}); break;
    case 2: loop(std::integral_constant<int, 2>{}); break;
    case 3: loop(std::integral_constant<int, 3>{}); break;
    case 4: loop(std::integral_constant<int, 4>{}); break;
    case 5: loop(std::integral_constant<int, 5>{}); break;
    case 6: loop(std::integral_constant<int, 6>{}); break;
    default: loop(std::integral_constant<int, 7>{}); break;
  }
  __builtin_amdgcn_s_setprio(0);
}

__device__ __forceinline__ unsigned vr_body(char* smem, int wave_id_, int xg, int h, int c, int r, int wg, const u16* __restrict__ Wv, const u16* __restrict__ Wo,
                                        const u16* __restrict__ Wq, const u16* __restrict__ Wk, char* ws, unsigned* tmo, u64 tag, const char* code) {
  const int wave = WAVE_ID;
  const unsigned po = (unsigned)(r >> 5) * 16384u + ((r >> 2) & 7) * 1024 + (r & 1) * 512 + ((r >> 1) & 1) * 128;
  const unsigned vo_ = (unsigned)(24 + (h >> 2)) * 16384u + (h & 3) * 2048 + c * 1024;
  const int hg = (wave + ROTW * (r & 3)) & 7;
  const int jr = ROTH ? r & 3 : 0;
  VRW W;
  {
    const int lane = lane_id();
    const int g = lane >> 4, q4 = (lane & 15) >> 2, p4 = lane & 3, fr = 4 * g + (q4 & 1) + 32 * (q4 >> 1);
    const unsigned lw = wave * 16384;
    const char* sv_ = uni((const char*)(Wv + (size_t)(128 * wave) * HD + h * DH + 64 * c));
    const char* so = uni((const char*)(Wo + (size_t)(512 * hg) * DM + 64 * (r >> 2) + 16 * (r & 1)));
    const unsigned vo = (unsigned)((32 * (lane >> 5) + ((lane >> 2) & 7)) * DM * 2 + ((lane >> 1) & 1) * 64 + (lane & 1) * 16);
    auto issue = [&](int p) {
#pragma unroll
      for (int i = 0; i < 8; ++i) {
        if (p >= 2 && (NTM & 8)) gldsw(so + (size_t)(128 * ((p - 2) ^ jr) + 64 * (i & 1) + 8 * (i >> 1)) * DM * 2, vo, lw + (p & 1) * 8192 + i * 1024);
        else if (p >= 2) glds16s<0>(so + (size_t)(128 * ((p - 2) ^ jr) + 64 * (i & 1) + 8 * (i >> 1)) * DM * 2, vo, lw + (p & 1) * 8192 + i * 1024);
        else gldsw(sv_ + (size_t)(8 * p + i) * 8 * HD * 2, (unsigned)((lane >> 3) * HD * 2 + (lane & 7) * 16), lw + (p & 1) * 8192 + i * 1024);
      }
    };
#define VTS(p) do { } while (0)
    if (wave == 0) STS(ws, wg, 7);
    issue(0); issue(1);
    const unsigned av = lw + (unsigned)(fr * 128 + p4 * 8), ao = lw + (unsigned)(fr * 64 + p4 * 8);
    WAIT_V(8); VTS(0); vr_wload<0>(W.v0, av); issue(2);
    WAIT_V(8); VTS(1); vr_wload<1>(W.v1, av); issue(3);
    WAIT_V(8); VTS(2); vr_wload<2>(W.o0, ao); issue(4);
    WAIT_V(8); vr_wload<3>(W.o1, ao); issue(5);
    if (wave == 0) init_issue(ws, 131072);
    WAIT_V(8); vr_wload<4>(W.o2, ao);
    WAIT_V(0); VTS(3);
    if (VHN) {
      const int rl = r - 20 * xg, hn = ((xg == 1 ? rl : rl - (rl >= (xg ? 8 : 16) ? 4 : 0)) * 8 + wave) * (6 * VHN) + (lane >> 1);
      const int pm = hn / 384, hr = hn - pm * 384, p = 8 - VHN + (pm >> 1), hh = 12 * xg + 1 + 4 * (hr >> 7);
      const u16* hp = ((pm & 1) ? Wk : Wq) + (size_t)(((pm & 1) ? (p + 2) & 7 : p) * 128 + (hr & 127)) * HD + min(hh, 31) * DH + (lane & 1) * 64;
      if (((r >> 2) & 7) != 3 && lane < 12 * VHN && pm < 2 * VHN) asm volatile("s_mov_b32 m0, %0\n\ts_nop 0\n\tglobal_load_lds_dword %1, off" :: "s"(155648 + wave * 256), "v"(hp) : "memory", "m0");
    }
    vr_wload<5>(W.o3, ao);
#undef VTS
  }
  if (wave == 0) STS(ws, wg, 5);
  if (wave == 0) STS(ws, wg, 6);
  BAR();
  if (wave == 0) STS(ws, wg, 0);
  const unsigned in = init_get(ws, tag, smem + 131072);
  const char* xb = uni(xch_base(ws) + ((size_t)3 << 21) + (size_t)((in & 2u) << 12));
  const char* ob = uni(xch_base(ws) + (size_t)((in & 1u) << 13));
  auto rearm = [&](int ps, bool ag) {
    const int lane = lane_id();
    unsigned z = SENT; asm volatile("" : "+v"(z));
    const u32x4 sv = u32x4{z, z, z, z};
    if (ag && lane < 16 * AREP) st16s(xb, (unsigned)(ps * 8 + (lane >> 4) * 2) * 16384u + po + ((lane >> 3) & 1) * 256 + (lane & 7) * 16, sv);
    st16s(xb, (unsigned)ps * 131072u + vo_ + lane * 16, sv);
  };
  __builtin_amdgcn_s_setprio(2);
  unsigned cap = 60000;
  int sl = 0;
  for (int s = 0; s < NSEG; ++s) {
    TSDECL;
    const int sn = sl == 2 ? 0 : sl + 1;
    const char* ag = xb + (size_t)((sl * 4 + (AREP == 4 ? xg : (AREP == 2 ? ARSEL : 0))) * 2 + (wave >> 2)) * 16384 + (wave & 3) * 2048;
    const char* vd = (LAST && s == NSEG - 1 ? ws + OFF_KVM + ((size_t)s * NH + h) * 4096 + 2048 + c * 1024 : xb + (size_t)sl * 131072 + vo_) + (wave & 2) * 256 + (wave & 1) * 128;
    const char* os = ob + (size_t)((sl * 4 + (OREP == 4 ? xg : ORSEL)) * 8 + hg) * 16384 + ((r >> 1) & 1) * 1024;
    const char* ad = xb + (size_t)((sn * 4 + (wave & 3)) * 2) * 16384 + po;
    if (s > 0) delay64<E_AG>();
    TS(0);
    unsigned long long tc; unsigned sp;
    unsigned long long* tsp = nullptr;
    vr_v(W, ag, vd, wave * 2048, (wave & 3) * 512, cap, sp, tc, tsp);
    TN(12, sp + 1);
    if (s == NSEG - 1) break;
    if (wave == 7 && s > 0) rearm(sl == 0 ? 2 : sl - 1, true);
    if (wave == 6 && s == NSEG - 4 && code) touch(code, (unsigned)lane_id() * 128, 155648);
    wait_clk((unsigned)tc, E_O);
    TS(3);
    const unsigned tb = bell_time(xb + (size_t)(48 + xg) * 16384 + hg * 512, (lane_id() & 3) * 128, (unsigned)(s + 1), cap);
    wait_rt(tb, D_O);
    TS(4);
    vr_r(W, os, ad, wave * 512, jr * 2048, cap, sp, tsp);
    TN(14, (sp & 0xFFFF) + 1);
    if (wave == 1) TSFLUSH(0x6078);
    sl = sn;
  }
  __builtin_amdgcn_s_setprio(0);
  if (cap == 0 && lane_id() == 0) st4_sc1(tmo, 2u);
  return in;
}
struct ModIn { u32x4 w[8]; unsigned cv; };
__device__ __forceinline__ void mod_issue(ModIn& I, int wave_id_, int j, const u16* __restrict__ c, const u16* __restrict__ wmod) {
  const int tid = opaque_tid(), k0 = (j >> 5) * 512 + (tid >> 3);
  I.cv = c[k0 + 64 * (tid & 7)];
#pragma unroll
  for (int i = 0; i < 8; ++i) I.w[i] = ldnt<1>(wmod + (size_t)(k0 + 64 * i) * (6 * DM) + (j & 31) * 64 + (tid & 7) * 8);
}
__device__ __forceinline__ void mod_job(char* smem, int wave_id_, int j, u64 tag, const ModIn& I, char* ws) {
  const int tid = opaque_tid(), lane = tid & 63, wave = WAVE_ID, cg = tid & 7;
  const float cv = bflo(I.cv), sl = bfr(cv / (1.0f + __expf(-cv)));
  float acc[8] = {0, 0, 0, 0, 0, 0, 0, 0};
#pragma unroll
  for (int i = 0; i < 8; ++i) {
    const float sv = __shfl(sl, (lane & 56) + i);
#pragma unroll
    for (int e = 0; e < 4; ++e) { acc[2 * e] += sv * bflo(I.w[i][e]); acc[2 * e + 1] += sv * bfhi(I.w[i][e]); }
  }
#pragma unroll
  for (int e = 0; e < 8; ++e) acc[e] = red16_32_add(add_ror8(acc[e]));
  float* red = (float*)(smem + L_GB);
  if (lane < 8) { *(f32x4*)(red + wave * 64 + cg * 8) = f32x4{acc[0], acc[1], acc[2], acc[3]}; *(f32x4*)(red + wave * 64 + cg * 8 + 4) = f32x4{acc[4], acc[5], acc[6], acc[7]}; }
  __syncthreads();
  if (tid < 32) {
    f32x2 s = f32x2{0, 0};
#pragma unroll
    for (int r = 0; r < 8; ++r) s += *(const f32x2*)(red + r * 64 + tid * 2);
    st16_sc1(ws + OFF_MOD + (j * 32 + tid) * 16, u32x4{__float_as_uint(s[0]), (unsigned)tag, __float_as_uint(s[1]), tag2(tag)});
  }
  BAR();
}
#define NIMM 3
constexpr unsigned TK(int m, int hl, int part, int bo, int be) { return (unsigned)(m | hl << 2 | part << 5 | bo << 8 | be << 16); }
#define QX 30
#define CA 36
#define CB 42
#define C2 46
constexpr unsigned task_tab(int t, int it) {
  return t >= 8 && t < 13 ? (it == 0 ? TK(1, t - 8, 0, 0, 64) : it == 1 ? (t < 12 ? TK(2, t - 4, 1, C2, 64) : TK(1, 5, 1, C2, 64)) : 0)
       : it > 0 ? 0
       : t < 8 ? TK(0, t, 1, QX, 64)
       : t < 16 ? TK(1, t - 8, 0, 0, C2)
       : t < 24 ? TK(2, t - 16, 0, 0, t < 18 ? CA : (t < 20 ? CB : C2))
       : t < 26 ? TK(2, t - 24, 1, CA, 64)
       : t == 26 ? TK(1, 6, 1, C2, (C2 + 64) / 2) : t == 27 ? TK(1, 6, 2, (C2 + 64) / 2, 64)
       : t < 30 ? TK(2, t - 26, 1, CB, 64)
       : t == 30 ? TK(1, 7, 1, C2, (C2 + 64) / 2) : t == 31 ? TK(1, 7, 2, (C2 + 64) / 2, 64)
       : TK(0, t - 32, 0, 0, QX);
}
template <int IT, int I = 0> __device__ __forceinline__ unsigned task_sel(int t) {
  if constexpr (I == 40) return 0u;
  else { unsigned r = task_sel<IT, I + 1>(t); constexpr unsigned c = task_tab(I, IT); if (c) asm volatile("s_cmp_eq_u32 %1, %2\n\ts_cselect_b32 %0, %3, %0" : "+s"(r) : "s"(t), "i"(I), "i"(c) : "scc"); return r; }
}
__global__ void __launch_bounds__(512, 2) k_mega(const u16* __restrict__ Wq, const u16* __restrict__ Wk, const u16* __restrict__ Wv,
                                                 const u16* __restrict__ Wo, const u16* __restrict__ w1, const u16* __restrict__ w3,
                                                 const u16* __restrict__ w2, const u16* __restrict__ x, const u16* __restrict__ cvec,
                                                 const u16* __restrict__ wmod, const u16* __restrict__ mem0, char* ws, u16* out) {
  extern __shared__ __attribute__((aligned(16))) char smem[];
  int wave_id_ = __builtin_amdgcn_readfirstlane((int)threadIdx.x >> 6);
  unsigned bid = blockIdx.x;
  asm volatile("" : "+s"(wave_id_), "+s"(bid));
  unsigned* tmo = (unsigned*)(ws + OFF_TMO);
  const unsigned tst = (unsigned)__builtin_amdgcn_s_memrealtime();
  unsigned long long* mts = nullptr;
  const float* mod = (const float*)(ws + OFF_MOD);
  volatile LAS unsigned* tslot = (volatile LAS unsigned*)(smem + L_SLOT + 768);
#define CPF 131072
  u64 pc; asm volatile("s_getpc_b64 %0" : "=s"(pc));
  const u64 pc0 = pc & ~(u64)127;
  if (CPF && wave_id_ == 1) {
    const unsigned o = (((bid >> 3) & 31) * 64 + lane_id()) * 64;
    if (o < CPF) asm volatile("s_mov_b32 m0, %0\n\ts_nop 0\n\tglobal_load_lds_dword %1, %2" :: "s"(155648 + 1024), "v"(o), "s"(pc & ~(u64)127) : "memory", "m0");
  }
  unsigned xcc; asm volatile("s_getreg_b32 %0, hwreg(HW_REG_XCC_ID)" : "=s"(xcc));
  xcc &= 7;
  const unsigned pred = xcc * 32 + ((bid >> 3) & 31);
  ModIn mi;
  auto modj = [](unsigned sl) { return sl >= 128 && ((sl + 8) & 31) < 16 ? (int)(((sl >> 5) & 3) * 16 + (sl & 15)) : -1; };
  if (modj(pred) >= 0) mod_issue(mi, wave_id_, modj(pred), cvec, wmod);
  u64 tag;
  asm volatile("s_load_dwordx2 %0, %1, 0x0\n\ts_waitcnt lgkmcnt(0)" : "=s"(tag) : "s"(ws + OFF_TAG) : "memory");
  tag += 1;
  if (opaque_tid() == 0) {
    unsigned sl = pred;
    const unsigned s0 = sl;
    for (unsigned i = 1; i <= 256; ++i) {
      if (__hip_atomic_exchange((u64*)(ws + OFF_CLAIM + sl * 128), tag, __ATOMIC_RELAXED, __HIP_MEMORY_SCOPE_AGENT) != tag) break;
      sl = i == 256 ? 0xFFFFu : (((s0 + (i & ~31u)) & 0xE0u) | ((s0 + i) & 31u));
    }
    *tslot = sl;
  }
  BAR();
  const int slot = (int)__builtin_amdgcn_readfirstlane(*tslot);
  BAR();
  if (slot >= 256) return;
  if (modj(slot) >= 0) {
    if (slot != (int)pred) mod_issue(mi, wave_id_, modj(slot), cvec, wmod);
    mod_job(smem, wave_id_, modj(slot), tag, mi, ws);
  }
  unsigned* cnt = nullptr;
  const int xg = slot >> 5;
  int t = xg == 3 ? 24 + (slot & 7) : slot & 31;
  int hx = -1; bool hs = false;
  if (xg < 3) {
    const int nq = xg == 2 ? 8 : 12;
#define HPF 1
    if (t < nq) {
      const int h = xg * 12 + t;
      qk_body(smem, wave_id_, xg, h, (h >> 3) * 32 + (h & 7), Wq, Wk, ws + OFF_K, ws + OFF_V, ws, tmo, tag);
      if (HPF && HB < NSEG && t < 8) {
        const int lane = lane_id(), wv = t * 8 + wave_id_;
#pragma unroll
        for (int j = 0; j < 2; ++j) touch(w2 + (size_t)(xg * 1024 + 256 + j * 128 + (wv & 32) * 16 + (wv & 31) * 4) * DM, (unsigned)lane * 128, wave_id_ * 256);
        if (wv < (xg == 2 ? 32 : 16)) touch(w2 + (size_t)(3072 + 256 + (wv & 15) * 16) * DM + (wv >= 16 ? 3 : xg) * 256, (unsigned)((lane >> 2) * DM * 2 + (lane & 3) * 128), wave_id_ * 256);
        WAIT_V(0);
      }
    } else {
      const int u = t - nq, r = xg * 20 + u, uu = u % 10;
      cnt = cnt_of(ws, vr_body(smem, wave_id_, xg, r >> 1, r & 1, r, (r >> 4) * 32 + 8 + (r & 15), Wv, Wo, Wq, Wk, ws, tmo, tag, CPF && HB < NSEG && u < 16 ? (const char*)pc0 + u * 8192 : nullptr));
      if (HB < NSEG && u < (HB < HC ? 20 : 10)) hx = uu < 8 ? xg * 8 + uu : (uu == 8 ? 24 + xg : (xg == 2 ? 27 : -1));
      hs = u >= 10;
    }
    if (hx < 0) return;
    t = 8;
  }
  const bool cx = xg == 3;
  const int xq = cx ? (slot >> 3) & 3 : xg & 3, wsl = xq * 32 + t;
#define GDEL 1200
#define GDX 1200
  if (cx || t < 8) l1_role(smem, wave_id_, cx, xq * 8 + (t & 7), x, mod, mem0, ws + OFF_H1, cnt, tag, tmo, mts);
#define KH 0
  if (KH && !cx && t >= 24) {
    const int it = (t & 7) * 8 + wave_id_, lane = lane_id();
    touch(((it & 16) ? Wk : Wv) + (size_t)(256 + (it & 8) * 64 + (it & 7) * 32) * HD + (8 * xq + 1 + 4 * (it >> 5)) * DH,
          (unsigned)((lane >> 1) * HD * 2 + (lane & 1) * 128), 155648 + wave_id_ * 256);
  }
  if (t < 8 || t >= 24) {
    while ((unsigned)__builtin_amdgcn_s_memrealtime() - tst < (cx ? GDX : GDEL)) __builtin_amdgcn_s_sleep(8);
    if (!cx && t >= 24) {
      cnt = cnt_of(ws, init_wait(ws, tag));
      mod2_job(smem, wave_id_, xq * 8 + t - 24, mod, cvec, wmod, cnt);
    }
  }
  auto u2_of = [&]() { return cx ? -1 : (t >= 13 && t < 16 ? t - 13 : (t >= 20 && t < 24 ? t - 17 : -1)); };
  auto f_of = [&]() {
    int fl = t < 8 ? t : (t < 18 ? t - 4 : (t < 20 ? t - 10 : t - 14));
    const int sa = (3 + 2 * xq) & 7, sb = sa + 8 < 14 ? sa + 8 : 13;
    if (fl == 12) fl = sa; else if (fl == sa) fl = 12;
    else if (fl == 13) fl = sb; else if (fl == sb) fl = 13;
    return (int)__builtin_amdgcn_readfirstlane(14 * xq + fl);
  };
  unsigned tk = task_sel<0>(hx >= 0 ? 99 : (cx ? t + 8 : t)), t2 = task_sel<1>(cx ? t + 8 : t), t3 = task_sel<2>(cx ? t + 8 : t);
  for (int it = 0; tk != 0; ++it, tk = t2, t2 = t3, t3 = 0) {
    const int m = tk & 3, hl = (tk >> 2) & 7, part = (tk >> 5) & 3, bo = (tk >> 8) & 255, ns = (int)(tk >> 16) - bo, h = 8 * xq + hl;
    const bool first = m != 0 && bo == 0, late = it > 0;
    const int lane = lane_id();
    const Dep d{nullptr, (unsigned)((lane < 32 ? PL1 : PL1B - 32) + lane) * 128, 8u, (unsigned)((lane < 32 ? 0 : 8) - bo)};
    if (wave_id_ == 0 && late) wait_l1(cnt + (PL1B - PL1) * 32, 7u, tmo);
    BAR();
    gemm7<R_QKV>(smem, wave_id_, (m == 0 ? Wq : (m == 1 ? Wk : Wv)) + h * DH, nullptr, HD, ws + OFF_H1 + (size_t)bo * 65536, 65536, d,
                    ws + (m == 0 ? OFF_Q : (m == 1 ? OFF_K : OFF_V)) + h * 8192 + (size_t)bo * 32 * 8192, 32 * 8192,
                    cnt, (PQKV + h) * 32 + 4 * part + m, tmo, wsl, mts, false, first ? NIMM : 0, ns, late ? ns : 0, bo * 257, tag);
  }
  __syncthreads();
  const int u2 = u2_of();
  const int f2 = hx >= 0 ? hx : (u2 < 0 ? -1 : (xq < 3 ? xq * 8 + u2 : (u2 < 3 ? u2 * 8 + 7 : 21 + u2)));
  const int fb = hx >= 0 ? (hs ? HB : HC) : 0, fe = hx >= 0 ? (hs ? HC : NSEG) : HB;
  if (cx || (f2 >= 0 && f2 < 24)) {
    const int ks = cx ? xq : f2 >> 3, cn = cx ? (t + 1) & 7 : f2 & 7;
    const int lane = lane_id();
    const Dep d{nullptr, (unsigned)(cx ? (PSA + 4 * xq + ((lane >> 3) & 3)) * 128 + (lane & 7) * 4 : (PF1 + 16 * ks + (lane & 15)) * 128), cx ? 4u : 1u, cx ? (unsigned)((lane >> 1) & 3) : 0u};
    gemm7<R_PART>(smem, wave_id_, (cx ? Wo : w2) + (size_t)ks * 1024 * DM + cn * 128, nullptr, DM, ws + (cx ? OFF_O : OFF_G) + ks * 16 * 4096, (cx ? 64 : 56) * 4096, d,
                  ws + (cx ? OFF_P : OFF_P2) + (ks * DM + cn * 128) * (TLP ? 64 : 2), (size_t)32 * PROW, cnt, ((cx ? PWO : PF2) + ks * 8 + cn) * 32 + (hx >= 0 ? (hs ? 2 : 1) : 0), tmo,
                  hx >= 0 ? 280 + hx : 128 + xq * 32 + (cx ? cn : 22 + u2), mts, !cx, 0, cx ? NSEG : fe, 0, 0, 0, fb);
  } else if (f2 >= 0) {
    const int c4 = f2 - 24;
    const int lane = lane_id();
    const Dep d{nullptr, (unsigned)(PF1 + 48 + (lane & 7)) * 128, 1u, 0u};
    gemm7<R_PART2>(smem, wave_id_, w2 + (size_t)3072 * DM + c4 * 256, nullptr, DM, ws + OFF_G + 48 * 4096, 56 * 4096, d,
                   ws + OFF_P2 + (3 * DM + c4 * 256) * (TLP ? 64 : 2), (size_t)32 * PROW, cnt, (PF2 + 24 + c4) * 32 + (hx >= 0 ? (hs ? 2 : 1) : 0), tmo, hx >= 0 ? 280 + hx : 128 + xq * 32 + 22 + u2, mts, true, 0, fe, 0, 0, 0, fb);
  } else if (t < 8 || (t >= 16 && t < 20) || t == 24 || t == 25) {
    int f = f_of();
    asm volatile("" : "+s"(f));
    const int lane = lane_id();
    const Dep d{nullptr, (unsigned)((PLN + (lane >> 3)) * 128 + (lane & 7) * 4), 1u, 0u};
    gemm7<R_F1>(smem, wave_id_, w1 + 64 * f, w3 + 64 * f, FF, ws + OFF_H2, 65536, d, ws + OFF_G + f * 4096, 56 * 4096, cnt, (PF1 + f) * 32, tmo, 128 + xq * 18 + 8 + f, mts, false);
  } else if (t >= 9 && t < 13) {
    fin_role(smem, wave_id_, xq * 4 + t - 9, mod, ws + OFF_P2, (const u16*)(ws + OFF_X1), out, cnt, tmo, mts);
    if (xq == (FL63 ? 0 : 3) && t == 12 && opaque_tid() == 0) {
      const u32x4 v = ld16_sc1(ws + OFF_TAG);
      const u64 tn = (((u64)v.y << 32) | v.x) + 1;
      st8_sc1(ws + OFF_TAG, u32x2{(unsigned)tn, (unsigned)(tn >> 32)});
    }
  } else if (t == 28 || t == 29) {
    row_role(smem, wave_id_, 8 * xq, t - 28, mod, ws + OFF_P, x, (u16*)(ws + OFF_X1), ws + OFF_H2, cnt, tmo, 256 + xq, mts);
  } else if (t >= 26) {
    sa_role(smem, wave_id_, 8 * xq + 2 * (t >= 30 ? t - 30 : t - 24), ws + OFF_Q, ws + OFF_K, ws + OFF_V, ws + OFF_KVM, ws + OFF_O, cnt, tmo, 128 + xq * 32 + 30 + (t & 1), mts);
  }
}

extern "C" void kernel_launch(void* const* d_in, const int* in_sizes, int n_in, void* d_out, int out_size, void* d_ws, size_t ws_size,
                              hipStream_t stream) {
  const u16* x    = (const u16*)d_in[0];
  const u16* mem0 = (const u16*)d_in[1];
  const u16* Wq   = (const u16*)d_in[2];
  const u16* Wk   = (const u16*)d_in[3];
  const u16* Wv   = (const u16*)d_in[4];
  const u16* Wo   = (const u16*)d_in[5];
  const u16* w1   = (const u16*)d_in[8];
  const u16* w3   = (const u16*)d_in[9];
  const u16* w2   = (const u16*)d_in[10];
  const u16* c    = (const u16*)d_in[11];
  const u16* wmod = (const u16*)d_in[12];
#define WSHIFT 0
  char* ws = (char*)d_ws + ((size_t)WSHIFT << 20);
  hipFuncSetAttribute((const void*)k_mega, hipFuncAttributeMaxDynamicSharedMemorySize, MEGA_LDS);
  k_mega<<<256, 512, MEGA_LDS, stream>>>(Wq, Wk, Wv, Wo, w1, w3, w2, x, c, wmod, mem0, ws, (u16*)d_out);
}
```

```cpp
#include <hip/hip_runtime.h>
#include <hip/hip_bf16.h>
#include <stdint.h>
#include <type_traits>
#pragma clang diagnostic ignored "-Winline-asm"
#pragma clang diagnostic ignored "-Wunused-value"

typedef unsigned short u16;
typedef short s16x4 __attribute__((ext_vector_type(4)));
typedef __bf16 bf16x8 __attribute__((ext_vector_type(8)));
typedef float f32x4 __attribute__((ext_vector_type(4)));
typedef float f32x2 __attribute__((ext_vector_type(2)));
typedef unsigned u32x4 __attribute__((ext_vector_type(4)));
typedef unsigned u32x2 __attribute__((ext_vector_type(2)));
#define LAS __attribute__((address_space(3)))

constexpr int SEQ = 2048, DM = 1024, NH = 32, DH = 128, HD = 4096, NMEM = 8, SEG = 32, NSEG = 64, FF = 3584;
constexpr unsigned SENT = 0xFFFFFFFFu;

__device__ __forceinline__ int lane_id() { int l; asm volatile("v_mbcnt_lo_u32_b32 %0, -1, 0\n\tv_mbcnt_hi_u32_b32 %0, -1, %0" : "=v"(l)); return l; }
#define WAVE_ID wave_id_
#define opaque_tid() (WAVE_ID * 64 + lane_id())
__device__ __forceinline__ float bf2f(u16 v) { return __builtin_bit_cast(float, (unsigned)v << 16); }
__device__ __forceinline__ float bflo(unsigned v) { return __builtin_bit_cast(float, v << 16); }
__device__ __forceinline__ float bfhi(unsigned v) { return __builtin_bit_cast(float, v & 0xffff0000u); }
__device__ __forceinline__ float bfr(float x) { return (float)(__bf16)x; }
__device__ __forceinline__ unsigned pack2(float a, float b) {
  typedef __bf16 bf16x2 __attribute__((ext_vector_type(2)));
  bf16x2 v; v[0] = (__bf16)a; v[1] = (__bf16)b; return __builtin_bit_cast(unsigned, v);
}
__device__ __forceinline__ unsigned nosent(unsigned v) { return v == SENT ? 0x7FC07FC0u : v; }
__device__ __forceinline__ bf16x8 asbf8(u32x4 v) { return __builtin_bit_cast(bf16x8, v); }
__device__ __forceinline__ s16x4 ass4(u32x2 v) { return __builtin_bit_cast(s16x4, v); }
__device__ __forceinline__ u32x2 trread(const char* lds_base, unsigned off) {
  s16x4 v = __builtin_amdgcn_ds_read_tr16_b64_v4i16((LAS s16x4*)(lds_base + off));
  return __builtin_bit_cast(u32x2, v);
}
__device__ __forceinline__ void glds16(const void* gsrc, unsigned lds_dst) {
  unsigned keep;
  asm volatile("s_mov_b32 %0, m0\n\ts_mov_b32 m0, %2\n\ts_nop 0\n\tglobal_load_lds_dwordx4 %1, off\n\ts_mov_b32 m0, %0"
               : "=&s"(keep) : "v"(gsrc), "s"(lds_dst) : "memory");
}
#define CNT_ " nt"
#define WNT_ " nt"
__device__ __forceinline__ void glds16_w(const void* gsrc, unsigned lds_dst) {
  unsigned keep;
  asm volatile("s_mov_b32 %0, m0\n\ts_mov_b32 m0, %2\n\ts_nop 0\n\tglobal_load_lds_dwordx4 %1, off" CNT_ "\n\ts_mov_b32 m0, %0"
               : "=&s"(keep) : "v"(gsrc), "s"(lds_dst) : "memory");
}
__device__ __forceinline__ void glds16_sc1(const void* gsrc, unsigned lds_dst) {
  unsigned keep;
  asm volatile("s_mov_b32 %0, m0\n\ts_mov_b32 m0, %2\n\ts_nop 0\n\tglobal_load_lds_dwordx4 %1, off sc1\n\ts_mov_b32 m0, %0"
               : "=&s"(keep) : "v"(gsrc), "s"(lds_dst) : "memory");
}
#define NTM 1
template <int B> __device__ __forceinline__ u32x4 ldnt(const void* p) { if (NTM & B) return __builtin_nontemporal_load((const u32x4*)p); else return *(const u32x4*)p; }
#define MFMA32(a, b, c) __builtin_amdgcn_mfma_f32_16x16x32_bf16((a), (b), (c), 0, 0, 0)

__device__ __forceinline__ void st16_sc1(void* p, u32x4 v) { asm volatile("global_store_dwordx4 %0, %1, off sc1\n\ts_nop 1" :: "v"(p), "v"(v) : "memory"); }
__device__ __forceinline__ void st8_sc1(void* p, u32x2 v) { asm volatile("global_store_dwordx2 %0, %1, off sc1\n\ts_nop 1" :: "v"(p), "v"(v) : "memory"); }
template <bool WT> __device__ __forceinline__ void st8(void* p, u32x2 v) { if (WT) st8_sc1(p, v); else *(u32x2*)p = v; }
template <bool WT> __device__ __forceinline__ void st16(void* p, u32x4 v) { if (WT) st16_sc1(p, v); else *(u32x4*)p = v; }
__device__ __forceinline__ u32x4 ld16_sc1(const void* p) {
  u32x4 r; asm volatile("global_load_dwordx4 %0, %1, off sc1\n\ts_waitcnt vmcnt(0)" : "=&v"(r) : "v"(p) : "memory"); return r;
}
__device__ __forceinline__ void ld16x4_sc1(const void* p, u32x4& a, u32x4& b, u32x4& c, u32x4& d) {
  asm volatile("global_load_dwordx4 %0, %4, off sc1\n\tglobal_load_dwordx4 %1, %4, off offset:1024 sc1\n\t"
               "global_load_dwordx4 %2, %4, off offset:2048 sc1\n\tglobal_load_dwordx4 %3, %4, off offset:3072 sc1\n\ts_waitcnt vmcnt(0)"
               : "=&v"(a), "=&v"(b), "=&v"(c), "=&v"(d) : "v"(p) : "memory");
}
typedef __amdgpu_buffer_rsrc_t rsrc_t;
__device__ __forceinline__ rsrc_t mkrs(const void* p) { return __builtin_amdgcn_make_buffer_rsrc((void*)p, 0, 0x7FFFFFFF, 0x00020000); }
__device__ __forceinline__ u32x4 bld16(rsrc_t r, unsigned off) { return __builtin_amdgcn_raw_buffer_load_b128(r, off, 0, 16); }
__device__ __forceinline__ u32x2 bld8(rsrc_t r, unsigned off) { return __builtin_amdgcn_raw_buffer_load_b64(r, off, 0, 16); }
__device__ __forceinline__ bool ok4(u32x4 v) { return v.x != SENT && v.y != SENT && v.z != SENT && v.w != SENT; }
constexpr int SPIN_CAP = 2000000;
__device__ __forceinline__ bool give_up(int spins, unsigned* tmo, unsigned code) {
  if ((spins & 4095) != 4095) return false;
  if (spins > SPIN_CAP) { __hip_atomic_store(tmo, code, __ATOMIC_RELAXED, __HIP_MEMORY_SCOPE_AGENT); return true; }
  return __hip_atomic_load(tmo, __ATOMIC_RELAXED, __HIP_MEMORY_SCOPE_AGENT) != 0u;
}
template <bool ACQ = true>
__device__ __forceinline__ void wait_cnt(unsigned* c, unsigned n, unsigned* tmo) {
  for (int spins = 0;; ++spins) {
    const unsigned v = __hip_atomic_load(c, __ATOMIC_RELAXED, __HIP_MEMORY_SCOPE_AGENT);
    if (v >= n) break;
    __builtin_amdgcn_s_sleep(8);
    if (give_up(spins, tmo, 5)) break;
  }
  if (ACQ) {
    __builtin_amdgcn_fence(__ATOMIC_ACQUIRE, "agent");
    asm volatile("s_waitcnt vmcnt(0)" ::: "memory");
  }
}

__device__ __forceinline__ const char* uni(const char* p) {
  const unsigned long long v = (unsigned long long)p;
  const unsigned lo = __builtin_amdgcn_readfirstlane((unsigned)v), hi = __builtin_amdgcn_readfirstlane((unsigned)(v >> 32));
  return (const char*)(((unsigned long long)hi << 32) | lo);
}
#define TSCLK "s_memrealtime"
#define TC(k) do { } while (0)
#define TS(k) do { } while (0)
#define TSDECL do { } while (0)
#define TSFLUSH(mask) do { } while (0)

constexpr size_t OFF_MOD  = 0;
constexpr size_t OFF_MOD2 = 65536;
constexpr size_t OFF_TMO  = 98304;
constexpr size_t OFF_TAG  = 131072;
constexpr size_t OFF_CLAIM = 135168;
constexpr size_t OFF_INIT = 196608;
constexpr size_t OFF_H1   = 262144;
constexpr size_t OFF_Q    = OFF_H1 + (size_t)SEQ * DM * 2;
constexpr size_t OFF_K    = OFF_Q + (size_t)SEQ * HD * 2;
constexpr size_t OFF_V    = OFF_K + (size_t)SEQ * HD * 2;
constexpr size_t OFF_O    = OFF_V + (size_t)SEQ * HD * 2;
constexpr size_t OFF_X1   = OFF_O + (size_t)SEQ * HD * 2;
constexpr size_t OFF_H2   = OFF_X1 + (size_t)SEQ * DM * 2;
constexpr size_t OFF_G    = OFF_H2 + (size_t)SEQ * DM * 2;
constexpr size_t OFF_KVM  = (size_t)92 << 20;
constexpr size_t OFF_XCH  = (size_t)100 << 20;
constexpr size_t OFF_P    = (size_t)112 << 20;
constexpr size_t OFF_P2   = (size_t)128 << 20;
constexpr size_t OFF_DUMP = (size_t)144 << 20;
constexpr int PROW = 4 * DM * 2;
#define TLQ 1
#define TLP 1
static_assert(OFF_G + (size_t)SEQ * FF * 2 <= OFF_KVM, "layout");
__device__ __forceinline__ char* xch_base(char* ws) { return (char*)(((size_t)(ws + OFF_XCH) + ((2u << 20) - 1)) & ~(size_t)((2u << 20) - 1)); }
#define XPAR 0
constexpr int PROG = 16;
constexpr int PQKV = 64;
constexpr int PL1 = 96;
constexpr int PL1B = 350;
constexpr int PSA = 128;
constexpr int PWO = 192;
constexpr int PLN = 224;
constexpr int PF2 = 320;
constexpr int PF1 = 448;
#define SDIR 40
#define SB 52
#define SC 60
#define MARG 20
#define MARL 15
#define MARF 8
#define LAST 52
#define HB 61
#define HC 62
#define MARX 10
#define MARH 35
#define SAW 0
constexpr int L_RED = 131072;
constexpr int L_SLOT = L_RED + 16384;
constexpr int L_GB = L_SLOT + 1024;
constexpr int MEGA_LDS = 163840;
static_assert(L_GB + 12288 + 2048 <= MEGA_LDS, "lds");
#define XPARC 0

__device__ __forceinline__ int bswz(int row) { return (((row >> 1) & 1) | (((row >> 3) & 1) << 1)) << 1; }
template <int OFF, bool SC1 = false>
__device__ __forceinline__ void glds16s(const char* sbase, unsigned voff, unsigned lds_base) {
  unsigned keep;
  if (SC1)
    asm volatile("s_mov_b32 %0, m0\n\ts_add_u32 m0, %3, %4\n\ts_nop 2\n\tglobal_load_lds_dwordx4 %1, %2 sc1\n\ts_mov_b32 m0, %0"
                 : "=&s"(keep) : "v"(voff), "s"(sbase), "s"(lds_base), "i"(OFF) : "memory", "scc");
  else
    asm volatile("s_mov_b32 %0, m0\n\ts_add_u32 m0, %3, %4\n\ts_nop 2\n\tglobal_load_lds_dwordx4 %1, %2\n\ts_mov_b32 m0, %0"
                 : "=&s"(keep) : "v"(voff), "s"(sbase), "s"(lds_base), "i"(OFF) : "memory", "scc");
}
template <int OFF>
__device__ __forceinline__ void glds16x2s(const char* sbase, unsigned voff, unsigned lds_base) {
  unsigned keep;
  asm volatile("s_mov_b32 %0, m0\n\ts_add_u32 m0, %3, %4\n\ts_nop 2\n\tglobal_load_lds_dwordx4 %1, %2 sc1\n\tglobal_load_lds_dwordx4 %1, %2 offset:1024 sc1\n\ts_mov_b32 m0, %0"
               : "=&s"(keep) : "v"(voff), "s"(sbase), "s"(lds_base), "i"(OFF) : "memory", "scc");
}
__device__ __forceinline__ void st16s(const char* sb, unsigned vo, u32x4 v) { asm volatile("global_store_dwordx4 %0, %1, %2 sc1\n\ts_nop 1" :: "v"(vo), "v"(v), "s"(sb) : "memory"); }
__device__ __forceinline__ void head_to_lds(const char* src, int lane, unsigned lds) {
  if (TLQ) {
    const int c = (lane & 15) ^ (lane >> 4);
    const unsigned v0 = (unsigned)((c >> 2) * 2048 + (lane >> 4) * 64 + (c & 3) * 16);
#pragma unroll
    for (int i = 0; i < 8; ++i) glds16s<0, true>(src + i * 256, v0 ^ ((i & 3) * 2048), lds + i * 1024);
  } else {
#pragma unroll
    for (int i = 0; i < 4; ++i) glds16x2s<0>(src + i * 2048, (unsigned)lane * 16, lds + i * 2048);
  }
}
#define TCHP ""
template <bool NT = false> __device__ __forceinline__ void touch(const void* sb, unsigned vo, unsigned lds) {
  if (NT) asm volatile("s_mov_b32 m0, %0\n\ts_nop 0\n\tglobal_load_lds_dword %1, %2 nt" :: "s"(lds), "v"(vo), "s"(sb) : "memory", "m0");
  else
  asm volatile("s_mov_b32 m0, %0\n\ts_nop 0\n\tglobal_load_lds_dword %1, %2" TCHP :: "s"(lds), "v"(vo), "s"(sb) : "memory", "m0");
}
#define WAIT_V(n) asm volatile("s_waitcnt vmcnt(" #n ")" ::: "memory")
#define BAR() do { asm volatile("s_waitcnt lgkmcnt(0)" ::: "memory"); __builtin_amdgcn_s_barrier(); asm volatile("" ::: "memory"); } while (0)
__device__ __forceinline__ void st4_sc1(void* p, unsigned v) { asm volatile("global_store_dword %0, %1, off sc1" :: "v"(p), "v"(v) : "memory"); }
__device__ __forceinline__ unsigned ld4_sc1(const void* p) {
  unsigned r; asm volatile("global_load_dword %0, %1, off sc1\n\ts_waitcnt vmcnt(0)" : "=&v"(r) : "v"(p) : "memory"); return r;
}
template <int CTRL> __device__ __forceinline__ unsigned dppu(unsigned x) { return (unsigned)__builtin_amdgcn_update_dpp(0, (int)x, CTRL, 0xf, 0xf, false); }
__device__ __forceinline__ unsigned rowmin16(unsigned v) {
  v = min(v, dppu<0x128>(v)); v = min(v, dppu<0x124>(v)); v = min(v, dppu<0x122>(v)); v = min(v, dppu<0x121>(v));
  return v;
}
__device__ __forceinline__ unsigned rdl(unsigned v, int l) { return (unsigned)__builtin_amdgcn_readlane((int)v, l); }
__device__ __forceinline__ unsigned qkv_avail(unsigned v, int m) {
  unsigned a = rdl(v, m);
#pragma unroll
  for (int j = 1; j < 4; ++j) { const unsigned p = rdl(v, 4 * j + m); if ((p >> 8) <= a) a = max(a, p & 255u); }
  return a;
}
constexpr float SCALE = 0.08838834764831845f;
constexpr float LOG2E = 1.4426950408889634f;
template <int N> __device__ __forceinline__ void delay64() {
  if (N >= 16) { __builtin_amdgcn_s_sleep(15); delay64<(N >= 16 ? N - 15 : 0)>(); } else if (N > 0) __builtin_amdgcn_s_sleep(N);
}
__device__ __forceinline__ void sw16(float x, float& a, float& b) {
  a = x; b = x; asm volatile("s_nop 1\n\tv_permlane16_swap_b32 %0, %1" : "+v"(a), "+v"(b));
}
__device__ __forceinline__ void sw32(float x, float& a, float& b) {
  a = x; b = x; asm volatile("s_nop 1\n\tv_permlane32_swap_b32 %0, %1" : "+v"(a), "+v"(b));
}
__device__ __forceinline__ float red16_32_max(float x) {
  float a, b; sw16(x, a, b); x = fmaxf(a, b); sw32(x, a, b); return fmaxf(a, b);
}
__device__ __forceinline__ float red16_32_add(float x) {
  float a, b; sw16(x, a, b); x = a + b; sw32(x, a, b); return a + b;
}
__device__ __forceinline__ unsigned cvtpk(float a, float b) { unsigned r; asm("v_cvt_pk_bf16_f32 %0, %1, %2" : "=v"(r) : "v"(a), "v"(b)); return r; }
__device__ __forceinline__ float max3(float a, float b, float c) { float r; asm("v_max3_f32 %0, %1, %2, %3" : "=v"(r) : "v"(a), "v"(b), "v"(c)); return r; }
__device__ __forceinline__ float max2(float a, float b) { float r; asm("v_max_f32 %0, %1, %2" : "=v"(r) : "v"(a), "v"(b)); return r; }
__device__ __forceinline__ unsigned ror8u(unsigned x) { return (unsigned)__builtin_amdgcn_update_dpp(0, (int)x, 0x128, 0xf, 0xf, false); }
__device__ __forceinline__ float ror8(float x) { return __builtin_bit_cast(float, ror8u(__builtin_bit_cast(unsigned, x))); }
__device__ __forceinline__ float add_ror8(float x) {
  return x + __builtin_bit_cast(float, __builtin_amdgcn_update_dpp(0, __builtin_bit_cast(int, x), 0x128, 0xf, 0xf, false));
}

__device__ __forceinline__ unsigned rt_now() { return (unsigned)__builtin_amdgcn_s_memrealtime(); }
__device__ __forceinline__ unsigned latest(unsigned v) {
  const unsigned now = rt_now() + 400u, age = rowmin16((now - v) & 0xFFFFFFu);
  return now - min(min(min(rdl(age, 0), rdl(age, 16)), min(rdl(age, 32), rdl(age, 48))), 900u);
}
__device__ __forceinline__ void wait_rt(unsigned t0, int d) {
  while ((int)(rt_now() - t0) < d) { }
}
__device__ __forceinline__ bool bells(bool ok, unsigned u, int b, int marg, int sb = SB) {
  if (b < sb || !__all(ok || (int)(u >> 24) > b)) return false;
  wait_rt(latest(!ok && (int)(u >> 24) == b + 1 ? u : rt_now() - 400u), marg);
  return true;
}
__device__ __forceinline__ void ld_fb(unsigned& v, unsigned& u, const void* sb, unsigned vo) {
  asm volatile("global_load_dword %0, %2, %3 sc1\n\tglobal_load_dword %1, %2, %3 offset:64 sc1\n\ts_waitcnt vmcnt(0)" : "=&v"(v), "=&v"(u) : "v"(vo), "s"(sb) : "memory");
}
__device__ __forceinline__ unsigned bell(int b, unsigned t) { return (unsigned)(b + 1) << 24 | (t & 0xFFFFFFu); }
typedef unsigned long long u64;
__device__ __forceinline__ unsigned tag2(u64 tag) { return (unsigned)(tag >> 32) ^ ((unsigned)tag * 0x9E3779B1u); }
__device__ __forceinline__ unsigned init_eval(u32x4 v, u64 tag) {
  if (!__all((v.x & ~63u) == (unsigned)tag << 6 && v.y == (unsigned)(tag >> 26))) return 0u;
  unsigned r = 8u;
#pragma unroll
  for (int c = 0; c < 3; ++c) r |= __popcll(__ballot(v.x & (1u << c))) > __popcll(__ballot(v.x & (8u << c))) ? 1u << c : 0u;
  return r ^ (XPAR * 3u + XPARC * 4u);
}
__device__ __forceinline__ unsigned init_wait(const char* ws, u64 tag) {
  for (int spins = 0; spins < SPIN_CAP; ++spins) {
    const unsigned r = init_eval(ld16_sc1(ws + OFF_INIT + lane_id() * 128), tag);
    if (r) return r;
    __builtin_amdgcn_s_sleep(4);
  }
  return 8u;
}
__device__ __forceinline__ void init_issue(const char* ws, unsigned lds) { glds16s<0, true>(uni(ws + OFF_INIT), (unsigned)lane_id() * 128u, lds); }
__device__ __forceinline__ unsigned init_get(const char* ws, u64 tag, const char* copy) {
  const unsigned r = init_eval(*(volatile LAS u32x4*)(copy + lane_id() * 16), tag);
  return r ? r : init_wait(ws, tag);
}
__device__ __forceinline__ unsigned* cnt_of(char* ws, unsigned in) { return (unsigned*)uni(xch_base(ws) + ((size_t)4 << 21) + (size_t)((in & 4u) << 11)); }
__device__ __forceinline__ void wait_l1(const unsigned* cnt, unsigned n, unsigned* tmo) {
  const int lane = lane_id();
  for (int spins = 0;; ++spins) {
    const unsigned v = ld4_sc1(cnt + (PL1 + (lane & 31)) * 32);
    if (__all(v >= n)) break;
    __builtin_amdgcn_s_sleep(8);
    if (give_up(spins, tmo, 12)) break;
  }
}
#define GKV 1
__device__ __forceinline__ void wait_k0(const unsigned* cnt, int h, unsigned* tmo) {
  for (int spins = 0;; ++spins) {
    const int l = lane_id();
    const unsigned v = ld4_sc1(cnt + (PQKV + h) * 32 + 1 + (l & 1));
    if (__all(v >= (unsigned)GKV)) break;
    __builtin_amdgcn_s_sleep(32);
    if (give_up(spins, tmo, 13)) break;
  }
}
#define WCLK(slot, b) do { } while (0)
#define WTM(slot, b, k) do { } while (0)
#define WTS(slot, b, k) do { } while (0)
#define WFB() do { } while (0)
#define STS(ws_, slot_, k_) do { } while (0)
typedef __bf16 bf16x2 __attribute__((ext_vector_type(2)));
__device__ __forceinline__ f32x2 pkfma(f32x2 a, f32x2 b, f32x2 c) { return __builtin_elementwise_fma(a, b, c); }
__device__ __forceinline__ float wavesum(float v) {
  v += __builtin_bit_cast(float, dppu<0x128>(__builtin_bit_cast(unsigned, v))); v += __builtin_bit_cast(float, dppu<0x124>(__builtin_bit_cast(unsigned, v)));
  v += __builtin_bit_cast(float, dppu<0x122>(__builtin_bit_cast(unsigned, v))); v += __builtin_bit_cast(float, dppu<0x121>(__builtin_bit_cast(unsigned, v)));
  return red16_32_add(v);
}
__device__ __forceinline__ float adds(unsigned v, unsigned sel, float c) { return __builtin_amdgcn_fdot2_f32_bf16(__builtin_bit_cast(bf16x2, v), __builtin_bit_cast(bf16x2, sel), c, false); }
__device__ __forceinline__ void sum4(const u32x4 (&p)[4], f32x2 (&a)[4]) {
  unsigned slo = 0x00003F80u, shi = 0x3F800000u;
  asm("" : "+s"(slo), "+s"(shi));
#pragma unroll
  for (int e = 0; e < 4; ++e) {
    float lo = bflo(p[0][e]), hi = bfhi(p[0][e]);
#pragma unroll
    for (int q = 1; q < 4; ++q) { lo = adds(p[q][e], slo, lo); hi = adds(p[q][e], shi, hi); }
    a[e] = f32x2{lo, hi};
  }
}
__device__ __forceinline__ u32x4 norm8(const f32x2 (&v)[4], const char* mt, float mb, float rden) {
  const f32x4 h0 = *(const f32x4*)(mt + 4096), h1 = *(const f32x4*)(mt + 4096 + 1024), c0 = *(const f32x4*)(mt + 8192), c1 = *(const f32x4*)(mt + 8192 + 1024);
  const f32x2 m2 = f32x2{mb, mb}, d2 = f32x2{rden, rden};
  const f32x2 r0 = pkfma(v[0] - m2, f32x2{c0[0], c0[1]} * d2, f32x2{h0[0], h0[1]}), r1 = pkfma(v[1] - m2, f32x2{c0[2], c0[3]} * d2, f32x2{h0[2], h0[3]});
  const f32x2 r2 = pkfma(v[2] - m2, f32x2{c1[0], c1[1]} * d2, f32x2{h1[0], h1[1]}), r3 = pkfma(v[3] - m2, f32x2{c1[2], c1[3]} * d2, f32x2{h1[2], h1[3]});
  return u32x4{pack2(r0[0], r0[1]), pack2(r1[0], r1[1]), pack2(r2[0], r2[1]), pack2(r3[0], r3[1])};
}
__device__ __forceinline__ void half_load(const char* P, const u16* res, int row, int hf, int lane, f32x2 (&a)[4], u32x4& rv) {
  const rsrc_t rp = mkrs(P), rr = mkrs(res);
  const unsigned po = TLP ? (unsigned)((row >> 5) * 32 * PROW + (hf * 16 + (lane >> 2)) * 2048 + (row & 31) * 64 + (lane & 3) * 16) : (unsigned)(row * PROW + hf * 1024 + lane * 16), ro = (unsigned)(row * DM * 2 + hf * 1024 + lane * 16);
  u32x4 p[4];
#pragma unroll
  for (int q = 0; q < 4; ++q) p[q] = bld16(rp, po + q * (TLP ? 65536 : 2048));
  if (res) rv = bld16(rr, ro);
  sum4(p, a);
}
__device__ __forceinline__ bool fin_half(bool fin, const char* P, const u16* res, const char* gbt, u16* __restrict__ o1, int row, int hf, int lane) {
  f32x2 a[4]; u32x4 rv;
  half_load(P, res, row, hf, lane, a, rv);
  const u32x4 gv = *(const u32x4*)(gbt + hf * 1024 + lane * 16);
  u32x4 o; f32x2 t = f32x2{0, 0};
#pragma unroll
  for (int e = 0; e < 4; ++e) { const f32x2 s = pkfma(f32x2{bflo(gv[e]), bfhi(gv[e])}, a[e], f32x2{bflo(rv[e]), bfhi(rv[e])}); o[e] = pack2(s[0], s[1]); t += s; }
  if (!fin && !__all(t[0] == t[0] && t[1] == t[1])) return false;
  st16_sc1(o1 + (size_t)row * DM + hf * 512 + lane * 8, o);
  return true;
}
#define LSL 2
#define FSL 2
#define GSL 2
#define XSL 2
#define DLN 10
#define LEB 64
#define MARE 25
#define LNE 1
template <int CTRL> __device__ __forceinline__ float dppf(float x) { return __builtin_bit_cast(float, dppu<CTRL>(__builtin_bit_cast(unsigned, x))); }
__device__ __forceinline__ bool ln_row(bool fin, int bb, unsigned* bl, const f32x2 (&a)[2][4], const f32x2 (&xf)[8], const f32x2 (&tg)[8], const f32x2 (&th)[8], const f32x2 (&tc)[8],
                                       const char* x1, const char* h2, unsigned xo, unsigned ho) {
  f32x2 v[8]; u32x4 o[2];
#pragma unroll
  for (int e = 0; e < 8; ++e) { const f32x2 t = pkfma(tg[e], a[e >> 2][e & 3], xf[e]); const unsigned u = pack2(t[0], t[1]); o[e >> 2][e & 3] = u; v[e] = f32x2{bflo(u), bfhi(u)}; }
  const float pv = __builtin_bit_cast(float, __builtin_amdgcn_readfirstlane(__float_as_int(v[0][0])));
  f32x2 d[8];
#pragma unroll
  for (int e = 0; e < 8; ++e) d[e] = v[e] - f32x2{pv, pv};
  const f32x2 s2 = ((d[0] + d[1]) + (d[2] + d[3])) + ((d[4] + d[5]) + (d[6] + d[7]));
  const f32x2 q2 = pkfma(d[3], d[3], pkfma(d[2], d[2], pkfma(d[1], d[1], d[0] * d[0]))) + pkfma(d[7], d[7], pkfma(d[6], d[6], pkfma(d[5], d[5], d[4] * d[4])));
  float s = s2[0] + s2[1], q = q2[0] + q2[1];
  if (!fin && !__all(s == s)) return false;
  if (LNE && bb >= SB && bl && lane_id() == 0) st4_sc1(bl, bell(bb, rt_now() + DLN + 30));
  s += dppf<0x128>(s); q += dppf<0x128>(q); s += dppf<0x124>(s); q += dppf<0x124>(q);
  s += dppf<0x122>(s); q += dppf<0x122>(q); s += dppf<0x121>(s); q += dppf<0x121>(q);
  asm volatile("s_nop 1\n\tv_permlane16_swap_b32 %0, %1" : "+v"(s), "+v"(q));
  s += q;
  { float x, y; sw32(s, x, y); s = x + y; }
  const float sh = __builtin_bit_cast(float, rdl(__builtin_bit_cast(unsigned, s), 0)) * (1.0f / DM), sq = __builtin_bit_cast(float, rdl(__builtin_bit_cast(unsigned, s), 16));
  const float mb = bfr(pv + sh), vb = bfr(fmaxf(sq * (1.0f / DM) - sh * sh, 0.0f));
  const float rden = __builtin_amdgcn_rcpf(bfr(__builtin_amdgcn_sqrtf(bfr(vb + 1e-5f))));
#pragma unroll
  for (int i = 0; i < 2; ++i) {
    u32x4 r;
#pragma unroll
    for (int e = 0; e < 4; ++e) { const f32x2 t = pkfma(v[4 * i + e] - f32x2{mb, mb}, tc[4 * i + e] * f32x2{rden, rden}, th[4 * i + e]); r[e] = pack2(t[0], t[1]); }
    st16s(h2, ho + i * 32768, r);
  }
  if (!LNE && bb >= SB && lane_id() == 0) st4_sc1(bl, bell(bb, rt_now() + DLN));
  st16s(x1, xo, o[0]); st16s(x1, xo + 1024, o[1]);
  return true;
}
__device__ __forceinline__ void row_table(char* tab, const float* mod, const unsigned* cnt, unsigned* tmo, int tid) {
  wait_l1(cnt + 1, 1u, tmo);
  const rsrc_t rm = mkrs((const char*)mod + OFF_MOD2);
#pragma unroll
  for (int i = 0; i < 2; ++i) {
    const int u = tid + 512 * i, a = u >> 8, c = u & 255;
    f32x4 v = __builtin_bit_cast(f32x4, bld16(rm, (unsigned)u * 16));
    v = f32x4{bfr(v[0]), bfr(v[1]), bfr(v[2]), bfr(v[3])};
    if (a == 2) v = f32x4{bfr(1.0f + v[0]), bfr(1.0f + v[1]), bfr(1.0f + v[2]), bfr(1.0f + v[3])};
    if (a < 3) *(f32x4*)(tab + a * 4096 + ((c >> 7) * 2 + (c & 1)) * 1024 + ((c >> 1) & 63) * 16) = v;
    else *(u32x2*)(tab + 12288 + c * 8) = u32x2{pack2(v[0], v[1]), pack2(v[2], v[3])};
  }
  __syncthreads();
}
__device__ __forceinline__ void row_role(char* smem, int wave_id_, int rbase, int par, const float* mod, const char* P, const u16* x, u16* x1, char* h2c,
                                         unsigned* cnt, unsigned* tmo, int slot, unsigned long long* mts) {
  const int tid = opaque_tid(), lane = tid & 63, wave = WAVE_ID;
  if (tid < 80) *(volatile LAS unsigned*)(smem + 16384 + tid * 4) = 0u;
  {
    static_assert(TLP && SB % 2 == 0, "");
    unsigned z = SENT; asm volatile("" : "+v"(z));
    const int r4 = rbase + 4 * par + (wave & 3);
    for (int bb = SB + (wave >> 2); bb < NSEG; bb += 2)
#pragma unroll
      for (int i = 0; i < 2; ++i) {
        st16s(uni((const char*)x1 + (size_t)bb * 65536), (unsigned)(r4 * DM * 2 + i * 1024 + lane * 16), u32x4{z, z, z, z});
        st16s(uni(h2c + (size_t)bb * 65536), (unsigned)(r4 * 128 + (lane & 7) * 16 + (lane >> 3) * 4096 + i * 32768), u32x4{z, z, z, z});
      }
    WAIT_V(0);
  }
  row_table(smem, mod, cnt, tmo, tid);
  const int rb = rbase + wave + 32 * par;
  const rsrc_t rp = mkrs(P), rr = mkrs(x);
  const unsigned l16 = lane * 16;
  const unsigned pwo = (unsigned)(PWO + (lane & 31)) * 128;
  unsigned* myp = cnt + (PLN + (rb >> 3)) * 32 + wave;
  f32x2 a[2][4];
  const int gp = wave >> 2, r4 = rbase + 4 * par + (wave & 3);
  volatile LAS unsigned* lwb = (volatile LAS unsigned*)(smem + 16384 + gp * 8);
  if (gp && lane == 0) st4_sc1(myp, 1u);
  f32x2 tg[8], th[8], tc[8];
#pragma unroll
  for (int i = 0; i < 4; ++i) {
    const char* mt = smem + lane * 16 + i * 1024;
    const f32x4 g_ = *(const f32x4*)mt, h_ = *(const f32x4*)(mt + 4096), c_ = *(const f32x4*)(mt + 8192);
    tg[2 * i] = f32x2{g_[0], g_[1]}; tg[2 * i + 1] = f32x2{g_[2], g_[3]}; th[2 * i] = f32x2{h_[0], h_[1]}; th[2 * i + 1] = f32x2{h_[2], h_[3]};
    tc[2 * i] = f32x2{c_[0], c_[1]}; tc[2 * i + 1] = f32x2{c_[2], c_[3]};
  }
  const unsigned xo = (unsigned)(r4 * DM * 2 + lane * 16), ho = (unsigned)(r4 * 128 + (((lane & 7) ^ (r4 & 7)) << 4) + (lane >> 3) * 4096);
  const unsigned po = (l16 >> 6) * 2048 + (l16 & 48);
  u32x4 rv[2];
#pragma unroll
  for (int i = 0; i < 2; ++i) rv[i] = __builtin_amdgcn_raw_buffer_load_b128(rr, xo + i * 1024, (unsigned)gp * 65536u, 0);
  unsigned wb = 0, d0 = 150, d1 = 150;
  for (int bb = gp; bb < NSEG; bb += 2) {
    f32x2 xf[8];
#pragma unroll
    for (int e = 0; e < 8; ++e) xf[e] = f32x2{bflo(rv[e >> 2][e & 3]), bfhi(rv[e >> 2][e & 3])};
    asm volatile("" : "+v"(xf[0]), "+v"(xf[1]), "+v"(xf[2]), "+v"(xf[3]), "+v"(xf[4]), "+v"(xf[5]), "+v"(xf[6]), "+v"(xf[7]));
    if (wb <= (unsigned)bb) {
      if ((wave & 3) == 0) {
        wb = (unsigned)(bb + 1);
        for (int spins = 0;; ++spins) {
          unsigned v, u;
          ld_fb(v, u, cnt, pwo);
          if (__all(v > (unsigned)bb + 2u)) { v = rowmin16(v); wb = min(rdl(v, 0), rdl(v, 16)); break; }
          if (__all(v > (unsigned)bb)) break;
          if (bb >= LEB && __all(v > (unsigned)bb || (u >> 24) > (unsigned)bb)) {
            const unsigned now = rt_now(), g0 = latest(v <= (unsigned)bb && (u >> 24) == (unsigned)bb + 1u ? u : now - 400u) + MARE, go = (int)(g0 - now) > 0 ? g0 : now;
            if (lane < 4) st4_sc1(myp + 16 + lane, bell(bb, go + max(d0, d1)));
            wait_rt(go, 0);
            wb |= 256u;
            break;
          }
          if (bells(v > (unsigned)bb, u, bb, MARL)) break;
          __builtin_amdgcn_s_sleep(LSL);
          if (give_up(spins, tmo, 11)) break;
        }
        lwb[0] = wb;
      } else {
        while (((wb = __builtin_amdgcn_readfirstlane(lwb[0])) & 255u) <= (unsigned)bb) __builtin_amdgcn_s_sleep(1);
      }
    }
    const bool rung = wb == (unsigned)bb + 257u;
    wb &= 255u;
    const unsigned t0 = rt_now();
    WTM(slot, bb, 0);
    bool fin = bb < SB;
    for (int spins = 0;; ++spins) {
      u32x4 pp[2][4];
#pragma unroll
      for (int i = 0; i < 2; ++i)
#pragma unroll
        for (int q = 0; q < 4; ++q) pp[i][q] = __builtin_amdgcn_raw_buffer_load_b128(rp, po + i * 32768, (unsigned)(bb * 32 * PROW + q * 65536 + r4 * 64), 16);
      sum4(pp[0], a[0]); sum4(pp[1], a[1]);
      if (ln_row(fin, bb, rung && spins == 0 ? nullptr : myp + 16, a, xf, tg, th, tc, uni((const char*)x1 + (size_t)bb * 65536), uni(h2c + (size_t)bb * 65536), xo, ho)) break;
      if (spins > 1) fin = __all(ld4_sc1((const char*)cnt + pwo) > (unsigned)bb) || give_up(spins, tmo, 11);
    }
    d1 = d0; d0 = min(rt_now() - t0 + 12u, 200u);
    WTM(slot, bb, 3);
#pragma unroll
    for (int i = 0; i < 2; ++i) rv[i] = __builtin_amdgcn_raw_buffer_load_b128(rr, xo + i * 1024, (unsigned)min(bb + 2, NSEG - 1) * 65536u, 0);
    WAIT_V(2);
    unsigned o = 0;
    if (lane == 0) o = __hip_atomic_fetch_add((LAS unsigned*)(smem + 16448 + bb * 4), 1u, __ATOMIC_RELAXED, __HIP_MEMORY_SCOPE_WORKGROUP);
    if (__builtin_amdgcn_readfirstlane(o) == 3u) {
      if (lane < 4) st4_sc1(myp - (wave & 3) + lane, (unsigned)(bb + 2));
      WTM(slot, bb, 2);
    }
  }
}
#define NCS 8
__device__ __forceinline__ void l1_role(char* smem, int wave_id_, bool fst, int c, const u16* __restrict__ x, const float* __restrict__ mod,
                                        const u16* __restrict__ mem0, char* __restrict__ h1c, unsigned*& cnt, u64 tag, unsigned* tmo, unsigned long long* mts) {
  const int tid = opaque_tid(), lane = tid & 63, wave = WAVE_ID;
  char* ws = (char*)mod - OFF_MOD;
  u32x4 xr[7][2];
  const int row0 = (fst ? 0 : 256) + 8 * c + wave;
  const u16* src = x + (size_t)row0 * DM + lane * 8;
  xr[0][0] = ldnt<2>(src); xr[0][1] = ldnt<2>(src + 512);
  volatile LAS unsigned* sg = (volatile LAS unsigned*)(smem + L_SLOT + 784);
  if (!fst) {
#pragma unroll
    for (int pp = 1; pp < 7; ++pp) { xr[pp][0] = ldnt<2>(src + (size_t)pp * 256 * DM); xr[pp][1] = ldnt<2>(src + (size_t)pp * 256 * DM + 512); }
  } else if ((wave & 3) != 3) {
    char* cal = xch_base(ws) + ((size_t)((wave & 3) == 0 ? 0 : (wave & 3) + 2) << 21) + (124 + (wave >> 2) * 2 + (c >> 4)) * 16384 + (c & 15) * 512;
    st16_sc1(cal + (lane >> 5) * 8192 + (lane & 31) * 16, u32x4{SENT, SENT, SENT, SENT});
    WAIT_V(0);
    unsigned best[2] = {1u << 30, 1u << 30};
    const char* sb = uni(cal);
#pragma unroll
    for (int r = 1; r < NCS; ++r) {
      unsigned long long t0, t1; unsigned v;
      asm volatile("s_memtime %0\n\ts_waitcnt lgkmcnt(0)\n\ts_load_dword %2, %3, %4 glc\n\ts_waitcnt lgkmcnt(0)\n\ts_memtime %1\n\ts_waitcnt lgkmcnt(0)"
                   : "=&s"(t0), "=&s"(t1), "=&s"(v) : "s"(sb), "i"((r & 1) * 8192 + (r >> 1) * 128) : "memory");
      if (r > 1) best[r & 1] = min(best[r & 1], (unsigned)(t1 - t0));
    }
    sg[2 * wave] = best[0]; sg[2 * wave + 1] = best[1];
  } else {
    const int pc = (c & 7) * 2 + (wave >> 2), g = lane >> 4, hf = (lane >> 2) & 1, row = ((lane >> 3) & 1) * 4 + (lane & 3);
    u32x4 v = *(const u32x4*)(mem0 + row * DM + 32 * (2 * pc + hf) + 8 * g);
    v.x = nosent(v.x); v.y = nosent(v.y); v.z = nosent(v.z); v.w = nosent(v.w);
    st16_sc1(xch_base(ws) + ((size_t)3 << 21) + (size_t)(((c >> 3) & 1) * 2 + (pc >> 3)) * 16384 + (c >> 4) * 8192 + (pc & 7) * 1024 + lane * 16, v);
    const int q = c * 2 + (wave >> 2);
    if (q < 36) {
      char* p = xch_base(ws) + (q < 16 ? ((size_t)3 << 21) + 48 * 16384 + q * 4096 : ((size_t)4 << 21) + (q - 16) * 4096) + lane * 16;
#pragma unroll
      for (int i = 0; i < 4; ++i) st16_sc1(p + i * 1024, u32x4{0u, 0u, 0u, 0u});
    } else if (q == 36 && lane == 0) st4_sc1(tmo, 0u);
  }
  if (fst && wave == 0) STS(ws, 128 + (c >> 3) * 32 + 24 + (c & 7), 2);
  {
    f32x4 v;
    for (int spins = 0; spins < SPIN_CAP; ++spins) {
      u32x4 m[4];
      asm volatile("s_nop 4\n\tglobal_load_dwordx4 %0, %4, %6 sc1\n\tglobal_load_dwordx4 %1, %4, %6 offset:16 sc1\n\t"
                   "global_load_dwordx4 %2, %5, %6 sc1\n\tglobal_load_dwordx4 %3, %5, %6 offset:16 sc1\n\ts_waitcnt vmcnt(0)"
                   : "=&v"(m[0]), "=&v"(m[1]), "=&v"(m[2]), "=&v"(m[3]) : "v"((unsigned)tid * 32), "v"((unsigned)tid * 32 + 16384), "s"(mod) : "memory");
      bool ok = true;
#pragma unroll
      for (int i = 0; i < 4; ++i) ok = ok && m[i].y == (unsigned)tag && m[i].w == tag2(tag);
      v = f32x4{__uint_as_float(m[0].x) + __uint_as_float(m[2].x), __uint_as_float(m[0].z) + __uint_as_float(m[2].z),
                __uint_as_float(m[1].x) + __uint_as_float(m[3].x), __uint_as_float(m[1].z) + __uint_as_float(m[3].z)};
      if (__all(ok)) break;
      __builtin_amdgcn_s_sleep(4);
    }
    v = f32x4{bfr(v[0]), bfr(v[1]), bfr(v[2]), bfr(v[3])};
    const int a = tid >> 8, cc = tid & 255;
    if (a) v = f32x4{bfr(1.0f + v[0]), bfr(1.0f + v[1]), bfr(1.0f + v[2]), bfr(1.0f + v[3])};
    *(f32x4*)(smem + 4096 + a * 4096 + ((cc >> 7) * 2 + (cc & 1)) * 1024 + ((cc >> 1) & 63) * 16) = v;
  }
  BAR();
  if (fst && wave == 0) STS(ws, 128 + (c >> 3) * 32 + 24 + (c & 7), 3);
#pragma unroll
  for (int p = 0; p < 7; ++p) {
    if (p > 0 && fst) break;
    f32x2 v[2][4];
#pragma unroll
    for (int i = 0; i < 2; ++i)
#pragma unroll
      for (int e = 0; e < 4; ++e) v[i][e] = f32x2{bflo(xr[p][i][e]), bfhi(xr[p][i][e])};
    const f32x2 s2 = ((v[0][0] + v[0][1]) + (v[0][2] + v[0][3])) + ((v[1][0] + v[1][1]) + (v[1][2] + v[1][3]));
    const float mean = wavesum(s2[0] + s2[1]) * (1.0f / DM);
    f32x2 q2 = f32x2{0, 0};
#pragma unroll
    for (int i = 0; i < 8; ++i) { const f32x2 d = v[i >> 2][i & 3] - f32x2{mean, mean}; q2 = pkfma(d, d, q2); }
    const float q = wavesum(q2[0] + q2[1]);
    if (p == 0 && fst && wave == 0) STS((char*)tmo - OFF_TMO, 128 + (c >> 3) * 32 + 24 + (c & 7), 5);
    const float mb = bfr(mean), vb = bfr(q * (1.0f / DM));
    const float rden = 1.0f / bfr(sqrtf(bfr(vb + 1e-5f)));
    const int row = 256 * p + row0, rb = row & 31;
    char* dst = h1c + (size_t)(row >> 5) * 65536 + (lane >> 3) * 4096 + rb * 128 + (((lane & 7) ^ (rb & 7)) << 4);
    const char* ml = smem + lane * 16;
#pragma unroll
    for (int i = 0; i < 2; ++i) st16_sc1(dst + i * 32768, norm8(v[i], ml + i * 2048, mb, rden));
  }
  if (fst) {
    unsigned votes = 0, xcc; asm volatile("s_getreg_b32 %0, hwreg(HW_REG_XCC_ID)" : "=s"(xcc));
#pragma unroll
    for (int i = 0; i < 3; ++i) {
      const unsigned b0 = min(sg[2 * i], sg[2 * i + 8]), b1 = min(sg[2 * i + 1], sg[2 * i + 9]), df = max(b0, b1) - min(b0, b1);
      votes |= df < 110u || df > 400u ? 0u : (((b1 < b0) != ((xcc & 4) != 0)) ? 1u : 8u) << i;
    }
    WAIT_V(0);
    __builtin_amdgcn_s_barrier();
    if (tid == 0) st8_sc1(ws + OFF_INIT + c * 128, u32x2{(unsigned)tag << 6 | votes, (unsigned)(tag >> 26)});
  } else {
    cnt = cnt_of(ws, init_wait(ws, tag));
    WAIT_V(0);
    __builtin_amdgcn_s_barrier();
    if (tid == 0) st4_sc1(cnt + (PL1B + c) * 32, 7u);
  }
  if (wave == 0) STS(ws, 128 + (c >> 3) * 32 + 24 + (c & 7), fst ? 6 : 7);
}
__device__ __forceinline__ void arm_job(int wave_id_, int j, u64 tag, char* ws) {
  const int hb = j + 32 * WAVE_ID, b = hb >> 1;
  if (b < 80) {
    const char* p = uni(xch_base(ws) + (b < 48 ? (size_t)((b >> 4) * 32 + (b & 15)) * 16384
                                      : ((size_t)3 << 21) + (size_t)(b < 56 ? 8 + ((b - 48) >> 2) * 8 + (b & 3) : b - 56 + 24) * 16384) + (hb & 1) * 8192);
#pragma unroll
    for (int i = 0; i < 8; ++i) st16s(p, (unsigned)(i * 1024 + lane_id() * 16), u32x4{SENT, SENT, SENT, SENT});
  }
  WAIT_V(0);
  BAR();
  if (opaque_tid() == 0) st8_sc1(ws + OFF_INIT + (32 + j) * 128, u32x2{(unsigned)tag << 6, (unsigned)(tag >> 26)});
}
__device__ __forceinline__ void mod2_job(char* smem, int wave_id_, int c, const float* __restrict__ mod, const u16* __restrict__ cvec, const u16* __restrict__ wmod, unsigned* cnt) {
  const int tid = opaque_tid();
  {
    const float sva = bf2f(cvec[tid]), svb = bf2f(cvec[tid + 512]);
    float* sv = (float*)(smem + 12288);
    sv[tid] = bfr(sva / (1.0f + __expf(-sva))); sv[tid + 512] = bfr(svb / (1.0f + __expf(-svb)));
  }
  __syncthreads();
  {
    const int cg = tid & 15, r0 = tid >> 4;
    const u16* wp = wmod + (size_t)r0 * (6 * DM) + 2 * DM + 128 * c + cg * 8;
    const float* sv = (const float*)(smem + 12288) + r0;
    f32x2 acc[4] = {f32x2{0, 0}, f32x2{0, 0}, f32x2{0, 0}, f32x2{0, 0}};
#pragma unroll 1
    for (int o = 0; o < 4; ++o) {
      u32x4 w[8];
#pragma unroll
      for (int i = 0; i < 8; ++i) w[i] = ldnt<1>(wp + (size_t)(o * 8 + i) * 32 * (6 * DM));
#pragma unroll
      for (int i = 0; i < 8; ++i) {
        const float s = sv[(o * 8 + i) * 32];
#pragma unroll
        for (int e = 0; e < 4; ++e) acc[e] = pkfma(f32x2{s, s}, f32x2{bflo(w[i][e]), bfhi(w[i][e])}, acc[e]);
      }
    }
    float* red = (float*)(smem + 16384);
    *(f32x4*)(red + r0 * 128 + cg * 8) = f32x4{acc[0][0], acc[0][1], acc[1][0], acc[1][1]};
    *(f32x4*)(red + r0 * 128 + cg * 8 + 4) = f32x4{acc[2][0], acc[2][1], acc[3][0], acc[3][1]};
    __syncthreads();
    if (tid < 128) {
      float s = 0;
#pragma unroll
      for (int r = 0; r < 32; ++r) s += red[r * 128 + tid];
      st4_sc1((char*)mod + OFF_MOD2 + (128 * c + tid) * 4, __builtin_bit_cast(unsigned, s));
    }
    WAIT_V(0);
    __builtin_amdgcn_s_barrier();
    if (tid == 0) st4_sc1(cnt + (PL1 + c) * 32 + 1, 1u);
  }
}
enum { R_QKV = 0, R_PART = 1, R_F1 = 2, R_PART2 = 3 };
struct Dep {
  const char* base; unsigned voff; unsigned mul, add;
};
__device__ __forceinline__ unsigned dep_eval(unsigned v, const Dep& d) {
  v = rowmin16(v * d.mul + (d.add + 64u));
  return min(min(rdl(v, 0), rdl(v, 16)), min(rdl(v, 32), rdl(v, 48))) - 64u;
}
typedef unsigned u32x32 __attribute__((ext_vector_type(32)));
typedef unsigned u32x16 __attribute__((ext_vector_type(16)));
__device__ __forceinline__ void fast_ld0(const u32x32 (&W)[4], u32x16& pd, unsigned ea, unsigned da, int& b, int& lf, int cp, unsigned up, unsigned sa, unsigned mu, unsigned ad,
    unsigned rw, unsigned ab, const char* p1, const char* p0, unsigned l1, unsigned l0, const unsigned (&av)[8], int la) {
  asm volatile("s_setprio 0\n\t"
    "s_cmp_eq_u32 %[la], 3\n\t"
    "s_waitcnt vmcnt(12)\n\t"
    "s_cbranch_scc1 20f\n\t"
    "s_waitcnt vmcnt(4)\n\t"
    "20:\n\t"
    "s_barrier\n\t"
    "ds_read_b128 v[96:99], %[ea] offset:0\n\t"
    "ds_read_b128 v[100:103], %[ea] offset:2048\n\t"
    "ds_read_b128 v[104:107], %[da] offset:0\n\t"
    "ds_read_b128 v[108:111], %[da] offset:2048\n\t"
    "ds_read_b128 v[112:115], %[ea] offset:4096\n\t"
    "ds_read_b128 v[116:119], %[ea] offset:6144\n\t"
    "s_branch 2f\n\t"
    ".p2align 6\n\t"
    "1:\n\t"
    "s_cmp_eq_u32 %[la], 3\n\t"
    "s_waitcnt vmcnt(12)\n\t"
    "s_cbranch_scc1 21f\n\t"
    "s_waitcnt vmcnt(4)\n\t"
    "21:\n\t"
    "s_barrier\n\t"
    "2:\n\t"
    "s_waitcnt lgkmcnt(4)\n\t"
    "v_mfma_f32_16x16x32_bf16 v[64:67], v[128:131], v[96:99], 0\n\t"
    "ds_read_b128 v[120:123], %[da] offset:4096\n\t"
    "s_add_u32 m0, %[l1], 0\n\t"
    "ds_write_b128 %[rw], v[80:83] offset:0\n\t"
    "v_mfma_f32_16x16x32_bf16 v[68:71], v[132:135], v[96:99], 0\n\t"
    "ds_read_b128 v[124:127], %[da] offset:6144\n\t"
    "global_load_lds_dwordx4 %[av0], s[36:37] sc1\n\t"
    "ds_write_b128 %[rw], v[84:87] offset:1024\n\t"
    "v_mfma_f32_16x16x32_bf16 v[72:75], v[128:131], v[100:103], 0\n\t"
    "s_add_u32 m0, %[l1], 32768\n\t"
    "ds_write_b128 %[rw], v[88:91] offset:2048\n\t"
    "v_mfma_f32_16x16x32_bf16 v[76:79], v[132:135], v[100:103], 0\n\t"
    "global_load_lds_dwordx4 %[av1], s[36:37] sc1\n\t"
    "ds_write_b128 %[rw], v[92:95] offset:3072\n\t"
    "s_waitcnt lgkmcnt(8)\n\t"
    "v_mfma_f32_16x16x32_bf16 v[64:67], v[136:139], v[104:107], v[64:67]\n\t"
    "ds_read_b128 v[96:99], %[ea] offset:8192\n\t"
    "s_add_u32 m0, %[l1], 4096\n\t"
    "v_mfma_f32_16x16x32_bf16 v[68:71], v[140:143], v[104:107], v[68:71]\n\t"
    "ds_read_b128 v[100:103], %[ea] offset:10240\n\t"
    "global_load_lds_dwordx4 %[av2], s[36:37] sc1\n\t"
    "v_mfma_f32_16x16x32_bf16 v[72:75], v[136:139], v[108:111], v[72:75]\n\t"
    "s_add_u32 m0, %[l1], 36864\n\t"
    "v_mfma_f32_16x16x32_bf16 v[76:79], v[140:143], v[108:111], v[76:79]\n\t"
    "global_load_lds_dwordx4 %[av3], s[36:37] sc1\n\t"
    "s_waitcnt lgkmcnt(8)\n\t"
    "v_mfma_f32_16x16x32_bf16 v[64:67], v[144:147], v[112:115], v[64:67]\n\t"
    "ds_read_b128 v[104:107], %[da] offset:8192\n\t"
    "s_add_u32 m0, %[l1], 8192\n\t"
    "v_mfma_f32_16x16x32_bf16 v[68:71], v[148:151], v[112:115], v[68:71]\n\t"
    "ds_read_b128 v[108:111], %[da] offset:10240\n\t"
    "global_load_lds_dwordx4 %[av4], s[36:37] sc1\n\t"
    "v_mfma_f32_16x16x32_bf16 v[72:75], v[144:147], v[116:119], v[72:75]\n\t"
    "s_add_u32 m0, %[l1], 40960\n\t"
    "v_mfma_f32_16x16x32_bf16 v[76:79], v[148:151], v[116:119], v[76:79]\n\t"
    "global_load_lds_dwordx4 %[av5], s[36:37] sc1\n\t"
    "s_waitcnt lgkmcnt(7)\n\t"
    "v_mfma_f32_16x16x32_bf16 v[64:67], v[152:155], v[120:123], v[64:67]\n\t"
    "ds_read_b128 v[112:115], %[ea] offset:12288\n\t"
    "s_add_u32 m0, %[l1], 12288\n\t"
    "v_mfma_f32_16x16x32_bf16 v[68:71], v[156:159], v[120:123], v[68:71]\n\t"
    "ds_read_b128 v[116:119], %[ea] offset:14336\n\t"
    "global_load_lds_dwordx4 %[av6], s[36:37] sc1\n\t"
    "v_mfma_f32_16x16x32_bf16 v[72:75], v[152:155], v[124:127], v[72:75]\n\t"
    "s_add_u32 m0, %[l1], 45056\n\t"
    "v_mfma_f32_16x16x32_bf16 v[76:79], v[156:159], v[124:127], v[76:79]\n\t"
    "global_load_lds_dwordx4 %[av7], s[36:37] sc1\n\t"
    "s_waitcnt lgkmcnt(4)\n\t"
    "v_mfma_f32_16x16x32_bf16 v[64:67], v[160:163], v[96:99], v[64:67]\n\t"
    "ds_read_b128 v[120:123], %[da] offset:12288\n\t"
    "v_mfma_f32_16x16x32_bf16 v[68:71], v[164:167], v[96:99], v[68:71]\n\t"
    "ds_read_b128 v[124:127], %[da] offset:14336\n\t"
    "v_mfma_f32_16x16x32_bf16 v[72:75], v[160:163], v[100:103], v[72:75]\n\t"
    "v_mfma_f32_16x16x32_bf16 v[76:79], v[164:167], v[100:103], v[76:79]\n\t"
    "s_waitcnt lgkmcnt(4)\n\t"
    "v_mfma_f32_16x16x32_bf16 v[64:67], v[168:171], v[104:107], v[64:67]\n\t"
    "ds_read_b128 v[96:99], %[ea] offset:16384\n\t"
    "v_mfma_f32_16x16x32_bf16 v[68:71], v[172:175], v[104:107], v[68:71]\n\t"
    "ds_read_b128 v[100:103], %[ea] offset:18432\n\t"
    "v_mfma_f32_16x16x32_bf16 v[72:75], v[168:171], v[108:111], v[72:75]\n\t"
    "v_mfma_f32_16x16x32_bf16 v[76:79], v[172:175], v[108:111], v[76:79]\n\t"
    "s_waitcnt lgkmcnt(4)\n\t"
    "v_mfma_f32_16x16x32_bf16 v[64:67], v[176:179], v[112:115], v[64:67]\n\t"
    "ds_read_b128 v[104:107], %[da] offset:16384\n\t"
    "v_mfma_f32_16x16x32_bf16 v[68:71], v[180:183], v[112:115], v[68:71]\n\t"
    "ds_read_b128 v[108:111], %[da] offset:18432\n\t"
    "v_mfma_f32_16x16x32_bf16 v[72:75], v[176:179], v[116:119], v[72:75]\n\t"
    "v_mfma_f32_16x16x32_bf16 v[76:79], v[180:183], v[116:119], v[76:79]\n\t"
    "s_waitcnt lgkmcnt(4)\n\t"
    "v_mfma_f32_16x16x32_bf16 v[64:67], v[184:187], v[120:123], v[64:67]\n\t"
    "ds_read_b128 v[112:115], %[ea] offset:20480\n\t"
    "v_mfma_f32_16x16x32_bf16 v[68:71], v[188:191], v[120:123], v[68:71]\n\t"
    "ds_read_b128 v[116:119], %[ea] offset:22528\n\t"
    "v_mfma_f32_16x16x32_bf16 v[72:75], v[184:187], v[124:127], v[72:75]\n\t"
    "v_mfma_f32_16x16x32_bf16 v[76:79], v[188:191], v[124:127], v[76:79]\n\t"
    "s_cmp_eq_u32 %[la], 3\n\t"
    "s_waitcnt vmcnt(12)\n\t"
    "s_cbranch_scc1 22f\n\t"
    "s_waitcnt vmcnt(4)\n\t"
    "22:\n\t"
    "s_barrier\n\t"
    "s_waitcnt lgkmcnt(4)\n\t"
    "v_mfma_f32_16x16x32_bf16 v[64:67], v[192:195], v[96:99], v[64:67]\n\t"
    "ds_read_b128 v[120:123], %[da] offset:20480\n\t"
    "s_add_u32 m0, %[l0], 0\n\t"
    "v_mfma_f32_16x16x32_bf16 v[68:71], v[196:199], v[96:99], v[68:71]\n\t"
    "ds_read_b128 v[124:127], %[da] offset:22528\n\t"
    "global_load_lds_dwordx4 %[av0], s[38:39] sc1\n\t"
    "v_mfma_f32_16x16x32_bf16 v[72:75], v[192:195], v[100:103], v[72:75]\n\t"
    "s_add_u32 m0, %[l0], 32768\n\t"
    "v_mfma_f32_16x16x32_bf16 v[76:79], v[196:199], v[100:103], v[76:79]\n\t"
    "global_load_lds_dwordx4 %[av1], s[38:39] sc1\n\t"
    "s_waitcnt lgkmcnt(4)\n\t"
    "v_mfma_f32_16x16x32_bf16 v[64:67], v[200:203], v[104:107], v[64:67]\n\t"
    "ds_read_b128 v[96:99], %[ea] offset:24576\n\t"
    "s_add_u32 m0, %[l0], 4096\n\t"
    "v_mfma_f32_16x16x32_bf16 v[68:71], v[204:207], v[104:107], v[68:71]\n\t"
    "ds_read_b128 v[100:103], %[ea] offset:26624\n\t"
    "global_load_lds_dwordx4 %[av2], s[38:39] sc1\n\t"
    "v_mfma_f32_16x16x32_bf16 v[72:75], v[200:203], v[108:111], v[72:75]\n\t"
    "s_add_u32 m0, %[l0], 36864\n\t"
    "v_mfma_f32_16x16x32_bf16 v[76:79], v[204:207], v[108:111], v[76:79]\n\t"
    "global_load_lds_dwordx4 %[av3], s[38:39] sc1\n\t"
    "s_add_u32 s36, s36, %[ab]\n\t"
    "s_addc_u32 s37, s37, 0\n\t"
    "s_waitcnt lgkmcnt(4)\n\t"
    "v_mfma_f32_16x16x32_bf16 v[64:67], v[208:211], v[112:115], v[64:67]\n\t"
    "ds_read_b128 v[104:107], %[da] offset:24576\n\t"
    "s_add_u32 m0, %[l0], 8192\n\t"
    "v_mfma_f32_16x16x32_bf16 v[68:71], v[212:215], v[112:115], v[68:71]\n\t"
    "ds_read_b128 v[108:111], %[da] offset:26624\n\t"
    "global_load_lds_dwordx4 %[av4], s[38:39] sc1\n\t"
    "v_mfma_f32_16x16x32_bf16 v[72:75], v[208:211], v[116:119], v[72:75]\n\t"
    "ds_read_b32 v39, %[sa]\n\t"
    "s_add_u32 m0, %[l0], 40960\n\t"
    "v_mfma_f32_16x16x32_bf16 v[76:79], v[212:215], v[116:119], v[76:79]\n\t"
    "global_load_lds_dwordx4 %[av5], s[38:39] sc1\n\t"
    "s_xor_b32 %[l1], %[l1], 0x10000\n\t"
    "s_waitcnt lgkmcnt(5)\n\t"
    "v_mfma_f32_16x16x32_bf16 v[64:67], v[216:219], v[120:123], v[64:67]\n\t"
    "ds_read_b128 v[112:115], %[ea] offset:28672\n\t"
    "s_add_u32 m0, %[l0], 12288\n\t"
    "v_mfma_f32_16x16x32_bf16 v[68:71], v[220:223], v[120:123], v[68:71]\n\t"
    "ds_read_b128 v[116:119], %[ea] offset:30720\n\t"
    "global_load_lds_dwordx4 %[av6], s[38:39] sc1\n\t"
    "v_mfma_f32_16x16x32_bf16 v[72:75], v[216:219], v[124:127], v[72:75]\n\t"
    "v_xor_b32 %[ea], 0x10000, %[ea]\n\t"
    "s_add_u32 m0, %[l0], 45056\n\t"
    "v_mfma_f32_16x16x32_bf16 v[76:79], v[220:223], v[124:127], v[76:79]\n\t"
    "global_load_lds_dwordx4 %[av7], s[38:39] sc1\n\t"
    "s_waitcnt lgkmcnt(5)\n\t"
    "v_mfma_f32_16x16x32_bf16 v[64:67], v[224:227], v[96:99], v[64:67]\n\t"
    "ds_read_b128 v[120:123], %[da] offset:28672\n\t"
    "v_mfma_f32_16x16x32_bf16 v[68:71], v[228:231], v[96:99], v[68:71]\n\t"
    "ds_read_b128 v[124:127], %[da] offset:30720\n\t"
    "v_mfma_f32_16x16x32_bf16 v[72:75], v[224:227], v[100:103], v[72:75]\n\t"
    "v_xor_b32 %[da], 0x10000, %[da]\n\t"
    "v_mfma_f32_16x16x32_bf16 v[76:79], v[228:231], v[100:103], v[76:79]\n\t"
    "s_waitcnt lgkmcnt(5)\n\t"
    "v_mfma_f32_16x16x32_bf16 v[64:67], v[232:235], v[104:107], v[64:67]\n\t"
    "ds_read_b128 v[96:99], %[ea] offset:0\n\t"
    "v_mfma_f32_16x16x32_bf16 v[68:71], v[236:239], v[104:107], v[68:71]\n\t"
    "ds_read_b128 v[100:103], %[ea] offset:2048\n\t"
    "v_mfma_f32_16x16x32_bf16 v[72:75], v[232:235], v[108:111], v[72:75]\n\t"
    "v_mfma_f32_16x16x32_bf16 v[76:79], v[236:239], v[108:111], v[76:79]\n\t"
    "s_waitcnt lgkmcnt(4)\n\t"
    "v_mfma_f32_16x16x32_bf16 v[64:67], v[240:243], v[112:115], v[64:67]\n\t"
    "ds_read_b128 v[104:107], %[da] offset:0\n\t"
    "v_mad_u32_u24 v39, v39, %[mu], %[ad]\n\t"
    "v_mfma_f32_16x16x32_bf16 v[68:71], v[244:247], v[112:115], v[68:71]\n\t"
    "ds_read_b128 v[108:111], %[da] offset:2048\n\t"
    "v_cmp_ge_i32 vcc, %[lf], v39\n\t"
    "s_cmp_eq_u64 vcc, 0\n\t"
    "s_addc_u32 %[lf], %[lf], 0\n\t"
    "v_mfma_f32_16x16x32_bf16 v[72:75], v[240:243], v[116:119], v[72:75]\n\t"
    "v_mfma_f32_16x16x32_bf16 v[76:79], v[244:247], v[116:119], v[76:79]\n\t"
    "v_cmp_ge_i32 vcc, %[lf], v39\n\t"
    "s_cmp_eq_u64 vcc, 0\n\t"
    "s_addc_u32 %[lf], %[lf], 0\n\t"
    "s_waitcnt lgkmcnt(4)\n\t"
    "v_mfma_f32_16x16x32_bf16 v[80:83], v[248:251], v[120:123], v[64:67]\n\t"
    "ds_read_b128 v[112:115], %[ea] offset:4096\n\t"
    "s_min_i32 %[lf], %[lf], %[cp]\n\t"
    "s_add_u32 %[b], %[b], 1\n\t"
    "v_mfma_f32_16x16x32_bf16 v[84:87], v[252:255], v[120:123], v[68:71]\n\t"
    "ds_read_b128 v[116:119], %[ea] offset:6144\n\t"
    "v_mfma_f32_16x16x32_bf16 v[88:91], v[248:251], v[124:127], v[72:75]\n\t"
    "v_mfma_f32_16x16x32_bf16 v[92:95], v[252:255], v[124:127], v[76:79]\n\t"
    "s_add_u32 s38, s38, %[ab]\n\t"
    "s_addc_u32 s39, s39, 0\n\t"
    "s_xor_b32 %[l0], %[l0], 0x10000\n\t"
    "s_sub_u32 m0, %[lf], %[b]\n\t"
    "s_sub_u32 m0, m0, 1\n\t"
    "s_cmp_lt_u32 m0, %[up]\n\t"
    "s_cbranch_scc1 1b\n\t"
    "s_waitcnt lgkmcnt(0)\n\t"
    "s_nop 4"
    : "=&{v[80:95]}"(pd), [ea] "+&v"(ea), [da] "+&v"(da), [b] "+&s"(b), [lf] "+&s"(lf), "+{s[36:37]}"(p1), "+{s[38:39]}"(p0), [sa] "+&v"(sa), [l1] "+&s"(l1), [l0] "+&s"(l0)
    : [rw] "v"(rw), [ab] "s"(ab), [cp] "s"(cp), [mu] "s"(mu), [ad] "v"(ad), [up] "s"(up), "{v[128:159]}"(W[0]), "{v[160:191]}"(W[1]), "{v[192:223]}"(W[2]), "{v[224:255]}"(W[3]), [av0] "v"(av[0]), [av1] "v"(av[1]), [av2] "v"(av[2]), [av3] "v"(av[3]), [av4] "v"(av[4]), [av5] "v"(av[5]), [av6] "v"(av[6]), [av7] "v"(av[7]), [la] "s"(la)
    : "v39", "v64", "v65", "v66", "v67", "v68", "v69", "v70", "v71", "v72", "v73", "v74", "v75", "v76", "v77", "v78", "v79", "v96", "v97", "v98", "v99", "v100", "v101", "v102", "v103", "v104", "v105", "v106", "v107", "v108", "v109", "v110", "v111", "v112", "v113", "v114", "v115", "v116", "v117", "v118", "v119", "v120", "v121", "v122", "v123", "v124", "v125", "v126", "v127", "vcc", "scc", "m0", "memory");
}
__device__ __forceinline__ void fast_ld1(const u32x32 (&W)[4], u32x16& pd, unsigned ea, unsigned da, int& b, int& lf, int cp, unsigned up, unsigned sa, unsigned mu, unsigned ad,
    unsigned rw, unsigned ab, const char* p1, const char* p0, unsigned l1, unsigned l0, const unsigned (&av)[8], int la) {
  asm volatile("s_setprio 0\n\t"
    "s_cmp_eq_u32 %[la], 3\n\t"
    "s_waitcnt vmcnt(12)\n\t"
    "s_cbranch_scc1 20f\n\t"
    "s_waitcnt vmcnt(4)\n\t"
    "20:\n\t"
    "s_barrier\n\t"
    "ds_read_b128 v[96:99], %[ea] offset:0\n\t"
    "ds_read_b128 v[100:103], %[ea] offset:2048\n\t"
    "ds_read_b128 v[104:107], %[da] offset:0\n\t"
    "ds_read_b128 v[108:111], %[da] offset:2048\n\t"
    "ds_read_b128 v[112:115], %[ea] offset:4096\n\t"
    "ds_read_b128 v[116:119], %[ea] offset:6144\n\t"
    "s_branch 2f\n\t"
    ".p2align 6\n\t"
    "1:\n\t"
    "s_cmp_eq_u32 %[la], 3\n\t"
    "s_waitcnt vmcnt(12)\n\t"
    "s_cbranch_scc1 21f\n\t"
    "s_waitcnt vmcnt(4)\n\t"
    "21:\n\t"
    "s_barrier\n\t"
    "2:\n\t"
    "s_waitcnt lgkmcnt(4)\n\t"
    "v_mfma_f32_16x16x32_bf16 v[64:67], v[128:131], v[96:99], 0\n\t"
    "ds_read_b128 v[120:123], %[da] offset:4096\n\t"
    "s_add_u32 m0, %[l1], 0\n\t"
    "ds_write_b128 %[rw], v[80:83] offset:0\n\t"
    "v_mfma_f32_16x16x32_bf16 v[68:71], v[132:135], v[96:99], 0\n\t"
    "ds_read_b128 v[124:127], %[da] offset:6144\n\t"
    "global_load_lds_dwordx4 %[av0], s[36:37] sc1\n\t"
    "ds_write_b128 %[rw], v[84:87] offset:1024\n\t"
    "v_mfma_f32_16x16x32_bf16 v[72:75], v[128:131], v[100:103], 0\n\t"
    "s_add_u32 m0, %[l1], 32768\n\t"
    "ds_write_b128 %[rw], v[88:91] offset:2048\n\t"
    "v_mfma_f32_16x16x32_bf16 v[76:79], v[132:135], v[100:103], 0\n\t"
    "global_load_lds_dwordx4 %[av1], s[36:37] sc1\n\t"
    "ds_write_b128 %[rw], v[92:95] offset:3072\n\t"
    "s_waitcnt lgkmcnt(8)\n\t"
    "v_mfma_f32_16x16x32_bf16 v[64:67], v[136:139], v[104:107], v[64:67]\n\t"
    "ds_read_b128 v[96:99], %[ea] offset:8192\n\t"
    "s_add_u32 m0, %[l1], 4096\n\t"
    "v_mfma_f32_16x16x32_bf16 v[68:71], v[140:143], v[104:107], v[68:71]\n\t"
    "ds_read_b128 v[100:103], %[ea] offset:10240\n\t"
    "global_load_lds_dwordx4 %[av2], s[36:37] sc1\n\t"
    "v_mfma_f32_16x16x32_bf16 v[72:75], v[136:139], v[108:111], v[72:75]\n\t"
    "s_add_u32 m0, %[l1], 36864\n\t"
    "v_mfma_f32_16x16x32_bf16 v[76:79], v[140:143], v[108:111], v[76:79]\n\t"
    "global_load_lds_dwordx4 %[av3], s[36:37] sc1\n\t"
    "s_waitcnt lgkmcnt(8)\n\t"
    "v_mfma_f32_16x16x32_bf16 v[64:67], v[144:147], v[112:115], v[64:67]\n\t"
    "ds_read_b128 v[104:107], %[da] offset:8192\n\t"
    "s_add_u32 m0, %[l1], 8192\n\t"
    "v_mfma_f32_16x16x32_bf16 v[68:71], v[148:151], v[112:115], v[68:71]\n\t"
    "ds_read_b128 v[108:111], %[da] offset:10240\n\t"
    "global_load_lds_dwordx4 %[av4], s[36:37] sc1\n\t"
    "v_mfma_f32_16x16x32_bf16 v[72:75], v[144:147], v[116:119], v[72:75]\n\t"
    "s_add_u32 m0, %[l1], 40960\n\t"
    "v_mfma_f32_16x16x32_bf16 v[76:79], v[148:151], v[116:119], v[76:79]\n\t"
    "global_load_lds_dwordx4 %[av5], s[36:37] sc1\n\t"
    "s_waitcnt lgkmcnt(7)\n\t"
    "v_mfma_f32_16x16x32_bf16 v[64:67], v[152:155], v[120:123], v[64:67]\n\t"
    "ds_read_b128 v[112:115], %[ea] offset:12288\n\t"
    "s_add_u32 m0, %[l1], 12288\n\t"
    "v_mfma_f32_16x16x32_bf16 v[68:71], v[156:159], v[120:123], v[68:71]\n\t"
    "ds_read_b128 v[116:119], %[ea] offset:14336\n\t"
    "global_load_lds_dwordx4 %[av6], s[36:37] sc1\n\t"
    "v_mfma_f32_16x16x32_bf16 v[72:75], v[152:155], v[124:127], v[72:75]\n\t"
    "s_add_u32 m0, %[l1], 45056\n\t"
    "v_mfma_f32_16x16x32_bf16 v[76:79], v[156:159], v[124:127], v[76:79]\n\t"
    "global_load_lds_dwordx4 %[av7], s[36:37] sc1\n\t"
    "s_waitcnt lgkmcnt(4)\n\t"
    "v_mfma_f32_16x16x32_bf16 v[64:67], v[160:163], v[96:99], v[64:67]\n\t"
    "ds_read_b128 v[120:123], %[da] offset:12288\n\t"
    "v_mfma_f32_16x16x32_bf16 v[68:71], v[164:167], v[96:99], v[68:71]\n\t"
    "ds_read_b128 v[124:127], %[da] offset:14336\n\t"
    "v_mfma_f32_16x16x32_bf16 v[72:75], v[160:163], v[100:103], v[72:75]\n\t"
    "v_mfma_f32_16x16x32_bf16 v[76:79], v[164:167], v[100:103], v[76:79]\n\t"
    "s_waitcnt lgkmcnt(4)\n\t"
    "v_mfma_f32_16x16x32_bf16 v[64:67], v[168:171], v[104:107], v[64:67]\n\t"
    "ds_read_b128 v[96:99], %[ea] offset:16384\n\t"
    "v_mfma_f32_16x16x32_bf16 v[68:71], v[172:175], v[104:107], v[68:71]\n\t"
    "ds_read_b128 v[100:103], %[ea] offset:18432\n\t"
    "v_mfma_f32_16x16x32_bf16 v[72:75], v[168:171], v[108:111], v[72:75]\n\t"
    "v_mfma_f32_16x16x32_bf16 v[76:79], v[172:175], v[108:111], v[76:79]\n\t"
    "s_waitcnt lgkmcnt(4)\n\t"
    "v_mfma_f32_16x16x32_bf16 v[64:67], v[176:179], v[112:115], v[64:67]\n\t"
    "ds_read_b128 v[104:107], %[da] offset:16384\n\t"
    "v_mfma_f32_16x16x32_bf16 v[68:71], v[180:183], v[112:115], v[68:71]\n\t"
    "ds_read_b128 v[108:111], %[da] offset:18432\n\t"
    "v_mfma_f32_16x16x32_bf16 v[72:75], v[176:179], v[116:119], v[72:75]\n\t"
    "v_mfma_f32_16x16x32_bf16 v[76:79], v[180:183], v[116:119], v[76:79]\n\t"
    "s_waitcnt lgkmcnt(4)\n\t"
    "v_mfma_f32_16x16x32_bf16 v[64:67], v[184:187], v[120:123], v[64:67]\n\t"
    "ds_read_b128 v[112:115], %[ea] offset:20480\n\t"
    "v_mfma_f32_16x16x32_bf16 v[68:71], v[188:191], v[120:123], v[68:71]\n\t"
    "ds_read_b128 v[116:119], %[ea] offset:22528\n\t"
    "v_mfma_f32_16x16x32_bf16 v[72:75], v[184:187], v[124:127], v[72:75]\n\t"
    "v_mfma_f32_16x16x32_bf16 v[76:79], v[188:191], v[124:127], v[76:79]\n\t"
    "s_cmp_eq_u32 %[la], 3\n\t"
    "s_waitcnt vmcnt(12)\n\t"
    "s_cbranch_scc1 22f\n\t"
    "s_waitcnt vmcnt(4)\n\t"
    "22:\n\t"
    "s_barrier\n\t"
    "s_waitcnt lgkmcnt(4)\n\t"
    "v_mfma_f32_16x16x32_bf16 v[64:67], v[192:195], v[96:99], v[64:67]\n\t"
    "ds_read_b128 v[120:123], %[da] offset:20480\n\t"
    "s_add_u32 m0, %[l0], 0\n\t"
    "v_mfma_f32_16x16x32_bf16 v[68:71], v[196:199], v[96:99], v[68:71]\n\t"
    "ds_read_b128 v[124:127], %[da] offset:22528\n\t"
    "global_load_lds_dwordx4 %[av0], s[38:39] sc1\n\t"
    "v_mfma_f32_16x16x32_bf16 v[72:75], v[192:195], v[100:103], v[72:75]\n\t"
    "s_add_u32 m0, %[l0], 32768\n\t"
    "v_mfma_f32_16x16x32_bf16 v[76:79], v[196:199], v[100:103], v[76:79]\n\t"
    "global_load_lds_dwordx4 %[av1], s[38:39] sc1\n\t"
    "s_waitcnt lgkmcnt(4)\n\t"
    "v_mfma_f32_16x16x32_bf16 v[64:67], v[200:203], v[104:107], v[64:67]\n\t"
    "ds_read_b128 v[96:99], %[ea] offset:24576\n\t"
    "s_add_u32 m0, %[l0], 4096\n\t"
    "v_mfma_f32_16x16x32_bf16 v[68:71], v[204:207], v[104:107], v[68:71]\n\t"
    "ds_read_b128 v[100:103], %[ea] offset:26624\n\t"
    "global_load_lds_dwordx4 %[av2], s[38:39] sc1\n\t"
    "v_mfma_f32_16x16x32_bf16 v[72:75], v[200:203], v[108:111], v[72:75]\n\t"
    "s_add_u32 m0, %[l0], 36864\n\t"
    "v_mfma_f32_16x16x32_bf16 v[76:79], v[204:207], v[108:111], v[76:79]\n\t"
    "global_load_lds_dwordx4 %[av3], s[38:39] sc1\n\t"
    "s_add_u32 s36, s36, %[ab]\n\t"
    "s_addc_u32 s37, s37, 0\n\t"
    "s_waitcnt lgkmcnt(4)\n\t"
    "v_mfma_f32_16x16x32_bf16 v[64:67], v[208:211], v[112:115], v[64:67]\n\t"
    "ds_read_b128 v[104:107], %[da] offset:24576\n\t"
    "s_add_u32 m0, %[l0], 8192\n\t"
    "v_mfma_f32_16x16x32_bf16 v[68:71], v[212:215], v[112:115], v[68:71]\n\t"
    "ds_read_b128 v[108:111], %[da] offset:26624\n\t"
    "global_load_lds_dwordx4 %[av4], s[38:39] sc1\n\t"
    "v_mfma_f32_16x16x32_bf16 v[72:75], v[208:211], v[116:119], v[72:75]\n\t"
    "ds_read_b32 v39, %[sa]\n\t"
    "s_add_u32 m0, %[l0], 40960\n\t"
    "v_mfma_f32_16x16x32_bf16 v[76:79], v[212:215], v[116:119], v[76:79]\n\t"
    "global_load_lds_dwordx4 %[av5], s[38:39] sc1\n\t"
    "s_xor_b32 %[l1], %[l1], 0x10000\n\t"
    "s_waitcnt lgkmcnt(5)\n\t"
    "v_mfma_f32_16x16x32_bf16 v[64:67], v[216:219], v[120:123], v[64:67]\n\t"
    "ds_read_b128 v[112:115], %[ea] offset:28672\n\t"
    "s_add_u32 m0, %[l0], 12288\n\t"
    "v_mfma_f32_16x16x32_bf16 v[68:71], v[220:223], v[120:123], v[68:71]\n\t"
    "ds_read_b128 v[116:119], %[ea] offset:30720\n\t"
    "global_load_lds_dwordx4 %[av6], s[38:39] sc1\n\t"
    "v_mfma_f32_16x16x32_bf16 v[72:75], v[216:219], v[124:127], v[72:75]\n\t"
    "v_xor_b32 %[ea], 0x10000, %[ea]\n\t"
    "s_add_u32 m0, %[l0], 45056\n\t"
    "v_mfma_f32_16x16x32_bf16 v[76:79], v[220:223], v[124:127], v[76:79]\n\t"
    "global_load_lds_dwordx4 %[av7], s[38:39] sc1\n\t"
    "s_waitcnt lgkmcnt(5)\n\t"
    "v_mfma_f32_16x16x32_bf16 v[64:67], v[224:227], v[96:99], v[64:67]\n\t"
    "ds_read_b128 v[120:123], %[da] offset:28672\n\t"
    "v_mfma_f32_16x16x32_bf16 v[68:71], v[228:231], v[96:99], v[68:71]\n\t"
    "ds_read_b128 v[124:127], %[da] offset:30720\n\t"
    "v_mfma_f32_16x16x32_bf16 v[72:75], v[224:227], v[100:103], v[72:75]\n\t"
    "v_xor_b32 %[da], 0x10000, %[da]\n\t"
    "v_mfma_f32_16x16x32_bf16 v[76:79], v[228:231], v[100:103], v[76:79]\n\t"
    "s_waitcnt lgkmcnt(5)\n\t"
    "v_mfma_f32_16x16x32_bf16 v[64:67], v[232:235], v[104:107], v[64:67]\n\t"
    "ds_read_b128 v[96:99], %[ea] offset:0\n\t"
    "v_mfma_f32_16x16x32_bf16 v[68:71], v[236:239], v[104:107], v[68:71]\n\t"
    "ds_read_b128 v[100:103], %[ea] offset:2048\n\t"
    "v_mfma_f32_16x16x32_bf16 v[72:75], v[232:235], v[108:111], v[72:75]\n\t"
    "v_mfma_f32_16x16x32_bf16 v[76:79], v[236:239], v[108:111], v[76:79]\n\t"
    "s_waitcnt lgkmcnt(4)\n\t"
    "v_mfma_f32_16x16x32_bf16 v[64:67], v[240:243], v[112:115], v[64:67]\n\t"
    "ds_read_b128 v[104:107], %[da] offset:0\n\t"
    "v_mad_u32_u24 v39, v39, %[mu], %[ad]\n\t"
    "v_mfma_f32_16x16x32_bf16 v[68:71], v[244:247], v[112:115], v[68:71]\n\t"
    "ds_read_b128 v[108:111], %[da] offset:2048\n\t"
    "v_cmp_ge_i32 vcc, %[lf], v39\n\t"
    "s_cmp_eq_u64 vcc, 0\n\t"
    "s_addc_u32 %[lf], %[lf], 0\n\t"
    "v_mfma_f32_16x16x32_bf16 v[72:75], v[240:243], v[116:119], v[72:75]\n\t"
    "v_mfma_f32_16x16x32_bf16 v[76:79], v[244:247], v[116:119], v[76:79]\n\t"
    "v_cmp_ge_i32 vcc, %[lf], v39\n\t"
    "s_cmp_eq_u64 vcc, 0\n\t"
    "s_addc_u32 %[lf], %[lf], 0\n\t"
    "s_waitcnt lgkmcnt(4)\n\t"
    "v_mfma_f32_16x16x32_bf16 v[80:83], v[248:251], v[120:123], v[64:67]\n\t"
    "ds_read_b128 v[112:115], %[ea] offset:4096\n\t"
    "s_min_i32 %[lf], %[lf], %[cp]\n\t"
    "s_add_u32 %[b], %[b], 1\n\t"
    "v_mfma_f32_16x16x32_bf16 v[84:87], v[252:255], v[120:123], v[68:71]\n\t"
    "ds_read_b128 v[116:119], %[ea] offset:6144\n\t"
    "v_mfma_f32_16x16x32_bf16 v[88:91], v[248:251], v[124:127], v[72:75]\n\t"
    "v_mfma_f32_16x16x32_bf16 v[92:95], v[252:255], v[124:127], v[76:79]\n\t"
    "s_add_u32 s38, s38, %[ab]\n\t"
    "s_addc_u32 s39, s39, 0\n\t"
    "s_xor_b32 %[l0], %[l0], 0x10000\n\t"
    "s_sub_u32 m0, %[lf], %[b]\n\t"
    "s_sub_u32 m0, m0, 1\n\t"
    "s_cmp_lt_u32 m0, %[up]\n\t"
    "s_cbranch_scc1 1b\n\t"
    "s_waitcnt lgkmcnt(0)\n\t"
    "s_nop 4"
    : "=&{v[80:95]}"(pd), [ea] "+&v"(ea), [da] "+&v"(da), [b] "+&s"(b), [lf] "+&s"(lf), "+{s[36:37]}"(p1), "+{s[38:39]}"(p0), [sa] "+&v"(sa), [l1] "+&s"(l1), [l0] "+&s"(l0)
    : [rw] "v"(rw), [ab] "s"(ab), [cp] "s"(cp), [mu] "s"(mu), [ad] "v"(ad), [up] "s"(up), "{v[128:159]}"(W[0]), "{v[160:191]}"(W[1]), "{v[192:223]}"(W[2]), "{v[224:255]}"(W[3]), [av0] "v"(av[0]), [av1] "v"(av[1]), [av2] "v"(av[2]), [av3] "v"(av[3]), [av4] "v"(av[4]), [av5] "v"(av[5]), [av6] "v"(av[6]), [av7] "v"(av[7]), [la] "s"(la)
    : "v39", "v64", "v65", "v66", "v67", "v68", "v69", "v70", "v71", "v72", "v73", "v74", "v75", "v76", "v77", "v78", "v79", "v96", "v97", "v98", "v99", "v100", "v101", "v102", "v103", "v104", "v105", "v106", "v107", "v108", "v109", "v110", "v111", "v112", "v113", "v114", "v115", "v116", "v117", "v118", "v119", "v120", "v121", "v122", "v123", "v124", "v125", "v126", "v127", "vcc", "scc", "m0", "memory");
}
__device__ __forceinline__ void fast_ld2(const u32x32 (&W)[4], u32x16& pd, unsigned ea, unsigned da, int& b, int& lf, int cp, unsigned up, unsigned sa, unsigned mu, unsigned ad,
    unsigned rw, unsigned ab, const char* p1, const char* p0, unsigned l1, unsigned l0, const unsigned (&av)[8], int la) {
  asm volatile("s_setprio 0\n\t"
    "s_cmp_eq_u32 %[la], 3\n\t"
    "s_waitcnt vmcnt(6)\n\t"
    "s_cbranch_scc1 20f\n\t"
    "s_waitcnt vmcnt(2)\n\t"
    "20:\n\t"
    "s_barrier\n\t"
    "ds_read_b128 v[96:99], %[ea] offset:0\n\t"
    "ds_read_b128 v[100:103], %[ea] offset:2048\n\t"
    "ds_read_b128 v[104:107], %[da] offset:0\n\t"
    "ds_read_b128 v[108:111], %[da] offset:2048\n\t"
    "ds_read_b128 v[112:115], %[ea] offset:4096\n\t"
    "ds_read_b128 v[116:119], %[ea] offset:6144\n\t"
    "s_branch 2f\n\t"
    ".p2align 6\n\t"
    "1:\n\t"
    "s_cmp_eq_u32 %[la], 3\n\t"
    "s_waitcnt vmcnt(6)\n\t"
    "s_cbranch_scc1 21f\n\t"
    "s_waitcnt vmcnt(2)\n\t"
    "21:\n\t"
    "s_barrier\n\t"
    "2:\n\t"
    "s_waitcnt lgkmcnt(4)\n\t"
    "v_mfma_f32_16x16x32_bf16 v[64:67], v[128:131], v[96:99], 0\n\t"
    "ds_read_b128 v[120:123], %[da] offset:4096\n\t"
    "s_add_u32 m0, %[l1], 0\n\t"
    "ds_write_b128 %[rw], v[80:83] offset:0\n\t"
    "v_mfma_f32_16x16x32_bf16 v[68:71], v[132:135], v[96:99], 0\n\t"
    "ds_read_b128 v[124:127], %[da] offset:6144\n\t"
    "global_load_lds_dwordx4 %[av0], s[36:37] sc1\n\t"
    "ds_write_b128 %[rw], v[84:87] offset:1024\n\t"
    "v_mfma_f32_16x16x32_bf16 v[72:75], v[128:131], v[100:103], 0\n\t"
    "ds_write_b128 %[rw], v[88:91] offset:2048\n\t"
    "v_mfma_f32_16x16x32_bf16 v[76:79], v[132:135], v[100:103], 0\n\t"
    "ds_write_b128 %[rw], v[92:95] offset:3072\n\t"
    "s_waitcnt lgkmcnt(8)\n\t"
    "v_mfma_f32_16x16x32_bf16 v[64:67], v[136:139], v[104:107], v[64:67]\n\t"
    "ds_read_b128 v[96:99], %[ea] offset:8192\n\t"
    "s_add_u32 m0, %[l1], 4096\n\t"
    "v_mfma_f32_16x16x32_bf16 v[68:71], v[140:143], v[104:107], v[68:71]\n\t"
    "ds_read_b128 v[100:103], %[ea] offset:10240\n\t"
    "global_load_lds_dwordx4 %[av2], s[36:37] sc1\n\t"
    "v_mfma_f32_16x16x32_bf16 v[72:75], v[136:139], v[108:111], v[72:75]\n\t"
    "v_mfma_f32_16x16x32_bf16 v[76:79], v[140:143], v[108:111], v[76:79]\n\t"
    "s_waitcnt lgkmcnt(8)\n\t"
    "v_mfma_f32_16x16x32_bf16 v[64:67], v[144:147], v[112:115], v[64:67]\n\t"
    "ds_read_b128 v[104:107], %[da] offset:8192\n\t"
    "s_add_u32 m0, %[l1], 8192\n\t"
    "v_mfma_f32_16x16x32_bf16 v[68:71], v[148:151], v[112:115], v[68:71]\n\t"
    "ds_read_b128 v[108:111], %[da] offset:10240\n\t"
    "global_load_lds_dwordx4 %[av4], s[36:37] sc1\n\t"
    "v_mfma_f32_16x16x32_bf16 v[72:75], v[144:147], v[116:119], v[72:75]\n\t"
    "v_mfma_f32_16x16x32_bf16 v[76:79], v[148:151], v[116:119], v[76:79]\n\t"
    "s_waitcnt lgkmcnt(7)\n\t"
    "v_mfma_f32_16x16x32_bf16 v[64:67], v[152:155], v[120:123], v[64:67]\n\t"
    "ds_read_b128 v[112:115], %[ea] offset:12288\n\t"
    "s_add_u32 m0, %[l1], 12288\n\t"
    "v_mfma_f32_16x16x32_bf16 v[68:71], v[156:159], v[120:123], v[68:71]\n\t"
    "ds_read_b128 v[116:119], %[ea] offset:14336\n\t"
    "global_load_lds_dwordx4 %[av6], s[36:37] sc1\n\t"
    "v_mfma_f32_16x16x32_bf16 v[72:75], v[152:155], v[124:127], v[72:75]\n\t"
    "v_mfma_f32_16x16x32_bf16 v[76:79], v[156:159], v[124:127], v[76:79]\n\t"
    "s_waitcnt lgkmcnt(4)\n\t"
    "v_mfma_f32_16x16x32_bf16 v[64:67], v[160:163], v[96:99], v[64:67]\n\t"
    "ds_read_b128 v[120:123], %[da] offset:12288\n\t"
    "v_mfma_f32_16x16x32_bf16 v[68:71], v[164:167], v[96:99], v[68:71]\n\t"
    "ds_read_b128 v[124:127], %[da] offset:14336\n\t"
    "v_mfma_f32_16x16x32_bf16 v[72:75], v[160:163], v[100:103], v[72:75]\n\t"
    "v_mfma_f32_16x16x32_bf16 v[76:79], v[164:167], v[100:103], v[76:79]\n\t"
    "s_waitcnt lgkmcnt(4)\n\t"
    "v_mfma_f32_16x16x32_bf16 v[64:67], v[168:171], v[104:107], v[64:67]\n\t"
    "ds_read_b128 v[96:99], %[ea] offset:16384\n\t"
    "v_mfma_f32_16x16x32_bf16 v[68:71], v[172:175], v[104:107], v[68:71]\n\t"
    "ds_read_b128 v[100:103], %[ea] offset:18432\n\t"
    "v_mfma_f32_16x16x32_bf16 v[72:75], v[168:171], v[108:111], v[72:75]\n\t"
    "v_mfma_f32_16x16x32_bf16 v[76:79], v[172:175], v[108:111], v[76:79]\n\t"
    "s_waitcnt lgkmcnt(4)\n\t"
    "v_mfma_f32_16x16x32_bf16 v[64:67], v[176:179], v[112:115], v[64:67]\n\t"
    "ds_read_b128 v[104:107], %[da] offset:16384\n\t"
    "v_mfma_f32_16x16x32_bf16 v[68:71], v[180:183], v[112:115], v[68:71]\n\t"
    "ds_read_b128 v[108:111], %[da] offset:18432\n\t"
    "v_mfma_f32_16x16x32_bf16 v[72:75], v[176:179], v[116:119], v[72:75]\n\t"
    "v_mfma_f32_16x16x32_bf16 v[76:79], v[180:183], v[116:119], v[76:79]\n\t"
    "s_waitcnt lgkmcnt(4)\n\t"
    "v_mfma_f32_16x16x32_bf16 v[64:67], v[184:187], v[120:123], v[64:67]\n\t"
    "ds_read_b128 v[112:115], %[ea] offset:20480\n\t"
    "v_mfma_f32_16x16x32_bf16 v[68:71], v[188:191], v[120:123], v[68:71]\n\t"
    "ds_read_b128 v[116:119], %[ea] offset:22528\n\t"
    "v_mfma_f32_16x16x32_bf16 v[72:75], v[184:187], v[124:127], v[72:75]\n\t"
    "v_mfma_f32_16x16x32_bf16 v[76:79], v[188:191], v[124:127], v[76:79]\n\t"
    "s_cmp_eq_u32 %[la], 3\n\t"
    "s_waitcnt vmcnt(6)\n\t"
    "s_cbranch_scc1 22f\n\t"
    "s_waitcnt vmcnt(2)\n\t"
    "22:\n\t"
    "s_barrier\n\t"
    "s_waitcnt lgkmcnt(4)\n\t"
    "v_mfma_f32_16x16x32_bf16 v[64:67], v[192:195], v[96:99], v[64:67]\n\t"
    "ds_read_b128 v[120:123], %[da] offset:20480\n\t"
    "s_add_u32 m0, %[l0], 0\n\t"
    "v_mfma_f32_16x16x32_bf16 v[68:71], v[196:199], v[96:99], v[68:71]\n\t"
    "ds_read_b128 v[124:127], %[da] offset:22528\n\t"
    "global_load_lds_dwordx4 %[av0], s[38:39] sc1\n\t"
    "v_mfma_f32_16x16x32_bf16 v[72:75], v[192:195], v[100:103], v[72:75]\n\t"
    "v_mfma_f32_16x16x32_bf16 v[76:79], v[196:199], v[100:103], v[76:79]\n\t"
    "s_waitcnt lgkmcnt(4)\n\t"
    "v_mfma_f32_16x16x32_bf16 v[64:67], v[200:203], v[104:107], v[64:67]\n\t"
    "ds_read_b128 v[96:99], %[ea] offset:24576\n\t"
    "s_add_u32 m0, %[l0], 4096\n\t"
    "v_mfma_f32_16x16x32_bf16 v[68:71], v[204:207], v[104:107], v[68:71]\n\t"
    "ds_read_b128 v[100:103], %[ea] offset:26624\n\t"
    "global_load_lds_dwordx4 %[av2], s[38:39] sc1\n\t"
    "v_mfma_f32_16x16x32_bf16 v[72:75], v[200:203], v[108:111], v[72:75]\n\t"
    "v_mfma_f32_16x16x32_bf16 v[76:79], v[204:207], v[108:111], v[76:79]\n\t"
    "s_add_u32 s36, s36, %[ab]\n\t"
    "s_addc_u32 s37, s37, 0\n\t"
    "s_waitcnt lgkmcnt(4)\n\t"
    "v_mfma_f32_16x16x32_bf16 v[64:67], v[208:211], v[112:115], v[64:67]\n\t"
    "ds_read_b128 v[104:107], %[da] offset:24576\n\t"
    "s_add_u32 m0, %[l0], 8192\n\t"
    "v_mfma_f32_16x16x32_bf16 v[68:71], v[212:215], v[112:115], v[68:71]\n\t"
    "ds_read_b128 v[108:111], %[da] offset:26624\n\t"
    "global_load_lds_dwordx4 %[av4], s[38:39] sc1\n\t"
    "v_mfma_f32_16x16x32_bf16 v[72:75], v[208:211], v[116:119], v[72:75]\n\t"
    "ds_read_b32 v39, %[sa]\n\t"
    "v_mfma_f32_16x16x32_bf16 v[76:79], v[212:215], v[116:119], v[76:79]\n\t"
    "s_xor_b32 %[l1], %[l1], 0x10000\n\t"
    "s_waitcnt lgkmcnt(5)\n\t"
    "v_mfma_f32_16x16x32_bf16 v[64:67], v[216:219], v[120:123], v[64:67]\n\t"
    "ds_read_b128 v[112:115], %[ea] offset:28672\n\t"
    "s_add_u32 m0, %[l0], 12288\n\t"
    "v_mfma_f32_16x16x32_bf16 v[68:71], v[220:223], v[120:123], v[68:71]\n\t"
    "ds_read_b128 v[116:119], %[ea] offset:30720\n\t"
    "global_load_lds_dwordx4 %[av6], s[38:39] sc1\n\t"
    "v_mfma_f32_16x16x32_bf16 v[72:75], v[216:219], v[124:127], v[72:75]\n\t"
    "v_xor_b32 %[ea], 0x10000, %[ea]\n\t"
    "v_mfma_f32_16x16x32_bf16 v[76:79], v[220:223], v[124:127], v[76:79]\n\t"
    "s_waitcnt lgkmcnt(5)\n\t"
    "v_mfma_f32_16x16x32_bf16 v[64:67], v[224:227], v[96:99], v[64:67]\n\t"
    "ds_read_b128 v[120:123], %[da] offset:28672\n\t"
    "v_mfma_f32_16x16x32_bf16 v[68:71], v[228:231], v[96:99], v[68:71]\n\t"
    "ds_read_b128 v[124:127], %[da] offset:30720\n\t"
    "v_mfma_f32_16x16x32_bf16 v[72:75], v[224:227], v[100:103], v[72:75]\n\t"
    "v_xor_b32 %[da], 0x10000, %[da]\n\t"
    "v_mfma_f32_16x16x32_bf16 v[76:79], v[228:231], v[100:103], v[76:79]\n\t"
    "s_waitcnt lgkmcnt(5)\n\t"
    "v_mfma_f32_16x16x32_bf16 v[64:67], v[232:235], v[104:107], v[64:67]\n\t"
    "ds_read_b128 v[96:99], %[ea] offset:0\n\t"
    "v_mfma_f32_16x16x32_bf16 v[68:71], v[236:239], v[104:107], v[68:71]\n\t"
    "ds_read_b128 v[100:103], %[ea] offset:2048\n\t"
    "v_mfma_f32_16x16x32_bf16 v[72:75], v[232:235], v[108:111], v[72:75]\n\t"
    "v_mfma_f32_16x16x32_bf16 v[76:79], v[236:239], v[108:111], v[76:79]\n\t"
    "s_waitcnt lgkmcnt(4)\n\t"
    "v_mfma_f32_16x16x32_bf16 v[64:67], v[240:243], v[112:115], v[64:67]\n\t"
    "ds_read_b128 v[104:107], %[da] offset:0\n\t"
    "v_mad_u32_u24 v39, v39, %[mu], %[ad]\n\t"
    "v_mfma_f32_16x16x32_bf16 v[68:71], v[244:247], v[112:115], v[68:71]\n\t"
    "ds_read_b128 v[108:111], %[da] offset:2048\n\t"
    "v_cmp_ge_i32 vcc, %[lf], v39\n\t"
    "s_cmp_eq_u64 vcc, 0\n\t"
    "s_addc_u32 %[lf], %[lf], 0\n\t"
    "v_mfma_f32_16x16x32_bf16 v[72:75], v[240:243], v[116:119], v[72:75]\n\t"
    "v_mfma_f32_16x16x32_bf16 v[76:79], v[244:247], v[116:119], v[76:79]\n\t"
    "v_cmp_ge_i32 vcc, %[lf], v39\n\t"
    "s_cmp_eq_u64 vcc, 0\n\t"
    "s_addc_u32 %[lf], %[lf], 0\n\t"
    "s_waitcnt lgkmcnt(4)\n\t"
    "v_mfma_f32_16x16x32_bf16 v[80:83], v[248:251], v[120:123], v[64:67]\n\t"
    "ds_read_b128 v[112:115], %[ea] offset:4096\n\t"
    "s_min_i32 %[lf], %[lf], %[cp]\n\t"
    "s_add_u32 %[b], %[b], 1\n\t"
    "v_mfma_f32_16x16x32_bf16 v[84:87], v[252:255], v[120:123], v[68:71]\n\t"
    "ds_read_b128 v[116:119], %[ea] offset:6144\n\t"
    "v_mfma_f32_16x16x32_bf16 v[88:91], v[248:251], v[124:127], v[72:75]\n\t"
    "v_mfma_f32_16x16x32_bf16 v[92:95], v[252:255], v[124:127], v[76:79]\n\t"
    "s_add_u32 s38, s38, %[ab]\n\t"
    "s_addc_u32 s39, s39, 0\n\t"
    "s_xor_b32 %[l0], %[l0], 0x10000\n\t"
    "s_sub_u32 m0, %[lf], %[b]\n\t"
    "s_sub_u32 m0, m0, 1\n\t"
    "s_cmp_lt_u32 m0, %[up]\n\t"
    "s_cbranch_scc1 1b\n\t"
    "s_waitcnt lgkmcnt(0)\n\t"
    "s_nop 4"
    : "=&{v[80:95]}"(pd), [ea] "+&v"(ea), [da] "+&v"(da), [b] "+&s"(b), [lf] "+&s"(lf), "+{s[36:37]}"(p1), "+{s[38:39]}"(p0), [sa] "+&v"(sa), [l1] "+&s"(l1), [l0] "+&s"(l0)
    : [rw] "v"(rw), [ab] "s"(ab), [cp] "s"(cp), [mu] "s"(mu), [ad] "v"(ad), [up] "s"(up), "{v[128:159]}"(W[0]), "{v[160:191]}"(W[1]), "{v[192:223]}"(W[2]), "{v[224:255]}"(W[3]), [av0] "v"(av[0]), [av1] "v"(av[1]), [av2] "v"(av[2]), [av3] "v"(av[3]), [av4] "v"(av[4]), [av5] "v"(av[5]), [av6] "v"(av[6]), [av7] "v"(av[7]), [la] "s"(la)
    : "v39", "v64", "v65", "v66", "v67", "v68", "v69", "v70", "v71", "v72", "v73", "v74", "v75", "v76", "v77", "v78", "v79", "v96", "v97", "v98", "v99", "v100", "v101", "v102", "v103", "v104", "v105", "v106", "v107", "v108", "v109", "v110", "v111", "v112", "v113", "v114", "v115", "v116", "v117", "v118", "v119", "v120", "v121", "v122", "v123", "v124", "v125", "v126", "v127", "vcc", "scc", "m0", "memory");
}
__device__ __forceinline__ void fast_st0(const u32x32 (&W)[4], u32x16& pd, unsigned ea, unsigned da, int& b, int& lf, int cp, unsigned up, unsigned sa, unsigned mu, unsigned ad,
    unsigned rw, unsigned ab, const char* p1, const char* p0, unsigned co0, unsigned co1, int po, int pf, unsigned w0, unsigned sl, unsigned dv, const char* db, const char* pg) {
  unsigned st;
  asm volatile("s_barrier\n\t"
    "ds_read_b128 v[96:99], %[ea] offset:0\n\t"
    "ds_read_b128 v[100:103], %[ea] offset:2048\n\t"
    "ds_read_b128 v[104:107], %[da] offset:0\n\t"
    "ds_read_b128 v[108:111], %[da] offset:2048\n\t"
    "ds_read_b128 v[112:115], %[ea] offset:4096\n\t"
    "ds_read_b128 v[116:119], %[ea] offset:6144\n\t"
    "s_branch 2f\n\t"
    ".p2align 6\n\t"
    "1:\n\t"
    "s_barrier\n\t"
    "2:\n\t"
    "s_waitcnt lgkmcnt(4)\n\t"
    "v_mfma_f32_16x16x32_bf16 v[64:67], v[128:131], v[96:99], 0\n\t"
    "ds_read_b128 v[120:123], %[da] offset:4096\n\t"
    "v_mfma_f32_16x16x32_bf16 v[68:71], v[132:135], v[96:99], 0\n\t"
    "ds_read_b128 v[124:127], %[da] offset:6144\n\t"
    "v_mfma_f32_16x16x32_bf16 v[72:75], v[128:131], v[100:103], 0\n\t"
    "v_mfma_f32_16x16x32_bf16 v[76:79], v[132:135], v[100:103], 0\n\t"
    "s_waitcnt lgkmcnt(4)\n\t"
    "v_mfma_f32_16x16x32_bf16 v[64:67], v[136:139], v[104:107], v[64:67]\n\t"
    "ds_read_b128 v[96:99], %[ea] offset:8192\n\t"
    "v_mfma_f32_16x16x32_bf16 v[68:71], v[140:143], v[104:107], v[68:71]\n\t"
    "ds_read_b128 v[100:103], %[ea] offset:10240\n\t"
    "v_mfma_f32_16x16x32_bf16 v[72:75], v[136:139], v[108:111], v[72:75]\n\t"
    "v_mfma_f32_16x16x32_bf16 v[76:79], v[140:143], v[108:111], v[76:79]\n\t"
    "s_cmp_eq_u32 %[w0], 1\n\t"
    "s_cbranch_scc1 7f\n\t"
    "8:\n\t"
    "s_waitcnt lgkmcnt(4)\n\t"
    "v_mfma_f32_16x16x32_bf16 v[64:67], v[144:147], v[112:115], v[64:67]\n\t"
    "ds_read_b128 v[104:107], %[da] offset:8192\n\t"
    "v_mfma_f32_16x16x32_bf16 v[68:71], v[148:151], v[112:115], v[68:71]\n\t"
    "ds_read_b128 v[108:111], %[da] offset:10240\n\t"
    "v_mfma_f32_16x16x32_bf16 v[72:75], v[144:147], v[116:119], v[72:75]\n\t"
    "v_mfma_f32_16x16x32_bf16 v[76:79], v[148:151], v[116:119], v[76:79]\n\t"
    "s_waitcnt lgkmcnt(4)\n\t"
    "v_mfma_f32_16x16x32_bf16 v[64:67], v[152:155], v[120:123], v[64:67]\n\t"
    "ds_read_b128 v[112:115], %[ea] offset:12288\n\t"
    "v_mfma_f32_16x16x32_bf16 v[68:71], v[156:159], v[120:123], v[68:71]\n\t"
    "ds_read_b128 v[116:119], %[ea] offset:14336\n\t"
    "v_mfma_f32_16x16x32_bf16 v[72:75], v[152:155], v[124:127], v[72:75]\n\t"
    "v_mfma_f32_16x16x32_bf16 v[76:79], v[156:159], v[124:127], v[76:79]\n\t"
    "s_waitcnt lgkmcnt(4)\n\t"
    "v_mfma_f32_16x16x32_bf16 v[64:67], v[160:163], v[96:99], v[64:67]\n\t"
    "ds_read_b128 v[120:123], %[da] offset:12288\n\t"
    "v_mfma_f32_16x16x32_bf16 v[68:71], v[164:167], v[96:99], v[68:71]\n\t"
    "ds_read_b128 v[124:127], %[da] offset:14336\n\t"
    "v_mfma_f32_16x16x32_bf16 v[72:75], v[160:163], v[100:103], v[72:75]\n\t"
    "v_mfma_f32_16x16x32_bf16 v[76:79], v[164:167], v[100:103], v[76:79]\n\t"
    "s_waitcnt lgkmcnt(4)\n\t"
    "v_mfma_f32_16x16x32_bf16 v[64:67], v[168:171], v[104:107], v[64:67]\n\t"
    "ds_read_b128 v[96:99], %[ea] offset:16384\n\t"
    "v_mfma_f32_16x16x32_bf16 v[68:71], v[172:175], v[104:107], v[68:71]\n\t"
    "ds_read_b128 v[100:103], %[ea] offset:18432\n\t"
    "v_mfma_f32_16x16x32_bf16 v[72:75], v[168:171], v[108:111], v[72:75]\n\t"
    "v_mfma_f32_16x16x32_bf16 v[76:79], v[172:175], v[108:111], v[76:79]\n\t"
    "s_waitcnt lgkmcnt(4)\n\t"
    "v_mfma_f32_16x16x32_bf16 v[64:67], v[176:179], v[112:115], v[64:67]\n\t"
    "ds_read_b128 v[104:107], %[da] offset:16384\n\t"
    "v_mfma_f32_16x16x32_bf16 v[68:71], v[180:183], v[112:115], v[68:71]\n\t"
    "ds_read_b128 v[108:111], %[da] offset:18432\n\t"
    "v_mfma_f32_16x16x32_bf16 v[72:75], v[176:179], v[116:119], v[72:75]\n\t"
    "v_mfma_f32_16x16x32_bf16 v[76:79], v[180:183], v[116:119], v[76:79]\n\t"
    "s_waitcnt lgkmcnt(4)\n\t"
    "v_mfma_f32_16x16x32_bf16 v[64:67], v[184:187], v[120:123], v[64:67]\n\t"
    "ds_read_b128 v[112:115], %[ea] offset:20480\n\t"
    "v_mfma_f32_16x16x32_bf16 v[68:71], v[188:191], v[120:123], v[68:71]\n\t"
    "ds_read_b128 v[116:119], %[ea] offset:22528\n\t"
    "v_mfma_f32_16x16x32_bf16 v[72:75], v[184:187], v[124:127], v[72:75]\n\t"
    "v_mfma_f32_16x16x32_bf16 v[76:79], v[188:191], v[124:127], v[76:79]\n\t"
    "s_waitcnt vmcnt(0)\n\t"
    "s_barrier\n\t"
    "s_waitcnt lgkmcnt(4)\n\t"
    "v_mfma_f32_16x16x32_bf16 v[64:67], v[192:195], v[96:99], v[64:67]\n\t"
    "ds_read_b128 v[120:123], %[da] offset:20480\n\t"
    "v_mfma_f32_16x16x32_bf16 v[68:71], v[196:199], v[96:99], v[68:71]\n\t"
    "ds_read_b128 v[124:127], %[da] offset:22528\n\t"
    "v_mfma_f32_16x16x32_bf16 v[72:75], v[192:195], v[100:103], v[72:75]\n\t"
    "ds_read_b128 v[48:51], %[rw] offset:0\n\t"
    "v_mfma_f32_16x16x32_bf16 v[76:79], v[196:199], v[100:103], v[76:79]\n\t"
    "ds_read_b128 v[52:55], %[rw] offset:1024\n\t"
    "s_cmp_eq_u32 %[w0], 0\n\t"
    "s_cbranch_scc1 5f\n\t"
    "6:\n\t"
    "s_waitcnt lgkmcnt(6)\n\t"
    "v_mfma_f32_16x16x32_bf16 v[64:67], v[200:203], v[104:107], v[64:67]\n\t"
    "ds_read_b128 v[96:99], %[ea] offset:24576\n\t"
    "v_mfma_f32_16x16x32_bf16 v[68:71], v[204:207], v[104:107], v[68:71]\n\t"
    "ds_read_b128 v[100:103], %[ea] offset:26624\n\t"
    "v_mfma_f32_16x16x32_bf16 v[72:75], v[200:203], v[108:111], v[72:75]\n\t"
    "ds_read_b128 v[56:59], %[rw] offset:2048\n\t"
    "v_mfma_f32_16x16x32_bf16 v[76:79], v[204:207], v[108:111], v[76:79]\n\t"
    "ds_read_b128 v[60:63], %[rw] offset:3072\n\t"
    "s_waitcnt lgkmcnt(0)\n\t"
    "v_mfma_f32_16x16x32_bf16 v[64:67], v[208:211], v[112:115], v[64:67]\n\t"
    "ds_read_b128 v[104:107], %[da] offset:24576\n\t"
    "v_add_f32 v48, v48, v80\n\t"
    "v_add_f32 v49, v49, v81\n\t"
    "v_mfma_f32_16x16x32_bf16 v[68:71], v[212:215], v[112:115], v[68:71]\n\t"
    "ds_read_b128 v[108:111], %[da] offset:26624\n\t"
    "v_add_f32 v50, v50, v82\n\t"
    "v_add_f32 v51, v51, v83\n\t"
    "v_mfma_f32_16x16x32_bf16 v[72:75], v[208:211], v[116:119], v[72:75]\n\t"
    "ds_read_b32 v39, %[sa]\n\t"
    "v_add_f32 v52, v52, v84\n\t"
    "v_mfma_f32_16x16x32_bf16 v[76:79], v[212:215], v[116:119], v[76:79]\n\t"
    "v_add_f32 v53, v53, v85\n\t"
    "v_add_f32 v54, v54, v86\n\t"
    "v_mfma_f32_16x16x32_bf16 v[64:67], v[216:219], v[120:123], v[64:67]\n\t"
    "ds_read_b128 v[112:115], %[ea] offset:28672\n\t"
    "v_add_f32 v55, v55, v87\n\t"
    "v_add_f32 v56, v56, v88\n\t"
    "v_mfma_f32_16x16x32_bf16 v[68:71], v[220:223], v[120:123], v[68:71]\n\t"
    "ds_read_b128 v[116:119], %[ea] offset:30720\n\t"
    "v_add_f32 v57, v57, v89\n\t"
    "v_mfma_f32_16x16x32_bf16 v[72:75], v[216:219], v[124:127], v[72:75]\n\t"
    "v_xor_b32 %[ea], 0x10000, %[ea]\n\t"
    "v_add_f32 v58, v58, v90\n\t"
    "v_add_f32 v59, v59, v91\n\t"
    "v_mfma_f32_16x16x32_bf16 v[76:79], v[220:223], v[124:127], v[76:79]\n\t"
    "v_add_f32 v60, v60, v92\n\t"
    "v_mfma_f32_16x16x32_bf16 v[64:67], v[224:227], v[96:99], v[64:67]\n\t"
    "ds_read_b128 v[120:123], %[da] offset:28672\n\t"
    "v_add_f32 v61, v61, v93\n\t"
    "v_add_f32 v62, v62, v94\n\t"
    "v_mfma_f32_16x16x32_bf16 v[68:71], v[228:231], v[96:99], v[68:71]\n\t"
    "ds_read_b128 v[124:127], %[da] offset:30720\n\t"
    "v_add_f32 v63, v63, v95\n\t"
    "v_cvt_pk_bf16_f32 v40, v48, v49\n\t"
    "v_mfma_f32_16x16x32_bf16 v[72:75], v[224:227], v[100:103], v[72:75]\n\t"
    "v_xor_b32 %[da], 0x10000, %[da]\n\t"
    "v_cvt_pk_bf16_f32 v41, v50, v51\n\t"
    "v_mfma_f32_16x16x32_bf16 v[76:79], v[228:231], v[100:103], v[76:79]\n\t"
    "v_cvt_pk_bf16_f32 v42, v52, v53\n\t"
    "v_cvt_pk_bf16_f32 v43, v54, v55\n\t"
    "s_waitcnt lgkmcnt(5)\n\t"
    "v_mfma_f32_16x16x32_bf16 v[64:67], v[232:235], v[104:107], v[64:67]\n\t"
    "ds_read_b128 v[96:99], %[ea] offset:0\n\t"
    "global_store_dwordx4 %[co0], v[40:43], s[36:37] sc1\n\t"
    "v_cvt_pk_bf16_f32 v44, v56, v57\n\t"
    "v_mfma_f32_16x16x32_bf16 v[68:71], v[236:239], v[104:107], v[68:71]\n\t"
    "ds_read_b128 v[100:103], %[ea] offset:2048\n\t"
    "v_cvt_pk_bf16_f32 v45, v58, v59\n\t"
    "v_mfma_f32_16x16x32_bf16 v[72:75], v[232:235], v[108:111], v[72:75]\n\t"
    "v_cvt_pk_bf16_f32 v46, v60, v61\n\t"
    "v_cvt_pk_bf16_f32 v47, v62, v63\n\t"
    "v_mfma_f32_16x16x32_bf16 v[76:79], v[236:239], v[108:111], v[76:79]\n\t"
    "global_store_dwordx4 %[co1], v[44:47], s[36:37] sc1\n\t"
    "s_waitcnt lgkmcnt(4)\n\t"
    "v_mfma_f32_16x16x32_bf16 v[64:67], v[240:243], v[112:115], v[64:67]\n\t"
    "ds_read_b128 v[104:107], %[da] offset:0\n\t"
    "v_mad_u32_u24 v39, v39, %[mu], %[ad]\n\t"
    "v_mfma_f32_16x16x32_bf16 v[68:71], v[244:247], v[112:115], v[68:71]\n\t"
    "ds_read_b128 v[108:111], %[da] offset:2048\n\t"
    "v_cmp_ge_i32 vcc, %[lf], v39\n\t"
    "s_cmp_eq_u64 vcc, 0\n\t"
    "s_addc_u32 %[lf], %[lf], 0\n\t"
    "v_mfma_f32_16x16x32_bf16 v[72:75], v[240:243], v[116:119], v[72:75]\n\t"
    "v_mfma_f32_16x16x32_bf16 v[76:79], v[244:247], v[116:119], v[76:79]\n\t"
    "v_cmp_ge_i32 vcc, %[lf], v39\n\t"
    "s_cmp_eq_u64 vcc, 0\n\t"
    "s_addc_u32 %[lf], %[lf], 0\n\t"
    "s_waitcnt lgkmcnt(4)\n\t"
    "v_mfma_f32_16x16x32_bf16 v[80:83], v[248:251], v[120:123], v[64:67]\n\t"
    "ds_read_b128 v[112:115], %[ea] offset:4096\n\t"
    "s_min_i32 %[lf], %[lf], %[cp]\n\t"
    "s_add_u32 %[b], %[b], 1\n\t"
    "v_mfma_f32_16x16x32_bf16 v[84:87], v[252:255], v[120:123], v[68:71]\n\t"
    "ds_read_b128 v[116:119], %[ea] offset:6144\n\t"
    "v_mfma_f32_16x16x32_bf16 v[88:91], v[248:251], v[124:127], v[72:75]\n\t"
    "v_mfma_f32_16x16x32_bf16 v[92:95], v[252:255], v[124:127], v[76:79]\n\t"
    "s_mov_b64 s[36:37], s[38:39]\n\t"
    "s_add_u32 s38, s38, %[ab]\n\t"
    "s_addc_u32 s39, s39, 0\n\t"
    "s_sub_u32 m0, %[lf], %[b]\n\t"
    "s_sub_u32 m0, m0, 1\n\t"
    "s_cmp_lt_u32 m0, %[up]\n\t"
    "s_cbranch_scc1 1b\n\t"
    "s_waitcnt lgkmcnt(0)\n\t"
    "s_nop 4\n\t"
    "s_branch 9f\n\t"
    "5:\n\t"
    "s_add_u32 %[st], %[b], %[po]\n\t"
    "s_max_i32 %[st], %[st], %[p0]\n\t"
    "v_mov_b32 v39, %[st]\n\t"
    "v_mov_b32 v38, 0\n\t"
    "s_mov_b64 exec, 1\n\t"
    "global_store_dword v38, v39, %[pg] sc1\n\t"
    "s_mov_b64 exec, -1\n\t"
    "s_mov_b32 %[p0], %[p1]\n\t"
    "s_branch 6b\n\t"
    "7:\n\t"
    "s_cmp_ge_i32 %[lf], %[cp]\n\t"
    "s_cbranch_scc1 8b\n\t"
    "s_mov_b32 m0, %[sl]\n\t"
    "s_nop 0\n\t"
    "global_load_lds_dword %[dv], %[db] sc1\n\t"
    "s_branch 8b\n\t"
    "9:"
    : "=&{v[80:95]}"(pd), [ea] "+&v"(ea), [da] "+&v"(da), [b] "+&s"(b), [lf] "+&s"(lf), "+{s[36:37]}"(p1), "+{s[38:39]}"(p0), [sa] "+&v"(sa), [st] "=&s"(st), [sl] "+&s"(sl), [p0] "+&s"(pf)
    : [rw] "v"(rw), [ab] "s"(ab), [cp] "s"(cp), [mu] "s"(mu), [ad] "v"(ad), [up] "s"(up), "{v[128:159]}"(W[0]), "{v[160:191]}"(W[1]), "{v[192:223]}"(W[2]), "{v[224:255]}"(W[3]), [co0] "v"(co0), [w0] "s"(w0), [dv] "v"(dv), [db] "s"(db), [pg] "s"(pg), [po] "s"(po - 1), [p1] "s"(b + po), [co1] "v"(co1)
    : "v38", "v39", "v40", "v41", "v42", "v43", "v44", "v45", "v46", "v47", "v48", "v49", "v50", "v51", "v52", "v53", "v54", "v55", "v56", "v57", "v58", "v59", "v60", "v61", "v62", "v63", "v64", "v65", "v66", "v67", "v68", "v69", "v70", "v71", "v72", "v73", "v74", "v75", "v76", "v77", "v78", "v79", "v96", "v97", "v98", "v99", "v100", "v101", "v102", "v103", "v104", "v105", "v106", "v107", "v108", "v109", "v110", "v111", "v112", "v113", "v114", "v115", "v116", "v117", "v118", "v119", "v120", "v121", "v122", "v123", "v124", "v125", "v126", "v127", "vcc", "scc", "m0", "memory");
}
__device__ __forceinline__ void fast_st1(const u32x32 (&W)[4], u32x16& pd, unsigned ea, unsigned da, int& b, int& lf, int cp, unsigned up, unsigned sa, unsigned mu, unsigned ad,
    unsigned rw, unsigned ab, const char* p1, const char* p0, unsigned co0, unsigned co1, int po, int pf, unsigned w0, unsigned sl, unsigned dv, const char* db, const char* pg) {
  unsigned st;
  asm volatile("s_barrier\n\t"
    "ds_read_b128 v[96:99], %[ea] offset:0\n\t"
    "ds_read_b128 v[100:103], %[ea] offset:2048\n\t"
    "ds_read_b128 v[104:107], %[da] offset:0\n\t"
    "ds_read_b128 v[108:111], %[da] offset:2048\n\t"
    "ds_read_b128 v[112:115], %[ea] offset:4096\n\t"
    "ds_read_b128 v[116:119], %[ea] offset:6144\n\t"
    "s_branch 2f\n\t"
    ".p2align 6\n\t"
    "1:\n\t"
    "s_barrier\n\t"
    "2:\n\t"
    "s_waitcnt lgkmcnt(4)\n\t"
    "v_mfma_f32_16x16x32_bf16 v[64:67], v[128:131], v[96:99], 0\n\t"
    "ds_read_b128 v[120:123], %[da] offset:4096\n\t"
    "v_mfma_f32_16x16x32_bf16 v[68:71], v[132:135], v[96:99], 0\n\t"
    "ds_read_b128 v[124:127], %[da] offset:6144\n\t"
    "v_mfma_f32_16x16x32_bf16 v[72:75], v[128:131], v[100:103], 0\n\t"
    "v_mfma_f32_16x16x32_bf16 v[76:79], v[132:135], v[100:103], 0\n\t"
    "s_waitcnt lgkmcnt(4)\n\t"
    "v_mfma_f32_16x16x32_bf16 v[64:67], v[136:139], v[104:107], v[64:67]\n\t"
    "ds_read_b128 v[96:99], %[ea] offset:8192\n\t"
    "v_mfma_f32_16x16x32_bf16 v[68:71], v[140:143], v[104:107], v[68:71]\n\t"
    "ds_read_b128 v[100:103], %[ea] offset:10240\n\t"
    "v_mfma_f32_16x16x32_bf16 v[72:75], v[136:139], v[108:111], v[72:75]\n\t"
    "v_mfma_f32_16x16x32_bf16 v[76:79], v[140:143], v[108:111], v[76:79]\n\t"
    "s_cmp_eq_u32 %[w0], 1\n\t"
    "s_cbranch_scc1 7f\n\t"
    "8:\n\t"
    "s_waitcnt lgkmcnt(4)\n\t"
    "v_mfma_f32_16x16x32_bf16 v[64:67], v[144:147], v[112:115], v[64:67]\n\t"
    "ds_read_b128 v[104:107], %[da] offset:8192\n\t"
    "v_mfma_f32_16x16x32_bf16 v[68:71], v[148:151], v[112:115], v[68:71]\n\t"
    "ds_read_b128 v[108:111], %[da] offset:10240\n\t"
    "v_mfma_f32_16x16x32_bf16 v[72:75], v[144:147], v[116:119], v[72:75]\n\t"
    "v_mfma_f32_16x16x32_bf16 v[76:79], v[148:151], v[116:119], v[76:79]\n\t"
    "s_waitcnt lgkmcnt(4)\n\t"
    "v_mfma_f32_16x16x32_bf16 v[64:67], v[152:155], v[120:123], v[64:67]\n\t"
    "ds_read_b128 v[112:115], %[ea] offset:12288\n\t"
    "v_mfma_f32_16x16x32_bf16 v[68:71], v[156:159], v[120:123], v[68:71]\n\t"
    "ds_read_b128 v[116:119], %[ea] offset:14336\n\t"
    "v_mfma_f32_16x16x32_bf16 v[72:75], v[152:155], v[124:127], v[72:75]\n\t"
    "v_mfma_f32_16x16x32_bf16 v[76:79], v[156:159], v[124:127], v[76:79]\n\t"
    "s_waitcnt lgkmcnt(4)\n\t"
    "v_mfma_f32_16x16x32_bf16 v[64:67], v[160:163], v[96:99], v[64:67]\n\t"
    "ds_read_b128 v[120:123], %[da] offset:12288\n\t"
    "v_mfma_f32_16x16x32_bf16 v[68:71], v[164:167], v[96:99], v[68:71]\n\t"
    "ds_read_b128 v[124:127], %[da] offset:14336\n\t"
    "v_mfma_f32_16x16x32_bf16 v[72:75], v[160:163], v[100:103], v[72:75]\n\t"
    "v_mfma_f32_16x16x32_bf16 v[76:79], v[164:167], v[100:103], v[76:79]\n\t"
    "s_waitcnt lgkmcnt(4)\n\t"
    "v_mfma_f32_16x16x32_bf16 v[64:67], v[168:171], v[104:107], v[64:67]\n\t"
    "ds_read_b128 v[96:99], %[ea] offset:16384\n\t"
    "v_mfma_f32_16x16x32_bf16 v[68:71], v[172:175], v[104:107], v[68:71]\n\t"
    "ds_read_b128 v[100:103], %[ea] offset:18432\n\t"
    "v_mfma_f32_16x16x32_bf16 v[72:75], v[168:171], v[108:111], v[72:75]\n\t"
    "v_mfma_f32_16x16x32_bf16 v[76:79], v[172:175], v[108:111], v[76:79]\n\t"
    "s_waitcnt lgkmcnt(4)\n\t"
    "v_mfma_f32_16x16x32_bf16 v[64:67], v[176:179], v[112:115], v[64:67]\n\t"
    "ds_read_b128 v[104:107], %[da] offset:16384\n\t"
    "v_mfma_f32_16x16x32_bf16 v[68:71], v[180:183], v[112:115], v[68:71]\n\t"
    "ds_read_b128 v[108:111], %[da] offset:18432\n\t"
    "v_mfma_f32_16x16x32_bf16 v[72:75], v[176:179], v[116:119], v[72:75]\n\t"
    "v_mfma_f32_16x16x32_bf16 v[76:79], v[180:183], v[116:119], v[76:79]\n\t"
    "s_waitcnt lgkmcnt(4)\n\t"
    "v_mfma_f32_16x16x32_bf16 v[64:67], v[184:187], v[120:123], v[64:67]\n\t"
    "ds_read_b128 v[112:115], %[ea] offset:20480\n\t"
    "v_mfma_f32_16x16x32_bf16 v[68:71], v[188:191], v[120:123], v[68:71]\n\t"
    "ds_read_b128 v[116:119], %[ea] offset:22528\n\t"
    "v_mfma_f32_16x16x32_bf16 v[72:75], v[184:187], v[124:127], v[72:75]\n\t"
    "v_mfma_f32_16x16x32_bf16 v[76:79], v[188:191], v[124:127], v[76:79]\n\t"
    "s_waitcnt vmcnt(0)\n\t"
    "s_barrier\n\t"
    "s_waitcnt lgkmcnt(4)\n\t"
    "v_mfma_f32_16x16x32_bf16 v[64:67], v[192:195], v[96:99], v[64:67]\n\t"
    "ds_read_b128 v[120:123], %[da] offset:20480\n\t"
    "v_mfma_f32_16x16x32_bf16 v[68:71], v[196:199], v[96:99], v[68:71]\n\t"
    "ds_read_b128 v[124:127], %[da] offset:22528\n\t"
    "v_mfma_f32_16x16x32_bf16 v[72:75], v[192:195], v[100:103], v[72:75]\n\t"
    "ds_read_b128 v[48:51], %[rw] offset:0\n\t"
    "v_mfma_f32_16x16x32_bf16 v[76:79], v[196:199], v[100:103], v[76:79]\n\t"
    "ds_read_b128 v[52:55], %[rw] offset:1024\n\t"
    "s_cmp_eq_u32 %[w0], 0\n\t"
    "s_cbranch_scc1 5f\n\t"
    "6:\n\t"
    "s_waitcnt lgkmcnt(6)\n\t"
    "v_mfma_f32_16x16x32_bf16 v[64:67], v[200:203], v[104:107], v[64:67]\n\t"
    "ds_read_b128 v[96:99], %[ea] offset:24576\n\t"
    "v_mfma_f32_16x16x32_bf16 v[68:71], v[204:207], v[104:107], v[68:71]\n\t"
    "ds_read_b128 v[100:103], %[ea] offset:26624\n\t"
    "v_mfma_f32_16x16x32_bf16 v[72:75], v[200:203], v[108:111], v[72:75]\n\t"
    "ds_read_b128 v[56:59], %[rw] offset:2048\n\t"
    "v_mfma_f32_16x16x32_bf16 v[76:79], v[204:207], v[108:111], v[76:79]\n\t"
    "ds_read_b128 v[60:63], %[rw] offset:3072\n\t"
    "s_waitcnt lgkmcnt(0)\n\t"
    "v_mfma_f32_16x16x32_bf16 v[64:67], v[208:211], v[112:115], v[64:67]\n\t"
    "ds_read_b128 v[104:107], %[da] offset:24576\n\t"
    "v_add_f32 v48, v48, v80\n\t"
    "v_add_f32 v49, v49, v81\n\t"
    "v_add_f32 v50, v50, v82\n\t"
    "v_mfma_f32_16x16x32_bf16 v[68:71], v[212:215], v[112:115], v[68:71]\n\t"
    "ds_read_b128 v[108:111], %[da] offset:26624\n\t"
    "v_add_f32 v51, v51, v83\n\t"
    "v_add_f32 v52, v52, v84\n\t"
    "v_add_f32 v53, v53, v85\n\t"
    "v_mfma_f32_16x16x32_bf16 v[72:75], v[208:211], v[116:119], v[72:75]\n\t"
    "ds_read_b32 v39, %[sa]\n\t"
    "v_add_f32 v54, v54, v86\n\t"
    "v_add_f32 v55, v55, v87\n\t"
    "v_add_f32 v56, v56, v88\n\t"
    "v_mfma_f32_16x16x32_bf16 v[76:79], v[212:215], v[116:119], v[76:79]\n\t"
    "v_add_f32 v57, v57, v89\n\t"
    "v_add_f32 v58, v58, v90\n\t"
    "v_add_f32 v59, v59, v91\n\t"
    "v_mfma_f32_16x16x32_bf16 v[64:67], v[216:219], v[120:123], v[64:67]\n\t"
    "ds_read_b128 v[112:115], %[ea] offset:28672\n\t"
    "v_add_f32 v60, v60, v92\n\t"
    "v_add_f32 v61, v61, v93\n\t"
    "v_add_f32 v62, v62, v94\n\t"
    "v_mfma_f32_16x16x32_bf16 v[68:71], v[220:223], v[120:123], v[68:71]\n\t"
    "ds_read_b128 v[116:119], %[ea] offset:30720\n\t"
    "v_add_f32 v63, v63, v95\n\t"
    "v_mul_f32 v40, %[nl], v48\n\t"
    "v_mul_f32 v41, %[nl], v49\n\t"
    "v_mfma_f32_16x16x32_bf16 v[72:75], v[216:219], v[124:127], v[72:75]\n\t"
    "v_xor_b32 %[ea], 0x10000, %[ea]\n\t"
    "v_mul_f32 v42, %[nl], v50\n\t"
    "v_mul_f32 v43, %[nl], v51\n\t"
    "v_mul_f32 v44, %[nl], v56\n\t"
    "v_mfma_f32_16x16x32_bf16 v[76:79], v[220:223], v[124:127], v[76:79]\n\t"
    "v_mul_f32 v45, %[nl], v57\n\t"
    "v_mul_f32 v46, %[nl], v58\n\t"
    "v_mul_f32 v47, %[nl], v59\n\t"
    "v_mfma_f32_16x16x32_bf16 v[64:67], v[224:227], v[96:99], v[64:67]\n\t"
    "ds_read_b128 v[120:123], %[da] offset:28672\n\t"
    "v_exp_f32 v40, v40\n\t"
    "v_exp_f32 v41, v41\n\t"
    "v_exp_f32 v42, v42\n\t"
    "v_mfma_f32_16x16x32_bf16 v[68:71], v[228:231], v[96:99], v[68:71]\n\t"
    "ds_read_b128 v[124:127], %[da] offset:30720\n\t"
    "v_exp_f32 v43, v43\n\t"
    "v_exp_f32 v44, v44\n\t"
    "v_exp_f32 v45, v45\n\t"
    "v_mfma_f32_16x16x32_bf16 v[72:75], v[224:227], v[100:103], v[72:75]\n\t"
    "v_xor_b32 %[da], 0x10000, %[da]\n\t"
    "v_exp_f32 v46, v46\n\t"
    "v_exp_f32 v47, v47\n\t"
    "v_add_f32 v40, 1.0, v40\n\t"
    "v_mfma_f32_16x16x32_bf16 v[76:79], v[228:231], v[100:103], v[76:79]\n\t"
    "v_add_f32 v41, 1.0, v41\n\t"
    "v_add_f32 v42, 1.0, v42\n\t"
    "v_add_f32 v43, 1.0, v43\n\t"
    "s_waitcnt lgkmcnt(5)\n\t"
    "v_mfma_f32_16x16x32_bf16 v[64:67], v[232:235], v[104:107], v[64:67]\n\t"
    "ds_read_b128 v[96:99], %[ea] offset:0\n\t"
    "v_add_f32 v44, 1.0, v44\n\t"
    "v_add_f32 v45, 1.0, v45\n\t"
    "v_add_f32 v46, 1.0, v46\n\t"
    "v_mfma_f32_16x16x32_bf16 v[68:71], v[236:239], v[104:107], v[68:71]\n\t"
    "ds_read_b128 v[100:103], %[ea] offset:2048\n\t"
    "v_add_f32 v47, 1.0, v47\n\t"
    "v_rcp_f32 v40, v40\n\t"
    "v_rcp_f32 v41, v41\n\t"
    "v_mfma_f32_16x16x32_bf16 v[72:75], v[232:235], v[108:111], v[72:75]\n\t"
    "v_rcp_f32 v42, v42\n\t"
    "v_rcp_f32 v43, v43\n\t"
    "v_rcp_f32 v44, v44\n\t"
    "v_mfma_f32_16x16x32_bf16 v[76:79], v[236:239], v[108:111], v[76:79]\n\t"
    "v_rcp_f32 v45, v45\n\t"
    "v_rcp_f32 v46, v46\n\t"
    "v_rcp_f32 v47, v47\n\t"
    "s_waitcnt lgkmcnt(4)\n\t"
    "v_mfma_f32_16x16x32_bf16 v[64:67], v[240:243], v[112:115], v[64:67]\n\t"
    "ds_read_b128 v[104:107], %[da] offset:0\n\t"
    "v_mad_u32_u24 v39, v39, %[mu], %[ad]\n\t"
    "v_mul_f32 v40, v48, v40\n\t"
    "v_mul_f32 v41, v49, v41\n\t"
    "v_mul_f32 v42, v50, v42\n\t"
    "v_mfma_f32_16x16x32_bf16 v[68:71], v[244:247], v[112:115], v[68:71]\n\t"
    "ds_read_b128 v[108:111], %[da] offset:2048\n\t"
    "v_cmp_ge_i32 vcc, %[lf], v39\n\t"
    "s_cmp_eq_u64 vcc, 0\n\t"
    "s_addc_u32 %[lf], %[lf], 0\n\t"
    "v_mul_f32 v43, v51, v43\n\t"
    "v_mul_f32 v44, v56, v44\n\t"
    "v_mul_f32 v45, v57, v45\n\t"
    "v_mfma_f32_16x16x32_bf16 v[72:75], v[240:243], v[116:119], v[72:75]\n\t"
    "v_mul_f32 v46, v58, v46\n\t"
    "v_mul_f32 v47, v59, v47\n\t"
    "v_mul_f32 v40, v40, v52\n\t"
    "v_mfma_f32_16x16x32_bf16 v[76:79], v[244:247], v[116:119], v[76:79]\n\t"
    "v_cmp_ge_i32 vcc, %[lf], v39\n\t"
    "s_cmp_eq_u64 vcc, 0\n\t"
    "s_addc_u32 %[lf], %[lf], 0\n\t"
    "v_mul_f32 v41, v41, v53\n\t"
    "v_mul_f32 v42, v42, v54\n\t"
    "v_mul_f32 v43, v43, v55\n\t"
    "s_waitcnt lgkmcnt(4)\n\t"
    "v_mfma_f32_16x16x32_bf16 v[80:83], v[248:251], v[120:123], v[64:67]\n\t"
    "ds_read_b128 v[112:115], %[ea] offset:4096\n\t"
    "s_min_i32 %[lf], %[lf], %[cp]\n\t"
    "s_add_u32 %[b], %[b], 1\n\t"
    "v_mul_f32 v44, v44, v60\n\t"
    "v_mul_f32 v45, v45, v61\n\t"
    "v_mul_f32 v46, v46, v62\n\t"
    "v_mfma_f32_16x16x32_bf16 v[84:87], v[252:255], v[120:123], v[68:71]\n\t"
    "ds_read_b128 v[116:119], %[ea] offset:6144\n\t"
    "v_mul_f32 v47, v47, v63\n\t"
    "v_cvt_pk_bf16_f32 v48, v40, v41\n\t"
    "v_cvt_pk_bf16_f32 v49, v42, v43\n\t"
    "v_mfma_f32_16x16x32_bf16 v[88:91], v[248:251], v[124:127], v[72:75]\n\t"
    "v_cvt_pk_bf16_f32 v50, v44, v45\n\t"
    "v_cvt_pk_bf16_f32 v51, v46, v47\n\t"
    "s_nop 1\n\t"
    "v_mfma_f32_16x16x32_bf16 v[92:95], v[252:255], v[124:127], v[76:79]\n\t"
    "v_permlane16_swap_b32 v48, v50\n\t"
    "v_permlane16_swap_b32 v49, v51\n\t"
    "global_store_dwordx4 %[co0], v[48:51], s[36:37] sc1\n\t"
    "s_mov_b64 s[36:37], s[38:39]\n\t"
    "s_add_u32 s38, s38, %[ab]\n\t"
    "s_addc_u32 s39, s39, 0\n\t"
    "s_sub_u32 m0, %[lf], %[b]\n\t"
    "s_sub_u32 m0, m0, 1\n\t"
    "s_cmp_lt_u32 m0, %[up]\n\t"
    "s_cbranch_scc1 1b\n\t"
    "s_waitcnt lgkmcnt(0)\n\t"
    "s_nop 4\n\t"
    "s_branch 9f\n\t"
    "5:\n\t"
    "s_add_u32 %[st], %[b], %[po]\n\t"
    "s_max_i32 %[st], %[st], %[p0]\n\t"
    "v_mov_b32 v39, %[st]\n\t"
    "v_mov_b32 v38, 0\n\t"
    "s_mov_b64 exec, 1\n\t"
    "global_store_dword v38, v39, %[pg] sc1\n\t"
    "s_mov_b64 exec, -1\n\t"
    "s_mov_b32 %[p0], %[p1]\n\t"
    "s_branch 6b\n\t"
    "7:\n\t"
    "s_cmp_ge_i32 %[lf], %[cp]\n\t"
    "s_cbranch_scc1 8b\n\t"
    "s_mov_b32 m0, %[sl]\n\t"
    "s_nop 0\n\t"
    "global_load_lds_dword %[dv], %[db] sc1\n\t"
    "s_branch 8b\n\t"
    "9:"
    : "=&{v[80:95]}"(pd), [ea] "+&v"(ea), [da] "+&v"(da), [b] "+&s"(b), [lf] "+&s"(lf), "+{s[36:37]}"(p1), "+{s[38:39]}"(p0), [sa] "+&v"(sa), [st] "=&s"(st), [sl] "+&s"(sl), [p0] "+&s"(pf)
    : [rw] "v"(rw), [ab] "s"(ab), [cp] "s"(cp), [mu] "s"(mu), [ad] "v"(ad), [up] "s"(up), "{v[128:159]}"(W[0]), "{v[160:191]}"(W[1]), "{v[192:223]}"(W[2]), "{v[224:255]}"(W[3]), [co0] "v"(co0), [w0] "s"(w0), [dv] "v"(dv), [db] "s"(db), [pg] "s"(pg), [po] "s"(po - 1), [p1] "s"(b + po), [nl] "s"(-LOG2E)
    : "v38", "v39", "v40", "v41", "v42", "v43", "v44", "v45", "v46", "v47", "v48", "v49", "v50", "v51", "v52", "v53", "v54", "v55", "v56", "v57", "v58", "v59", "v60", "v61", "v62", "v63", "v64", "v65", "v66", "v67", "v68", "v69", "v70", "v71", "v72", "v73", "v74", "v75", "v76", "v77", "v78", "v79", "v96", "v97", "v98", "v99", "v100", "v101", "v102", "v103", "v104", "v105", "v106", "v107", "v108", "v109", "v110", "v111", "v112", "v113", "v114", "v115", "v116", "v117", "v118", "v119", "v120", "v121", "v122", "v123", "v124", "v125", "v126", "v127", "vcc", "scc", "m0", "memory");
}
__device__ __forceinline__ void fast_st2(const u32x32 (&W)[4], u32x16& pd, unsigned ea, unsigned da, int& b, int& lf, int cp, unsigned up, unsigned sa, unsigned mu, unsigned ad,
    unsigned rw, unsigned ab, const char* p1, const char* p0, unsigned co0, unsigned co1, int po, int pf, unsigned w0, unsigned sl, unsigned dv, const char* db, const char* pg) {
  unsigned st;
  asm volatile("s_barrier\n\t"
    "ds_read_b128 v[96:99], %[ea] offset:0\n\t"
    "ds_read_b128 v[100:103], %[ea] offset:2048\n\t"
    "ds_read_b128 v[104:107], %[da] offset:0\n\t"
    "ds_read_b128 v[108:111], %[da] offset:2048\n\t"
    "ds_read_b128 v[112:115], %[ea] offset:4096\n\t"
    "ds_read_b128 v[116:119], %[ea] offset:6144\n\t"
    "s_branch 2f\n\t"
    ".p2align 6\n\t"
    "1:\n\t"
    "s_barrier\n\t"
    "2:\n\t"
    "s_waitcnt lgkmcnt(4)\n\t"
    "v_mfma_f32_16x16x32_bf16 v[64:67], v[128:131], v[96:99], 0\n\t"
    "ds_read_b128 v[120:123], %[da] offset:4096\n\t"
    "v_mfma_f32_16x16x32_bf16 v[68:71], v[132:135], v[96:99], 0\n\t"
    "ds_read_b128 v[124:127], %[da] offset:6144\n\t"
    "v_mfma_f32_16x16x32_bf16 v[72:75], v[128:131], v[100:103], 0\n\t"
    "v_mfma_f32_16x16x32_bf16 v[76:79], v[132:135], v[100:103], 0\n\t"
    "s_waitcnt lgkmcnt(4)\n\t"
    "v_mfma_f32_16x16x32_bf16 v[64:67], v[136:139], v[104:107], v[64:67]\n\t"
    "ds_read_b128 v[96:99], %[ea] offset:8192\n\t"
    "v_mfma_f32_16x16x32_bf16 v[68:71], v[140:143], v[104:107], v[68:71]\n\t"
    "ds_read_b128 v[100:103], %[ea] offset:10240\n\t"
    "v_mfma_f32_16x16x32_bf16 v[72:75], v[136:139], v[108:111], v[72:75]\n\t"
    "v_mfma_f32_16x16x32_bf16 v[76:79], v[140:143], v[108:111], v[76:79]\n\t"
    "s_cmp_eq_u32 %[w0], 1\n\t"
    "s_cbranch_scc1 7f\n\t"
    "8:\n\t"
    "s_waitcnt lgkmcnt(4)\n\t"
    "v_mfma_f32_16x16x32_bf16 v[64:67], v[144:147], v[112:115], v[64:67]\n\t"
    "ds_read_b128 v[104:107], %[da] offset:8192\n\t"
    "v_mfma_f32_16x16x32_bf16 v[68:71], v[148:151], v[112:115], v[68:71]\n\t"
    "ds_read_b128 v[108:111], %[da] offset:10240\n\t"
    "v_mfma_f32_16x16x32_bf16 v[72:75], v[144:147], v[116:119], v[72:75]\n\t"
    "v_mfma_f32_16x16x32_bf16 v[76:79], v[148:151], v[116:119], v[76:79]\n\t"
    "s_waitcnt lgkmcnt(4)\n\t"
    "v_mfma_f32_16x16x32_bf16 v[64:67], v[152:155], v[120:123], v[64:67]\n\t"
    "ds_read_b128 v[112:115], %[ea] offset:12288\n\t"
    "v_mfma_f32_16x16x32_bf16 v[68:71], v[156:159], v[120:123], v[68:71]\n\t"
    "ds_read_b128 v[116:119], %[ea] offset:14336\n\t"
    "v_mfma_f32_16x16x32_bf16 v[72:75], v[152:155], v[124:127], v[72:75]\n\t"
    "v_mfma_f32_16x16x32_bf16 v[76:79], v[156:159], v[124:127], v[76:79]\n\t"
    "s_waitcnt lgkmcnt(4)\n\t"
    "v_mfma_f32_16x16x32_bf16 v[64:67], v[160:163], v[96:99], v[64:67]\n\t"
    "ds_read_b128 v[120:123], %[da] offset:12288\n\t"
    "v_mfma_f32_16x16x32_bf16 v[68:71], v[164:167], v[96:99], v[68:71]\n\t"
    "ds_read_b128 v[124:127], %[da] offset:14336\n\t"
    "v_mfma_f32_16x16x32_bf16 v[72:75], v[160:163], v[100:103], v[72:75]\n\t"
    "v_mfma_f32_16x16x32_bf16 v[76:79], v[164:167], v[100:103], v[76:79]\n\t"
    "s_waitcnt lgkmcnt(4)\n\t"
    "v_mfma_f32_16x16x32_bf16 v[64:67], v[168:171], v[104:107], v[64:67]\n\t"
    "ds_read_b128 v[96:99], %[ea] offset:16384\n\t"
    "v_mfma_f32_16x16x32_bf16 v[68:71], v[172:175], v[104:107], v[68:71]\n\t"
    "ds_read_b128 v[100:103], %[ea] offset:18432\n\t"
    "v_mfma_f32_16x16x32_bf16 v[72:75], v[168:171], v[108:111], v[72:75]\n\t"
    "v_mfma_f32_16x16x32_bf16 v[76:79], v[172:175], v[108:111], v[76:79]\n\t"
    "s_waitcnt lgkmcnt(4)\n\t"
    "v_mfma_f32_16x16x32_bf16 v[64:67], v[176:179], v[112:115], v[64:67]\n\t"
    "ds_read_b128 v[104:107], %[da] offset:16384\n\t"
    "v_mfma_f32_16x16x32_bf16 v[68:71], v[180:183], v[112:115], v[68:71]\n\t"
    "ds_read_b128 v[108:111], %[da] offset:18432\n\t"
    "v_mfma_f32_16x16x32_bf16 v[72:75], v[176:179], v[116:119], v[72:75]\n\t"
    "v_mfma_f32_16x16x32_bf16 v[76:79], v[180:183], v[116:119], v[76:79]\n\t"
    "s_waitcnt lgkmcnt(4)\n\t"
    "v_mfma_f32_16x16x32_bf16 v[64:67], v[184:187], v[120:123], v[64:67]\n\t"
    "ds_read_b128 v[112:115], %[ea] offset:20480\n\t"
    "v_mfma_f32_16x16x32_bf16 v[68:71], v[188:191], v[120:123], v[68:71]\n\t"
    "ds_read_b128 v[116:119], %[ea] offset:22528\n\t"
    "v_mfma_f32_16x16x32_bf16 v[72:75], v[184:187], v[124:127], v[72:75]\n\t"
    "v_mfma_f32_16x16x32_bf16 v[76:79], v[188:191], v[124:127], v[76:79]\n\t"
    "s_waitcnt vmcnt(0)\n\t"
    "s_barrier\n\t"
    "s_waitcnt lgkmcnt(4)\n\t"
    "v_mfma_f32_16x16x32_bf16 v[64:67], v[192:195], v[96:99], v[64:67]\n\t"
    "ds_read_b128 v[120:123], %[da] offset:20480\n\t"
    "v_mfma_f32_16x16x32_bf16 v[68:71], v[196:199], v[96:99], v[68:71]\n\t"
    "ds_read_b128 v[124:127], %[da] offset:22528\n\t"
    "v_mfma_f32_16x16x32_bf16 v[72:75], v[192:195], v[100:103], v[72:75]\n\t"
    "ds_read_b128 v[48:51], %[rw] offset:0\n\t"
    "v_mfma_f32_16x16x32_bf16 v[76:79], v[196:199], v[100:103], v[76:79]\n\t"
    "ds_read_b128 v[52:55], %[rw] offset:1024\n\t"
    "s_cmp_eq_u32 %[w0], 0\n\t"
    "s_cbranch_scc1 5f\n\t"
    "6:\n\t"
    "s_waitcnt lgkmcnt(6)\n\t"
    "v_mfma_f32_16x16x32_bf16 v[64:67], v[200:203], v[104:107], v[64:67]\n\t"
    "ds_read_b128 v[96:99], %[ea] offset:24576\n\t"
    "v_mfma_f32_16x16x32_bf16 v[68:71], v[204:207], v[104:107], v[68:71]\n\t"
    "ds_read_b128 v[100:103], %[ea] offset:26624\n\t"
    "v_mfma_f32_16x16x32_bf16 v[72:75], v[200:203], v[108:111], v[72:75]\n\t"
    "ds_read_b128 v[56:59], %[rw] offset:2048\n\t"
    "v_mfma_f32_16x16x32_bf16 v[76:79], v[204:207], v[108:111], v[76:79]\n\t"
    "ds_read_b128 v[60:63], %[rw] offset:3072\n\t"
    "s_waitcnt lgkmcnt(0)\n\t"
    "v_mfma_f32_16x16x32_bf16 v[64:67], v[208:211], v[112:115], v[64:67]\n\t"
    "ds_read_b128 v[104:107], %[da] offset:24576\n\t"
    "v_cvt_pk_bf16_f32 v40, v80, v81\n\t"
    "v_cvt_pk_bf16_f32 v41, v82, v83\n\t"
    "v_mfma_f32_16x16x32_bf16 v[68:71], v[212:215], v[112:115], v[68:71]\n\t"
    "ds_read_b128 v[108:111], %[da] offset:26624\n\t"
    "v_cvt_pk_bf16_f32 v42, v84, v85\n\t"
    "v_mfma_f32_16x16x32_bf16 v[72:75], v[208:211], v[116:119], v[72:75]\n\t"
    "ds_read_b32 v39, %[sa]\n\t"
    "v_cvt_pk_bf16_f32 v43, v86, v87\n\t"
    "v_mfma_f32_16x16x32_bf16 v[76:79], v[212:215], v[116:119], v[76:79]\n\t"
    "global_store_dwordx4 %[co0], v[40:43], s[36:37] sc1\n\t"
    "v_mfma_f32_16x16x32_bf16 v[64:67], v[216:219], v[120:123], v[64:67]\n\t"
    "ds_read_b128 v[112:115], %[ea] offset:28672\n\t"
    "v_cvt_pk_bf16_f32 v44, v88, v89\n\t"
    "v_cvt_pk_bf16_f32 v45, v90, v91\n\t"
    "v_mfma_f32_16x16x32_bf16 v[68:71], v[220:223], v[120:123], v[68:71]\n\t"
    "ds_read_b128 v[116:119], %[ea] offset:30720\n\t"
    "v_cvt_pk_bf16_f32 v46, v92, v93\n\t"
    "v_mfma_f32_16x16x32_bf16 v[72:75], v[216:219], v[124:127], v[72:75]\n\t"
    "v_xor_b32 %[ea], 0x10000, %[ea]\n\t"
    "v_cvt_pk_bf16_f32 v47, v94, v95\n\t"
    "v_mfma_f32_16x16x32_bf16 v[76:79], v[220:223], v[124:127], v[76:79]\n\t"
    "global_store_dwordx4 %[co1], v[44:47], s[36:37] sc1\n\t"
    "v_mfma_f32_16x16x32_bf16 v[64:67], v[224:227], v[96:99], v[64:67]\n\t"
    "ds_read_b128 v[120:123], %[da] offset:28672\n\t"
    "v_cvt_pk_bf16_f32 v48, v48, v49\n\t"
    "v_cvt_pk_bf16_f32 v49, v50, v51\n\t"
    "v_mfma_f32_16x16x32_bf16 v[68:71], v[228:231], v[96:99], v[68:71]\n\t"
    "ds_read_b128 v[124:127], %[da] offset:30720\n\t"
    "v_cvt_pk_bf16_f32 v50, v52, v53\n\t"
    "v_mfma_f32_16x16x32_bf16 v[72:75], v[224:227], v[100:103], v[72:75]\n\t"
    "v_xor_b32 %[da], 0x10000, %[da]\n\t"
    "v_cvt_pk_bf16_f32 v51, v54, v55\n\t"
    "v_mfma_f32_16x16x32_bf16 v[76:79], v[228:231], v[100:103], v[76:79]\n\t"
    "global_store_dwordx4 %[co0], v[48:51], s[36:37] offset:2048 sc1\n\t"
    "s_waitcnt lgkmcnt(5)\n\t"
    "v_mfma_f32_16x16x32_bf16 v[64:67], v[232:235], v[104:107], v[64:67]\n\t"
    "ds_read_b128 v[96:99], %[ea] offset:0\n\t"
    "v_cvt_pk_bf16_f32 v56, v56, v57\n\t"
    "v_cvt_pk_bf16_f32 v57, v58, v59\n\t"
    "v_mfma_f32_16x16x32_bf16 v[68:71], v[236:239], v[104:107], v[68:71]\n\t"
    "ds_read_b128 v[100:103], %[ea] offset:2048\n\t"
    "v_cvt_pk_bf16_f32 v58, v60, v61\n\t"
    "v_mfma_f32_16x16x32_bf16 v[72:75], v[232:235], v[108:111], v[72:75]\n\t"
    "v_cvt_pk_bf16_f32 v59, v62, v63\n\t"
    "v_mfma_f32_16x16x32_bf16 v[76:79], v[236:239], v[108:111], v[76:79]\n\t"
    "global_store_dwordx4 %[co1], v[56:59], s[36:37] offset:2048 sc1\n\t"
    "s_waitcnt lgkmcnt(4)\n\t"
    "v_mfma_f32_16x16x32_bf16 v[64:67], v[240:243], v[112:115], v[64:67]\n\t"
    "ds_read_b128 v[104:107], %[da] offset:0\n\t"
    "v_mad_u32_u24 v39, v39, %[mu], %[ad]\n\t"
    "v_mfma_f32_16x16x32_bf16 v[68:71], v[244:247], v[112:115], v[68:71]\n\t"
    "ds_read_b128 v[108:111], %[da] offset:2048\n\t"
    "v_cmp_ge_i32 vcc, %[lf], v39\n\t"
    "s_cmp_eq_u64 vcc, 0\n\t"
    "s_addc_u32 %[lf], %[lf], 0\n\t"
    "v_mfma_f32_16x16x32_bf16 v[72:75], v[240:243], v[116:119], v[72:75]\n\t"
    "v_mfma_f32_16x16x32_bf16 v[76:79], v[244:247], v[116:119], v[76:79]\n\t"
    "v_cmp_ge_i32 vcc, %[lf], v39\n\t"
    "s_cmp_eq_u64 vcc, 0\n\t"
    "s_addc_u32 %[lf], %[lf], 0\n\t"
    "s_waitcnt lgkmcnt(4)\n\t"
    "v_mfma_f32_16x16x32_bf16 v[80:83], v[248:251], v[120:123], v[64:67]\n\t"
    "ds_read_b128 v[112:115], %[ea] offset:4096\n\t"
    "s_min_i32 %[lf], %[lf], %[cp]\n\t"
    "s_add_u32 %[b], %[b], 1\n\t"
    "v_mfma_f32_16x16x32_bf16 v[84:87], v[252:255], v[120:123], v[68:71]\n\t"
    "ds_read_b128 v[116:119], %[ea] offset:6144\n\t"
    "v_mfma_f32_16x16x32_bf16 v[88:91], v[248:251], v[124:127], v[72:75]\n\t"
    "v_mfma_f32_16x16x32_bf16 v[92:95], v[252:255], v[124:127], v[76:79]\n\t"
    "s_mov_b64 s[36:37], s[38:39]\n\t"
    "s_add_u32 s38, s38, %[ab]\n\t"
    "s_addc_u32 s39, s39, 0\n\t"
    "s_sub_u32 m0, %[lf], %[b]\n\t"
    "s_sub_u32 m0, m0, 1\n\t"
    "s_cmp_lt_u32 m0, %[up]\n\t"
    "s_cbranch_scc1 1b\n\t"
    "s_waitcnt lgkmcnt(0)\n\t"
    "s_nop 4\n\t"
    "s_branch 9f\n\t"
    "5:\n\t"
    "s_add_u32 %[st], %[b], %[po]\n\t"
    "s_max_i32 %[st], %[st], %[p0]\n\t"
    "v_mov_b32 v39, %[st]\n\t"
    "v_mov_b32 v38, 0\n\t"
    "s_mov_b64 exec, 1\n\t"
    "global_store_dword v38, v39, %[pg] sc1\n\t"
    "s_mov_b64 exec, -1\n\t"
    "s_mov_b32 %[p0], %[p1]\n\t"
    "s_branch 6b\n\t"
    "7:\n\t"
    "s_cmp_ge_i32 %[lf], %[cp]\n\t"
    "s_cbranch_scc1 8b\n\t"
    "s_mov_b32 m0, %[sl]\n\t"
    "s_nop 0\n\t"
    "global_load_lds_dword %[dv], %[db] sc1\n\t"
    "s_branch 8b\n\t"
    "9:"
    : "=&{v[80:95]}"(pd), [ea] "+&v"(ea), [da] "+&v"(da), [b] "+&s"(b), [lf] "+&s"(lf), "+{s[36:37]}"(p1), "+{s[38:39]}"(p0), [sa] "+&v"(sa), [st] "=&s"(st), [sl] "+&s"(sl), [p0] "+&s"(pf)
    : [rw] "v"(rw), [ab] "s"(ab), [cp] "s"(cp), [mu] "s"(mu), [ad] "v"(ad), [up] "s"(up), "{v[128:159]}"(W[0]), "{v[160:191]}"(W[1]), "{v[192:223]}"(W[2]), "{v[224:255]}"(W[3]), [co0] "v"(co0), [w0] "s"(w0), [dv] "v"(dv), [db] "s"(db), [pg] "s"(pg), [po] "s"(po - 1), [p1] "s"(b + po), [co1] "v"(co1)
    : "v38", "v39", "v40", "v41", "v42", "v43", "v44", "v45", "v46", "v47", "v48", "v49", "v50", "v51", "v52", "v53", "v54", "v55", "v56", "v57", "v58", "v59", "v60", "v61", "v62", "v63", "v64", "v65", "v66", "v67", "v68", "v69", "v70", "v71", "v72", "v73", "v74", "v75", "v76", "v77", "v78", "v79", "v96", "v97", "v98", "v99", "v100", "v101", "v102", "v103", "v104", "v105", "v106", "v107", "v108", "v109", "v110", "v111", "v112", "v113", "v114", "v115", "v116", "v117", "v118", "v119", "v120", "v121", "v122", "v123", "v124", "v125", "v126", "v127", "vcc", "scc", "m0", "memory");
}
__device__ __forceinline__ void lean_ld0(const u32x32 (&W)[4], unsigned ea, unsigned da, unsigned x, unsigned lb, const char* s0, const char* s1, const unsigned (&av)[8], unsigned ps, unsigned dv, const char* db, unsigned rw, unsigned fa, unsigned fv,
    unsigned ga, unsigned mu, unsigned ad, int th, int tb, unsigned& sv, unsigned& su, unsigned& got) {
  asm volatile("s_setprio 2\n\t"
    "v_mov_b32 v76, %[ga]\n\t"
    "ds_read_b32 v76, v76 offset:24\n\t"
    "ds_read_b128 v[96:99], %[ea] offset:0\n\t"
    "ds_read_b128 v[100:103], %[ea] offset:2048\n\t"
    "ds_read_b128 v[104:107], %[da] offset:0\n\t"
    "ds_read_b128 v[108:111], %[da] offset:2048\n\t"
    "ds_read_b128 v[112:115], %[ea] offset:4096\n\t"
    "ds_read_b128 v[116:119], %[ea] offset:6144\n\t"
    "s_waitcnt lgkmcnt(6)\n\t"
    "v_cmp_ne_u32 vcc, 0, v76\n\t"
    "s_mov_b32 %[got], -1\n\t"
    "s_cbranch_vccnz 12f\n\t"
    "s_waitcnt lgkmcnt(4)\n\t"
    "v_mfma_f32_16x16x32_bf16 v[80:83], v[128:131], v[96:99], 0\n\t"
    "ds_read_b128 v[120:123], %[da] offset:4096\n\t"
    "v_mfma_f32_16x16x32_bf16 v[84:87], v[132:135], v[96:99], 0\n\t"
    "ds_read_b128 v[124:127], %[da] offset:6144\n\t"
    "v_mfma_f32_16x16x32_bf16 v[88:91], v[128:131], v[100:103], 0\n\t"
    "v_mfma_f32_16x16x32_bf16 v[92:95], v[132:135], v[100:103], 0\n\t"
    "s_waitcnt vmcnt(24)\n\t"
    "s_cmp_lt_u32 %[x], 2\n\t"
    "s_cbranch_scc0 41f\n\t"
    "s_waitcnt vmcnt(8)\n\t"
    "41:\n\t"
    "s_barrier\n\t"
    "s_waitcnt lgkmcnt(4)\n\t"
    "v_mfma_f32_16x16x32_bf16 v[80:83], v[136:139], v[104:107], v[80:83]\n\t"
    "ds_read_b128 v[96:99], %[ea] offset:8192\n\t"
    "v_mfma_f32_16x16x32_bf16 v[84:87], v[140:143], v[104:107], v[84:87]\n\t"
    "ds_read_b128 v[100:103], %[ea] offset:10240\n\t"
    "v_mfma_f32_16x16x32_bf16 v[88:91], v[136:139], v[108:111], v[88:91]\n\t"
    "v_mfma_f32_16x16x32_bf16 v[92:95], v[140:143], v[108:111], v[92:95]\n\t"
    "s_waitcnt lgkmcnt(4)\n\t"
    "v_mfma_f32_16x16x32_bf16 v[80:83], v[144:147], v[112:115], v[80:83]\n\t"
    "ds_read_b128 v[104:107], %[da] offset:8192\n\t"
    "v_mfma_f32_16x16x32_bf16 v[84:87], v[148:151], v[112:115], v[84:87]\n\t"
    "ds_read_b128 v[108:111], %[da] offset:10240\n\t"
    "v_mfma_f32_16x16x32_bf16 v[88:91], v[144:147], v[116:119], v[88:91]\n\t"
    "v_mfma_f32_16x16x32_bf16 v[92:95], v[148:151], v[116:119], v[92:95]\n\t"
    "s_waitcnt lgkmcnt(4)\n\t"
    "v_mfma_f32_16x16x32_bf16 v[80:83], v[152:155], v[120:123], v[80:83]\n\t"
    "ds_read_b128 v[112:115], %[ea] offset:12288\n\t"
    "v_mfma_f32_16x16x32_bf16 v[84:87], v[156:159], v[120:123], v[84:87]\n\t"
    "ds_read_b128 v[116:119], %[ea] offset:14336\n\t"
    "v_mfma_f32_16x16x32_bf16 v[88:91], v[152:155], v[124:127], v[88:91]\n\t"
    "v_mfma_f32_16x16x32_bf16 v[92:95], v[156:159], v[124:127], v[92:95]\n\t"
    "s_waitcnt lgkmcnt(4)\n\t"
    "v_mfma_f32_16x16x32_bf16 v[80:83], v[160:163], v[96:99], v[80:83]\n\t"
    "ds_read_b128 v[120:123], %[da] offset:12288\n\t"
    "v_mfma_f32_16x16x32_bf16 v[84:87], v[164:167], v[96:99], v[84:87]\n\t"
    "ds_read_b128 v[124:127], %[da] offset:14336\n\t"
    "v_mfma_f32_16x16x32_bf16 v[88:91], v[160:163], v[100:103], v[88:91]\n\t"
    "v_mfma_f32_16x16x32_bf16 v[92:95], v[164:167], v[100:103], v[92:95]\n\t"
    "s_waitcnt vmcnt(20)\n\t"
    "s_cmp_lt_u32 %[x], 2\n\t"
    "s_cbranch_scc0 45f\n\t"
    "s_waitcnt vmcnt(4)\n\t"
    "45:\n\t"
    "s_barrier\n\t"
    "s_waitcnt lgkmcnt(4)\n\t"
    "v_mfma_f32_16x16x32_bf16 v[80:83], v[168:171], v[104:107], v[80:83]\n\t"
    "ds_read_b128 v[96:99], %[ea] offset:16384\n\t"
    "v_mfma_f32_16x16x32_bf16 v[84:87], v[172:175], v[104:107], v[84:87]\n\t"
    "ds_read_b128 v[100:103], %[ea] offset:18432\n\t"
    "v_mfma_f32_16x16x32_bf16 v[88:91], v[168:171], v[108:111], v[88:91]\n\t"
    "v_mfma_f32_16x16x32_bf16 v[92:95], v[172:175], v[108:111], v[92:95]\n\t"
    "s_waitcnt lgkmcnt(4)\n\t"
    "v_mfma_f32_16x16x32_bf16 v[80:83], v[176:179], v[112:115], v[80:83]\n\t"
    "ds_read_b128 v[104:107], %[da] offset:16384\n\t"
    "v_mfma_f32_16x16x32_bf16 v[84:87], v[180:183], v[112:115], v[84:87]\n\t"
    "ds_read_b128 v[108:111], %[da] offset:18432\n\t"
    "v_mfma_f32_16x16x32_bf16 v[88:91], v[176:179], v[116:119], v[88:91]\n\t"
    "v_mfma_f32_16x16x32_bf16 v[92:95], v[180:183], v[116:119], v[92:95]\n\t"
    "s_waitcnt lgkmcnt(4)\n\t"
    "v_mfma_f32_16x16x32_bf16 v[80:83], v[184:187], v[120:123], v[80:83]\n\t"
    "ds_read_b128 v[112:115], %[ea] offset:20480\n\t"
    "v_mfma_f32_16x16x32_bf16 v[84:87], v[188:191], v[120:123], v[84:87]\n\t"
    "ds_read_b128 v[116:119], %[ea] offset:22528\n\t"
    "v_mfma_f32_16x16x32_bf16 v[88:91], v[184:187], v[124:127], v[88:91]\n\t"
    "v_mfma_f32_16x16x32_bf16 v[92:95], v[188:191], v[124:127], v[92:95]\n\t"
    "s_waitcnt lgkmcnt(4)\n\t"
    "v_mfma_f32_16x16x32_bf16 v[80:83], v[192:195], v[96:99], v[80:83]\n\t"
    "ds_read_b128 v[120:123], %[da] offset:20480\n\t"
    "v_mfma_f32_16x16x32_bf16 v[84:87], v[196:199], v[96:99], v[84:87]\n\t"
    "ds_read_b128 v[124:127], %[da] offset:22528\n\t"
    "v_mfma_f32_16x16x32_bf16 v[88:91], v[192:195], v[100:103], v[88:91]\n\t"
    "v_mfma_f32_16x16x32_bf16 v[92:95], v[196:199], v[100:103], v[92:95]\n\t"
    "s_waitcnt vmcnt(16)\n\t"
    "s_cmp_lt_u32 %[x], 2\n\t"
    "s_cbranch_scc0 49f\n\t"
    "s_waitcnt vmcnt(0)\n\t"
    "49:\n\t"
    "s_barrier\n\t"
    "s_waitcnt lgkmcnt(4)\n\t"
    "v_mfma_f32_16x16x32_bf16 v[80:83], v[200:203], v[104:107], v[80:83]\n\t"
    "ds_read_b128 v[96:99], %[ea] offset:24576\n\t"
    "v_mfma_f32_16x16x32_bf16 v[84:87], v[204:207], v[104:107], v[84:87]\n\t"
    "ds_read_b128 v[100:103], %[ea] offset:26624\n\t"
    "v_mfma_f32_16x16x32_bf16 v[88:91], v[200:203], v[108:111], v[88:91]\n\t"
    "v_mfma_f32_16x16x32_bf16 v[92:95], v[204:207], v[108:111], v[92:95]\n\t"
    "s_waitcnt lgkmcnt(4)\n\t"
    "v_mfma_f32_16x16x32_bf16 v[80:83], v[208:211], v[112:115], v[80:83]\n\t"
    "ds_read_b128 v[104:107], %[da] offset:24576\n\t"
    "v_mfma_f32_16x16x32_bf16 v[84:87], v[212:215], v[112:115], v[84:87]\n\t"
    "ds_read_b128 v[108:111], %[da] offset:26624\n\t"
    "v_mfma_f32_16x16x32_bf16 v[88:91], v[208:211], v[116:119], v[88:91]\n\t"
    "v_mfma_f32_16x16x32_bf16 v[92:95], v[212:215], v[116:119], v[92:95]\n\t"
    "s_cmp_eq_u32 %[ps], 0\n\t"
    "s_cbranch_scc1 10f\n\t"
    "global_load_dword %[sv], %[dv], %[db] sc1\n\t"
    "global_load_dword %[su], %[dv], %[db] offset:64 sc1\n\t"
    "10:\n\t"
    "s_waitcnt lgkmcnt(4)\n\t"
    "v_mfma_f32_16x16x32_bf16 v[80:83], v[216:219], v[120:123], v[80:83]\n\t"
    "ds_read_b128 v[112:115], %[ea] offset:28672\n\t"
    "v_mfma_f32_16x16x32_bf16 v[84:87], v[220:223], v[120:123], v[84:87]\n\t"
    "ds_read_b128 v[116:119], %[ea] offset:30720\n\t"
    "v_mfma_f32_16x16x32_bf16 v[88:91], v[216:219], v[124:127], v[88:91]\n\t"
    "v_mfma_f32_16x16x32_bf16 v[92:95], v[220:223], v[124:127], v[92:95]\n\t"
    "s_waitcnt lgkmcnt(4)\n\t"
    "v_mfma_f32_16x16x32_bf16 v[80:83], v[224:227], v[96:99], v[80:83]\n\t"
    "ds_read_b128 v[120:123], %[da] offset:28672\n\t"
    "v_mfma_f32_16x16x32_bf16 v[84:87], v[228:231], v[96:99], v[84:87]\n\t"
    "ds_read_b128 v[124:127], %[da] offset:30720\n\t"
    "v_mfma_f32_16x16x32_bf16 v[88:91], v[224:227], v[100:103], v[88:91]\n\t"
    "v_mfma_f32_16x16x32_bf16 v[92:95], v[228:231], v[100:103], v[92:95]\n\t"
    "s_waitcnt lgkmcnt(4)\n\t"
    "v_mfma_f32_16x16x32_bf16 v[80:83], v[232:235], v[104:107], v[80:83]\n\t"
    "v_mfma_f32_16x16x32_bf16 v[84:87], v[236:239], v[104:107], v[84:87]\n\t"
    "v_mfma_f32_16x16x32_bf16 v[88:91], v[232:235], v[108:111], v[88:91]\n\t"
    "v_mfma_f32_16x16x32_bf16 v[92:95], v[236:239], v[108:111], v[92:95]\n\t"
    "s_waitcnt lgkmcnt(2)\n\t"
    "v_mfma_f32_16x16x32_bf16 v[80:83], v[240:243], v[112:115], v[80:83]\n\t"
    "v_mfma_f32_16x16x32_bf16 v[84:87], v[244:247], v[112:115], v[84:87]\n\t"
    "v_mfma_f32_16x16x32_bf16 v[88:91], v[240:243], v[116:119], v[88:91]\n\t"
    "v_mfma_f32_16x16x32_bf16 v[92:95], v[244:247], v[116:119], v[92:95]\n\t"
    "s_waitcnt lgkmcnt(0)\n\t"
    "v_mfma_f32_16x16x32_bf16 v[80:83], v[248:251], v[120:123], v[80:83]\n\t"
    "s_memrealtime s[36:37]\n\t"
    "v_mfma_f32_16x16x32_bf16 v[84:87], v[252:255], v[120:123], v[84:87]\n\t"
    "v_mfma_f32_16x16x32_bf16 v[88:91], v[248:251], v[124:127], v[88:91]\n\t"
    "v_mfma_f32_16x16x32_bf16 v[92:95], v[252:255], v[124:127], v[92:95]\n\t"
    "s_mov_b32 %[got], 0\n\t"
    "v_mov_b32 v76, %[fa]\n\t"
    "v_mov_b32 v77, %[fv]\n\t"
    "v_add_u32 v78, 1, v77\n\t"
    "v_mov_b32 v79, %[ga]\n\t"
    "s_waitcnt lgkmcnt(0)\n\t"
    "ds_write_b128 %[rw], v[80:83] offset:0\n\t"
    "ds_write_b128 %[rw], v[84:87] offset:1024\n\t"
    "ds_write_b128 %[rw], v[88:91] offset:2048\n\t"
    "ds_write_b128 %[rw], v[92:95] offset:3072\n\t"
    "ds_write_b32 v76, v77\n\t"
    "s_cmp_eq_u32 %[ps], 0\n\t"
    "s_cbranch_scc1 9f\n\t"
    "s_waitcnt vmcnt(0)\n\t"
    "v_mad_u32_u24 v76, %[sv], %[mu], %[ad]\n\t"
    "v_cmp_ge_i32 vcc, %[th], v76\n\t"
    "s_cbranch_vccz 11f\n\t"
    "s_sub_u32 s36, s36, 20\n\t"
    "v_sub_u32 v76, s36, %[su]\n\t"
    "v_lshrrev_b32 v77, 24, %[su]\n\t"
    "v_lshlrev_b32 v76, 8, v76\n\t"
    "v_ashrrev_i32 v76, 31, v76\n\t"
    "v_add_u32 v77, v77, v76\n\t"
    "v_cmp_lt_i32 s[36:37], %[tb], v77\n\t"
    "s_andn2_b64 vcc, vcc, s[36:37]\n\t"
    "s_cbranch_scc1 9f\n\t"
    "11:\n\t"
    "ds_write2_b32 v79, v78, v78 offset1:1\n\t"
    "s_add_u32 m0, %[lb], 0\n\t"
    "s_nop 0\n\t"
    "global_load_lds_dwordx4 %[av0], %[s0] sc1\n\t"
    "s_add_u32 m0, %[lb], 32768\n\t"
    "s_nop 0\n\t"
    "global_load_lds_dwordx4 %[av1], %[s0] sc1\n\t"
    "s_add_u32 m0, %[lb], 4096\n\t"
    "s_nop 0\n\t"
    "global_load_lds_dwordx4 %[av2], %[s0] sc1\n\t"
    "s_add_u32 m0, %[lb], 36864\n\t"
    "s_nop 0\n\t"
    "global_load_lds_dwordx4 %[av3], %[s0] sc1\n\t"
    "s_add_u32 m0, %[lb], 8192\n\t"
    "s_nop 0\n\t"
    "global_load_lds_dwordx4 %[av4], %[s0] sc1\n\t"
    "s_add_u32 m0, %[lb], 40960\n\t"
    "s_nop 0\n\t"
    "global_load_lds_dwordx4 %[av5], %[s0] sc1\n\t"
    "s_add_u32 m0, %[lb], 12288\n\t"
    "s_nop 0\n\t"
    "global_load_lds_dwordx4 %[av6], %[s0] sc1\n\t"
    "s_add_u32 m0, %[lb], 45056\n\t"
    "s_nop 0\n\t"
    "global_load_lds_dwordx4 %[av7], %[s0] sc1\n\t"
    "s_add_u32 m0, %[lb], 16384\n\t"
    "s_nop 0\n\t"
    "global_load_lds_dwordx4 %[av0], %[s1] sc1\n\t"
    "s_add_u32 m0, %[lb], 49152\n\t"
    "s_nop 0\n\t"
    "global_load_lds_dwordx4 %[av1], %[s1] sc1\n\t"
    "s_add_u32 m0, %[lb], 20480\n\t"
    "s_nop 0\n\t"
    "global_load_lds_dwordx4 %[av2], %[s1] sc1\n\t"
    "s_add_u32 m0, %[lb], 53248\n\t"
    "s_nop 0\n\t"
    "global_load_lds_dwordx4 %[av3], %[s1] sc1\n\t"
    "s_add_u32 m0, %[lb], 24576\n\t"
    "s_nop 0\n\t"
    "global_load_lds_dwordx4 %[av4], %[s1] sc1\n\t"
    "s_add_u32 m0, %[lb], 57344\n\t"
    "s_nop 0\n\t"
    "global_load_lds_dwordx4 %[av5], %[s1] sc1\n\t"
    "s_add_u32 m0, %[lb], 28672\n\t"
    "s_nop 0\n\t"
    "global_load_lds_dwordx4 %[av6], %[s1] sc1\n\t"
    "s_add_u32 m0, %[lb], 61440\n\t"
    "s_nop 0\n\t"
    "global_load_lds_dwordx4 %[av7], %[s1] sc1\n\t"
    "s_mov_b32 %[got], 1\n\t"
    "s_branch 9f\n\t"
    "12:\n\t"
    "s_waitcnt lgkmcnt(0)\n\t"
    "9:"
    : [sv] "+&v"(sv), [su] "+&v"(su), [got] "=&s"(got)
    : [ea] "v"(ea), [da] "v"(da), [x] "s"(x), "{v[128:159]}"(W[0]), "{v[160:191]}"(W[1]), "{v[192:223]}"(W[2]), "{v[224:255]}"(W[3]), [lb] "s"(lb), [s0] "s"(s0), [s1] "s"(s1), [av0] "v"(av[0]), [av1] "v"(av[1]), [av2] "v"(av[2]), [av3] "v"(av[3]), [av4] "v"(av[4]), [av5] "v"(av[5]), [av6] "v"(av[6]), [av7] "v"(av[7]), [ps] "s"(ps), [dv] "v"(dv), [db] "s"(db), [rw] "v"(rw), [fa] "s"(fa), [fv] "s"(fv), [ga] "s"(ga), [mu] "s"(mu), [ad] "v"(ad), [th] "s"(th), [tb] "s"(tb)
    : "v96", "v97", "v98", "v99", "v100", "v101", "v102", "v103", "v104", "v105", "v106", "v107", "v108", "v109", "v110", "v111", "v112", "v113", "v114", "v115", "v116", "v117", "v118", "v119", "v120", "v121", "v122", "v123", "v124", "v125", "v126", "v127", "v76", "v77", "v78", "v79", "v80", "v81", "v82", "v83", "v84", "v85", "v86", "v87", "v88", "v89", "v90", "v91", "v92", "v93", "v94", "v95", "vcc", "scc", "m0", "memory", "s36", "s37", "s38", "s39");
}
__device__ __forceinline__ void lean_dm0(const u32x32 (&W)[4], unsigned ea, unsigned da, unsigned x, unsigned lb, const char* s0, const char* s1, const unsigned (&av)[8], unsigned ps, unsigned dv, const char* db, unsigned rw, unsigned fa, unsigned fv,
    unsigned ga, unsigned mu, unsigned ad, int th, int tb, unsigned& sv, unsigned& su, unsigned& got) {
  asm volatile("s_setprio 2\n\t"
    "v_mov_b32 v76, %[ga]\n\t"
    "ds_read_b32 v76, v76 offset:24\n\t"
    "ds_read_b128 v[96:99], %[ea] offset:0\n\t"
    "ds_read_b128 v[100:103], %[ea] offset:2048\n\t"
    "ds_read_b128 v[104:107], %[da] offset:0\n\t"
    "ds_read_b128 v[108:111], %[da] offset:2048\n\t"
    "ds_read_b128 v[112:115], %[ea] offset:4096\n\t"
    "ds_read_b128 v[116:119], %[ea] offset:6144\n\t"
    "s_waitcnt lgkmcnt(6)\n\t"
    "v_cmp_ne_u32 vcc, 0, v76\n\t"
    "s_mov_b32 %[got], -1\n\t"
    "s_cbranch_vccnz 12f\n\t"
    "s_waitcnt lgkmcnt(4)\n\t"
    "v_mfma_f32_16x16x32_bf16 v[80:83], v[128:131], v[96:99], 0\n\t"
    "ds_read_b128 v[120:123], %[da] offset:4096\n\t"
    "s_add_u32 m0, %[lb], 0\n\t"
    "v_mfma_f32_16x16x32_bf16 v[84:87], v[132:135], v[96:99], 0\n\t"
    "ds_read_b128 v[124:127], %[da] offset:6144\n\t"
    "global_load_lds_dwordx4 %[av0], %[s0] sc1\n\t"
    "v_mfma_f32_16x16x32_bf16 v[88:91], v[128:131], v[100:103], 0\n\t"
    "s_add_u32 m0, %[lb], 32768\n\t"
    "v_mfma_f32_16x16x32_bf16 v[92:95], v[132:135], v[100:103], 0\n\t"
    "global_load_lds_dwordx4 %[av1], %[s0] sc1\n\t"
    "s_waitcnt vmcnt(10)\n\t"
    "s_barrier\n\t"
    "s_waitcnt lgkmcnt(4)\n\t"
    "v_mfma_f32_16x16x32_bf16 v[80:83], v[136:139], v[104:107], v[80:83]\n\t"
    "ds_read_b128 v[96:99], %[ea] offset:8192\n\t"
    "s_add_u32 m0, %[lb], 4096\n\t"
    "v_mfma_f32_16x16x32_bf16 v[84:87], v[140:143], v[104:107], v[84:87]\n\t"
    "ds_read_b128 v[100:103], %[ea] offset:10240\n\t"
    "global_load_lds_dwordx4 %[av2], %[s0] sc1\n\t"
    "v_mfma_f32_16x16x32_bf16 v[88:91], v[136:139], v[108:111], v[88:91]\n\t"
    "s_add_u32 m0, %[lb], 36864\n\t"
    "v_mfma_f32_16x16x32_bf16 v[92:95], v[140:143], v[108:111], v[92:95]\n\t"
    "global_load_lds_dwordx4 %[av3], %[s0] sc1\n\t"
    "s_waitcnt lgkmcnt(4)\n\t"
    "v_mfma_f32_16x16x32_bf16 v[80:83], v[144:147], v[112:115], v[80:83]\n\t"
    "ds_read_b128 v[104:107], %[da] offset:8192\n\t"
    "s_add_u32 m0, %[lb], 8192\n\t"
    "v_mfma_f32_16x16x32_bf16 v[84:87], v[148:151], v[112:115], v[84:87]\n\t"
    "ds_read_b128 v[108:111], %[da] offset:10240\n\t"
    "global_load_lds_dwordx4 %[av4], %[s0] sc1\n\t"
    "v_mfma_f32_16x16x32_bf16 v[88:91], v[144:147], v[116:119], v[88:91]\n\t"
    "s_add_u32 m0, %[lb], 40960\n\t"
    "v_mfma_f32_16x16x32_bf16 v[92:95], v[148:151], v[116:119], v[92:95]\n\t"
    "global_load_lds_dwordx4 %[av5], %[s0] sc1\n\t"
    "s_waitcnt lgkmcnt(4)\n\t"
    "v_mfma_f32_16x16x32_bf16 v[80:83], v[152:155], v[120:123], v[80:83]\n\t"
    "ds_read_b128 v[112:115], %[ea] offset:12288\n\t"
    "s_add_u32 m0, %[lb], 12288\n\t"
    "v_mfma_f32_16x16x32_bf16 v[84:87], v[156:159], v[120:123], v[84:87]\n\t"
    "ds_read_b128 v[116:119], %[ea] offset:14336\n\t"
    "global_load_lds_dwordx4 %[av6], %[s0] sc1\n\t"
    "v_mfma_f32_16x16x32_bf16 v[88:91], v[152:155], v[124:127], v[88:91]\n\t"
    "s_add_u32 m0, %[lb], 45056\n\t"
    "v_mfma_f32_16x16x32_bf16 v[92:95], v[156:159], v[124:127], v[92:95]\n\t"
    "global_load_lds_dwordx4 %[av7], %[s0] sc1\n\t"
    "s_waitcnt lgkmcnt(4)\n\t"
    "v_mfma_f32_16x16x32_bf16 v[80:83], v[160:163], v[96:99], v[80:83]\n\t"
    "ds_read_b128 v[120:123], %[da] offset:12288\n\t"
    "s_add_u32 m0, %[lb], 16384\n\t"
    "v_mfma_f32_16x16x32_bf16 v[84:87], v[164:167], v[96:99], v[84:87]\n\t"
    "ds_read_b128 v[124:127], %[da] offset:14336\n\t"
    "global_load_lds_dwordx4 %[av0], %[s1] sc1\n\t"
    "v_mfma_f32_16x16x32_bf16 v[88:91], v[160:163], v[100:103], v[88:91]\n\t"
    "s_add_u32 m0, %[lb], 49152\n\t"
    "v_mfma_f32_16x16x32_bf16 v[92:95], v[164:167], v[100:103], v[92:95]\n\t"
    "global_load_lds_dwordx4 %[av1], %[s1] sc1\n\t"
    "s_waitcnt vmcnt(14)\n\t"
    "s_barrier\n\t"
    "s_waitcnt lgkmcnt(4)\n\t"
    "v_mfma_f32_16x16x32_bf16 v[80:83], v[168:171], v[104:107], v[80:83]\n\t"
    "ds_read_b128 v[96:99], %[ea] offset:16384\n\t"
    "s_add_u32 m0, %[lb], 20480\n\t"
    "v_mfma_f32_16x16x32_bf16 v[84:87], v[172:175], v[104:107], v[84:87]\n\t"
    "ds_read_b128 v[100:103], %[ea] offset:18432\n\t"
    "global_load_lds_dwordx4 %[av2], %[s1] sc1\n\t"
    "v_mfma_f32_16x16x32_bf16 v[88:91], v[168:171], v[108:111], v[88:91]\n\t"
    "s_add_u32 m0, %[lb], 53248\n\t"
    "v_mfma_f32_16x16x32_bf16 v[92:95], v[172:175], v[108:111], v[92:95]\n\t"
    "global_load_lds_dwordx4 %[av3], %[s1] sc1\n\t"
    "s_waitcnt lgkmcnt(4)\n\t"
    "v_mfma_f32_16x16x32_bf16 v[80:83], v[176:179], v[112:115], v[80:83]\n\t"
    "ds_read_b128 v[104:107], %[da] offset:16384\n\t"
    "s_add_u32 m0, %[lb], 24576\n\t"
    "v_mfma_f32_16x16x32_bf16 v[84:87], v[180:183], v[112:115], v[84:87]\n\t"
    "ds_read_b128 v[108:111], %[da] offset:18432\n\t"
    "global_load_lds_dwordx4 %[av4], %[s1] sc1\n\t"
    "v_mfma_f32_16x16x32_bf16 v[88:91], v[176:179], v[116:119], v[88:91]\n\t"
    "s_add_u32 m0, %[lb], 57344\n\t"
    "v_mfma_f32_16x16x32_bf16 v[92:95], v[180:183], v[116:119], v[92:95]\n\t"
    "global_load_lds_dwordx4 %[av5], %[s1] sc1\n\t"
    "s_waitcnt lgkmcnt(4)\n\t"
    "v_mfma_f32_16x16x32_bf16 v[80:83], v[184:187], v[120:123], v[80:83]\n\t"
    "ds_read_b128 v[112:115], %[ea] offset:20480\n\t"
    "s_add_u32 m0, %[lb], 28672\n\t"
    "v_mfma_f32_16x16x32_bf16 v[84:87], v[188:191], v[120:123], v[84:87]\n\t"
    "ds_read_b128 v[116:119], %[ea] offset:22528\n\t"
    "global_load_lds_dwordx4 %[av6], %[s1] sc1\n\t"
    "v_mfma_f32_16x16x32_bf16 v[88:91], v[184:187], v[124:127], v[88:91]\n\t"
    "s_add_u32 m0, %[lb], 61440\n\t"
    "v_mfma_f32_16x16x32_bf16 v[92:95], v[188:191], v[124:127], v[92:95]\n\t"
    "global_load_lds_dwordx4 %[av7], %[s1] sc1\n\t"
    "s_waitcnt lgkmcnt(4)\n\t"
    "v_mfma_f32_16x16x32_bf16 v[80:83], v[192:195], v[96:99], v[80:83]\n\t"
    "ds_read_b128 v[120:123], %[da] offset:20480\n\t"
    "v_mfma_f32_16x16x32_bf16 v[84:87], v[196:199], v[96:99], v[84:87]\n\t"
    "ds_read_b128 v[124:127], %[da] offset:22528\n\t"
    "v_mfma_f32_16x16x32_bf16 v[88:91], v[192:195], v[100:103], v[88:91]\n\t"
    "v_mfma_f32_16x16x32_bf16 v[92:95], v[196:199], v[100:103], v[92:95]\n\t"
    "s_waitcnt vmcnt(16)\n\t"
    "s_barrier\n\t"
    "s_waitcnt lgkmcnt(4)\n\t"
    "v_mfma_f32_16x16x32_bf16 v[80:83], v[200:203], v[104:107], v[80:83]\n\t"
    "ds_read_b128 v[96:99], %[ea] offset:24576\n\t"
    "v_mfma_f32_16x16x32_bf16 v[84:87], v[204:207], v[104:107], v[84:87]\n\t"
    "ds_read_b128 v[100:103], %[ea] offset:26624\n\t"
    "v_mfma_f32_16x16x32_bf16 v[88:91], v[200:203], v[108:111], v[88:91]\n\t"
    "v_mfma_f32_16x16x32_bf16 v[92:95], v[204:207], v[108:111], v[92:95]\n\t"
    "s_waitcnt lgkmcnt(4)\n\t"
    "v_mfma_f32_16x16x32_bf16 v[80:83], v[208:211], v[112:115], v[80:83]\n\t"
    "ds_read_b128 v[104:107], %[da] offset:24576\n\t"
    "v_mfma_f32_16x16x32_bf16 v[84:87], v[212:215], v[112:115], v[84:87]\n\t"
    "ds_read_b128 v[108:111], %[da] offset:26624\n\t"
    "v_mfma_f32_16x16x32_bf16 v[88:91], v[208:211], v[116:119], v[88:91]\n\t"
    "v_mfma_f32_16x16x32_bf16 v[92:95], v[212:215], v[116:119], v[92:95]\n\t"
    "s_waitcnt lgkmcnt(4)\n\t"
    "v_mfma_f32_16x16x32_bf16 v[80:83], v[216:219], v[120:123], v[80:83]\n\t"
    "ds_read_b128 v[112:115], %[ea] offset:28672\n\t"
    "v_mfma_f32_16x16x32_bf16 v[84:87], v[220:223], v[120:123], v[84:87]\n\t"
    "ds_read_b128 v[116:119], %[ea] offset:30720\n\t"
    "v_mfma_f32_16x16x32_bf16 v[88:91], v[216:219], v[124:127], v[88:91]\n\t"
    "v_mfma_f32_16x16x32_bf16 v[92:95], v[220:223], v[124:127], v[92:95]\n\t"
    "s_waitcnt lgkmcnt(4)\n\t"
    "v_mfma_f32_16x16x32_bf16 v[80:83], v[224:227], v[96:99], v[80:83]\n\t"
    "ds_read_b128 v[120:123], %[da] offset:28672\n\t"
    "v_mfma_f32_16x16x32_bf16 v[84:87], v[228:231], v[96:99], v[84:87]\n\t"
    "ds_read_b128 v[124:127], %[da] offset:30720\n\t"
    "v_mfma_f32_16x16x32_bf16 v[88:91], v[224:227], v[100:103], v[88:91]\n\t"
    "v_mfma_f32_16x16x32_bf16 v[92:95], v[228:231], v[100:103], v[92:95]\n\t"
    "s_waitcnt lgkmcnt(4)\n\t"
    "v_mfma_f32_16x16x32_bf16 v[80:83], v[232:235], v[104:107], v[80:83]\n\t"
    "v_mfma_f32_16x16x32_bf16 v[84:87], v[236:239], v[104:107], v[84:87]\n\t"
    "v_mfma_f32_16x16x32_bf16 v[88:91], v[232:235], v[108:111], v[88:91]\n\t"
    "v_mfma_f32_16x16x32_bf16 v[92:95], v[236:239], v[108:111], v[92:95]\n\t"
    "s_waitcnt lgkmcnt(2)\n\t"
    "v_mfma_f32_16x16x32_bf16 v[80:83], v[240:243], v[112:115], v[80:83]\n\t"
    "v_mfma_f32_16x16x32_bf16 v[84:87], v[244:247], v[112:115], v[84:87]\n\t"
    "v_mfma_f32_16x16x32_bf16 v[88:91], v[240:243], v[116:119], v[88:91]\n\t"
    "v_mfma_f32_16x16x32_bf16 v[92:95], v[244:247], v[116:119], v[92:95]\n\t"
    "s_waitcnt lgkmcnt(0)\n\t"
    "v_mfma_f32_16x16x32_bf16 v[80:83], v[248:251], v[120:123], v[80:83]\n\t"
    "v_mfma_f32_16x16x32_bf16 v[84:87], v[252:255], v[120:123], v[84:87]\n\t"
    "v_mfma_f32_16x16x32_bf16 v[88:91], v[248:251], v[124:127], v[88:91]\n\t"
    "v_mfma_f32_16x16x32_bf16 v[92:95], v[252:255], v[124:127], v[92:95]\n\t"
    "s_cmp_eq_u32 %[ps], 0\n\t"
    "s_cbranch_scc1 10f\n\t"
    "global_load_dword %[sv], %[dv], %[db] sc1\n\t"
    "global_load_dword %[su], %[dv], %[db] offset:64 sc1\n\t"
    "10:\n\t"
    "s_mov_b32 %[got], 0\n\t"
    "v_mov_b32 v76, %[fa]\n\t"
    "v_mov_b32 v77, %[fv]\n\t"
    "v_add_u32 v78, 1, v77\n\t"
    "v_mov_b32 v79, %[ga]\n\t"
    "ds_write_b128 %[rw], v[80:83] offset:0\n\t"
    "ds_write_b128 %[rw], v[84:87] offset:1024\n\t"
    "ds_write_b128 %[rw], v[88:91] offset:2048\n\t"
    "ds_write_b128 %[rw], v[92:95] offset:3072\n\t"
    "ds_write_b32 v76, v77\n\t"
    "s_cmp_eq_u32 %[ps], 0\n\t"
    "s_cbranch_scc1 9f\n\t"
    "s_waitcnt vmcnt(0)\n\t"
    "s_branch 9f\n\t"
    "12:\n\t"
    "s_waitcnt lgkmcnt(0)\n\t"
    "9:"
    : [sv] "+&v"(sv), [su] "+&v"(su), [got] "=&s"(got)
    : [ea] "v"(ea), [da] "v"(da), [x] "s"(x), "{v[128:159]}"(W[0]), "{v[160:191]}"(W[1]), "{v[192:223]}"(W[2]), "{v[224:255]}"(W[3]), [lb] "s"(lb), [s0] "s"(s0), [s1] "s"(s1), [av0] "v"(av[0]), [av1] "v"(av[1]), [av2] "v"(av[2]), [av3] "v"(av[3]), [av4] "v"(av[4]), [av5] "v"(av[5]), [av6] "v"(av[6]), [av7] "v"(av[7]), [ps] "s"(ps), [dv] "v"(dv), [db] "s"(db), [rw] "v"(rw), [fa] "s"(fa), [fv] "s"(fv), [ga] "s"(ga), [mu] "s"(mu), [ad] "v"(ad), [th] "s"(th), [tb] "s"(tb)
    : "v96", "v97", "v98", "v99", "v100", "v101", "v102", "v103", "v104", "v105", "v106", "v107", "v108", "v109", "v110", "v111", "v112", "v113", "v114", "v115", "v116", "v117", "v118", "v119", "v120", "v121", "v122", "v123", "v124", "v125", "v126", "v127", "v76", "v77", "v78", "v79", "v80", "v81", "v82", "v83", "v84", "v85", "v86", "v87", "v88", "v89", "v90", "v91", "v92", "v93", "v94", "v95", "vcc", "scc", "m0", "memory");
}
__device__ __forceinline__ void lean_ld1(const u32x32 (&W)[4], unsigned ea, unsigned da, unsigned x, unsigned lb, const char* s0, const char* s1, const unsigned (&av)[8], unsigned ps, unsigned dv, const char* db, unsigned rw, unsigned fa, unsigned fv,
    unsigned ga, unsigned mu, unsigned ad, int th, int tb, unsigned& sv, unsigned& su, unsigned& got) {
  asm volatile("s_setprio 2\n\t"
    "v_mov_b32 v76, %[ga]\n\t"
    "ds_read_b32 v76, v76 offset:24\n\t"
    "ds_read_b128 v[96:99], %[ea] offset:0\n\t"
    "ds_read_b128 v[100:103], %[ea] offset:2048\n\t"
    "ds_read_b128 v[104:107], %[da] offset:0\n\t"
    "ds_read_b128 v[108:111], %[da] offset:2048\n\t"
    "ds_read_b128 v[112:115], %[ea] offset:4096\n\t"
    "ds_read_b128 v[116:119], %[ea] offset:6144\n\t"
    "s_waitcnt lgkmcnt(6)\n\t"
    "v_cmp_ne_u32 vcc, 0, v76\n\t"
    "s_mov_b32 %[got], -1\n\t"
    "s_cbranch_vccnz 12f\n\t"
    "s_waitcnt lgkmcnt(4)\n\t"
    "v_mfma_f32_16x16x32_bf16 v[80:83], v[128:131], v[96:99], 0\n\t"
    "ds_read_b128 v[120:123], %[da] offset:4096\n\t"
    "v_mfma_f32_16x16x32_bf16 v[84:87], v[132:135], v[96:99], 0\n\t"
    "ds_read_b128 v[124:127], %[da] offset:6144\n\t"
    "v_mfma_f32_16x16x32_bf16 v[88:91], v[128:131], v[100:103], 0\n\t"
    "v_mfma_f32_16x16x32_bf16 v[92:95], v[132:135], v[100:103], 0\n\t"
    "s_waitcnt vmcnt(12)\n\t"
    "s_cmp_lt_u32 %[x], 2\n\t"
    "s_cbranch_scc0 41f\n\t"
    "s_waitcnt vmcnt(4)\n\t"
    "41:\n\t"
    "s_barrier\n\t"
    "s_waitcnt lgkmcnt(4)\n\t"
    "v_mfma_f32_16x16x32_bf16 v[80:83], v[136:139], v[104:107], v[80:83]\n\t"
    "ds_read_b128 v[96:99], %[ea] offset:8192\n\t"
    "v_mfma_f32_16x16x32_bf16 v[84:87], v[140:143], v[104:107], v[84:87]\n\t"
    "ds_read_b128 v[100:103], %[ea] offset:10240\n\t"
    "v_mfma_f32_16x16x32_bf16 v[88:91], v[136:139], v[108:111], v[88:91]\n\t"
    "v_mfma_f32_16x16x32_bf16 v[92:95], v[140:143], v[108:111], v[92:95]\n\t"
    "s_waitcnt lgkmcnt(4)\n\t"
    "v_mfma_f32_16x16x32_bf16 v[80:83], v[144:147], v[112:115], v[80:83]\n\t"
    "ds_read_b128 v[104:107], %[da] offset:8192\n\t"
    "v_mfma_f32_16x16x32_bf16 v[84:87], v[148:151], v[112:115], v[84:87]\n\t"
    "ds_read_b128 v[108:111], %[da] offset:10240\n\t"
    "v_mfma_f32_16x16x32_bf16 v[88:91], v[144:147], v[116:119], v[88:91]\n\t"
    "v_mfma_f32_16x16x32_bf16 v[92:95], v[148:151], v[116:119], v[92:95]\n\t"
    "s_waitcnt lgkmcnt(4)\n\t"
    "v_mfma_f32_16x16x32_bf16 v[80:83], v[152:155], v[120:123], v[80:83]\n\t"
    "ds_read_b128 v[112:115], %[ea] offset:12288\n\t"
    "v_mfma_f32_16x16x32_bf16 v[84:87], v[156:159], v[120:123], v[84:87]\n\t"
    "ds_read_b128 v[116:119], %[ea] offset:14336\n\t"
    "v_mfma_f32_16x16x32_bf16 v[88:91], v[152:155], v[124:127], v[88:91]\n\t"
    "v_mfma_f32_16x16x32_bf16 v[92:95], v[156:159], v[124:127], v[92:95]\n\t"
    "s_waitcnt lgkmcnt(4)\n\t"
    "v_mfma_f32_16x16x32_bf16 v[80:83], v[160:163], v[96:99], v[80:83]\n\t"
    "ds_read_b128 v[120:123], %[da] offset:12288\n\t"
    "v_mfma_f32_16x16x32_bf16 v[84:87], v[164:167], v[96:99], v[84:87]\n\t"
    "ds_read_b128 v[124:127], %[da] offset:14336\n\t"
    "v_mfma_f32_16x16x32_bf16 v[88:91], v[160:163], v[100:103], v[88:91]\n\t"
    "v_mfma_f32_16x16x32_bf16 v[92:95], v[164:167], v[100:103], v[92:95]\n\t"
    "s_waitcnt vmcnt(10)\n\t"
    "s_cmp_lt_u32 %[x], 2\n\t"
    "s_cbranch_scc0 45f\n\t"
    "s_waitcnt vmcnt(2)\n\t"
    "45:\n\t"
    "s_barrier\n\t"
    "s_waitcnt lgkmcnt(4)\n\t"
    "v_mfma_f32_16x16x32_bf16 v[80:83], v[168:171], v[104:107], v[80:83]\n\t"
    "ds_read_b128 v[96:99], %[ea] offset:16384\n\t"
    "v_mfma_f32_16x16x32_bf16 v[84:87], v[172:175], v[104:107], v[84:87]\n\t"
    "ds_read_b128 v[100:103], %[ea] offset:18432\n\t"
    "v_mfma_f32_16x16x32_bf16 v[88:91], v[168:171], v[108:111], v[88:91]\n\t"
    "v_mfma_f32_16x16x32_bf16 v[92:95], v[172:175], v[108:111], v[92:95]\n\t"
    "s_waitcnt lgkmcnt(4)\n\t"
    "v_mfma_f32_16x16x32_bf16 v[80:83], v[176:179], v[112:115], v[80:83]\n\t"
    "ds_read_b128 v[104:107], %[da] offset:16384\n\t"
    "v_mfma_f32_16x16x32_bf16 v[84:87], v[180:183], v[112:115], v[84:87]\n\t"
    "ds_read_b128 v[108:111], %[da] offset:18432\n\t"
    "v_mfma_f32_16x16x32_bf16 v[88:91], v[176:179], v[116:119], v[88:91]\n\t"
    "v_mfma_f32_16x16x32_bf16 v[92:95], v[180:183], v[116:119], v[92:95]\n\t"
    "s_waitcnt lgkmcnt(4)\n\t"
    "v_mfma_f32_16x16x32_bf16 v[80:83], v[184:187], v[120:123], v[80:83]\n\t"
    "ds_read_b128 v[112:115], %[ea] offset:20480\n\t"
    "v_mfma_f32_16x16x32_bf16 v[84:87], v[188:191], v[120:123], v[84:87]\n\t"
    "ds_read_b128 v[116:119], %[ea] offset:22528\n\t"
    "v_mfma_f32_16x16x32_bf16 v[88:91], v[184:187], v[124:127], v[88:91]\n\t"
    "v_mfma_f32_16x16x32_bf16 v[92:95], v[188:191], v[124:127], v[92:95]\n\t"
    "s_waitcnt lgkmcnt(4)\n\t"
    "v_mfma_f32_16x16x32_bf16 v[80:83], v[192:195], v[96:99], v[80:83]\n\t"
    "ds_read_b128 v[120:123], %[da] offset:20480\n\t"
    "v_mfma_f32_16x16x32_bf16 v[84:87], v[196:199], v[96:99], v[84:87]\n\t"
    "ds_read_b128 v[124:127], %[da] offset:22528\n\t"
    "v_mfma_f32_16x16x32_bf16 v[88:91], v[192:195], v[100:103], v[88:91]\n\t"
    "v_mfma_f32_16x16x32_bf16 v[92:95], v[196:199], v[100:103], v[92:95]\n\t"
    "s_waitcnt vmcnt(8)\n\t"
    "s_cmp_lt_u32 %[x], 2\n\t"
    "s_cbranch_scc0 49f\n\t"
    "s_waitcnt vmcnt(0)\n\t"
    "49:\n\t"
    "s_barrier\n\t"
    "s_waitcnt lgkmcnt(4)\n\t"
    "v_mfma_f32_16x16x32_bf16 v[80:83], v[200:203], v[104:107], v[80:83]\n\t"
    "ds_read_b128 v[96:99], %[ea] offset:24576\n\t"
    "v_mfma_f32_16x16x32_bf16 v[84:87], v[204:207], v[104:107], v[84:87]\n\t"
    "ds_read_b128 v[100:103], %[ea] offset:26624\n\t"
    "v_mfma_f32_16x16x32_bf16 v[88:91], v[200:203], v[108:111], v[88:91]\n\t"
    "v_mfma_f32_16x16x32_bf16 v[92:95], v[204:207], v[108:111], v[92:95]\n\t"
    "s_waitcnt lgkmcnt(4)\n\t"
    "v_mfma_f32_16x16x32_bf16 v[80:83], v[208:211], v[112:115], v[80:83]\n\t"
    "ds_read_b128 v[104:107], %[da] offset:24576\n\t"
    "v_mfma_f32_16x16x32_bf16 v[84:87], v[212:215], v[112:115], v[84:87]\n\t"
    "ds_read_b128 v[108:111], %[da] offset:26624\n\t"
    "v_mfma_f32_16x16x32_bf16 v[88:91], v[208:211], v[116:119], v[88:91]\n\t"
    "v_mfma_f32_16x16x32_bf16 v[92:95], v[212:215], v[116:119], v[92:95]\n\t"
    "s_cmp_eq_u32 %[ps], 0\n\t"
    "s_cbranch_scc1 10f\n\t"
    "global_load_dword %[sv], %[dv], %[db] sc1\n\t"
    "global_load_dword %[su], %[dv], %[db] offset:64 sc1\n\t"
    "10:\n\t"
    "s_waitcnt lgkmcnt(4)\n\t"
    "v_mfma_f32_16x16x32_bf16 v[80:83], v[216:219], v[120:123], v[80:83]\n\t"
    "ds_read_b128 v[112:115], %[ea] offset:28672\n\t"
    "v_mfma_f32_16x16x32_bf16 v[84:87], v[220:223], v[120:123], v[84:87]\n\t"
    "ds_read_b128 v[116:119], %[ea] offset:30720\n\t"
    "v_mfma_f32_16x16x32_bf16 v[88:91], v[216:219], v[124:127], v[88:91]\n\t"
    "v_mfma_f32_16x16x32_bf16 v[92:95], v[220:223], v[124:127], v[92:95]\n\t"
    "s_waitcnt lgkmcnt(4)\n\t"
    "v_mfma_f32_16x16x32_bf16 v[80:83], v[224:227], v[96:99], v[80:83]\n\t"
    "ds_read_b128 v[120:123], %[da] offset:28672\n\t"
    "v_mfma_f32_16x16x32_bf16 v[84:87], v[228:231], v[96:99], v[84:87]\n\t"
    "ds_read_b128 v[124:127], %[da] offset:30720\n\t"
    "v_mfma_f32_16x16x32_bf16 v[88:91], v[224:227], v[100:103], v[88:91]\n\t"
    "v_mfma_f32_16x16x32_bf16 v[92:95], v[228:231], v[100:103], v[92:95]\n\t"
    "s_waitcnt lgkmcnt(4)\n\t"
    "v_mfma_f32_16x16x32_bf16 v[80:83], v[232:235], v[104:107], v[80:83]\n\t"
    "v_mfma_f32_16x16x32_bf16 v[84:87], v[236:239], v[104:107], v[84:87]\n\t"
    "v_mfma_f32_16x16x32_bf16 v[88:91], v[232:235], v[108:111], v[88:91]\n\t"
    "v_mfma_f32_16x16x32_bf16 v[92:95], v[236:239], v[108:111], v[92:95]\n\t"
    "s_waitcnt lgkmcnt(2)\n\t"
    "v_mfma_f32_16x16x32_bf16 v[80:83], v[240:243], v[112:115], v[80:83]\n\t"
    "v_mfma_f32_16x16x32_bf16 v[84:87], v[244:247], v[112:115], v[84:87]\n\t"
    "v_mfma_f32_16x16x32_bf16 v[88:91], v[240:243], v[116:119], v[88:91]\n\t"
    "v_mfma_f32_16x16x32_bf16 v[92:95], v[244:247], v[116:119], v[92:95]\n\t"
    "s_waitcnt lgkmcnt(0)\n\t"
    "v_mfma_f32_16x16x32_bf16 v[80:83], v[248:251], v[120:123], v[80:83]\n\t"
    "s_memrealtime s[36:37]\n\t"
    "v_mfma_f32_16x16x32_bf16 v[84:87], v[252:255], v[120:123], v[84:87]\n\t"
    "v_mfma_f32_16x16x32_bf16 v[88:91], v[248:251], v[124:127], v[88:91]\n\t"
    "v_mfma_f32_16x16x32_bf16 v[92:95], v[252:255], v[124:127], v[92:95]\n\t"
    "s_mov_b32 %[got], 0\n\t"
    "v_mov_b32 v76, %[fa]\n\t"
    "v_mov_b32 v77, %[fv]\n\t"
    "v_add_u32 v78, 1, v77\n\t"
    "v_mov_b32 v79, %[ga]\n\t"
    "s_waitcnt lgkmcnt(0)\n\t"
    "ds_write_b128 %[rw], v[80:83] offset:0\n\t"
    "ds_write_b128 %[rw], v[84:87] offset:1024\n\t"
    "ds_write_b128 %[rw], v[88:91] offset:2048\n\t"
    "ds_write_b128 %[rw], v[92:95] offset:3072\n\t"
    "ds_write_b32 v76, v77\n\t"
    "s_cmp_eq_u32 %[ps], 0\n\t"
    "s_cbranch_scc1 9f\n\t"
    "s_waitcnt vmcnt(0)\n\t"
    "v_mad_u32_u24 v76, %[sv], %[mu], %[ad]\n\t"
    "v_cmp_ge_i32 vcc, %[th], v76\n\t"
    "s_cbranch_vccz 11f\n\t"
    "s_sub_u32 s36, s36, 20\n\t"
    "v_sub_u32 v76, s36, %[su]\n\t"
    "v_lshrrev_b32 v77, 24, %[su]\n\t"
    "v_lshlrev_b32 v76, 8, v76\n\t"
    "v_ashrrev_i32 v76, 31, v76\n\t"
    "v_add_u32 v77, v77, v76\n\t"
    "v_cmp_lt_i32 s[36:37], %[tb], v77\n\t"
    "s_andn2_b64 vcc, vcc, s[36:37]\n\t"
    "s_cbranch_scc1 9f\n\t"
    "11:\n\t"
    "ds_write2_b32 v79, v78, v78 offset1:1\n\t"
    "s_add_u32 m0, %[lb], 0\n\t"
    "s_nop 0\n\t"
    "global_load_lds_dwordx4 %[av0], %[s0] sc1\n\t"
    "s_add_u32 m0, %[lb], 4096\n\t"
    "s_nop 0\n\t"
    "global_load_lds_dwordx4 %[av2], %[s0] sc1\n\t"
    "s_add_u32 m0, %[lb], 8192\n\t"
    "s_nop 0\n\t"
    "global_load_lds_dwordx4 %[av4], %[s0] sc1\n\t"
    "s_add_u32 m0, %[lb], 12288\n\t"
    "s_nop 0\n\t"
    "global_load_lds_dwordx4 %[av6], %[s0] sc1\n\t"
    "s_add_u32 m0, %[lb], 16384\n\t"
    "s_nop 0\n\t"
    "global_load_lds_dwordx4 %[av0], %[s1] sc1\n\t"
    "s_add_u32 m0, %[lb], 20480\n\t"
    "s_nop 0\n\t"
    "global_load_lds_dwordx4 %[av2], %[s1] sc1\n\t"
    "s_add_u32 m0, %[lb], 24576\n\t"
    "s_nop 0\n\t"
    "global_load_lds_dwordx4 %[av4], %[s1] sc1\n\t"
    "s_add_u32 m0, %[lb], 28672\n\t"
    "s_nop 0\n\t"
    "global_load_lds_dwordx4 %[av6], %[s1] sc1\n\t"
    "s_mov_b32 %[got], 1\n\t"
    "s_branch 9f\n\t"
    "12:\n\t"
    "s_waitcnt lgkmcnt(0)\n\t"
    "9:"
    : [sv] "+&v"(sv), [su] "+&v"(su), [got] "=&s"(got)
    : [ea] "v"(ea), [da] "v"(da), [x] "s"(x), "{v[128:159]}"(W[0]), "{v[160:191]}"(W[1]), "{v[192:223]}"(W[2]), "{v[224:255]}"(W[3]), [lb] "s"(lb), [s0] "s"(s0), [s1] "s"(s1), [av0] "v"(av[0]), [av1] "v"(av[1]), [av2] "v"(av[2]), [av3] "v"(av[3]), [av4] "v"(av[4]), [av5] "v"(av[5]), [av6] "v"(av[6]), [av7] "v"(av[7]), [ps] "s"(ps), [dv] "v"(dv), [db] "s"(db), [rw] "v"(rw), [fa] "s"(fa), [fv] "s"(fv), [ga] "s"(ga), [mu] "s"(mu), [ad] "v"(ad), [th] "s"(th), [tb] "s"(tb)
    : "v96", "v97", "v98", "v99", "v100", "v101", "v102", "v103", "v104", "v105", "v106", "v107", "v108", "v109", "v110", "v111", "v112", "v113", "v114", "v115", "v116", "v117", "v118", "v119", "v120", "v121", "v122", "v123", "v124", "v125", "v126", "v127", "v76", "v77", "v78", "v79", "v80", "v81", "v82", "v83", "v84", "v85", "v86", "v87", "v88", "v89", "v90", "v91", "v92", "v93", "v94", "v95", "vcc", "scc", "m0", "memory", "s36", "s37", "s38", "s39");
}
__device__ __forceinline__ void lean_dm1(const u32x32 (&W)[4], unsigned ea, unsigned da, unsigned x, unsigned lb, const char* s0, const char* s1, const unsigned (&av)[8], unsigned ps, unsigned dv, const char* db, unsigned rw, unsigned fa, unsigned fv,
    unsigned ga, unsigned mu, unsigned ad, int th, int tb, unsigned& sv, unsigned& su, unsigned& got) {
  asm volatile("s_setprio 2\n\t"
    "v_mov_b32 v76, %[ga]\n\t"
    "ds_read_b32 v76, v76 offset:24\n\t"
    "ds_read_b128 v[96:99], %[ea] offset:0\n\t"
    "ds_read_b128 v[100:103], %[ea] offset:2048\n\t"
    "ds_read_b128 v[104:107], %[da] offset:0\n\t"
    "ds_read_b128 v[108:111], %[da] offset:2048\n\t"
    "ds_read_b128 v[112:115], %[ea] offset:4096\n\t"
    "ds_read_b128 v[116:119], %[ea] offset:6144\n\t"
    "s_waitcnt lgkmcnt(6)\n\t"
    "v_cmp_ne_u32 vcc, 0, v76\n\t"
    "s_mov_b32 %[got], -1\n\t"
    "s_cbranch_vccnz 12f\n\t"
    "s_waitcnt lgkmcnt(4)\n\t"
    "v_mfma_f32_16x16x32_bf16 v[80:83], v[128:131], v[96:99], 0\n\t"
    "ds_read_b128 v[120:123], %[da] offset:4096\n\t"
    "s_add_u32 m0, %[lb], 0\n\t"
    "v_mfma_f32_16x16x32_bf16 v[84:87], v[132:135], v[96:99], 0\n\t"
    "ds_read_b128 v[124:127], %[da] offset:6144\n\t"
    "global_load_lds_dwordx4 %[av0], %[s0] sc1\n\t"
    "v_mfma_f32_16x16x32_bf16 v[88:91], v[128:131], v[100:103], 0\n\t"
    "v_mfma_f32_16x16x32_bf16 v[92:95], v[132:135], v[100:103], 0\n\t"
    "s_waitcnt vmcnt(5)\n\t"
    "s_barrier\n\t"
    "s_waitcnt lgkmcnt(4)\n\t"
    "v_mfma_f32_16x16x32_bf16 v[80:83], v[136:139], v[104:107], v[80:83]\n\t"
    "ds_read_b128 v[96:99], %[ea] offset:8192\n\t"
    "s_add_u32 m0, %[lb], 4096\n\t"
    "v_mfma_f32_16x16x32_bf16 v[84:87], v[140:143], v[104:107], v[84:87]\n\t"
    "ds_read_b128 v[100:103], %[ea] offset:10240\n\t"
    "global_load_lds_dwordx4 %[av2], %[s0] sc1\n\t"
    "v_mfma_f32_16x16x32_bf16 v[88:91], v[136:139], v[108:111], v[88:91]\n\t"
    "v_mfma_f32_16x16x32_bf16 v[92:95], v[140:143], v[108:111], v[92:95]\n\t"
    "s_waitcnt lgkmcnt(4)\n\t"
    "v_mfma_f32_16x16x32_bf16 v[80:83], v[144:147], v[112:115], v[80:83]\n\t"
    "ds_read_b128 v[104:107], %[da] offset:8192\n\t"
    "s_add_u32 m0, %[lb], 8192\n\t"
    "v_mfma_f32_16x16x32_bf16 v[84:87], v[148:151], v[112:115], v[84:87]\n\t"
    "ds_read_b128 v[108:111], %[da] offset:10240\n\t"
    "global_load_lds_dwordx4 %[av4], %[s0] sc1\n\t"
    "v_mfma_f32_16x16x32_bf16 v[88:91], v[144:147], v[116:119], v[88:91]\n\t"
    "v_mfma_f32_16x16x32_bf16 v[92:95], v[148:151], v[116:119], v[92:95]\n\t"
    "s_waitcnt lgkmcnt(4)\n\t"
    "v_mfma_f32_16x16x32_bf16 v[80:83], v[152:155], v[120:123], v[80:83]\n\t"
    "ds_read_b128 v[112:115], %[ea] offset:12288\n\t"
    "s_add_u32 m0, %[lb], 12288\n\t"
    "v_mfma_f32_16x16x32_bf16 v[84:87], v[156:159], v[120:123], v[84:87]\n\t"
    "ds_read_b128 v[116:119], %[ea] offset:14336\n\t"
    "global_load_lds_dwordx4 %[av6], %[s0] sc1\n\t"
    "v_mfma_f32_16x16x32_bf16 v[88:91], v[152:155], v[124:127], v[88:91]\n\t"
    "v_mfma_f32_16x16x32_bf16 v[92:95], v[156:159], v[124:127], v[92:95]\n\t"
    "s_waitcnt lgkmcnt(4)\n\t"
    "v_mfma_f32_16x16x32_bf16 v[80:83], v[160:163], v[96:99], v[80:83]\n\t"
    "ds_read_b128 v[120:123], %[da] offset:12288\n\t"
    "s_add_u32 m0, %[lb], 16384\n\t"
    "v_mfma_f32_16x16x32_bf16 v[84:87], v[164:167], v[96:99], v[84:87]\n\t"
    "ds_read_b128 v[124:127], %[da] offset:14336\n\t"
    "global_load_lds_dwordx4 %[av0], %[s1] sc1\n\t"
    "v_mfma_f32_16x16x32_bf16 v[88:91], v[160:163], v[100:103], v[88:91]\n\t"
    "v_mfma_f32_16x16x32_bf16 v[92:95], v[164:167], v[100:103], v[92:95]\n\t"
    "s_waitcnt vmcnt(7)\n\t"
    "s_barrier\n\t"
    "s_waitcnt lgkmcnt(4)\n\t"
    "v_mfma_f32_16x16x32_bf16 v[80:83], v[168:171], v[104:107], v[80:83]\n\t"
    "ds_read_b128 v[96:99], %[ea] offset:16384\n\t"
    "s_add_u32 m0, %[lb], 20480\n\t"
    "v_mfma_f32_16x16x32_bf16 v[84:87], v[172:175], v[104:107], v[84:87]\n\t"
    "ds_read_b128 v[100:103], %[ea] offset:18432\n\t"
    "global_load_lds_dwordx4 %[av2], %[s1] sc1\n\t"
    "v_mfma_f32_16x16x32_bf16 v[88:91], v[168:171], v[108:111], v[88:91]\n\t"
    "v_mfma_f32_16x16x32_bf16 v[92:95], v[172:175], v[108:111], v[92:95]\n\t"
    "s_waitcnt lgkmcnt(4)\n\t"
    "v_mfma_f32_16x16x32_bf16 v[80:83], v[176:179], v[112:115], v[80:83]\n\t"
    "ds_read_b128 v[104:107], %[da] offset:16384\n\t"
    "s_add_u32 m0, %[lb], 24576\n\t"
    "v_mfma_f32_16x16x32_bf16 v[84:87], v[180:183], v[112:115], v[84:87]\n\t"
    "ds_read_b128 v[108:111], %[da] offset:18432\n\t"
    "global_load_lds_dwordx4 %[av4], %[s1] sc1\n\t"
    "v_mfma_f32_16x16x32_bf16 v[88:91], v[176:179], v[116:119], v[88:91]\n\t"
    "v_mfma_f32_16x16x32_bf16 v[92:95], v[180:183], v[116:119], v[92:95]\n\t"
    "s_waitcnt lgkmcnt(4)\n\t"
    "v_mfma_f32_16x16x32_bf16 v[80:83], v[184:187], v[120:123], v[80:83]\n\t"
    "ds_read_b128 v[112:115], %[ea] offset:20480\n\t"
    "s_add_u32 m0, %[lb], 28672\n\t"
    "v_mfma_f32_16x16x32_bf16 v[84:87], v[188:191], v[120:123], v[84:87]\n\t"
    "ds_read_b128 v[116:119], %[ea] offset:22528\n\t"
    "global_load_lds_dwordx4 %[av6], %[s1] sc1\n\t"
    "v_mfma_f32_16x16x32_bf16 v[88:91], v[184:187], v[124:127], v[88:91]\n\t"
    "v_mfma_f32_16x16x32_bf16 v[92:95], v[188:191], v[124:127], v[92:95]\n\t"
    "s_waitcnt lgkmcnt(4)\n\t"
    "v_mfma_f32_16x16x32_bf16 v[80:83], v[192:195], v[96:99], v[80:83]\n\t"
    "ds_read_b128 v[120:123], %[da] offset:20480\n\t"
    "v_mfma_f32_16x16x32_bf16 v[84:87], v[196:199], v[96:99], v[84:87]\n\t"
    "ds_read_b128 v[124:127], %[da] offset:22528\n\t"
    "v_mfma_f32_16x16x32_bf16 v[88:91], v[192:195], v[100:103], v[88:91]\n\t"
    "v_mfma_f32_16x16x32_bf16 v[92:95], v[196:199], v[100:103], v[92:95]\n\t"
    "s_waitcnt vmcnt(8)\n\t"
    "s_barrier\n\t"
    "s_waitcnt lgkmcnt(4)\n\t"
    "v_mfma_f32_16x16x32_bf16 v[80:83], v[200:203], v[104:107], v[80:83]\n\t"
    "ds_read_b128 v[96:99], %[ea] offset:24576\n\t"
    "v_mfma_f32_16x16x32_bf16 v[84:87], v[204:207], v[104:107], v[84:87]\n\t"
    "ds_read_b128 v[100:103], %[ea] offset:26624\n\t"
    "v_mfma_f32_16x16x32_bf16 v[88:91], v[200:203], v[108:111], v[88:91]\n\t"
    "v_mfma_f32_16x16x32_bf16 v[92:95], v[204:207], v[108:111], v[92:95]\n\t"
    "s_waitcnt lgkmcnt(4)\n\t"
    "v_mfma_f32_16x16x32_bf16 v[80:83], v[208:211], v[112:115], v[80:83]\n\t"
    "ds_read_b128 v[104:107], %[da] offset:24576\n\t"
    "v_mfma_f32_16x16x32_bf16 v[84:87], v[212:215], v[112:115], v[84:87]\n\t"
    "ds_read_b128 v[108:111], %[da] offset:26624\n\t"
    "v_mfma_f32_16x16x32_bf16 v[88:91], v[208:211], v[116:119], v[88:91]\n\t"
    "v_mfma_f32_16x16x32_bf16 v[92:95], v[212:215], v[116:119], v[92:95]\n\t"
    "s_waitcnt lgkmcnt(4)\n\t"
    "v_mfma_f32_16x16x32_bf16 v[80:83], v[216:219], v[120:123], v[80:83]\n\t"
    "ds_read_b128 v[112:115], %[ea] offset:28672\n\t"
    "v_mfma_f32_16x16x32_bf16 v[84:87], v[220:223], v[120:123], v[84:87]\n\t"
    "ds_read_b128 v[116:119], %[ea] offset:30720\n\t"
    "v_mfma_f32_16x16x32_bf16 v[88:91], v[216:219], v[124:127], v[88:91]\n\t"
    "v_mfma_f32_16x16x32_bf16 v[92:95], v[220:223], v[124:127], v[92:95]\n\t"
    "s_waitcnt lgkmcnt(4)\n\t"
    "v_mfma_f32_16x16x32_bf16 v[80:83], v[224:227], v[96:99], v[80:83]\n\t"
    "ds_read_b128 v[120:123], %[da] offset:28672\n\t"
    "v_mfma_f32_16x16x32_bf16 v[84:87], v[228:231], v[96:99], v[84:87]\n\t"
    "ds_read_b128 v[124:127], %[da] offset:30720\n\t"
    "v_mfma_f32_16x16x32_bf16 v[88:91], v[224:227], v[100:103], v[88:91]\n\t"
    "v_mfma_f32_16x16x32_bf16 v[92:95], v[228:231], v[100:103], v[92:95]\n\t"
    "s_waitcnt lgkmcnt(4)\n\t"
    "v_mfma_f32_16x16x32_bf16 v[80:83], v[232:235], v[104:107], v[80:83]\n\t"
    "v_mfma_f32_16x16x32_bf16 v[84:87], v[236:239], v[104:107], v[84:87]\n\t"
    "v_mfma_f32_16x16x32_bf16 v[88:91], v[232:235], v[108:111], v[88:91]\n\t"
    "v_mfma_f32_16x16x32_bf16 v[92:95], v[236:239], v[108:111], v[92:95]\n\t"
    "s_waitcnt lgkmcnt(2)\n\t"
    "v_mfma_f32_16x16x32_bf16 v[80:83], v[240:243], v[112:115], v[80:83]\n\t"
    "v_mfma_f32_16x16x32_bf16 v[84:87], v[244:247], v[112:115], v[84:87]\n\t"
    "v_mfma_f32_16x16x32_bf16 v[88:91], v[240:243], v[116:119], v[88:91]\n\t"
    "v_mfma_f32_16x16x32_bf16 v[92:95], v[244:247], v[116:119], v[92:95]\n\t"
    "s_waitcnt lgkmcnt(0)\n\t"
    "v_mfma_f32_16x16x32_bf16 v[80:83], v[248:251], v[120:123], v[80:83]\n\t"
    "v_mfma_f32_16x16x32_bf16 v[84:87], v[252:255], v[120:123], v[84:87]\n\t"
    "v_mfma_f32_16x16x32_bf16 v[88:91], v[248:251], v[124:127], v[88:91]\n\t"
    "v_mfma_f32_16x16x32_bf16 v[92:95], v[252:255], v[124:127], v[92:95]\n\t"
    "s_cmp_eq_u32 %[ps], 0\n\t"
    "s_cbranch_scc1 10f\n\t"
    "global_load_dword %[sv], %[dv], %[db] sc1\n\t"
    "global_load_dword %[su], %[dv], %[db] offset:64 sc1\n\t"
    "10:\n\t"
    "s_mov_b32 %[got], 0\n\t"
    "v_mov_b32 v76, %[fa]\n\t"
    "v_mov_b32 v77, %[fv]\n\t"
    "v_add_u32 v78, 1, v77\n\t"
    "v_mov_b32 v79, %[ga]\n\t"
    "ds_write_b128 %[rw], v[80:83] offset:0\n\t"
    "ds_write_b128 %[rw], v[84:87] offset:1024\n\t"
    "ds_write_b128 %[rw], v[88:91] offset:2048\n\t"
    "ds_write_b128 %[rw], v[92:95] offset:3072\n\t"
    "ds_write_b32 v76, v77\n\t"
    "s_cmp_eq_u32 %[ps], 0\n\t"
    "s_cbranch_scc1 9f\n\t"
    "s_waitcnt vmcnt(0)\n\t"
    "s_branch 9f\n\t"
    "12:\n\t"
    "s_waitcnt lgkmcnt(0)\n\t"
    "9:"
    : [sv] "+&v"(sv), [su] "+&v"(su), [got] "=&s"(got)
    : [ea] "v"(ea), [da] "v"(da), [x] "s"(x), "{v[128:159]}"(W[0]), "{v[160:191]}"(W[1]), "{v[192:223]}"(W[2]), "{v[224:255]}"(W[3]), [lb] "s"(lb), [s0] "s"(s0), [s1] "s"(s1), [av0] "v"(av[0]), [av1] "v"(av[1]), [av2] "v"(av[2]), [av3] "v"(av[3]), [av4] "v"(av[4]), [av5] "v"(av[5]), [av6] "v"(av[6]), [av7] "v"(av[7]), [ps] "s"(ps), [dv] "v"(dv), [db] "s"(db), [rw] "v"(rw), [fa] "s"(fa), [fv] "s"(fv), [ga] "s"(ga), [mu] "s"(mu), [ad] "v"(ad), [th] "s"(th), [tb] "s"(tb)
    : "v96", "v97", "v98", "v99", "v100", "v101", "v102", "v103", "v104", "v105", "v106", "v107", "v108", "v109", "v110", "v111", "v112", "v113", "v114", "v115", "v116", "v117", "v118", "v119", "v120", "v121", "v122", "v123", "v124", "v125", "v126", "v127", "v76", "v77", "v78", "v79", "v80", "v81", "v82", "v83", "v84", "v85", "v86", "v87", "v88", "v89", "v90", "v91", "v92", "v93", "v94", "v95", "vcc", "scc", "m0", "memory");
}
__device__ __forceinline__ void lean_st0(const u32x32 (&W)[4], unsigned ea, unsigned da, unsigned x, u32x16& pd, unsigned pv, unsigned cn, const char* pg, unsigned bl, unsigned& tk, unsigned& st) {
  asm volatile("v_mov_b32 v76, %[cn]\n\t"
    "ds_read_b32 v76, v76 offset:28\n\t"
    "ds_read_b128 v[96:99], %[ea] offset:0\n\t"
    "ds_read_b128 v[100:103], %[ea] offset:2048\n\t"
    "ds_read_b128 v[104:107], %[da] offset:0\n\t"
    "ds_read_b128 v[108:111], %[da] offset:2048\n\t"
    "ds_read_b128 v[112:115], %[ea] offset:4096\n\t"
    "ds_read_b128 v[116:119], %[ea] offset:6144\n\t"
    "s_waitcnt lgkmcnt(6)\n\t"
    "v_cmp_ne_u32 vcc, 0, v76\n\t"
    "s_mov_b32 %[st], -1\n\t"
    "s_cbranch_vccnz 12f\n\t"
    "s_waitcnt lgkmcnt(4)\n\t"
    "v_mfma_f32_16x16x32_bf16 v[80:83], v[128:131], v[96:99], 0\n\t"
    "ds_read_b128 v[120:123], %[da] offset:4096\n\t"
    "v_mfma_f32_16x16x32_bf16 v[84:87], v[132:135], v[96:99], 0\n\t"
    "ds_read_b128 v[124:127], %[da] offset:6144\n\t"
    "v_mfma_f32_16x16x32_bf16 v[88:91], v[128:131], v[100:103], 0\n\t"
    "v_mfma_f32_16x16x32_bf16 v[92:95], v[132:135], v[100:103], 0\n\t"
    "s_barrier\n\t"
    "s_bitcmp1_b32 %[x], 2\n\t"
    "s_cbranch_scc1 7f\n\t"
    "8:\n\t"
    "s_bitcmp1_b32 %[x], 3\n\t"
    "s_cbranch_scc1 13f\n\t"
    "14:\n\t"
    "s_waitcnt lgkmcnt(4)\n\t"
    "v_mfma_f32_16x16x32_bf16 v[80:83], v[136:139], v[104:107], v[80:83]\n\t"
    "ds_read_b128 v[96:99], %[ea] offset:8192\n\t"
    "v_mfma_f32_16x16x32_bf16 v[84:87], v[140:143], v[104:107], v[84:87]\n\t"
    "ds_read_b128 v[100:103], %[ea] offset:10240\n\t"
    "v_mfma_f32_16x16x32_bf16 v[88:91], v[136:139], v[108:111], v[88:91]\n\t"
    "v_mfma_f32_16x16x32_bf16 v[92:95], v[140:143], v[108:111], v[92:95]\n\t"
    "s_waitcnt lgkmcnt(4)\n\t"
    "v_mfma_f32_16x16x32_bf16 v[80:83], v[144:147], v[112:115], v[80:83]\n\t"
    "ds_read_b128 v[104:107], %[da] offset:8192\n\t"
    "v_mfma_f32_16x16x32_bf16 v[84:87], v[148:151], v[112:115], v[84:87]\n\t"
    "ds_read_b128 v[108:111], %[da] offset:10240\n\t"
    "v_mfma_f32_16x16x32_bf16 v[88:91], v[144:147], v[116:119], v[88:91]\n\t"
    "v_mfma_f32_16x16x32_bf16 v[92:95], v[148:151], v[116:119], v[92:95]\n\t"
    "s_waitcnt lgkmcnt(4)\n\t"
    "v_mfma_f32_16x16x32_bf16 v[80:83], v[152:155], v[120:123], v[80:83]\n\t"
    "ds_read_b128 v[112:115], %[ea] offset:12288\n\t"
    "v_mfma_f32_16x16x32_bf16 v[84:87], v[156:159], v[120:123], v[84:87]\n\t"
    "ds_read_b128 v[116:119], %[ea] offset:14336\n\t"
    "v_mfma_f32_16x16x32_bf16 v[88:91], v[152:155], v[124:127], v[88:91]\n\t"
    "v_mfma_f32_16x16x32_bf16 v[92:95], v[156:159], v[124:127], v[92:95]\n\t"
    "s_waitcnt lgkmcnt(4)\n\t"
    "v_mfma_f32_16x16x32_bf16 v[80:83], v[160:163], v[96:99], v[80:83]\n\t"
    "ds_read_b128 v[120:123], %[da] offset:12288\n\t"
    "v_mfma_f32_16x16x32_bf16 v[84:87], v[164:167], v[96:99], v[84:87]\n\t"
    "ds_read_b128 v[124:127], %[da] offset:14336\n\t"
    "v_mfma_f32_16x16x32_bf16 v[88:91], v[160:163], v[100:103], v[88:91]\n\t"
    "v_mfma_f32_16x16x32_bf16 v[92:95], v[164:167], v[100:103], v[92:95]\n\t"
    "s_barrier\n\t"
    "s_waitcnt lgkmcnt(4)\n\t"
    "v_mfma_f32_16x16x32_bf16 v[80:83], v[168:171], v[104:107], v[80:83]\n\t"
    "ds_read_b128 v[96:99], %[ea] offset:16384\n\t"
    "v_mfma_f32_16x16x32_bf16 v[84:87], v[172:175], v[104:107], v[84:87]\n\t"
    "ds_read_b128 v[100:103], %[ea] offset:18432\n\t"
    "v_mfma_f32_16x16x32_bf16 v[88:91], v[168:171], v[108:111], v[88:91]\n\t"
    "v_mfma_f32_16x16x32_bf16 v[92:95], v[172:175], v[108:111], v[92:95]\n\t"
    "s_waitcnt lgkmcnt(4)\n\t"
    "v_mfma_f32_16x16x32_bf16 v[80:83], v[176:179], v[112:115], v[80:83]\n\t"
    "ds_read_b128 v[104:107], %[da] offset:16384\n\t"
    "v_mfma_f32_16x16x32_bf16 v[84:87], v[180:183], v[112:115], v[84:87]\n\t"
    "ds_read_b128 v[108:111], %[da] offset:18432\n\t"
    "v_mfma_f32_16x16x32_bf16 v[88:91], v[176:179], v[116:119], v[88:91]\n\t"
    "v_mfma_f32_16x16x32_bf16 v[92:95], v[180:183], v[116:119], v[92:95]\n\t"
    "s_waitcnt lgkmcnt(4)\n\t"
    "v_mfma_f32_16x16x32_bf16 v[80:83], v[184:187], v[120:123], v[80:83]\n\t"
    "ds_read_b128 v[112:115], %[ea] offset:20480\n\t"
    "v_mfma_f32_16x16x32_bf16 v[84:87], v[188:191], v[120:123], v[84:87]\n\t"
    "ds_read_b128 v[116:119], %[ea] offset:22528\n\t"
    "v_mfma_f32_16x16x32_bf16 v[88:91], v[184:187], v[124:127], v[88:91]\n\t"
    "v_mfma_f32_16x16x32_bf16 v[92:95], v[188:191], v[124:127], v[92:95]\n\t"
    "s_waitcnt lgkmcnt(4)\n\t"
    "v_mfma_f32_16x16x32_bf16 v[80:83], v[192:195], v[96:99], v[80:83]\n\t"
    "ds_read_b128 v[120:123], %[da] offset:20480\n\t"
    "v_mfma_f32_16x16x32_bf16 v[84:87], v[196:199], v[96:99], v[84:87]\n\t"
    "ds_read_b128 v[124:127], %[da] offset:22528\n\t"
    "v_mfma_f32_16x16x32_bf16 v[88:91], v[192:195], v[100:103], v[88:91]\n\t"
    "v_mfma_f32_16x16x32_bf16 v[92:95], v[196:199], v[100:103], v[92:95]\n\t"
    "s_barrier\n\t"
    "s_and_b32 %[st], %[x], 3\n\t"
    "s_cbranch_scc1 3f\n\t"
    "4:\n\t"
    "s_waitcnt lgkmcnt(4)\n\t"
    "v_mfma_f32_16x16x32_bf16 v[80:83], v[200:203], v[104:107], v[80:83]\n\t"
    "ds_read_b128 v[96:99], %[ea] offset:24576\n\t"
    "v_mfma_f32_16x16x32_bf16 v[84:87], v[204:207], v[104:107], v[84:87]\n\t"
    "ds_read_b128 v[100:103], %[ea] offset:26624\n\t"
    "v_mfma_f32_16x16x32_bf16 v[88:91], v[200:203], v[108:111], v[88:91]\n\t"
    "v_mfma_f32_16x16x32_bf16 v[92:95], v[204:207], v[108:111], v[92:95]\n\t"
    "s_waitcnt lgkmcnt(4)\n\t"
    "v_mfma_f32_16x16x32_bf16 v[80:83], v[208:211], v[112:115], v[80:83]\n\t"
    "ds_read_b128 v[104:107], %[da] offset:24576\n\t"
    "v_mfma_f32_16x16x32_bf16 v[84:87], v[212:215], v[112:115], v[84:87]\n\t"
    "ds_read_b128 v[108:111], %[da] offset:26624\n\t"
    "v_mfma_f32_16x16x32_bf16 v[88:91], v[208:211], v[116:119], v[88:91]\n\t"
    "v_mfma_f32_16x16x32_bf16 v[92:95], v[212:215], v[116:119], v[92:95]\n\t"
    "s_waitcnt lgkmcnt(4)\n\t"
    "v_mfma_f32_16x16x32_bf16 v[80:83], v[216:219], v[120:123], v[80:83]\n\t"
    "ds_read_b128 v[112:115], %[ea] offset:28672\n\t"
    "v_mfma_f32_16x16x32_bf16 v[84:87], v[220:223], v[120:123], v[84:87]\n\t"
    "ds_read_b128 v[116:119], %[ea] offset:30720\n\t"
    "v_mfma_f32_16x16x32_bf16 v[88:91], v[216:219], v[124:127], v[88:91]\n\t"
    "v_mfma_f32_16x16x32_bf16 v[92:95], v[220:223], v[124:127], v[92:95]\n\t"
    "s_waitcnt lgkmcnt(4)\n\t"
    "v_mfma_f32_16x16x32_bf16 v[80:83], v[224:227], v[96:99], v[80:83]\n\t"
    "ds_read_b128 v[120:123], %[da] offset:28672\n\t"
    "v_mfma_f32_16x16x32_bf16 v[84:87], v[228:231], v[96:99], v[84:87]\n\t"
    "ds_read_b128 v[124:127], %[da] offset:30720\n\t"
    "v_mfma_f32_16x16x32_bf16 v[88:91], v[224:227], v[100:103], v[88:91]\n\t"
    "v_mfma_f32_16x16x32_bf16 v[92:95], v[228:231], v[100:103], v[92:95]\n\t"
    "s_waitcnt lgkmcnt(4)\n\t"
    "v_mfma_f32_16x16x32_bf16 v[80:83], v[232:235], v[104:107], v[80:83]\n\t"
    "v_mfma_f32_16x16x32_bf16 v[84:87], v[236:239], v[104:107], v[84:87]\n\t"
    "v_mfma_f32_16x16x32_bf16 v[88:91], v[232:235], v[108:111], v[88:91]\n\t"
    "v_mfma_f32_16x16x32_bf16 v[92:95], v[236:239], v[108:111], v[92:95]\n\t"
    "s_waitcnt lgkmcnt(2)\n\t"
    "v_mfma_f32_16x16x32_bf16 v[80:83], v[240:243], v[112:115], v[80:83]\n\t"
    "v_mfma_f32_16x16x32_bf16 v[84:87], v[244:247], v[112:115], v[84:87]\n\t"
    "v_mfma_f32_16x16x32_bf16 v[88:91], v[240:243], v[116:119], v[88:91]\n\t"
    "v_mfma_f32_16x16x32_bf16 v[92:95], v[244:247], v[116:119], v[92:95]\n\t"
    "s_waitcnt lgkmcnt(0)\n\t"
    "v_mfma_f32_16x16x32_bf16 v[80:83], v[248:251], v[120:123], v[80:83]\n\t"
    "v_mfma_f32_16x16x32_bf16 v[84:87], v[252:255], v[120:123], v[84:87]\n\t"
    "v_mfma_f32_16x16x32_bf16 v[88:91], v[248:251], v[124:127], v[88:91]\n\t"
    "v_mfma_f32_16x16x32_bf16 v[92:95], v[252:255], v[124:127], v[92:95]\n\t"
    "s_mov_b32 %[st], 0\n\t"
    "s_nop 5\n\t"
    "s_branch 9f\n\t"
    "13:\n\t"
    "s_memrealtime s[36:37]\n\t"
    "v_mov_b32 v79, 0\n\t"
    "s_waitcnt lgkmcnt(0)\n\t"
    "s_mov_b32 %[tk], s36\n\t"
    "s_add_u32 s36, s36, %[bl]\n\t"
    "s_and_b32 s36, s36, 0xffffff\n\t"
    "s_and_b32 s37, %[bl], 0xff000000\n\t"
    "s_or_b32 s36, s36, s37\n\t"
    "v_mov_b32 v78, s36\n\t"
    "s_mov_b64 exec, 1\n\t"
    "global_store_dword v79, v78, %[pg] offset:64 sc1\n\t"
    "s_mov_b64 exec, -1\n\t"
    "s_branch 14b\n\t"
    "3:\n\t"
    "7:\n\t"
    "s_and_b32 %[st], %[pv], 1\n\t"
    "s_mul_i32 %[st], %[st], 36\n\t"
    "s_add_u32 %[st], %[st], %[cn]\n\t"
    "v_mov_b32 v76, %[st]\n\t"
    "v_mov_b32 v77, 1\n\t"
    "v_mov_b32 v78, %[pv]\n\t"
    "v_mov_b32 v79, 0\n\t"
    "s_waitcnt vmcnt(0)\n\t"
    "s_mov_b64 exec, 1\n\t"
    "ds_add_rtn_u32 v77, v76, v77\n\t"
    "s_waitcnt lgkmcnt(0)\n\t"
    "v_readfirstlane_b32 %[st], v77\n\t"
    "s_and_b32 %[st], %[st], 3\n\t"
    "s_cmp_lg_u32 %[st], 3\n\t"
    "s_cbranch_scc1 6f\n\t"
    "global_store_dword v79, v78, %[pg] sc1\n\t"
    "6:\n\t"
    "s_mov_b64 exec, -1\n\t"
    "s_bitcmp1_b32 %[x], 2\n\t"
    "s_cbranch_scc1 8b\n\t"
    "s_branch 4b\n\t"
    "12:\n\t"
    "s_waitcnt lgkmcnt(0)\n\t"
    "9:"
    : "=&{v[80:95]}"(pd), [st] "=&s"(st), [tk] "+&s"(tk)
    : [ea] "v"(ea), [da] "v"(da), [x] "s"(x), "{v[128:159]}"(W[0]), "{v[160:191]}"(W[1]), "{v[192:223]}"(W[2]), "{v[224:255]}"(W[3]), [pv] "s"(pv), [cn] "s"(cn), [pg] "s"(pg), [bl] "s"(bl)
    : "v96", "v97", "v98", "v99", "v100", "v101", "v102", "v103", "v104", "v105", "v106", "v107", "v108", "v109", "v110", "v111", "v112", "v113", "v114", "v115", "v116", "v117", "v118", "v119", "v120", "v121", "v122", "v123", "v124", "v125", "v126", "v127", "v76", "v77", "v78", "v79", "vcc", "scc", "m0", "memory", "s36", "s37", "s38", "s39");
}
__device__ __forceinline__ void lean_ep0(const u32x32 (&W)[4], unsigned ea, unsigned da, unsigned x, unsigned pv, unsigned cn, const char* pg, unsigned bl, unsigned& tk, unsigned& st,
    unsigned rw, unsigned fa, unsigned fv, const char* cb, unsigned co0, unsigned co1, unsigned& bad, unsigned& pk) {
  asm volatile("v_mov_b32 v76, %[cn]\n\t"
    "ds_read_b32 v76, v76 offset:28\n\t"
    "ds_read_b128 v[96:99], %[ea] offset:0\n\t"
    "ds_read_b128 v[100:103], %[ea] offset:2048\n\t"
    "ds_read_b128 v[104:107], %[da] offset:0\n\t"
    "ds_read_b128 v[108:111], %[da] offset:2048\n\t"
    "ds_read_b128 v[112:115], %[ea] offset:4096\n\t"
    "ds_read_b128 v[116:119], %[ea] offset:6144\n\t"
    "s_waitcnt lgkmcnt(6)\n\t"
    "v_cmp_ne_u32 vcc, 0, v76\n\t"
    "s_mov_b32 %[st], -1\n\t"
    "s_cbranch_vccnz 12f\n\t"
    "s_waitcnt lgkmcnt(4)\n\t"
    "v_mfma_f32_16x16x32_bf16 v[80:83], v[128:131], v[96:99], 0\n\t"
    "ds_read_b128 v[120:123], %[da] offset:4096\n\t"
    "v_mfma_f32_16x16x32_bf16 v[84:87], v[132:135], v[96:99], 0\n\t"
    "ds_read_b128 v[124:127], %[da] offset:6144\n\t"
    "v_mfma_f32_16x16x32_bf16 v[88:91], v[128:131], v[100:103], 0\n\t"
    "v_mfma_f32_16x16x32_bf16 v[92:95], v[132:135], v[100:103], 0\n\t"
    "s_barrier\n\t"
    "s_bitcmp1_b32 %[x], 2\n\t"
    "s_cbranch_scc1 7f\n\t"
    "8:\n\t"
    "s_bitcmp1_b32 %[x], 3\n\t"
    "s_cbranch_scc1 13f\n\t"
    "14:\n\t"
    "s_waitcnt lgkmcnt(4)\n\t"
    "v_mfma_f32_16x16x32_bf16 v[80:83], v[136:139], v[104:107], v[80:83]\n\t"
    "ds_read_b128 v[96:99], %[ea] offset:8192\n\t"
    "v_mfma_f32_16x16x32_bf16 v[84:87], v[140:143], v[104:107], v[84:87]\n\t"
    "ds_read_b128 v[100:103], %[ea] offset:10240\n\t"
    "v_mfma_f32_16x16x32_bf16 v[88:91], v[136:139], v[108:111], v[88:91]\n\t"
    "v_mfma_f32_16x16x32_bf16 v[92:95], v[140:143], v[108:111], v[92:95]\n\t"
    "s_waitcnt lgkmcnt(4)\n\t"
    "v_mfma_f32_16x16x32_bf16 v[80:83], v[144:147], v[112:115], v[80:83]\n\t"
    "ds_read_b128 v[104:107], %[da] offset:8192\n\t"
    "v_mfma_f32_16x16x32_bf16 v[84:87], v[148:151], v[112:115], v[84:87]\n\t"
    "ds_read_b128 v[108:111], %[da] offset:10240\n\t"
    "v_mfma_f32_16x16x32_bf16 v[88:91], v[144:147], v[116:119], v[88:91]\n\t"
    "v_mfma_f32_16x16x32_bf16 v[92:95], v[148:151], v[116:119], v[92:95]\n\t"
    "s_waitcnt lgkmcnt(4)\n\t"
    "v_mfma_f32_16x16x32_bf16 v[80:83], v[152:155], v[120:123], v[80:83]\n\t"
    "ds_read_b128 v[112:115], %[ea] offset:12288\n\t"
    "v_mfma_f32_16x16x32_bf16 v[84:87], v[156:159], v[120:123], v[84:87]\n\t"
    "ds_read_b128 v[116:119], %[ea] offset:14336\n\t"
    "v_mfma_f32_16x16x32_bf16 v[88:91], v[152:155], v[124:127], v[88:91]\n\t"
    "v_mfma_f32_16x16x32_bf16 v[92:95], v[156:159], v[124:127], v[92:95]\n\t"
    "s_waitcnt lgkmcnt(4)\n\t"
    "v_mfma_f32_16x16x32_bf16 v[80:83], v[160:163], v[96:99], v[80:83]\n\t"
    "ds_read_b128 v[120:123], %[da] offset:12288\n\t"
    "v_mfma_f32_16x16x32_bf16 v[84:87], v[164:167], v[96:99], v[84:87]\n\t"
    "ds_read_b128 v[124:127], %[da] offset:14336\n\t"
    "v_mfma_f32_16x16x32_bf16 v[88:91], v[160:163], v[100:103], v[88:91]\n\t"
    "v_mfma_f32_16x16x32_bf16 v[92:95], v[164:167], v[100:103], v[92:95]\n\t"
    "s_barrier\n\t"
    "s_waitcnt lgkmcnt(4)\n\t"
    "v_mfma_f32_16x16x32_bf16 v[80:83], v[168:171], v[104:107], v[80:83]\n\t"
    "ds_read_b128 v[96:99], %[ea] offset:16384\n\t"
    "v_mfma_f32_16x16x32_bf16 v[84:87], v[172:175], v[104:107], v[84:87]\n\t"
    "ds_read_b128 v[100:103], %[ea] offset:18432\n\t"
    "v_mfma_f32_16x16x32_bf16 v[88:91], v[168:171], v[108:111], v[88:91]\n\t"
    "v_mfma_f32_16x16x32_bf16 v[92:95], v[172:175], v[108:111], v[92:95]\n\t"
    "s_waitcnt lgkmcnt(4)\n\t"
    "v_mfma_f32_16x16x32_bf16 v[80:83], v[176:179], v[112:115], v[80:83]\n\t"
    "ds_read_b128 v[104:107], %[da] offset:16384\n\t"
    "v_mfma_f32_16x16x32_bf16 v[84:87], v[180:183], v[112:115], v[84:87]\n\t"
    "ds_read_b128 v[108:111], %[da] offset:18432\n\t"
    "v_mfma_f32_16x16x32_bf16 v[88:91], v[176:179], v[116:119], v[88:91]\n\t"
    "v_mfma_f32_16x16x32_bf16 v[92:95], v[180:183], v[116:119], v[92:95]\n\t"
    "s_waitcnt lgkmcnt(4)\n\t"
    "v_mfma_f32_16x16x32_bf16 v[80:83], v[184:187], v[120:123], v[80:83]\n\t"
    "ds_read_b128 v[112:115], %[ea] offset:20480\n\t"
    "v_mfma_f32_16x16x32_bf16 v[84:87], v[188:191], v[120:123], v[84:87]\n\t"
    "ds_read_b128 v[116:119], %[ea] offset:22528\n\t"
    "v_mfma_f32_16x16x32_bf16 v[88:91], v[184:187], v[124:127], v[88:91]\n\t"
    "v_mfma_f32_16x16x32_bf16 v[92:95], v[188:191], v[124:127], v[92:95]\n\t"
    "s_waitcnt lgkmcnt(4)\n\t"
    "v_mfma_f32_16x16x32_bf16 v[80:83], v[192:195], v[96:99], v[80:83]\n\t"
    "ds_read_b128 v[120:123], %[da] offset:20480\n\t"
    "v_mfma_f32_16x16x32_bf16 v[84:87], v[196:199], v[96:99], v[84:87]\n\t"
    "ds_read_b128 v[124:127], %[da] offset:22528\n\t"
    "v_mfma_f32_16x16x32_bf16 v[88:91], v[192:195], v[100:103], v[88:91]\n\t"
    "v_mfma_f32_16x16x32_bf16 v[92:95], v[196:199], v[100:103], v[92:95]\n\t"
    "s_barrier\n\t"
    "s_and_b32 %[st], %[x], 3\n\t"
    "s_cbranch_scc1 3f\n\t"
    "4:\n\t"
    "s_waitcnt lgkmcnt(4)\n\t"
    "v_mfma_f32_16x16x32_bf16 v[80:83], v[200:203], v[104:107], v[80:83]\n\t"
    "ds_read_b128 v[96:99], %[ea] offset:24576\n\t"
    "v_mfma_f32_16x16x32_bf16 v[84:87], v[204:207], v[104:107], v[84:87]\n\t"
    "ds_read_b128 v[100:103], %[ea] offset:26624\n\t"
    "v_mfma_f32_16x16x32_bf16 v[88:91], v[200:203], v[108:111], v[88:91]\n\t"
    "v_mfma_f32_16x16x32_bf16 v[92:95], v[204:207], v[108:111], v[92:95]\n\t"
    "s_waitcnt lgkmcnt(4)\n\t"
    "v_mfma_f32_16x16x32_bf16 v[80:83], v[208:211], v[112:115], v[80:83]\n\t"
    "ds_read_b128 v[104:107], %[da] offset:24576\n\t"
    "v_mfma_f32_16x16x32_bf16 v[84:87], v[212:215], v[112:115], v[84:87]\n\t"
    "ds_read_b128 v[108:111], %[da] offset:26624\n\t"
    "v_mfma_f32_16x16x32_bf16 v[88:91], v[208:211], v[116:119], v[88:91]\n\t"
    "v_mfma_f32_16x16x32_bf16 v[92:95], v[212:215], v[116:119], v[92:95]\n\t"
    "s_waitcnt lgkmcnt(4)\n\t"
    "v_mfma_f32_16x16x32_bf16 v[80:83], v[216:219], v[120:123], v[80:83]\n\t"
    "ds_read_b128 v[112:115], %[ea] offset:28672\n\t"
    "v_mfma_f32_16x16x32_bf16 v[84:87], v[220:223], v[120:123], v[84:87]\n\t"
    "ds_read_b128 v[116:119], %[ea] offset:30720\n\t"
    "v_mfma_f32_16x16x32_bf16 v[88:91], v[216:219], v[124:127], v[88:91]\n\t"
    "v_mfma_f32_16x16x32_bf16 v[92:95], v[220:223], v[124:127], v[92:95]\n\t"
    "s_waitcnt lgkmcnt(4)\n\t"
    "v_mfma_f32_16x16x32_bf16 v[80:83], v[224:227], v[96:99], v[80:83]\n\t"
    "ds_read_b128 v[120:123], %[da] offset:28672\n\t"
    "v_mfma_f32_16x16x32_bf16 v[84:87], v[228:231], v[96:99], v[84:87]\n\t"
    "ds_read_b128 v[124:127], %[da] offset:30720\n\t"
    "v_mfma_f32_16x16x32_bf16 v[88:91], v[224:227], v[100:103], v[88:91]\n\t"
    "v_mfma_f32_16x16x32_bf16 v[92:95], v[228:231], v[100:103], v[92:95]\n\t"
    "s_waitcnt lgkmcnt(4)\n\t"
    "v_mfma_f32_16x16x32_bf16 v[80:83], v[232:235], v[104:107], v[80:83]\n\t"
    "v_mfma_f32_16x16x32_bf16 v[84:87], v[236:239], v[104:107], v[84:87]\n\t"
    "v_mfma_f32_16x16x32_bf16 v[88:91], v[232:235], v[108:111], v[88:91]\n\t"
    "v_mfma_f32_16x16x32_bf16 v[92:95], v[236:239], v[108:111], v[92:95]\n\t"
    "s_waitcnt lgkmcnt(2)\n\t"
    "v_mfma_f32_16x16x32_bf16 v[80:83], v[240:243], v[112:115], v[80:83]\n\t"
    "ds_read_b32 v39, %[fa]\n\t"
    "ds_read_b128 v[48:51], %[rw] offset:0\n\t"
    "v_mfma_f32_16x16x32_bf16 v[84:87], v[244:247], v[112:115], v[84:87]\n\t"
    "ds_read_b128 v[52:55], %[rw] offset:1024\n\t"
    "v_mfma_f32_16x16x32_bf16 v[88:91], v[240:243], v[116:119], v[88:91]\n\t"
    "ds_read_b128 v[56:59], %[rw] offset:2048\n\t"
    "v_mfma_f32_16x16x32_bf16 v[92:95], v[244:247], v[116:119], v[92:95]\n\t"
    "ds_read_b128 v[60:63], %[rw] offset:3072\n\t"
    "s_waitcnt lgkmcnt(5)\n\t"
    "v_mfma_f32_16x16x32_bf16 v[80:83], v[248:251], v[120:123], v[80:83]\n\t"
    "v_mfma_f32_16x16x32_bf16 v[84:87], v[252:255], v[120:123], v[84:87]\n\t"
    "v_mfma_f32_16x16x32_bf16 v[88:91], v[248:251], v[124:127], v[88:91]\n\t"
    "v_mfma_f32_16x16x32_bf16 v[92:95], v[252:255], v[124:127], v[92:95]\n\t"
    "s_mov_b32 %[st], 0\n\t"
    "21:\n\t"
    "s_waitcnt lgkmcnt(0)\n\t"
    "v_cmp_ne_u32 vcc, %[fv], v39\n\t"
    "s_cbranch_vccnz 20f\n\t"
    "v_mov_b32 v39, %[cn]\n\t"
    "s_nop 0\n\t"
    "v_pk_add_f32 v[48:49], v[48:49], v[80:81]\n\t"
    "v_pk_add_f32 v[50:51], v[50:51], v[82:83]\n\t"
    "v_pk_add_f32 v[52:53], v[52:53], v[84:85]\n\t"
    "v_pk_add_f32 v[54:55], v[54:55], v[86:87]\n\t"
    "v_pk_add_f32 v[56:57], v[56:57], v[88:89]\n\t"
    "v_pk_add_f32 v[58:59], v[58:59], v[90:91]\n\t"
    "v_pk_add_f32 v[60:61], v[60:61], v[92:93]\n\t"
    "v_pk_add_f32 v[62:63], v[62:63], v[94:95]\n\t"
    "v_cvt_pk_bf16_f32 v40, v48, v49\n\t"
    "v_cvt_pk_bf16_f32 v41, v50, v51\n\t"
    "v_cvt_pk_bf16_f32 v42, v52, v53\n\t"
    "v_cvt_pk_bf16_f32 v43, v54, v55\n\t"
    "global_store_dwordx4 %[co0], v[40:43], %[cb] sc1\n\t"
    "ds_read_b32 v38, v39 offset:4\n\t"
    "v_cvt_pk_bf16_f32 v44, v56, v57\n\t"
    "v_cvt_pk_bf16_f32 v45, v58, v59\n\t"
    "v_cvt_pk_bf16_f32 v46, v60, v61\n\t"
    "v_cvt_pk_bf16_f32 v47, v62, v63\n\t"
    "global_store_dwordx4 %[co1], v[44:47], %[cb] sc1\n\t"
    "s_memrealtime s[36:37]\n\t"
    "v_cmp_u_f32 vcc, v48, v56\n\t"
    "s_waitcnt lgkmcnt(0)\n\t"
    "s_or_b32 %[bad], vcc_lo, vcc_hi\n\t"
    "v_readfirstlane_b32 %[pk], v38\n\t"
    "s_sub_u32 %[tk], s36, %[tk]\n\t"
    "s_branch 9f\n\t"
    "20:\n\t"
    "ds_read_b32 v39, %[fa]\n\t"
    "ds_read_b128 v[48:51], %[rw] offset:0\n\t"
    "ds_read_b128 v[52:55], %[rw] offset:1024\n\t"
    "ds_read_b128 v[56:59], %[rw] offset:2048\n\t"
    "ds_read_b128 v[60:63], %[rw] offset:3072\n\t"
    "s_branch 21b\n\t"
    "13:\n\t"
    "s_memrealtime s[36:37]\n\t"
    "v_mov_b32 v79, 0\n\t"
    "s_waitcnt lgkmcnt(0)\n\t"
    "s_mov_b32 %[tk], s36\n\t"
    "s_add_u32 s36, s36, %[bl]\n\t"
    "s_and_b32 s36, s36, 0xffffff\n\t"
    "s_and_b32 s37, %[bl], 0xff000000\n\t"
    "s_or_b32 s36, s36, s37\n\t"
    "v_mov_b32 v78, s36\n\t"
    "s_mov_b64 exec, 1\n\t"
    "global_store_dword v79, v78, %[pg] offset:64 sc1\n\t"
    "s_mov_b64 exec, -1\n\t"
    "s_branch 14b\n\t"
    "3:\n\t"
    "7:\n\t"
    "s_and_b32 %[st], %[pv], 1\n\t"
    "s_mul_i32 %[st], %[st], 36\n\t"
    "s_add_u32 %[st], %[st], %[cn]\n\t"
    "v_mov_b32 v76, %[st]\n\t"
    "v_mov_b32 v77, 1\n\t"
    "v_mov_b32 v78, %[pv]\n\t"
    "v_mov_b32 v79, 0\n\t"
    "s_waitcnt vmcnt(0)\n\t"
    "s_mov_b64 exec, 1\n\t"
    "ds_add_rtn_u32 v77, v76, v77\n\t"
    "s_waitcnt lgkmcnt(0)\n\t"
    "v_readfirstlane_b32 %[st], v77\n\t"
    "s_and_b32 %[st], %[st], 3\n\t"
    "s_cmp_lg_u32 %[st], 3\n\t"
    "s_cbranch_scc1 6f\n\t"
    "global_store_dword v79, v78, %[pg] sc1\n\t"
    "6:\n\t"
    "s_mov_b64 exec, -1\n\t"
    "s_bitcmp1_b32 %[x], 2\n\t"
    "s_cbranch_scc1 8b\n\t"
    "s_branch 4b\n\t"
    "12:\n\t"
    "s_waitcnt lgkmcnt(0)\n\t"
    "9:"
    : [st] "=&s"(st), [tk] "+&s"(tk), [bad] "=&s"(bad), [pk] "=&s"(pk)
    : [ea] "v"(ea), [da] "v"(da), [x] "s"(x), "{v[128:159]}"(W[0]), "{v[160:191]}"(W[1]), "{v[192:223]}"(W[2]), "{v[224:255]}"(W[3]), [pv] "s"(pv), [cn] "s"(cn), [pg] "s"(pg), [bl] "s"(bl), [rw] "v"(rw), [fa] "v"(fa), [fv] "s"(fv), [cb] "s"(cb), [co0] "v"(co0), [co1] "v"(co1)
    : "v96", "v97", "v98", "v99", "v100", "v101", "v102", "v103", "v104", "v105", "v106", "v107", "v108", "v109", "v110", "v111", "v112", "v113", "v114", "v115", "v116", "v117", "v118", "v119", "v120", "v121", "v122", "v123", "v124", "v125", "v126", "v127", "v76", "v77", "v78", "v79", "v38", "v39", "v40", "v41", "v42", "v43", "v44", "v45", "v46", "v47", "v48", "v49", "v50", "v51", "v52", "v53", "v54", "v55", "v56", "v57", "v58", "v59", "v60", "v61", "v62", "v63", "v80", "v81", "v82", "v83", "v84", "v85", "v86", "v87", "v88", "v89", "v90", "v91", "v92", "v93", "v94", "v95", "vcc", "scc", "m0", "memory", "s36", "s37", "s38", "s39");
}
__device__ __forceinline__ void lean_ep1(const u32x32 (&W)[4], unsigned ea, unsigned da, unsigned x, unsigned pv, unsigned cn, const char* pg, unsigned bl, unsigned& tk, unsigned& st,
    unsigned rw, unsigned fa, unsigned fv, const char* cb, unsigned co0, unsigned co1, unsigned& bad, unsigned& pk) {
  asm volatile("v_mov_b32 v76, %[cn]\n\t"
    "ds_read_b32 v76, v76 offset:28\n\t"
    "ds_read_b128 v[96:99], %[ea] offset:0\n\t"
    "ds_read_b128 v[100:103], %[ea] offset:2048\n\t"
    "ds_read_b128 v[104:107], %[da] offset:0\n\t"
    "ds_read_b128 v[108:111], %[da] offset:2048\n\t"
    "ds_read_b128 v[112:115], %[ea] offset:4096\n\t"
    "ds_read_b128 v[116:119], %[ea] offset:6144\n\t"
    "s_waitcnt lgkmcnt(6)\n\t"
    "v_cmp_ne_u32 vcc, 0, v76\n\t"
    "s_mov_b32 %[st], -1\n\t"
    "s_cbranch_vccnz 12f\n\t"
    "s_waitcnt lgkmcnt(4)\n\t"
    "v_mfma_f32_16x16x32_bf16 v[80:83], v[128:131], v[96:99], 0\n\t"
    "ds_read_b128 v[120:123], %[da] offset:4096\n\t"
    "v_mfma_f32_16x16x32_bf16 v[84:87], v[132:135], v[96:99], 0\n\t"
    "ds_read_b128 v[124:127], %[da] offset:6144\n\t"
    "v_mfma_f32_16x16x32_bf16 v[88:91], v[128:131], v[100:103], 0\n\t"
    "v_mfma_f32_16x16x32_bf16 v[92:95], v[132:135], v[100:103], 0\n\t"
    "s_barrier\n\t"
    "s_bitcmp1_b32 %[x], 2\n\t"
    "s_cbranch_scc1 7f\n\t"
    "8:\n\t"
    "s_bitcmp1_b32 %[x], 3\n\t"
    "s_cbranch_scc1 13f\n\t"
    "14:\n\t"
    "s_waitcnt lgkmcnt(4)\n\t"
    "v_mfma_f32_16x16x32_bf16 v[80:83], v[136:139], v[104:107], v[80:83]\n\t"
    "ds_read_b128 v[96:99], %[ea] offset:8192\n\t"
    "v_mfma_f32_16x16x32_bf16 v[84:87], v[140:143], v[104:107], v[84:87]\n\t"
    "ds_read_b128 v[100:103], %[ea] offset:10240\n\t"
    "v_mfma_f32_16x16x32_bf16 v[88:91], v[136:139], v[108:111], v[88:91]\n\t"
    "v_mfma_f32_16x16x32_bf16 v[92:95], v[140:143], v[108:111], v[92:95]\n\t"
    "s_waitcnt lgkmcnt(4)\n\t"
    "v_mfma_f32_16x16x32_bf16 v[80:83], v[144:147], v[112:115], v[80:83]\n\t"
    "ds_read_b128 v[104:107], %[da] offset:8192\n\t"
    "v_mfma_f32_16x16x32_bf16 v[84:87], v[148:151], v[112:115], v[84:87]\n\t"
    "ds_read_b128 v[108:111], %[da] offset:10240\n\t"
    "v_mfma_f32_16x16x32_bf16 v[88:91], v[144:147], v[116:119], v[88:91]\n\t"
    "v_mfma_f32_16x16x32_bf16 v[92:95], v[148:151], v[116:119], v[92:95]\n\t"
    "s_waitcnt lgkmcnt(4)\n\t"
    "v_mfma_f32_16x16x32_bf16 v[80:83], v[152:155], v[120:123], v[80:83]\n\t"
    "ds_read_b128 v[112:115], %[ea] offset:12288\n\t"
    "v_mfma_f32_16x16x32_bf16 v[84:87], v[156:159], v[120:123], v[84:87]\n\t"
    "ds_read_b128 v[116:119], %[ea] offset:14336\n\t"
    "v_mfma_f32_16x16x32_bf16 v[88:91], v[152:155], v[124:127], v[88:91]\n\t"
    "v_mfma_f32_16x16x32_bf16 v[92:95], v[156:159], v[124:127], v[92:95]\n\t"
    "s_waitcnt lgkmcnt(4)\n\t"
    "v_mfma_f32_16x16x32_bf16 v[80:83], v[160:163], v[96:99], v[80:83]\n\t"
    "ds_read_b128 v[120:123], %[da] offset:12288\n\t"
    "v_mfma_f32_16x16x32_bf16 v[84:87], v[164:167], v[96:99], v[84:87]\n\t"
    "ds_read_b128 v[124:127], %[da] offset:14336\n\t"
    "v_mfma_f32_16x16x32_bf16 v[88:91], v[160:163], v[100:103], v[88:91]\n\t"
    "v_mfma_f32_16x16x32_bf16 v[92:95], v[164:167], v[100:103], v[92:95]\n\t"
    "s_barrier\n\t"
    "s_waitcnt lgkmcnt(4)\n\t"
    "v_mfma_f32_16x16x32_bf16 v[80:83], v[168:171], v[104:107], v[80:83]\n\t"
    "ds_read_b128 v[96:99], %[ea] offset:16384\n\t"
    "v_mfma_f32_16x16x32_bf16 v[84:87], v[172:175], v[104:107], v[84:87]\n\t"
    "ds_read_b128 v[100:103], %[ea] offset:18432\n\t"
    "v_mfma_f32_16x16x32_bf16 v[88:91], v[168:171], v[108:111], v[88:91]\n\t"
    "v_mfma_f32_16x16x32_bf16 v[92:95], v[172:175], v[108:111], v[92:95]\n\t"
    "s_waitcnt lgkmcnt(4)\n\t"
    "v_mfma_f32_16x16x32_bf16 v[80:83], v[176:179], v[112:115], v[80:83]\n\t"
    "ds_read_b128 v[104:107], %[da] offset:16384\n\t"
    "v_mfma_f32_16x16x32_bf16 v[84:87], v[180:183], v[112:115], v[84:87]\n\t"
    "ds_read_b128 v[108:111], %[da] offset:18432\n\t"
    "v_mfma_f32_16x16x32_bf16 v[88:91], v[176:179], v[116:119], v[88:91]\n\t"
    "v_mfma_f32_16x16x32_bf16 v[92:95], v[180:183], v[116:119], v[92:95]\n\t"
    "s_waitcnt lgkmcnt(4)\n\t"
    "v_mfma_f32_16x16x32_bf16 v[80:83], v[184:187], v[120:123], v[80:83]\n\t"
    "ds_read_b128 v[112:115], %[ea] offset:20480\n\t"
    "v_mfma_f32_16x16x32_bf16 v[84:87], v[188:191], v[120:123], v[84:87]\n\t"
    "ds_read_b128 v[116:119], %[ea] offset:22528\n\t"
    "v_mfma_f32_16x16x32_bf16 v[88:91], v[184:187], v[124:127], v[88:91]\n\t"
    "v_mfma_f32_16x16x32_bf16 v[92:95], v[188:191], v[124:127], v[92:95]\n\t"
    "s_waitcnt lgkmcnt(4)\n\t"
    "v_mfma_f32_16x16x32_bf16 v[80:83], v[192:195], v[96:99], v[80:83]\n\t"
    "ds_read_b128 v[120:123], %[da] offset:20480\n\t"
    "v_mfma_f32_16x16x32_bf16 v[84:87], v[196:199], v[96:99], v[84:87]\n\t"
    "ds_read_b128 v[124:127], %[da] offset:22528\n\t"
    "v_mfma_f32_16x16x32_bf16 v[88:91], v[192:195], v[100:103], v[88:91]\n\t"
    "v_mfma_f32_16x16x32_bf16 v[92:95], v[196:199], v[100:103], v[92:95]\n\t"
    "s_barrier\n\t"
    "s_and_b32 %[st], %[x], 3\n\t"
    "s_cbranch_scc1 3f\n\t"
    "4:\n\t"
    "s_waitcnt lgkmcnt(4)\n\t"
    "v_mfma_f32_16x16x32_bf16 v[80:83], v[200:203], v[104:107], v[80:83]\n\t"
    "ds_read_b128 v[96:99], %[ea] offset:24576\n\t"
    "v_mfma_f32_16x16x32_bf16 v[84:87], v[204:207], v[104:107], v[84:87]\n\t"
    "ds_read_b128 v[100:103], %[ea] offset:26624\n\t"
    "v_mfma_f32_16x16x32_bf16 v[88:91], v[200:203], v[108:111], v[88:91]\n\t"
    "v_mfma_f32_16x16x32_bf16 v[92:95], v[204:207], v[108:111], v[92:95]\n\t"
    "s_waitcnt lgkmcnt(4)\n\t"
    "v_mfma_f32_16x16x32_bf16 v[80:83], v[208:211], v[112:115], v[80:83]\n\t"
    "ds_read_b128 v[104:107], %[da] offset:24576\n\t"
    "v_mfma_f32_16x16x32_bf16 v[84:87], v[212:215], v[112:115], v[84:87]\n\t"
    "ds_read_b128 v[108:111], %[da] offset:26624\n\t"
    "v_mfma_f32_16x16x32_bf16 v[88:91], v[208:211], v[116:119], v[88:91]\n\t"
    "v_mfma_f32_16x16x32_bf16 v[92:95], v[212:215], v[116:119], v[92:95]\n\t"
    "s_waitcnt lgkmcnt(4)\n\t"
    "v_mfma_f32_16x16x32_bf16 v[80:83], v[216:219], v[120:123], v[80:83]\n\t"
    "ds_read_b128 v[112:115], %[ea] offset:28672\n\t"
    "v_mfma_f32_16x16x32_bf16 v[84:87], v[220:223], v[120:123], v[84:87]\n\t"
    "ds_read_b128 v[116:119], %[ea] offset:30720\n\t"
    "v_mfma_f32_16x16x32_bf16 v[88:91], v[216:219], v[124:127], v[88:91]\n\t"
    "v_mfma_f32_16x16x32_bf16 v[92:95], v[220:223], v[124:127], v[92:95]\n\t"
    "s_waitcnt lgkmcnt(4)\n\t"
    "v_mfma_f32_16x16x32_bf16 v[80:83], v[224:227], v[96:99], v[80:83]\n\t"
    "ds_read_b128 v[120:123], %[da] offset:28672\n\t"
    "v_mfma_f32_16x16x32_bf16 v[84:87], v[228:231], v[96:99], v[84:87]\n\t"
    "ds_read_b128 v[124:127], %[da] offset:30720\n\t"
    "v_mfma_f32_16x16x32_bf16 v[88:91], v[224:227], v[100:103], v[88:91]\n\t"
    "v_mfma_f32_16x16x32_bf16 v[92:95], v[228:231], v[100:103], v[92:95]\n\t"
    "s_waitcnt lgkmcnt(4)\n\t"
    "v_mfma_f32_16x16x32_bf16 v[80:83], v[232:235], v[104:107], v[80:83]\n\t"
    "v_mfma_f32_16x16x32_bf16 v[84:87], v[236:239], v[104:107], v[84:87]\n\t"
    "v_mfma_f32_16x16x32_bf16 v[88:91], v[232:235], v[108:111], v[88:91]\n\t"
    "v_mfma_f32_16x16x32_bf16 v[92:95], v[236:239], v[108:111], v[92:95]\n\t"
    "s_waitcnt lgkmcnt(2)\n\t"
    "v_mfma_f32_16x16x32_bf16 v[80:83], v[240:243], v[112:115], v[80:83]\n\t"
    "ds_read_b32 v39, %[fa]\n\t"
    "ds_read_b128 v[48:51], %[rw] offset:0\n\t"
    "v_mfma_f32_16x16x32_bf16 v[84:87], v[244:247], v[112:115], v[84:87]\n\t"
    "ds_read_b128 v[52:55], %[rw] offset:1024\n\t"
    "v_mfma_f32_16x16x32_bf16 v[88:91], v[240:243], v[116:119], v[88:91]\n\t"
    "ds_read_b128 v[56:59], %[rw] offset:2048\n\t"
    "v_mfma_f32_16x16x32_bf16 v[92:95], v[244:247], v[116:119], v[92:95]\n\t"
    "ds_read_b128 v[60:63], %[rw] offset:3072\n\t"
    "s_waitcnt lgkmcnt(5)\n\t"
    "v_mfma_f32_16x16x32_bf16 v[80:83], v[248:251], v[120:123], v[80:83]\n\t"
    "v_mfma_f32_16x16x32_bf16 v[84:87], v[252:255], v[120:123], v[84:87]\n\t"
    "v_mfma_f32_16x16x32_bf16 v[88:91], v[248:251], v[124:127], v[88:91]\n\t"
    "v_mfma_f32_16x16x32_bf16 v[92:95], v[252:255], v[124:127], v[92:95]\n\t"
    "s_mov_b32 %[st], 0\n\t"
    "21:\n\t"
    "s_waitcnt lgkmcnt(0)\n\t"
    "v_cmp_ne_u32 vcc, %[fv], v39\n\t"
    "s_cbranch_vccnz 20f\n\t"
    "v_mov_b32 v39, %[cn]\n\t"
    "s_nop 0\n\t"
    "v_pk_add_f32 v[48:49], v[48:49], v[80:81]\n\t"
    "v_pk_add_f32 v[50:51], v[50:51], v[82:83]\n\t"
    "v_pk_add_f32 v[52:53], v[52:53], v[84:85]\n\t"
    "v_pk_add_f32 v[54:55], v[54:55], v[86:87]\n\t"
    "v_pk_add_f32 v[56:57], v[56:57], v[88:89]\n\t"
    "v_pk_add_f32 v[58:59], v[58:59], v[90:91]\n\t"
    "v_pk_add_f32 v[60:61], v[60:61], v[92:93]\n\t"
    "v_pk_add_f32 v[62:63], v[62:63], v[94:95]\n\t"
    "v_mul_f32 v40, %[nl], v48\n\t"
    "v_mul_f32 v41, %[nl], v49\n\t"
    "v_mul_f32 v42, %[nl], v50\n\t"
    "v_mul_f32 v43, %[nl], v51\n\t"
    "v_mul_f32 v44, %[nl], v56\n\t"
    "v_mul_f32 v45, %[nl], v57\n\t"
    "v_mul_f32 v46, %[nl], v58\n\t"
    "v_mul_f32 v47, %[nl], v59\n\t"
    "v_exp_f32 v40, v40\n\t"
    "v_exp_f32 v41, v41\n\t"
    "v_exp_f32 v42, v42\n\t"
    "v_exp_f32 v43, v43\n\t"
    "v_exp_f32 v44, v44\n\t"
    "v_exp_f32 v45, v45\n\t"
    "v_exp_f32 v46, v46\n\t"
    "v_exp_f32 v47, v47\n\t"
    "v_pk_add_f32 v[40:41], v[40:41], 1.0 op_sel_hi:[1,0]\n\t"
    "v_pk_add_f32 v[42:43], v[42:43], 1.0 op_sel_hi:[1,0]\n\t"
    "v_pk_add_f32 v[44:45], v[44:45], 1.0 op_sel_hi:[1,0]\n\t"
    "v_pk_add_f32 v[46:47], v[46:47], 1.0 op_sel_hi:[1,0]\n\t"
    "v_rcp_f32 v40, v40\n\t"
    "v_rcp_f32 v41, v41\n\t"
    "v_rcp_f32 v42, v42\n\t"
    "v_rcp_f32 v43, v43\n\t"
    "v_rcp_f32 v44, v44\n\t"
    "v_rcp_f32 v45, v45\n\t"
    "v_rcp_f32 v46, v46\n\t"
    "v_rcp_f32 v47, v47\n\t"
    "v_pk_mul_f32 v[40:41], v[48:49], v[40:41]\n\t"
    "v_pk_mul_f32 v[42:43], v[50:51], v[42:43]\n\t"
    "v_pk_mul_f32 v[44:45], v[56:57], v[44:45]\n\t"
    "v_pk_mul_f32 v[46:47], v[58:59], v[46:47]\n\t"
    "v_pk_mul_f32 v[40:41], v[40:41], v[52:53]\n\t"
    "v_pk_mul_f32 v[42:43], v[42:43], v[54:55]\n\t"
    "v_pk_mul_f32 v[44:45], v[44:45], v[60:61]\n\t"
    "v_pk_mul_f32 v[46:47], v[46:47], v[62:63]\n\t"
    "v_cvt_pk_bf16_f32 v48, v40, v41\n\t"
    "v_cvt_pk_bf16_f32 v49, v42, v43\n\t"
    "v_cvt_pk_bf16_f32 v50, v44, v45\n\t"
    "ds_read_b32 v38, v39 offset:4\n\t"
    "v_cvt_pk_bf16_f32 v51, v46, v47\n\t"
    "s_nop 1\n\t"
    "v_permlane16_swap_b32 v48, v50\n\t"
    "v_permlane16_swap_b32 v49, v51\n\t"
    "global_store_dwordx4 %[co0], v[48:51], %[cb] sc1\n\t"
    "s_memrealtime s[36:37]\n\t"
    "v_cmp_u_f32 vcc, v52, v60\n\t"
    "s_waitcnt lgkmcnt(0)\n\t"
    "s_or_b32 %[bad], vcc_lo, vcc_hi\n\t"
    "v_readfirstlane_b32 %[pk], v38\n\t"
    "s_sub_u32 %[tk], s36, %[tk]\n\t"
    "s_branch 9f\n\t"
    "20:\n\t"
    "ds_read_b32 v39, %[fa]\n\t"
    "ds_read_b128 v[48:51], %[rw] offset:0\n\t"
    "ds_read_b128 v[52:55], %[rw] offset:1024\n\t"
    "ds_read_b128 v[56:59], %[rw] offset:2048\n\t"
    "ds_read_b128 v[60:63], %[rw] offset:3072\n\t"
    "s_branch 21b\n\t"
    "13:\n\t"
    "s_memrealtime s[36:37]\n\t"
    "v_mov_b32 v79, 0\n\t"
    "s_waitcnt lgkmcnt(0)\n\t"
    "s_mov_b32 %[tk], s36\n\t"
    "s_add_u32 s36, s36, %[bl]\n\t"
    "s_and_b32 s36, s36, 0xffffff\n\t"
    "s_and_b32 s37, %[bl], 0xff000000\n\t"
    "s_or_b32 s36, s36, s37\n\t"
    "v_mov_b32 v78, s36\n\t"
    "s_mov_b64 exec, 1\n\t"
    "global_store_dword v79, v78, %[pg] offset:64 sc1\n\t"
    "s_mov_b64 exec, -1\n\t"
    "s_branch 14b\n\t"
    "3:\n\t"
    "7:\n\t"
    "s_and_b32 %[st], %[pv], 1\n\t"
    "s_mul_i32 %[st], %[st], 36\n\t"
    "s_add_u32 %[st], %[st], %[cn]\n\t"
    "v_mov_b32 v76, %[st]\n\t"
    "v_mov_b32 v77, 1\n\t"
    "v_mov_b32 v78, %[pv]\n\t"
    "v_mov_b32 v79, 0\n\t"
    "s_waitcnt vmcnt(0)\n\t"
    "s_mov_b64 exec, 1\n\t"
    "ds_add_rtn_u32 v77, v76, v77\n\t"
    "s_waitcnt lgkmcnt(0)\n\t"
    "v_readfirstlane_b32 %[st], v77\n\t"
    "s_and_b32 %[st], %[st], 3\n\t"
    "s_cmp_lg_u32 %[st], 3\n\t"
    "s_cbranch_scc1 6f\n\t"
    "global_store_dword v79, v78, %[pg] sc1\n\t"
    "6:\n\t"
    "s_mov_b64 exec, -1\n\t"
    "s_bitcmp1_b32 %[x], 2\n\t"
    "s_cbranch_scc1 8b\n\t"
    "s_branch 4b\n\t"
    "12:\n\t"
    "s_waitcnt lgkmcnt(0)\n\t"
    "9:"
    : [st] "=&s"(st), [tk] "+&s"(tk), [bad] "=&s"(bad), [pk] "=&s"(pk)
    : [ea] "v"(ea), [da] "v"(da), [x] "s"(x), "{v[128:159]}"(W[0]), "{v[160:191]}"(W[1]), "{v[192:223]}"(W[2]), "{v[224:255]}"(W[3]), [pv] "s"(pv), [cn] "s"(cn), [pg] "s"(pg), [bl] "s"(bl), [rw] "v"(rw), [fa] "v"(fa), [fv] "s"(fv), [cb] "s"(cb), [co0] "v"(co0), [nl] "s"(-LOG2E)
    : "v96", "v97", "v98", "v99", "v100", "v101", "v102", "v103", "v104", "v105", "v106", "v107", "v108", "v109", "v110", "v111", "v112", "v113", "v114", "v115", "v116", "v117", "v118", "v119", "v120", "v121", "v122", "v123", "v124", "v125", "v126", "v127", "v76", "v77", "v78", "v79", "v38", "v39", "v40", "v41", "v42", "v43", "v44", "v45", "v46", "v47", "v48", "v49", "v50", "v51", "v52", "v53", "v54", "v55", "v56", "v57", "v58", "v59", "v60", "v61", "v62", "v63", "v80", "v81", "v82", "v83", "v84", "v85", "v86", "v87", "v88", "v89", "v90", "v91", "v92", "v93", "v94", "v95", "vcc", "scc", "m0", "memory", "s36", "s37", "s38", "s39");
}
__device__ __forceinline__ void lean_ep2(const u32x32 (&W)[4], unsigned ea, unsigned da, unsigned x, unsigned pv, unsigned cn, const char* pg, unsigned bl, unsigned& tk, unsigned& st,
    unsigned rw, unsigned fa, unsigned fv, const char* cb, unsigned co0, unsigned co1, unsigned& bad, unsigned& pk) {
  asm volatile("v_mov_b32 v76, %[cn]\n\t"
    "ds_read_b32 v76, v76 offset:28\n\t"
    "ds_read_b128 v[96:99], %[ea] offset:0\n\t"
    "ds_read_b128 v[100:103], %[ea] offset:2048\n\t"
    "ds_read_b128 v[104:107], %[da] offset:0\n\t"
    "ds_read_b128 v[108:111], %[da] offset:2048\n\t"
    "ds_read_b128 v[112:115], %[ea] offset:4096\n\t"
    "ds_read_b128 v[116:119], %[ea] offset:6144\n\t"
    "s_waitcnt lgkmcnt(6)\n\t"
    "v_cmp_ne_u32 vcc, 0, v76\n\t"
    "s_mov_b32 %[st], -1\n\t"
    "s_cbranch_vccnz 12f\n\t"
    "s_waitcnt lgkmcnt(4)\n\t"
    "v_mfma_f32_16x16x32_bf16 v[80:83], v[128:131], v[96:99], 0\n\t"
    "ds_read_b128 v[120:123], %[da] offset:4096\n\t"
    "v_mfma_f32_16x16x32_bf16 v[84:87], v[132:135], v[96:99], 0\n\t"
    "ds_read_b128 v[124:127], %[da] offset:6144\n\t"
    "v_mfma_f32_16x16x32_bf16 v[88:91], v[128:131], v[100:103], 0\n\t"
    "v_mfma_f32_16x16x32_bf16 v[92:95], v[132:135], v[100:103], 0\n\t"
    "s_barrier\n\t"
    "s_bitcmp1_b32 %[x], 2\n\t"
    "s_cbranch_scc1 7f\n\t"
    "8:\n\t"
    "s_bitcmp1_b32 %[x], 3\n\t"
    "s_cbranch_scc1 13f\n\t"
    "14:\n\t"
    "s_waitcnt lgkmcnt(4)\n\t"
    "v_mfma_f32_16x16x32_bf16 v[80:83], v[136:139], v[104:107], v[80:83]\n\t"
    "ds_read_b128 v[96:99], %[ea] offset:8192\n\t"
    "v_mfma_f32_16x16x32_bf16 v[84:87], v[140:143], v[104:107], v[84:87]\n\t"
    "ds_read_b128 v[100:103], %[ea] offset:10240\n\t"
    "v_mfma_f32_16x16x32_bf16 v[88:91], v[136:139], v[108:111], v[88:91]\n\t"
    "v_mfma_f32_16x16x32_bf16 v[92:95], v[140:143], v[108:111], v[92:95]\n\t"
    "s_waitcnt lgkmcnt(4)\n\t"
    "v_mfma_f32_16x16x32_bf16 v[80:83], v[144:147], v[112:115], v[80:83]\n\t"
    "ds_read_b128 v[104:107], %[da] offset:8192\n\t"
    "v_mfma_f32_16x16x32_bf16 v[84:87], v[148:151], v[112:115], v[84:87]\n\t"
    "ds_read_b128 v[108:111], %[da] offset:10240\n\t"
    "v_mfma_f32_16x16x32_bf16 v[88:91], v[144:147], v[116:119], v[88:91]\n\t"
    "v_mfma_f32_16x16x32_bf16 v[92:95], v[148:151], v[116:119], v[92:95]\n\t"
    "s_waitcnt lgkmcnt(4)\n\t"
    "v_mfma_f32_16x16x32_bf16 v[80:83], v[152:155], v[120:123], v[80:83]\n\t"
    "ds_read_b128 v[112:115], %[ea] offset:12288\n\t"
    "v_mfma_f32_16x16x32_bf16 v[84:87], v[156:159], v[120:123], v[84:87]\n\t"
    "ds_read_b128 v[116:119], %[ea] offset:14336\n\t"
    "v_mfma_f32_16x16x32_bf16 v[88:91], v[152:155], v[124:127], v[88:91]\n\t"
    "v_mfma_f32_16x16x32_bf16 v[92:95], v[156:159], v[124:127], v[92:95]\n\t"
    "s_waitcnt lgkmcnt(4)\n\t"
    "v_mfma_f32_16x16x32_bf16 v[80:83], v[160:163], v[96:99], v[80:83]\n\t"
    "ds_read_b128 v[120:123], %[da] offset:12288\n\t"
    "v_mfma_f32_16x16x32_bf16 v[84:87], v[164:167], v[96:99], v[84:87]\n\t"
    "ds_read_b128 v[124:127], %[da] offset:14336\n\t"
    "v_mfma_f32_16x16x32_bf16 v[88:91], v[160:163], v[100:103], v[88:91]\n\t"
    "v_mfma_f32_16x16x32_bf16 v[92:95], v[164:167], v[100:103], v[92:95]\n\t"
    "s_barrier\n\t"
    "s_waitcnt lgkmcnt(4)\n\t"
    "v_mfma_f32_16x16x32_bf16 v[80:83], v[168:171], v[104:107], v[80:83]\n\t"
    "ds_read_b128 v[96:99], %[ea] offset:16384\n\t"
    "v_mfma_f32_16x16x32_bf16 v[84:87], v[172:175], v[104:107], v[84:87]\n\t"
    "ds_read_b128 v[100:103], %[ea] offset:18432\n\t"
    "v_mfma_f32_16x16x32_bf16 v[88:91], v[168:171], v[108:111], v[88:91]\n\t"
    "v_mfma_f32_16x16x32_bf16 v[92:95], v[172:175], v[108:111], v[92:95]\n\t"
    "s_waitcnt lgkmcnt(4)\n\t"
    "v_mfma_f32_16x16x32_bf16 v[80:83], v[176:179], v[112:115], v[80:83]\n\t"
    "ds_read_b128 v[104:107], %[da] offset:16384\n\t"
    "v_mfma_f32_16x16x32_bf16 v[84:87], v[180:183], v[112:115], v[84:87]\n\t"
    "ds_read_b128 v[108:111], %[da] offset:18432\n\t"
    "v_mfma_f32_16x16x32_bf16 v[88:91], v[176:179], v[116:119], v[88:91]\n\t"
    "v_mfma_f32_16x16x32_bf16 v[92:95], v[180:183], v[116:119], v[92:95]\n\t"
    "s_waitcnt lgkmcnt(4)\n\t"
    "v_mfma_f32_16x16x32_bf16 v[80:83], v[184:187], v[120:123], v[80:83]\n\t"
    "ds_read_b128 v[112:115], %[ea] offset:20480\n\t"
    "v_mfma_f32_16x16x32_bf16 v[84:87], v[188:191], v[120:123], v[84:87]\n\t"
    "ds_read_b128 v[116:119], %[ea] offset:22528\n\t"
    "v_mfma_f32_16x16x32_bf16 v[88:91], v[184:187], v[124:127], v[88:91]\n\t"
    "v_mfma_f32_16x16x32_bf16 v[92:95], v[188:191], v[124:127], v[92:95]\n\t"
    "s_waitcnt lgkmcnt(4)\n\t"
    "v_mfma_f32_16x16x32_bf16 v[80:83], v[192:195], v[96:99], v[80:83]\n\t"
    "ds_read_b128 v[120:123], %[da] offset:20480\n\t"
    "v_mfma_f32_16x16x32_bf16 v[84:87], v[196:199], v[96:99], v[84:87]\n\t"
    "ds_read_b128 v[124:127], %[da] offset:22528\n\t"
    "v_mfma_f32_16x16x32_bf16 v[88:91], v[192:195], v[100:103], v[88:91]\n\t"
    "v_mfma_f32_16x16x32_bf16 v[92:95], v[196:199], v[100:103], v[92:95]\n\t"
    "s_barrier\n\t"
    "s_and_b32 %[st], %[x], 3\n\t"
    "s_cbranch_scc1 3f\n\t"
    "4:\n\t"
    "s_waitcnt lgkmcnt(4)\n\t"
    "v_mfma_f32_16x16x32_bf16 v[80:83], v[200:203], v[104:107], v[80:83]\n\t"
    "ds_read_b128 v[96:99], %[ea] offset:24576\n\t"
    "v_mfma_f32_16x16x32_bf16 v[84:87], v[204:207], v[104:107], v[84:87]\n\t"
    "ds_read_b128 v[100:103], %[ea] offset:26624\n\t"
    "v_mfma_f32_16x16x32_bf16 v[88:91], v[200:203], v[108:111], v[88:91]\n\t"
    "v_mfma_f32_16x16x32_bf16 v[92:95], v[204:207], v[108:111], v[92:95]\n\t"
    "s_waitcnt lgkmcnt(4)\n\t"
    "v_mfma_f32_16x16x32_bf16 v[80:83], v[208:211], v[112:115], v[80:83]\n\t"
    "ds_read_b128 v[104:107], %[da] offset:24576\n\t"
    "v_mfma_f32_16x16x32_bf16 v[84:87], v[212:215], v[112:115], v[84:87]\n\t"
    "ds_read_b128 v[108:111], %[da] offset:26624\n\t"
    "v_mfma_f32_16x16x32_bf16 v[88:91], v[208:211], v[116:119], v[88:91]\n\t"
    "v_mfma_f32_16x16x32_bf16 v[92:95], v[212:215], v[116:119], v[92:95]\n\t"
    "s_waitcnt lgkmcnt(4)\n\t"
    "v_mfma_f32_16x16x32_bf16 v[80:83], v[216:219], v[120:123], v[80:83]\n\t"
    "ds_read_b128 v[112:115], %[ea] offset:28672\n\t"
    "v_mfma_f32_16x16x32_bf16 v[84:87], v[220:223], v[120:123], v[84:87]\n\t"
    "ds_read_b128 v[116:119], %[ea] offset:30720\n\t"
    "v_mfma_f32_16x16x32_bf16 v[88:91], v[216:219], v[124:127], v[88:91]\n\t"
    "v_mfma_f32_16x16x32_bf16 v[92:95], v[220:223], v[124:127], v[92:95]\n\t"
    "s_waitcnt lgkmcnt(4)\n\t"
    "v_mfma_f32_16x16x32_bf16 v[80:83], v[224:227], v[96:99], v[80:83]\n\t"
    "ds_read_b128 v[120:123], %[da] offset:28672\n\t"
    "v_mfma_f32_16x16x32_bf16 v[84:87], v[228:231], v[96:99], v[84:87]\n\t"
    "ds_read_b128 v[124:127], %[da] offset:30720\n\t"
    "v_mfma_f32_16x16x32_bf16 v[88:91], v[224:227], v[100:103], v[88:91]\n\t"
    "v_mfma_f32_16x16x32_bf16 v[92:95], v[228:231], v[100:103], v[92:95]\n\t"
    "s_waitcnt lgkmcnt(4)\n\t"
    "v_mfma_f32_16x16x32_bf16 v[80:83], v[232:235], v[104:107], v[80:83]\n\t"
    "v_mfma_f32_16x16x32_bf16 v[84:87], v[236:239], v[104:107], v[84:87]\n\t"
    "v_mfma_f32_16x16x32_bf16 v[88:91], v[232:235], v[108:111], v[88:91]\n\t"
    "v_mfma_f32_16x16x32_bf16 v[92:95], v[236:239], v[108:111], v[92:95]\n\t"
    "s_waitcnt lgkmcnt(2)\n\t"
    "v_mfma_f32_16x16x32_bf16 v[80:83], v[240:243], v[112:115], v[80:83]\n\t"
    "ds_read_b32 v39, %[fa]\n\t"
    "ds_read_b128 v[48:51], %[rw] offset:0\n\t"
    "v_mfma_f32_16x16x32_bf16 v[84:87], v[244:247], v[112:115], v[84:87]\n\t"
    "ds_read_b128 v[52:55], %[rw] offset:1024\n\t"
    "v_mfma_f32_16x16x32_bf16 v[88:91], v[240:243], v[116:119], v[88:91]\n\t"
    "ds_read_b128 v[56:59], %[rw] offset:2048\n\t"
    "v_mfma_f32_16x16x32_bf16 v[92:95], v[244:247], v[116:119], v[92:95]\n\t"
    "ds_read_b128 v[60:63], %[rw] offset:3072\n\t"
    "s_waitcnt lgkmcnt(5)\n\t"
    "v_mfma_f32_16x16x32_bf16 v[80:83], v[248:251], v[120:123], v[80:83]\n\t"
    "v_mfma_f32_16x16x32_bf16 v[84:87], v[252:255], v[120:123], v[84:87]\n\t"
    "v_mfma_f32_16x16x32_bf16 v[88:91], v[248:251], v[124:127], v[88:91]\n\t"
    "v_mfma_f32_16x16x32_bf16 v[92:95], v[252:255], v[124:127], v[92:95]\n\t"
    "s_mov_b32 %[st], 0\n\t"
    "21:\n\t"
    "s_waitcnt lgkmcnt(0)\n\t"
    "v_cmp_ne_u32 vcc, %[fv], v39\n\t"
    "s_cbranch_vccnz 20f\n\t"
    "v_mov_b32 v39, %[cn]\n\t"
    "s_nop 0\n\t"
    "v_cvt_pk_bf16_f32 v40, v80, v81\n\t"
    "v_cvt_pk_bf16_f32 v41, v82, v83\n\t"
    "v_cvt_pk_bf16_f32 v42, v84, v85\n\t"
    "v_cvt_pk_bf16_f32 v43, v86, v87\n\t"
    "global_store_dwordx4 %[co0], v[40:43], %[cb] sc1\n\t"
    "v_cvt_pk_bf16_f32 v44, v88, v89\n\t"
    "v_cvt_pk_bf16_f32 v45, v90, v91\n\t"
    "v_cvt_pk_bf16_f32 v46, v92, v93\n\t"
    "v_cvt_pk_bf16_f32 v47, v94, v95\n\t"
    "global_store_dwordx4 %[co1], v[44:47], %[cb] sc1\n\t"
    "v_cvt_pk_bf16_f32 v48, v48, v49\n\t"
    "v_cvt_pk_bf16_f32 v49, v50, v51\n\t"
    "v_cvt_pk_bf16_f32 v50, v52, v53\n\t"
    "v_cvt_pk_bf16_f32 v51, v54, v55\n\t"
    "global_store_dwordx4 %[co0], v[48:51], %[cb] offset:2048 sc1\n\t"
    "ds_read_b32 v38, v39 offset:4\n\t"
    "v_cvt_pk_bf16_f32 v56, v56, v57\n\t"
    "v_cvt_pk_bf16_f32 v57, v58, v59\n\t"
    "v_cvt_pk_bf16_f32 v58, v60, v61\n\t"
    "v_cvt_pk_bf16_f32 v59, v62, v63\n\t"
    "global_store_dwordx4 %[co1], v[56:59], %[cb] offset:2048 sc1\n\t"
    "s_memrealtime s[36:37]\n\t"
    "v_cmp_u_f32 vcc, v80, v88\n\t"
    "s_waitcnt lgkmcnt(0)\n\t"
    "s_or_b32 %[bad], vcc_lo, vcc_hi\n\t"
    "v_readfirstlane_b32 %[pk], v38\n\t"
    "s_sub_u32 %[tk], s36, %[tk]\n\t"
    "s_branch 9f\n\t"
    "20:\n\t"
    "ds_read_b32 v39, %[fa]\n\t"
    "ds_read_b128 v[48:51], %[rw] offset:0\n\t"
    "ds_read_b128 v[52:55], %[rw] offset:1024\n\t"
    "ds_read_b128 v[56:59], %[rw] offset:2048\n\t"
    "ds_read_b128 v[60:63], %[rw] offset:3072\n\t"
    "s_branch 21b\n\t"
    "13:\n\t"
    "s_memrealtime s[36:37]\n\t"
    "v_mov_b32 v79, 0\n\t"
    "s_waitcnt lgkmcnt(0)\n\t"
    "s_mov_b32 %[tk], s36\n\t"
    "s_add_u32 s36, s36, %[bl]\n\t"
    "s_and_b32 s36, s36, 0xffffff\n\t"
    "s_and_b32 s37, %[bl], 0xff000000\n\t"
    "s_or_b32 s36, s36, s37\n\t"
    "v_mov_b32 v78, s36\n\t"
    "s_mov_b64 exec, 1\n\t"
    "global_store_dword v79, v78, %[pg] offset:64 sc1\n\t"
    "s_mov_b64 exec, -1\n\t"
    "s_branch 14b\n\t"
    "3:\n\t"
    "7:\n\t"
    "s_and_b32 %[st], %[pv], 1\n\t"
    "s_mul_i32 %[st], %[st], 36\n\t"
    "s_add_u32 %[st], %[st], %[cn]\n\t"
    "v_mov_b32 v76, %[st]\n\t"
    "v_mov_b32 v77, 1\n\t"
    "v_mov_b32 v78, %[pv]\n\t"
    "v_mov_b32 v79, 0\n\t"
    "s_waitcnt vmcnt(0)\n\t"
    "s_mov_b64 exec, 1\n\t"
    "ds_add_rtn_u32 v77, v76, v77\n\t"
    "s_waitcnt lgkmcnt(0)\n\t"
    "v_readfirstlane_b32 %[st], v77\n\t"
    "s_and_b32 %[st], %[st], 3\n\t"
    "s_cmp_lg_u32 %[st], 3\n\t"
    "s_cbranch_scc1 6f\n\t"
    "global_store_dword v79, v78, %[pg] sc1\n\t"
    "6:\n\t"
    "s_mov_b64 exec, -1\n\t"
    "s_bitcmp1_b32 %[x], 2\n\t"
    "s_cbranch_scc1 8b\n\t"
    "s_branch 4b\n\t"
    "12:\n\t"
    "s_waitcnt lgkmcnt(0)\n\t"
    "9:"
    : [st] "=&s"(st), [tk] "+&s"(tk), [bad] "=&s"(bad), [pk] "=&s"(pk)
    : [ea] "v"(ea), [da] "v"(da), [x] "s"(x), "{v[128:159]}"(W[0]), "{v[160:191]}"(W[1]), "{v[192:223]}"(W[2]), "{v[224:255]}"(W[3]), [pv] "s"(pv), [cn] "s"(cn), [pg] "s"(pg), [bl] "s"(bl), [rw] "v"(rw), [fa] "v"(fa), [fv] "s"(fv), [cb] "s"(cb), [co0] "v"(co0), [co1] "v"(co1)
    : "v96", "v97", "v98", "v99", "v100", "v101", "v102", "v103", "v104", "v105", "v106", "v107", "v108", "v109", "v110", "v111", "v112", "v113", "v114", "v115", "v116", "v117", "v118", "v119", "v120", "v121", "v122", "v123", "v124", "v125", "v126", "v127", "v76", "v77", "v78", "v79", "v38", "v39", "v40", "v41", "v42", "v43", "v44", "v45", "v46", "v47", "v48", "v49", "v50", "v51", "v52", "v53", "v54", "v55", "v56", "v57", "v58", "v59", "v60", "v61", "v62", "v63", "v80", "v81", "v82", "v83", "v84", "v85", "v86", "v87", "v88", "v89", "v90", "v91", "v92", "v93", "v94", "v95", "vcc", "scc", "m0", "memory", "s36", "s37", "s38", "s39");
}
#define LEAN_QB 1
#define PREK 10
#define NEARLY 16
__device__ __forceinline__ void fin_ld(const u32x16& pd, unsigned rw, unsigned fa, unsigned fv) {
  asm volatile("ds_write_b128 %[rw], v[80:83] offset:0\n\t"
    "ds_write_b128 %[rw], v[84:87] offset:1024\n\t"
    "ds_write_b128 %[rw], v[88:91] offset:2048\n\t"
    "ds_write_b128 %[rw], v[92:95] offset:3072\n\t"
    "ds_write_b32 %[fa], %[fv]"
    :
    : [rw] "v"(rw), "{v[80:95]}"(pd), [fa] "v"(fa), [fv] "v"(fv)
    : "memory");
}
__device__ __forceinline__ void fin_st0(const u32x16& pd, unsigned rw, unsigned fa, unsigned fv, const char* cb, unsigned co0, unsigned co1, unsigned pa, unsigned& bad, unsigned& pk) {
  asm volatile("1:\n\t"
    "ds_read_b32 v39, %[fa]\n\t"
    "ds_read_b128 v[48:51], %[rw] offset:0\n\t"
    "ds_read_b128 v[52:55], %[rw] offset:1024\n\t"
    "ds_read_b128 v[56:59], %[rw] offset:2048\n\t"
    "ds_read_b128 v[60:63], %[rw] offset:3072\n\t"
    "s_waitcnt lgkmcnt(2)\n\t"
    "v_cmp_ne_u32 vcc, v39, %[fv]\n\t"
    "s_cbranch_vccz 2f\n\t"
    "s_waitcnt lgkmcnt(0)\n\t"
    "s_branch 1b\n\t"
    "2:\n\t"
    "v_pk_add_f32 v[48:49], v[48:49], v[80:81]\n\t"
    "v_pk_add_f32 v[50:51], v[50:51], v[82:83]\n\t"
    "v_pk_add_f32 v[52:53], v[52:53], v[84:85]\n\t"
    "v_pk_add_f32 v[54:55], v[54:55], v[86:87]\n\t"
    "v_cvt_pk_bf16_f32 v40, v48, v49\n\t"
    "v_cvt_pk_bf16_f32 v41, v50, v51\n\t"
    "v_cvt_pk_bf16_f32 v42, v52, v53\n\t"
    "v_cvt_pk_bf16_f32 v43, v54, v55\n\t"
    "global_store_dwordx4 %[co0], v[40:43], %[cb] sc1\n\t"
    "s_waitcnt lgkmcnt(0)\n\t"
    "v_mov_b32 v39, %[pa]\n\t"
    "ds_read_b32 v39, v39\n\t"
    "v_pk_add_f32 v[56:57], v[56:57], v[88:89]\n\t"
    "v_pk_add_f32 v[58:59], v[58:59], v[90:91]\n\t"
    "v_pk_add_f32 v[60:61], v[60:61], v[92:93]\n\t"
    "v_pk_add_f32 v[62:63], v[62:63], v[94:95]\n\t"
    "v_cvt_pk_bf16_f32 v44, v56, v57\n\t"
    "v_cvt_pk_bf16_f32 v45, v58, v59\n\t"
    "v_cvt_pk_bf16_f32 v46, v60, v61\n\t"
    "v_cvt_pk_bf16_f32 v47, v62, v63\n\t"
    "global_store_dwordx4 %[co1], v[44:47], %[cb] sc1\n\t"
    "v_cmp_u_f32 vcc, v48, v56\n\t"
    "s_waitcnt lgkmcnt(0)\n\t"
    "s_or_b32 %[bad], vcc_lo, vcc_hi\n\t"
    "v_readfirstlane_b32 %[pk], v39"
    : [bad] "=&s"(bad), [pk] "=&s"(pk)
    : [rw] "v"(rw), "{v[80:95]}"(pd), [fa] "v"(fa), [fv] "v"(fv), [cb] "s"(cb), [co0] "v"(co0), [co1] "v"(co1), [pa] "s"(pa)
    : "v39", "v40", "v41", "v42", "v43", "v44", "v45", "v46", "v47", "v48", "v49", "v50", "v51", "v52", "v53", "v54", "v55", "v56", "v57", "v58", "v59", "v60", "v61", "v62", "v63", "vcc", "scc", "memory");
}
__device__ __forceinline__ void fin_st1(const u32x16& pd, unsigned rw, unsigned fa, unsigned fv, const char* cb, unsigned co0, unsigned co1, unsigned pa, unsigned& bad, unsigned& pk) {
  asm volatile("1:\n\t"
    "ds_read_b32 v39, %[fa]\n\t"
    "ds_read_b128 v[48:51], %[rw] offset:0\n\t"
    "ds_read_b128 v[52:55], %[rw] offset:1024\n\t"
    "ds_read_b128 v[56:59], %[rw] offset:2048\n\t"
    "ds_read_b128 v[60:63], %[rw] offset:3072\n\t"
    "s_waitcnt lgkmcnt(2)\n\t"
    "v_cmp_ne_u32 vcc, v39, %[fv]\n\t"
    "s_cbranch_vccz 2f\n\t"
    "s_waitcnt lgkmcnt(0)\n\t"
    "s_branch 1b\n\t"
    "2:\n\t"
    "v_pk_add_f32 v[48:49], v[48:49], v[80:81]\n\t"
    "v_pk_add_f32 v[50:51], v[50:51], v[82:83]\n\t"
    "v_pk_add_f32 v[52:53], v[52:53], v[84:85]\n\t"
    "v_pk_add_f32 v[54:55], v[54:55], v[86:87]\n\t"
    "s_waitcnt lgkmcnt(0)\n\t"
    "v_mov_b32 v39, %[pa]\n\t"
    "ds_read_b32 v39, v39\n\t"
    "v_pk_add_f32 v[56:57], v[56:57], v[88:89]\n\t"
    "v_pk_add_f32 v[58:59], v[58:59], v[90:91]\n\t"
    "v_pk_add_f32 v[60:61], v[60:61], v[92:93]\n\t"
    "v_pk_add_f32 v[62:63], v[62:63], v[94:95]\n\t"
    "v_mul_f32 v40, %[nl], v48\n\t"
    "v_mul_f32 v41, %[nl], v49\n\t"
    "v_mul_f32 v42, %[nl], v50\n\t"
    "v_mul_f32 v43, %[nl], v51\n\t"
    "v_mul_f32 v44, %[nl], v56\n\t"
    "v_mul_f32 v45, %[nl], v57\n\t"
    "v_mul_f32 v46, %[nl], v58\n\t"
    "v_mul_f32 v47, %[nl], v59\n\t"
    "v_exp_f32 v40, v40\n\t"
    "v_exp_f32 v41, v41\n\t"
    "v_exp_f32 v42, v42\n\t"
    "v_exp_f32 v43, v43\n\t"
    "v_exp_f32 v44, v44\n\t"
    "v_exp_f32 v45, v45\n\t"
    "v_exp_f32 v46, v46\n\t"
    "v_exp_f32 v47, v47\n\t"
    "v_pk_add_f32 v[40:41], v[40:41], 1.0 op_sel_hi:[1,0]\n\t"
    "v_pk_add_f32 v[42:43], v[42:43], 1.0 op_sel_hi:[1,0]\n\t"
    "v_pk_add_f32 v[44:45], v[44:45], 1.0 op_sel_hi:[1,0]\n\t"
    "v_pk_add_f32 v[46:47], v[46:47], 1.0 op_sel_hi:[1,0]\n\t"
    "v_rcp_f32 v40, v40\n\t"
    "v_rcp_f32 v41, v41\n\t"
    "v_rcp_f32 v42, v42\n\t"
    "v_rcp_f32 v43, v43\n\t"
    "v_rcp_f32 v44, v44\n\t"
    "v_rcp_f32 v45, v45\n\t"
    "v_rcp_f32 v46, v46\n\t"
    "v_rcp_f32 v47, v47\n\t"
    "v_pk_mul_f32 v[40:41], v[48:49], v[40:41]\n\t"
    "v_pk_mul_f32 v[42:43], v[50:51], v[42:43]\n\t"
    "v_pk_mul_f32 v[44:45], v[56:57], v[44:45]\n\t"
    "v_pk_mul_f32 v[46:47], v[58:59], v[46:47]\n\t"
    "v_pk_mul_f32 v[40:41], v[40:41], v[52:53]\n\t"
    "v_pk_mul_f32 v[42:43], v[42:43], v[54:55]\n\t"
    "v_pk_mul_f32 v[44:45], v[44:45], v[60:61]\n\t"
    "v_pk_mul_f32 v[46:47], v[46:47], v[62:63]\n\t"
    "v_cvt_pk_bf16_f32 v48, v40, v41\n\t"
    "v_cvt_pk_bf16_f32 v49, v42, v43\n\t"
    "v_cvt_pk_bf16_f32 v50, v44, v45\n\t"
    "v_cvt_pk_bf16_f32 v51, v46, v47\n\t"
    "s_nop 1\n\t"
    "v_permlane16_swap_b32 v48, v50\n\t"
    "v_permlane16_swap_b32 v49, v51\n\t"
    "global_store_dwordx4 %[co0], v[48:51], %[cb] sc1\n\t"
    "v_cmp_u_f32 vcc, v52, v60\n\t"
    "s_waitcnt lgkmcnt(0)\n\t"
    "s_or_b32 %[bad], vcc_lo, vcc_hi\n\t"
    "v_readfirstlane_b32 %[pk], v39"
    : [bad] "=&s"(bad), [pk] "=&s"(pk)
    : [rw] "v"(rw), "{v[80:95]}"(pd), [fa] "v"(fa), [fv] "v"(fv), [cb] "s"(cb), [co0] "v"(co0), [nl] "s"(-LOG2E), [pa] "s"(pa)
    : "v39", "v40", "v41", "v42", "v43", "v44", "v45", "v46", "v47", "v48", "v49", "v50", "v51", "v52", "v53", "v54", "v55", "v56", "v57", "v58", "v59", "v60", "v61", "v62", "v63", "vcc", "scc", "memory");
}
__device__ __forceinline__ void fin_st2(const u32x16& pd, unsigned rw, unsigned fa, unsigned fv, const char* cb, unsigned co0, unsigned co1, unsigned pa, unsigned& bad, unsigned& pk) {
  asm volatile("s_nop 1\n\t"
    "v_cvt_pk_bf16_f32 v40, v80, v81\n\t"
    "v_cvt_pk_bf16_f32 v41, v82, v83\n\t"
    "v_cvt_pk_bf16_f32 v42, v84, v85\n\t"
    "v_cvt_pk_bf16_f32 v43, v86, v87\n\t"
    "global_store_dwordx4 %[co0], v[40:43], %[cb] sc1\n\t"
    "v_cvt_pk_bf16_f32 v44, v88, v89\n\t"
    "v_cvt_pk_bf16_f32 v45, v90, v91\n\t"
    "v_cvt_pk_bf16_f32 v46, v92, v93\n\t"
    "v_cvt_pk_bf16_f32 v47, v94, v95\n\t"
    "global_store_dwordx4 %[co1], v[44:47], %[cb] sc1\n\t"
    "1:\n\t"
    "ds_read_b32 v39, %[fa]\n\t"
    "ds_read_b128 v[48:51], %[rw] offset:0\n\t"
    "ds_read_b128 v[52:55], %[rw] offset:1024\n\t"
    "ds_read_b128 v[56:59], %[rw] offset:2048\n\t"
    "ds_read_b128 v[60:63], %[rw] offset:3072\n\t"
    "s_waitcnt lgkmcnt(2)\n\t"
    "v_cmp_ne_u32 vcc, v39, %[fv]\n\t"
    "s_cbranch_vccz 2f\n\t"
    "s_waitcnt lgkmcnt(0)\n\t"
    "s_branch 1b\n\t"
    "2:\n\t"
    "v_cvt_pk_bf16_f32 v48, v48, v49\n\t"
    "v_cvt_pk_bf16_f32 v49, v50, v51\n\t"
    "v_cvt_pk_bf16_f32 v50, v52, v53\n\t"
    "v_cvt_pk_bf16_f32 v51, v54, v55\n\t"
    "global_store_dwordx4 %[co0], v[48:51], %[cb] offset:2048 sc1\n\t"
    "s_waitcnt lgkmcnt(0)\n\t"
    "v_mov_b32 v39, %[pa]\n\t"
    "ds_read_b32 v39, v39\n\t"
    "v_cvt_pk_bf16_f32 v56, v56, v57\n\t"
    "v_cvt_pk_bf16_f32 v57, v58, v59\n\t"
    "v_cvt_pk_bf16_f32 v58, v60, v61\n\t"
    "v_cvt_pk_bf16_f32 v59, v62, v63\n\t"
    "global_store_dwordx4 %[co1], v[56:59], %[cb] offset:2048 sc1\n\t"
    "v_cmp_u_f32 vcc, v80, v88\n\t"
    "s_waitcnt lgkmcnt(0)\n\t"
    "s_or_b32 %[bad], vcc_lo, vcc_hi\n\t"
    "v_readfirstlane_b32 %[pk], v39"
    : [bad] "=&s"(bad), [pk] "=&s"(pk)
    : [rw] "v"(rw), "{v[80:95]}"(pd), [fa] "v"(fa), [fv] "v"(fv), [cb] "s"(cb), [co0] "v"(co0), [co1] "v"(co1), [pa] "s"(pa)
    : "v39", "v40", "v41", "v42", "v43", "v44", "v45", "v46", "v47", "v48", "v49", "v50", "v51", "v52", "v53", "v54", "v55", "v56", "v57", "v58", "v59", "v60", "v61", "v62", "v63", "vcc", "scc", "memory");
}
template <int R> __device__ __forceinline__ void gw_load(u32x32& w, unsigned a0, unsigned a1);
template <> __device__ __forceinline__ void gw_load<0>(u32x32& w, unsigned a0, unsigned a1) {
  asm volatile("ds_read_b64_tr_b16 v[128:129], %1 offset:0\n\t"
    "ds_read_b64_tr_b16 v[130:131], %1 offset:512\n\t"
    "ds_read_b64_tr_b16 v[132:133], %2 offset:0\n\t"
    "ds_read_b64_tr_b16 v[134:135], %2 offset:512\n\t"
    "ds_read_b64_tr_b16 v[136:137], %1 offset:4096\n\t"
    "ds_read_b64_tr_b16 v[138:139], %1 offset:4608\n\t"
    "ds_read_b64_tr_b16 v[140:141], %2 offset:4096\n\t"
    "ds_read_b64_tr_b16 v[142:143], %2 offset:4608\n\t"
    "s_waitcnt lgkmcnt(0)\n\t"
    "ds_read_b64_tr_b16 v[144:145], %1 offset:16384\n\t"
    "ds_read_b64_tr_b16 v[146:147], %1 offset:16896\n\t"
    "ds_read_b64_tr_b16 v[148:149], %2 offset:16384\n\t"
    "ds_read_b64_tr_b16 v[150:151], %2 offset:16896\n\t"
    "ds_read_b64_tr_b16 v[152:153], %1 offset:20480\n\t"
    "ds_read_b64_tr_b16 v[154:155], %1 offset:20992\n\t"
    "ds_read_b64_tr_b16 v[156:157], %2 offset:20480\n\t"
    "ds_read_b64_tr_b16 v[158:159], %2 offset:20992\n\t"
    "s_waitcnt lgkmcnt(0)"
    : "=&{v[128:159]}"(w) : "v"(a0), "v"(a1) : "memory");
}
template <> __device__ __forceinline__ void gw_load<1>(u32x32& w, unsigned a0, unsigned a1) {
  asm volatile("ds_read_b64_tr_b16 v[160:161], %1 offset:0\n\t"
    "ds_read_b64_tr_b16 v[162:163], %1 offset:512\n\t"
    "ds_read_b64_tr_b16 v[164:165], %2 offset:0\n\t"
    "ds_read_b64_tr_b16 v[166:167], %2 offset:512\n\t"
    "ds_read_b64_tr_b16 v[168:169], %1 offset:4096\n\t"
    "ds_read_b64_tr_b16 v[170:171], %1 offset:4608\n\t"
    "ds_read_b64_tr_b16 v[172:173], %2 offset:4096\n\t"
    "ds_read_b64_tr_b16 v[174:175], %2 offset:4608\n\t"
    "s_waitcnt lgkmcnt(0)\n\t"
    "ds_read_b64_tr_b16 v[176:177], %1 offset:16384\n\t"
    "ds_read_b64_tr_b16 v[178:179], %1 offset:16896\n\t"
    "ds_read_b64_tr_b16 v[180:181], %2 offset:16384\n\t"
    "ds_read_b64_tr_b16 v[182:183], %2 offset:16896\n\t"
    "ds_read_b64_tr_b16 v[184:185], %1 offset:20480\n\t"
    "ds_read_b64_tr_b16 v[186:187], %1 offset:20992\n\t"
    "ds_read_b64_tr_b16 v[188:189], %2 offset:20480\n\t"
    "ds_read_b64_tr_b16 v[190:191], %2 offset:20992\n\t"
    "s_waitcnt lgkmcnt(0)"
    : "=&{v[160:191]}"(w) : "v"(a0), "v"(a1) : "memory");
}
template <> __device__ __forceinline__ void gw_load<2>(u32x32& w, unsigned a0, unsigned a1) {
  asm volatile("ds_read_b64_tr_b16 v[192:193], %1 offset:0\n\t"
    "ds_read_b64_tr_b16 v[194:195], %1 offset:512\n\t"
    "ds_read_b64_tr_b16 v[196:197], %2 offset:0\n\t"
    "ds_read_b64_tr_b16 v[198:199], %2 offset:512\n\t"
    "ds_read_b64_tr_b16 v[200:201], %1 offset:4096\n\t"
    "ds_read_b64_tr_b16 v[202:203], %1 offset:4608\n\t"
    "ds_read_b64_tr_b16 v[204:205], %2 offset:4096\n\t"
    "ds_read_b64_tr_b16 v[206:207], %2 offset:4608\n\t"
    "s_waitcnt lgkmcnt(0)\n\t"
    "ds_read_b64_tr_b16 v[208:209], %1 offset:16384\n\t"
    "ds_read_b64_tr_b16 v[210:211], %1 offset:16896\n\t"
    "ds_read_b64_tr_b16 v[212:213], %2 offset:16384\n\t"
    "ds_read_b64_tr_b16 v[214:215], %2 offset:16896\n\t"
    "ds_read_b64_tr_b16 v[216:217], %1 offset:20480\n\t"
    "ds_read_b64_tr_b16 v[218:219], %1 offset:20992\n\t"
    "ds_read_b64_tr_b16 v[220:221], %2 offset:20480\n\t"
    "ds_read_b64_tr_b16 v[222:223], %2 offset:20992\n\t"
    "s_waitcnt lgkmcnt(0)"
    : "=&{v[192:223]}"(w) : "v"(a0), "v"(a1) : "memory");
}
template <> __device__ __forceinline__ void gw_load<3>(u32x32& w, unsigned a0, unsigned a1) {
  asm volatile("ds_read_b64_tr_b16 v[224:225], %1 offset:0\n\t"
    "ds_read_b64_tr_b16 v[226:227], %1 offset:512\n\t"
    "ds_read_b64_tr_b16 v[228:229], %2 offset:0\n\t"
    "ds_read_b64_tr_b16 v[230:231], %2 offset:512\n\t"
    "ds_read_b64_tr_b16 v[232:233], %1 offset:4096\n\t"
    "ds_read_b64_tr_b16 v[234:235], %1 offset:4608\n\t"
    "ds_read_b64_tr_b16 v[236:237], %2 offset:4096\n\t"
    "ds_read_b64_tr_b16 v[238:239], %2 offset:4608\n\t"
    "s_waitcnt lgkmcnt(0)\n\t"
    "ds_read_b64_tr_b16 v[240:241], %1 offset:16384\n\t"
    "ds_read_b64_tr_b16 v[242:243], %1 offset:16896\n\t"
    "ds_read_b64_tr_b16 v[244:245], %2 offset:16384\n\t"
    "ds_read_b64_tr_b16 v[246:247], %2 offset:16896\n\t"
    "ds_read_b64_tr_b16 v[248:249], %1 offset:20480\n\t"
    "ds_read_b64_tr_b16 v[250:251], %1 offset:20992\n\t"
    "ds_read_b64_tr_b16 v[252:253], %2 offset:20480\n\t"
    "ds_read_b64_tr_b16 v[254:255], %2 offset:20992\n\t"
    "s_waitcnt lgkmcnt(0)"
    : "=&{v[224:255]}"(w) : "v"(a0), "v"(a1) : "memory");
}
template <int I, int E, class F> __device__ __forceinline__ void sfor(F&& f) { if constexpr (I < E) { f(std::integral_constant<int, I>{}); sfor<I + 1, E>(f); } }
#define W2C 0
#define FL63 0
template <int ROLE, int KQ, class F>
__device__ __forceinline__ void wload(char* smem, int wave_id_, const u16* W0, const u16* W1, int ldb, u32x32 (&bw)[4], int rot, F&& hook) {
  constexpr int ABUF = 65536;
  const int tid = opaque_tid(), lane = tid & 63, wave = WAVE_ID;
  const int kq = KQ == 2 ? wave >> 2 : 0, nq = KQ == 2 ? wave & 3 : wave;
  const int g = lane >> 4, r16 = lane & 15, q4 = r16 >> 2, p4 = lane & 3;
  const unsigned ldsb = __builtin_amdgcn_readfirstlane((unsigned)(size_t)(LAS char*)smem);
  {
    const int srow = 8 * wave + (lane >> 3), ch = (lane & 7) ^ bswz(srow);
    const unsigned voff = (unsigned)(srow * ldb + ch * 8) * 2;
    const char* w0 = uni((const char*)W0); const char* w1 = uni((const char*)(ROLE == R_F1 ? W1 : W0 + 64));
    const size_t kb = (size_t)64 * ldb * 2;
    auto wstage = [&](int r) {
      const unsigned ld = ldsb + (r & 1) * ABUF + wave * 1024;
#pragma unroll
      for (int i = 0; i < 8; ++i) {
        const int kbi = KQ == 2 ? (ROLE == R_PART2 ? 0 : (i >> 2) * 8) + ((2 * r + ((i >> 1) & 1)) ^ rot) : 2 * r + (i >> 2);
        const char* src = KQ == 2 ? ((i & 1) ? w1 : w0) + kbi * kb + (ROLE == R_PART2 ? (i >> 2) * 256 : 0) : w0 + kbi * kb + (i & 3) * 128;
        unsigned keep;
        if (W2C && (ROLE == R_PART || ROLE == R_PART2)) glds16s<0>(src, voff, ld + i * 8192);
        else
        asm volatile("s_mov_b32 %0, m0\n\ts_mov_b32 m0, %3\n\ts_nop 2\n\tglobal_load_lds_dwordx4 %1, %2" WNT_ "\n\ts_mov_b32 m0, %0"
                     : "=&s"(keep) : "v"(voff), "s"(src), "s"(ld + i * 8192) : "memory");
      }
    };
    const int brow = 8 * g + q4, bs = bswz(brow);
    unsigned la[2];
#pragma unroll
    for (int j = 0; j < 2; ++j) {
      int blk, c16, sub;
      if (ROLE == R_F1) { blk = j; c16 = nq * 2 + (p4 >> 1); sub = (p4 & 1) * 8; }
      else { blk = nq >> 1; c16 = (nq & 1) * 4 + p4; sub = j * 8; }
      int set = kq;
      if (ROLE == R_PART2 && TLP) { set = nq >> 1; blk = nq & 1; c16 = kq * 4 + p4; }
      la[j] = (unsigned)((KQ == 2 ? set * 4 + blk : blk) * 8192 + brow * 128 + ((c16 ^ bs) << 4) + sub);
    }
    hook(4);
    wstage(0); wstage(1);
    sfor<0, 4>([&](auto r_c) __attribute__((always_inline)) {
      constexpr int r = decltype(r_c)::value;
      if (r < 3) WAIT_V(8); else WAIT_V(0);
      __builtin_amdgcn_s_barrier(); asm volatile("" ::: "memory");
      gw_load<r>(bw[r], ldsb + (r & 1) * ABUF + la[0], ldsb + (r & 1) * ABUF + la[1]);
      BAR();
      hook(r);
      if (r + 2 < 4) wstage(r + 2);
    });
  }
}
#define DSR2(d0, d1, a0_, a1_, off) asm volatile("ds_read_b128 %0, %2 offset:%4\n\tds_read_b128 %1, %3 offset:%4" : "=&v"(d0), "=&v"(d1) : "v"(a0_), "v"(a1_), "i"(off))
template <int N> __device__ __forceinline__ void waitl(u32x4& a, u32x4& b) { asm volatile("s_waitcnt lgkmcnt(%2)" : "+v"(a), "+v"(b) : "i"(N)); }
__device__ __forceinline__ void glds1(const char* sbase, unsigned voff, unsigned lds) {
  unsigned keep;
  asm volatile("s_mov_b32 %0, m0\n\ts_mov_b32 m0, %3\n\ts_nop 2\n\tglobal_load_lds_dwordx4 %1, %2 sc1\n\ts_mov_b32 m0, %0"
               : "=&s"(keep) : "v"(voff), "s"(sbase), "s"(lds) : "memory");
}
#define XS(i) do { } while (0)
#define XFL(b_) do { } while (0)
#define XA_ 0
#define XF_ 0
#define XE_ 0
#define XP_ 0
#define PUBK 9
#define LATE 0
#define NOWP 0
#define TB 48
#define TDM 1
#define TDA 15
#define TPUB 1
#define DUR0 160
#define PUP 4
#define PDQ 5
#define PDM 2
#define PDEEP 5
template <int ROLE>
__device__ __forceinline__ void gemm7(char* smem, int wave_id_, const u16* W0, const u16* W1, int ldb, const char* A_, size_t a_blk, Dep dep,
                                      char* C_, size_t c_blk, unsigned*& cnt, int myw, unsigned* tmo, int slot, unsigned long long* mts,
                                      bool gl, int nimm = 0, int nseg = NSEG, int wm0 = 0, int pofs = 0, u64 tag = 0, int b0 = 0) {
  dep.base = (const char*)cnt;
  unsigned* myprog = cnt + myw;
  volatile LAS unsigned* wsl = (volatile LAS unsigned*)(smem + L_SLOT + 512);
  if (opaque_tid() < 144) *(volatile LAS unsigned*)(smem + L_SLOT + opaque_tid() * 4) = 0u;
  const int tid = opaque_tid(), lane = tid & 63, wave = WAVE_ID;
  const int kq = wave >> 2, nq = wave & 3;
  const unsigned ldsb = __builtin_amdgcn_readfirstlane((unsigned)(size_t)(LAS char*)smem);
  constexpr int rot = 0;
  const char* A = uni(A_) + nq * 1024;
  const char* Cu = uni(C_);
  constexpr int PD = PDM;
  constexpr bool P2C = ROLE == R_PART2;
  int pw = 0;
  int wm = wm0, nh = 2 * b0;
  constexpr bool SPEC = ROLE != R_QKV;
  const int sbi = gl ? SC : SB, sbo = ROLE == R_F1 || gl ? SC : SB;
  auto lgo = [&](int b, unsigned& v, unsigned& u, bool pre, bool force) {
    if (wave == 4) {
      for (int spins = 0; __builtin_amdgcn_readfirstlane(wsl[2]) != (unsigned)(b + 1); ++spins) {
        if (!pre) ld_fb(v, u, dep.base, dep.voff);
        const bool ok = (int)(v * dep.mul + dep.add) > b;
        if (__all(ok)) break;
        if (SPEC && !force && bells(ok, u, b, b0 ? MARH : MARG, sbi)) break;
        if (!pre) { if (gl) __builtin_amdgcn_s_sleep(GSL); else __builtin_amdgcn_s_sleep(XSL); }
        pre = false;
        if (give_up(spins, tmo, 9)) { v = 255u; break; }
      }
      wsl[2] = (unsigned)(b + 1);
    } else {
      while (__builtin_amdgcn_readfirstlane(wsl[2]) != (unsigned)(b + 1)) __builtin_amdgcn_s_sleep(1);
    }
  };
  auto lwm = [&](int b, unsigned v) {
    unsigned w;
    if (wave == 4) {
      const int w2 = (int)dep_eval(v, dep);
      w = (unsigned)min(max(max(w2, wm), b + 1), 255);
      wsl[0] = ((unsigned)(b + 1) << 8) | w;
    } else {
      for (;;) {
        w = __builtin_amdgcn_readfirstlane(wsl[0]);
        if ((w >> 8) == (unsigned)(b + 1)) break;
        __builtin_amdgcn_s_sleep(1);
      }
      w &= 255u;
    }
    return (int)w;
  };
  auto pubw = [&](int n) {
    WAIT_V(0);
    if (lane == 0) {
      const unsigned o = __hip_atomic_fetch_add((LAS unsigned*)(smem + L_SLOT + 516 + ((n + pofs) & 1) * 36), 1u, __ATOMIC_RELAXED, __HIP_MEMORY_SCOPE_WORKGROUP);
      if ((o & 3u) == 3u) {
        st4_sc1(myprog, (unsigned)(n + pofs));
      }
    }
    asm volatile("s_waitcnt lgkmcnt(0)" ::: "memory");
  };
  auto hsrc = [&](int hh) { return A + (size_t)(hh >> 1) * a_blk + ((hh ^ (rot >> 2)) & 1) * 16384; };
  auto hlds = [&](int hh) { return ldsb + (unsigned)(((hh >> 1) & 1) * 65536 + (hh & 1) * 16384 + nq * 1024); };
#define POFF(i_) (((i_) & 1) * 32768 + ((i_) >> 1) * 4096)
#define PIECE(s_, l_, i_) asm volatile("s_add_u32 m0, %2, %3\n\ts_nop 0\n\tglobal_load_lds_dwordx4 %0, %1 sc1" :: "v"(av[i_]), "s"(s_), "s"(l_), "i"(POFF(i_)) : "memory", "scc", "m0")
  int na = max(sbo, b0);
  auto arm = [&](int bb) {
    unsigned z = SENT; asm volatile("" : "+v"(z));
    constexpr int N = ROLE == R_F1 ? 1 : (P2C ? 4 : 2);
    const unsigned o = nq * N * 1024 + lane_id() * 16;
    const char* p = Cu + (size_t)bb * c_blk;
#pragma unroll
    for (int i = 0; i < N; ++i) st16s(p, o + i * 1024, u32x4{z, z, z, z});
  };
  u32x32 BW[4];
  wload<ROLE, 2>(smem, wave_id_, W0, W1, ldb, BW, rot, [&](int r) {
    if (ROLE == R_QKV && nimm > 0 && wave == 0) STS((char*)tmo - OFF_TMO, 128 + slot, 3 + r);
    if (SPEC && r == 2 && (ROLE != R_PART || gl)) { for (na += kq; na < nseg; na += 2) arm(na); na = nseg + 1; }
    if (ROLE == R_QKV && nimm > 0 && r == 1 && wave == 4) init_issue((char*)tmo - OFF_TMO, ldsb + L_RED);
    if (ROLE == R_QKV && nimm > 0 && r == 2) {
      cnt = cnt_of((char*)tmo - OFF_TMO, init_get((char*)tmo - OFF_TMO, tag, smem + L_RED));
      dep.base = (const char*)cnt; myprog = cnt + myw;
      if (kq) {
        const unsigned l16 = lane_id() * 16;
#pragma unroll
        for (int i = 0; i < 16; ++i) glds1(hsrc(i >> 3) + (i & 1) * 32768 + ((((i & 7) >> 1) ^ rot) & 3) * 4096, l16, hlds(i >> 3) + POFF(i & 7));
      }
      nh = 2; wm = 8;
    }
  });
  if (ROLE == R_QKV && nimm > 0 && opaque_tid() == 0) STS((char*)tmo - OFF_TMO, 128 + slot, 0);
  if (wave == 0) WFB();
  auto body = [&](auto ldr_c) __attribute__((always_inline)) {
    constexpr bool LDR = decltype(ldr_c)::value;
    if (XP_ == 0 ? !LDR : (XP_ == 1 ? LDR : false)) __builtin_amdgcn_s_setprio(1);
    unsigned av[8];
#pragma unroll
    for (int i = 0; i < 8; ++i) av[i] = lane * 16 + (P2C ? 0 : (i & 1) * 32768) + (((i >> 1) ^ rot) & 3) * 4096;
    auto issue_all = [&](int hh) {
      if (LDR) {
        const char* s_ = hsrc(hh); const unsigned l_ = hlds(hh);
#pragma unroll
        for (int i = 0; i < 8; ++i) if (!(P2C && (i & 1))) PIECE(s_, l_, i);
      }
    };
    int b = b0;
    bool pre = false;
    unsigned smu = 0, tk = 0;
    unsigned smp = 0, got = 0;
    while (b < nseg) {
      const bool fast = (wm < nseg ? wm : nseg) >= b + PD && b >= nimm && nh <= 2 * b + 2;
      if (SPEC && b >= TB && !fast) break;
      const bool pl = wm <= b || pre, cold = nh < 2 * b + 2, nh3 = nh == 2 * b + 3;
      if (!fast) {
        XS(0);
        if (LDR && wm <= b && !got) lgo(b, smp, smu, false, true);
        XS(1);
        if (got) nh += NEARLY / 8;
        { const int upto = nh3 ? 2 * b + 4 : 2 * b + 2; for (; nh < upto; ++nh) issue_all(nh); }
        if (LDR && pl) wm = lwm(b, smp);
        XS(2);
      }
      const int ln = lane_id(), g = ln >> 4, r16 = ln & 15;
      const unsigned e0 = ldsb + (unsigned)((P2C ? 0 : kq * 32768) + (b & 1) * 65536 + (gl ? (g >> 1) * 512 + r16 * 32 + (g & 1) * 16 : r16 * 128 + ((g ^ (r16 & 7)) << 4)));
      const unsigned d0 = e0 ^ (gl ? 1024u : 64u);
      const unsigned rw = ldsb + L_RED + nq * 4096 + ln * 16;
      const unsigned co0 = ROLE == R_F1 ? (unsigned)((g & 1) * 2048 + nq * 512 + r16 * 32 + (g >> 1) * 16)
                         : ((ROLE == R_QKV ? TLQ : TLP) ? (unsigned)(nq * (P2C ? 4096 : 2048) + r16 * 64 + g * 16)
                         : (ROLE == R_QKV ? (unsigned)(r16 * 256 + (((4 * nq + g) ^ r16) << 4)) : (unsigned)(r16 * PROW + (32 * nq + 8 * g) * 2)));
      const unsigned co1 = co0 + ((ROLE == R_QKV ? TLQ : TLP) ? 1024u : (ROLE == R_QKV ? 4096u : 16u * PROW));
      u32x16 pv_;
      bool pr2 = false;

      if (fast) {
        __builtin_amdgcn_s_barrier(); asm volatile("" ::: "memory");
        const int LA = (wm < nseg ? wm : nseg) >= b + (ROLE == R_QKV ? PDQ : PDEEP) ? 3 : 2;
        for (; nh < 2 * b + LA; ++nh) issue_all(nh);
        int lf = (wm < nseg ? wm : nseg) - (LA - 1), cp = nseg - (LA - 1);
        const unsigned sa = ldsb + L_SLOT + ln * 4, ad = dep.add - (unsigned)(LA - 1);
        const unsigned up = LA == 3 ? 1000u : (unsigned)PUP;
#define FARGS BW, pv_, e0, d0, b, lf, cp, up, sa, dep.mul, ad, rw, (unsigned)a_blk, hsrc(2 * b + LA), hsrc(2 * b + LA + 1), hlds(2 * b + LA), hlds(2 * b + LA + 1), av
        if constexpr (LDR) (P2C ? fast_ld2 : (ROLE == R_F1 ? fast_ld1 : fast_ld0))(FARGS, LA);
#undef FARGS
        else {
          const char* c0_ = uni((const char*)tmo - OFF_TMO + OFF_DUMP); const char* c1_ = Cu + (size_t)b * c_blk;
          const int sf = b + pofs;
          const char* pg = uni((const char*)myprog);
          (ROLE == R_F1 ? fast_st1 : (P2C ? fast_st2 : fast_st0))(BW, pv_, e0, d0, b, lf, cp, up, sa, dep.mul, ad, rw, (unsigned)c_blk, c0_, c1_, co0, co1, pofs, sf, (unsigned)wave,
                                                                   ldsb + L_SLOT, dep.voff, dep.base, pg);
        }
        wm = lf + LA - 1 > wm ? lf + LA - 1 : wm;
        nh = 2 * b + 2;
        --b;
        __builtin_amdgcn_s_barrier(); asm volatile("" ::: "memory");
      } else {
        if (LDR) { if (nh >= 2 * b + 4) { if (P2C) WAIT_V(14); else WAIT_V(28); } else { if (P2C) WAIT_V(6); else WAIT_V(12); } }
        BAR();
        XS(3);
        if (ROLE == R_QKV && nimm > 0 && b < 2 && opaque_tid() == 0) STS((char*)tmo - OFF_TMO, 128 + slot, 1 + b);
        if constexpr (!LDR) {
          const unsigned wv = wsl[0];
          unsigned st;
          lean_st0(BW, e0, d0, __builtin_amdgcn_readfirstlane(pw > 0 ? (cold ? 4u : 1u) : 0u), pv_, (unsigned)(pw + pofs), ldsb + L_SLOT + 516, uni((const char*)myprog), 0u, tk, st);
          if (pl) wm = (int)(__builtin_amdgcn_readfirstlane(wv) & 255u);
        }
        const bool dm = nh == 2 * b + 2 && wm > b + 1 && b + 1 < nseg;
        pr2 = b + 1 < nseg && wm < nseg;
        if constexpr (LDR) {
          const bool er = nh == 2 * b + 2 && !dm;
          const unsigned ps = pr2 && (wave == 4 || er) ? (unsigned)(b + 1) : 0u;
#define LARGS BW, e0, d0, (unsigned)(nh - 2 * b - 2), hlds(nh), hsrc(nh), hsrc(nh + 1), av, ps, dep.voff, dep.base, \
              rw, ldsb + L_SLOT + 528 + nq * 4, (unsigned)(b + 1), ldsb + L_SLOT + 520, dep.mul, dep.add, er ? b + 1 : 0x7FFFFFFF, 0x7FFFFFFF, smp, smu, got
          if (dm) (P2C ? lean_dm1 : lean_dm0)(LARGS); else (P2C ? lean_ld1 : lean_ld0)(LARGS);
#undef LARGS
          got = __builtin_amdgcn_readfirstlane(got);
        }
        if (dm) nh += 2;
        XS(5);
      }
      unsigned bad = 0, pk = 0;
      if constexpr (LDR) { if (fast) fin_ld(pv_, rw, ldsb + L_SLOT + 528 + nq * 4, (unsigned)(b + 1)); }
      else (ROLE == R_F1 ? fin_st1 : (P2C ? fin_st2 : fin_st0))(pv_, rw, ldsb + L_SLOT + 528 + nq * 4, (unsigned)(b + 1), Cu + (size_t)b * c_blk, co0, co1, ldsb + L_SLOT + 524, bad, pk);
      XS(6);
      pre = pr2;
      bool now = (nh < 2 * b + 4 && wm < b + 1 + PD) || b + 1 >= nseg || b < nimm;
      if (!LDR && now) {
        if (NOWP == 0 && b >= nimm && pk == (unsigned)(b + 2)) now = false;
        else pubw(b + 1);
      }
      XS(7);
      XFL(b);
      pw = now ? 0 : b + 1;
      if (SPEC && !LDR && !fast && b >= 16 && na < nseg && na > b + 1) { arm(na); arm(na + 1); na += 2; }
      ++b;
    }
    if (SPEC && b < nseg) {
      static_assert(!SPEC || (NEARLY == 16 && TLP), "");
      const int ln = lane_id(), g = ln >> 4, r16 = ln & 15;
      const unsigned e0 = ldsb + (unsigned)((P2C ? 0 : kq * 32768) + (gl ? (g >> 1) * 512 + r16 * 32 + (g & 1) * 16 : r16 * 128 + ((g ^ (r16 & 7)) << 4)));
      const unsigned rw = ldsb + L_RED + nq * 4096 + ln * 16;
      const unsigned co0 = ROLE == R_F1 ? (unsigned)((g & 1) * 2048 + nq * 512 + r16 * 32 + (g >> 1) * 16) : (unsigned)(nq * (P2C ? 4096 : 2048) + r16 * 64 + g * 16);
      const char* pg = uni((const char*)myprog);
      unsigned fa = ldsb + L_SLOT + 528 + nq * 4; asm volatile("" : "+v"(fa));
      bool force = false, dmx = false;
      unsigned dur = DUR0, dp = 0, x8 = __builtin_amdgcn_readfirstlane(wave == 0 ? 8u : 0u);
      if (nh >= 2 * b + 2) got = 1;
      pre = false;
      for (;;) {
        unsigned rd;
        XS(0);
        if (LDR && b < nseg) {
          if (!got) { lgo(b, smp, smu, pre, force); issue_all(2 * b); issue_all(2 * b + 1); }
          XS(2);
          if (P2C) WAIT_V(6); else WAIT_V(12);
        }
        if (!LDR && b >= sbo && na <= nseg) { for (na = max(na, b); na < nseg; ++na) arm(na); WAIT_V(0); ++na; }
        const unsigned ea = e0 + (b & 1) * 65536;
        BAR();
        XS(3);
        if (b >= nseg) rd = __builtin_amdgcn_readfirstlane(wsl[8]);
        else if constexpr (LDR) {
          const int th = b + 1 < nseg ? b + 1 : 0;
#define LARGS BW, ea, ea ^ (gl ? 1024u : 64u), 0u, hlds(2 * b + 2), hsrc(2 * b + 2), hsrc(2 * b + 3), av, (unsigned)th, dep.voff, dep.base, \
              rw, ldsb + L_SLOT + 528 + nq * 4, (unsigned)(b + 1), ldsb + L_SLOT + 520, dep.mul, dep.add, th, th >= sbi ? th : 0x7FFFFFFF, smp, smu, got
          if (dmx) {
            *(volatile LAS u32x2*)(wsl + 2) = u32x2{(unsigned)(b + 2), (unsigned)(b + 2)};
            (P2C ? lean_dm1 : lean_dm0)(LARGS);
          } else (P2C ? lean_ld1 : lean_ld0)(LARGS);
#undef LARGS
          got = __builtin_amdgcn_readfirstlane(got) | (dmx ? 1u : 0u);
          rd = got >> 1;
          dmx = TDM && got == 1u && b + 2 < nseg
             && __all((int)(smp * dep.mul + dep.add) > b + 2 || (b + 2 >= sbi && (int)(smu >> 24) + ((int)((rt_now() + (dmx ? TDA : 0) - MARG - smu) << 8) >> 31) > b + 2));
          pre = true;
        } else {
          unsigned bad, pk;
          (ROLE == R_F1 ? lean_ep1 : (P2C ? lean_ep2 : lean_ep0))(BW, ea, ea ^ (gl ? 1024u : 64u), (unsigned)min(pw, 1) | (b >= sbo - 2 ? x8 : 0u), (unsigned)pw, ldsb + L_SLOT + 516, pg,
              (unsigned)(b + 1) << 24 | dur, tk, rd, rw, fa, (unsigned)(b + 1), Cu + (size_t)b * c_blk, co0, co0 + 1024u, bad, pk);
          XS(6);
          if (!rd) {
            pw = b + 1;
            { const unsigned d = min(tk, 300u); dur = max(d, dp); dp = d; }
            if (bad && !force && b >= sbi) wsl[8] = 1u;
            else if ((ROLE == R_PART && !gl && b == sbo - 1) || pk != (unsigned)(b + 2)) {
              pubw(b + 1);
              pw = 0;
            }
            if (na < nseg && na > b + 1) { arm(na); arm(na + 1); na += 2; }
          }
        }
        XS(7);
        if (rd) {
          WAIT_V(0);
          BAR();
          if (wave == 4 && ln < 11) wsl[ln] = 0u;
          BAR();
          --b; pw = 0; pre = false; got = 0; force = true;
          continue;
        }
        if (b >= nseg) break;
        force = false;
        ++b;
      }
    }
    __builtin_amdgcn_s_setprio(0);
  };
  if (kq) body(std::true_type{}); else body(std::false_type{});
#undef PIECE
#undef POFF
  WAIT_V(0);
  BAR();
}
__device__ __forceinline__ void fin_role(char* smem, int wave_id_, int fidx, const float* mod, const char* P2, const u16* x1, u16* out, unsigned* cnt, unsigned* tmo,
                                         unsigned long long* mts) {
  const int tid = opaque_tid(), lane = tid & 63, wave = WAVE_ID;
  row_table(smem + L_GB, mod, cnt, tmo, tid);
  volatile LAS unsigned* wsl = (volatile LAS unsigned*)(smem + L_SLOT + 512);
  int wf = 0, par = 0;
  const unsigned pf2 = (unsigned)(PF2 + ((lane & 31) < 28 ? (lane & 31) : 0)) * 128, pln = (unsigned)((PLN + (lane >> 3)) * 128 + (lane & 7) * 4);
  for (int nf = fidx >> 3; nf < NSEG; nf += !FL63 || nf < NSEG - 3 ? 2 : (nf == NSEG - 2 ? 1 : 3)) {
    if (wf <= nf) {
      if (wave == 0) {
        unsigned w;
        for (int spins = 0;; ++spins) {
          unsigned v, u;
          ld_fb(v, u, cnt, pf2 + (nf >= HC ? 4 : (nf >= HB ? 8 : 0)));
          if (!__all(v > (unsigned)nf) && bells(v > (unsigned)nf, u, nf, nf >= HB ? MARX : MARF, SC)) { w = 0; break; }
          v = rowmin16(v);
          w = min(rdl(v, 0), rdl(v, 16));
          if ((int)w > nf) break;
          __builtin_amdgcn_s_sleep(FSL);
          if (give_up(spins, tmo, 9)) { w = NSEG; break; }
        }
        if (lane == 0) wsl[par] = w;
      }
      BAR();
      wf = (int)__builtin_amdgcn_readfirstlane(wsl[par]);
      par ^= 1;
    }
    int ln = lane; asm volatile("" : "+v"(ln));
    if (wave == 0) WTS(256 + 8 + fidx, nf / 2, 0);
    bool fin = nf < SB;
    for (int spins = 0; !fin_half(fin, P2, x1, smem + L_GB + 12288, out, nf * 32 + 4 * (fidx & 7) + (wave >> 1), wave & 1, ln); ++spins)
      if (spins > 1) fin = __all(ld4_sc1((const char*)cnt + pf2 + (nf >= HC ? 4 : (nf >= HB ? 8 : 0))) > (unsigned)nf && ld4_sc1((const char*)cnt + pln) > (unsigned)nf) || give_up(spins, tmo, 9);
    if (wave == 0) WTS(256 + 8 + fidx, nf / 2, 2);
  }
}
#define LD8(r, vo, sb) asm volatile("global_load_dwordx4 %0, %8, %12 sc1\n\tglobal_load_dwordx4 %1, %9, %12 sc1\n\t" \
    "global_load_dwordx4 %2, %10, %12 sc1\n\tglobal_load_dwordx4 %3, %11, %12 sc1\n\t" \
    "global_load_dwordx4 %4, %8, %13 sc1\n\tglobal_load_dwordx4 %5, %9, %13 sc1\n\t" \
    "global_load_dwordx4 %6, %10, %13 sc1\n\tglobal_load_dwordx4 %7, %11, %13 sc1" \
    : "=&v"(r[0][0]), "=&v"(r[0][1]), "=&v"(r[0][2]), "=&v"(r[0][3]), "=&v"(r[1][0]), "=&v"(r[1][1]), "=&v"(r[1][2]), "=&v"(r[1][3]) \
    : "v"(vo), "v"(vo ^ (TLQ ? 2048u : 64u)), "v"(vo ^ (TLQ ? 4096u : 128u)), "v"(vo ^ (TLQ ? 6144u : 192u)), "s"(sb), "s"((sb) + (TLQ ? 1024 : 4096)) : "memory")
#define TIE8(r) "+v"(r[0][0]), "+v"(r[0][1]), "+v"(r[0][2]), "+v"(r[0][3]), "+v"(r[1][0]), "+v"(r[1][1]), "+v"(r[1][2]), "+v"(r[1][3])
#define SADL 255
#define SAE 110
#define DSA 20
#define SASL 8
__device__ __forceinline__ void sa_role(char* smem, int wave_id_, int hbase, const char* __restrict__ Q, const char* __restrict__ Kx, const char* __restrict__ V,
                                        const char* kvm, char* __restrict__ O, unsigned* cnt, unsigned* tmo, int slot, unsigned long long* mts) {
  constexpr float LS = LOG2E * SCALE;
  const int tid = opaque_tid(), wave = WAVE_ID;
  const int h = hbase + (wave & 1), s0 = wave >> 1;
  char* vl = smem + wave * 17408;
  const unsigned ldsw = __builtin_amdgcn_readfirstlane((unsigned)(size_t)(LAS char*)vl);
  unsigned avail = 0;
  {
    static_assert(SB % 4 == 0, "");
    unsigned z = SENT; asm volatile("" : "+v"(z));
    for (int s = SB + s0; s < NSEG; s += 4)
#pragma unroll
      for (int i = 0; i < 8; ++i) st16s(uni(O + ((size_t)s * 64 + 2 * h) * 4096), (unsigned)(i * 1024 + (tid & 63) * 16), u32x4{z, z, z, z});
  }
  for (int s = s0, it = 1; s < NSEG; s += 4, ++it) {
    u32x4 qv[2][4], kv[2][4];
    int lane = tid & 63; asm volatile("" : "+v"(lane));
    const int g = lane >> 4, r16 = lane & 15, q4 = r16 >> 2, p4 = lane & 3;
    auto request = [&](int sr) {
      const size_t o = ((size_t)sr * NH + h) * 8192;
      const char* qs = uni(Q + o); const char* ks = uni(Kx + o); const char* vs = uni(V + o);
      const unsigned fo = TLQ ? (unsigned)(r16 * 64 + g * 16) : (unsigned)(r16 * 256 + ((g ^ r16) << 4));
      LD8(qv, fo, qs); LD8(kv, fo, ks);
      head_to_lds(vs, lane, ldsw);
    };
    {
      for (int spins = 0; avail <= (unsigned)s; ++spins) {
        const unsigned p = ld4_sc1(cnt + (PQKV + h) * 32 + (lane & 15));
        avail = min(min(qkv_avail(p, 0), qkv_avail(p, 1)), qkv_avail(p, 2));
        if (avail > (unsigned)s) break;
        __builtin_amdgcn_s_sleep(32);
        if (give_up(spins, tmo, 10)) break;
      }
      request(s);
      asm volatile("s_waitcnt vmcnt(0)" : TIE8(qv), TIE8(kv) :: "memory");
    }
    WTM(slot, s, 0);
    f32x4 s00 = f32x4{0, 0, 0, 0}, s01 = s00, s11 = s00;
#pragma unroll
    for (int ks = 0; ks < 4; ++ks) {
      s00 = MFMA32(asbf8(kv[0][ks]), asbf8(qv[0][ks]), s00);
      s01 = MFMA32(asbf8(kv[0][ks]), asbf8(qv[1][ks]), s01);
      s11 = MFMA32(asbf8(kv[1][ks]), asbf8(qv[1][ks]), s11);
    }
    float mx[2], sum[2];
    u32x4 pseg[2];
    {
#pragma unroll
      for (int r = 0; r < 4; ++r) { const bool ok = 4 * g + r <= r16; s00[r] = ok ? s00[r] : -3.0e38f; s11[r] = ok ? s11[r] : -3.0e38f; }
      mx[0] = red16_32_max(max3(max3(s00[0], s00[1], s00[2]), s00[3], -1.0e38f));
      mx[1] = red16_32_max(max3(max3(fmaxf(fmaxf(s01[0], s01[1]), fmaxf(s01[2], s01[3])), s11[0], -1.0e38f), max3(s11[1], s11[2], s11[3]), -1.0e38f));
      const float n0 = -mx[0] * LS, n1 = -mx[1] * LS;
#pragma unroll
      for (int r = 0; r < 4; ++r) {
        s00[r] = __builtin_amdgcn_exp2f(s00[r] * LS + n0); s01[r] = __builtin_amdgcn_exp2f(s01[r] * LS + n1); s11[r] = __builtin_amdgcn_exp2f(s11[r] * LS + n1);
      }
      sum[0] = (s00[0] + s00[1]) + (s00[2] + s00[3]);
      sum[1] = ((s01[0] + s01[1]) + (s01[2] + s01[3])) + ((s11[0] + s11[1]) + (s11[2] + s11[3]));
      pseg[0] = u32x4{pack2(s00[0], s00[1]), pack2(s00[2], s00[3]), 0u, 0u};
      pseg[1] = u32x4{pack2(s01[0], s01[1]), pack2(s01[2], s01[3]), pack2(s11[0], s11[1]), pack2(s11[2], s11[3])};
    }
    f32x4 o[8][2];
#pragma unroll
    for (int dt = 0; dt < 8; ++dt) {
      const int row0 = 4 * g + q4;
      const unsigned xo = (unsigned)(row0 * 256 + (((4 * (dt >> 1) + p4) ^ row0) << 4) + (dt & 1) * 8);
      const u32x2 lo = trread(vl, xo), hi = trread(vl, xo + 4096);
      o[dt][0] = MFMA32(asbf8(u32x4{lo.x, lo.y, 0u, 0u}), asbf8(pseg[0]), (f32x4{0, 0, 0, 0}));
      o[dt][1] = MFMA32(asbf8(u32x4{lo.x, lo.y, hi.x, hi.y}), asbf8(pseg[1]), (f32x4{0, 0, 0, 0}));
    }
    float n0[2]; f32x4 sm[2];
#pragma unroll
    for (int b = 0; b < 2; ++b) { const float t = red16_32_add(sum[b]); sm[b] = f32x4{t, t, t, t}; n0[b] = -mx[b] * LS; }
    unsigned oa = (unsigned)(r16 * 128 + ((g ^ (r16 & 7)) << 4)), ka = (unsigned)(g * 256 + (r16 & 7) * 16), va = (unsigned)lane * 16;
    unsigned one = g < 2 ? 0x3F803F80u : 0u;
    u32x4 kmv[4], vmv[4];
#pragma unroll
    for (int i = 0; i < 4; ++i) vmv[i] = u32x4{0u, 0u, 0u, 0u};
#pragma unroll
    for (int dt = 0; dt < 8; ++dt) asm volatile("" : "+v"(o[dt][0]), "+v"(o[dt][1]), "+v"(vmv[dt >> 1]));
    asm volatile("s_waitcnt lgkmcnt(0)" : "+v"(sm[0]), "+v"(sm[1]), "+v"(n0[0]), "+v"(n0[1]), "+v"(oa), "+v"(ka), "+v"(va), "+v"(one) :: "memory");
    unsigned pg;
    for (int spins = 0;; ++spins) {
      pg = ld4_sc1(cnt + (PROG + h) * 32 + ((lane & 4) ? 5 : (lane & 3)));
      if (__all((pg >> 24) + (s >= SDIR ? 1u : 0u) > (unsigned)s)) break;
      __builtin_amdgcn_s_sleep(SASL);
      if (give_up(spins, tmo, 6)) break;
    }
    if (__all((pg >> 24) == (unsigned)s)) wait_rt(latest(pg), s == NSEG - 1 ? SADL - LAST : SADL);
    const char* mk = uni(kvm + ((size_t)s * NH + h) * 4096);
    for (int spins = 0;; ++spins) {
      asm volatile("global_load_dwordx4 %0, %8, %10 sc1\n\tglobal_load_dwordx4 %1, %8, %10 offset:128 sc1\n\t"
                   "global_load_dwordx4 %2, %8, %10 offset:1024 sc1\n\tglobal_load_dwordx4 %3, %8, %10 offset:1152 sc1\n\ts_mov_b32 exec_hi, 0\n\t"
                   "global_load_dwordx4 %4, %9, %10 offset:2048 sc1\n\tglobal_load_dwordx4 %5, %9, %10 offset:2560 sc1\n\t"
                   "global_load_dwordx4 %6, %9, %10 offset:3072 sc1\n\tglobal_load_dwordx4 %7, %9, %10 offset:3584 sc1\n\ts_mov_b32 exec_hi, -1\n\ts_waitcnt vmcnt(0)"
                   : "=&v"(kmv[0]), "=&v"(kmv[1]), "=&v"(kmv[2]), "=&v"(kmv[3]), "+v"(vmv[0]), "+v"(vmv[1]), "+v"(vmv[2]), "+v"(vmv[3])
                   : "v"(ka), "v"(va), "s"(mk) : "memory");
      unsigned mx_ = 0;
#pragma unroll
      for (int i = 0; i < 4; ++i) mx_ = max(max(max(max(mx_, kmv[i].x), kmv[i].y), max(kmv[i].z, kmv[i].w)), max(max(vmv[i].x, vmv[i].y), max(vmv[i].z, vmv[i].w)));
      if (__all(mx_ != SENT)) break;
      __builtin_amdgcn_s_sleep(2);
      if (give_up(spins, tmo, 6)) break;
    }
    if (SAE && s >= SB && lane == 0) st4_sc1(cnt + (PSA + (hbase >> 1)) * 32 + 16 + wave, bell(s, rt_now() + SAE));
    WTM(slot, s, 1);
    f32x4 tt[2] = {f32x4{0, 0, 0, 0}, f32x4{0, 0, 0, 0}};
#pragma unroll
    for (int ks = 0; ks < 4; ++ks) { tt[0] = MFMA32(asbf8(kmv[ks]), asbf8(qv[0][ks]), tt[0]); tt[1] = MFMA32(asbf8(kmv[ks]), asbf8(qv[1][ks]), tt[1]); }
#pragma unroll
    for (int b = 0; b < 2; ++b)
#pragma unroll
      for (int r = 0; r < 4; ++r) tt[b][r] = tt[b][r] * LS + n0[b];
    if (!__all(max3(max3(tt[0][0], tt[0][1], tt[0][2]), max3(tt[0][3], tt[1][0], tt[1][1]), max2(tt[1][2], tt[1][3])) <= 40.0f)) {
#pragma unroll
      for (int b = 0; b < 2; ++b) {
        float m = max3(max3(tt[b][0], tt[b][1], tt[b][2]), tt[b][3], 0.0f);
        { float x, y; sw16(m, x, y); m = max2(x, y); }
        const float f = __builtin_amdgcn_exp2f(-m);
        tt[b] -= m; sm[b] *= f;
#pragma unroll
        for (int dt = 0; dt < 8; ++dt) o[dt][b] *= f;
      }
    }
    u32x2 pm[2]; float inv[2];
#pragma unroll
    for (int b = 0; b < 2; ++b) {
      pm[b] = u32x2{pack2(__builtin_amdgcn_exp2f(tt[b][0]), __builtin_amdgcn_exp2f(tt[b][1])), pack2(__builtin_amdgcn_exp2f(tt[b][2]), __builtin_amdgcn_exp2f(tt[b][3]))};
      sm[b] = __builtin_amdgcn_mfma_f32_16x16x16bf16_1k(ass4(u32x2{one, one}), ass4(pm[b]), sm[b], 0, 0, 0);
    }
#pragma unroll
    for (int dt = 0; dt < 8; ++dt)
#pragma unroll
      for (int b = 0; b < 2; ++b) o[dt][b] = __builtin_amdgcn_mfma_f32_16x16x16bf16_1k(ass4((dt & 1) ? u32x2{vmv[dt >> 1].z, vmv[dt >> 1].w} : u32x2{vmv[dt >> 1].x, vmv[dt >> 1].y}), ass4(pm[b]), o[dt][b], 0, 0, 0);
    inv[0] = __builtin_amdgcn_rcpf(sm[0][0]); inv[1] = __builtin_amdgcn_rcpf(sm[1][0]);
    const char* od = uni(O + ((size_t)s * 64 + 2 * h) * 4096);
#pragma unroll
    for (int j = 0; j < 4; ++j) {
#pragma unroll
      for (int b = 0; b < 2; ++b) {
        const f32x4 r0 = o[2 * j][b] * inv[b], r1 = o[2 * j + 1][b] * inv[b];
        st16s(od + (j >> 1) * 4096 + b * 2048, oa ^ ((j & 1) * 64u), u32x4{pack2(r0[0], r0[1]), pack2(r0[2], r0[3]), pack2(r1[0], r1[1]), pack2(r1[2], r1[3])});
      }
    }
    if (!SAE && s >= SB && lane == 0) st4_sc1(cnt + (PSA + (hbase >> 1)) * 32 + 16 + wave, bell(s, rt_now() + DSA));
    WTM(slot, s, 3);
    WAIT_V(0);
    if (lane == 0) st4_sc1(cnt + (PSA + (hbase >> 1)) * 32 + wave, (unsigned)it);
    WTM(slot, s, 2);
  }
}
#undef LD8
#undef TIE8

constexpr int Q_MEM = 0;
constexpr int Q_K   = 16384;
constexpr int Q_V   = 24576;
constexpr int Q_QK  = 32768;
constexpr int Q_TM  = 36864;
constexpr int Q_CB  = 40960;
constexpr int Q_RING = 32768;
constexpr int V_P1  = 0;
constexpr int V_P2  = 16384;
constexpr int NCHAIN = 96;
#define VHN 2
#define ROTH 0
#define NQQ 6
#define NQK 4
#define E_AG 9
#define RDYI -2
#define ROTW 2
#define DQU 6
#define DQM 18
#define DQD 1
#define ARSEL (xg >> 1)
#define ORSEL (xg >> 1)
#define D_Q 105
#define D_O 4
#define E_B 6
#define E_O 1900
#define KDEL 5
#define VPOS 0

typedef __bf16 bf16x16 __attribute__((ext_vector_type(16)));
typedef unsigned u32x8 __attribute__((ext_vector_type(8)));
__device__ __forceinline__ f32x4 SMF(u32x4 a, u32x8 b, f32x4 c, int idx) {
  return __builtin_amdgcn_smfmac_f32_16x16x64_bf16(__builtin_bit_cast(bf16x8, a), __builtin_bit_cast(bf16x16, b), c, idx, 0, 0);
}
__device__ __forceinline__ float fold32(float a, float b) {
  const auto p = __builtin_amdgcn_permlane32_swap(__float_as_uint(a), __float_as_uint(b), false, false);
  return __uint_as_float(p[0]) + __uint_as_float(p[1]);
}
__device__ __forceinline__ f32x2 rowsum(f32x4 x) { return f32x2{fold32(x[0], x[2]), fold32(x[1], x[3])}; }
__device__ __forceinline__ unsigned nosent1(unsigned v) { return v < 0xFFFFFFFEu ? v : 0xFFFFFFFEu; }
__device__ __forceinline__ void st4s(const char* sb, unsigned vo, unsigned v) { asm volatile("global_store_dword %0, %1, %2 sc1\n\ts_nop 0" :: "v"(vo), "v"(v), "s"(sb) : "memory"); }
__device__ __forceinline__ void st8s(const char* sb, unsigned vo, u32x2 v) { asm volatile("global_store_dwordx2 %0, %1, %2 sc1\n\ts_nop 1" :: "v"(vo), "v"(v), "s"(sb) : "memory"); }
__device__ __forceinline__ void gldsw(const char* sbase, unsigned voff, unsigned lds) {
  unsigned keep;
  asm volatile("s_mov_b32 %0, m0\n\ts_mov_b32 m0, %3\n\ts_nop 2\n\tglobal_load_lds_dwordx4 %1, %2" CNT_ "\n\ts_mov_b32 m0, %0"
               : "=&s"(keep) : "v"(voff), "s"(sbase), "s"(lds) : "memory");
}
__device__ __forceinline__ void wait_clk(unsigned t0, unsigned d) {
  while ((unsigned)__builtin_amdgcn_s_memtime() - t0 < d) __builtin_amdgcn_s_sleep(1);
}
__device__ __forceinline__ unsigned bell_time(const char* a, unsigned vo, unsigned tag, unsigned cap) {
  unsigned v;
  for (unsigned spins = 0;; ++spins) {
    asm volatile("global_load_dword %0, %1, %2 sc1\n\ts_waitcnt vmcnt(0)" : "=&v"(v) : "v"(vo), "s"(a) : "memory");
    if (__all((v >> 24) >= tag) || spins >= cap) break;
  }
  const unsigned now = (unsigned)__builtin_amdgcn_s_memrealtime() + 250u;
  const unsigned age = rowmin16((now - v) & 0xFFFFFFu);
  return now - min(min(rdl(age, 0), rdl(age, 16)), 500u);
}
#define WBAR() do { __builtin_amdgcn_s_barrier(); asm volatile("" ::: "memory"); } while (0)
#define TN(k, v) do { } while (0)
__device__ __forceinline__ void stage_proj(const char* src, unsigned lds, int tid, int wave) {
  const int row = tid >> 4, f = 2 * ((row & 1) | (((row >> 2) & 1) << 1));
#pragma unroll
  for (int j = 0; j < 4; ++j)
    gldsw(src + (size_t)j * 32 * HD * 2, (unsigned)(row * HD * 2 + (((tid & 15) ^ f ^ ((j & 1) << 3)) << 4)), lds + (wave * 64 + 512 * j) * 16);
}
__device__ __forceinline__ unsigned proj_base(int T, int lane) {
  const int g = lane >> 4, q4 = (lane & 15) >> 2, p4 = lane & 3, fs = 2 * ((q4 & 1) | ((g & 1) << 1) | ((q4 >> 1) << 2));
  return (unsigned)((4 * g + (q4 & 1) + 32 * (q4 >> 1)) * 256 + (((2 * T) ^ fs) << 4) + p4 * 8);
}
__device__ __forceinline__ u32x8 proj_frag(const char* smem, unsigned a) {
  const u32x2 t0 = trread(smem, a), t1 = trread(smem, a + 512), t2 = trread(smem, a + 4096), t3 = trread(smem, a + 4608);
  return u32x8{t0.x, t0.y, t1.x, t1.y, t2.x, t2.y, t3.x, t3.y};
}
__device__ __forceinline__ void flag_frags(unsigned fa, unsigned a, unsigned want, bf16x8 (&f)[4]) {
  u32x2 t[8];
  for (;;) {
    u32x4 fl;
    asm volatile("ds_read_b128 %0, %9\n\t"
                 "ds_read_b64_tr_b16 %1, %10\n\tds_read_b64_tr_b16 %2, %10 offset:64\n\t"
                 "ds_read_b64_tr_b16 %3, %10 offset:512\n\tds_read_b64_tr_b16 %4, %10 offset:576\n\t"
                 "ds_read_b64_tr_b16 %5, %10 offset:1024\n\tds_read_b64_tr_b16 %6, %10 offset:1088\n\t"
                 "ds_read_b64_tr_b16 %7, %10 offset:1536\n\tds_read_b64_tr_b16 %8, %10 offset:1600\n\ts_waitcnt lgkmcnt(0)"
                 : "=&v"(fl), "=&v"(t[0]), "=&v"(t[1]), "=&v"(t[2]), "=&v"(t[3]), "=&v"(t[4]), "=&v"(t[5]), "=&v"(t[6]), "=&v"(t[7])
                 : "v"(fa), "v"(a) : "memory");
    if (__builtin_amdgcn_readfirstlane(min(min(fl.x, fl.y), min(fl.z, fl.w))) >= want) break;
  }
#pragma unroll
  for (int ks = 0; ks < 4; ++ks) f[ks] = asbf8(u32x4{t[2 * ks].x, t[2 * ks].y, t[2 * ks + 1].x, t[2 * ks + 1].y});
}
__device__ __forceinline__ void wait_flags(const char* p, unsigned want) {
  for (;;) {
    const u32x4 fl = *(volatile LAS u32x4*)p;
    if (__builtin_amdgcn_readfirstlane(min(min(fl.x, fl.y), min(fl.z, fl.w))) >= want) break;
  }
}
__device__ __forceinline__ f32x4 zero4() { float z = 0.0f; asm volatile("" : "+v"(z)); return f32x4{z, z, z, z}; }
__device__ __forceinline__ unsigned lds_word(const char* p) { return __builtin_amdgcn_readfirstlane(*(volatile LAS unsigned*)p); }

#define MFMA16(a, b, c) __builtin_amdgcn_mfma_f32_16x16x16bf16_1k((a), (b), (c), 0, 0, 0)
#define UF(x) __uint_as_float(x)
__device__ __forceinline__ unsigned umax3(unsigned a, unsigned b, unsigned c) { return max(max(a, b), c); }
__device__ __forceinline__ f32x2 logit2(float a, float b) {
  return f32x2{a, b};
}
constexpr float LSC = LOG2E * SCALE;

struct QKW { u32x32 w0, w1, w2, w3, w4, w5; u32x8 x; };
template <int W_> __device__ __forceinline__ void qk_step(const QKW& W, const char* ag, const char* vs, const char* od, const char* kv, unsigned& cap, unsigned& sp,
                                                          unsigned long long& tc, unsigned long long* ts, unsigned& d0, unsigned& d1);
template <> __device__ __forceinline__ void qk_step<0>(const QKW& W, const char* ag, const char* vs, const char* od, const char* kv, unsigned& cap, unsigned& sp,
                                                        unsigned long long& tc, unsigned long long* ts, unsigned& d0, unsigned& d1) {
  unsigned st, bg;
  asm volatile("v_mbcnt_lo_u32_b32 v60, -1, 0\n\t"
    "v_mbcnt_hi_u32_b32 v60, -1, v60\n\t"
    "v_lshlrev_b32 v6, 4, v60\n\t"
    "s_mov_b32 %[sp], 0\n\t"
    "s_setprio 3\n\t"
    "global_load_dwordx4 v[40:43], v6, %[ag] sc1\n\t"
    "global_load_dwordx4 v[44:47], v6, %[ag] offset:1024 sc1\n\t"
    "s_memtime %[tc]\n\t"
    "v_add_u32 v7, 40960, v6\n\t"
    "v_add_u32 v8, 98304, v6\n\t"
    "ds_read_b128 v[24:27], v7 offset:0\n\t"
    "ds_read_b128 v[28:31], v7 offset:1024\n\t"
    "ds_read_b128 v[32:35], v7 offset:8192\n\t"
    "ds_read_b128 v[36:39], v7 offset:9216\n\t"
    "v_lshrrev_b32 v48, 4, v60\n\t"
    "v_and_b32 v49, 15, v60\n\t"
    "v_and_b32 v61, 4, v60\n\t"
    "v_mul_u32_u24 v61, 0x2aaa, v61\n\t"
    "v_lshrrev_b32 v9, 1, v60\n\t"
    "v_and_b32 v9, 2, v9\n\t"
    "v_or3_b32 v9, v9, v61, %[c44]\n\t"
    "v_add_u32 v14, 0, v6\n\t"
    "v_lshlrev_b32 v61, 2, v48\n\t"
    "v_lshl_or_b32 v10, v49, 4, v61\n\t"
    "v_lshrrev_b32 v50, 2, v49\n\t"
    "v_and_b32 v51, 3, v60\n\t"
    "v_and_b32 v61, 1, v60\n\t"
    "v_lshlrev_b32 v61, 3, v61\n\t"
    "v_lshl_or_b32 v11, v50, 4, v61\n\t"
    "v_lshl_or_b32 v11, v48, 7, v11\n\t"
    "v_xor_b32 v12, v48, v49\n\t"
    "v_lshlrev_b32 v12, 4, v12\n\t"
    "v_lshl_or_b32 v12, v49, 8, v12\n\t"
    "v_lshl_or_b32 v50, v48, 2, v50\n\t"
    "v_or_b32 v51, 0, v51\n\t"
    "v_xor_b32 v51, v51, v50\n\t"
    "v_lshlrev_b32 v13, 4, v51\n\t"
    "v_lshl_or_b32 v13, v50, 8, v13\n\t"
    "v_and_b32 v61, 3, v60\n\t"
    "v_lshlrev_b32 v61, 4, v61\n\t"
    "v_lshl_or_b32 v15, v48, 6, v61\n\t"
    "v_and_b32 v61, 8, v60\n\t"
    "v_lshl_or_b32 v15, v61, 14, v15\n\t"
    "v_and_b32 v61, 4, v60\n\t"
    "v_lshl_or_b32 v15, v61, 8, v15\n\t"
    "v_add_u32 v15, 0, v15\n\t"
    "3:\n\t"
    "v_mov_b32 v16, 0\n\t"
    "v_mov_b32 v17, 0\n\t"
    "v_mov_b32 v18, 0\n\t"
    "v_mov_b32 v19, 0\n\t"
    "v_mov_b32 v20, 0\n\t"
    "v_mov_b32 v21, 0\n\t"
    "v_mov_b32 v22, 0\n\t"
    "v_mov_b32 v23, 0\n\t"
    "s_waitcnt vmcnt(0) lgkmcnt(0)\n\t"
    "14:\n\t"
    "ds_write_b128 v6, v[40:43] offset:0\n\t"
    "ds_write_b128 v6, v[44:47] offset:1024\n\t"
    "v_smfmac_f32_16x16x64_bf16 v[16:19], v[40:43], v[64:71], v9\n\t"
    "v_max3_u32 v60, v40, v41, v42\n\t"
    "v_max3_u32 v61, v43, v44, v45\n\t"
    "v_smfmac_f32_16x16x64_bf16 v[20:23], v[40:43], v[72:79], v9\n\t"
    "v_max3_u32 v60, v60, v46, v47\n\t"
    "v_max_u32 v60, v60, v61\n\t"
    "v_cmp_eq_u32 vcc, -1, v60\n\t"
    "s_cbranch_vccnz 1f\n\t"
    "2:\n\t"
    "s_waitcnt lgkmcnt(0)\n\t"
    "s_cmp_eq_u64 %[kv], 0\n\t"
    "s_barrier\n\t"
    "s_cbranch_scc1 20f\n\t"
    "ds_read_b128 v[40:43], v6 offset:12288\n\t"
    "ds_read_b128 v[48:51], v6 offset:2048\n\t"
    "v_smfmac_f32_16x16x64_bf16 v[16:19], v[44:47], v[80:87], v9\n\t"
    "ds_read_b128 v[52:55], v6 offset:3072\n\t"
    "ds_read_b128 v[56:59], v6 offset:4096\n\t"
    "v_smfmac_f32_16x16x64_bf16 v[20:23], v[44:47], v[88:95], v9\n\t"
    "ds_read_b128 v[60:63], v6 offset:13312\n\t"
    "ds_read_b128 v[44:47], v6 offset:5120\n\t"
    "s_waitcnt lgkmcnt(5)\n\t"
    "v_smfmac_f32_16x16x64_bf16 v[16:19], v[40:43], v[24:31], v9\n\t"
    "ds_read_b128 v[24:27], v7 offset:16384\n\t"
    "ds_read_b128 v[28:31], v7 offset:17408\n\t"
    "v_smfmac_f32_16x16x64_bf16 v[20:23], v[40:43], v[32:39], v9\n\t"
    "ds_read_b128 v[32:35], v7 offset:24576\n\t"
    "ds_read_b128 v[36:39], v7 offset:25600\n\t"
    "ds_read_b128 v[40:43], v6 offset:6144\n\t"
    "s_waitcnt lgkmcnt(9)\n\t"
    "v_smfmac_f32_16x16x64_bf16 v[16:19], v[48:51], v[96:103], v9\n\t"
    "v_smfmac_f32_16x16x64_bf16 v[20:23], v[48:51], v[104:111], v9\n\t"
    "ds_read_b128 v[48:51], v6 offset:7168\n\t"
    "s_waitcnt lgkmcnt(9)\n\t"
    "v_smfmac_f32_16x16x64_bf16 v[16:19], v[52:55], v[112:119], v9\n\t"
    "v_smfmac_f32_16x16x64_bf16 v[20:23], v[52:55], v[120:127], v9\n\t"
    "ds_read_b128 v[52:55], v6 offset:14336\n\t"
    "s_waitcnt lgkmcnt(9)\n\t"
    "v_smfmac_f32_16x16x64_bf16 v[16:19], v[56:59], v[128:135], v9\n\t"
    "v_smfmac_f32_16x16x64_bf16 v[20:23], v[56:59], v[136:143], v9\n\t"
    "ds_read_b128 v[56:59], v6 offset:8192\n\t"
    "s_waitcnt lgkmcnt(6)\n\t"
    "v_smfmac_f32_16x16x64_bf16 v[16:19], v[60:63], v[24:31], v9\n\t"
    "ds_read_b128 v[24:27], v8 offset:0\n\t"
    "ds_read_b128 v[28:31], v8 offset:1024\n\t"
    "s_waitcnt lgkmcnt(6)\n\t"
    "v_smfmac_f32_16x16x64_bf16 v[20:23], v[60:63], v[32:39], v9\n\t"
    "ds_read_b128 v[32:35], v8 offset:8192\n\t"
    "ds_read_b128 v[36:39], v8 offset:9216\n\t"
    "ds_read_b128 v[60:63], v6 offset:9216\n\t"
    "v_smfmac_f32_16x16x64_bf16 v[16:19], v[44:47], v[144:151], v9\n\t"
    "v_smfmac_f32_16x16x64_bf16 v[20:23], v[44:47], v[152:159], v9\n\t"
    "ds_read_b128 v[44:47], v6 offset:10240\n\t"
    "s_waitcnt lgkmcnt(9)\n\t"
    "v_smfmac_f32_16x16x64_bf16 v[16:19], v[40:43], v[160:167], v9\n\t"
    "v_smfmac_f32_16x16x64_bf16 v[20:23], v[40:43], v[168:175], v9\n\t"
    "ds_read_b128 v[40:43], v6 offset:15360\n\t"
    "s_waitcnt lgkmcnt(9)\n\t"
    "v_smfmac_f32_16x16x64_bf16 v[16:19], v[48:51], v[176:183], v9\n\t"
    "v_smfmac_f32_16x16x64_bf16 v[20:23], v[48:51], v[184:191], v9\n\t"
    "ds_read_b128 v[48:51], v6 offset:11264\n\t"
    "s_waitcnt lgkmcnt(6)\n\t"
    "v_smfmac_f32_16x16x64_bf16 v[16:19], v[52:55], v[24:31], v9\n\t"
    "ds_read_b128 v[24:27], v8 offset:16384\n\t"
    "ds_read_b128 v[28:31], v8 offset:17408\n\t"
    "s_waitcnt lgkmcnt(6)\n\t"
    "v_smfmac_f32_16x16x64_bf16 v[20:23], v[52:55], v[32:39], v9\n\t"
    "ds_read_b128 v[32:35], v8 offset:24576\n\t"
    "ds_read_b128 v[36:39], v8 offset:25600\n\t"
    "v_smfmac_f32_16x16x64_bf16 v[16:19], v[56:59], v[192:199], v9\n\t"
    "v_smfmac_f32_16x16x64_bf16 v[20:23], v[56:59], v[200:207], v9\n\t"
    "s_waitcnt lgkmcnt(7)\n\t"
    "v_smfmac_f32_16x16x64_bf16 v[16:19], v[60:63], v[208:215], v9\n\t"
    "v_smfmac_f32_16x16x64_bf16 v[20:23], v[60:63], v[216:223], v9\n\t"
    "s_waitcnt lgkmcnt(6)\n\t"
    "v_smfmac_f32_16x16x64_bf16 v[16:19], v[44:47], v[224:231], v9\n\t"
    "v_smfmac_f32_16x16x64_bf16 v[20:23], v[44:47], v[232:239], v9\n\t"
    "s_waitcnt lgkmcnt(2)\n\t"
    "v_smfmac_f32_16x16x64_bf16 v[16:19], v[40:43], v[24:31], v9\n\t"
    "s_waitcnt lgkmcnt(0)\n\t"
    "v_smfmac_f32_16x16x64_bf16 v[20:23], v[40:43], v[32:39], v9\n\t"
    "v_smfmac_f32_16x16x64_bf16 v[16:19], v[48:51], v[240:247], v9\n\t"
    "v_smfmac_f32_16x16x64_bf16 v[20:23], v[48:51], v[248:255], v9\n\t"
    "v_xor_b32 v7, 64, v12\n\t"
    "v_xor_b32 v8, 0x80, v12\n\t"
    "v_xor_b32 v6, 0xc0, v12\n\t"
    "ds_read_b128 v[24:27], v12 offset:16384\n\t"
    "ds_read_b128 v[28:31], v12 offset:20480\n\t"
    "ds_read_b128 v[32:35], v7 offset:16384\n\t"
    "ds_read_b128 v[36:39], v7 offset:20480\n\t"
    "v_permlane16_swap_b32 v16, v18\n\t"
    "v_permlane16_swap_b32 v17, v19\n\t"
    "v_permlane16_swap_b32 v20, v22\n\t"
    "v_permlane16_swap_b32 v21, v23\n\t"
    "v_add_f32 v16, v16, v18\n\t"
    "v_add_f32 v17, v17, v19\n\t"
    "v_add_f32 v20, v20, v22\n\t"
    "v_mul_f32 v16, %[lsc], v16\n\t"
    "v_mul_f32 v17, %[lsc], v17\n\t"
    "v_cvt_pk_bf16_f32 v16, v16, v17\n\t"
    "v_add_f32 v21, v21, v23\n\t"
    "ds_write_b32 v10, v16 offset:32768\n\t"
    "v_mul_f32 v20, %[lsc], v20\n\t"
    "v_mul_f32 v21, %[lsc], v21\n\t"
    "v_cvt_pk_bf16_f32 v20, v20, v21\n\t"
    "ds_write_b32 v10, v20 offset:33024\n\t"
    "ds_read_b128 v[40:43], v8 offset:16384\n\t"
    "ds_read_b128 v[44:47], v8 offset:20480\n\t"
    "ds_read_b128 v[48:51], v6 offset:16384\n\t"
    "ds_read_b128 v[52:55], v6 offset:20480\n\t"
    "s_waitcnt lgkmcnt(4)\n\t"
    "s_barrier\n\t"
    "ds_read_b64_tr_b16 v[16:17], v11 offset:32768\n\t"
    "ds_read_b64_tr_b16 v[18:19], v11 offset:32832\n\t"
    "ds_read_b64_tr_b16 v[20:21], v11 offset:33280\n\t"
    "ds_read_b64_tr_b16 v[22:23], v11 offset:33344\n\t"
    "ds_read_b64_tr_b16 v[56:57], v11 offset:33792\n\t"
    "ds_read_b64_tr_b16 v[58:59], v11 offset:33856\n\t"
    "ds_read_b64_tr_b16 v[60:61], v11 offset:34304\n\t"
    "ds_read_b64_tr_b16 v[62:63], v11 offset:34368\n\t"
    "v_mov_b32 v6, 0\n\t"
    "v_mov_b32 v7, 0\n\t"
    "v_mov_b32 v8, 0\n\t"
    "v_mov_b32 v9, 0\n\t"
    "s_mov_b32 exec_hi, 0\n\t"
    "global_load_dwordx4 v[6:9], v14, %[vs] sc1\n\t"
    "s_mov_b32 exec_hi, -1\n\t"
    "s_waitcnt lgkmcnt(6)\n\t"
    "v_mfma_f32_16x16x32_bf16 v[24:27], v[24:27], v[16:19], 0\n\t"
    "v_mfma_f32_16x16x32_bf16 v[28:31], v[28:31], v[16:19], 0\n\t"
    "s_waitcnt lgkmcnt(4)\n\t"
    "v_mfma_f32_16x16x32_bf16 v[24:27], v[32:35], v[20:23], v[24:27]\n\t"
    "v_mfma_f32_16x16x32_bf16 v[28:31], v[36:39], v[20:23], v[28:31]\n\t"
    "ds_read_b64_tr_b16 v[32:33], v13 offset:24576\n\t"
    "ds_read_b64_tr_b16 v[34:35], v13 offset:28672\n\t"
    "ds_read_b64_tr_b16 v[36:37], v13 offset:24584\n\t"
    "ds_read_b64_tr_b16 v[38:39], v13 offset:28680\n\t"
    "s_waitcnt lgkmcnt(6)\n\t"
    "v_mfma_f32_16x16x32_bf16 v[24:27], v[40:43], v[56:59], v[24:27]\n\t"
    "v_mfma_f32_16x16x32_bf16 v[28:31], v[44:47], v[56:59], v[28:31]\n\t"
    "s_waitcnt lgkmcnt(4)\n\t"
    "v_mfma_f32_16x16x32_bf16 v[24:27], v[48:51], v[60:63], v[24:27]\n\t"
    "v_mfma_f32_16x16x32_bf16 v[28:31], v[52:55], v[60:63], v[28:31]\n\t"
    "s_setprio 1\n\t"
    "v_mov_b32 v52, %[one]\n\t"
    "v_mov_b32 v53, %[one]\n\t"
    "v_mov_b32 v54, %[one]\n\t"
    "v_mov_b32 v55, %[one]\n\t"
    "s_nop 2\n\t"
    "v_max3_f32 v10, |v24|, |v25|, |v26|\n\t"
    "v_max3_f32 v40, |v27|, |v28|, |v29|\n\t"
    "v_max3_f32 v10, v10, |v30|, |v31|\n\t"
    "v_max_f32 v10, v10, v40\n\t"
    "v_cmp_nge_f32 vcc, %[big], v10\n\t"
    "s_mov_b32 %[bg], %[big]\n\t"
    "v_mov_b32 v10, 0\n\t"
    "s_cbranch_vccnz 12f\n\t"
    "13:\n\t"
    "v_exp_f32 v24, v24\n\t"
    "v_exp_f32 v25, v25\n\t"
    "v_exp_f32 v26, v26\n\t"
    "v_exp_f32 v27, v27\n\t"
    "v_exp_f32 v28, v28\n\t"
    "v_exp_f32 v29, v29\n\t"
    "v_exp_f32 v30, v30\n\t"
    "v_exp_f32 v31, v31\n\t"
    "v_cvt_pk_bf16_f32 v40, v24, v25\n\t"
    "v_cvt_pk_bf16_f32 v41, v26, v27\n\t"
    "v_cvt_pk_bf16_f32 v42, v28, v29\n\t"
    "v_cvt_pk_bf16_f32 v43, v30, v31\n\t"
    "s_setprio 3\n\t"
    "s_waitcnt lgkmcnt(0)\n\t"
    "s_nop 0\n\t"
    "v_mfma_f32_16x16x32_bf16 v[44:47], v[32:35], v[40:43], 0\n\t"
    "v_mfma_f32_16x16x32_bf16 v[48:51], v[36:39], v[40:43], 0\n\t"
    "v_mfma_f32_16x16x32_bf16 v[52:55], v[52:55], v[40:43], 0\n\t"
    "v_mov_b32 v12, 0\n\t"
    "v_mov_b32 v13, 0\n\t"
    "s_mov_b32 exec_hi, 0\n\t"
    "v_mov_b32 v12, %[one]\n\t"
    "v_mov_b32 v13, %[one]\n\t"
    "s_mov_b32 exec_hi, -1\n\t"
    "s_barrier\n\t"
    "ds_read_b64_tr_b16 v[24:25], v11 offset:34816\n\t"
    "ds_read_b64_tr_b16 v[26:27], v11 offset:34880\n\t"
    "ds_read_b64_tr_b16 v[28:29], v11 offset:35328\n\t"
    "ds_read_b64_tr_b16 v[30:31], v11 offset:35392\n\t"
    "ds_read_b64_tr_b16 v[32:33], v11 offset:35840\n\t"
    "ds_read_b64_tr_b16 v[34:35], v11 offset:35904\n\t"
    "ds_read_b64_tr_b16 v[36:37], v11 offset:36352\n\t"
    "ds_read_b64_tr_b16 v[38:39], v11 offset:36416\n\t"
    "s_waitcnt lgkmcnt(6)\n\t"
    "v_mfma_f32_16x16x32_bf16 v[40:43], v[24:27], v[16:19], 0\n\t"
    "s_waitcnt lgkmcnt(4)\n\t"
    "v_mfma_f32_16x16x32_bf16 v[40:43], v[28:31], v[20:23], v[40:43]\n\t"
    "s_waitcnt lgkmcnt(2)\n\t"
    "v_mfma_f32_16x16x32_bf16 v[40:43], v[32:35], v[56:59], v[40:43]\n\t"
    "s_waitcnt lgkmcnt(0)\n\t"
    "v_mfma_f32_16x16x32_bf16 v[40:43], v[36:39], v[60:63], v[40:43]\n\t"
    "5:\n\t"
    "s_waitcnt vmcnt(0)\n\t"
    "v_max3_u32 v11, v6, v7, v8\n\t"
    "v_max_u32 v11, v11, v9\n\t"
    "v_cmp_eq_u32 vcc, -1, v11\n\t"
    "s_cbranch_vccnz 4f\n\t"
    "6:\n\t"
    "s_nop 2\n\t"
    "v_exp_f32 v28, v40\n\t"
    "v_max3_f32 v11, v40, v41, v42\n\t"
    "v_exp_f32 v29, v41\n\t"
    "v_max_f32 v11, v11, v43\n\t"
    "v_exp_f32 v30, v42\n\t"
    "v_cmp_nge_f32 vcc, %[bg], v11\n\t"
    "v_exp_f32 v31, v43\n\t"
    "s_cbranch_vccnz 10f\n\t"
    "11:\n\t"
    "v_cvt_pk_bf16_f32 v24, v28, v29\n\t"
    "v_cvt_pk_bf16_f32 v25, v30, v31\n\t"
    "s_nop 1\n\t"
    "v_mfma_f32_16x16x16_bf16 v[52:55], v[12:13], v[24:25], v[52:55]\n\t"
    "v_mfma_f32_16x16x16_bf16 v[44:47], v[6:7], v[24:25], v[44:47]\n\t"
    "v_mfma_f32_16x16x16_bf16 v[48:51], v[8:9], v[24:25], v[48:51]\n\t"
    "s_nop 5\n\t"
    "v_rcp_f32 v26, v52\n\t"
    "s_nop 0\n\t"
    "v_pk_mul_f32 v[44:45], v[44:45], v[26:27] op_sel_hi:[1,0]\n\t"
    "v_pk_mul_f32 v[46:47], v[46:47], v[26:27] op_sel_hi:[1,0]\n\t"
    "v_pk_mul_f32 v[48:49], v[48:49], v[26:27] op_sel_hi:[1,0]\n\t"
    "v_pk_mul_f32 v[50:51], v[50:51], v[26:27] op_sel_hi:[1,0]\n\t"
    "v_cvt_pk_bf16_f32 v36, v44, v45\n\t"
    "v_cvt_pk_bf16_f32 v37, v46, v47\n\t"
    "v_cvt_pk_bf16_f32 v38, v48, v49\n\t"
    "v_cvt_pk_bf16_f32 v39, v50, v51\n\t"
    "v_min_u32 v36, -2, v36\n\t"
    "v_min_u32 v37, -2, v37\n\t"
    "v_min_u32 v38, -2, v38\n\t"
    "v_min_u32 v39, -2, v39\n\t"
    "global_store_dwordx4 v15, v[36:39], %[od] sc1\n\t"
    "s_barrier\n\t"
    "s_mov_b32 exec_hi, 0\n\t"
    "global_store_dwordx4 v14, v[6:9], %[kv] sc1\n\t"
    "s_mov_b32 exec_hi, -1\n\t"
    "s_nop 0\n\t"
    "s_waitcnt lgkmcnt(0)\n\t"
    "s_branch 9f\n\t"
    "1:\n\t"
    "s_add_u32 %[sp], %[sp], 1\n\t"
    "s_cmp_ge_u32 %[sp], %[cap]\n\t"
    "s_cbranch_scc1 7f\n\t"
    "global_load_dwordx4 v[40:43], v6, %[ag] sc1\n\t"
    "global_load_dwordx4 v[44:47], v6, %[ag] offset:1024 sc1\n\t"
    "s_waitcnt lgkmcnt(0)\n\t"
    "s_memtime %[tc]\n\t"
    "s_nop 7\n\t"
    "s_branch 3b\n\t"
    "7:\n\t"
    "s_mov_b32 %[cap], 0\n\t"
    "s_branch 2b\n\t"
    "20:\n\t"
    "s_barrier\n\t"
    "s_barrier\n\t"
    "s_barrier\n\t"
    "s_branch 9f\n\t"
    "4:\n\t"
    "s_add_u32 %[sp], %[sp], 0x10000\n\t"
    "s_lshr_b32 %[st], %[sp], 16\n\t"
    "s_cmp_ge_u32 %[st], %[cap]\n\t"
    "s_cbranch_scc1 8f\n\t"
    "s_mov_b32 exec_hi, 0\n\t"
    "global_load_dwordx4 v[6:9], v14, %[vs] sc1\n\t"
    "s_mov_b32 exec_hi, -1\n\t"
    "s_branch 5b\n\t"
    "8:\n\t"
    "s_mov_b32 %[cap], 0\n\t"
    "s_branch 6b\n\t"
    "10:\n\t"
    "v_max_f32 v11, v11, v10\n\t"
    "v_mov_b32 v26, v11\n\t"
    "s_nop 1\n\t"
    "v_permlane16_swap_b32 v11, v26\n\t"
    "v_max_f32 v11, v11, v26\n\t"
    "v_sub_f32 v28, v40, v11\n\t"
    "v_sub_f32 v29, v41, v11\n\t"
    "v_sub_f32 v30, v42, v11\n\t"
    "v_sub_f32 v31, v43, v11\n\t"
    "v_sub_f32 v11, v10, v11\n\t"
    "v_exp_f32 v11, v11\n\t"
    "v_exp_f32 v28, v28\n\t"
    "v_exp_f32 v29, v29\n\t"
    "v_exp_f32 v30, v30\n\t"
    "v_exp_f32 v31, v31\n\t"
    "v_mul_f32 v44, v44, v11\n\t"
    "v_mul_f32 v45, v45, v11\n\t"
    "v_mul_f32 v46, v46, v11\n\t"
    "v_mul_f32 v47, v47, v11\n\t"
    "v_mul_f32 v48, v48, v11\n\t"
    "v_mul_f32 v49, v49, v11\n\t"
    "v_mul_f32 v50, v50, v11\n\t"
    "v_mul_f32 v51, v51, v11\n\t"
    "v_mul_f32 v52, v52, v11\n\t"
    "v_mul_f32 v53, v53, v11\n\t"
    "v_mul_f32 v54, v54, v11\n\t"
    "v_mul_f32 v55, v55, v11\n\t"
    "s_branch 11b\n\t"
    "12:\n\t"
    "v_max3_f32 v10, v24, v25, v26\n\t"
    "v_max3_f32 v40, v27, v28, v29\n\t"
    "v_max3_f32 v10, v10, v30, v31\n\t"
    "v_max3_f32 v10, v10, v40, %[neg]\n\t"
    "v_mov_b32 v40, v10\n\t"
    "s_nop 1\n\t"
    "v_permlane16_swap_b32 v10, v40\n\t"
    "v_max_f32 v10, v10, v40\n\t"
    "v_mov_b32 v40, v10\n\t"
    "s_nop 1\n\t"
    "v_permlane32_swap_b32 v10, v40\n\t"
    "v_max_f32 v10, v10, v40\n\t"
    "v_sub_f32 v24, v24, v10\n\t"
    "v_sub_f32 v25, v25, v10\n\t"
    "v_sub_f32 v26, v26, v10\n\t"
    "v_sub_f32 v27, v27, v10\n\t"
    "v_sub_f32 v28, v28, v10\n\t"
    "v_sub_f32 v29, v29, v10\n\t"
    "v_sub_f32 v30, v30, v10\n\t"
    "v_sub_f32 v31, v31, v10\n\t"
    "s_mov_b32 %[bg], 0x7fc00000\n\t"
    "s_branch 13b\n\t"
    "9:"
    : [tc] "=&s"(tc), [sp] "=&s"(sp), [cap] "+s"(cap), [st] "=&s"(st), [bg] "=&s"(bg)
    : [ag] "s"(ag), [c44] "s"(0x4444u), [kv] "s"(kv), [vs] "s"(vs), [od] "s"(od), [od2] "s"(od + 262144), [neg] "s"(-1.0e38f), [lsc] "s"(LSC), [one] "s"(0x3F803F80u), [big] "s"(24.0f), "{v[64:95]}"(W.w0), "{v[96:127]}"(W.w1), "{v[128:159]}"(W.w2), "{v[160:191]}"(W.w3), "{v[192:223]}"(W.w4), "{v[224:255]}"(W.w5)
    : "v6", "v7", "v8", "v9", "v10", "v11", "v12", "v13", "v14", "v15", "v16", "v17", "v18", "v19", "v20", "v21", "v22", "v23", "v24", "v25", "v26", "v27", "v28", "v29", "v30", "v31", "v32", "v33", "v34", "v35", "v36", "v37", "v38", "v39", "v40", "v41", "v42", "v43", "v44", "v45", "v46", "v47", "v48", "v49", "v50", "v51", "v52", "v53", "v54", "v55", "v56", "v57", "v58", "v59", "v60", "v61", "v62", "v63", "vcc", "scc", "memory");
}
template <> __device__ __forceinline__ void qk_step<1>(const QKW& W, const char* ag, const char* vs, const char* od, const char* kv, unsigned& cap, unsigned& sp,
                                                        unsigned long long& tc, unsigned long long* ts, unsigned& d0, unsigned& d1) {
  unsigned st, bg;
  asm volatile("v_mbcnt_lo_u32_b32 v60, -1, 0\n\t"
    "v_mbcnt_hi_u32_b32 v60, -1, v60\n\t"
    "v_lshlrev_b32 v6, 4, v60\n\t"
    "s_mov_b32 %[sp], 0\n\t"
    "s_setprio 3\n\t"
    "global_load_dwordx4 v[40:43], v6, %[ag] sc1\n\t"
    "global_load_dwordx4 v[44:47], v6, %[ag] offset:1024 sc1\n\t"
    "s_memtime %[tc]\n\t"
    "v_add_u32 v7, 43008, v6\n\t"
    "v_add_u32 v8, 100352, v6\n\t"
    "ds_read_b128 v[24:27], v7 offset:0\n\t"
    "ds_read_b128 v[28:31], v7 offset:1024\n\t"
    "ds_read_b128 v[32:35], v7 offset:8192\n\t"
    "ds_read_b128 v[36:39], v7 offset:9216\n\t"
    "v_lshrrev_b32 v48, 4, v60\n\t"
    "v_and_b32 v49, 15, v60\n\t"
    "v_and_b32 v61, 4, v60\n\t"
    "v_mul_u32_u24 v61, 0x2aaa, v61\n\t"
    "v_lshrrev_b32 v9, 1, v60\n\t"
    "v_and_b32 v9, 2, v9\n\t"
    "v_or3_b32 v9, v9, v61, %[c44]\n\t"
    "v_add_u32 v14, 512, v6\n\t"
    "v_lshlrev_b32 v61, 2, v48\n\t"
    "v_lshl_or_b32 v10, v49, 4, v61\n\t"
    "v_lshrrev_b32 v50, 2, v49\n\t"
    "v_and_b32 v51, 3, v60\n\t"
    "v_and_b32 v61, 1, v60\n\t"
    "v_lshlrev_b32 v61, 3, v61\n\t"
    "v_lshl_or_b32 v11, v50, 4, v61\n\t"
    "v_lshl_or_b32 v11, v48, 7, v11\n\t"
    "v_xor_b32 v12, v48, v49\n\t"
    "v_lshlrev_b32 v12, 4, v12\n\t"
    "v_lshl_or_b32 v12, v49, 8, v12\n\t"
    "v_lshl_or_b32 v50, v48, 2, v50\n\t"
    "v_or_b32 v51, 4, v51\n\t"
    "v_xor_b32 v51, v51, v50\n\t"
    "v_lshlrev_b32 v13, 4, v51\n\t"
    "v_lshl_or_b32 v13, v50, 8, v13\n\t"
    "v_and_b32 v61, 3, v60\n\t"
    "v_lshlrev_b32 v61, 4, v61\n\t"
    "v_lshl_or_b32 v15, v48, 6, v61\n\t"
    "v_and_b32 v61, 8, v60\n\t"
    "v_lshl_or_b32 v15, v61, 14, v15\n\t"
    "v_and_b32 v61, 4, v60\n\t"
    "v_lshl_or_b32 v15, v61, 8, v15\n\t"
    "v_add_u32 v15, 256, v15\n\t"
    "3:\n\t"
    "v_mov_b32 v16, 0\n\t"
    "v_mov_b32 v17, 0\n\t"
    "v_mov_b32 v18, 0\n\t"
    "v_mov_b32 v19, 0\n\t"
    "v_mov_b32 v20, 0\n\t"
    "v_mov_b32 v21, 0\n\t"
    "v_mov_b32 v22, 0\n\t"
    "v_mov_b32 v23, 0\n\t"
    "s_waitcnt vmcnt(0) lgkmcnt(0)\n\t"
    "14:\n\t"
    "ds_write_b128 v6, v[40:43] offset:2048\n\t"
    "ds_write_b128 v6, v[44:47] offset:3072\n\t"
    "v_smfmac_f32_16x16x64_bf16 v[16:19], v[40:43], v[96:103], v9\n\t"
    "v_max3_u32 v60, v40, v41, v42\n\t"
    "v_max3_u32 v61, v43, v44, v45\n\t"
    "v_smfmac_f32_16x16x64_bf16 v[20:23], v[40:43], v[104:111], v9\n\t"
    "v_max3_u32 v60, v60, v46, v47\n\t"
    "v_max_u32 v60, v60, v61\n\t"
    "v_cmp_eq_u32 vcc, -1, v60\n\t"
    "s_cbranch_vccnz 1f\n\t"
    "2:\n\t"
    "s_waitcnt lgkmcnt(0)\n\t"
    "s_cmp_eq_u64 %[kv], 0\n\t"
    "s_barrier\n\t"
    "s_cbranch_scc1 20f\n\t"
    "ds_read_b128 v[40:43], v6 offset:12288\n\t"
    "ds_read_b128 v[48:51], v6 offset:0\n\t"
    "v_smfmac_f32_16x16x64_bf16 v[16:19], v[44:47], v[112:119], v9\n\t"
    "ds_read_b128 v[52:55], v6 offset:1024\n\t"
    "ds_read_b128 v[56:59], v6 offset:4096\n\t"
    "v_smfmac_f32_16x16x64_bf16 v[20:23], v[44:47], v[120:127], v9\n\t"
    "ds_read_b128 v[60:63], v6 offset:13312\n\t"
    "ds_read_b128 v[44:47], v6 offset:5120\n\t"
    "s_waitcnt lgkmcnt(5)\n\t"
    "v_smfmac_f32_16x16x64_bf16 v[16:19], v[40:43], v[24:31], v9\n\t"
    "ds_read_b128 v[24:27], v7 offset:16384\n\t"
    "ds_read_b128 v[28:31], v7 offset:17408\n\t"
    "v_smfmac_f32_16x16x64_bf16 v[20:23], v[40:43], v[32:39], v9\n\t"
    "ds_read_b128 v[32:35], v7 offset:24576\n\t"
    "ds_read_b128 v[36:39], v7 offset:25600\n\t"
    "ds_read_b128 v[40:43], v6 offset:6144\n\t"
    "s_waitcnt lgkmcnt(9)\n\t"
    "v_smfmac_f32_16x16x64_bf16 v[16:19], v[48:51], v[64:71], v9\n\t"
    "v_smfmac_f32_16x16x64_bf16 v[20:23], v[48:51], v[72:79], v9\n\t"
    "ds_read_b128 v[48:51], v6 offset:7168\n\t"
    "s_waitcnt lgkmcnt(9)\n\t"
    "v_smfmac_f32_16x16x64_bf16 v[16:19], v[52:55], v[80:87], v9\n\t"
    "v_smfmac_f32_16x16x64_bf16 v[20:23], v[52:55], v[88:95], v9\n\t"
    "ds_read_b128 v[52:55], v6 offset:14336\n\t"
    "s_waitcnt lgkmcnt(9)\n\t"
    "v_smfmac_f32_16x16x64_bf16 v[16:19], v[56:59], v[128:135], v9\n\t"
    "v_smfmac_f32_16x16x64_bf16 v[20:23], v[56:59], v[136:143], v9\n\t"
    "ds_read_b128 v[56:59], v6 offset:8192\n\t"
    "s_waitcnt lgkmcnt(6)\n\t"
    "v_smfmac_f32_16x16x64_bf16 v[16:19], v[60:63], v[24:31], v9\n\t"
    "ds_read_b128 v[24:27], v8 offset:0\n\t"
    "ds_read_b128 v[28:31], v8 offset:1024\n\t"
    "s_waitcnt lgkmcnt(6)\n\t"
    "v_smfmac_f32_16x16x64_bf16 v[20:23], v[60:63], v[32:39], v9\n\t"
    "ds_read_b128 v[32:35], v8 offset:8192\n\t"
    "ds_read_b128 v[36:39], v8 offset:9216\n\t"
    "ds_read_b128 v[60:63], v6 offset:9216\n\t"
    "v_smfmac_f32_16x16x64_bf16 v[16:19], v[44:47], v[144:151], v9\n\t"
    "v_smfmac_f32_16x16x64_bf16 v[20:23], v[44:47], v[152:159], v9\n\t"
    "ds_read_b128 v[44:47], v6 offset:10240\n\t"
    "s_waitcnt lgkmcnt(9)\n\t"
    "v_smfmac_f32_16x16x64_bf16 v[16:19], v[40:43], v[160:167], v9\n\t"
    "v_smfmac_f32_16x16x64_bf16 v[20:23], v[40:43], v[168:175], v9\n\t"
    "ds_read_b128 v[40:43], v6 offset:15360\n\t"
    "s_waitcnt lgkmcnt(9)\n\t"
    "v_smfmac_f32_16x16x64_bf16 v[16:19], v[48:51], v[176:183], v9\n\t"
    "v_smfmac_f32_16x16x64_bf16 v[20:23], v[48:51], v[184:191], v9\n\t"
    "ds_read_b128 v[48:51], v6 offset:11264\n\t"
    "s_waitcnt lgkmcnt(6)\n\t"
    "v_smfmac_f32_16x16x64_bf16 v[16:19], v[52:55], v[24:31], v9\n\t"
    "ds_read_b128 v[24:27], v8 offset:16384\n\t"
    "ds_read_b128 v[28:31], v8 offset:17408\n\t"
    "s_waitcnt lgkmcnt(6)\n\t"
    "v_smfmac_f32_16x16x64_bf16 v[20:23], v[52:55], v[32:39], v9\n\t"
    "ds_read_b128 v[32:35], v8 offset:24576\n\t"
    "ds_read_b128 v[36:39], v8 offset:25600\n\t"
    "v_smfmac_f32_16x16x64_bf16 v[16:19], v[56:59], v[192:199], v9\n\t"
    "v_smfmac_f32_16x16x64_bf16 v[20:23], v[56:59], v[200:207], v9\n\t"
    "s_waitcnt lgkmcnt(7)\n\t"
    "v_smfmac_f32_16x16x64_bf16 v[16:19], v[60:63], v[208:215], v9\n\t"
    "v_smfmac_f32_16x16x64_bf16 v[20:23], v[60:63], v[216:223], v9\n\t"
    "s_waitcnt lgkmcnt(6)\n\t"
    "v_smfmac_f32_16x16x64_bf16 v[16:19], v[44:47], v[224:231], v9\n\t"
    "v_smfmac_f32_16x16x64_bf16 v[20:23], v[44:47], v[232:239], v9\n\t"
    "s_waitcnt lgkmcnt(2)\n\t"
    "v_smfmac_f32_16x16x64_bf16 v[16:19], v[40:43], v[24:31], v9\n\t"
    "s_waitcnt lgkmcnt(0)\n\t"
    "v_smfmac_f32_16x16x64_bf16 v[20:23], v[40:43], v[32:39], v9\n\t"
    "v_smfmac_f32_16x16x64_bf16 v[16:19], v[48:51], v[240:247], v9\n\t"
    "v_smfmac_f32_16x16x64_bf16 v[20:23], v[48:51], v[248:255], v9\n\t"
    "v_xor_b32 v7, 64, v12\n\t"
    "v_xor_b32 v8, 0x80, v12\n\t"
    "v_xor_b32 v6, 0xc0, v12\n\t"
    "ds_read_b128 v[24:27], v12 offset:16384\n\t"
    "ds_read_b128 v[28:31], v12 offset:20480\n\t"
    "ds_read_b128 v[32:35], v7 offset:16384\n\t"
    "ds_read_b128 v[36:39], v7 offset:20480\n\t"
    "v_permlane16_swap_b32 v16, v18\n\t"
    "v_permlane16_swap_b32 v17, v19\n\t"
    "v_permlane16_swap_b32 v20, v22\n\t"
    "v_permlane16_swap_b32 v21, v23\n\t"
    "v_add_f32 v16, v16, v18\n\t"
    "v_add_f32 v17, v17, v19\n\t"
    "v_add_f32 v20, v20, v22\n\t"
    "v_mul_f32 v16, %[lsc], v16\n\t"
    "v_mul_f32 v17, %[lsc], v17\n\t"
    "v_cvt_pk_bf16_f32 v16, v16, v17\n\t"
    "v_add_f32 v21, v21, v23\n\t"
    "ds_write_b32 v10, v16 offset:33280\n\t"
    "v_mul_f32 v20, %[lsc], v20\n\t"
    "v_mul_f32 v21, %[lsc], v21\n\t"
    "v_cvt_pk_bf16_f32 v20, v20, v21\n\t"
    "ds_write_b32 v10, v20 offset:33536\n\t"
    "ds_read_b128 v[40:43], v8 offset:16384\n\t"
    "ds_read_b128 v[44:47], v8 offset:20480\n\t"
    "ds_read_b128 v[48:51], v6 offset:16384\n\t"
    "ds_read_b128 v[52:55], v6 offset:20480\n\t"
    "s_waitcnt lgkmcnt(4)\n\t"
    "s_barrier\n\t"
    "ds_read_b64_tr_b16 v[16:17], v11 offset:32768\n\t"
    "ds_read_b64_tr_b16 v[18:19], v11 offset:32832\n\t"
    "ds_read_b64_tr_b16 v[20:21], v11 offset:33280\n\t"
    "ds_read_b64_tr_b16 v[22:23], v11 offset:33344\n\t"
    "ds_read_b64_tr_b16 v[56:57], v11 offset:33792\n\t"
    "ds_read_b64_tr_b16 v[58:59], v11 offset:33856\n\t"
    "ds_read_b64_tr_b16 v[60:61], v11 offset:34304\n\t"
    "ds_read_b64_tr_b16 v[62:63], v11 offset:34368\n\t"
    "v_mov_b32 v6, 0\n\t"
    "v_mov_b32 v7, 0\n\t"
    "v_mov_b32 v8, 0\n\t"
    "v_mov_b32 v9, 0\n\t"
    "s_mov_b32 exec_hi, 0\n\t"
    "global_load_dwordx4 v[6:9], v14, %[vs] sc1\n\t"
    "s_mov_b32 exec_hi, -1\n\t"
    "s_waitcnt lgkmcnt(6)\n\t"
    "v_mfma_f32_16x16x32_bf16 v[24:27], v[24:27], v[16:19], 0\n\t"
    "v_mfma_f32_16x16x32_bf16 v[28:31], v[28:31], v[16:19], 0\n\t"
    "s_waitcnt lgkmcnt(4)\n\t"
    "v_mfma_f32_16x16x32_bf16 v[24:27], v[32:35], v[20:23], v[24:27]\n\t"
    "v_mfma_f32_16x16x32_bf16 v[28:31], v[36:39], v[20:23], v[28:31]\n\t"
    "ds_read_b64_tr_b16 v[32:33], v13 offset:24576\n\t"
    "ds_read_b64_tr_b16 v[34:35], v13 offset:28672\n\t"
    "ds_read_b64_tr_b16 v[36:37], v13 offset:24584\n\t"
    "ds_read_b64_tr_b16 v[38:39], v13 offset:28680\n\t"
    "s_waitcnt lgkmcnt(6)\n\t"
    "v_mfma_f32_16x16x32_bf16 v[24:27], v[40:43], v[56:59], v[24:27]\n\t"
    "v_mfma_f32_16x16x32_bf16 v[28:31], v[44:47], v[56:59], v[28:31]\n\t"
    "s_waitcnt lgkmcnt(4)\n\t"
    "v_mfma_f32_16x16x32_bf16 v[24:27], v[48:51], v[60:63], v[24:27]\n\t"
    "v_mfma_f32_16x16x32_bf16 v[28:31], v[52:55], v[60:63], v[28:31]\n\t"
    "s_setprio 1\n\t"
    "v_mov_b32 v52, %[one]\n\t"
    "v_mov_b32 v53, %[one]\n\t"
    "v_mov_b32 v54, %[one]\n\t"
    "v_mov_b32 v55, %[one]\n\t"
    "s_nop 2\n\t"
    "v_max3_f32 v10, |v24|, |v25|, |v26|\n\t"
    "v_max3_f32 v40, |v27|, |v28|, |v29|\n\t"
    "v_max3_f32 v10, v10, |v30|, |v31|\n\t"
    "v_max_f32 v10, v10, v40\n\t"
    "v_cmp_nge_f32 vcc, %[big], v10\n\t"
    "s_mov_b32 %[bg], %[big]\n\t"
    "v_mov_b32 v10, 0\n\t"
    "s_cbranch_vccnz 12f\n\t"
    "13:\n\t"
    "v_exp_f32 v24, v24\n\t"
    "v_exp_f32 v25, v25\n\t"
    "v_exp_f32 v26, v26\n\t"
    "v_exp_f32 v27, v27\n\t"
    "v_exp_f32 v28, v28\n\t"
    "v_exp_f32 v29, v29\n\t"
    "v_exp_f32 v30, v30\n\t"
    "v_exp_f32 v31, v31\n\t"
    "v_cvt_pk_bf16_f32 v40, v24, v25\n\t"
    "v_cvt_pk_bf16_f32 v41, v26, v27\n\t"
    "v_cvt_pk_bf16_f32 v42, v28, v29\n\t"
    "v_cvt_pk_bf16_f32 v43, v30, v31\n\t"
    "s_setprio 3\n\t"
    "s_waitcnt lgkmcnt(0)\n\t"
    "s_nop 0\n\t"
    "v_mfma_f32_16x16x32_bf16 v[44:47], v[32:35], v[40:43], 0\n\t"
    "v_mfma_f32_16x16x32_bf16 v[48:51], v[36:39], v[40:43], 0\n\t"
    "v_mfma_f32_16x16x32_bf16 v[52:55], v[52:55], v[40:43], 0\n\t"
    "v_mov_b32 v12, 0\n\t"
    "v_mov_b32 v13, 0\n\t"
    "s_mov_b32 exec_hi, 0\n\t"
    "v_mov_b32 v12, %[one]\n\t"
    "v_mov_b32 v13, %[one]\n\t"
    "s_mov_b32 exec_hi, -1\n\t"
    "s_barrier\n\t"
    "ds_read_b64_tr_b16 v[24:25], v11 offset:34816\n\t"
    "ds_read_b64_tr_b16 v[26:27], v11 offset:34880\n\t"
    "ds_read_b64_tr_b16 v[28:29], v11 offset:35328\n\t"
    "ds_read_b64_tr_b16 v[30:31], v11 offset:35392\n\t"
    "ds_read_b64_tr_b16 v[32:33], v11 offset:35840\n\t"
    "ds_read_b64_tr_b16 v[34:35], v11 offset:35904\n\t"
    "ds_read_b64_tr_b16 v[36:37], v11 offset:36352\n\t"
    "ds_read_b64_tr_b16 v[38:39], v11 offset:36416\n\t"
    "s_waitcnt lgkmcnt(6)\n\t"
    "v_mfma_f32_16x16x32_bf16 v[40:43], v[24:27], v[16:19], 0\n\t"
    "s_waitcnt lgkmcnt(4)\n\t"
    "v_mfma_f32_16x16x32_bf16 v[40:43], v[28:31], v[20:23], v[40:43]\n\t"
    "s_waitcnt lgkmcnt(2)\n\t"
    "v_mfma_f32_16x16x32_bf16 v[40:43], v[32:35], v[56:59], v[40:43]\n\t"
    "s_waitcnt lgkmcnt(0)\n\t"
    "v_mfma_f32_16x16x32_bf16 v[40:43], v[36:39], v[60:63], v[40:43]\n\t"
    "5:\n\t"
    "s_waitcnt vmcnt(0)\n\t"
    "v_max3_u32 v11, v6, v7, v8\n\t"
    "v_max_u32 v11, v11, v9\n\t"
    "v_cmp_eq_u32 vcc, -1, v11\n\t"
    "s_cbranch_vccnz 4f\n\t"
    "6:\n\t"
    "s_nop 2\n\t"
    "v_exp_f32 v28, v40\n\t"
    "v_max3_f32 v11, v40, v41, v42\n\t"
    "v_exp_f32 v29, v41\n\t"
    "v_max_f32 v11, v11, v43\n\t"
    "v_exp_f32 v30, v42\n\t"
    "v_cmp_nge_f32 vcc, %[bg], v11\n\t"
    "v_exp_f32 v31, v43\n\t"
    "s_cbranch_vccnz 10f\n\t"
    "11:\n\t"
    "v_cvt_pk_bf16_f32 v24, v28, v29\n\t"
    "v_cvt_pk_bf16_f32 v25, v30, v31\n\t"
    "s_nop 1\n\t"
    "v_mfma_f32_16x16x16_bf16 v[52:55], v[12:13], v[24:25], v[52:55]\n\t"
    "v_mfma_f32_16x16x16_bf16 v[44:47], v[6:7], v[24:25], v[44:47]\n\t"
    "v_mfma_f32_16x16x16_bf16 v[48:51], v[8:9], v[24:25], v[48:51]\n\t"
    "s_nop 5\n\t"
    "v_rcp_f32 v26, v52\n\t"
    "s_nop 0\n\t"
    "v_pk_mul_f32 v[44:45], v[44:45], v[26:27] op_sel_hi:[1,0]\n\t"
    "v_pk_mul_f32 v[46:47], v[46:47], v[26:27] op_sel_hi:[1,0]\n\t"
    "v_pk_mul_f32 v[48:49], v[48:49], v[26:27] op_sel_hi:[1,0]\n\t"
    "v_pk_mul_f32 v[50:51], v[50:51], v[26:27] op_sel_hi:[1,0]\n\t"
    "v_cvt_pk_bf16_f32 v36, v44, v45\n\t"
    "v_cvt_pk_bf16_f32 v37, v46, v47\n\t"
    "v_cvt_pk_bf16_f32 v38, v48, v49\n\t"
    "v_cvt_pk_bf16_f32 v39, v50, v51\n\t"
    "v_min_u32 v36, -2, v36\n\t"
    "v_min_u32 v37, -2, v37\n\t"
    "v_min_u32 v38, -2, v38\n\t"
    "v_min_u32 v39, -2, v39\n\t"
    "global_store_dwordx4 v15, v[36:39], %[od] sc1\n\t"
    "s_barrier\n\t"
    "s_mov_b32 exec_hi, 0\n\t"
    "global_store_dwordx4 v14, v[6:9], %[kv] sc1\n\t"
    "s_mov_b32 exec_hi, -1\n\t"
    "s_nop 0\n\t"
    "s_waitcnt lgkmcnt(0)\n\t"
    "s_branch 9f\n\t"
    "1:\n\t"
    "s_add_u32 %[sp], %[sp], 1\n\t"
    "s_cmp_ge_u32 %[sp], %[cap]\n\t"
    "s_cbranch_scc1 7f\n\t"
    "global_load_dwordx4 v[40:43], v6, %[ag] sc1\n\t"
    "global_load_dwordx4 v[44:47], v6, %[ag] offset:1024 sc1\n\t"
    "s_waitcnt lgkmcnt(0)\n\t"
    "s_memtime %[tc]\n\t"
    "s_nop 7\n\t"
    "s_branch 3b\n\t"
    "7:\n\t"
    "s_mov_b32 %[cap], 0\n\t"
    "s_branch 2b\n\t"
    "20:\n\t"
    "s_barrier\n\t"
    "s_barrier\n\t"
    "s_barrier\n\t"
    "s_branch 9f\n\t"
    "4:\n\t"
    "s_add_u32 %[sp], %[sp], 0x10000\n\t"
    "s_lshr_b32 %[st], %[sp], 16\n\t"
    "s_cmp_ge_u32 %[st], %[cap]\n\t"
    "s_cbranch_scc1 8f\n\t"
    "s_mov_b32 exec_hi, 0\n\t"
    "global_load_dwordx4 v[6:9], v14, %[vs] sc1\n\t"
    "s_mov_b32 exec_hi, -1\n\t"
    "s_branch 5b\n\t"
    "8:\n\t"
    "s_mov_b32 %[cap], 0\n\t"
    "s_branch 6b\n\t"
    "10:\n\t"
    "v_max_f32 v11, v11, v10\n\t"
    "v_mov_b32 v26, v11\n\t"
    "s_nop 1\n\t"
    "v_permlane16_swap_b32 v11, v26\n\t"
    "v_max_f32 v11, v11, v26\n\t"
    "v_sub_f32 v28, v40, v11\n\t"
    "v_sub_f32 v29, v41, v11\n\t"
    "v_sub_f32 v30, v42, v11\n\t"
    "v_sub_f32 v31, v43, v11\n\t"
    "v_sub_f32 v11, v10, v11\n\t"
    "v_exp_f32 v11, v11\n\t"
    "v_exp_f32 v28, v28\n\t"
    "v_exp_f32 v29, v29\n\t"
    "v_exp_f32 v30, v30\n\t"
    "v_exp_f32 v31, v31\n\t"
    "v_mul_f32 v44, v44, v11\n\t"
    "v_mul_f32 v45, v45, v11\n\t"
    "v_mul_f32 v46, v46, v11\n\t"
    "v_mul_f32 v47, v47, v11\n\t"
    "v_mul_f32 v48, v48, v11\n\t"
    "v_mul_f32 v49, v49, v11\n\t"
    "v_mul_f32 v50, v50, v11\n\t"
    "v_mul_f32 v51, v51, v11\n\t"
    "v_mul_f32 v52, v52, v11\n\t"
    "v_mul_f32 v53, v53, v11\n\t"
    "v_mul_f32 v54, v54, v11\n\t"
    "v_mul_f32 v55, v55, v11\n\t"
    "s_branch 11b\n\t"
    "12:\n\t"
    "v_max3_f32 v10, v24, v25, v26\n\t"
    "v_max3_f32 v40, v27, v28, v29\n\t"
    "v_max3_f32 v10, v10, v30, v31\n\t"
    "v_max3_f32 v10, v10, v40, %[neg]\n\t"
    "v_mov_b32 v40, v10\n\t"
    "s_nop 1\n\t"
    "v_permlane16_swap_b32 v10, v40\n\t"
    "v_max_f32 v10, v10, v40\n\t"
    "v_mov_b32 v40, v10\n\t"
    "s_nop 1\n\t"
    "v_permlane32_swap_b32 v10, v40\n\t"
    "v_max_f32 v10, v10, v40\n\t"
    "v_sub_f32 v24, v24, v10\n\t"
    "v_sub_f32 v25, v25, v10\n\t"
    "v_sub_f32 v26, v26, v10\n\t"
    "v_sub_f32 v27, v27, v10\n\t"
    "v_sub_f32 v28, v28, v10\n\t"
    "v_sub_f32 v29, v29, v10\n\t"
    "v_sub_f32 v30, v30, v10\n\t"
    "v_sub_f32 v31, v31, v10\n\t"
    "s_mov_b32 %[bg], 0x7fc00000\n\t"
    "s_branch 13b\n\t"
    "9:"
    : [tc] "=&s"(tc), [sp] "=&s"(sp), [cap] "+s"(cap), [st] "=&s"(st), [bg] "=&s"(bg)
    : [ag] "s"(ag), [c44] "s"(0x4444u), [kv] "s"(kv), [vs] "s"(vs), [od] "s"(od), [od2] "s"(od + 262144), [neg] "s"(-1.0e38f), [lsc] "s"(LSC), [one] "s"(0x3F803F80u), [big] "s"(24.0f), "{v[64:95]}"(W.w0), "{v[96:127]}"(W.w1), "{v[128:159]}"(W.w2), "{v[160:191]}"(W.w3), "{v[192:223]}"(W.w4), "{v[224:255]}"(W.w5)
    : "v6", "v7", "v8", "v9", "v10", "v11", "v12", "v13", "v14", "v15", "v16", "v17", "v18", "v19", "v20", "v21", "v22", "v23", "v24", "v25", "v26", "v27", "v28", "v29", "v30", "v31", "v32", "v33", "v34", "v35", "v36", "v37", "v38", "v39", "v40", "v41", "v42", "v43", "v44", "v45", "v46", "v47", "v48", "v49", "v50", "v51", "v52", "v53", "v54", "v55", "v56", "v57", "v58", "v59", "v60", "v61", "v62", "v63", "vcc", "scc", "memory");
}
template <> __device__ __forceinline__ void qk_step<2>(const QKW& W, const char* ag, const char* vs, const char* od, const char* kv, unsigned& cap, unsigned& sp,
                                                        unsigned long long& tc, unsigned long long* ts, unsigned& d0, unsigned& d1) {
  unsigned st, bg;
  asm volatile("v_mbcnt_lo_u32_b32 v60, -1, 0\n\t"
    "v_mbcnt_hi_u32_b32 v60, -1, v60\n\t"
    "v_lshlrev_b32 v6, 4, v60\n\t"
    "s_mov_b32 %[sp], 0\n\t"
    "s_setprio 3\n\t"
    "global_load_dwordx4 v[40:43], v6, %[ag] sc1\n\t"
    "global_load_dwordx4 v[44:47], v6, %[ag] offset:1024 sc1\n\t"
    "s_memtime %[tc]\n\t"
    "v_add_u32 v7, 45056, v6\n\t"
    "v_add_u32 v8, 102400, v6\n\t"
    "ds_read_b128 v[24:27], v7 offset:0\n\t"
    "ds_read_b128 v[28:31], v7 offset:1024\n\t"
    "ds_read_b128 v[32:35], v7 offset:8192\n\t"
    "ds_read_b128 v[36:39], v7 offset:9216\n\t"
    "v_lshrrev_b32 v48, 4, v60\n\t"
    "v_and_b32 v49, 15, v60\n\t"
    "v_and_b32 v61, 4, v60\n\t"
    "v_mul_u32_u24 v61, 0x2aaa, v61\n\t"
    "v_lshrrev_b32 v9, 1, v60\n\t"
    "v_and_b32 v9, 2, v9\n\t"
    "v_or3_b32 v9, v9, v61, %[c44]\n\t"
    "v_add_u32 v14, 1024, v6\n\t"
    "v_lshlrev_b32 v61, 2, v48\n\t"
    "v_lshl_or_b32 v10, v49, 4, v61\n\t"
    "v_lshrrev_b32 v50, 2, v49\n\t"
    "v_and_b32 v51, 3, v60\n\t"
    "v_and_b32 v61, 1, v60\n\t"
    "v_lshlrev_b32 v61, 3, v61\n\t"
    "v_lshl_or_b32 v11, v50, 4, v61\n\t"
    "v_lshl_or_b32 v11, v48, 7, v11\n\t"
    "v_xor_b32 v12, v48, v49\n\t"
    "v_lshlrev_b32 v12, 4, v12\n\t"
    "v_lshl_or_b32 v12, v49, 8, v12\n\t"
    "v_lshl_or_b32 v50, v48, 2, v50\n\t"
    "v_or_b32 v51, 8, v51\n\t"
    "v_xor_b32 v51, v51, v50\n\t"
    "v_lshlrev_b32 v13, 4, v51\n\t"
    "v_lshl_or_b32 v13, v50, 8, v13\n\t"
    "v_and_b32 v61, 3, v60\n\t"
    "v_lshlrev_b32 v61, 4, v61\n\t"
    "v_lshl_or_b32 v15, v48, 6, v61\n\t"
    "v_and_b32 v61, 8, v60\n\t"
    "v_lshl_or_b32 v15, v61, 14, v15\n\t"
    "v_and_b32 v61, 4, v60\n\t"
    "v_lshl_or_b32 v15, v61, 8, v15\n\t"
    "v_add_u32 v15, 512, v15\n\t"
    "3:\n\t"
    "v_mov_b32 v16, 0\n\t"
    "v_mov_b32 v17, 0\n\t"
    "v_mov_b32 v18, 0\n\t"
    "v_mov_b32 v19, 0\n\t"
    "v_mov_b32 v20, 0\n\t"
    "v_mov_b32 v21, 0\n\t"
    "v_mov_b32 v22, 0\n\t"
    "v_mov_b32 v23, 0\n\t"
    "s_waitcnt vmcnt(0) lgkmcnt(0)\n\t"
    "14:\n\t"
    "ds_write_b128 v6, v[40:43] offset:4096\n\t"
    "ds_write_b128 v6, v[44:47] offset:5120\n\t"
    "v_smfmac_f32_16x16x64_bf16 v[16:19], v[40:43], v[128:135], v9\n\t"
    "v_max3_u32 v60, v40, v41, v42\n\t"
    "v_max3_u32 v61, v43, v44, v45\n\t"
    "v_smfmac_f32_16x16x64_bf16 v[20:23], v[40:43], v[136:143], v9\n\t"
    "v_max3_u32 v60, v60, v46, v47\n\t"
    "v_max_u32 v60, v60, v61\n\t"
    "v_cmp_eq_u32 vcc, -1, v60\n\t"
    "s_cbranch_vccnz 1f\n\t"
    "2:\n\t"
    "s_waitcnt lgkmcnt(0)\n\t"
    "s_cmp_eq_u64 %[kv], 0\n\t"
    "s_barrier\n\t"
    "s_cbranch_scc1 20f\n\t"
    "ds_read_b128 v[40:43], v6 offset:12288\n\t"
    "ds_read_b128 v[48:51], v6 offset:0\n\t"
    "v_smfmac_f32_16x16x64_bf16 v[16:19], v[44:47], v[144:151], v9\n\t"
    "ds_read_b128 v[52:55], v6 offset:1024\n\t"
    "ds_read_b128 v[56:59], v6 offset:2048\n\t"
    "v_smfmac_f32_16x16x64_bf16 v[20:23], v[44:47], v[152:159], v9\n\t"
    "ds_read_b128 v[60:63], v6 offset:13312\n\t"
    "ds_read_b128 v[44:47], v6 offset:3072\n\t"
    "s_waitcnt lgkmcnt(5)\n\t"
    "v_smfmac_f32_16x16x64_bf16 v[16:19], v[40:43], v[24:31], v9\n\t"
    "ds_read_b128 v[24:27], v7 offset:16384\n\t"
    "ds_read_b128 v[28:31], v7 offset:17408\n\t"
    "v_smfmac_f32_16x16x64_bf16 v[20:23], v[40:43], v[32:39], v9\n\t"
    "ds_read_b128 v[32:35], v7 offset:24576\n\t"
    "ds_read_b128 v[36:39], v7 offset:25600\n\t"
    "ds_read_b128 v[40:43], v6 offset:6144\n\t"
    "s_waitcnt lgkmcnt(9)\n\t"
    "v_smfmac_f32_16x16x64_bf16 v[16:19], v[48:51], v[64:71], v9\n\t"
    "v_smfmac_f32_16x16x64_bf16 v[20:23], v[48:51], v[72:79], v9\n\t"
    "ds_read_b128 v[48:51], v6 offset:7168\n\t"
    "s_waitcnt lgkmcnt(9)\n\t"
    "v_smfmac_f32_16x16x64_bf16 v[16:19], v[52:55], v[80:87], v9\n\t"
    "v_smfmac_f32_16x16x64_bf16 v[20:23], v[52:55], v[88:95], v9\n\t"
    "ds_read_b128 v[52:55], v6 offset:14336\n\t"
    "s_waitcnt lgkmcnt(9)\n\t"
    "v_smfmac_f32_16x16x64_bf16 v[16:19], v[56:59], v[96:103], v9\n\t"
    "v_smfmac_f32_16x16x64_bf16 v[20:23], v[56:59], v[104:111], v9\n\t"
    "ds_read_b128 v[56:59], v6 offset:8192\n\t"
    "s_waitcnt lgkmcnt(6)\n\t"
    "v_smfmac_f32_16x16x64_bf16 v[16:19], v[60:63], v[24:31], v9\n\t"
    "ds_read_b128 v[24:27], v8 offset:0\n\t"
    "ds_read_b128 v[28:31], v8 offset:1024\n\t"
    "s_waitcnt lgkmcnt(6)\n\t"
    "v_smfmac_f32_16x16x64_bf16 v[20:23], v[60:63], v[32:39], v9\n\t"
    "ds_read_b128 v[32:35], v8 offset:8192\n\t"
    "ds_read_b128 v[36:39], v8 offset:9216\n\t"
    "ds_read_b128 v[60:63], v6 offset:9216\n\t"
    "v_smfmac_f32_16x16x64_bf16 v[16:19], v[44:47], v[112:119], v9\n\t"
    "v_smfmac_f32_16x16x64_bf16 v[20:23], v[44:47], v[120:127], v9\n\t"
    "ds_read_b128 v[44:47], v6 offset:10240\n\t"
    "s_waitcnt lgkmcnt(9)\n\t"
    "v_smfmac_f32_16x16x64_bf16 v[16:19], v[40:43], v[160:167], v9\n\t"
    "v_smfmac_f32_16x16x64_bf16 v[20:23], v[40:43], v[168:175], v9\n\t"
    "ds_read_b128 v[40:43], v6 offset:15360\n\t"
    "s_waitcnt lgkmcnt(9)\n\t"
    "v_smfmac_f32_16x16x64_bf16 v[16:19], v[48:51], v[176:183], v9\n\t"
    "v_smfmac_f32_16x16x64_bf16 v[20:23], v[48:51], v[184:191], v9\n\t"
    "ds_read_b128 v[48:51], v6 offset:11264\n\t"
    "s_waitcnt lgkmcnt(6)\n\t"
    "v_smfmac_f32_16x16x64_bf16 v[16:19], v[52:55], v[24:31], v9\n\t"
    "ds_read_b128 v[24:27], v8 offset:16384\n\t"
    "ds_read_b128 v[28:31], v8 offset:17408\n\t"
    "s_waitcnt lgkmcnt(6)\n\t"
    "v_smfmac_f32_16x16x64_bf16 v[20:23], v[52:55], v[32:39], v9\n\t"
    "ds_read_b128 v[32:35], v8 offset:24576\n\t"
    "ds_read_b128 v[36:39], v8 offset:25600\n\t"
    "v_smfmac_f32_16x16x64_bf16 v[16:19], v[56:59], v[192:199], v9\n\t"
    "v_smfmac_f32_16x16x64_bf16 v[20:23], v[56:59], v[200:207], v9\n\t"
    "s_waitcnt lgkmcnt(7)\n\t"
    "v_smfmac_f32_16x16x64_bf16 v[16:19], v[60:63], v[208:215], v9\n\t"
    "v_smfmac_f32_16x16x64_bf16 v[20:23], v[60:63], v[216:223], v9\n\t"
    "s_waitcnt lgkmcnt(6)\n\t"
    "v_smfmac_f32_16x16x64_bf16 v[16:19], v[44:47], v[224:231], v9\n\t"
    "v_smfmac_f32_16x16x64_bf16 v[20:23], v[44:47], v[232:239], v9\n\t"
    "s_waitcnt lgkmcnt(2)\n\t"
    "v_smfmac_f32_16x16x64_bf16 v[16:19], v[40:43], v[24:31], v9\n\t"
    "s_waitcnt lgkmcnt(0)\n\t"
    "v_smfmac_f32_16x16x64_bf16 v[20:23], v[40:43], v[32:39], v9\n\t"
    "v_smfmac_f32_16x16x64_bf16 v[16:19], v[48:51], v[240:247], v9\n\t"
    "v_smfmac_f32_16x16x64_bf16 v[20:23], v[48:51], v[248:255], v9\n\t"
    "v_xor_b32 v7, 64, v12\n\t"
    "v_xor_b32 v8, 0x80, v12\n\t"
    "v_xor_b32 v6, 0xc0, v12\n\t"
    "ds_read_b128 v[24:27], v12 offset:16384\n\t"
    "ds_read_b128 v[28:31], v12 offset:20480\n\t"
    "ds_read_b128 v[32:35], v7 offset:16384\n\t"
    "ds_read_b128 v[36:39], v7 offset:20480\n\t"
    "v_permlane16_swap_b32 v16, v18\n\t"
    "v_permlane16_swap_b32 v17, v19\n\t"
    "v_permlane16_swap_b32 v20, v22\n\t"
    "v_permlane16_swap_b32 v21, v23\n\t"
    "v_add_f32 v16, v16, v18\n\t"
    "v_add_f32 v17, v17, v19\n\t"
    "v_add_f32 v20, v20, v22\n\t"
    "v_mul_f32 v16, %[lsc], v16\n\t"
    "v_mul_f32 v17, %[lsc], v17\n\t"
    "v_cvt_pk_bf16_f32 v16, v16, v17\n\t"
    "v_add_f32 v21, v21, v23\n\t"
    "ds_write_b32 v10, v16 offset:33792\n\t"
    "v_mul_f32 v20, %[lsc], v20\n\t"
    "v_mul_f32 v21, %[lsc], v21\n\t"
    "v_cvt_pk_bf16_f32 v20, v20, v21\n\t"
    "ds_write_b32 v10, v20 offset:34048\n\t"
    "ds_read_b128 v[40:43], v8 offset:16384\n\t"
    "ds_read_b128 v[44:47], v8 offset:20480\n\t"
    "ds_read_b128 v[48:51], v6 offset:16384\n\t"
    "ds_read_b128 v[52:55], v6 offset:20480\n\t"
    "s_waitcnt lgkmcnt(4)\n\t"
    "s_barrier\n\t"
    "ds_read_b64_tr_b16 v[16:17], v11 offset:32768\n\t"
    "ds_read_b64_tr_b16 v[18:19], v11 offset:32832\n\t"
    "ds_read_b64_tr_b16 v[20:21], v11 offset:33280\n\t"
    "ds_read_b64_tr_b16 v[22:23], v11 offset:33344\n\t"
    "ds_read_b64_tr_b16 v[56:57], v11 offset:33792\n\t"
    "ds_read_b64_tr_b16 v[58:59], v11 offset:33856\n\t"
    "ds_read_b64_tr_b16 v[60:61], v11 offset:34304\n\t"
    "ds_read_b64_tr_b16 v[62:63], v11 offset:34368\n\t"
    "v_mov_b32 v6, 0\n\t"
    "v_mov_b32 v7, 0\n\t"
    "v_mov_b32 v8, 0\n\t"
    "v_mov_b32 v9, 0\n\t"
    "s_mov_b32 exec_hi, 0\n\t"
    "global_load_dwordx4 v[6:9], v14, %[vs] sc1\n\t"
    "s_mov_b32 exec_hi, -1\n\t"
    "s_waitcnt lgkmcnt(6)\n\t"
    "v_mfma_f32_16x16x32_bf16 v[24:27], v[24:27], v[16:19], 0\n\t"
    "v_mfma_f32_16x16x32_bf16 v[28:31], v[28:31], v[16:19], 0\n\t"
    "s_waitcnt lgkmcnt(4)\n\t"
    "v_mfma_f32_16x16x32_bf16 v[24:27], v[32:35], v[20:23], v[24:27]\n\t"
    "v_mfma_f32_16x16x32_bf16 v[28:31], v[36:39], v[20:23], v[28:31]\n\t"
    "ds_read_b64_tr_b16 v[32:33], v13 offset:24576\n\t"
    "ds_read_b64_tr_b16 v[34:35], v13 offset:28672\n\t"
    "ds_read_b64_tr_b16 v[36:37], v13 offset:24584\n\t"
    "ds_read_b64_tr_b16 v[38:39], v13 offset:28680\n\t"
    "s_waitcnt lgkmcnt(6)\n\t"
    "v_mfma_f32_16x16x32_bf16 v[24:27], v[40:43], v[56:59], v[24:27]\n\t"
    "v_mfma_f32_16x16x32_bf16 v[28:31], v[44:47], v[56:59], v[28:31]\n\t"
    "s_waitcnt lgkmcnt(4)\n\t"
    "v_mfma_f32_16x16x32_bf16 v[24:27], v[48:51], v[60:63], v[24:27]\n\t"
    "v_mfma_f32_16x16x32_bf16 v[28:31], v[52:55], v[60:63], v[28:31]\n\t"
    "s_setprio 1\n\t"
    "v_mov_b32 v52, %[one]\n\t"
    "v_mov_b32 v53, %[one]\n\t"
    "v_mov_b32 v54, %[one]\n\t"
    "v_mov_b32 v55, %[one]\n\t"
    "s_nop 2\n\t"
    "v_max3_f32 v10, |v24|, |v25|, |v26|\n\t"
    "v_max3_f32 v40, |v27|, |v28|, |v29|\n\t"
    "v_max3_f32 v10, v10, |v30|, |v31|\n\t"
    "v_max_f32 v10, v10, v40\n\t"
    "v_cmp_nge_f32 vcc, %[big], v10\n\t"
    "s_mov_b32 %[bg], %[big]\n\t"
    "v_mov_b32 v10, 0\n\t"
    "s_cbranch_vccnz 12f\n\t"
    "13:\n\t"
    "v_exp_f32 v24, v24\n\t"
    "v_exp_f32 v25, v25\n\t"
    "v_exp_f32 v26, v26\n\t"
    "v_exp_f32 v27, v27\n\t"
    "v_exp_f32 v28, v28\n\t"
    "v_exp_f32 v29, v29\n\t"
    "v_exp_f32 v30, v30\n\t"
    "v_exp_f32 v31, v31\n\t"
    "v_cvt_pk_bf16_f32 v40, v24, v25\n\t"
    "v_cvt_pk_bf16_f32 v41, v26, v27\n\t"
    "v_cvt_pk_bf16_f32 v42, v28, v29\n\t"
    "v_cvt_pk_bf16_f32 v43, v30, v31\n\t"
    "s_setprio 3\n\t"
    "s_waitcnt lgkmcnt(0)\n\t"
    "s_nop 0\n\t"
    "v_mfma_f32_16x16x32_bf16 v[44:47], v[32:35], v[40:43], 0\n\t"
    "v_mfma_f32_16x16x32_bf16 v[48:51], v[36:39], v[40:43], 0\n\t"
    "v_mfma_f32_16x16x32_bf16 v[52:55], v[52:55], v[40:43], 0\n\t"
    "v_mov_b32 v12, 0\n\t"
    "v_mov_b32 v13, 0\n\t"
    "s_mov_b32 exec_hi, 0\n\t"
    "v_mov_b32 v12, %[one]\n\t"
    "v_mov_b32 v13, %[one]\n\t"
    "s_mov_b32 exec_hi, -1\n\t"
    "s_barrier\n\t"
    "ds_read_b64_tr_b16 v[24:25], v11 offset:34816\n\t"
    "ds_read_b64_tr_b16 v[26:27], v11 offset:34880\n\t"
    "ds_read_b64_tr_b16 v[28:29], v11 offset:35328\n\t"
    "ds_read_b64_tr_b16 v[30:31], v11 offset:35392\n\t"
    "ds_read_b64_tr_b16 v[32:33], v11 offset:35840\n\t"
    "ds_read_b64_tr_b16 v[34:35], v11 offset:35904\n\t"
    "ds_read_b64_tr_b16 v[36:37], v11 offset:36352\n\t"
    "ds_read_b64_tr_b16 v[38:39], v11 offset:36416\n\t"
    "s_waitcnt lgkmcnt(6)\n\t"
    "v_mfma_f32_16x16x32_bf16 v[40:43], v[24:27], v[16:19], 0\n\t"
    "s_waitcnt lgkmcnt(4)\n\t"
    "v_mfma_f32_16x16x32_bf16 v[40:43], v[28:31], v[20:23], v[40:43]\n\t"
    "s_waitcnt lgkmcnt(2)\n\t"
    "v_mfma_f32_16x16x32_bf16 v[40:43], v[32:35], v[56:59], v[40:43]\n\t"
    "s_waitcnt lgkmcnt(0)\n\t"
    "v_mfma_f32_16x16x32_bf16 v[40:43], v[36:39], v[60:63], v[40:43]\n\t"
    "5:\n\t"
    "s_waitcnt vmcnt(0)\n\t"
    "v_max3_u32 v11, v6, v7, v8\n\t"
    "v_max_u32 v11, v11, v9\n\t"
    "v_cmp_eq_u32 vcc, -1, v11\n\t"
    "s_cbranch_vccnz 4f\n\t"
    "6:\n\t"
    "s_nop 2\n\t"
    "v_exp_f32 v28, v40\n\t"
    "v_max3_f32 v11, v40, v41, v42\n\t"
    "v_exp_f32 v29, v41\n\t"
    "v_max_f32 v11, v11, v43\n\t"
    "v_exp_f32 v30, v42\n\t"
    "v_cmp_nge_f32 vcc, %[bg], v11\n\t"
    "v_exp_f32 v31, v43\n\t"
    "s_cbranch_vccnz 10f\n\t"
    "11:\n\t"
    "v_cvt_pk_bf16_f32 v24, v28, v29\n\t"
    "v_cvt_pk_bf16_f32 v25, v30, v31\n\t"
    "s_nop 1\n\t"
    "v_mfma_f32_16x16x16_bf16 v[52:55], v[12:13], v[24:25], v[52:55]\n\t"
    "v_mfma_f32_16x16x16_bf16 v[44:47], v[6:7], v[24:25], v[44:47]\n\t"
    "v_mfma_f32_16x16x16_bf16 v[48:51], v[8:9], v[24:25], v[48:51]\n\t"
    "s_nop 5\n\t"
    "v_rcp_f32 v26, v52\n\t"
    "s_nop 0\n\t"
    "v_pk_mul_f32 v[44:45], v[44:45], v[26:27] op_sel_hi:[1,0]\n\t"
    "v_pk_mul_f32 v[46:47], v[46:47], v[26:27] op_sel_hi:[1,0]\n\t"
    "v_pk_mul_f32 v[48:49], v[48:49], v[26:27] op_sel_hi:[1,0]\n\t"
    "v_pk_mul_f32 v[50:51], v[50:51], v[26:27] op_sel_hi:[1,0]\n\t"
    "v_cvt_pk_bf16_f32 v36, v44, v45\n\t"
    "v_cvt_pk_bf16_f32 v37, v46, v47\n\t"
    "v_cvt_pk_bf16_f32 v38, v48, v49\n\t"
    "v_cvt_pk_bf16_f32 v39, v50, v51\n\t"
    "v_min_u32 v36, -2, v36\n\t"
    "v_min_u32 v37, -2, v37\n\t"
    "v_min_u32 v38, -2, v38\n\t"
    "v_min_u32 v39, -2, v39\n\t"
    "global_store_dwordx4 v15, v[36:39], %[od] sc1\n\t"
    "s_barrier\n\t"
    "s_mov_b32 exec_hi, 0\n\t"
    "global_store_dwordx4 v14, v[6:9], %[kv] sc1\n\t"
    "s_mov_b32 exec_hi, -1\n\t"
    "s_nop 0\n\t"
    "s_waitcnt lgkmcnt(0)\n\t"
    "s_branch 9f\n\t"
    "1:\n\t"
    "s_add_u32 %[sp], %[sp], 1\n\t"
    "s_cmp_ge_u32 %[sp], %[cap]\n\t"
    "s_cbranch_scc1 7f\n\t"
    "global_load_dwordx4 v[40:43], v6, %[ag] sc1\n\t"
    "global_load_dwordx4 v[44:47], v6, %[ag] offset:1024 sc1\n\t"
    "s_waitcnt lgkmcnt(0)\n\t"
    "s_memtime %[tc]\n\t"
    "s_nop 7\n\t"
    "s_branch 3b\n\t"
    "7:\n\t"
    "s_mov_b32 %[cap], 0\n\t"
    "s_branch 2b\n\t"
    "20:\n\t"
    "s_barrier\n\t"
    "s_barrier\n\t"
    "s_barrier\n\t"
    "s_branch 9f\n\t"
    "4:\n\t"
    "s_add_u32 %[sp], %[sp], 0x10000\n\t"
    "s_lshr_b32 %[st], %[sp], 16\n\t"
    "s_cmp_ge_u32 %[st], %[cap]\n\t"
    "s_cbranch_scc1 8f\n\t"
    "s_mov_b32 exec_hi, 0\n\t"
    "global_load_dwordx4 v[6:9], v14, %[vs] sc1\n\t"
    "s_mov_b32 exec_hi, -1\n\t"
    "s_branch 5b\n\t"
    "8:\n\t"
    "s_mov_b32 %[cap], 0\n\t"
    "s_branch 6b\n\t"
    "10:\n\t"
    "v_max_f32 v11, v11, v10\n\t"
    "v_mov_b32 v26, v11\n\t"
    "s_nop 1\n\t"
    "v_permlane16_swap_b32 v11, v26\n\t"
    "v_max_f32 v11, v11, v26\n\t"
    "v_sub_f32 v28, v40, v11\n\t"
    "v_sub_f32 v29, v41, v11\n\t"
    "v_sub_f32 v30, v42, v11\n\t"
    "v_sub_f32 v31, v43, v11\n\t"
    "v_sub_f32 v11, v10, v11\n\t"
    "v_exp_f32 v11, v11\n\t"
    "v_exp_f32 v28, v28\n\t"
    "v_exp_f32 v29, v29\n\t"
    "v_exp_f32 v30, v30\n\t"
    "v_exp_f32 v31, v31\n\t"
    "v_mul_f32 v44, v44, v11\n\t"
    "v_mul_f32 v45, v45, v11\n\t"
    "v_mul_f32 v46, v46, v11\n\t"
    "v_mul_f32 v47, v47, v11\n\t"
    "v_mul_f32 v48, v48, v11\n\t"
    "v_mul_f32 v49, v49, v11\n\t"
    "v_mul_f32 v50, v50, v11\n\t"
    "v_mul_f32 v51, v51, v11\n\t"
    "v_mul_f32 v52, v52, v11\n\t"
    "v_mul_f32 v53, v53, v11\n\t"
    "v_mul_f32 v54, v54, v11\n\t"
    "v_mul_f32 v55, v55, v11\n\t"
    "s_branch 11b\n\t"
    "12:\n\t"
    "v_max3_f32 v10, v24, v25, v26\n\t"
    "v_max3_f32 v40, v27, v28, v29\n\t"
    "v_max3_f32 v10, v10, v30, v31\n\t"
    "v_max3_f32 v10, v10, v40, %[neg]\n\t"
    "v_mov_b32 v40, v10\n\t"
    "s_nop 1\n\t"
    "v_permlane16_swap_b32 v10, v40\n\t"
    "v_max_f32 v10, v10, v40\n\t"
    "v_mov_b32 v40, v10\n\t"
    "s_nop 1\n\t"
    "v_permlane32_swap_b32 v10, v40\n\t"
    "v_max_f32 v10, v10, v40\n\t"
    "v_sub_f32 v24, v24, v10\n\t"
    "v_sub_f32 v25, v25, v10\n\t"
    "v_sub_f32 v26, v26, v10\n\t"
    "v_sub_f32 v27, v27, v10\n\t"
    "v_sub_f32 v28, v28, v10\n\t"
    "v_sub_f32 v29, v29, v10\n\t"
    "v_sub_f32 v30, v30, v10\n\t"
    "v_sub_f32 v31, v31, v10\n\t"
    "s_mov_b32 %[bg], 0x7fc00000\n\t"
    "s_branch 13b\n\t"
    "9:"
    : [tc] "=&s"(tc), [sp] "=&s"(sp), [cap] "+s"(cap), [st] "=&s"(st), [bg] "=&s"(bg)
    : [ag] "s"(ag), [c44] "s"(0x4444u), [kv] "s"(kv), [vs] "s"(vs), [od] "s"(od), [od2] "s"(od + 262144), [neg] "s"(-1.0e38f), [lsc] "s"(LSC), [one] "s"(0x3F803F80u), [big] "s"(24.0f), "{v[64:95]}"(W.w0), "{v[96:127]}"(W.w1), "{v[128:159]}"(W.w2), "{v[160:191]}"(W.w3), "{v[192:223]}"(W.w4), "{v[224:255]}"(W.w5)
    : "v6", "v7", "v8", "v9", "v10", "v11", "v12", "v13", "v14", "v15", "v16", "v17", "v18", "v19", "v20", "v21", "v22", "v23", "v24", "v25", "v26", "v27", "v28", "v29", "v30", "v31", "v32", "v33", "v34", "v35", "v36", "v37", "v38", "v39", "v40", "v41", "v42", "v43", "v44", "v45", "v46", "v47", "v48", "v49", "v50", "v51", "v52", "v53", "v54", "v55", "v56", "v57", "v58", "v59", "v60", "v61", "v62", "v63", "vcc", "scc", "memory");
}
template <> __device__ __forceinline__ void qk_step<3>(const QKW& W, const char* ag, const char* vs, const char* od, const char* kv, unsigned& cap, unsigned& sp,
                                                        unsigned long long& tc, unsigned long long* ts, unsigned& d0, unsigned& d1) {
  unsigned st, bg;
  asm volatile("v_mbcnt_lo_u32_b32 v60, -1, 0\n\t"
    "v_mbcnt_hi_u32_b32 v60, -1, v60\n\t"
    "v_lshlrev_b32 v6, 4, v60\n\t"
    "s_mov_b32 %[sp], 0\n\t"
    "s_setprio 3\n\t"
    "global_load_dwordx4 v[40:43], v6, %[ag] sc1\n\t"
    "global_load_dwordx4 v[44:47], v6, %[ag] offset:1024 sc1\n\t"
    "s_memtime %[tc]\n\t"
    "v_add_u32 v7, 47104, v6\n\t"
    "v_add_u32 v8, 104448, v6\n\t"
    "ds_read_b128 v[24:27], v7 offset:0\n\t"
    "ds_read_b128 v[28:31], v7 offset:1024\n\t"
    "ds_read_b128 v[32:35], v7 offset:8192\n\t"
    "ds_read_b128 v[36:39], v7 offset:9216\n\t"
    "v_lshrrev_b32 v48, 4, v60\n\t"
    "v_and_b32 v49, 15, v60\n\t"
    "v_and_b32 v61, 4, v60\n\t"
    "v_mul_u32_u24 v61, 0x2aaa, v61\n\t"
    "v_lshrrev_b32 v9, 1, v60\n\t"
    "v_and_b32 v9, 2, v9\n\t"
    "v_or3_b32 v9, v9, v61, %[c44]\n\t"
    "v_add_u32 v14, 1536, v6\n\t"
    "v_lshlrev_b32 v61, 2, v48\n\t"
    "v_lshl_or_b32 v10, v49, 4, v61\n\t"
    "v_lshrrev_b32 v50, 2, v49\n\t"
    "v_and_b32 v51, 3, v60\n\t"
    "v_and_b32 v61, 1, v60\n\t"
    "v_lshlrev_b32 v61, 3, v61\n\t"
    "v_lshl_or_b32 v11, v50, 4, v61\n\t"
    "v_lshl_or_b32 v11, v48, 7, v11\n\t"
    "v_xor_b32 v12, v48, v49\n\t"
    "v_lshlrev_b32 v12, 4, v12\n\t"
    "v_lshl_or_b32 v12, v49, 8, v12\n\t"
    "v_lshl_or_b32 v50, v48, 2, v50\n\t"
    "v_or_b32 v51, 12, v51\n\t"
    "v_xor_b32 v51, v51, v50\n\t"
    "v_lshlrev_b32 v13, 4, v51\n\t"
    "v_lshl_or_b32 v13, v50, 8, v13\n\t"
    "v_and_b32 v61, 3, v60\n\t"
    "v_lshlrev_b32 v61, 4, v61\n\t"
    "v_lshl_or_b32 v15, v48, 6, v61\n\t"
    "v_and_b32 v61, 8, v60\n\t"
    "v_lshl_or_b32 v15, v61, 14, v15\n\t"
    "v_and_b32 v61, 4, v60\n\t"
    "v_lshl_or_b32 v15, v61, 8, v15\n\t"
    "v_add_u32 v15, 768, v15\n\t"
    "3:\n\t"
    "v_mov_b32 v16, 0\n\t"
    "v_mov_b32 v17, 0\n\t"
    "v_mov_b32 v18, 0\n\t"
    "v_mov_b32 v19, 0\n\t"
    "v_mov_b32 v20, 0\n\t"
    "v_mov_b32 v21, 0\n\t"
    "v_mov_b32 v22, 0\n\t"
    "v_mov_b32 v23, 0\n\t"
    "s_waitcnt vmcnt(0) lgkmcnt(0)\n\t"
    "14:\n\t"
    "ds_write_b128 v6, v[40:43] offset:6144\n\t"
    "ds_write_b128 v6, v[44:47] offset:7168\n\t"
    "v_smfmac_f32_16x16x64_bf16 v[16:19], v[40:43], v[160:167], v9\n\t"
    "v_max3_u32 v60, v40, v41, v42\n\t"
    "v_max3_u32 v61, v43, v44, v45\n\t"
    "v_smfmac_f32_16x16x64_bf16 v[20:23], v[40:43], v[168:175], v9\n\t"
    "v_max3_u32 v60, v60, v46, v47\n\t"
    "v_max_u32 v60, v60, v61\n\t"
    "v_cmp_eq_u32 vcc, -1, v60\n\t"
    "s_cbranch_vccnz 1f\n\t"
    "2:\n\t"
    "s_waitcnt lgkmcnt(0)\n\t"
    "s_cmp_eq_u64 %[kv], 0\n\t"
    "s_barrier\n\t"
    "s_cbranch_scc1 20f\n\t"
    "ds_read_b128 v[40:43], v6 offset:12288\n\t"
    "ds_read_b128 v[48:51], v6 offset:0\n\t"
    "v_smfmac_f32_16x16x64_bf16 v[16:19], v[44:47], v[176:183], v9\n\t"
    "ds_read_b128 v[52:55], v6 offset:1024\n\t"
    "ds_read_b128 v[56:59], v6 offset:2048\n\t"
    "v_smfmac_f32_16x16x64_bf16 v[20:23], v[44:47], v[184:191], v9\n\t"
    "ds_read_b128 v[60:63], v6 offset:13312\n\t"
    "ds_read_b128 v[44:47], v6 offset:3072\n\t"
    "s_waitcnt lgkmcnt(5)\n\t"
    "v_smfmac_f32_16x16x64_bf16 v[16:19], v[40:43], v[24:31], v9\n\t"
    "ds_read_b128 v[24:27], v7 offset:16384\n\t"
    "ds_read_b128 v[28:31], v7 offset:17408\n\t"
    "v_smfmac_f32_16x16x64_bf16 v[20:23], v[40:43], v[32:39], v9\n\t"
    "ds_read_b128 v[32:35], v7 offset:24576\n\t"
    "ds_read_b128 v[36:39], v7 offset:25600\n\t"
    "ds_read_b128 v[40:43], v6 offset:4096\n\t"
    "s_waitcnt lgkmcnt(9)\n\t"
    "v_smfmac_f32_16x16x64_bf16 v[16:19], v[48:51], v[64:71], v9\n\t"
    "v_smfmac_f32_16x16x64_bf16 v[20:23], v[48:51], v[72:79], v9\n\t"
    "ds_read_b128 v[48:51], v6 offset:5120\n\t"
    "s_waitcnt lgkmcnt(9)\n\t"
    "v_smfmac_f32_16x16x64_bf16 v[16:19], v[52:55], v[80:87], v9\n\t"
    "v_smfmac_f32_16x16x64_bf16 v[20:23], v[52:55], v[88:95], v9\n\t"
    "ds_read_b128 v[52:55], v6 offset:14336\n\t"
    "s_waitcnt lgkmcnt(9)\n\t"
    "v_smfmac_f32_16x16x64_bf16 v[16:19], v[56:59], v[96:103], v9\n\t"
    "v_smfmac_f32_16x16x64_bf16 v[20:23], v[56:59], v[104:111], v9\n\t"
    "ds_read_b128 v[56:59], v6 offset:8192\n\t"
    "s_waitcnt lgkmcnt(6)\n\t"
    "v_smfmac_f32_16x16x64_bf16 v[16:19], v[60:63], v[24:31], v9\n\t"
    "ds_read_b128 v[24:27], v8 offset:0\n\t"
    "ds_read_b128 v[28:31], v8 offset:1024\n\t"
    "s_waitcnt lgkmcnt(6)\n\t"
    "v_smfmac_f32_16x16x64_bf16 v[20:23], v[60:63], v[32:39], v9\n\t"
    "ds_read_b128 v[32:35], v8 offset:8192\n\t"
    "ds_read_b128 v[36:39], v8 offset:9216\n\t"
    "ds_read_b128 v[60:63], v6 offset:9216\n\t"
    "v_smfmac_f32_16x16x64_bf16 v[16:19], v[44:47], v[112:119], v9\n\t"
    "v_smfmac_f32_16x16x64_bf16 v[20:23], v[44:47], v[120:127], v9\n\t"
    "ds_read_b128 v[44:47], v6 offset:10240\n\t"
    "s_waitcnt lgkmcnt(9)\n\t"
    "v_smfmac_f32_16x16x64_bf16 v[16:19], v[40:43], v[128:135], v9\n\t"
    "v_smfmac_f32_16x16x64_bf16 v[20:23], v[40:43], v[136:143], v9\n\t"
    "ds_read_b128 v[40:43], v6 offset:15360\n\t"
    "s_waitcnt lgkmcnt(9)\n\t"
    "v_smfmac_f32_16x16x64_bf16 v[16:19], v[48:51], v[144:151], v9\n\t"
    "v_smfmac_f32_16x16x64_bf16 v[20:23], v[48:51], v[152:159], v9\n\t"
    "ds_read_b128 v[48:51], v6 offset:11264\n\t"
    "s_waitcnt lgkmcnt(6)\n\t"
    "v_smfmac_f32_16x16x64_bf16 v[16:19], v[52:55], v[24:31], v9\n\t"
    "ds_read_b128 v[24:27], v8 offset:16384\n\t"
    "ds_read_b128 v[28:31], v8 offset:17408\n\t"
    "s_waitcnt lgkmcnt(6)\n\t"
    "v_smfmac_f32_16x16x64_bf16 v[20:23], v[52:55], v[32:39], v9\n\t"
    "ds_read_b128 v[32:35], v8 offset:24576\n\t"
    "ds_read_b128 v[36:39], v8 offset:25600\n\t"
    "v_smfmac_f32_16x16x64_bf16 v[16:19], v[56:59], v[192:199], v9\n\t"
    "v_smfmac_f32_16x16x64_bf16 v[20:23], v[56:59], v[200:207], v9\n\t"
    "s_waitcnt lgkmcnt(7)\n\t"
    "v_smfmac_f32_16x16x64_bf16 v[16:19], v[60:63], v[208:215], v9\n\t"
    "v_smfmac_f32_16x16x64_bf16 v[20:23], v[60:63], v[216:223], v9\n\t"
    "s_waitcnt lgkmcnt(6)\n\t"
    "v_smfmac_f32_16x16x64_bf16 v[16:19], v[44:47], v[224:231], v9\n\t"
    "v_smfmac_f32_16x16x64_bf16 v[20:23], v[44:47], v[232:239], v9\n\t"
    "s_waitcnt lgkmcnt(2)\n\t"
    "v_smfmac_f32_16x16x64_bf16 v[16:19], v[40:43], v[24:31], v9\n\t"
    "s_waitcnt lgkmcnt(0)\n\t"
    "v_smfmac_f32_16x16x64_bf16 v[20:23], v[40:43], v[32:39], v9\n\t"
    "v_smfmac_f32_16x16x64_bf16 v[16:19], v[48:51], v[240:247], v9\n\t"
    "v_smfmac_f32_16x16x64_bf16 v[20:23], v[48:51], v[248:255], v9\n\t"
    "v_xor_b32 v7, 64, v12\n\t"
    "v_xor_b32 v8, 0x80, v12\n\t"
    "v_xor_b32 v6, 0xc0, v12\n\t"
    "ds_read_b128 v[24:27], v12 offset:16384\n\t"
    "ds_read_b128 v[28:31], v12 offset:20480\n\t"
    "ds_read_b128 v[32:35], v7 offset:16384\n\t"
    "ds_read_b128 v[36:39], v7 offset:20480\n\t"
    "v_permlane16_swap_b32 v16, v18\n\t"
    "v_permlane16_swap_b32 v17, v19\n\t"
    "v_permlane16_swap_b32 v20, v22\n\t"
    "v_permlane16_swap_b32 v21, v23\n\t"
    "v_add_f32 v16, v16, v18\n\t"
    "v_add_f32 v17, v17, v19\n\t"
    "v_add_f32 v20, v20, v22\n\t"
    "v_mul_f32 v16, %[lsc], v16\n\t"
    "v_mul_f32 v17, %[lsc], v17\n\t"
    "v_cvt_pk_bf16_f32 v16, v16, v17\n\t"
    "v_add_f32 v21, v21, v23\n\t"
    "ds_write_b32 v10, v16 offset:34304\n\t"
    "v_mul_f32 v20, %[lsc], v20\n\t"
    "v_mul_f32 v21, %[lsc], v21\n\t"
    "v_cvt_pk_bf16_f32 v20, v20, v21\n\t"
    "ds_write_b32 v10, v20 offset:34560\n\t"
    "ds_read_b128 v[40:43], v8 offset:16384\n\t"
    "ds_read_b128 v[44:47], v8 offset:20480\n\t"
    "ds_read_b128 v[48:51], v6 offset:16384\n\t"
    "ds_read_b128 v[52:55], v6 offset:20480\n\t"
    "s_waitcnt lgkmcnt(4)\n\t"
    "s_barrier\n\t"
    "ds_read_b64_tr_b16 v[16:17], v11 offset:32768\n\t"
    "ds_read_b64_tr_b16 v[18:19], v11 offset:32832\n\t"
    "ds_read_b64_tr_b16 v[20:21], v11 offset:33280\n\t"
    "ds_read_b64_tr_b16 v[22:23], v11 offset:33344\n\t"
    "ds_read_b64_tr_b16 v[56:57], v11 offset:33792\n\t"
    "ds_read_b64_tr_b16 v[58:59], v11 offset:33856\n\t"
    "ds_read_b64_tr_b16 v[60:61], v11 offset:34304\n\t"
    "ds_read_b64_tr_b16 v[62:63], v11 offset:34368\n\t"
    "v_mov_b32 v6, 0\n\t"
    "v_mov_b32 v7, 0\n\t"
    "v_mov_b32 v8, 0\n\t"
    "v_mov_b32 v9, 0\n\t"
    "s_mov_b32 exec_hi, 0\n\t"
    "global_load_dwordx4 v[6:9], v14, %[vs] sc1\n\t"
    "s_mov_b32 exec_hi, -1\n\t"
    "s_waitcnt lgkmcnt(6)\n\t"
    "v_mfma_f32_16x16x32_bf16 v[24:27], v[24:27], v[16:19], 0\n\t"
    "v_mfma_f32_16x16x32_bf16 v[28:31], v[28:31], v[16:19], 0\n\t"
    "s_waitcnt lgkmcnt(4)\n\t"
    "v_mfma_f32_16x16x32_bf16 v[24:27], v[32:35], v[20:23], v[24:27]\n\t"
    "v_mfma_f32_16x16x32_bf16 v[28:31], v[36:39], v[20:23], v[28:31]\n\t"
    "ds_read_b64_tr_b16 v[32:33], v13 offset:24576\n\t"
    "ds_read_b64_tr_b16 v[34:35], v13 offset:28672\n\t"
    "ds_read_b64_tr_b16 v[36:37], v13 offset:24584\n\t"
    "ds_read_b64_tr_b16 v[38:39], v13 offset:28680\n\t"
    "s_waitcnt lgkmcnt(6)\n\t"
    "v_mfma_f32_16x16x32_bf16 v[24:27], v[40:43], v[56:59], v[24:27]\n\t"
    "v_mfma_f32_16x16x32_bf16 v[28:31], v[44:47], v[56:59], v[28:31]\n\t"
    "s_waitcnt lgkmcnt(4)\n\t"
    "v_mfma_f32_16x16x32_bf16 v[24:27], v[48:51], v[60:63], v[24:27]\n\t"
    "v_mfma_f32_16x16x32_bf16 v[28:31], v[52:55], v[60:63], v[28:31]\n\t"
    "s_setprio 1\n\t"
    "v_mov_b32 v52, %[one]\n\t"
    "v_mov_b32 v53, %[one]\n\t"
    "v_mov_b32 v54, %[one]\n\t"
    "v_mov_b32 v55, %[one]\n\t"
    "s_nop 2\n\t"
    "v_max3_f32 v10, |v24|, |v25|, |v26|\n\t"
    "v_max3_f32 v40, |v27|, |v28|, |v29|\n\t"
    "v_max3_f32 v10, v10, |v30|, |v31|\n\t"
    "v_max_f32 v10, v10, v40\n\t"
    "v_cmp_nge_f32 vcc, %[big], v10\n\t"
    "s_mov_b32 %[bg], %[big]\n\t"
    "v_mov_b32 v10, 0\n\t"
    "s_cbranch_vccnz 12f\n\t"
    "13:\n\t"
    "v_exp_f32 v24, v24\n\t"
    "v_exp_f32 v25, v25\n\t"
    "v_exp_f32 v26, v26\n\t"
    "v_exp_f32 v27, v27\n\t"
    "v_exp_f32 v28, v28\n\t"
    "v_exp_f32 v29, v29\n\t"
    "v_exp_f32 v30, v30\n\t"
    "v_exp_f32 v31, v31\n\t"
    "v_cvt_pk_bf16_f32 v40, v24, v25\n\t"
    "v_cvt_pk_bf16_f32 v41, v26, v27\n\t"
    "v_cvt_pk_bf16_f32 v42, v28, v29\n\t"
    "v_cvt_pk_bf16_f32 v43, v30, v31\n\t"
    "s_setprio 3\n\t"
    "s_waitcnt lgkmcnt(0)\n\t"
    "s_nop 0\n\t"
    "v_mfma_f32_16x16x32_bf16 v[44:47], v[32:35], v[40:43], 0\n\t"
    "v_mfma_f32_16x16x32_bf16 v[48:51], v[36:39], v[40:43], 0\n\t"
    "v_mfma_f32_16x16x32_bf16 v[52:55], v[52:55], v[40:43], 0\n\t"
    "v_mov_b32 v12, 0\n\t"
    "v_mov_b32 v13, 0\n\t"
    "s_mov_b32 exec_hi, 0\n\t"
    "v_mov_b32 v12, %[one]\n\t"
    "v_mov_b32 v13, %[one]\n\t"
    "s_mov_b32 exec_hi, -1\n\t"
    "s_barrier\n\t"
    "ds_read_b64_tr_b16 v[24:25], v11 offset:34816\n\t"
    "ds_read_b64_tr_b16 v[26:27], v11 offset:34880\n\t"
    "ds_read_b64_tr_b16 v[28:29], v11 offset:35328\n\t"
    "ds_read_b64_tr_b16 v[30:31], v11 offset:35392\n\t"
    "ds_read_b64_tr_b16 v[32:33], v11 offset:35840\n\t"
    "ds_read_b64_tr_b16 v[34:35], v11 offset:35904\n\t"
    "ds_read_b64_tr_b16 v[36:37], v11 offset:36352\n\t"
    "ds_read_b64_tr_b16 v[38:39], v11 offset:36416\n\t"
    "s_waitcnt lgkmcnt(6)\n\t"
    "v_mfma_f32_16x16x32_bf16 v[40:43], v[24:27], v[16:19], 0\n\t"
    "s_waitcnt lgkmcnt(4)\n\t"
    "v_mfma_f32_16x16x32_bf16 v[40:43], v[28:31], v[20:23], v[40:43]\n\t"
    "s_waitcnt lgkmcnt(2)\n\t"
    "v_mfma_f32_16x16x32_bf16 v[40:43], v[32:35], v[56:59], v[40:43]\n\t"
    "s_waitcnt lgkmcnt(0)\n\t"
    "v_mfma_f32_16x16x32_bf16 v[40:43], v[36:39], v[60:63], v[40:43]\n\t"
    "5:\n\t"
    "s_waitcnt vmcnt(0)\n\t"
    "v_max3_u32 v11, v6, v7, v8\n\t"
    "v_max_u32 v11, v11, v9\n\t"
    "v_cmp_eq_u32 vcc, -1, v11\n\t"
    "s_cbranch_vccnz 4f\n\t"
    "6:\n\t"
    "s_nop 2\n\t"
    "v_exp_f32 v28, v40\n\t"
    "v_max3_f32 v11, v40, v41, v42\n\t"
    "v_exp_f32 v29, v41\n\t"
    "v_max_f32 v11, v11, v43\n\t"
    "v_exp_f32 v30, v42\n\t"
    "v_cmp_nge_f32 vcc, %[bg], v11\n\t"
    "v_exp_f32 v31, v43\n\t"
    "s_cbranch_vccnz 10f\n\t"
    "11:\n\t"
    "v_cvt_pk_bf16_f32 v24, v28, v29\n\t"
    "v_cvt_pk_bf16_f32 v25, v30, v31\n\t"
    "s_nop 1\n\t"
    "v_mfma_f32_16x16x16_bf16 v[52:55], v[12:13], v[24:25], v[52:55]\n\t"
    "v_mfma_f32_16x16x16_bf16 v[44:47], v[6:7], v[24:25], v[44:47]\n\t"
    "v_mfma_f32_16x16x16_bf16 v[48:51], v[8:9], v[24:25], v[48:51]\n\t"
    "s_nop 5\n\t"
    "v_rcp_f32 v26, v52\n\t"
    "s_nop 0\n\t"
    "v_pk_mul_f32 v[44:45], v[44:45], v[26:27] op_sel_hi:[1,0]\n\t"
    "v_pk_mul_f32 v[46:47], v[46:47], v[26:27] op_sel_hi:[1,0]\n\t"
    "v_pk_mul_f32 v[48:49], v[48:49], v[26:27] op_sel_hi:[1,0]\n\t"
    "v_pk_mul_f32 v[50:51], v[50:51], v[26:27] op_sel_hi:[1,0]\n\t"
    "v_cvt_pk_bf16_f32 v36, v44, v45\n\t"
    "v_cvt_pk_bf16_f32 v37, v46, v47\n\t"
    "v_cvt_pk_bf16_f32 v38, v48, v49\n\t"
    "v_cvt_pk_bf16_f32 v39, v50, v51\n\t"
    "v_min_u32 v36, -2, v36\n\t"
    "v_min_u32 v37, -2, v37\n\t"
    "v_min_u32 v38, -2, v38\n\t"
    "v_min_u32 v39, -2, v39\n\t"
    "global_store_dwordx4 v15, v[36:39], %[od] sc1\n\t"
    "s_barrier\n\t"
    "s_mov_b32 exec_hi, 0\n\t"
    "global_store_dwordx4 v14, v[6:9], %[kv] sc1\n\t"
    "s_mov_b32 exec_hi, -1\n\t"
    "s_nop 0\n\t"
    "s_waitcnt lgkmcnt(0)\n\t"
    "s_branch 9f\n\t"
    "1:\n\t"
    "s_add_u32 %[sp], %[sp], 1\n\t"
    "s_cmp_ge_u32 %[sp], %[cap]\n\t"
    "s_cbranch_scc1 7f\n\t"
    "global_load_dwordx4 v[40:43], v6, %[ag] sc1\n\t"
    "global_load_dwordx4 v[44:47], v6, %[ag] offset:1024 sc1\n\t"
    "s_waitcnt lgkmcnt(0)\n\t"
    "s_memtime %[tc]\n\t"
    "s_nop 7\n\t"
    "s_branch 3b\n\t"
    "7:\n\t"
    "s_mov_b32 %[cap], 0\n\t"
    "s_branch 2b\n\t"
    "20:\n\t"
    "s_barrier\n\t"
    "s_barrier\n\t"
    "s_barrier\n\t"
    "s_branch 9f\n\t"
    "4:\n\t"
    "s_add_u32 %[sp], %[sp], 0x10000\n\t"
    "s_lshr_b32 %[st], %[sp], 16\n\t"
    "s_cmp_ge_u32 %[st], %[cap]\n\t"
    "s_cbranch_scc1 8f\n\t"
    "s_mov_b32 exec_hi, 0\n\t"
    "global_load_dwordx4 v[6:9], v14, %[vs] sc1\n\t"
    "s_mov_b32 exec_hi, -1\n\t"
    "s_branch 5b\n\t"
    "8:\n\t"
    "s_mov_b32 %[cap], 0\n\t"
    "s_branch 6b\n\t"
    "10:\n\t"
    "v_max_f32 v11, v11, v10\n\t"
    "v_mov_b32 v26, v11\n\t"
    "s_nop 1\n\t"
    "v_permlane16_swap_b32 v11, v26\n\t"
    "v_max_f32 v11, v11, v26\n\t"
    "v_sub_f32 v28, v40, v11\n\t"
    "v_sub_f32 v29, v41, v11\n\t"
    "v_sub_f32 v30, v42, v11\n\t"
    "v_sub_f32 v31, v43, v11\n\t"
    "v_sub_f32 v11, v10, v11\n\t"
    "v_exp_f32 v11, v11\n\t"
    "v_exp_f32 v28, v28\n\t"
    "v_exp_f32 v29, v29\n\t"
    "v_exp_f32 v30, v30\n\t"
    "v_exp_f32 v31, v31\n\t"
    "v_mul_f32 v44, v44, v11\n\t"
    "v_mul_f32 v45, v45, v11\n\t"
    "v_mul_f32 v46, v46, v11\n\t"
    "v_mul_f32 v47, v47, v11\n\t"
    "v_mul_f32 v48, v48, v11\n\t"
    "v_mul_f32 v49, v49, v11\n\t"
    "v_mul_f32 v50, v50, v11\n\t"
    "v_mul_f32 v51, v51, v11\n\t"
    "v_mul_f32 v52, v52, v11\n\t"
    "v_mul_f32 v53, v53, v11\n\t"
    "v_mul_f32 v54, v54, v11\n\t"
    "v_mul_f32 v55, v55, v11\n\t"
    "s_branch 11b\n\t"
    "12:\n\t"
    "v_max3_f32 v10, v24, v25, v26\n\t"
    "v_max3_f32 v40, v27, v28, v29\n\t"
    "v_max3_f32 v10, v10, v30, v31\n\t"
    "v_max3_f32 v10, v10, v40, %[neg]\n\t"
    "v_mov_b32 v40, v10\n\t"
    "s_nop 1\n\t"
    "v_permlane16_swap_b32 v10, v40\n\t"
    "v_max_f32 v10, v10, v40\n\t"
    "v_mov_b32 v40, v10\n\t"
    "s_nop 1\n\t"
    "v_permlane32_swap_b32 v10, v40\n\t"
    "v_max_f32 v10, v10, v40\n\t"
    "v_sub_f32 v24, v24, v10\n\t"
    "v_sub_f32 v25, v25, v10\n\t"
    "v_sub_f32 v26, v26, v10\n\t"
    "v_sub_f32 v27, v27, v10\n\t"
    "v_sub_f32 v28, v28, v10\n\t"
    "v_sub_f32 v29, v29, v10\n\t"
    "v_sub_f32 v30, v30, v10\n\t"
    "v_sub_f32 v31, v31, v10\n\t"
    "s_mov_b32 %[bg], 0x7fc00000\n\t"
    "s_branch 13b\n\t"
    "9:"
    : [tc] "=&s"(tc), [sp] "=&s"(sp), [cap] "+s"(cap), [st] "=&s"(st), [bg] "=&s"(bg)
    : [ag] "s"(ag), [c44] "s"(0x4444u), [kv] "s"(kv), [vs] "s"(vs), [od] "s"(od), [od2] "s"(od + 262144), [neg] "s"(-1.0e38f), [lsc] "s"(LSC), [one] "s"(0x3F803F80u), [big] "s"(24.0f), "{v[64:95]}"(W.w0), "{v[96:127]}"(W.w1), "{v[128:159]}"(W.w2), "{v[160:191]}"(W.w3), "{v[192:223]}"(W.w4), "{v[224:255]}"(W.w5)
    : "v6", "v7", "v8", "v9", "v10", "v11", "v12", "v13", "v14", "v15", "v16", "v17", "v18", "v19", "v20", "v21", "v22", "v23", "v24", "v25", "v26", "v27", "v28", "v29", "v30", "v31", "v32", "v33", "v34", "v35", "v36", "v37", "v38", "v39", "v40", "v41", "v42", "v43", "v44", "v45", "v46", "v47", "v48", "v49", "v50", "v51", "v52", "v53", "v54", "v55", "v56", "v57", "v58", "v59", "v60", "v61", "v62", "v63", "vcc", "scc", "memory");
}
template <> __device__ __forceinline__ void qk_step<4>(const QKW& W, const char* ag, const char* vs, const char* od, const char* kv, unsigned& cap, unsigned& sp,
                                                        unsigned long long& tc, unsigned long long* ts, unsigned& d0, unsigned& d1) {
  unsigned st, bg;
  asm volatile("v_mbcnt_lo_u32_b32 v52, -1, 0\n\t"
    "v_mbcnt_hi_u32_b32 v52, -1, v52\n\t"
    "v_lshlrev_b32 v6, 4, v52\n\t"
    "s_mov_b32 %[sp], 0\n\t"
    "global_load_dwordx4 v[36:39], v6, %[ag] sc1\n\t"
    "global_load_dwordx4 v[40:43], v6, %[ag] offset:1024 sc1\n\t"
    "s_memtime %[tc]\n\t"
    "v_add_u32 v7, 73728, v6\n\t"
    "v_add_u32 v8, 131072, v6\n\t"
    "ds_read_b128 v[20:23], v7 offset:8192\n\t"
    "ds_read_b128 v[24:27], v7 offset:9216\n\t"
    "ds_read_b128 v[28:31], v7 offset:0\n\t"
    "ds_read_b128 v[32:35], v7 offset:1024\n\t"
    "v_lshrrev_b32 v44, 4, v52\n\t"
    "v_and_b32 v45, 15, v52\n\t"
    "v_and_b32 v53, 4, v52\n\t"
    "v_mul_u32_u24 v53, 0x2aaa, v53\n\t"
    "v_lshrrev_b32 v9, 1, v52\n\t"
    "v_and_b32 v9, 2, v9\n\t"
    "v_or3_b32 v9, v9, v53, %[c44]\n\t"
    "v_lshlrev_b32 v53, 2, v44\n\t"
    "v_lshl_or_b32 v10, v45, 4, v53\n\t"
    "3:\n\t"
    "v_mov_b32 v12, 0\n\t"
    "v_mov_b32 v13, 0\n\t"
    "v_mov_b32 v14, 0\n\t"
    "v_mov_b32 v15, 0\n\t"
    "v_mov_b32 v16, 0\n\t"
    "v_mov_b32 v17, 0\n\t"
    "v_mov_b32 v18, 0\n\t"
    "v_mov_b32 v19, 0\n\t"
    "s_waitcnt vmcnt(0) lgkmcnt(0)\n\t"
    "14:\n\t"
    "ds_write_b128 v6, v[36:39] offset:8192\n\t"
    "ds_write_b128 v6, v[40:43] offset:9216\n\t"
    "v_smfmac_f32_16x16x64_bf16 v[12:15], v[36:39], v[128:135], v9\n\t"
    "v_max3_u32 v52, v36, v37, v38\n\t"
    "v_max3_u32 v53, v39, v40, v41\n\t"
    "v_smfmac_f32_16x16x64_bf16 v[16:19], v[36:39], v[136:143], v9\n\t"
    "v_max3_u32 v52, v52, v42, v43\n\t"
    "v_max_u32 v52, v52, v53\n\t"
    "v_cmp_eq_u32 vcc, -1, v52\n\t"
    "s_cbranch_vccnz 1f\n\t"
    "2:\n\t"
    "s_waitcnt lgkmcnt(0)\n\t"
    "s_barrier\n\t"
    "s_memrealtime s[36:37]\n\t"
    "ds_read_b128 v[36:39], v6 offset:0\n\t"
    "ds_read_b128 v[44:47], v6 offset:4096\n\t"
    "v_smfmac_f32_16x16x64_bf16 v[12:15], v[40:43], v[144:151], v9\n\t"
    "ds_read_b128 v[48:51], v6 offset:5120\n\t"
    "ds_read_b128 v[52:55], v6 offset:6144\n\t"
    "v_smfmac_f32_16x16x64_bf16 v[16:19], v[40:43], v[152:159], v9\n\t"
    "ds_read_b128 v[40:43], v6 offset:1024\n\t"
    "s_waitcnt lgkmcnt(0)\n\t"
    "s_mov_b32 s38, s36\n\t"
    "s_min_u32 s37, %[d0], %[d1]\n\t"
    "s_add_u32 s36, s36, s37\n\t"
    "s_and_b32 s36, s36, 0xffffff\n\t"
    "s_or_b32 s36, s36, %[sn]\n\t"
    "v_lshlrev_b32 v11, 10, v6\n\t"
    "v_mov_b32 v5, s36\n\t"
    "s_mov_b64 exec, 15\n\t"
    "global_store_dword v11, v5, %[vs] offset:-4096 sc1\n\t"
    "s_mov_b64 exec, -1\n\t"
    "s_sleep 0\n\t"
    "v_smfmac_f32_16x16x64_bf16 v[12:15], v[36:39], v[56:63], v9\n\t"
    "v_smfmac_f32_16x16x64_bf16 v[16:19], v[36:39], v[28:35], v9\n\t"
    "ds_read_b128 v[28:31], v7 offset:16384\n\t"
    "ds_read_b128 v[32:35], v7 offset:17408\n\t"
    "ds_read_b128 v[36:39], v6 offset:7168\n\t"
    "v_smfmac_f32_16x16x64_bf16 v[12:15], v[44:47], v[64:71], v9\n\t"
    "v_smfmac_f32_16x16x64_bf16 v[16:19], v[44:47], v[72:79], v9\n\t"
    "ds_read_b128 v[44:47], v6 offset:10240\n\t"
    "v_smfmac_f32_16x16x64_bf16 v[12:15], v[48:51], v[80:87], v9\n\t"
    "v_smfmac_f32_16x16x64_bf16 v[16:19], v[48:51], v[88:95], v9\n\t"
    "ds_read_b128 v[48:51], v6 offset:11264\n\t"
    "v_smfmac_f32_16x16x64_bf16 v[12:15], v[52:55], v[96:103], v9\n\t"
    "v_smfmac_f32_16x16x64_bf16 v[16:19], v[52:55], v[104:111], v9\n\t"
    "ds_read_b128 v[52:55], v6 offset:2048\n\t"
    "v_smfmac_f32_16x16x64_bf16 v[12:15], v[40:43], v[20:27], v9\n\t"
    "ds_read_b128 v[20:23], v8 offset:0\n\t"
    "ds_read_b128 v[24:27], v8 offset:1024\n\t"
    "s_waitcnt lgkmcnt(6)\n\t"
    "v_smfmac_f32_16x16x64_bf16 v[16:19], v[40:43], v[28:35], v9\n\t"
    "ds_read_b128 v[28:31], v8 offset:8192\n\t"
    "ds_read_b128 v[32:35], v8 offset:9216\n\t"
    "ds_read_b128 v[40:43], v6 offset:12288\n\t"
    "s_waitcnt lgkmcnt(8)\n\t"
    "v_smfmac_f32_16x16x64_bf16 v[12:15], v[36:39], v[112:119], v9\n\t"
    "v_smfmac_f32_16x16x64_bf16 v[16:19], v[36:39], v[120:127], v9\n\t"
    "ds_read_b128 v[36:39], v6 offset:13312\n\t"
    "s_waitcnt lgkmcnt(8)\n\t"
    "v_smfmac_f32_16x16x64_bf16 v[12:15], v[44:47], v[160:167], v9\n\t"
    "v_smfmac_f32_16x16x64_bf16 v[16:19], v[44:47], v[168:175], v9\n\t"
    "ds_read_b128 v[44:47], v6 offset:14336\n\t"
    "s_barrier\n\t"
    "s_setprio 2\n\t"
    "s_waitcnt lgkmcnt(8)\n\t"
    "v_smfmac_f32_16x16x64_bf16 v[12:15], v[48:51], v[176:183], v9\n\t"
    "v_smfmac_f32_16x16x64_bf16 v[16:19], v[48:51], v[184:191], v9\n\t"
    "ds_read_b128 v[48:51], v6 offset:3072\n\t"
    "s_waitcnt lgkmcnt(6)\n\t"
    "v_smfmac_f32_16x16x64_bf16 v[12:15], v[52:55], v[20:27], v9\n\t"
    "ds_read_b128 v[20:23], v8 offset:16384\n\t"
    "ds_read_b128 v[24:27], v8 offset:17408\n\t"
    "s_waitcnt lgkmcnt(6)\n\t"
    "v_smfmac_f32_16x16x64_bf16 v[16:19], v[52:55], v[28:35], v9\n\t"
    "ds_read_b128 v[28:31], v8 offset:24576\n\t"
    "ds_read_b128 v[32:35], v8 offset:25600\n\t"
    "ds_read_b128 v[52:55], v6 offset:15360\n\t"
    "s_waitcnt lgkmcnt(8)\n\t"
    "v_smfmac_f32_16x16x64_bf16 v[12:15], v[40:43], v[192:199], v9\n\t"
    "v_smfmac_f32_16x16x64_bf16 v[16:19], v[40:43], v[200:207], v9\n\t"
    "s_waitcnt lgkmcnt(7)\n\t"
    "v_smfmac_f32_16x16x64_bf16 v[12:15], v[36:39], v[208:215], v9\n\t"
    "v_smfmac_f32_16x16x64_bf16 v[16:19], v[36:39], v[216:223], v9\n\t"
    "s_waitcnt lgkmcnt(6)\n\t"
    "v_smfmac_f32_16x16x64_bf16 v[12:15], v[44:47], v[224:231], v9\n\t"
    "v_smfmac_f32_16x16x64_bf16 v[16:19], v[44:47], v[232:239], v9\n\t"
    "s_waitcnt lgkmcnt(3)\n\t"
    "v_smfmac_f32_16x16x64_bf16 v[12:15], v[48:51], v[20:27], v9\n\t"
    "s_waitcnt lgkmcnt(1)\n\t"
    "v_smfmac_f32_16x16x64_bf16 v[16:19], v[48:51], v[28:35], v9\n\t"
    "s_waitcnt lgkmcnt(0)\n\t"
    "v_smfmac_f32_16x16x64_bf16 v[12:15], v[52:55], v[240:247], v9\n\t"
    "v_smfmac_f32_16x16x64_bf16 v[16:19], v[52:55], v[248:255], v9\n\t"
    "s_nop 6\n\t"
    "v_permlane16_swap_b32 v12, v14\n\t"
    "v_permlane16_swap_b32 v13, v15\n\t"
    "v_permlane16_swap_b32 v16, v18\n\t"
    "v_permlane16_swap_b32 v17, v19\n\t"
    "v_add_f32 v12, v12, v14\n\t"
    "v_add_f32 v13, v13, v15\n\t"
    "v_add_f32 v16, v16, v18\n\t"
    "v_cvt_pk_bf16_f32 v12, v12, v13\n\t"
    "v_add_f32 v17, v17, v19\n\t"
    "ds_write_b32 v10, v12 offset:34816\n\t"
    "v_cvt_pk_bf16_f32 v16, v16, v17\n\t"
    "ds_write_b32 v10, v16 offset:35072\n\t"
    "s_waitcnt lgkmcnt(0)\n\t"
    "s_setprio 0\n\t"
    "s_barrier\n\t"
    "s_memrealtime s[36:37]\n\t"
    "s_waitcnt lgkmcnt(0)\n\t"
    "s_and_b32 s36, s36, 0xffffff\n\t"
    "s_or_b32 s36, s36, %[sn]\n\t"
    "v_lshlrev_b32 v7, 10, v6\n\t"
    "v_mov_b32 v8, s36\n\t"
    "s_mov_b64 exec, 15\n\t"
    "global_store_dword v7, v8, %[vs] offset:0 sc1\n\t"
    "s_mov_b64 exec, -1\n\t"
    "s_barrier\n\t"
    "s_memrealtime s[36:37]\n\t"
    "s_mov_b32 %[d1], %[d0]\n\t"
    "s_waitcnt lgkmcnt(0)\n\t"
    "s_sub_u32 %[d0], s36, s38\n\t"
    "s_min_u32 %[d0], %[d0], 200\n\t"
    "s_waitcnt lgkmcnt(0)\n\t"
    "s_branch 9f\n\t"
    "1:\n\t"
    "s_add_u32 %[sp], %[sp], 1\n\t"
    "s_cmp_ge_u32 %[sp], %[cap]\n\t"
    "s_cbranch_scc1 7f\n\t"
    "global_load_dwordx4 v[36:39], v6, %[ag] sc1\n\t"
    "global_load_dwordx4 v[40:43], v6, %[ag] offset:1024 sc1\n\t"
    "s_waitcnt lgkmcnt(0)\n\t"
    "s_memtime %[tc]\n\t"
    "s_nop 7\n\t"
    "s_branch 3b\n\t"
    "7:\n\t"
    "s_mov_b32 %[cap], 0\n\t"
    "s_branch 2b\n\t"
    "9:"
    : [tc] "=&s"(tc), [sp] "=&s"(sp), [cap] "+s"(cap), [d0] "+s"(d0), [d1] "+s"(d1)
    : [ag] "s"(ag), [c44] "s"(0x4444u), [vs] "s"(vs), [sn] "s"((unsigned)(size_t)od), "{v[64:95]}"(W.w0), "{v[96:127]}"(W.w1), "{v[128:159]}"(W.w2), "{v[160:191]}"(W.w3), "{v[192:223]}"(W.w4), "{v[224:255]}"(W.w5), "{v[56:63]}"(W.x)
    : "v5", "v6", "v7", "v8", "v9", "v10", "v11", "v12", "v13", "v14", "v15", "v16", "v17", "v18", "v19", "v20", "v21", "v22", "v23", "v24", "v25", "v26", "v27", "v28", "v29", "v30", "v31", "v32", "v33", "v34", "v35", "v36", "v37", "v38", "v39", "v40", "v41", "v42", "v43", "v44", "v45", "v46", "v47", "v48", "v49", "v50", "v51", "v52", "v53", "v54", "v55", "vcc", "scc", "memory", "s36", "s37", "s38");
}
template <> __device__ __forceinline__ void qk_step<5>(const QKW& W, const char* ag, const char* vs, const char* od, const char* kv, unsigned& cap, unsigned& sp,
                                                        unsigned long long& tc, unsigned long long* ts, unsigned& d0, unsigned& d1) {
  unsigned st, bg;
  asm volatile("v_mbcnt_lo_u32_b32 v52, -1, 0\n\t"
    "v_mbcnt_hi_u32_b32 v52, -1, v52\n\t"
    "v_lshlrev_b32 v6, 4, v52\n\t"
    "s_mov_b32 %[sp], 0\n\t"
    "global_load_dwordx4 v[36:39], v6, %[ag] sc1\n\t"
    "global_load_dwordx4 v[40:43], v6, %[ag] offset:1024 sc1\n\t"
    "s_memtime %[tc]\n\t"
    "v_add_u32 v7, 75776, v6\n\t"
    "v_add_u32 v8, 133120, v6\n\t"
    "ds_read_b128 v[20:23], v7 offset:8192\n\t"
    "ds_read_b128 v[24:27], v7 offset:9216\n\t"
    "ds_read_b128 v[28:31], v7 offset:0\n\t"
    "ds_read_b128 v[32:35], v7 offset:1024\n\t"
    "v_lshrrev_b32 v44, 4, v52\n\t"
    "v_and_b32 v45, 15, v52\n\t"
    "v_and_b32 v53, 4, v52\n\t"
    "v_mul_u32_u24 v53, 0x2aaa, v53\n\t"
    "v_lshrrev_b32 v9, 1, v52\n\t"
    "v_and_b32 v9, 2, v9\n\t"
    "v_or3_b32 v9, v9, v53, %[c44]\n\t"
    "v_lshlrev_b32 v53, 2, v44\n\t"
    "v_lshl_or_b32 v10, v45, 4, v53\n\t"
    "v_and_b32 v53, 1, v52\n\t"
    "v_lshlrev_b32 v53, 3, v53\n\t"
    "v_lshl_or_b32 v11, v44, 7, v53\n\t"
    "v_and_b32 v53, 12, v52\n\t"
    "v_lshl_or_b32 v11, v53, 2, v11\n\t"
    "v_and_b32 v53, 2, v52\n\t"
    "v_lshl_or_b32 v11, v53, 8, v11\n\t"
    "3:\n\t"
    "v_mov_b32 v12, 0\n\t"
    "v_mov_b32 v13, 0\n\t"
    "v_mov_b32 v14, 0\n\t"
    "v_mov_b32 v15, 0\n\t"
    "v_mov_b32 v16, 0\n\t"
    "v_mov_b32 v17, 0\n\t"
    "v_mov_b32 v18, 0\n\t"
    "v_mov_b32 v19, 0\n\t"
    "s_waitcnt vmcnt(0) lgkmcnt(0)\n\t"
    "14:\n\t"
    "ds_write_b128 v6, v[36:39] offset:10240\n\t"
    "ds_write_b128 v6, v[40:43] offset:11264\n\t"
    "v_smfmac_f32_16x16x64_bf16 v[12:15], v[36:39], v[160:167], v9\n\t"
    "v_max3_u32 v52, v36, v37, v38\n\t"
    "v_max3_u32 v53, v39, v40, v41\n\t"
    "v_smfmac_f32_16x16x64_bf16 v[16:19], v[36:39], v[168:175], v9\n\t"
    "v_max3_u32 v52, v52, v42, v43\n\t"
    "v_max_u32 v52, v52, v53\n\t"
    "v_cmp_eq_u32 vcc, -1, v52\n\t"
    "s_cbranch_vccnz 1f\n\t"
    "2:\n\t"
    "s_waitcnt lgkmcnt(0)\n\t"
    "s_barrier\n\t"
    "ds_read_b128 v[36:39], v6 offset:0\n\t"
    "ds_read_b128 v[44:47], v6 offset:4096\n\t"
    "v_smfmac_f32_16x16x64_bf16 v[12:15], v[40:43], v[176:183], v9\n\t"
    "ds_read_b128 v[48:51], v6 offset:5120\n\t"
    "ds_read_b128 v[52:55], v6 offset:6144\n\t"
    "v_smfmac_f32_16x16x64_bf16 v[16:19], v[40:43], v[184:191], v9\n\t"
    "ds_read_b128 v[40:43], v6 offset:1024\n\t"
    "s_sleep 0\n\t"
    "s_waitcnt lgkmcnt(4)\n\t"
    "v_smfmac_f32_16x16x64_bf16 v[12:15], v[36:39], v[56:63], v9\n\t"
    "v_smfmac_f32_16x16x64_bf16 v[16:19], v[36:39], v[28:35], v9\n\t"
    "ds_read_b128 v[28:31], v7 offset:16384\n\t"
    "ds_read_b128 v[32:35], v7 offset:17408\n\t"
    "ds_read_b128 v[36:39], v6 offset:7168\n\t"
    "s_waitcnt lgkmcnt(6)\n\t"
    "v_smfmac_f32_16x16x64_bf16 v[12:15], v[44:47], v[64:71], v9\n\t"
    "v_smfmac_f32_16x16x64_bf16 v[16:19], v[44:47], v[72:79], v9\n\t"
    "ds_read_b128 v[44:47], v6 offset:8192\n\t"
    "s_waitcnt lgkmcnt(6)\n\t"
    "v_smfmac_f32_16x16x64_bf16 v[12:15], v[48:51], v[80:87], v9\n\t"
    "v_smfmac_f32_16x16x64_bf16 v[16:19], v[48:51], v[88:95], v9\n\t"
    "ds_read_b128 v[48:51], v6 offset:9216\n\t"
    "s_waitcnt lgkmcnt(6)\n\t"
    "v_smfmac_f32_16x16x64_bf16 v[12:15], v[52:55], v[96:103], v9\n\t"
    "v_smfmac_f32_16x16x64_bf16 v[16:19], v[52:55], v[104:111], v9\n\t"
    "ds_read_b128 v[52:55], v6 offset:2048\n\t"
    "s_waitcnt lgkmcnt(6)\n\t"
    "v_smfmac_f32_16x16x64_bf16 v[12:15], v[40:43], v[20:27], v9\n\t"
    "ds_read_b128 v[20:23], v8 offset:0\n\t"
    "ds_read_b128 v[24:27], v8 offset:1024\n\t"
    "s_waitcnt lgkmcnt(6)\n\t"
    "v_smfmac_f32_16x16x64_bf16 v[16:19], v[40:43], v[28:35], v9\n\t"
    "ds_read_b128 v[28:31], v8 offset:8192\n\t"
    "ds_read_b128 v[32:35], v8 offset:9216\n\t"
    "ds_read_b128 v[40:43], v6 offset:12288\n\t"
    "s_waitcnt lgkmcnt(8)\n\t"
    "v_smfmac_f32_16x16x64_bf16 v[12:15], v[36:39], v[112:119], v9\n\t"
    "v_smfmac_f32_16x16x64_bf16 v[16:19], v[36:39], v[120:127], v9\n\t"
    "ds_read_b128 v[36:39], v6 offset:13312\n\t"
    "s_waitcnt lgkmcnt(8)\n\t"
    "v_smfmac_f32_16x16x64_bf16 v[12:15], v[44:47], v[128:135], v9\n\t"
    "v_smfmac_f32_16x16x64_bf16 v[16:19], v[44:47], v[136:143], v9\n\t"
    "ds_read_b128 v[44:47], v6 offset:14336\n\t"
    "s_barrier\n\t"
    "s_setprio 2\n\t"
    "s_waitcnt lgkmcnt(8)\n\t"
    "v_smfmac_f32_16x16x64_bf16 v[12:15], v[48:51], v[144:151], v9\n\t"
    "v_smfmac_f32_16x16x64_bf16 v[16:19], v[48:51], v[152:159], v9\n\t"
    "ds_read_b128 v[48:51], v6 offset:3072\n\t"
    "s_waitcnt lgkmcnt(6)\n\t"
    "v_smfmac_f32_16x16x64_bf16 v[12:15], v[52:55], v[20:27], v9\n\t"
    "ds_read_b128 v[20:23], v8 offset:16384\n\t"
    "ds_read_b128 v[24:27], v8 offset:17408\n\t"
    "s_waitcnt lgkmcnt(6)\n\t"
    "v_smfmac_f32_16x16x64_bf16 v[16:19], v[52:55], v[28:35], v9\n\t"
    "ds_read_b128 v[28:31], v8 offset:24576\n\t"
    "ds_read_b128 v[32:35], v8 offset:25600\n\t"
    "ds_read_b128 v[52:55], v6 offset:15360\n\t"
    "s_waitcnt lgkmcnt(8)\n\t"
    "v_smfmac_f32_16x16x64_bf16 v[12:15], v[40:43], v[192:199], v9\n\t"
    "v_smfmac_f32_16x16x64_bf16 v[16:19], v[40:43], v[200:207], v9\n\t"
    "s_waitcnt lgkmcnt(7)\n\t"
    "v_smfmac_f32_16x16x64_bf16 v[12:15], v[36:39], v[208:215], v9\n\t"
    "v_smfmac_f32_16x16x64_bf16 v[16:19], v[36:39], v[216:223], v9\n\t"
    "s_waitcnt lgkmcnt(6)\n\t"
    "v_smfmac_f32_16x16x64_bf16 v[12:15], v[44:47], v[224:231], v9\n\t"
    "v_smfmac_f32_16x16x64_bf16 v[16:19], v[44:47], v[232:239], v9\n\t"
    "s_waitcnt lgkmcnt(3)\n\t"
    "v_smfmac_f32_16x16x64_bf16 v[12:15], v[48:51], v[20:27], v9\n\t"
    "s_waitcnt lgkmcnt(1)\n\t"
    "v_smfmac_f32_16x16x64_bf16 v[16:19], v[48:51], v[28:35], v9\n\t"
    "s_waitcnt lgkmcnt(0)\n\t"
    "v_smfmac_f32_16x16x64_bf16 v[12:15], v[52:55], v[240:247], v9\n\t"
    "v_smfmac_f32_16x16x64_bf16 v[16:19], v[52:55], v[248:255], v9\n\t"
    "s_nop 6\n\t"
    "v_permlane16_swap_b32 v12, v14\n\t"
    "v_permlane16_swap_b32 v13, v15\n\t"
    "v_permlane16_swap_b32 v16, v18\n\t"
    "v_permlane16_swap_b32 v17, v19\n\t"
    "v_add_f32 v12, v12, v14\n\t"
    "v_add_f32 v13, v13, v15\n\t"
    "v_add_f32 v16, v16, v18\n\t"
    "v_cvt_pk_bf16_f32 v12, v12, v13\n\t"
    "v_add_f32 v17, v17, v19\n\t"
    "ds_write_b32 v10, v12 offset:35328\n\t"
    "v_cvt_pk_bf16_f32 v16, v16, v17\n\t"
    "ds_write_b32 v10, v16 offset:35584\n\t"
    "s_waitcnt lgkmcnt(0)\n\t"
    "s_setprio 0\n\t"
    "s_barrier\n\t"
    "ds_read_b64_tr_b16 v[36:37], v11 offset:34816\n\t"
    "ds_read_b64_tr_b16 v[38:39], v11 offset:34880\n\t"
    "ds_read_b64_tr_b16 v[40:41], v11 offset:35840\n\t"
    "ds_read_b64_tr_b16 v[42:43], v11 offset:35904\n\t"
    "s_waitcnt lgkmcnt(0)\n\t"
    "v_min_u32 v36, -2, v36\n\t"
    "v_min_u32 v37, -2, v37\n\t"
    "v_min_u32 v38, -2, v38\n\t"
    "v_min_u32 v39, -2, v39\n\t"
    "v_min_u32 v40, -2, v40\n\t"
    "v_min_u32 v41, -2, v41\n\t"
    "v_min_u32 v42, -2, v42\n\t"
    "v_min_u32 v43, -2, v43\n\t"
    "global_store_dwordx4 v6, v[36:39], %[vs] sc1\n\t"
    "global_store_dwordx4 v6, v[40:43], %[vs] offset:1024 sc1\n\t"
    "s_nop 1\n\t"
    "s_barrier\n\t"
    "s_waitcnt lgkmcnt(0)\n\t"
    "s_branch 9f\n\t"
    "1:\n\t"
    "s_add_u32 %[sp], %[sp], 1\n\t"
    "s_cmp_ge_u32 %[sp], %[cap]\n\t"
    "s_cbranch_scc1 7f\n\t"
    "global_load_dwordx4 v[36:39], v6, %[ag] sc1\n\t"
    "global_load_dwordx4 v[40:43], v6, %[ag] offset:1024 sc1\n\t"
    "s_waitcnt lgkmcnt(0)\n\t"
    "s_memtime %[tc]\n\t"
    "s_nop 7\n\t"
    "s_branch 3b\n\t"
    "7:\n\t"
    "s_mov_b32 %[cap], 0\n\t"
    "s_branch 2b\n\t"
    "9:"
    : [tc] "=&s"(tc), [sp] "=&s"(sp), [cap] "+s"(cap)
    : [ag] "s"(ag), [c44] "s"(0x4444u), [vs] "s"(vs), "{v[64:95]}"(W.w0), "{v[96:127]}"(W.w1), "{v[128:159]}"(W.w2), "{v[160:191]}"(W.w3), "{v[192:223]}"(W.w4), "{v[224:255]}"(W.w5), "{v[56:63]}"(W.x)
    : "v6", "v7", "v8", "v9", "v10", "v11", "v12", "v13", "v14", "v15", "v16", "v17", "v18", "v19", "v20", "v21", "v22", "v23", "v24", "v25", "v26", "v27", "v28", "v29", "v30", "v31", "v32", "v33", "v34", "v35", "v36", "v37", "v38", "v39", "v40", "v41", "v42", "v43", "v44", "v45", "v46", "v47", "v48", "v49", "v50", "v51", "v52", "v53", "v54", "v55", "vcc", "scc", "memory");
}
template <> __device__ __forceinline__ void qk_step<6>(const QKW& W, const char* ag, const char* vs, const char* od, const char* kv, unsigned& cap, unsigned& sp,
                                                        unsigned long long& tc, unsigned long long* ts, unsigned& d0, unsigned& d1) {
  unsigned st, bg;
  asm volatile("v_mbcnt_lo_u32_b32 v52, -1, 0\n\t"
    "v_mbcnt_hi_u32_b32 v52, -1, v52\n\t"
    "v_lshlrev_b32 v6, 4, v52\n\t"
    "s_mov_b32 %[sp], 0\n\t"
    "global_load_dwordx4 v[36:39], v6, %[ag] sc1\n\t"
    "global_load_dwordx4 v[40:43], v6, %[ag] offset:1024 sc1\n\t"
    "s_memtime %[tc]\n\t"
    "v_add_u32 v7, 77824, v6\n\t"
    "v_add_u32 v8, 135168, v6\n\t"
    "ds_read_b128 v[20:23], v7 offset:8192\n\t"
    "ds_read_b128 v[24:27], v7 offset:9216\n\t"
    "ds_read_b128 v[28:31], v7 offset:0\n\t"
    "ds_read_b128 v[32:35], v7 offset:1024\n\t"
    "v_lshrrev_b32 v44, 4, v52\n\t"
    "v_and_b32 v45, 15, v52\n\t"
    "v_and_b32 v53, 4, v52\n\t"
    "v_mul_u32_u24 v53, 0x2aaa, v53\n\t"
    "v_lshrrev_b32 v9, 1, v52\n\t"
    "v_and_b32 v9, 2, v9\n\t"
    "v_or3_b32 v9, v9, v53, %[c44]\n\t"
    "v_lshlrev_b32 v53, 2, v44\n\t"
    "v_lshl_or_b32 v10, v45, 4, v53\n\t"
    "3:\n\t"
    "v_mov_b32 v12, 0\n\t"
    "v_mov_b32 v13, 0\n\t"
    "v_mov_b32 v14, 0\n\t"
    "v_mov_b32 v15, 0\n\t"
    "v_mov_b32 v16, 0\n\t"
    "v_mov_b32 v17, 0\n\t"
    "v_mov_b32 v18, 0\n\t"
    "v_mov_b32 v19, 0\n\t"
    "s_waitcnt vmcnt(0) lgkmcnt(0)\n\t"
    "14:\n\t"
    "ds_write_b128 v6, v[36:39] offset:12288\n\t"
    "ds_write_b128 v6, v[40:43] offset:13312\n\t"
    "v_smfmac_f32_16x16x64_bf16 v[12:15], v[36:39], v[192:199], v9\n\t"
    "v_max3_u32 v52, v36, v37, v38\n\t"
    "v_max3_u32 v53, v39, v40, v41\n\t"
    "v_smfmac_f32_16x16x64_bf16 v[16:19], v[36:39], v[200:207], v9\n\t"
    "v_max3_u32 v52, v52, v42, v43\n\t"
    "v_max_u32 v52, v52, v53\n\t"
    "v_cmp_eq_u32 vcc, -1, v52\n\t"
    "s_cbranch_vccnz 1f\n\t"
    "2:\n\t"
    "s_waitcnt lgkmcnt(0)\n\t"
    "s_barrier\n\t"
    "ds_read_b128 v[36:39], v6 offset:0\n\t"
    "ds_read_b128 v[44:47], v6 offset:4096\n\t"
    "v_smfmac_f32_16x16x64_bf16 v[12:15], v[40:43], v[208:215], v9\n\t"
    "ds_read_b128 v[48:51], v6 offset:5120\n\t"
    "ds_read_b128 v[52:55], v6 offset:6144\n\t"
    "v_smfmac_f32_16x16x64_bf16 v[16:19], v[40:43], v[216:223], v9\n\t"
    "ds_read_b128 v[40:43], v6 offset:1024\n\t"
    "s_sleep 0\n\t"
    "s_waitcnt lgkmcnt(4)\n\t"
    "v_smfmac_f32_16x16x64_bf16 v[12:15], v[36:39], v[56:63], v9\n\t"
    "v_smfmac_f32_16x16x64_bf16 v[16:19], v[36:39], v[28:35], v9\n\t"
    "ds_read_b128 v[28:31], v7 offset:16384\n\t"
    "ds_read_b128 v[32:35], v7 offset:17408\n\t"
    "ds_read_b128 v[36:39], v6 offset:7168\n\t"
    "s_waitcnt lgkmcnt(6)\n\t"
    "v_smfmac_f32_16x16x64_bf16 v[12:15], v[44:47], v[64:71], v9\n\t"
    "v_smfmac_f32_16x16x64_bf16 v[16:19], v[44:47], v[72:79], v9\n\t"
    "ds_read_b128 v[44:47], v6 offset:8192\n\t"
    "s_waitcnt lgkmcnt(6)\n\t"
    "v_smfmac_f32_16x16x64_bf16 v[12:15], v[48:51], v[80:87], v9\n\t"
    "v_smfmac_f32_16x16x64_bf16 v[16:19], v[48:51], v[88:95], v9\n\t"
    "ds_read_b128 v[48:51], v6 offset:9216\n\t"
    "s_waitcnt lgkmcnt(6)\n\t"
    "v_smfmac_f32_16x16x64_bf16 v[12:15], v[52:55], v[96:103], v9\n\t"
    "v_smfmac_f32_16x16x64_bf16 v[16:19], v[52:55], v[104:111], v9\n\t"
    "ds_read_b128 v[52:55], v6 offset:2048\n\t"
    "s_waitcnt lgkmcnt(6)\n\t"
    "v_smfmac_f32_16x16x64_bf16 v[12:15], v[40:43], v[20:27], v9\n\t"
    "ds_read_b128 v[20:23], v8 offset:0\n\t"
    "ds_read_b128 v[24:27], v8 offset:1024\n\t"
    "s_waitcnt lgkmcnt(6)\n\t"
    "v_smfmac_f32_16x16x64_bf16 v[16:19], v[40:43], v[28:35], v9\n\t"
    "ds_read_b128 v[28:31], v8 offset:8192\n\t"
    "ds_read_b128 v[32:35], v8 offset:9216\n\t"
    "ds_read_b128 v[40:43], v6 offset:10240\n\t"
    "s_waitcnt lgkmcnt(8)\n\t"
    "v_smfmac_f32_16x16x64_bf16 v[12:15], v[36:39], v[112:119], v9\n\t"
    "v_smfmac_f32_16x16x64_bf16 v[16:19], v[36:39], v[120:127], v9\n\t"
    "ds_read_b128 v[36:39], v6 offset:11264\n\t"
    "s_waitcnt lgkmcnt(8)\n\t"
    "v_smfmac_f32_16x16x64_bf16 v[12:15], v[44:47], v[128:135], v9\n\t"
    "v_smfmac_f32_16x16x64_bf16 v[16:19], v[44:47], v[136:143], v9\n\t"
    "ds_read_b128 v[44:47], v6 offset:14336\n\t"
    "s_barrier\n\t"
    "s_setprio 2\n\t"
    "s_waitcnt lgkmcnt(8)\n\t"
    "v_smfmac_f32_16x16x64_bf16 v[12:15], v[48:51], v[144:151], v9\n\t"
    "v_smfmac_f32_16x16x64_bf16 v[16:19], v[48:51], v[152:159], v9\n\t"
    "ds_read_b128 v[48:51], v6 offset:3072\n\t"
    "s_waitcnt lgkmcnt(6)\n\t"
    "v_smfmac_f32_16x16x64_bf16 v[12:15], v[52:55], v[20:27], v9\n\t"
    "ds_read_b128 v[20:23], v8 offset:16384\n\t"
    "ds_read_b128 v[24:27], v8 offset:17408\n\t"
    "s_waitcnt lgkmcnt(6)\n\t"
    "v_smfmac_f32_16x16x64_bf16 v[16:19], v[52:55], v[28:35], v9\n\t"
    "ds_read_b128 v[28:31], v8 offset:24576\n\t"
    "ds_read_b128 v[32:35], v8 offset:25600\n\t"
    "ds_read_b128 v[52:55], v6 offset:15360\n\t"
    "s_waitcnt lgkmcnt(8)\n\t"
    "v_smfmac_f32_16x16x64_bf16 v[12:15], v[40:43], v[160:167], v9\n\t"
    "v_smfmac_f32_16x16x64_bf16 v[16:19], v[40:43], v[168:175], v9\n\t"
    "s_waitcnt lgkmcnt(7)\n\t"
    "v_smfmac_f32_16x16x64_bf16 v[12:15], v[36:39], v[176:183], v9\n\t"
    "v_smfmac_f32_16x16x64_bf16 v[16:19], v[36:39], v[184:191], v9\n\t"
    "s_waitcnt lgkmcnt(6)\n\t"
    "v_smfmac_f32_16x16x64_bf16 v[12:15], v[44:47], v[224:231], v9\n\t"
    "v_smfmac_f32_16x16x64_bf16 v[16:19], v[44:47], v[232:239], v9\n\t"
    "s_waitcnt lgkmcnt(3)\n\t"
    "v_smfmac_f32_16x16x64_bf16 v[12:15], v[48:51], v[20:27], v9\n\t"
    "s_waitcnt lgkmcnt(1)\n\t"
    "v_smfmac_f32_16x16x64_bf16 v[16:19], v[48:51], v[28:35], v9\n\t"
    "s_waitcnt lgkmcnt(0)\n\t"
    "v_smfmac_f32_16x16x64_bf16 v[12:15], v[52:55], v[240:247], v9\n\t"
    "v_smfmac_f32_16x16x64_bf16 v[16:19], v[52:55], v[248:255], v9\n\t"
    "s_nop 6\n\t"
    "v_permlane16_swap_b32 v12, v14\n\t"
    "v_permlane16_swap_b32 v13, v15\n\t"
    "v_permlane16_swap_b32 v16, v18\n\t"
    "v_permlane16_swap_b32 v17, v19\n\t"
    "v_add_f32 v12, v12, v14\n\t"
    "v_add_f32 v13, v13, v15\n\t"
    "v_add_f32 v16, v16, v18\n\t"
    "v_cvt_pk_bf16_f32 v12, v12, v13\n\t"
    "v_add_f32 v17, v17, v19\n\t"
    "ds_write_b32 v10, v12 offset:35840\n\t"
    "v_cvt_pk_bf16_f32 v16, v16, v17\n\t"
    "ds_write_b32 v10, v16 offset:36096\n\t"
    "s_waitcnt lgkmcnt(0)\n\t"
    "s_setprio 0\n\t"
    "s_barrier\n\t"
    "s_barrier\n\t"
    "s_waitcnt lgkmcnt(0)\n\t"
    "s_branch 9f\n\t"
    "1:\n\t"
    "s_add_u32 %[sp], %[sp], 1\n\t"
    "s_cmp_ge_u32 %[sp], %[cap]\n\t"
    "s_cbranch_scc1 7f\n\t"
    "global_load_dwordx4 v[36:39], v6, %[ag] sc1\n\t"
    "global_load_dwordx4 v[40:43], v6, %[ag] offset:1024 sc1\n\t"
    "s_waitcnt lgkmcnt(0)\n\t"
    "s_memtime %[tc]\n\t"
    "s_nop 7\n\t"
    "s_branch 3b\n\t"
    "7:\n\t"
    "s_mov_b32 %[cap], 0\n\t"
    "s_branch 2b\n\t"
    "9:"
    : [tc] "=&s"(tc), [sp] "=&s"(sp), [cap] "+s"(cap)
    : [ag] "s"(ag), [c44] "s"(0x4444u), "{v[64:95]}"(W.w0), "{v[96:127]}"(W.w1), "{v[128:159]}"(W.w2), "{v[160:191]}"(W.w3), "{v[192:223]}"(W.w4), "{v[224:255]}"(W.w5), "{v[56:63]}"(W.x)
    : "v6", "v7", "v8", "v9", "v10", "v11", "v12", "v13", "v14", "v15", "v16", "v17", "v18", "v19", "v20", "v21", "v22", "v23", "v24", "v25", "v26", "v27", "v28", "v29", "v30", "v31", "v32", "v33", "v34", "v35", "v36", "v37", "v38", "v39", "v40", "v41", "v42", "v43", "v44", "v45", "v46", "v47", "v48", "v49", "v50", "v51", "v52", "v53", "v54", "v55", "vcc", "scc", "memory");
}
template <> __device__ __forceinline__ void qk_step<7>(const QKW& W, const char* ag, const char* vs, const char* od, const char* kv, unsigned& cap, unsigned& sp,
                                                        unsigned long long& tc, unsigned long long* ts, unsigned& d0, unsigned& d1) {
  unsigned st, bg;
  asm volatile("v_mbcnt_lo_u32_b32 v52, -1, 0\n\t"
    "v_mbcnt_hi_u32_b32 v52, -1, v52\n\t"
    "v_lshlrev_b32 v6, 4, v52\n\t"
    "s_mov_b32 %[sp], 0\n\t"
    "global_load_dwordx4 v[36:39], v6, %[ag] sc1\n\t"
    "global_load_dwordx4 v[40:43], v6, %[ag] offset:1024 sc1\n\t"
    "s_memtime %[tc]\n\t"
    "v_add_u32 v7, 79872, v6\n\t"
    "v_add_u32 v8, 137216, v6\n\t"
    "ds_read_b128 v[20:23], v7 offset:8192\n\t"
    "ds_read_b128 v[24:27], v7 offset:9216\n\t"
    "ds_read_b128 v[28:31], v7 offset:0\n\t"
    "ds_read_b128 v[32:35], v7 offset:1024\n\t"
    "v_lshrrev_b32 v44, 4, v52\n\t"
    "v_and_b32 v45, 15, v52\n\t"
    "v_and_b32 v53, 4, v52\n\t"
    "v_mul_u32_u24 v53, 0x2aaa, v53\n\t"
    "v_lshrrev_b32 v9, 1, v52\n\t"
    "v_and_b32 v9, 2, v9\n\t"
    "v_or3_b32 v9, v9, v53, %[c44]\n\t"
    "v_lshlrev_b32 v53, 2, v44\n\t"
    "v_lshl_or_b32 v10, v45, 4, v53\n\t"
    "3:\n\t"
    "v_mov_b32 v12, 0\n\t"
    "v_mov_b32 v13, 0\n\t"
    "v_mov_b32 v14, 0\n\t"
    "v_mov_b32 v15, 0\n\t"
    "v_mov_b32 v16, 0\n\t"
    "v_mov_b32 v17, 0\n\t"
    "v_mov_b32 v18, 0\n\t"
    "v_mov_b32 v19, 0\n\t"
    "s_waitcnt vmcnt(0) lgkmcnt(0)\n\t"
    "14:\n\t"
    "ds_write_b128 v6, v[36:39] offset:14336\n\t"
    "ds_write_b128 v6, v[40:43] offset:15360\n\t"
    "v_smfmac_f32_16x16x64_bf16 v[12:15], v[36:39], v[224:231], v9\n\t"
    "v_max3_u32 v52, v36, v37, v38\n\t"
    "v_max3_u32 v53, v39, v40, v41\n\t"
    "v_smfmac_f32_16x16x64_bf16 v[16:19], v[36:39], v[232:239], v9\n\t"
    "v_max3_u32 v52, v52, v42, v43\n\t"
    "v_max_u32 v52, v52, v53\n\t"
    "v_cmp_eq_u32 vcc, -1, v52\n\t"
    "s_cbranch_vccnz 1f\n\t"
    "2:\n\t"
    "s_waitcnt lgkmcnt(0)\n\t"
    "s_barrier\n\t"
    "ds_read_b128 v[36:39], v6 offset:0\n\t"
    "ds_read_b128 v[44:47], v6 offset:4096\n\t"
    "v_smfmac_f32_16x16x64_bf16 v[12:15], v[40:43], v[240:247], v9\n\t"
    "ds_read_b128 v[48:51], v6 offset:5120\n\t"
    "ds_read_b128 v[52:55], v6 offset:6144\n\t"
    "v_smfmac_f32_16x16x64_bf16 v[16:19], v[40:43], v[248:255], v9\n\t"
    "ds_read_b128 v[40:43], v6 offset:1024\n\t"
    "s_sleep 0\n\t"
    "s_waitcnt lgkmcnt(4)\n\t"
    "v_smfmac_f32_16x16x64_bf16 v[12:15], v[36:39], v[56:63], v9\n\t"
    "v_smfmac_f32_16x16x64_bf16 v[16:19], v[36:39], v[28:35], v9\n\t"
    "ds_read_b128 v[28:31], v7 offset:16384\n\t"
    "ds_read_b128 v[32:35], v7 offset:17408\n\t"
    "ds_read_b128 v[36:39], v6 offset:7168\n\t"
    "s_waitcnt lgkmcnt(6)\n\t"
    "v_smfmac_f32_16x16x64_bf16 v[12:15], v[44:47], v[64:71], v9\n\t"
    "v_smfmac_f32_16x16x64_bf16 v[16:19], v[44:47], v[72:79], v9\n\t"
    "ds_read_b128 v[44:47], v6 offset:8192\n\t"
    "s_waitcnt lgkmcnt(6)\n\t"
    "v_smfmac_f32_16x16x64_bf16 v[12:15], v[48:51], v[80:87], v9\n\t"
    "v_smfmac_f32_16x16x64_bf16 v[16:19], v[48:51], v[88:95], v9\n\t"
    "ds_read_b128 v[48:51], v6 offset:9216\n\t"
    "s_waitcnt lgkmcnt(6)\n\t"
    "v_smfmac_f32_16x16x64_bf16 v[12:15], v[52:55], v[96:103], v9\n\t"
    "v_smfmac_f32_16x16x64_bf16 v[16:19], v[52:55], v[104:111], v9\n\t"
    "ds_read_b128 v[52:55], v6 offset:2048\n\t"
    "s_waitcnt lgkmcnt(6)\n\t"
    "v_smfmac_f32_16x16x64_bf16 v[12:15], v[40:43], v[20:27], v9\n\t"
    "ds_read_b128 v[20:23], v8 offset:0\n\t"
    "ds_read_b128 v[24:27], v8 offset:1024\n\t"
    "s_waitcnt lgkmcnt(6)\n\t"
    "v_smfmac_f32_16x16x64_bf16 v[16:19], v[40:43], v[28:35], v9\n\t"
    "ds_read_b128 v[28:31], v8 offset:8192\n\t"
    "ds_read_b128 v[32:35], v8 offset:9216\n\t"
    "ds_read_b128 v[40:43], v6 offset:10240\n\t"
    "s_waitcnt lgkmcnt(8)\n\t"
    "v_smfmac_f32_16x16x64_bf16 v[12:15], v[36:39], v[112:119], v9\n\t"
    "v_smfmac_f32_16x16x64_bf16 v[16:19], v[36:39], v[120:127], v9\n\t"
    "ds_read_b128 v[36:39], v6 offset:11264\n\t"
    "s_waitcnt lgkmcnt(8)\n\t"
    "v_smfmac_f32_16x16x64_bf16 v[12:15], v[44:47], v[128:135], v9\n\t"
    "v_smfmac_f32_16x16x64_bf16 v[16:19], v[44:47], v[136:143], v9\n\t"
    "ds_read_b128 v[44:47], v6 offset:12288\n\t"
    "s_barrier\n\t"
    "s_setprio 2\n\t"
    "s_waitcnt lgkmcnt(8)\n\t"
    "v_smfmac_f32_16x16x64_bf16 v[12:15], v[48:51], v[144:151], v9\n\t"
    "v_smfmac_f32_16x16x64_bf16 v[16:19], v[48:51], v[152:159], v9\n\t"
    "ds_read_b128 v[48:51], v6 offset:3072\n\t"
    "s_waitcnt lgkmcnt(6)\n\t"
    "v_smfmac_f32_16x16x64_bf16 v[12:15], v[52:55], v[20:27], v9\n\t"
    "ds_read_b128 v[20:23], v8 offset:16384\n\t"
    "ds_read_b128 v[24:27], v8 offset:17408\n\t"
    "s_waitcnt lgkmcnt(6)\n\t"
    "v_smfmac_f32_16x16x64_bf16 v[16:19], v[52:55], v[28:35], v9\n\t"
    "ds_read_b128 v[28:31], v8 offset:24576\n\t"
    "ds_read_b128 v[32:35], v8 offset:25600\n\t"
    "ds_read_b128 v[52:55], v6 offset:13312\n\t"
    "s_waitcnt lgkmcnt(8)\n\t"
    "v_smfmac_f32_16x16x64_bf16 v[12:15], v[40:43], v[160:167], v9\n\t"
    "v_smfmac_f32_16x16x64_bf16 v[16:19], v[40:43], v[168:175], v9\n\t"
    "s_waitcnt lgkmcnt(7)\n\t"
    "v_smfmac_f32_16x16x64_bf16 v[12:15], v[36:39], v[176:183], v9\n\t"
    "v_smfmac_f32_16x16x64_bf16 v[16:19], v[36:39], v[184:191], v9\n\t"
    "s_waitcnt lgkmcnt(6)\n\t"
    "v_smfmac_f32_16x16x64_bf16 v[12:15], v[44:47], v[192:199], v9\n\t"
    "v_smfmac_f32_16x16x64_bf16 v[16:19], v[44:47], v[200:207], v9\n\t"
    "s_waitcnt lgkmcnt(3)\n\t"
    "v_smfmac_f32_16x16x64_bf16 v[12:15], v[48:51], v[20:27], v9\n\t"
    "s_waitcnt lgkmcnt(1)\n\t"
    "v_smfmac_f32_16x16x64_bf16 v[16:19], v[48:51], v[28:35], v9\n\t"
    "s_waitcnt lgkmcnt(0)\n\t"
    "v_smfmac_f32_16x16x64_bf16 v[12:15], v[52:55], v[208:215], v9\n\t"
    "v_smfmac_f32_16x16x64_bf16 v[16:19], v[52:55], v[216:223], v9\n\t"
    "s_nop 6\n\t"
    "v_permlane16_swap_b32 v12, v14\n\t"
    "v_permlane16_swap_b32 v13, v15\n\t"
    "v_permlane16_swap_b32 v16, v18\n\t"
    "v_permlane16_swap_b32 v17, v19\n\t"
    "v_add_f32 v12, v12, v14\n\t"
    "v_add_f32 v13, v13, v15\n\t"
    "v_add_f32 v16, v16, v18\n\t"
    "v_cvt_pk_bf16_f32 v12, v12, v13\n\t"
    "v_add_f32 v17, v17, v19\n\t"
    "ds_write_b32 v10, v12 offset:36352\n\t"
    "v_cvt_pk_bf16_f32 v16, v16, v17\n\t"
    "ds_write_b32 v10, v16 offset:36608\n\t"
    "s_waitcnt lgkmcnt(0)\n\t"
    "s_setprio 0\n\t"
    "s_barrier\n\t"
    "s_barrier\n\t"
    "s_waitcnt lgkmcnt(0)\n\t"
    "s_branch 9f\n\t"
    "1:\n\t"
    "s_add_u32 %[sp], %[sp], 1\n\t"
    "s_cmp_ge_u32 %[sp], %[cap]\n\t"
    "s_cbranch_scc1 7f\n\t"
    "global_load_dwordx4 v[36:39], v6, %[ag] sc1\n\t"
    "global_load_dwordx4 v[40:43], v6, %[ag] offset:1024 sc1\n\t"
    "s_waitcnt lgkmcnt(0)\n\t"
    "s_memtime %[tc]\n\t"
    "s_nop 7\n\t"
    "s_branch 3b\n\t"
    "7:\n\t"
    "s_mov_b32 %[cap], 0\n\t"
    "s_branch 2b\n\t"
    "9:"
    : [tc] "=&s"(tc), [sp] "=&s"(sp), [cap] "+s"(cap)
    : [ag] "s"(ag), [c44] "s"(0x4444u), "{v[64:95]}"(W.w0), "{v[96:127]}"(W.w1), "{v[128:159]}"(W.w2), "{v[160:191]}"(W.w3), "{v[192:223]}"(W.w4), "{v[224:255]}"(W.w5), "{v[56:63]}"(W.x)
    : "v6", "v7", "v8", "v9", "v10", "v11", "v12", "v13", "v14", "v15", "v16", "v17", "v18", "v19", "v20", "v21", "v22", "v23", "v24", "v25", "v26", "v27", "v28", "v29", "v30", "v31", "v32", "v33", "v34", "v35", "v36", "v37", "v38", "v39", "v40", "v41", "v42", "v43", "v44", "v45", "v46", "v47", "v48", "v49", "v50", "v51", "v52", "v53", "v54", "v55", "vcc", "scc", "memory");
}
template <int I> __device__ __forceinline__ void qk_wload(u32x32& w, unsigned a0, unsigned a1);
template <> __device__ __forceinline__ void qk_wload<0>(u32x32& w, unsigned a0, unsigned a1) {
  asm volatile("ds_read_b64_tr_b16 v[64:65], %1 offset:0\n\t"
    "ds_read_b64_tr_b16 v[66:67], %1 offset:512\n\t"
    "ds_read_b64_tr_b16 v[68:69], %1 offset:4096\n\t"
    "ds_read_b64_tr_b16 v[70:71], %1 offset:4608\n\t"
    "ds_read_b64_tr_b16 v[72:73], %2 offset:0\n\t"
    "ds_read_b64_tr_b16 v[74:75], %2 offset:512\n\t"
    "ds_read_b64_tr_b16 v[76:77], %2 offset:4096\n\t"
    "ds_read_b64_tr_b16 v[78:79], %2 offset:4608\n\t"
    "s_waitcnt lgkmcnt(0)\n\t"
    "ds_read_b64_tr_b16 v[80:81], %1 offset:16384\n\t"
    "ds_read_b64_tr_b16 v[82:83], %1 offset:16896\n\t"
    "ds_read_b64_tr_b16 v[84:85], %1 offset:20480\n\t"
    "ds_read_b64_tr_b16 v[86:87], %1 offset:20992\n\t"
    "ds_read_b64_tr_b16 v[88:89], %2 offset:16384\n\t"
    "ds_read_b64_tr_b16 v[90:91], %2 offset:16896\n\t"
    "ds_read_b64_tr_b16 v[92:93], %2 offset:20480\n\t"
    "ds_read_b64_tr_b16 v[94:95], %2 offset:20992\n\t"
    "s_waitcnt lgkmcnt(0)"
    : "=&{v[64:95]}"(w) : "v"(a0), "v"(a1) : "memory");
}
template <> __device__ __forceinline__ void qk_wload<1>(u32x32& w, unsigned a0, unsigned a1) {
  asm volatile("ds_read_b64_tr_b16 v[96:97], %1 offset:0\n\t"
    "ds_read_b64_tr_b16 v[98:99], %1 offset:512\n\t"
    "ds_read_b64_tr_b16 v[100:101], %1 offset:4096\n\t"
    "ds_read_b64_tr_b16 v[102:103], %1 offset:4608\n\t"
    "ds_read_b64_tr_b16 v[104:105], %2 offset:0\n\t"
    "ds_read_b64_tr_b16 v[106:107], %2 offset:512\n\t"
    "ds_read_b64_tr_b16 v[108:109], %2 offset:4096\n\t"
    "ds_read_b64_tr_b16 v[110:111], %2 offset:4608\n\t"
    "s_waitcnt lgkmcnt(0)\n\t"
    "ds_read_b64_tr_b16 v[112:113], %1 offset:16384\n\t"
    "ds_read_b64_tr_b16 v[114:115], %1 offset:16896\n\t"
    "ds_read_b64_tr_b16 v[116:117], %1 offset:20480\n\t"
    "ds_read_b64_tr_b16 v[118:119], %1 offset:20992\n\t"
    "ds_read_b64_tr_b16 v[120:121], %2 offset:16384\n\t"
    "ds_read_b64_tr_b16 v[122:123], %2 offset:16896\n\t"
    "ds_read_b64_tr_b16 v[124:125], %2 offset:20480\n\t"
    "ds_read_b64_tr_b16 v[126:127], %2 offset:20992\n\t"
    "s_waitcnt lgkmcnt(0)"
    : "=&{v[96:127]}"(w) : "v"(a0), "v"(a1) : "memory");
}
template <> __device__ __forceinline__ void qk_wload<2>(u32x32& w, unsigned a0, unsigned a1) {
  asm volatile("ds_read_b64_tr_b16 v[128:129], %1 offset:0\n\t"
    "ds_read_b64_tr_b16 v[130:131], %1 offset:512\n\t"
    "ds_read_b64_tr_b16 v[132:133], %1 offset:4096\n\t"
    "ds_read_b64_tr_b16 v[134:135], %1 offset:4608\n\t"
    "ds_read_b64_tr_b16 v[136:137], %2 offset:0\n\t"
    "ds_read_b64_tr_b16 v[138:139], %2 offset:512\n\t"
    "ds_read_b64_tr_b16 v[140:141], %2 offset:4096\n\t"
    "ds_read_b64_tr_b16 v[142:143], %2 offset:4608\n\t"
    "s_waitcnt lgkmcnt(0)\n\t"
    "ds_read_b64_tr_b16 v[144:145], %1 offset:16384\n\t"
    "ds_read_b64_tr_b16 v[146:147], %1 offset:16896\n\t"
    "ds_read_b64_tr_b16 v[148:149], %1 offset:20480\n\t"
    "ds_read_b64_tr_b16 v[150:151], %1 offset:20992\n\t"
    "ds_read_b64_tr_b16 v[152:153], %2 offset:16384\n\t"
    "ds_read_b64_tr_b16 v[154:155], %2 offset:16896\n\t"
    "ds_read_b64_tr_b16 v[156:157], %2 offset:20480\n\t"
    "ds_read_b64_tr_b16 v[158:159], %2 offset:20992\n\t"
    "s_waitcnt lgkmcnt(0)"
    : "=&{v[128:159]}"(w) : "v"(a0), "v"(a1) : "memory");
}
template <> __device__ __forceinline__ void qk_wload<3>(u32x32& w, unsigned a0, unsigned a1) {
  asm volatile("ds_read_b64_tr_b16 v[160:161], %1 offset:0\n\t"
    "ds_read_b64_tr_b16 v[162:163], %1 offset:512\n\t"
    "ds_read_b64_tr_b16 v[164:165], %1 offset:4096\n\t"
    "ds_read_b64_tr_b16 v[166:167], %1 offset:4608\n\t"
    "ds_read_b64_tr_b16 v[168:169], %2 offset:0\n\t"
    "ds_read_b64_tr_b16 v[170:171], %2 offset:512\n\t"
    "ds_read_b64_tr_b16 v[172:173], %2 offset:4096\n\t"
    "ds_read_b64_tr_b16 v[174:175], %2 offset:4608\n\t"
    "s_waitcnt lgkmcnt(0)\n\t"
    "ds_read_b64_tr_b16 v[176:177], %1 offset:16384\n\t"
    "ds_read_b64_tr_b16 v[178:179], %1 offset:16896\n\t"
    "ds_read_b64_tr_b16 v[180:181], %1 offset:20480\n\t"
    "ds_read_b64_tr_b16 v[182:183], %1 offset:20992\n\t"
    "ds_read_b64_tr_b16 v[184:185], %2 offset:16384\n\t"
    "ds_read_b64_tr_b16 v[186:187], %2 offset:16896\n\t"
    "ds_read_b64_tr_b16 v[188:189], %2 offset:20480\n\t"
    "ds_read_b64_tr_b16 v[190:191], %2 offset:20992\n\t"
    "s_waitcnt lgkmcnt(0)"
    : "=&{v[160:191]}"(w) : "v"(a0), "v"(a1) : "memory");
}
template <> __device__ __forceinline__ void qk_wload<4>(u32x32& w, unsigned a0, unsigned a1) {
  asm volatile("ds_read_b64_tr_b16 v[192:193], %1 offset:0\n\t"
    "ds_read_b64_tr_b16 v[194:195], %1 offset:512\n\t"
    "ds_read_b64_tr_b16 v[196:197], %1 offset:4096\n\t"
    "ds_read_b64_tr_b16 v[198:199], %1 offset:4608\n\t"
    "ds_read_b64_tr_b16 v[200:201], %2 offset:0\n\t"
    "ds_read_b64_tr_b16 v[202:203], %2 offset:512\n\t"
    "ds_read_b64_tr_b16 v[204:205], %2 offset:4096\n\t"
    "ds_read_b64_tr_b16 v[206:207], %2 offset:4608\n\t"
    "s_waitcnt lgkmcnt(0)\n\t"
    "ds_read_b64_tr_b16 v[208:209], %1 offset:16384\n\t"
    "ds_read_b64_tr_b16 v[210:211], %1 offset:16896\n\t"
    "ds_read_b64_tr_b16 v[212:213], %1 offset:20480\n\t"
    "ds_read_b64_tr_b16 v[214:215], %1 offset:20992\n\t"
    "ds_read_b64_tr_b16 v[216:217], %2 offset:16384\n\t"
    "ds_read_b64_tr_b16 v[218:219], %2 offset:16896\n\t"
    "ds_read_b64_tr_b16 v[220:221], %2 offset:20480\n\t"
    "ds_read_b64_tr_b16 v[222:223], %2 offset:20992\n\t"
    "s_waitcnt lgkmcnt(0)"
    : "=&{v[192:223]}"(w) : "v"(a0), "v"(a1) : "memory");
}
template <> __device__ __forceinline__ void qk_wload<5>(u32x32& w, unsigned a0, unsigned a1) {
  asm volatile("ds_read_b64_tr_b16 v[224:225], %1 offset:0\n\t"
    "ds_read_b64_tr_b16 v[226:227], %1 offset:512\n\t"
    "ds_read_b64_tr_b16 v[228:229], %1 offset:4096\n\t"
    "ds_read_b64_tr_b16 v[230:231], %1 offset:4608\n\t"
    "ds_read_b64_tr_b16 v[232:233], %2 offset:0\n\t"
    "ds_read_b64_tr_b16 v[234:235], %2 offset:512\n\t"
    "ds_read_b64_tr_b16 v[236:237], %2 offset:4096\n\t"
    "ds_read_b64_tr_b16 v[238:239], %2 offset:4608\n\t"
    "s_waitcnt lgkmcnt(0)\n\t"
    "ds_read_b64_tr_b16 v[240:241], %1 offset:16384\n\t"
    "ds_read_b64_tr_b16 v[242:243], %1 offset:16896\n\t"
    "ds_read_b64_tr_b16 v[244:245], %1 offset:20480\n\t"
    "ds_read_b64_tr_b16 v[246:247], %1 offset:20992\n\t"
    "ds_read_b64_tr_b16 v[248:249], %2 offset:16384\n\t"
    "ds_read_b64_tr_b16 v[250:251], %2 offset:16896\n\t"
    "ds_read_b64_tr_b16 v[252:253], %2 offset:20480\n\t"
    "ds_read_b64_tr_b16 v[254:255], %2 offset:20992\n\t"
    "s_waitcnt lgkmcnt(0)"
    : "=&{v[224:255]}"(w) : "v"(a0), "v"(a1) : "memory");
}
#define QK_OWN 0
template <int I> __device__ __forceinline__ void qk_swap(u32x32& w, unsigned a);
template <> __device__ __forceinline__ void qk_swap<0>(u32x32& w, unsigned a) {
  asm volatile("ds_read_b128 v[8:11], %1 offset:0\n\t"
    "ds_read_b128 v[12:15], %1 offset:1024\n\t"
    "ds_read_b128 v[16:19], %1 offset:8192\n\t"
    "ds_read_b128 v[20:23], %1 offset:9216\n\t"
    "ds_read_b128 v[24:27], %1 offset:16384\n\t"
    "ds_read_b128 v[28:31], %1 offset:17408\n\t"
    "ds_read_b128 v[32:35], %1 offset:24576\n\t"
    "ds_read_b128 v[36:39], %1 offset:25600\n\t"
    "ds_write_b128 %1, v[64:67] offset:0\n\t"
    "ds_write_b128 %1, v[68:71] offset:1024\n\t"
    "ds_write_b128 %1, v[72:75] offset:8192\n\t"
    "ds_write_b128 %1, v[76:79] offset:9216\n\t"
    "ds_write_b128 %1, v[80:83] offset:16384\n\t"
    "ds_write_b128 %1, v[84:87] offset:17408\n\t"
    "ds_write_b128 %1, v[88:91] offset:24576\n\t"
    "ds_write_b128 %1, v[92:95] offset:25600\n\t"
    "s_waitcnt lgkmcnt(0)\n\t"
    "v_mov_b32 v64, v8\n\t"
    "v_mov_b32 v65, v9\n\t"
    "v_mov_b32 v66, v10\n\t"
    "v_mov_b32 v67, v11\n\t"
    "v_mov_b32 v68, v12\n\t"
    "v_mov_b32 v69, v13\n\t"
    "v_mov_b32 v70, v14\n\t"
    "v_mov_b32 v71, v15\n\t"
    "v_mov_b32 v72, v16\n\t"
    "v_mov_b32 v73, v17\n\t"
    "v_mov_b32 v74, v18\n\t"
    "v_mov_b32 v75, v19\n\t"
    "v_mov_b32 v76, v20\n\t"
    "v_mov_b32 v77, v21\n\t"
    "v_mov_b32 v78, v22\n\t"
    "v_mov_b32 v79, v23\n\t"
    "v_mov_b32 v80, v24\n\t"
    "v_mov_b32 v81, v25\n\t"
    "v_mov_b32 v82, v26\n\t"
    "v_mov_b32 v83, v27\n\t"
    "v_mov_b32 v84, v28\n\t"
    "v_mov_b32 v85, v29\n\t"
    "v_mov_b32 v86, v30\n\t"
    "v_mov_b32 v87, v31\n\t"
    "v_mov_b32 v88, v32\n\t"
    "v_mov_b32 v89, v33\n\t"
    "v_mov_b32 v90, v34\n\t"
    "v_mov_b32 v91, v35\n\t"
    "v_mov_b32 v92, v36\n\t"
    "v_mov_b32 v93, v37\n\t"
    "v_mov_b32 v94, v38\n\t"
    "v_mov_b32 v95, v39"
    :: "{v[64:95]}"(w), "v"(a) : "v8", "v9", "v10", "v11", "v12", "v13", "v14", "v15", "v16", "v17", "v18", "v19", "v20", "v21", "v22", "v23", "v24", "v25", "v26", "v27", "v28", "v29", "v30", "v31", "v32", "v33", "v34", "v35", "v36", "v37", "v38", "v39", "memory");
}
template <> __device__ __forceinline__ void qk_swap<1>(u32x32& w, unsigned a) {
  asm volatile("ds_read_b128 v[8:11], %1 offset:0\n\t"
    "ds_read_b128 v[12:15], %1 offset:1024\n\t"
    "ds_read_b128 v[16:19], %1 offset:8192\n\t"
    "ds_read_b128 v[20:23], %1 offset:9216\n\t"
    "ds_read_b128 v[24:27], %1 offset:16384\n\t"
    "ds_read_b128 v[28:31], %1 offset:17408\n\t"
    "ds_read_b128 v[32:35], %1 offset:24576\n\t"
    "ds_read_b128 v[36:39], %1 offset:25600\n\t"
    "ds_write_b128 %1, v[96:99] offset:0\n\t"
    "ds_write_b128 %1, v[100:103] offset:1024\n\t"
    "ds_write_b128 %1, v[104:107] offset:8192\n\t"
    "ds_write_b128 %1, v[108:111] offset:9216\n\t"
    "ds_write_b128 %1, v[112:115] offset:16384\n\t"
    "ds_write_b128 %1, v[116:119] offset:17408\n\t"
    "ds_write_b128 %1, v[120:123] offset:24576\n\t"
    "ds_write_b128 %1, v[124:127] offset:25600\n\t"
    "s_waitcnt lgkmcnt(0)\n\t"
    "v_mov_b32 v96, v8\n\t"
    "v_mov_b32 v97, v9\n\t"
    "v_mov_b32 v98, v10\n\t"
    "v_mov_b32 v99, v11\n\t"
    "v_mov_b32 v100, v12\n\t"
    "v_mov_b32 v101, v13\n\t"
    "v_mov_b32 v102, v14\n\t"
    "v_mov_b32 v103, v15\n\t"
    "v_mov_b32 v104, v16\n\t"
    "v_mov_b32 v105, v17\n\t"
    "v_mov_b32 v106, v18\n\t"
    "v_mov_b32 v107, v19\n\t"
    "v_mov_b32 v108, v20\n\t"
    "v_mov_b32 v109, v21\n\t"
    "v_mov_b32 v110, v22\n\t"
    "v_mov_b32 v111, v23\n\t"
    "v_mov_b32 v112, v24\n\t"
    "v_mov_b32 v113, v25\n\t"
    "v_mov_b32 v114, v26\n\t"
    "v_mov_b32 v115, v27\n\t"
    "v_mov_b32 v116, v28\n\t"
    "v_mov_b32 v117, v29\n\t"
    "v_mov_b32 v118, v30\n\t"
    "v_mov_b32 v119, v31\n\t"
    "v_mov_b32 v120, v32\n\t"
    "v_mov_b32 v121, v33\n\t"
    "v_mov_b32 v122, v34\n\t"
    "v_mov_b32 v123, v35\n\t"
    "v_mov_b32 v124, v36\n\t"
    "v_mov_b32 v125, v37\n\t"
    "v_mov_b32 v126, v38\n\t"
    "v_mov_b32 v127, v39"
    :: "{v[96:127]}"(w), "v"(a) : "v8", "v9", "v10", "v11", "v12", "v13", "v14", "v15", "v16", "v17", "v18", "v19", "v20", "v21", "v22", "v23", "v24", "v25", "v26", "v27", "v28", "v29", "v30", "v31", "v32", "v33", "v34", "v35", "v36", "v37", "v38", "v39", "memory");
}
template <> __device__ __forceinline__ void qk_swap<2>(u32x32& w, unsigned a) {
  asm volatile("ds_read_b128 v[8:11], %1 offset:0\n\t"
    "ds_read_b128 v[12:15], %1 offset:1024\n\t"
    "ds_read_b128 v[16:19], %1 offset:8192\n\t"
    "ds_read_b128 v[20:23], %1 offset:9216\n\t"
    "ds_read_b128 v[24:27], %1 offset:16384\n\t"
    "ds_read_b128 v[28:31], %1 offset:17408\n\t"
    "ds_read_b128 v[32:35], %1 offset:24576\n\t"
    "ds_read_b128 v[36:39], %1 offset:25600\n\t"
    "ds_write_b128 %1, v[128:131] offset:0\n\t"
    "ds_write_b128 %1, v[132:135] offset:1024\n\t"
    "ds_write_b128 %1, v[136:139] offset:8192\n\t"
    "ds_write_b128 %1, v[140:143] offset:9216\n\t"
    "ds_write_b128 %1, v[144:147] offset:16384\n\t"
    "ds_write_b128 %1, v[148:151] offset:17408\n\t"
    "ds_write_b128 %1, v[152:155] offset:24576\n\t"
    "ds_write_b128 %1, v[156:159] offset:25600\n\t"
    "s_waitcnt lgkmcnt(0)\n\t"
    "v_mov_b32 v128, v8\n\t"
    "v_mov_b32 v129, v9\n\t"
    "v_mov_b32 v130, v10\n\t"
    "v_mov_b32 v131, v11\n\t"
    "v_mov_b32 v132, v12\n\t"
    "v_mov_b32 v133, v13\n\t"
    "v_mov_b32 v134, v14\n\t"
    "v_mov_b32 v135, v15\n\t"
    "v_mov_b32 v136, v16\n\t"
    "v_mov_b32 v137, v17\n\t"
    "v_mov_b32 v138, v18\n\t"
    "v_mov_b32 v139, v19\n\t"
    "v_mov_b32 v140, v20\n\t"
    "v_mov_b32 v141, v21\n\t"
    "v_mov_b32 v142, v22\n\t"
    "v_mov_b32 v143, v23\n\t"
    "v_mov_b32 v144, v24\n\t"
    "v_mov_b32 v145, v25\n\t"
    "v_mov_b32 v146, v26\n\t"
    "v_mov_b32 v147, v27\n\t"
    "v_mov_b32 v148, v28\n\t"
    "v_mov_b32 v149, v29\n\t"
    "v_mov_b32 v150, v30\n\t"
    "v_mov_b32 v151, v31\n\t"
    "v_mov_b32 v152, v32\n\t"
    "v_mov_b32 v153, v33\n\t"
    "v_mov_b32 v154, v34\n\t"
    "v_mov_b32 v155, v35\n\t"
    "v_mov_b32 v156, v36\n\t"
    "v_mov_b32 v157, v37\n\t"
    "v_mov_b32 v158, v38\n\t"
    "v_mov_b32 v159, v39"
    :: "{v[128:159]}"(w), "v"(a) : "v8", "v9", "v10", "v11", "v12", "v13", "v14", "v15", "v16", "v17", "v18", "v19", "v20", "v21", "v22", "v23", "v24", "v25", "v26", "v27", "v28", "v29", "v30", "v31", "v32", "v33", "v34", "v35", "v36", "v37", "v38", "v39", "memory");
}
template <> __device__ __forceinline__ void qk_swap<3>(u32x32& w, unsigned a) {
  asm volatile("ds_read_b128 v[8:11], %1 offset:0\n\t"
    "ds_read_b128 v[12:15], %1 offset:1024\n\t"
    "ds_read_b128 v[16:19], %1 offset:8192\n\t"
    "ds_read_b128 v[20:23], %1 offset:9216\n\t"
    "ds_read_b128 v[24:27], %1 offset:16384\n\t"
    "ds_read_b128 v[28:31], %1 offset:17408\n\t"
    "ds_read_b128 v[32:35], %1 offset:24576\n\t"
    "ds_read_b128 v[36:39], %1 offset:25600\n\t"
    "ds_write_b128 %1, v[160:163] offset:0\n\t"
    "ds_write_b128 %1, v[164:167] offset:1024\n\t"
    "ds_write_b128 %1, v[168:171] offset:8192\n\t"
    "ds_write_b128 %1, v[172:175] offset:9216\n\t"
    "ds_write_b128 %1, v[176:179] offset:16384\n\t"
    "ds_write_b128 %1, v[180:183] offset:17408\n\t"
    "ds_write_b128 %1, v[184:187] offset:24576\n\t"
    "ds_write_b128 %1, v[188:191] offset:25600\n\t"
    "s_waitcnt lgkmcnt(0)\n\t"
    "v_mov_b32 v160, v8\n\t"
    "v_mov_b32 v161, v9\n\t"
    "v_mov_b32 v162, v10\n\t"
    "v_mov_b32 v163, v11\n\t"
    "v_mov_b32 v164, v12\n\t"
    "v_mov_b32 v165, v13\n\t"
    "v_mov_b32 v166, v14\n\t"
    "v_mov_b32 v167, v15\n\t"
    "v_mov_b32 v168, v16\n\t"
    "v_mov_b32 v169, v17\n\t"
    "v_mov_b32 v170, v18\n\t"
    "v_mov_b32 v171, v19\n\t"
    "v_mov_b32 v172, v20\n\t"
    "v_mov_b32 v173, v21\n\t"
    "v_mov_b32 v174, v22\n\t"
    "v_mov_b32 v175, v23\n\t"
    "v_mov_b32 v176, v24\n\t"
    "v_mov_b32 v177, v25\n\t"
    "v_mov_b32 v178, v26\n\t"
    "v_mov_b32 v179, v27\n\t"
    "v_mov_b32 v180, v28\n\t"
    "v_mov_b32 v181, v29\n\t"
    "v_mov_b32 v182, v30\n\t"
    "v_mov_b32 v183, v31\n\t"
    "v_mov_b32 v184, v32\n\t"
    "v_mov_b32 v185, v33\n\t"
    "v_mov_b32 v186, v34\n\t"
    "v_mov_b32 v187, v35\n\t"
    "v_mov_b32 v188, v36\n\t"
    "v_mov_b32 v189, v37\n\t"
    "v_mov_b32 v190, v38\n\t"
    "v_mov_b32 v191, v39"
    :: "{v[160:191]}"(w), "v"(a) : "v8", "v9", "v10", "v11", "v12", "v13", "v14", "v15", "v16", "v17", "v18", "v19", "v20", "v21", "v22", "v23", "v24", "v25", "v26", "v27", "v28", "v29", "v30", "v31", "v32", "v33", "v34", "v35", "v36", "v37", "v38", "v39", "memory");
}
template <> __device__ __forceinline__ void qk_swap<4>(u32x32& w, unsigned a) {
  asm volatile("ds_read_b128 v[8:11], %1 offset:0\n\t"
    "ds_read_b128 v[12:15], %1 offset:1024\n\t"
    "ds_read_b128 v[16:19], %1 offset:8192\n\t"
    "ds_read_b128 v[20:23], %1 offset:9216\n\t"
    "ds_read_b128 v[24:27], %1 offset:16384\n\t"
    "ds_read_b128 v[28:31], %1 offset:17408\n\t"
    "ds_read_b128 v[32:35], %1 offset:24576\n\t"
    "ds_read_b128 v[36:39], %1 offset:25600\n\t"
    "ds_write_b128 %1, v[192:195] offset:0\n\t"
    "ds_write_b128 %1, v[196:199] offset:1024\n\t"
    "ds_write_b128 %1, v[200:203] offset:8192\n\t"
    "ds_write_b128 %1, v[204:207] offset:9216\n\t"
    "ds_write_b128 %1, v[208:211] offset:16384\n\t"
    "ds_write_b128 %1, v[212:215] offset:17408\n\t"
    "ds_write_b128 %1, v[216:219] offset:24576\n\t"
    "ds_write_b128 %1, v[220:223] offset:25600\n\t"
    "s_waitcnt lgkmcnt(0)\n\t"
    "v_mov_b32 v192, v8\n\t"
    "v_mov_b32 v193, v9\n\t"
    "v_mov_b32 v194, v10\n\t"
    "v_mov_b32 v195, v11\n\t"
    "v_mov_b32 v196, v12\n\t"
    "v_mov_b32 v197, v13\n\t"
    "v_mov_b32 v198, v14\n\t"
    "v_mov_b32 v199, v15\n\t"
    "v_mov_b32 v200, v16\n\t"
    "v_mov_b32 v201, v17\n\t"
    "v_mov_b32 v202, v18\n\t"
    "v_mov_b32 v203, v19\n\t"
    "v_mov_b32 v204, v20\n\t"
    "v_mov_b32 v205, v21\n\t"
    "v_mov_b32 v206, v22\n\t"
    "v_mov_b32 v207, v23\n\t"
    "v_mov_b32 v208, v24\n\t"
    "v_mov_b32 v209, v25\n\t"
    "v_mov_b32 v210, v26\n\t"
    "v_mov_b32 v211, v27\n\t"
    "v_mov_b32 v212, v28\n\t"
    "v_mov_b32 v213, v29\n\t"
    "v_mov_b32 v214, v30\n\t"
    "v_mov_b32 v215, v31\n\t"
    "v_mov_b32 v216, v32\n\t"
    "v_mov_b32 v217, v33\n\t"
    "v_mov_b32 v218, v34\n\t"
    "v_mov_b32 v219, v35\n\t"
    "v_mov_b32 v220, v36\n\t"
    "v_mov_b32 v221, v37\n\t"
    "v_mov_b32 v222, v38\n\t"
    "v_mov_b32 v223, v39"
    :: "{v[192:223]}"(w), "v"(a) : "v8", "v9", "v10", "v11", "v12", "v13", "v14", "v15", "v16", "v17", "v18", "v19", "v20", "v21", "v22", "v23", "v24", "v25", "v26", "v27", "v28", "v29", "v30", "v31", "v32", "v33", "v34", "v35", "v36", "v37", "v38", "v39", "memory");
}
template <> __device__ __forceinline__ void qk_swap<5>(u32x32& w, unsigned a) {
  asm volatile("ds_read_b128 v[8:11], %1 offset:0\n\t"
    "ds_read_b128 v[12:15], %1 offset:1024\n\t"
    "ds_read_b128 v[16:19], %1 offset:8192\n\t"
    "ds_read_b128 v[20:23], %1 offset:9216\n\t"
    "ds_read_b128 v[24:27], %1 offset:16384\n\t"
    "ds_read_b128 v[28:31], %1 offset:17408\n\t"
    "ds_read_b128 v[32:35], %1 offset:24576\n\t"
    "ds_read_b128 v[36:39], %1 offset:25600\n\t"
    "ds_write_b128 %1, v[224:227] offset:0\n\t"
    "ds_write_b128 %1, v[228:231] offset:1024\n\t"
    "ds_write_b128 %1, v[232:235] offset:8192\n\t"
    "ds_write_b128 %1, v[236:239] offset:9216\n\t"
    "ds_write_b128 %1, v[240:243] offset:16384\n\t"
    "ds_write_b128 %1, v[244:247] offset:17408\n\t"
    "ds_write_b128 %1, v[248:251] offset:24576\n\t"
    "ds_write_b128 %1, v[252:255] offset:25600\n\t"
    "s_waitcnt lgkmcnt(0)\n\t"
    "v_mov_b32 v224, v8\n\t"
    "v_mov_b32 v225, v9\n\t"
    "v_mov_b32 v226, v10\n\t"
    "v_mov_b32 v227, v11\n\t"
    "v_mov_b32 v228, v12\n\t"
    "v_mov_b32 v229, v13\n\t"
    "v_mov_b32 v230, v14\n\t"
    "v_mov_b32 v231, v15\n\t"
    "v_mov_b32 v232, v16\n\t"
    "v_mov_b32 v233, v17\n\t"
    "v_mov_b32 v234, v18\n\t"
    "v_mov_b32 v235, v19\n\t"
    "v_mov_b32 v236, v20\n\t"
    "v_mov_b32 v237, v21\n\t"
    "v_mov_b32 v238, v22\n\t"
    "v_mov_b32 v239, v23\n\t"
    "v_mov_b32 v240, v24\n\t"
    "v_mov_b32 v241, v25\n\t"
    "v_mov_b32 v242, v26\n\t"
    "v_mov_b32 v243, v27\n\t"
    "v_mov_b32 v244, v28\n\t"
    "v_mov_b32 v245, v29\n\t"
    "v_mov_b32 v246, v30\n\t"
    "v_mov_b32 v247, v31\n\t"
    "v_mov_b32 v248, v32\n\t"
    "v_mov_b32 v249, v33\n\t"
    "v_mov_b32 v250, v34\n\t"
    "v_mov_b32 v251, v35\n\t"
    "v_mov_b32 v252, v36\n\t"
    "v_mov_b32 v253, v37\n\t"
    "v_mov_b32 v254, v38\n\t"
    "v_mov_b32 v255, v39"
    :: "{v[224:255]}"(w), "v"(a) : "v8", "v9", "v10", "v11", "v12", "v13", "v14", "v15", "v16", "v17", "v18", "v19", "v20", "v21", "v22", "v23", "v24", "v25", "v26", "v27", "v28", "v29", "v30", "v31", "v32", "v33", "v34", "v35", "v36", "v37", "v38", "v39", "memory");
}
template <int I> __device__ __forceinline__ void qk_swapk(u32x32& w, u32x8& x, unsigned a);
template <> __device__ __forceinline__ void qk_swapk<2>(u32x32& w, u32x8& x, unsigned a) {
  asm volatile("ds_read_b128 v[16:19], %2 offset:0\n\t"
    "ds_read_b128 v[20:23], %2 offset:1024\n\t"
    "ds_read_b128 v[24:27], %2 offset:8192\n\t"
    "ds_read_b128 v[28:31], %2 offset:9216\n\t"
    "ds_read_b128 v[32:35], %2 offset:16384\n\t"
    "ds_read_b128 v[36:39], %2 offset:17408\n\t"
    "ds_write_b128 %2, v[136:139] offset:0\n\t"
    "ds_write_b128 %2, v[140:143] offset:1024\n\t"
    "ds_write_b128 %2, v[144:147] offset:8192\n\t"
    "ds_write_b128 %2, v[148:151] offset:9216\n\t"
    "ds_write_b128 %2, v[152:155] offset:16384\n\t"
    "ds_write_b128 %2, v[156:159] offset:17408\n\t"
    "v_swap_b32 v128, v56\n\t"
    "v_swap_b32 v129, v57\n\t"
    "v_swap_b32 v130, v58\n\t"
    "v_swap_b32 v131, v59\n\t"
    "v_swap_b32 v132, v60\n\t"
    "v_swap_b32 v133, v61\n\t"
    "v_swap_b32 v134, v62\n\t"
    "v_swap_b32 v135, v63\n\t"
    "s_waitcnt lgkmcnt(0)\n\t"
    "v_mov_b32 v136, v16\n\t"
    "v_mov_b32 v137, v17\n\t"
    "v_mov_b32 v138, v18\n\t"
    "v_mov_b32 v139, v19\n\t"
    "v_mov_b32 v140, v20\n\t"
    "v_mov_b32 v141, v21\n\t"
    "v_mov_b32 v142, v22\n\t"
    "v_mov_b32 v143, v23\n\t"
    "v_mov_b32 v144, v24\n\t"
    "v_mov_b32 v145, v25\n\t"
    "v_mov_b32 v146, v26\n\t"
    "v_mov_b32 v147, v27\n\t"
    "v_mov_b32 v148, v28\n\t"
    "v_mov_b32 v149, v29\n\t"
    "v_mov_b32 v150, v30\n\t"
    "v_mov_b32 v151, v31\n\t"
    "v_mov_b32 v152, v32\n\t"
    "v_mov_b32 v153, v33\n\t"
    "v_mov_b32 v154, v34\n\t"
    "v_mov_b32 v155, v35\n\t"
    "v_mov_b32 v156, v36\n\t"
    "v_mov_b32 v157, v37\n\t"
    "v_mov_b32 v158, v38\n\t"
    "v_mov_b32 v159, v39"
    :: "{v[128:159]}"(w), "{v[56:63]}"(x), "v"(a) : "v16", "v17", "v18", "v19", "v20", "v21", "v22", "v23", "v24", "v25", "v26", "v27", "v28", "v29", "v30", "v31", "v32", "v33", "v34", "v35", "v36", "v37", "v38", "v39", "memory");
}
template <> __device__ __forceinline__ void qk_swapk<3>(u32x32& w, u32x8& x, unsigned a) {
  asm volatile("ds_read_b128 v[16:19], %2 offset:0\n\t"
    "ds_read_b128 v[20:23], %2 offset:1024\n\t"
    "ds_read_b128 v[24:27], %2 offset:8192\n\t"
    "ds_read_b128 v[28:31], %2 offset:9216\n\t"
    "ds_read_b128 v[32:35], %2 offset:16384\n\t"
    "ds_read_b128 v[36:39], %2 offset:17408\n\t"
    "ds_write_b128 %2, v[168:171] offset:0\n\t"
    "ds_write_b128 %2, v[172:175] offset:1024\n\t"
    "ds_write_b128 %2, v[176:179] offset:8192\n\t"
    "ds_write_b128 %2, v[180:183] offset:9216\n\t"
    "ds_write_b128 %2, v[184:187] offset:16384\n\t"
    "ds_write_b128 %2, v[188:191] offset:17408\n\t"
    "v_swap_b32 v160, v56\n\t"
    "v_swap_b32 v161, v57\n\t"
    "v_swap_b32 v162, v58\n\t"
    "v_swap_b32 v163, v59\n\t"
    "v_swap_b32 v164, v60\n\t"
    "v_swap_b32 v165, v61\n\t"
    "v_swap_b32 v166, v62\n\t"
    "v_swap_b32 v167, v63\n\t"
    "s_waitcnt lgkmcnt(0)\n\t"
    "v_mov_b32 v168, v16\n\t"
    "v_mov_b32 v169, v17\n\t"
    "v_mov_b32 v170, v18\n\t"
    "v_mov_b32 v171, v19\n\t"
    "v_mov_b32 v172, v20\n\t"
    "v_mov_b32 v173, v21\n\t"
    "v_mov_b32 v174, v22\n\t"
    "v_mov_b32 v175, v23\n\t"
    "v_mov_b32 v176, v24\n\t"
    "v_mov_b32 v177, v25\n\t"
    "v_mov_b32 v178, v26\n\t"
    "v_mov_b32 v179, v27\n\t"
    "v_mov_b32 v180, v28\n\t"
    "v_mov_b32 v181, v29\n\t"
    "v_mov_b32 v182, v30\n\t"
    "v_mov_b32 v183, v31\n\t"
    "v_mov_b32 v184, v32\n\t"
    "v_mov_b32 v185, v33\n\t"
    "v_mov_b32 v186, v34\n\t"
    "v_mov_b32 v187, v35\n\t"
    "v_mov_b32 v188, v36\n\t"
    "v_mov_b32 v189, v37\n\t"
    "v_mov_b32 v190, v38\n\t"
    "v_mov_b32 v191, v39"
    :: "{v[160:191]}"(w), "{v[56:63]}"(x), "v"(a) : "v16", "v17", "v18", "v19", "v20", "v21", "v22", "v23", "v24", "v25", "v26", "v27", "v28", "v29", "v30", "v31", "v32", "v33", "v34", "v35", "v36", "v37", "v38", "v39", "memory");
}
template <> __device__ __forceinline__ void qk_swapk<4>(u32x32& w, u32x8& x, unsigned a) {
  asm volatile("ds_read_b128 v[16:19], %2 offset:0\n\t"
    "ds_read_b128 v[20:23], %2 offset:1024\n\t"
    "ds_read_b128 v[24:27], %2 offset:8192\n\t"
    "ds_read_b128 v[28:31], %2 offset:9216\n\t"
    "ds_read_b128 v[32:35], %2 offset:16384\n\t"
    "ds_read_b128 v[36:39], %2 offset:17408\n\t"
    "ds_write_b128 %2, v[200:203] offset:0\n\t"
    "ds_write_b128 %2, v[204:207] offset:1024\n\t"
    "ds_write_b128 %2, v[208:211] offset:8192\n\t"
    "ds_write_b128 %2, v[212:215] offset:9216\n\t"
    "ds_write_b128 %2, v[216:219] offset:16384\n\t"
    "ds_write_b128 %2, v[220:223] offset:17408\n\t"
    "v_swap_b32 v192, v56\n\t"
    "v_swap_b32 v193, v57\n\t"
    "v_swap_b32 v194, v58\n\t"
    "v_swap_b32 v195, v59\n\t"
    "v_swap_b32 v196, v60\n\t"
    "v_swap_b32 v197, v61\n\t"
    "v_swap_b32 v198, v62\n\t"
    "v_swap_b32 v199, v63\n\t"
    "s_waitcnt lgkmcnt(0)\n\t"
    "v_mov_b32 v200, v16\n\t"
    "v_mov_b32 v201, v17\n\t"
    "v_mov_b32 v202, v18\n\t"
    "v_mov_b32 v203, v19\n\t"
    "v_mov_b32 v204, v20\n\t"
    "v_mov_b32 v205, v21\n\t"
    "v_mov_b32 v206, v22\n\t"
    "v_mov_b32 v207, v23\n\t"
    "v_mov_b32 v208, v24\n\t"
    "v_mov_b32 v209, v25\n\t"
    "v_mov_b32 v210, v26\n\t"
    "v_mov_b32 v211, v27\n\t"
    "v_mov_b32 v212, v28\n\t"
    "v_mov_b32 v213, v29\n\t"
    "v_mov_b32 v214, v30\n\t"
    "v_mov_b32 v215, v31\n\t"
    "v_mov_b32 v216, v32\n\t"
    "v_mov_b32 v217, v33\n\t"
    "v_mov_b32 v218, v34\n\t"
    "v_mov_b32 v219, v35\n\t"
    "v_mov_b32 v220, v36\n\t"
    "v_mov_b32 v221, v37\n\t"
    "v_mov_b32 v222, v38\n\t"
    "v_mov_b32 v223, v39"
    :: "{v[192:223]}"(w), "{v[56:63]}"(x), "v"(a) : "v16", "v17", "v18", "v19", "v20", "v21", "v22", "v23", "v24", "v25", "v26", "v27", "v28", "v29", "v30", "v31", "v32", "v33", "v34", "v35", "v36", "v37", "v38", "v39", "memory");
}
template <> __device__ __forceinline__ void qk_swapk<5>(u32x32& w, u32x8& x, unsigned a) {
  asm volatile("ds_read_b128 v[16:19], %2 offset:0\n\t"
    "ds_read_b128 v[20:23], %2 offset:1024\n\t"
    "ds_read_b128 v[24:27], %2 offset:8192\n\t"
    "ds_read_b128 v[28:31], %2 offset:9216\n\t"
    "ds_read_b128 v[32:35], %2 offset:16384\n\t"
    "ds_read_b128 v[36:39], %2 offset:17408\n\t"
    "ds_write_b128 %2, v[232:235] offset:0\n\t"
    "ds_write_b128 %2, v[236:239] offset:1024\n\t"
    "ds_write_b128 %2, v[240:243] offset:8192\n\t"
    "ds_write_b128 %2, v[244:247] offset:9216\n\t"
    "ds_write_b128 %2, v[248:251] offset:16384\n\t"
    "ds_write_b128 %2, v[252:255] offset:17408\n\t"
    "v_swap_b32 v224, v56\n\t"
    "v_swap_b32 v225, v57\n\t"
    "v_swap_b32 v226, v58\n\t"
    "v_swap_b32 v227, v59\n\t"
    "v_swap_b32 v228, v60\n\t"
    "v_swap_b32 v229, v61\n\t"
    "v_swap_b32 v230, v62\n\t"
    "v_swap_b32 v231, v63\n\t"
    "s_waitcnt lgkmcnt(0)\n\t"
    "v_mov_b32 v232, v16\n\t"
    "v_mov_b32 v233, v17\n\t"
    "v_mov_b32 v234, v18\n\t"
    "v_mov_b32 v235, v19\n\t"
    "v_mov_b32 v236, v20\n\t"
    "v_mov_b32 v237, v21\n\t"
    "v_mov_b32 v238, v22\n\t"
    "v_mov_b32 v239, v23\n\t"
    "v_mov_b32 v240, v24\n\t"
    "v_mov_b32 v241, v25\n\t"
    "v_mov_b32 v242, v26\n\t"
    "v_mov_b32 v243, v27\n\t"
    "v_mov_b32 v244, v28\n\t"
    "v_mov_b32 v245, v29\n\t"
    "v_mov_b32 v246, v30\n\t"
    "v_mov_b32 v247, v31\n\t"
    "v_mov_b32 v248, v32\n\t"
    "v_mov_b32 v249, v33\n\t"
    "v_mov_b32 v250, v34\n\t"
    "v_mov_b32 v251, v35\n\t"
    "v_mov_b32 v252, v36\n\t"
    "v_mov_b32 v253, v37\n\t"
    "v_mov_b32 v254, v38\n\t"
    "v_mov_b32 v255, v39"
    :: "{v[224:255]}"(w), "{v[56:63]}"(x), "v"(a) : "v16", "v17", "v18", "v19", "v20", "v21", "v22", "v23", "v24", "v25", "v26", "v27", "v28", "v29", "v30", "v31", "v32", "v33", "v34", "v35", "v36", "v37", "v38", "v39", "memory");
}
template <int I> __device__ __forceinline__ void vr_wload(u32x32& w, unsigned a);
template <> __device__ __forceinline__ void vr_wload<0>(u32x32& w, unsigned a) {
  asm volatile("ds_read_b64_tr_b16 v[64:65], %1 offset:0\n\t"
    "ds_read_b64_tr_b16 v[66:67], %1 offset:256\n\t"
    "ds_read_b64_tr_b16 v[68:69], %1 offset:2048\n\t"
    "ds_read_b64_tr_b16 v[70:71], %1 offset:2304\n\t"
    "ds_read_b64_tr_b16 v[72:73], %1 offset:32\n\t"
    "ds_read_b64_tr_b16 v[74:75], %1 offset:288\n\t"
    "ds_read_b64_tr_b16 v[76:77], %1 offset:2080\n\t"
    "ds_read_b64_tr_b16 v[78:79], %1 offset:2336\n\t"
    "s_waitcnt lgkmcnt(0)\n\t"
    "ds_read_b64_tr_b16 v[80:81], %1 offset:64\n\t"
    "ds_read_b64_tr_b16 v[82:83], %1 offset:320\n\t"
    "ds_read_b64_tr_b16 v[84:85], %1 offset:2112\n\t"
    "ds_read_b64_tr_b16 v[86:87], %1 offset:2368\n\t"
    "ds_read_b64_tr_b16 v[88:89], %1 offset:96\n\t"
    "ds_read_b64_tr_b16 v[90:91], %1 offset:352\n\t"
    "ds_read_b64_tr_b16 v[92:93], %1 offset:2144\n\t"
    "ds_read_b64_tr_b16 v[94:95], %1 offset:2400\n\t"
    "s_waitcnt lgkmcnt(0)"
    : "=&{v[64:95]}"(w) : "v"(a) : "memory");
}
template <> __device__ __forceinline__ void vr_wload<1>(u32x32& w, unsigned a) {
  asm volatile("ds_read_b64_tr_b16 v[96:97], %1 offset:8192\n\t"
    "ds_read_b64_tr_b16 v[98:99], %1 offset:8448\n\t"
    "ds_read_b64_tr_b16 v[100:101], %1 offset:10240\n\t"
    "ds_read_b64_tr_b16 v[102:103], %1 offset:10496\n\t"
    "ds_read_b64_tr_b16 v[104:105], %1 offset:8224\n\t"
    "ds_read_b64_tr_b16 v[106:107], %1 offset:8480\n\t"
    "ds_read_b64_tr_b16 v[108:109], %1 offset:10272\n\t"
    "ds_read_b64_tr_b16 v[110:111], %1 offset:10528\n\t"
    "s_waitcnt lgkmcnt(0)\n\t"
    "ds_read_b64_tr_b16 v[112:113], %1 offset:8256\n\t"
    "ds_read_b64_tr_b16 v[114:115], %1 offset:8512\n\t"
    "ds_read_b64_tr_b16 v[116:117], %1 offset:10304\n\t"
    "ds_read_b64_tr_b16 v[118:119], %1 offset:10560\n\t"
    "ds_read_b64_tr_b16 v[120:121], %1 offset:8288\n\t"
    "ds_read_b64_tr_b16 v[122:123], %1 offset:8544\n\t"
    "ds_read_b64_tr_b16 v[124:125], %1 offset:10336\n\t"
    "ds_read_b64_tr_b16 v[126:127], %1 offset:10592\n\t"
    "s_waitcnt lgkmcnt(0)"
    : "=&{v[96:127]}"(w) : "v"(a) : "memory");
}
template <> __device__ __forceinline__ void vr_wload<2>(u32x32& w, unsigned a) {
  asm volatile("ds_read_b64_tr_b16 v[128:129], %1 offset:0\n\t"
    "ds_read_b64_tr_b16 v[130:131], %1 offset:128\n\t"
    "ds_read_b64_tr_b16 v[132:133], %1 offset:1024\n\t"
    "ds_read_b64_tr_b16 v[134:135], %1 offset:1152\n\t"
    "ds_read_b64_tr_b16 v[136:137], %1 offset:32\n\t"
    "ds_read_b64_tr_b16 v[138:139], %1 offset:160\n\t"
    "ds_read_b64_tr_b16 v[140:141], %1 offset:1056\n\t"
    "ds_read_b64_tr_b16 v[142:143], %1 offset:1184\n\t"
    "s_waitcnt lgkmcnt(0)\n\t"
    "ds_read_b64_tr_b16 v[144:145], %1 offset:4096\n\t"
    "ds_read_b64_tr_b16 v[146:147], %1 offset:4224\n\t"
    "ds_read_b64_tr_b16 v[148:149], %1 offset:5120\n\t"
    "ds_read_b64_tr_b16 v[150:151], %1 offset:5248\n\t"
    "ds_read_b64_tr_b16 v[152:153], %1 offset:4128\n\t"
    "ds_read_b64_tr_b16 v[154:155], %1 offset:4256\n\t"
    "ds_read_b64_tr_b16 v[156:157], %1 offset:5152\n\t"
    "ds_read_b64_tr_b16 v[158:159], %1 offset:5280\n\t"
    "s_waitcnt lgkmcnt(0)"
    : "=&{v[128:159]}"(w) : "v"(a) : "memory");
}
template <> __device__ __forceinline__ void vr_wload<3>(u32x32& w, unsigned a) {
  asm volatile("ds_read_b64_tr_b16 v[160:161], %1 offset:8192\n\t"
    "ds_read_b64_tr_b16 v[162:163], %1 offset:8320\n\t"
    "ds_read_b64_tr_b16 v[164:165], %1 offset:9216\n\t"
    "ds_read_b64_tr_b16 v[166:167], %1 offset:9344\n\t"
    "ds_read_b64_tr_b16 v[168:169], %1 offset:8224\n\t"
    "ds_read_b64_tr_b16 v[170:171], %1 offset:8352\n\t"
    "ds_read_b64_tr_b16 v[172:173], %1 offset:9248\n\t"
    "ds_read_b64_tr_b16 v[174:175], %1 offset:9376\n\t"
    "s_waitcnt lgkmcnt(0)\n\t"
    "ds_read_b64_tr_b16 v[176:177], %1 offset:12288\n\t"
    "ds_read_b64_tr_b16 v[178:179], %1 offset:12416\n\t"
    "ds_read_b64_tr_b16 v[180:181], %1 offset:13312\n\t"
    "ds_read_b64_tr_b16 v[182:183], %1 offset:13440\n\t"
    "ds_read_b64_tr_b16 v[184:185], %1 offset:12320\n\t"
    "ds_read_b64_tr_b16 v[186:187], %1 offset:12448\n\t"
    "ds_read_b64_tr_b16 v[188:189], %1 offset:13344\n\t"
    "ds_read_b64_tr_b16 v[190:191], %1 offset:13472\n\t"
    "s_waitcnt lgkmcnt(0)"
    : "=&{v[160:191]}"(w) : "v"(a) : "memory");
}
template <> __device__ __forceinline__ void vr_wload<4>(u32x32& w, unsigned a) {
  asm volatile("ds_read_b64_tr_b16 v[192:193], %1 offset:0\n\t"
    "ds_read_b64_tr_b16 v[194:195], %1 offset:128\n\t"
    "ds_read_b64_tr_b16 v[196:197], %1 offset:1024\n\t"
    "ds_read_b64_tr_b16 v[198:199], %1 offset:1152\n\t"
    "ds_read_b64_tr_b16 v[200:201], %1 offset:32\n\t"
    "ds_read_b64_tr_b16 v[202:203], %1 offset:160\n\t"
    "ds_read_b64_tr_b16 v[204:205], %1 offset:1056\n\t"
    "ds_read_b64_tr_b16 v[206:207], %1 offset:1184\n\t"
    "s_waitcnt lgkmcnt(0)\n\t"
    "ds_read_b64_tr_b16 v[208:209], %1 offset:4096\n\t"
    "ds_read_b64_tr_b16 v[210:211], %1 offset:4224\n\t"
    "ds_read_b64_tr_b16 v[212:213], %1 offset:5120\n\t"
    "ds_read_b64_tr_b16 v[214:215], %1 offset:5248\n\t"
    "ds_read_b64_tr_b16 v[216:217], %1 offset:4128\n\t"
    "ds_read_b64_tr_b16 v[218:219], %1 offset:4256\n\t"
    "ds_read_b64_tr_b16 v[220:221], %1 offset:5152\n\t"
    "ds_read_b64_tr_b16 v[222:223], %1 offset:5280\n\t"
    "s_waitcnt lgkmcnt(0)"
    : "=&{v[192:223]}"(w) : "v"(a) : "memory");
}
template <> __device__ __forceinline__ void vr_wload<5>(u32x32& w, unsigned a) {
  asm volatile("ds_read_b64_tr_b16 v[224:225], %1 offset:8192\n\t"
    "ds_read_b64_tr_b16 v[226:227], %1 offset:8320\n\t"
    "ds_read_b64_tr_b16 v[228:229], %1 offset:9216\n\t"
    "ds_read_b64_tr_b16 v[230:231], %1 offset:9344\n\t"
    "ds_read_b64_tr_b16 v[232:233], %1 offset:8224\n\t"
    "ds_read_b64_tr_b16 v[234:235], %1 offset:8352\n\t"
    "ds_read_b64_tr_b16 v[236:237], %1 offset:9248\n\t"
    "ds_read_b64_tr_b16 v[238:239], %1 offset:9376\n\t"
    "s_waitcnt lgkmcnt(0)\n\t"
    "ds_read_b64_tr_b16 v[240:241], %1 offset:12288\n\t"
    "ds_read_b64_tr_b16 v[242:243], %1 offset:12416\n\t"
    "ds_read_b64_tr_b16 v[244:245], %1 offset:13312\n\t"
    "ds_read_b64_tr_b16 v[246:247], %1 offset:13440\n\t"
    "ds_read_b64_tr_b16 v[248:249], %1 offset:12320\n\t"
    "ds_read_b64_tr_b16 v[250:251], %1 offset:12448\n\t"
    "ds_read_b64_tr_b16 v[252:253], %1 offset:13344\n\t"
    "ds_read_b64_tr_b16 v[254:255], %1 offset:13472\n\t"
    "s_waitcnt lgkmcnt(0)"
    : "=&{v[224:255]}"(w) : "v"(a) : "memory");
}
#define OREP 2
#define AREP 2
struct VRW { u32x32 v0, v1, o0, o1, o2, o3; };
__device__ __forceinline__ void vr_v(const VRW& W, const char* ag, const char* vd, unsigned wa, unsigned ra, unsigned& cap, unsigned& sp, unsigned long long& tc, unsigned long long* ts) {
  asm volatile("v_mbcnt_lo_u32_b32 v16, -1, 0\n\t"
    "v_mbcnt_hi_u32_b32 v16, -1, v16\n\t"
    "v_lshlrev_b32 v17, 4, v16\n\t"
    "s_mov_b32 %[sp], 0\n\t"
    "global_load_dwordx4 v[24:27], v17, %[ag] sc1\n\t"
    "global_load_dwordx4 v[28:31], v17, %[ag] offset:1024 sc1\n\t"
    "s_memtime %[tc]\n\t"
    "v_and_b32 v22, 4, v16\n\t"
    "v_mul_u32_u24 v22, 0x2aaa, v22\n\t"
    "v_lshrrev_b32 v18, 1, v16\n\t"
    "v_and_b32 v18, 2, v18\n\t"
    "v_or3_b32 v18, v18, v22, %[c44]\n\t"
    "v_lshl_add_u32 v19, v16, 3, %[wa]\n\t"
    "v_lshl_add_u32 v20, v16, 3, %[ra]\n\t"
    "v_and_b32 v22, 40, v16\n\t"
    "v_lshlrev_b32 v21, 3, v22\n\t"
    "v_and_b32 v22, 3, v16\n\t"
    "v_lshl_or_b32 v21, v22, 4, v21\n\t"
    "v_and_b32 v22, 4, v16\n\t"
    "v_lshl_or_b32 v21, v22, 1, v21\n\t"
    "v_lshrrev_b32 v22, 2, v16\n\t"
    "v_and_or_b32 v21, v22, 4, v21\n\t"
    "3:\n\t"
    "v_mov_b32 v32, 0\n\t"
    "v_mov_b32 v33, 0\n\t"
    "v_mov_b32 v34, 0\n\t"
    "v_mov_b32 v35, 0\n\t"
    "v_mov_b32 v36, 0\n\t"
    "v_mov_b32 v37, 0\n\t"
    "v_mov_b32 v38, 0\n\t"
    "v_mov_b32 v39, 0\n\t"
    "v_mov_b32 v40, 0\n\t"
    "v_mov_b32 v41, 0\n\t"
    "v_mov_b32 v42, 0\n\t"
    "v_mov_b32 v43, 0\n\t"
    "v_mov_b32 v44, 0\n\t"
    "v_mov_b32 v45, 0\n\t"
    "v_mov_b32 v46, 0\n\t"
    "v_mov_b32 v47, 0\n\t"
    "s_waitcnt vmcnt(0) lgkmcnt(0)\n\t"
    "v_smfmac_f32_16x16x64_bf16 v[32:35], v[24:27], v[64:71], v18\n\t"
    "v_smfmac_f32_16x16x64_bf16 v[32:35], v[28:31], v[96:103], v18\n\t"
    "v_max3_u32 v22, v24, v25, v26\n\t"
    "v_max3_u32 v23, v27, v28, v29\n\t"
    "v_smfmac_f32_16x16x64_bf16 v[36:39], v[24:27], v[72:79], v18\n\t"
    "v_max3_u32 v22, v22, v30, v31\n\t"
    "v_max_u32 v22, v22, v23\n\t"
    "v_smfmac_f32_16x16x64_bf16 v[36:39], v[28:31], v[104:111], v18\n\t"
    "v_cmp_eq_u32 vcc, -1, v22\n\t"
    "s_cbranch_vccnz 1f\n\t"
    "2:\n\t"
    "v_smfmac_f32_16x16x64_bf16 v[40:43], v[24:27], v[80:87], v18\n\t"
    "v_permlane16_swap_b32 v32, v34\n\t"
    "v_permlane16_swap_b32 v33, v35\n\t"
    "v_smfmac_f32_16x16x64_bf16 v[40:43], v[28:31], v[112:119], v18\n\t"
    "v_pk_add_f32 v[32:33], v[32:33], v[34:35]\n\t"
    "ds_write_b64 v19, v[32:33] offset:0\n\t"
    "v_smfmac_f32_16x16x64_bf16 v[44:47], v[24:27], v[88:95], v18\n\t"
    "s_nop 0\n\t"
    "v_permlane16_swap_b32 v36, v38\n\t"
    "v_permlane16_swap_b32 v37, v39\n\t"
    "v_smfmac_f32_16x16x64_bf16 v[44:47], v[28:31], v[120:127], v18\n\t"
    "v_pk_add_f32 v[36:37], v[36:37], v[38:39]\n\t"
    "ds_write_b64 v19, v[36:37] offset:512\n\t"
    "v_permlane16_swap_b32 v40, v42\n\t"
    "v_permlane16_swap_b32 v41, v43\n\t"
    "v_pk_add_f32 v[40:41], v[40:41], v[42:43]\n\t"
    "ds_write_b64 v19, v[40:41] offset:1024\n\t"
    "s_nop 1\n\t"
    "v_permlane16_swap_b32 v44, v46\n\t"
    "v_permlane16_swap_b32 v45, v47\n\t"
    "v_pk_add_f32 v[44:45], v[44:45], v[46:47]\n\t"
    "ds_write_b64 v19, v[44:45] offset:1536\n\t"
    "s_cmp_ge_u32 %[wa], 8192\n\t"
    "s_waitcnt lgkmcnt(0)\n\t"
    "s_barrier\n\t"
    "s_cbranch_scc1 9f\n\t"
    "ds_read_b64 v[48:49], v20 offset:0\n\t"
    "ds_read_b64 v[50:51], v20 offset:2048\n\t"
    "ds_read_b64 v[52:53], v20 offset:4096\n\t"
    "ds_read_b64 v[54:55], v20 offset:6144\n\t"
    "ds_read_b64 v[56:57], v20 offset:8192\n\t"
    "ds_read_b64 v[58:59], v20 offset:10240\n\t"
    "ds_read_b64 v[60:61], v20 offset:12288\n\t"
    "ds_read_b64 v[62:63], v20 offset:14336\n\t"
    "s_waitcnt lgkmcnt(6)\n\t"
    "v_pk_add_f32 v[48:49], v[48:49], v[50:51]\n\t"
    "s_waitcnt lgkmcnt(4)\n\t"
    "v_pk_add_f32 v[52:53], v[52:53], v[54:55]\n\t"
    "v_pk_add_f32 v[48:49], v[48:49], v[52:53]\n\t"
    "s_waitcnt lgkmcnt(2)\n\t"
    "v_pk_add_f32 v[56:57], v[56:57], v[58:59]\n\t"
    "s_waitcnt lgkmcnt(0)\n\t"
    "v_pk_add_f32 v[60:61], v[60:61], v[62:63]\n\t"
    "v_pk_add_f32 v[56:57], v[56:57], v[60:61]\n\t"
    "v_pk_add_f32 v[48:49], v[48:49], v[56:57]\n\t"
    "v_cvt_pk_bf16_f32 v22, v48, v49\n\t"
    "v_min_u32 v22, -2, v22\n\t"
    "global_store_dword v21, v22, %[vd] sc1\n\t"
    "s_waitcnt lgkmcnt(0)\n\t"
    "s_branch 9f\n\t"
    "1:\n\t"
    "s_add_u32 %[sp], %[sp], 1\n\t"
    "s_cmp_ge_u32 %[sp], %[cap]\n\t"
    "s_cbranch_scc1 7f\n\t"
    "global_load_dwordx4 v[24:27], v17, %[ag] sc1\n\t"
    "global_load_dwordx4 v[28:31], v17, %[ag] offset:1024 sc1\n\t"
    "s_waitcnt lgkmcnt(0)\n\t"
    "s_memtime %[tc]\n\t"
    "s_nop 7\n\t"
    "s_branch 3b\n\t"
    "7:\n\t"
    "s_mov_b32 %[cap], 0\n\t"
    "s_branch 2b\n\t"
    "9:"
    : [tc] "=&s"(tc), [sp] "=&s"(sp), [cap] "+s"(cap)
    : [ag] "s"(ag), [vd] "s"(vd), [wa] "s"(wa), [ra] "s"(ra), [c44] "s"(0x4444u), "{v[64:95]}"(W.v0), "{v[96:127]}"(W.v1), "{v[128:159]}"(W.o0), "{v[160:191]}"(W.o1), "{v[192:223]}"(W.o2), "{v[224:255]}"(W.o3)
    : "v16", "v17", "v18", "v19", "v20", "v21", "v22", "v23", "v24", "v25", "v26", "v27", "v28", "v29", "v30", "v31", "v32", "v33", "v34", "v35", "v36", "v37", "v38", "v39", "v40", "v41", "v42", "v43", "v44", "v45", "v46", "v47", "v48", "v49", "v50", "v51", "v52", "v53", "v54", "v55", "v56", "v57", "v58", "v59", "v60", "v61", "v62", "v63", "vcc", "scc", "memory");
}
__device__ __forceinline__ void vr_r(const VRW& W, const char* os, const char* ad, unsigned wa, unsigned jx, unsigned& cap, unsigned& sp, unsigned long long* ts) {
  asm volatile("v_mbcnt_lo_u32_b32 v16, -1, 0\n\t"
    "v_mbcnt_hi_u32_b32 v16, -1, v16\n\t"
    "v_lshl_or_b32 v17, v16, 4, %[jx]\n\t"
    "global_load_dwordx4 v[24:27], v17, %[os] sc1\n\t"
    "v_xor_b32 v18, 2048, v17\n\t"
    "global_load_dwordx4 v[28:31], v18, %[os] sc1\n\t"
    "v_xor_b32 v57, 4096, v17\n\t"
    "global_load_dwordx4 v[32:35], v57, %[os] sc1\n\t"
    "v_xor_b32 v58, 6144, v17\n\t"
    "global_load_dwordx4 v[36:39], v58, %[os] sc1\n\t"
    "s_mov_b32 %[sp], 0\n\t"
    "v_and_b32 v23, 4, v16\n\t"
    "v_mul_u32_u24 v23, 0x2aaa, v23\n\t"
    "v_lshrrev_b32 v19, 1, v16\n\t"
    "v_and_b32 v19, 2, v19\n\t"
    "v_or3_b32 v19, v19, v23, %[c44]\n\t"
    "v_and_b32 v23, 15, v16\n\t"
    "v_lshl_add_u32 v20, v23, 2, %[wa]\n\t"
    "v_and_b32 v23, 48, v16\n\t"
    "v_lshl_add_u32 v20, v23, 3, v20\n\t"
    "v_and_b32 v23, 32, v16\n\t"
    "v_and_b32 v21, 28, v16\n\t"
    "v_lshlrev_b32 v21, 4, v21\n\t"
    "v_or_b32 v21, v21, v23\n\t"
    "v_lshlrev_b32 v22, 3, v23\n\t"
    "v_and_b32 v23, 3, v16\n\t"
    "v_lshl_or_b32 v21, v23, 3, v21\n\t"
    "v_and_b32 v23, 31, v16\n\t"
    "v_lshl_or_b32 v22, v23, 2, v22\n\t"
    "3:\n\t"
    "v_mov_b32 v40, 0\n\t"
    "v_mov_b32 v41, 0\n\t"
    "v_mov_b32 v42, 0\n\t"
    "v_mov_b32 v43, 0\n\t"
    "v_mov_b32 v44, 0\n\t"
    "v_mov_b32 v45, 0\n\t"
    "v_mov_b32 v46, 0\n\t"
    "v_mov_b32 v47, 0\n\t"
    "v_mov_b32 v48, 0\n\t"
    "v_mov_b32 v49, 0\n\t"
    "v_mov_b32 v50, 0\n\t"
    "v_mov_b32 v51, 0\n\t"
    "v_mov_b32 v52, 0\n\t"
    "v_mov_b32 v53, 0\n\t"
    "v_mov_b32 v54, 0\n\t"
    "v_mov_b32 v55, 0\n\t"
    "v_mov_b32 v56, 0\n\t"
    "s_waitcnt vmcnt(3)\n\t"
    "v_smfmac_f32_16x16x64_bf16 v[44:47], v[24:27], v[144:151], v19\n\t"
    "v_max3_u32 v56, v56, v24, v25\n\t"
    "v_smfmac_f32_16x16x64_bf16 v[48:51], v[24:27], v[136:143], v19\n\t"
    "v_max3_u32 v56, v56, v26, v27\n\t"
    "v_smfmac_f32_16x16x64_bf16 v[40:43], v[24:27], v[128:135], v19\n\t"
    "v_smfmac_f32_16x16x64_bf16 v[52:55], v[24:27], v[152:159], v19\n\t"
    "s_waitcnt vmcnt(2)\n\t"
    "v_smfmac_f32_16x16x64_bf16 v[44:47], v[28:31], v[176:183], v19\n\t"
    "v_max3_u32 v56, v56, v28, v29\n\t"
    "v_smfmac_f32_16x16x64_bf16 v[48:51], v[28:31], v[168:175], v19\n\t"
    "v_max3_u32 v56, v56, v30, v31\n\t"
    "v_smfmac_f32_16x16x64_bf16 v[40:43], v[28:31], v[160:167], v19\n\t"
    "v_smfmac_f32_16x16x64_bf16 v[52:55], v[28:31], v[184:191], v19\n\t"
    "s_waitcnt vmcnt(1)\n\t"
    "v_smfmac_f32_16x16x64_bf16 v[44:47], v[32:35], v[208:215], v19\n\t"
    "v_max3_u32 v56, v56, v32, v33\n\t"
    "v_smfmac_f32_16x16x64_bf16 v[48:51], v[32:35], v[200:207], v19\n\t"
    "v_max3_u32 v56, v56, v34, v35\n\t"
    "v_smfmac_f32_16x16x64_bf16 v[40:43], v[32:35], v[192:199], v19\n\t"
    "v_smfmac_f32_16x16x64_bf16 v[52:55], v[32:35], v[216:223], v19\n\t"
    "s_waitcnt vmcnt(0)\n\t"
    "v_smfmac_f32_16x16x64_bf16 v[44:47], v[36:39], v[240:247], v19\n\t"
    "v_max3_u32 v56, v56, v36, v37\n\t"
    "v_smfmac_f32_16x16x64_bf16 v[48:51], v[36:39], v[232:239], v19\n\t"
    "v_max3_u32 v56, v56, v38, v39\n\t"
    "v_smfmac_f32_16x16x64_bf16 v[40:43], v[36:39], v[224:231], v19\n\t"
    "v_cmp_eq_u32 vcc, -1, v56\n\t"
    "v_smfmac_f32_16x16x64_bf16 v[52:55], v[36:39], v[248:255], v19\n\t"
    "s_cbranch_vccnz 4f\n\t"
    "5:\n\t"
    "s_nop 2\n\t"
    "v_permlane32_swap_b32 v44, v48\n\t"
    "v_permlane32_swap_b32 v45, v49\n\t"
    "v_permlane32_swap_b32 v46, v50\n\t"
    "v_permlane32_swap_b32 v47, v51\n\t"
    "s_mov_b32 exec_hi, 0\n\t"
    "v_pk_add_f32 v[40:41], v[40:41], v[48:49]\n\t"
    "v_pk_add_f32 v[42:43], v[42:43], v[50:51]\n\t"
    "s_mov_b64 exec, %[hi]\n\t"
    "v_pk_add_f32 v[40:41], v[44:45], v[52:53]\n\t"
    "v_pk_add_f32 v[42:43], v[46:47], v[54:55]\n\t"
    "s_mov_b32 exec_lo, -1\n\t"
    "s_nop 0\n\t"
    "v_permlane16_swap_b32 v40, v42\n\t"
    "v_permlane16_swap_b32 v41, v43\n\t"
    "v_add_f32 v40, v40, v42\n\t"
    "v_add_f32 v41, v41, v43\n\t"
    "ds_write_b32 v20, v40 offset:16384\n\t"
    "ds_write_b32 v20, v41 offset:16448\n\t"
    "s_cmp_ge_u32 %[wa], 1024\n\t"
    "s_waitcnt lgkmcnt(0)\n\t"
    "s_barrier\n\t"
    "s_cbranch_scc1 9f\n\t"
    "ds_read_b64 v[24:25], v21 offset:16384\n\t"
    "ds_read_b64 v[26:27], v21 offset:16896\n\t"
    "ds_read_b64 v[28:29], v21 offset:17408\n\t"
    "ds_read_b64 v[30:31], v21 offset:17920\n\t"
    "ds_read_b64 v[32:33], v21 offset:18432\n\t"
    "ds_read_b64 v[34:35], v21 offset:18944\n\t"
    "ds_read_b64 v[36:37], v21 offset:19456\n\t"
    "ds_read_b64 v[38:39], v21 offset:19968\n\t"
    "s_waitcnt lgkmcnt(6)\n\t"
    "v_pk_add_f32 v[24:25], v[24:25], v[26:27]\n\t"
    "s_waitcnt lgkmcnt(4)\n\t"
    "v_pk_add_f32 v[28:29], v[28:29], v[30:31]\n\t"
    "v_pk_add_f32 v[24:25], v[24:25], v[28:29]\n\t"
    "s_waitcnt lgkmcnt(2)\n\t"
    "v_pk_add_f32 v[32:33], v[32:33], v[34:35]\n\t"
    "s_waitcnt lgkmcnt(0)\n\t"
    "v_pk_add_f32 v[36:37], v[36:37], v[38:39]\n\t"
    "v_pk_add_f32 v[32:33], v[32:33], v[36:37]\n\t"
    "v_pk_add_f32 v[24:25], v[24:25], v[32:33]\n\t"
    "v_cvt_pk_bf16_f32 v23, v24, v25\n\t"
    "v_min_u32 v23, -2, v23\n\t"
    "global_store_dword v22, v23, %[ad] sc1\n\t"
    "s_waitcnt lgkmcnt(0)\n\t"
    "s_branch 9f\n\t"
    "4:\n\t"
    "s_add_u32 %[sp], %[sp], 1\n\t"
    "s_cmp_ge_u32 %[sp], %[cap]\n\t"
    "s_cbranch_scc1 7f\n\t"
    "global_load_dwordx4 v[24:27], v17, %[os] sc1\n\t"
    "global_load_dwordx4 v[28:31], v18, %[os] sc1\n\t"
    "global_load_dwordx4 v[32:35], v57, %[os] sc1\n\t"
    "global_load_dwordx4 v[36:39], v58, %[os] sc1\n\t"
    "s_nop 7\n\t"
    "s_branch 3b\n\t"
    "7:\n\t"
    "s_mov_b32 %[cap], 0\n\t"
    "s_branch 5b\n\t"
    "9:"
    : [sp] "=&s"(sp), [cap] "+s"(cap)
    : [os] "s"(os), [ad] "s"(ad), [wa] "s"(wa), [jx] "s"(jx), [c44] "s"(0x4444u), [hi] "s"(0xFFFFFFFF00000000ull), "{v[64:95]}"(W.v0), "{v[96:127]}"(W.v1), "{v[128:159]}"(W.o0), "{v[160:191]}"(W.o1), "{v[192:223]}"(W.o2), "{v[224:255]}"(W.o3)
    : "v16", "v17", "v18", "v19", "v20", "v21", "v22", "v23", "v24", "v25", "v26", "v27", "v28", "v29", "v30", "v31", "v32", "v33", "v34", "v35", "v36", "v37", "v38", "v39", "v40", "v41", "v42", "v43", "v44", "v45", "v46", "v47", "v48", "v49", "v50", "v51", "v52", "v53", "v54", "v55", "v56", "v57", "v58", "v59", "v60", "v61", "v62", "v63", "vcc", "scc", "memory");
}
__device__ __forceinline__ void qk_body(char* smem, int wave_id_, int xg, int h, int wg, const u16* __restrict__ Wq, const u16* __restrict__ Wk,
                                        const char* __restrict__ Kx, const char* __restrict__ V, char* ws, unsigned* tmo, u64 tag) {
  const int wave = WAVE_ID;
  const int ty = wave >> 2, w4 = wave & 3;
  const char* wq = uni((const char*)(Wq + h * DH)); const char* wk = uni((const char*)(Wk + h * DH));
  auto stage = [&](int u) {
    stage_proj((u & 1) ? wk + (size_t)(((u >> 1) + 2) & 7) * 128 * HD * 2 : wq + (size_t)(u >> 1) * 128 * HD * 2, ((u + 4) % 5) * 32768, wave * 64 + lane_id(), wave);
  };
  QKW W;
  if (wave == 0) STS(ws, wg, 7);
  stage(0); stage(1); stage(2); stage(3);
#define WLD(i, w) do { WAIT_V(12); WBAR(); stage(2 * (i) + 4); if (ty) { WAIT_V(12); WBAR(); stage(2 * (i) + 5); } \
    { const int l_ = lane_id(); const unsigned o_ = ty ? ((2 * (i) + 5) % 5) * 32768 : ((2 * (i) + 4) % 5) * 32768; qk_wload<i>(w, proj_base(2 * w4, l_) + o_, proj_base(2 * w4 + 1, l_) + o_); } \
    if (!ty) { WAIT_V(12); WBAR(); stage(2 * (i) + 5); } if (wave == 0) STS(ws, wg, 1 + (i)); } while (0)
  WLD(0, W.w0); WLD(1, W.w1); WLD(2, W.w2); WLD(3, W.w3); WLD(4, W.w4); WLD(5, W.w5);
#undef WLD
  unsigned in = 0;
#pragma unroll
  for (int i = 0; i < 2; ++i) {
    if (i == 0) WAIT_V(8); else WAIT_V(0);
    WBAR();
    if (i == 0 && wave == 1) init_issue(ws, Q_V);
    if (i == 1) in = init_get(ws, tag, smem + Q_V);
    const int lane = lane_id();
    const unsigned o = Q_RING + ty * 32768 + i * 65536, f0 = proj_base(2 * w4, lane) + o, f1 = proj_base(2 * w4 + 1, lane) + o;
    u32x8 x[4] = {proj_frag(smem, f0), proj_frag(smem, f1), proj_frag(smem, f0 + 16384), proj_frag(smem, f1 + 16384)};
    asm volatile("" : "+v"(x[0]), "+v"(x[1]), "+v"(x[2]), "+v"(x[3]));
    BAR();
    char* d = smem + Q_CB + i * 57344 + ty * (i ? 32768 : 24576) + w4 * 2048 + lane * 16;
#pragma unroll
    for (int j = 0; j < 4; ++j)
      if (i || j || !ty) { *(u32x4*)(d + j * 8192) = u32x4{x[j][0], x[j][1], x[j][2], x[j][3]}; *(u32x4*)(d + j * 8192 + 1024) = u32x4{x[j][4], x[j][5], x[j][6], x[j][7]}; }
    if (i == 0 && ty) { *(u32x4*)(smem + Q_MEM + wave * 2048 + lane * 16) = u32x4{x[0][0], x[0][1], x[0][2], x[0][3]}; *(u32x4*)(smem + Q_MEM + wave * 2048 + 1024 + lane * 16) = u32x4{x[0][4], x[0][5], x[0][6], x[0][7]}; }
  }
  if (wave == 4) *(volatile LAS u32x2*)(smem + Q_TM) = u32x2{0u, 0u};
  BAR();
  if (wave == 0) STS(ws, wg, 0);
  unsigned* cnt = cnt_of(ws, in);
  const char* xb = uni(xch_base(ws) + ((size_t)3 << 21) + (size_t)((in & 2u) << 12));
  const char* oh = uni(xch_base(ws) + (size_t)((in & 1u) << 13) + (size_t)(h >> 2) * 16384 + (h & 3) * 2048);
  auto rearm = [&](int ps) {
    unsigned z = SENT; asm volatile("" : "+v"(z));
    const unsigned l16 = lane_id() * 16;
#pragma unroll
    for (int r = 0; r < 2 * OREP; ++r) st16s(oh + (size_t)(ps * 4 + (r >> 1)) * 131072, (r & 1) * 1024u + l16, u32x4{z, z, z, z});
  };
  const char* dbw = xb + (size_t)48 * 16384 + 4096 + h * 128;
  unsigned avail = 0;
  auto stage_kv = [&](int s) {
    if (avail <= (unsigned)s) {
      for (int spins = 0;; ++spins) {
        const unsigned p = ld4_sc1(cnt + (PQKV + h) * 32 + (lane_id() & 15));
        avail = min(qkv_avail(p, 1), qkv_avail(p, 2));
        if (avail > (unsigned)s) break;
        __builtin_amdgcn_s_sleep(8);
        if (give_up(spins, tmo, 5)) break;
      }
    }
    const char* ks = uni(Kx + ((size_t)s * NH + h) * 8192); const char* vs = uni(V + ((size_t)s * NH + h) * 8192);
    const int lane = lane_id();
    head_to_lds(ks, lane, Q_K); head_to_lds(vs, lane, Q_V);
  };
  auto loop = [&](auto w_c) __attribute__((always_inline)) {
    constexpr int WV = decltype(w_c)::value;
    if (WV >= 4) asm volatile("ds_read_b128 v[56:59], %1\n\tds_read_b128 v[60:63], %1 offset:1024\n\ts_waitcnt lgkmcnt(0)" : "=&{v[56:63]}"(W.x) : "v"(Q_MEM + WV * 2048 + lane_id() * 16) : "memory");
    if (WV == 6) stage_kv(0);
    if (QK_OWN) {
      constexpr int PI = WV < 4 ? WV : WV - 2;
      if constexpr (QK_OWN == 3 && WV >= 4) qk_swapk<PI>(PI == 2 ? W.w2 : PI == 3 ? W.w3 : PI == 4 ? W.w4 : W.w5, W.x, Q_CB + 32768 + (WV & 3) * 2048 + lane_id() * 16);
      else qk_swap<PI>(PI == 0 ? W.w0 : PI == 1 ? W.w1 : PI == 2 ? W.w2 : PI == 3 ? W.w3 : PI == 4 ? W.w4 : W.w5, Q_CB + 57344 + (WV >> 2) * 32768 + (WV & 3) * 2048 + lane_id() * 16);
    }
    if (WV < 4) __builtin_amdgcn_s_setprio(2);
    unsigned t0 = 0, cap = 60000, d0 = 105, d1 = 200;
    int dq = D_Q;
    int sl = 0;
    for (int s = 0; s < NSEG; ++s) {
      TSDECL;
      const char* ag = xb + (size_t)((sl * 4 + (AREP == 4 ? xg : (AREP == 2 ? ARSEL : 0))) * 2 + (WV >> 2)) * 16384 + (WV & 3) * 2048;
      const char* vs = WV == 4 ? dbw : WV == 5 ? ws + OFF_KVM + ((size_t)s * NH + h) * 4096 : xb + (size_t)(24 + sl * 8 + (h >> 2)) * 16384 + (h & 3) * 2048;
      const char* od = WV == 4 ? (const char*)(size_t)((unsigned)(s + 1) << 24) : oh + (size_t)sl * 524288;
      if (s > 0) wait_rt(t0, dq);
      TS(0);
      unsigned long long tc; unsigned sp;
      qk_step<WV>(W, ag, vs, od, LAST && s == NSEG - 1 ? nullptr : ws + OFF_KVM + ((size_t)s * NH + h) * 4096 + 2048, cap, sp, tc, nullptr, d0, d1);
      dq = (sp & 0xFFFFu) ? min(dq + DQU, D_Q + DQM) : max(dq - DQD, D_Q);
      if ((WV < 4 || WV == 5) && !(LAST && s == NSEG - 1)) {
        const unsigned pv = (unsigned)(s + 1) << 24 | (rt_now() & 0xFFFFFFu);
        WAIT_V(0);
        if (lane_id() == 0) st4_sc1(cnt + (PROG + h) * 32 + WV, pv);
      }
      if (WV == 6 && s + 1 < NSEG - 1) stage_kv(s + 1);
      if (WV == 7 && s > 0 && s < NSEG - 1) rearm(sl == 0 ? 2 : sl - 1);
      if (WV == 7 && s >= 8 && s < 8 + NSEG - SDIR) {
        unsigned z = SENT; asm volatile("" : "+v"(z));
        const char* kd = ws + OFF_KVM + ((size_t)(s - 8 + SDIR) * NH + h) * 4096;
#pragma unroll
        for (int r = 0; r < 4; ++r) st16s(kd, r * 1024u + lane_id() * 16, u32x4{z, z, z, z});
      }
      if (s < NSEG - 1) {
        if (WV == 4) {
          delay64<E_B>();
          t0 = bell_time(xb + (size_t)(48 + xg) * 16384 + 4096, (lane_id() & 31) * 128, (unsigned)(s + 1), cap);
          *(volatile LAS u32x2*)(smem + Q_TM) = u32x2{(unsigned)(s + 1), t0};
        } else {
          for (;;) {
            const u32x2 v = *(volatile LAS u32x2*)(smem + Q_TM);
            t0 = __builtin_amdgcn_readfirstlane(v.y);
            if (__builtin_amdgcn_readfirstlane(v.x) == (unsigned)(s + 1)) break;
            __builtin_amdgcn_s_sleep(1);
          }
        }
      }
      sl = sl == 2 ? 0 : sl + 1;
    }
    if (cap == 0 && lane_id() == 0) st4_sc1(tmo, 1u);
  };
  switch (wave) {
    case 0: loop(std::integral_constant<int, 0>{}); break;
    case 1: loop(std::integral_constant<int, 1>{}); break;
    case 2: loop(std::integral_constant<int, 2>{}); break;
    case 3: loop(std::integral_constant<int, 3>{}); break;
    case 4: loop(std::integral_constant<int, 4>{}); break;
    case 5: loop(std::integral_constant<int, 5>{}); break;
    case 6: loop(std::integral_constant<int, 6>{}); break;
    default: loop(std::integral_constant<int, 7>{}); break;
  }
  __builtin_amdgcn_s_setprio(0);
}

__device__ __forceinline__ unsigned vr_body(char* smem, int wave_id_, int xg, int h, int c, int r, int wg, const u16* __restrict__ Wv, const u16* __restrict__ Wo,
                                        const u16* __restrict__ Wq, const u16* __restrict__ Wk, char* ws, unsigned* tmo, u64 tag, const char* code) {
  const int wave = WAVE_ID;
  const unsigned po = (unsigned)(r >> 5) * 16384u + ((r >> 2) & 7) * 1024 + (r & 1) * 512 + ((r >> 1) & 1) * 128;
  const unsigned vo_ = (unsigned)(24 + (h >> 2)) * 16384u + (h & 3) * 2048 + c * 1024;
  const int hg = (wave + ROTW * (r & 3)) & 7;
  const int jr = ROTH ? r & 3 : 0;
  VRW W;
  {
    const int lane = lane_id();
    const int g = lane >> 4, q4 = (lane & 15) >> 2, p4 = lane & 3, fr = 4 * g + (q4 & 1) + 32 * (q4 >> 1);
    const unsigned lw = wave * 16384;
    const char* sv_ = uni((const char*)(Wv + (size_t)(128 * wave) * HD + h * DH + 64 * c));
    const char* so = uni((const char*)(Wo + (size_t)(512 * hg) * DM + 64 * (r >> 2) + 16 * (r & 1)));
    const unsigned vo = (unsigned)((32 * (lane >> 5) + ((lane >> 2) & 7)) * DM * 2 + ((lane >> 1) & 1) * 64 + (lane & 1) * 16);
    auto issue = [&](int p) {
#pragma unroll
      for (int i = 0; i < 8; ++i) {
        if (p >= 2 && (NTM & 8)) gldsw(so + (size_t)(128 * ((p - 2) ^ jr) + 64 * (i & 1) + 8 * (i >> 1)) * DM * 2, vo, lw + (p & 1) * 8192 + i * 1024);
        else if (p >= 2) glds16s<0>(so + (size_t)(128 * ((p - 2) ^ jr) + 64 * (i & 1) + 8 * (i >> 1)) * DM * 2, vo, lw + (p & 1) * 8192 + i * 1024);
        else gldsw(sv_ + (size_t)(8 * p + i) * 8 * HD * 2, (unsigned)((lane >> 3) * HD * 2 + (lane & 7) * 16), lw + (p & 1) * 8192 + i * 1024);
      }
    };
#define VTS(p) do { } while (0)
    if (wave == 0) STS(ws, wg, 7);
    issue(0); issue(1);
    const unsigned av = lw + (unsigned)(fr * 128 + p4 * 8), ao = lw + (unsigned)(fr * 64 + p4 * 8);
    WAIT_V(8); VTS(0); vr_wload<0>(W.v0, av); issue(2);
    WAIT_V(8); VTS(1); vr_wload<1>(W.v1, av); issue(3);
    WAIT_V(8); VTS(2); vr_wload<2>(W.o0, ao); issue(4);
    WAIT_V(8); vr_wload<3>(W.o1, ao); issue(5);
    if (wave == 0) init_issue(ws, 131072);
    WAIT_V(8); vr_wload<4>(W.o2, ao);
    WAIT_V(0); VTS(3);
    if (VHN) {
      const int rl = r - 20 * xg, hn = ((xg == 1 ? rl : rl - (rl >= (xg ? 8 : 16) ? 4 : 0)) * 8 + wave) * (6 * VHN) + (lane >> 1);
      const int pm = hn / 384, hr = hn - pm * 384, p = 8 - VHN + (pm >> 1), hh = 12 * xg + 1 + 4 * (hr >> 7);
      const u16* hp = ((pm & 1) ? Wk : Wq) + (size_t)(((pm & 1) ? (p + 2) & 7 : p) * 128 + (hr & 127)) * HD + min(hh, 31) * DH + (lane & 1) * 64;
      if (((r >> 2) & 7) != 3 && lane < 12 * VHN && pm < 2 * VHN) asm volatile("s_mov_b32 m0, %0\n\ts_nop 0\n\tglobal_load_lds_dword %1, off" :: "s"(155648 + wave * 256), "v"(hp) : "memory", "m0");
    }
    vr_wload<5>(W.o3, ao);
#undef VTS
  }
  if (wave == 0) STS(ws, wg, 5);
  if (wave == 0) STS(ws, wg, 6);
  BAR();
  if (wave == 0) STS(ws, wg, 0);
  const unsigned in = init_get(ws, tag, smem + 131072);
  const char* xb = uni(xch_base(ws) + ((size_t)3 << 21) + (size_t)((in & 2u) << 12));
  const char* ob = uni(xch_base(ws) + (size_t)((in & 1u) << 13));
  auto rearm = [&](int ps, bool ag) {
    const int lane = lane_id();
    unsigned z = SENT; asm volatile("" : "+v"(z));
    const u32x4 sv = u32x4{z, z, z, z};
    if (ag && lane < 16 * AREP) st16s(xb, (unsigned)(ps * 8 + (lane >> 4) * 2) * 16384u + po + ((lane >> 3) & 1) * 256 + (lane & 7) * 16, sv);
    st16s(xb, (unsigned)ps * 131072u + vo_ + lane * 16, sv);
  };
  __builtin_amdgcn_s_setprio(2);
  unsigned cap = 60000;
  int sl = 0;
  for (int s = 0; s < NSEG; ++s) {
    TSDECL;
    const int sn = sl == 2 ? 0 : sl + 1;
    const char* ag = xb + (size_t)((sl * 4 + (AREP == 4 ? xg : (AREP == 2 ? ARSEL : 0))) * 2 + (wave >> 2)) * 16384 + (wave & 3) * 2048;
    const char* vd = (LAST && s == NSEG - 1 ? ws + OFF_KVM + ((size_t)s * NH + h) * 4096 + 2048 + c * 1024 : xb + (size_t)sl * 131072 + vo_) + (wave & 2) * 256 + (wave & 1) * 128;
    const char* os = ob + (size_t)((sl * 4 + (OREP == 4 ? xg : ORSEL)) * 8 + hg) * 16384 + ((r >> 1) & 1) * 1024;
    const char* ad = xb + (size_t)((sn * 4 + (wave & 3)) * 2) * 16384 + po;
    if (s > 0) delay64<E_AG>();
    TS(0);
    unsigned long long tc; unsigned sp;
    unsigned long long* tsp = nullptr;
    vr_v(W, ag, vd, wave * 2048, (wave & 3) * 512, cap, sp, tc, tsp);
    TN(12, sp + 1);
    if (s == NSEG - 1) break;
    if (wave == 7 && s > 0) rearm(sl == 0 ? 2 : sl - 1, true);
    if (wave == 6 && s == NSEG - 4 && code) touch(code, (unsigned)lane_id() * 128, 155648);
    wait_clk((unsigned)tc, E_O);
    TS(3);
    const unsigned tb = bell_time(xb + (size_t)(48 + xg) * 16384 + hg * 512, (lane_id() & 3) * 128, (unsigned)(s + 1), cap);
    wait_rt(tb, D_O);
    TS(4);
    vr_r(W, os, ad, wave * 512, jr * 2048, cap, sp, tsp);
    TN(14, (sp & 0xFFFF) + 1);
    if (wave == 1) TSFLUSH(0x6078);
    sl = sn;
  }
  __builtin_amdgcn_s_setprio(0);
  if (cap == 0 && lane_id() == 0) st4_sc1(tmo, 2u);
  return in;
}
struct ModIn { u32x4 w[8]; unsigned cv; };
__device__ __forceinline__ void mod_issue(ModIn& I, int wave_id_, int j, const u16* __restrict__ c, const u16* __restrict__ wmod) {
  const int tid = opaque_tid(), k0 = (j >> 5) * 512 + (tid >> 3);
  I.cv = c[k0 + 64 * (tid & 7)];
#pragma unroll
  for (int i = 0; i < 8; ++i) I.w[i] = ldnt<1>(wmod + (size_t)(k0 + 64 * i) * (6 * DM) + (j & 31) * 64 + (tid & 7) * 8);
}
__device__ __forceinline__ void mod_job(char* smem, int wave_id_, int j, u64 tag, const ModIn& I, char* ws) {
  const int tid = opaque_tid(), lane = tid & 63, wave = WAVE_ID, cg = tid & 7;
  const float cv = bflo(I.cv), sl = bfr(cv / (1.0f + __expf(-cv)));
  float acc[8] = {0, 0, 0, 0, 0, 0, 0, 0};
#pragma unroll
  for (int i = 0; i < 8; ++i) {
    const float sv = __shfl(sl, (lane & 56) + i);
#pragma unroll
    for (int e = 0; e < 4; ++e) { acc[2 * e] += sv * bflo(I.w[i][e]); acc[2 * e + 1] += sv * bfhi(I.w[i][e]); }
  }
#pragma unroll
  for (int e = 0; e < 8; ++e) acc[e] = red16_32_add(add_ror8(acc[e]));
  float* red = (float*)(smem + L_GB);
  if (lane < 8) { *(f32x4*)(red + wave * 64 + cg * 8) = f32x4{acc[0], acc[1], acc[2], acc[3]}; *(f32x4*)(red + wave * 64 + cg * 8 + 4) = f32x4{acc[4], acc[5], acc[6], acc[7]}; }
  __syncthreads();
  if (tid < 32) {
    f32x2 s = f32x2{0, 0};
#pragma unroll
    for (int r = 0; r < 8; ++r) s += *(const f32x2*)(red + r * 64 + tid * 2);
    st16_sc1(ws + OFF_MOD + (j * 32 + tid) * 16, u32x4{__float_as_uint(s[0]), (unsigned)tag, __float_as_uint(s[1]), tag2(tag)});
  }
  BAR();
}
#define NIMM 3
constexpr unsigned TK(int m, int hl, int part, int bo, int be) { return (unsigned)(m | hl << 2 | part << 5 | bo << 8 | be << 16); }
#define QX 30
#define CA 36
#define CB 42
#define C2 46
constexpr unsigned task_tab(int t, int it) {
  return t >= 8 && t < 13 ? (it == 0 ? TK(1, t - 8, 0, 0, 64) : it == 1 ? (t < 12 ? TK(2, t - 4, 1, C2, 64) : TK(1, 5, 1, C2, 64)) : 0)
       : it > 0 ? 0
       : t < 8 ? TK(0, t, 1, QX, 64)
       : t < 16 ? TK(1, t - 8, 0, 0, C2)
       : t < 24 ? TK(2, t - 16, 0, 0, t < 18 ? CA : (t < 20 ? CB : C2))
       : t < 26 ? TK(2, t - 24, 1, CA, 64)
       : t == 26 ? TK(1, 6, 1, C2, (C2 + 64) / 2) : t == 27 ? TK(1, 6, 2, (C2 + 64) / 2, 64)
       : t < 30 ? TK(2, t - 26, 1, CB, 64)
       : t == 30 ? TK(1, 7, 1, C2, (C2 + 64) / 2) : t == 31 ? TK(1, 7, 2, (C2 + 64) / 2, 64)
       : TK(0, t - 32, 0, 0, QX);
}
template <int IT, int I = 0> __device__ __forceinline__ unsigned task_sel(int t) {
  if constexpr (I == 40) return 0u;
  else { unsigned r = task_sel<IT, I + 1>(t); constexpr unsigned c = task_tab(I, IT); if (c) asm volatile("s_cmp_eq_u32 %1, %2\n\ts_cselect_b32 %0, %3, %0" : "+s"(r) : "s"(t), "i"(I), "i"(c) : "scc"); return r; }
}
__global__ void __launch_bounds__(512, 2) k_mega(const u16* __restrict__ Wq, const u16* __restrict__ Wk, const u16* __restrict__ Wv,
                                                 const u16* __restrict__ Wo, const u16* __restrict__ w1, const u16* __restrict__ w3,
                                                 const u16* __restrict__ w2, const u16* __restrict__ x, const u16* __restrict__ cvec,
                                                 const u16* __restrict__ wmod, const u16* __restrict__ mem0, char* ws, u16* out) {
  extern __shared__ __attribute__((aligned(16))) char smem[];
  int wave_id_ = __builtin_amdgcn_readfirstlane((int)threadIdx.x >> 6);
  unsigned bid = blockIdx.x;
  asm volatile("" : "+s"(wave_id_), "+s"(bid));
  unsigned* tmo = (unsigned*)(ws + OFF_TMO);
  const unsigned tst = (unsigned)__builtin_amdgcn_s_memrealtime();
  unsigned long long* mts = nullptr;
  const float* mod = (const float*)(ws + OFF_MOD);
  volatile LAS unsigned* tslot = (volatile LAS unsigned*)(smem + L_SLOT + 768);
#define CPF 131072
  u64 pc; asm volatile("s_getpc_b64 %0" : "=s"(pc));
  const u64 pc0 = pc & ~(u64)127;
  if (CPF && wave_id_ == 1) {
    const unsigned o = (((bid >> 3) & 31) * 64 + lane_id()) * 64;
    if (o < CPF) asm volatile("s_mov_b32 m0, %0\n\ts_nop 0\n\tglobal_load_lds_dword %1, %2" :: "s"(155648 + 1024), "v"(o), "s"(pc & ~(u64)127) : "memory", "m0");
  }
  unsigned xcc; asm volatile("s_getreg_b32 %0, hwreg(HW_REG_XCC_ID)" : "=s"(xcc));
  xcc &= 7;
  const unsigned pred = xcc * 32 + ((bid >> 3) & 31);
  ModIn mi;
  auto modj = [](unsigned sl) { return sl >= 128 && ((sl + 8) & 31) < 16 ? (int)(((sl >> 5) & 3) * 16 + (sl & 15)) : -1; };
  if (modj(pred) >= 0) mod_issue(mi, wave_id_, modj(pred), cvec, wmod);
  u64 tag;
  asm volatile("s_load_dwordx2 %0, %1, 0x0\n\ts_waitcnt lgkmcnt(0)" : "=s"(tag) : "s"(ws + OFF_TAG) : "memory");
  tag += 1;
  if (opaque_tid() == 0) {
    unsigned sl = pred;
    const unsigned s0 = sl;
    for (unsigned i = 1; i <= 256; ++i) {
      if (__hip_atomic_exchange((u64*)(ws + OFF_CLAIM + sl * 128), tag, __ATOMIC_RELAXED, __HIP_MEMORY_SCOPE_AGENT) != tag) break;
      sl = i == 256 ? 0xFFFFu : (((s0 + (i & ~31u)) & 0xE0u) | ((s0 + i) & 31u));
    }
    *tslot = sl;
  }
  BAR();
  const int slot = (int)__builtin_amdgcn_readfirstlane(*tslot);
  BAR();
  if (slot >= 256) return;
  if (modj(slot) >= 0) {
    if (slot != (int)pred) mod_issue(mi, wave_id_, modj(slot), cvec, wmod);
    mod_job(smem, wave_id_, modj(slot), tag, mi, ws);
  }
  unsigned* cnt = nullptr;
  const int xg = slot >> 5;
  int t = xg == 3 ? 24 + (slot & 7) : slot & 31;
  int hx = -1; bool hs = false;
  if (xg < 3) {
    const int nq = xg == 2 ? 8 : 12;
#define HPF 1
    if (t < nq) {
      const int h = xg * 12 + t;
      qk_body(smem, wave_id_, xg, h, (h >> 3) * 32 + (h & 7), Wq, Wk, ws + OFF_K, ws + OFF_V, ws, tmo, tag);
      if (HPF && HB < NSEG && t < 8) {
        const int lane = lane_id(), wv = t * 8 + wave_id_;
#pragma unroll
        for (int j = 0; j < 2; ++j) touch(w2 + (size_t)(xg * 1024 + 256 + j * 128 + (wv & 32) * 16 + (wv & 31) * 4) * DM, (unsigned)lane * 128, wave_id_ * 256);
        if (wv < (xg == 2 ? 32 : 16)) touch(w2 + (size_t)(3072 + 256 + (wv & 15) * 16) * DM + (wv >= 16 ? 3 : xg) * 256, (unsigned)((lane >> 2) * DM * 2 + (lane & 3) * 128), wave_id_ * 256);
        WAIT_V(0);
      }
    } else {
      const int u = t - nq, r = xg * 20 + u, uu = u % 10;
      cnt = cnt_of(ws, vr_body(smem, wave_id_, xg, r >> 1, r & 1, r, (r >> 4) * 32 + 8 + (r & 15), Wv, Wo, Wq, Wk, ws, tmo, tag, CPF && HB < NSEG && u < 16 ? (const char*)pc0 + u * 8192 : nullptr));
      if (HB < NSEG && u < (HB < HC ? 20 : 10)) hx = uu < 8 ? xg * 8 + uu : (uu == 8 ? 24 + xg : (xg == 2 ? 27 : -1));
      hs = u >= 10;
    }
    if (hx < 0) return;
    t = 8;
  }
  const bool cx = xg == 3;
  const int xq = cx ? (slot >> 3) & 3 : xg & 3, wsl = xq * 32 + t;
#define GDEL 1200
#define GDX 1400
  if (cx || t < 8) l1_role(smem, wave_id_, cx, xq * 8 + (t & 7), x, mod, mem0, ws + OFF_H1, cnt, tag, tmo, mts);
#define KH 0
  if (KH && !cx && t >= 24) {
    const int it = (t & 7) * 8 + wave_id_, lane = lane_id();
    touch(((it & 16) ? Wk : Wv) + (size_t)(256 + (it & 8) * 64 + (it & 7) * 32) * HD + (8 * xq + 1 + 4 * (it >> 5)) * DH,
          (unsigned)((lane >> 1) * HD * 2 + (lane & 1) * 128), 155648 + wave_id_ * 256);
  }
  if (t < 8 || t >= 24) {
    if (!cx && t >= 24) arm_job(wave_id_, xq * 8 + t - 24, tag, ws);
    if (!cx && t >= 24 && wave_id_ == 0) STS(ws, 128 + xq * 32 + t - 24, 4);
    while ((unsigned)__builtin_amdgcn_s_memrealtime() - tst < (cx ? GDX : GDEL)) __builtin_amdgcn_s_sleep(8);
    if (t >= 24) cnt = cnt_of(ws, init_wait(ws, tag));
    if (!cx && t >= 24) {
      mod2_job(smem, wave_id_, xq * 8 + t - 24, mod, cvec, wmod, cnt);
    }
  }
  auto u2_of = [&]() { return cx ? -1 : (t >= 13 && t < 16 ? t - 13 : (t >= 20 && t < 24 ? t - 17 : -1)); };
  auto f_of = [&]() {
    int fl = t < 8 ? t : (t < 18 ? t - 4 : (t < 20 ? t - 10 : t - 14));
    const int sa = (3 + 2 * xq) & 7, sb = sa + 8 < 14 ? sa + 8 : 13;
    if (fl == 12) fl = sa; else if (fl == sa) fl = 12;
    else if (fl == 13) fl = sb; else if (fl == sb) fl = 13;
    return (int)__builtin_amdgcn_readfirstlane(14 * xq + fl);
  };
  unsigned tk = task_sel<0>(hx >= 0 ? 99 : (cx ? t + 8 : t)), t2 = task_sel<1>(cx ? t + 8 : t), t3 = task_sel<2>(cx ? t + 8 : t);
  for (int it = 0; tk != 0; ++it, tk = t2, t2 = t3, t3 = 0) {
    const int m = tk & 3, hl = (tk >> 2) & 7, part = (tk >> 5) & 3, bo = (tk >> 8) & 255, ns = (int)(tk >> 16) - bo, h = 8 * xq + hl;
    const bool first = m != 0 && bo == 0, late = it > 0;
    const int lane = lane_id();
    const Dep d{nullptr, (unsigned)(PL1B + (lane & 31)) * 128, 8u, (unsigned)(8 - bo)};
    if (wave_id_ == 0 && late) wait_l1(cnt + (PL1B - PL1) * 32, 7u, tmo);
    BAR();
    gemm7<R_QKV>(smem, wave_id_, (m == 0 ? Wq : (m == 1 ? Wk : Wv)) + h * DH, nullptr, HD, ws + OFF_H1 + (size_t)bo * 65536, 65536, d,
                    ws + (m == 0 ? OFF_Q : (m == 1 ? OFF_K : OFF_V)) + h * 8192 + (size_t)bo * 32 * 8192, 32 * 8192,
                    cnt, (PQKV + h) * 32 + 4 * part + m, tmo, wsl, mts, false, first ? NIMM : 0, ns, late ? ns : 0, bo * 257, tag);
  }
  __syncthreads();
  const int u2 = u2_of();
  const int f2 = hx >= 0 ? hx : (u2 < 0 ? -1 : (xq < 3 ? xq * 8 + u2 : (u2 < 3 ? u2 * 8 + 7 : 21 + u2)));
  const int fb = hx >= 0 ? (hs ? HB : HC) : 0, fe = hx >= 0 ? (hs ? HC : NSEG) : HB;
  if (cx || (f2 >= 0 && f2 < 24)) {
    const int ks = cx ? xq : f2 >> 3, cn = cx ? (t + 1) & 7 : f2 & 7;
    const int lane = lane_id();
    const Dep d{nullptr, (unsigned)(cx ? (PSA + 4 * xq + ((lane >> 3) & 3)) * 128 + (lane & 7) * 4 : (PF1 + 16 * ks + (lane & 15)) * 128), cx ? 4u : 1u, cx ? (unsigned)((lane >> 1) & 3) : 0u};
    gemm7<R_PART>(smem, wave_id_, (cx ? Wo : w2) + (size_t)ks * 1024 * DM + cn * 128, nullptr, DM, ws + (cx ? OFF_O : OFF_G) + ks * 16 * 4096, (cx ? 64 : 56) * 4096, d,
                  ws + (cx ? OFF_P : OFF_P2) + (ks * DM + cn * 128) * (TLP ? 64 : 2), (size_t)32 * PROW, cnt, ((cx ? PWO : PF2) + ks * 8 + cn) * 32 + (hx >= 0 ? (hs ? 2 : 1) : 0), tmo,
                  hx >= 0 ? 280 + hx : 128 + xq * 32 + (cx ? cn : 22 + u2), mts, !cx, 0, cx ? NSEG : fe, 0, 0, 0, fb);
  } else if (f2 >= 0) {
    const int c4 = f2 - 24;
    const int lane = lane_id();
    const Dep d{nullptr, (unsigned)(PF1 + 48 + (lane & 7)) * 128, 1u, 0u};
    gemm7<R_PART2>(smem, wave_id_, w2 + (size_t)3072 * DM + c4 * 256, nullptr, DM, ws + OFF_G + 48 * 4096, 56 * 4096, d,
                   ws + OFF_P2 + (3 * DM + c4 * 256) * (TLP ? 64 : 2), (size_t)32 * PROW, cnt, (PF2 + 24 + c4) * 32 + (hx >= 0 ? (hs ? 2 : 1) : 0), tmo, hx >= 0 ? 280 + hx : 128 + xq * 32 + 22 + u2, mts, true, 0, fe, 0, 0, 0, fb);
  } else if (t < 8 || (t >= 16 && t < 20) || t == 24 || t == 25) {
    int f = f_of();
    asm volatile("" : "+s"(f));
    const int lane = lane_id();
    const Dep d{nullptr, (unsigned)((PLN + (lane >> 3)) * 128 + (lane & 7) * 4), 1u, 0u};
    gemm7<R_F1>(smem, wave_id_, w1 + 64 * f, w3 + 64 * f, FF, ws + OFF_H2, 65536, d, ws + OFF_G + f * 4096, 56 * 4096, cnt, (PF1 + f) * 32, tmo, 128 + xq * 18 + 8 + f, mts, false);
  } else if (t >= 9 && t < 13) {
    fin_role(smem, wave_id_, xq * 4 + t - 9, mod, ws + OFF_P2, (const u16*)(ws + OFF_X1), out, cnt, tmo, mts);
    if (xq == (FL63 ? 0 : 3) && t == 12 && opaque_tid() == 0) {
      const u32x4 v = ld16_sc1(ws + OFF_TAG);
      const u64 tn = (((u64)v.y << 32) | v.x) + 1;
      st8_sc1(ws + OFF_TAG, u32x2{(unsigned)tn, (unsigned)(tn >> 32)});
    }
  } else if (t == 28 || t == 29) {
    row_role(smem, wave_id_, 8 * xq, t - 28, mod, ws + OFF_P, x, (u16*)(ws + OFF_X1), ws + OFF_H2, cnt, tmo, 256 + xq, mts);
  } else if (t >= 26) {
    sa_role(smem, wave_id_, 8 * xq + 2 * (t >= 30 ? t - 30 : t - 24), ws + OFF_Q, ws + OFF_K, ws + OFF_V, ws + OFF_KVM, ws + OFF_O, cnt, tmo, 128 + xq * 32 + 30 + (t & 1), mts);
  }
}

extern "C" void kernel_launch(void* const* d_in, const int* in_sizes, int n_in, void* d_out, int out_size, void* d_ws, size_t ws_size,
                              hipStream_t stream) {
  const u16* x    = (const u16*)d_in[0];
  const u16* mem0 = (const u16*)d_in[1];
  const u16* Wq   = (const u16*)d_in[2];
  const u16* Wk   = (const u16*)d_in[3];
  const u16* Wv   = (const u16*)d_in[4];
  const u16* Wo   = (const u16*)d_in[5];
  const u16* w1   = (const u16*)d_in[8];
  const u16* w3   = (const u16*)d_in[9];
  const u16* w2   = (const u16*)d_in[10];
  const u16* c    = (const u16*)d_in[11];
  const u16* wmod = (const u16*)d_in[12];
#define WSHIFT 0
  char* ws = (char*)d_ws + ((size_t)WSHIFT << 20);
  hipFuncSetAttribute((const void*)k_mega, hipFuncAttributeMaxDynamicSharedMemorySize, MEGA_LDS);
  k_mega<<<256, 512, MEGA_LDS, stream>>>(Wq, Wk, Wv, Wo, w1, w3, w2, x, c, wmod, mem0, ws, (u16*)d_out);
}
```

```cpp
#include <hip/hip_runtime.h>
#include <hip/hip_bf16.h>
#include <stdint.h>
#include <type_traits>
#pragma clang diagnostic ignored "-Winline-asm"
#pragma clang diagnostic ignored "-Wunused-value"

typedef unsigned short u16;
typedef short s16x4 __attribute__((ext_vector_type(4)));
typedef __bf16 bf16x8 __attribute__((ext_vector_type(8)));
typedef float f32x4 __attribute__((ext_vector_type(4)));
typedef float f32x2 __attribute__((ext_vector_type(2)));
typedef unsigned u32x4 __attribute__((ext_vector_type(4)));
typedef unsigned u32x2 __attribute__((ext_vector_type(2)));
#define LAS __attribute__((address_space(3)))

constexpr int SEQ = 2048, DM = 1024, NH = 32, DH = 128, HD = 4096, NMEM = 8, SEG = 32, NSEG = 64, FF = 3584;
constexpr unsigned SENT = 0xFFFFFFFFu;

__device__ __forceinline__ int lane_id() { int l; asm volatile("v_mbcnt_lo_u32_b32 %0, -1, 0\n\tv_mbcnt_hi_u32_b32 %0, -1, %0" : "=v"(l)); return l; }
#define WAVE_ID wave_id_
#define opaque_tid() (WAVE_ID * 64 + lane_id())
__device__ __forceinline__ float bf2f(u16 v) { return __builtin_bit_cast(float, (unsigned)v << 16); }
__device__ __forceinline__ float bflo(unsigned v) { return __builtin_bit_cast(float, v << 16); }
__device__ __forceinline__ float bfhi(unsigned v) { return __builtin_bit_cast(float, v & 0xffff0000u); }
__device__ __forceinline__ float bfr(float x) { return (float)(__bf16)x; }
__device__ __forceinline__ unsigned pack2(float a, float b) {
  typedef __bf16 bf16x2 __attribute__((ext_vector_type(2)));
  bf16x2 v; v[0] = (__bf16)a; v[1] = (__bf16)b; return __builtin_bit_cast(unsigned, v);
}
__device__ __forceinline__ unsigned nosent(unsigned v) { return v == SENT ? 0x7FC07FC0u : v; }
__device__ __forceinline__ bf16x8 asbf8(u32x4 v) { return __builtin_bit_cast(bf16x8, v); }
__device__ __forceinline__ s16x4 ass4(u32x2 v) { return __builtin_bit_cast(s16x4, v); }
__device__ __forceinline__ u32x2 trread(const char* lds_base, unsigned off) {
  s16x4 v = __builtin_amdgcn_ds_read_tr16_b64_v4i16((LAS s16x4*)(lds_base + off));
  return __builtin_bit_cast(u32x2, v);
}
__device__ __forceinline__ void glds16(const void* gsrc, unsigned lds_dst) {
  unsigned keep;
  asm volatile("s_mov_b32 %0, m0\n\ts_mov_b32 m0, %2\n\ts_nop 0\n\tglobal_load_lds_dwordx4 %1, off\n\ts_mov_b32 m0, %0"
               : "=&s"(keep) : "v"(gsrc), "s"(lds_dst) : "memory");
}
#define CNT_ " nt"
#define WNT_ " nt"
__device__ __forceinline__ void glds16_w(const void* gsrc, unsigned lds_dst) {
  unsigned keep;
  asm volatile("s_mov_b32 %0, m0\n\ts_mov_b32 m0, %2\n\ts_nop 0\n\tglobal_load_lds_dwordx4 %1, off" CNT_ "\n\ts_mov_b32 m0, %0"
               : "=&s"(keep) : "v"(gsrc), "s"(lds_dst) : "memory");
}
__device__ __forceinline__ void glds16_sc1(const void* gsrc, unsigned lds_dst) {
  unsigned keep;
  asm volatile("s_mov_b32 %0, m0\n\ts_mov_b32 m0, %2\n\ts_nop 0\n\tglobal_load_lds_dwordx4 %1, off sc1\n\ts_mov_b32 m0, %0"
               : "=&s"(keep) : "v"(gsrc), "s"(lds_dst) : "memory");
}
#define NTM 1
template <int B> __device__ __forceinline__ u32x4 ldnt(const void* p) { if (NTM & B) return __builtin_nontemporal_load((const u32x4*)p); else return *(const u32x4*)p; }
#define MFMA32(a, b, c) __builtin_amdgcn_mfma_f32_16x16x32_bf16((a), (b), (c), 0, 0, 0)

__device__ __forceinline__ void st16_sc1(void* p, u32x4 v) { asm volatile("global_store_dwordx4 %0, %1, off sc1\n\ts_nop 1" :: "v"(p), "v"(v) : "memory"); }
__device__ __forceinline__ void st8_sc1(void* p, u32x2 v) { asm volatile("global_store_dwordx2 %0, %1, off sc1\n\ts_nop 1" :: "v"(p), "v"(v) : "memory"); }
template <bool WT> __device__ __forceinline__ void st8(void* p, u32x2 v) { if (WT) st8_sc1(p, v); else *(u32x2*)p = v; }
template <bool WT> __device__ __forceinline__ void st16(void* p, u32x4 v) { if (WT) st16_sc1(p, v); else *(u32x4*)p = v; }
__device__ __forceinline__ u32x4 ld16_sc1(const void* p) {
  u32x4 r; asm volatile("global_load_dwordx4 %0, %1, off sc1\n\ts_waitcnt vmcnt(0)" : "=&v"(r) : "v"(p) : "memory"); return r;
}
__device__ __forceinline__ void ld16x4_sc1(const void* p, u32x4& a, u32x4& b, u32x4& c, u32x4& d) {
  asm volatile("global_load_dwordx4 %0, %4, off sc1\n\tglobal_load_dwordx4 %1, %4, off offset:1024 sc1\n\t"
               "global_load_dwordx4 %2, %4, off offset:2048 sc1\n\tglobal_load_dwordx4 %3, %4, off offset:3072 sc1\n\ts_waitcnt vmcnt(0)"
               : "=&v"(a), "=&v"(b), "=&v"(c), "=&v"(d) : "v"(p) : "memory");
}
typedef __amdgpu_buffer_rsrc_t rsrc_t;
__device__ __forceinline__ rsrc_t mkrs(const void* p) { return __builtin_amdgcn_make_buffer_rsrc((void*)p, 0, 0x7FFFFFFF, 0x00020000); }
__device__ __forceinline__ u32x4 bld16(rsrc_t r, unsigned off) { return __builtin_amdgcn_raw_buffer_load_b128(r, off, 0, 16); }
__device__ __forceinline__ u32x2 bld8(rsrc_t r, unsigned off) { return __builtin_amdgcn_raw_buffer_load_b64(r, off, 0, 16); }
__device__ __forceinline__ bool ok4(u32x4 v) { return v.x != SENT && v.y != SENT && v.z != SENT && v.w != SENT; }
constexpr int SPIN_CAP = 2000000;
__device__ __forceinline__ bool give_up(int spins, unsigned* tmo, unsigned code) {
  if ((spins & 4095) != 4095) return false;
  if (spins > SPIN_CAP) { __hip_atomic_store(tmo, code, __ATOMIC_RELAXED, __HIP_MEMORY_SCOPE_AGENT); return true; }
  return __hip_atomic_load(tmo, __ATOMIC_RELAXED, __HIP_MEMORY_SCOPE_AGENT) != 0u;
}
template <bool ACQ = true>
__device__ __forceinline__ void wait_cnt(unsigned* c, unsigned n, unsigned* tmo) {
  for (int spins = 0;; ++spins) {
    const unsigned v = __hip_atomic_load(c, __ATOMIC_RELAXED, __HIP_MEMORY_SCOPE_AGENT);
    if (v >= n) break;
    __builtin_amdgcn_s_sleep(8);
    if (give_up(spins, tmo, 5)) break;
  }
  if (ACQ) {
    __builtin_amdgcn_fence(__ATOMIC_ACQUIRE, "agent");
    asm volatile("s_waitcnt vmcnt(0)" ::: "memory");
  }
}

__device__ __forceinline__ const char* uni(const char* p) {
  const unsigned long long v = (unsigned long long)p;
  const unsigned lo = __builtin_amdgcn_readfirstlane((unsigned)v), hi = __builtin_amdgcn_readfirstlane((unsigned)(v >> 32));
  return (const char*)(((unsigned long long)hi << 32) | lo);
}
#define TSCLK "s_memrealtime"
#define TC(k) do { } while (0)
#define TS(k) do { } while (0)
#define TSDECL do { } while (0)
#define TSFLUSH(mask) do { } while (0)

constexpr size_t OFF_MOD  = 0;
constexpr size_t OFF_MOD2 = 65536;
constexpr size_t OFF_TMO  = 98304;
constexpr size_t OFF_TAG  = 131072;
constexpr size_t OFF_CLAIM = 135168;
constexpr size_t OFF_INIT = 196608;
constexpr size_t OFF_H1   = 262144;
constexpr size_t OFF_Q    = OFF_H1 + (size_t)SEQ * DM * 2;
constexpr size_t OFF_K    = OFF_Q + (size_t)SEQ * HD * 2;
constexpr size_t OFF_V    = OFF_K + (size_t)SEQ * HD * 2;
constexpr size_t OFF_O    = OFF_V + (size_t)SEQ * HD * 2;
constexpr size_t OFF_X1   = OFF_O + (size_t)SEQ * HD * 2;
constexpr size_t OFF_H2   = OFF_X1 + (size_t)SEQ * DM * 2;
constexpr size_t OFF_G    = OFF_H2 + (size_t)SEQ * DM * 2;
constexpr size_t OFF_KVM  = (size_t)92 << 20;
constexpr size_t OFF_XCH  = (size_t)100 << 20;
constexpr size_t OFF_P    = (size_t)112 << 20;
constexpr size_t OFF_P2   = (size_t)128 << 20;
constexpr size_t OFF_DUMP = (size_t)144 << 20;
constexpr int PROW = 4 * DM * 2;
#define TLQ 1
#define TLP 1
static_assert(OFF_G + (size_t)SEQ * FF * 2 <= OFF_KVM, "layout");
__device__ __forceinline__ char* xch_base(char* ws) { return (char*)(((size_t)(ws + OFF_XCH) + ((2u << 20) - 1)) & ~(size_t)((2u << 20) - 1)); }
#define XPAR 0
constexpr int PROG = 16;
constexpr int PQKV = 64;
constexpr int PL1 = 96;
constexpr int PL1B = 350;
constexpr int PSA = 128;
constexpr int PWO = 192;
constexpr int PLN = 224;
constexpr int PF2 = 320;
constexpr int PF1 = 448;
#define SDIR 40
#define SB 52
#define SC 60
#define MARG 20
#define MARL 15
#define MARF 8
#define SPRO 1
#define LAST 52
#define HB 61
#define HC 62
#define MARX 10
#define MARH 35
#define SAW 0
constexpr int L_RED = 131072;
constexpr int L_SLOT = L_RED + 16384;
constexpr int L_GB = L_SLOT + 1024;
constexpr int MEGA_LDS = 163840;
static_assert(L_GB + 12288 + 2048 <= MEGA_LDS, "lds");
#define XPARC 0

__device__ __forceinline__ int bswz(int row) { return (((row >> 1) & 1) | (((row >> 3) & 1) << 1)) << 1; }
template <int OFF, bool SC1 = false>
__device__ __forceinline__ void glds16s(const char* sbase, unsigned voff, unsigned lds_base) {
  unsigned keep;
  if (SC1)
    asm volatile("s_mov_b32 %0, m0\n\ts_add_u32 m0, %3, %4\n\ts_nop 2\n\tglobal_load_lds_dwordx4 %1, %2 sc1\n\ts_mov_b32 m0, %0"
                 : "=&s"(keep) : "v"(voff), "s"(sbase), "s"(lds_base), "i"(OFF) : "memory", "scc");
  else
    asm volatile("s_mov_b32 %0, m0\n\ts_add_u32 m0, %3, %4\n\ts_nop 2\n\tglobal_load_lds_dwordx4 %1, %2\n\ts_mov_b32 m0, %0"
                 : "=&s"(keep) : "v"(voff), "s"(sbase), "s"(lds_base), "i"(OFF) : "memory", "scc");
}
template <int OFF>
__device__ __forceinline__ void glds16x2s(const char* sbase, unsigned voff, unsigned lds_base) {
  unsigned keep;
  asm volatile("s_mov_b32 %0, m0\n\ts_add_u32 m0, %3, %4\n\ts_nop 2\n\tglobal_load_lds_dwordx4 %1, %2 sc1\n\tglobal_load_lds_dwordx4 %1, %2 offset:1024 sc1\n\ts_mov_b32 m0, %0"
               : "=&s"(keep) : "v"(voff), "s"(sbase), "s"(lds_base), "i"(OFF) : "memory", "scc");
}
__device__ __forceinline__ void st16s(const char* sb, unsigned vo, u32x4 v) { asm volatile("global_store_dwordx4 %0, %1, %2 sc1\n\ts_nop 1" :: "v"(vo), "v"(v), "s"(sb) : "memory"); }
__device__ __forceinline__ void head_to_lds(const char* src, int lane, unsigned lds) {
  if (TLQ) {
    const int c = (lane & 15) ^ (lane >> 4);
    const unsigned v0 = (unsigned)((c >> 2) * 2048 + (lane >> 4) * 64 + (c & 3) * 16);
#pragma unroll
    for (int i = 0; i < 8; ++i) glds16s<0, true>(src + i * 256, v0 ^ ((i & 3) * 2048), lds + i * 1024);
  } else {
#pragma unroll
    for (int i = 0; i < 4; ++i) glds16x2s<0>(src + i * 2048, (unsigned)lane * 16, lds + i * 2048);
  }
}
#define TCHP ""
template <bool NT = false> __device__ __forceinline__ void touch(const void* sb, unsigned vo, unsigned lds) {
  if (NT) asm volatile("s_mov_b32 m0, %0\n\ts_nop 0\n\tglobal_load_lds_dword %1, %2 nt" :: "s"(lds), "v"(vo), "s"(sb) : "memory", "m0");
  else
  asm volatile("s_mov_b32 m0, %0\n\ts_nop 0\n\tglobal_load_lds_dword %1, %2" TCHP :: "s"(lds), "v"(vo), "s"(sb) : "memory", "m0");
}
#define WAIT_V(n) asm volatile("s_waitcnt vmcnt(" #n ")" ::: "memory")
#define BAR() do { asm volatile("s_waitcnt lgkmcnt(0)" ::: "memory"); __builtin_amdgcn_s_barrier(); asm volatile("" ::: "memory"); } while (0)
__device__ __forceinline__ void st4_sc1(void* p, unsigned v) { asm volatile("global_store_dword %0, %1, off sc1" :: "v"(p), "v"(v) : "memory"); }
__device__ __forceinline__ unsigned ld4_sc1(const void* p) {
  unsigned r; asm volatile("global_load_dword %0, %1, off sc1\n\ts_waitcnt vmcnt(0)" : "=&v"(r) : "v"(p) : "memory"); return r;
}
template <int CTRL> __device__ __forceinline__ unsigned dppu(unsigned x) { return (unsigned)__builtin_amdgcn_update_dpp(0, (int)x, CTRL, 0xf, 0xf, false); }
__device__ __forceinline__ unsigned rowmin16(unsigned v) {
  v = min(v, dppu<0x128>(v)); v = min(v, dppu<0x124>(v)); v = min(v, dppu<0x122>(v)); v = min(v, dppu<0x121>(v));
  return v;
}
__device__ __forceinline__ unsigned rdl(unsigned v, int l) { return (unsigned)__builtin_amdgcn_readlane((int)v, l); }
__device__ __forceinline__ unsigned qkv_avail(unsigned v, int m) {
  unsigned a = rdl(v, m);
#pragma unroll
  for (int j = 1; j < 4; ++j) { const unsigned p = rdl(v, 4 * j + m); if ((p >> 8) <= a) a = max(a, p & 255u); }
  return a;
}
constexpr float SCALE = 0.08838834764831845f;
constexpr float LOG2E = 1.4426950408889634f;
template <int N> __device__ __forceinline__ void delay64() {
  if (N >= 16) { __builtin_amdgcn_s_sleep(15); delay64<(N >= 16 ? N - 15 : 0)>(); } else if (N > 0) __builtin_amdgcn_s_sleep(N);
}
__device__ __forceinline__ void sw16(float x, float& a, float& b) {
  a = x; b = x; asm volatile("s_nop 1\n\tv_permlane16_swap_b32 %0, %1" : "+v"(a), "+v"(b));
}
__device__ __forceinline__ void sw32(float x, float& a, float& b) {
  a = x; b = x; asm volatile("s_nop 1\n\tv_permlane32_swap_b32 %0, %1" : "+v"(a), "+v"(b));
}
__device__ __forceinline__ float red16_32_max(float x) {
  float a, b; sw16(x, a, b); x = fmaxf(a, b); sw32(x, a, b); return fmaxf(a, b);
}
__device__ __forceinline__ float red16_32_add(float x) {
  float a, b; sw16(x, a, b); x = a + b; sw32(x, a, b); return a + b;
}
__device__ __forceinline__ unsigned cvtpk(float a, float b) { unsigned r; asm("v_cvt_pk_bf16_f32 %0, %1, %2" : "=v"(r) : "v"(a), "v"(b)); return r; }
__device__ __forceinline__ float max3(float a, float b, float c) { float r; asm("v_max3_f32 %0, %1, %2, %3" : "=v"(r) : "v"(a), "v"(b), "v"(c)); return r; }
__device__ __forceinline__ float max2(float a, float b) { float r; asm("v_max_f32 %0, %1, %2" : "=v"(r) : "v"(a), "v"(b)); return r; }
__device__ __forceinline__ unsigned ror8u(unsigned x) { return (unsigned)__builtin_amdgcn_update_dpp(0, (int)x, 0x128, 0xf, 0xf, false); }
__device__ __forceinline__ float ror8(float x) { return __builtin_bit_cast(float, ror8u(__builtin_bit_cast(unsigned, x))); }
__device__ __forceinline__ float add_ror8(float x) {
  return x + __builtin_bit_cast(float, __builtin_amdgcn_update_dpp(0, __builtin_bit_cast(int, x), 0x128, 0xf, 0xf, false));
}

__device__ __forceinline__ unsigned rt_now() { return (unsigned)__builtin_amdgcn_s_memrealtime(); }
__device__ __forceinline__ unsigned latest(unsigned v) {
  const unsigned now = rt_now() + 400u, age = rowmin16((now - v) & 0xFFFFFFu);
  return now - min(min(min(rdl(age, 0), rdl(age, 16)), min(rdl(age, 32), rdl(age, 48))), 900u);
}
__device__ __forceinline__ void wait_rt(unsigned t0, int d) {
  while ((int)(rt_now() - t0) < d) { }
}
__device__ __forceinline__ bool bells(bool ok, unsigned u, int b, int marg, int sb = SB) {
  if (b < sb || !__all(ok || (int)(u >> 24) > b)) return false;
  wait_rt(latest(!ok && (int)(u >> 24) == b + 1 ? u : rt_now() - 400u), marg);
  return true;
}
__device__ __forceinline__ void ld_fb(unsigned& v, unsigned& u, const void* sb, unsigned vo) {
  asm volatile("global_load_dword %0, %2, %3 sc1\n\tglobal_load_dword %1, %2, %3 offset:64 sc1\n\ts_waitcnt vmcnt(0)" : "=&v"(v), "=&v"(u) : "v"(vo), "s"(sb) : "memory");
}
__device__ __forceinline__ unsigned bell(int b, unsigned t) { return (unsigned)(b + 1) << 24 | (t & 0xFFFFFFu); }
typedef unsigned long long u64;
__device__ __forceinline__ unsigned tag2(u64 tag) { return (unsigned)(tag >> 32) ^ ((unsigned)tag * 0x9E3779B1u); }
__device__ __forceinline__ unsigned init_eval(u32x4 v, u64 tag) {
  if (!__all((v.x & ~63u) == (unsigned)tag << 6 && v.y == (unsigned)(tag >> 26))) return 0u;
  unsigned r = 8u;
#pragma unroll
  for (int c = 0; c < 3; ++c) r |= __popcll(__ballot(v.x & (1u << c))) > __popcll(__ballot(v.x & (8u << c))) ? 1u << c : 0u;
  return r ^ (XPAR * 3u + XPARC * 4u);
}
__device__ __forceinline__ unsigned init_wait(const char* ws, u64 tag) {
  for (int spins = 0; spins < SPIN_CAP; ++spins) {
    const unsigned r = init_eval(ld16_sc1(ws + OFF_INIT + lane_id() * 128), tag);
    if (r) return r;
    __builtin_amdgcn_s_sleep(4);
  }
  return 8u;
}
__device__ __forceinline__ void init_issue(const char* ws, unsigned lds) { glds16s<0, true>(uni(ws + OFF_INIT), (unsigned)lane_id() * 128u, lds); }
__device__ __forceinline__ unsigned init_get(const char* ws, u64 tag, const char* copy) {
  const unsigned r = init_eval(*(volatile LAS u32x4*)(copy + lane_id() * 16), tag);
  return r ? r : init_wait(ws, tag);
}
__device__ __forceinline__ unsigned* cnt_of(char* ws, unsigned in) { return (unsigned*)uni(xch_base(ws) + ((size_t)4 << 21) + (size_t)((in & 4u) << 11)); }
__device__ __forceinline__ void wait_l1(const unsigned* cnt, unsigned n, unsigned* tmo) {
  const int lane = lane_id();
  for (int spins = 0;; ++spins) {
    const unsigned v = ld4_sc1(cnt + (PL1 + (lane & 31)) * 32);
    if (__all(v >= n)) break;
    __builtin_amdgcn_s_sleep(8);
    if (give_up(spins, tmo, 12)) break;
  }
}
#define GKV 1
__device__ __forceinline__ void wait_k0(const unsigned* cnt, int h, unsigned* tmo) {
  for (int spins = 0;; ++spins) {
    const int l = lane_id();
    const unsigned v = ld4_sc1(cnt + (PQKV + h) * 32 + 1 + (l & 1));
    if (__all(v >= (unsigned)GKV)) break;
    __builtin_amdgcn_s_sleep(32);
    if (give_up(spins, tmo, 13)) break;
  }
}
#define WCLK(slot, b) do { } while (0)
#define WTM(slot, b, k) do { } while (0)
#define WTS(slot, b, k) do { } while (0)
#define WFB() do { } while (0)
#define STS(ws_, slot_, k_) do { } while (0)
typedef __bf16 bf16x2 __attribute__((ext_vector_type(2)));
__device__ __forceinline__ f32x2 pkfma(f32x2 a, f32x2 b, f32x2 c) { return __builtin_elementwise_fma(a, b, c); }
__device__ __forceinline__ float wavesum(float v) {
  v += __builtin_bit_cast(float, dppu<0x128>(__builtin_bit_cast(unsigned, v))); v += __builtin_bit_cast(float, dppu<0x124>(__builtin_bit_cast(unsigned, v)));
  v += __builtin_bit_cast(float, dppu<0x122>(__builtin_bit_cast(unsigned, v))); v += __builtin_bit_cast(float, dppu<0x121>(__builtin_bit_cast(unsigned, v)));
  return red16_32_add(v);
}
__device__ __forceinline__ float adds(unsigned v, unsigned sel, float c) { return __builtin_amdgcn_fdot2_f32_bf16(__builtin_bit_cast(bf16x2, v), __builtin_bit_cast(bf16x2, sel), c, false); }
__device__ __forceinline__ void sum4(const u32x4 (&p)[4], f32x2 (&a)[4]) {
  unsigned slo = 0x00003F80u, shi = 0x3F800000u;
  asm("" : "+s"(slo), "+s"(shi));
#pragma unroll
  for (int e = 0; e < 4; ++e) {
    float lo = bflo(p[0][e]), hi = bfhi(p[0][e]);
#pragma unroll
    for (int q = 1; q < 4; ++q) { lo = adds(p[q][e], slo, lo); hi = adds(p[q][e], shi, hi); }
    a[e] = f32x2{lo, hi};
  }
}
__device__ __forceinline__ u32x4 norm8(const f32x2 (&v)[4], const char* mt, float mb, float rden) {
  const f32x4 h0 = *(const f32x4*)(mt + 4096), h1 = *(const f32x4*)(mt + 4096 + 1024), c0 = *(const f32x4*)(mt + 8192), c1 = *(const f32x4*)(mt + 8192 + 1024);
  const f32x2 m2 = f32x2{mb, mb}, d2 = f32x2{rden, rden};
  const f32x2 r0 = pkfma(v[0] - m2, f32x2{c0[0], c0[1]} * d2, f32x2{h0[0], h0[1]}), r1 = pkfma(v[1] - m2, f32x2{c0[2], c0[3]} * d2, f32x2{h0[2], h0[3]});
  const f32x2 r2 = pkfma(v[2] - m2, f32x2{c1[0], c1[1]} * d2, f32x2{h1[0], h1[1]}), r3 = pkfma(v[3] - m2, f32x2{c1[2], c1[3]} * d2, f32x2{h1[2], h1[3]});
  return u32x4{pack2(r0[0], r0[1]), pack2(r1[0], r1[1]), pack2(r2[0], r2[1]), pack2(r3[0], r3[1])};
}
__device__ __forceinline__ void half_load(const char* P, const u16* res, int row, int hf, int lane, f32x2 (&a)[4], u32x4& rv) {
  const rsrc_t rp = mkrs(P), rr = mkrs(res);
  const unsigned po = TLP ? (unsigned)((row >> 5) * 32 * PROW + (hf * 16 + (lane >> 2)) * 2048 + (row & 31) * 64 + (lane & 3) * 16) : (unsigned)(row * PROW + hf * 1024 + lane * 16), ro = (unsigned)(row * DM * 2 + hf * 1024 + lane * 16);
  u32x4 p[4];
#pragma unroll
  for (int q = 0; q < 4; ++q) p[q] = bld16(rp, po + q * (TLP ? 65536 : 2048));
  if (res) rv = bld16(rr, ro);
  sum4(p, a);
}
__device__ __forceinline__ bool fin_half(bool fin, const char* P, const u16* res, const char* gbt, u16* __restrict__ o1, int row, int hf, int lane) {
  f32x2 a[4]; u32x4 rv;
  half_load(P, res, row, hf, lane, a, rv);
  const u32x4 gv = *(const u32x4*)(gbt + hf * 1024 + lane * 16);
  u32x4 o; f32x2 t = f32x2{0, 0};
#pragma unroll
  for (int e = 0; e < 4; ++e) { const f32x2 s = pkfma(f32x2{bflo(gv[e]), bfhi(gv[e])}, a[e], f32x2{bflo(rv[e]), bfhi(rv[e])}); o[e] = pack2(s[0], s[1]); t += s; }
  if (!fin && !__all(t[0] == t[0] && t[1] == t[1])) return false;
  st16_sc1(o1 + (size_t)row * DM + hf * 512 + lane * 8, o);
  return true;
}
#define LSL 2
#define FSL 2
#define GSL 2
#define XSL 2
#define DLN 10
#define LEB 64
#define MARE 25
#define LNE 1
template <int CTRL> __device__ __forceinline__ float dppf(float x) { return __builtin_bit_cast(float, dppu<CTRL>(__builtin_bit_cast(unsigned, x))); }
__device__ __forceinline__ bool ln_row(bool fin, int bb, unsigned* bl, const f32x2 (&a)[2][4], const f32x2 (&xf)[8], const f32x2 (&tg)[8], const f32x2 (&th)[8], const f32x2 (&tc)[8],
                                       const char* x1, const char* h2, unsigned xo, unsigned ho) {
  f32x2 v[8]; u32x4 o[2];
#pragma unroll
  for (int e = 0; e < 8; ++e) { const f32x2 t = pkfma(tg[e], a[e >> 2][e & 3], xf[e]); const unsigned u = pack2(t[0], t[1]); o[e >> 2][e & 3] = u; v[e] = f32x2{bflo(u), bfhi(u)}; }
  const float pv = __builtin_bit_cast(float, __builtin_amdgcn_readfirstlane(__float_as_int(v[0][0])));
  f32x2 d[8];
#pragma unroll
  for (int e = 0; e < 8; ++e) d[e] = v[e] - f32x2{pv, pv};
  const f32x2 s2 = ((d[0] + d[1]) + (d[2] + d[3])) + ((d[4] + d[5]) + (d[6] + d[7]));
  const f32x2 q2 = pkfma(d[3], d[3], pkfma(d[2], d[2], pkfma(d[1], d[1], d[0] * d[0]))) + pkfma(d[7], d[7], pkfma(d[6], d[6], pkfma(d[5], d[5], d[4] * d[4])));
  float s = s2[0] + s2[1], q = q2[0] + q2[1];
  if (!fin && !__all(s == s)) return false;
  if (LNE && bb >= SB && bl && lane_id() == 0) st4_sc1(bl, bell(bb, rt_now() + DLN + 30));
  s += dppf<0x128>(s); q += dppf<0x128>(q); s += dppf<0x124>(s); q += dppf<0x124>(q);
  s += dppf<0x122>(s); q += dppf<0x122>(q); s += dppf<0x121>(s); q += dppf<0x121>(q);
  asm volatile("s_nop 1\n\tv_permlane16_swap_b32 %0, %1" : "+v"(s), "+v"(q));
  s += q;
  { float x, y; sw32(s, x, y); s = x + y; }
  const float sh = __builtin_bit_cast(float, rdl(__builtin_bit_cast(unsigned, s), 0)) * (1.0f / DM), sq = __builtin_bit_cast(float, rdl(__builtin_bit_cast(unsigned, s), 16));
  const float mb = bfr(pv + sh), vb = bfr(fmaxf(sq * (1.0f / DM) - sh * sh, 0.0f));
  const float rden = __builtin_amdgcn_rcpf(bfr(__builtin_amdgcn_sqrtf(bfr(vb + 1e-5f))));
#pragma unroll
  for (int i = 0; i < 2; ++i) {
    u32x4 r;
#pragma unroll
    for (int e = 0; e < 4; ++e) { const f32x2 t = pkfma(v[4 * i + e] - f32x2{mb, mb}, tc[4 * i + e] * f32x2{rden, rden}, th[4 * i + e]); r[e] = pack2(t[0], t[1]); }
    st16s(h2, ho + i * 32768, r);
  }
  if (!LNE && bb >= SB && lane_id() == 0) st4_sc1(bl, bell(bb, rt_now() + DLN));
  st16s(x1, xo, o[0]); st16s(x1, xo + 1024, o[1]);
  return true;
}
__device__ __forceinline__ void row_table(char* tab, const float* mod, const unsigned* cnt, unsigned* tmo, int tid) {
  wait_l1(cnt + 1, 1u, tmo);
  const rsrc_t rm = mkrs((const char*)mod + OFF_MOD2);
#pragma unroll
  for (int i = 0; i < 2; ++i) {
    const int u = tid + 512 * i, a = u >> 8, c = u & 255;
    f32x4 v = __builtin_bit_cast(f32x4, bld16(rm, (unsigned)u * 16));
    v = f32x4{bfr(v[0]), bfr(v[1]), bfr(v[2]), bfr(v[3])};
    if (a == 2) v = f32x4{bfr(1.0f + v[0]), bfr(1.0f + v[1]), bfr(1.0f + v[2]), bfr(1.0f + v[3])};
    if (a < 3) *(f32x4*)(tab + a * 4096 + ((c >> 7) * 2 + (c & 1)) * 1024 + ((c >> 1) & 63) * 16) = v;
    else *(u32x2*)(tab + 12288 + c * 8) = u32x2{pack2(v[0], v[1]), pack2(v[2], v[3])};
  }
  __syncthreads();
}
__device__ __forceinline__ void row_role(char* smem, int wave_id_, int rbase, int par, const float* mod, const char* P, const u16* x, u16* x1, char* h2c,
                                         unsigned* cnt, unsigned* tmo, int slot, unsigned long long* mts) {
  const int tid = opaque_tid(), lane = tid & 63, wave = WAVE_ID;
  if (tid < 80) *(volatile LAS unsigned*)(smem + 16384 + tid * 4) = 0u;
  {
    static_assert(TLP && SB % 2 == 0, "");
    unsigned z = SENT; asm volatile("" : "+v"(z));
    const int r4 = rbase + 4 * par + (wave & 3);
    for (int bb = SB + (wave >> 2); bb < NSEG; bb += 2)
#pragma unroll
      for (int i = 0; i < 2; ++i) {
        st16s(uni((const char*)x1 + (size_t)bb * 65536), (unsigned)(r4 * DM * 2 + i * 1024 + lane * 16), u32x4{z, z, z, z});
        st16s(uni(h2c + (size_t)bb * 65536), (unsigned)(r4 * 128 + (lane & 7) * 16 + (lane >> 3) * 4096 + i * 32768), u32x4{z, z, z, z});
      }
    WAIT_V(0);
  }
  row_table(smem, mod, cnt, tmo, tid);
  const int rb = rbase + wave + 32 * par;
  const rsrc_t rp = mkrs(P), rr = mkrs(x);
  const unsigned l16 = lane * 16;
  const unsigned pwo = (unsigned)(PWO + (lane & 31)) * 128;
  unsigned* myp = cnt + (PLN + (rb >> 3)) * 32 + wave;
  f32x2 a[2][4];
  const int gp = wave >> 2, r4 = rbase + 4 * par + (wave & 3);
  volatile LAS unsigned* lwb = (volatile LAS unsigned*)(smem + 16384 + gp * 8);
  if (gp && lane == 0) st4_sc1(myp, 1u);
  f32x2 tg[8], th[8], tc[8];
#pragma unroll
  for (int i = 0; i < 4; ++i) {
    const char* mt = smem + lane * 16 + i * 1024;
    const f32x4 g_ = *(const f32x4*)mt, h_ = *(const f32x4*)(mt + 4096), c_ = *(const f32x4*)(mt + 8192);
    tg[2 * i] = f32x2{g_[0], g_[1]}; tg[2 * i + 1] = f32x2{g_[2], g_[3]}; th[2 * i] = f32x2{h_[0], h_[1]}; th[2 * i + 1] = f32x2{h_[2], h_[3]};
    tc[2 * i] = f32x2{c_[0], c_[1]}; tc[2 * i + 1] = f32x2{c_[2], c_[3]};
  }
  const unsigned xo = (unsigned)(r4 * DM * 2 + lane * 16), ho = (unsigned)(r4 * 128 + (((lane & 7) ^ (r4 & 7)) << 4) + (lane >> 3) * 4096);
  const unsigned po = (l16 >> 6) * 2048 + (l16 & 48);
  u32x4 rv[2];
#pragma unroll
  for (int i = 0; i < 2; ++i) rv[i] = __builtin_amdgcn_raw_buffer_load_b128(rr, xo + i * 1024, (unsigned)gp * 65536u, 0);
  unsigned wb = 0, d0 = 150, d1 = 150;
  for (int bb = gp; bb < NSEG; bb += 2) {
    f32x2 xf[8];
#pragma unroll
    for (int e = 0; e < 8; ++e) xf[e] = f32x2{bflo(rv[e >> 2][e & 3]), bfhi(rv[e >> 2][e & 3])};
    asm volatile("" : "+v"(xf[0]), "+v"(xf[1]), "+v"(xf[2]), "+v"(xf[3]), "+v"(xf[4]), "+v"(xf[5]), "+v"(xf[6]), "+v"(xf[7]));
    if (wb <= (unsigned)bb) {
      if ((wave & 3) == 0) {
        wb = (unsigned)(bb + 1);
        for (int spins = 0;; ++spins) {
          unsigned v, u;
          ld_fb(v, u, cnt, pwo);
          if (__all(v > (unsigned)bb + 2u)) { v = rowmin16(v); wb = min(rdl(v, 0), rdl(v, 16)); break; }
          if (__all(v > (unsigned)bb)) break;
          if (bb >= LEB && __all(v > (unsigned)bb || (u >> 24) > (unsigned)bb)) {
            const unsigned now = rt_now(), g0 = latest(v <= (unsigned)bb && (u >> 24) == (unsigned)bb + 1u ? u : now - 400u) + MARE, go = (int)(g0 - now) > 0 ? g0 : now;
            if (lane < 4) st4_sc1(myp + 16 + lane, bell(bb, go + max(d0, d1)));
            wait_rt(go, 0);
            wb |= 256u;
            break;
          }
          if (bells(v > (unsigned)bb, u, bb, MARL)) break;
          __builtin_amdgcn_s_sleep(LSL);
          if (give_up(spins, tmo, 11)) break;
        }
        lwb[0] = wb;
      } else {
        while (((wb = __builtin_amdgcn_readfirstlane(lwb[0])) & 255u) <= (unsigned)bb) __builtin_amdgcn_s_sleep(1);
      }
    }
    const bool rung = wb == (unsigned)bb + 257u;
    wb &= 255u;
    const unsigned t0 = rt_now();
    WTM(slot, bb, 0);
    bool fin = bb < SB;
    for (int spins = 0;; ++spins) {
      u32x4 pp[2][4];
#pragma unroll
      for (int i = 0; i < 2; ++i)
#pragma unroll
        for (int q = 0; q < 4; ++q) pp[i][q] = __builtin_amdgcn_raw_buffer_load_b128(rp, po + i * 32768, (unsigned)(bb * 32 * PROW + q * 65536 + r4 * 64), 16);
      sum4(pp[0], a[0]); sum4(pp[1], a[1]);
      if (ln_row(fin, bb, rung && spins == 0 ? nullptr : myp + 16, a, xf, tg, th, tc, uni((const char*)x1 + (size_t)bb * 65536), uni(h2c + (size_t)bb * 65536), xo, ho)) break;
      if (spins > 1) fin = __all(ld4_sc1((const char*)cnt + pwo) > (unsigned)bb) || give_up(spins, tmo, 11);
    }
    d1 = d0; d0 = min(rt_now() - t0 + 12u, 200u);
    WTM(slot, bb, 3);
#pragma unroll
    for (int i = 0; i < 2; ++i) rv[i] = __builtin_amdgcn_raw_buffer_load_b128(rr, xo + i * 1024, (unsigned)min(bb + 2, NSEG - 1) * 65536u, 0);
    WAIT_V(2);
    unsigned o = 0;
    if (lane == 0) o = __hip_atomic_fetch_add((LAS unsigned*)(smem + 16448 + bb * 4), 1u, __ATOMIC_RELAXED, __HIP_MEMORY_SCOPE_WORKGROUP);
    if (__builtin_amdgcn_readfirstlane(o) == 3u) {
      if (lane < 4) st4_sc1(myp - (wave & 3) + lane, (unsigned)(bb + 2));
      WTM(slot, bb, 2);
    }
  }
}
#define NCS 8
__device__ __forceinline__ void l1_role(char* smem, int wave_id_, bool fst, int c, const u16* __restrict__ x, const float* __restrict__ mod,
                                        const u16* __restrict__ mem0, char* __restrict__ h1c, unsigned*& cnt, u64 tag, unsigned* tmo, unsigned long long* mts) {
  const int tid = opaque_tid(), lane = tid & 63, wave = WAVE_ID;
  char* ws = (char*)mod - OFF_MOD;
  u32x4 xr[7][2];
  const int row0 = (fst ? 0 : 256) + 8 * c + wave;
  const u16* src = x + (size_t)row0 * DM + lane * 8;
  xr[0][0] = ldnt<2>(src); xr[0][1] = ldnt<2>(src + 512);
  volatile LAS unsigned* sg = (volatile LAS unsigned*)(smem + L_SLOT + 784);
  if (!fst) {
#pragma unroll
    for (int pp = 1; pp < 7; ++pp) { xr[pp][0] = ldnt<2>(src + (size_t)pp * 256 * DM); xr[pp][1] = ldnt<2>(src + (size_t)pp * 256 * DM + 512); }
  } else if ((wave & 3) != 3) {
    char* cal = xch_base(ws) + ((size_t)((wave & 3) == 0 ? 0 : (wave & 3) + 2) << 21) + (124 + (wave >> 2) * 2 + (c >> 4)) * 16384 + (c & 15) * 512;
    st16_sc1(cal + (lane >> 5) * 8192 + (lane & 31) * 16, u32x4{SENT, SENT, SENT, SENT});
    WAIT_V(0);
    unsigned best[2] = {1u << 30, 1u << 30};
    const char* sb = uni(cal);
#pragma unroll
    for (int r = 1; r < NCS; ++r) {
      unsigned long long t0, t1; unsigned v;
      asm volatile("s_memtime %0\n\ts_waitcnt lgkmcnt(0)\n\ts_load_dword %2, %3, %4 glc\n\ts_waitcnt lgkmcnt(0)\n\ts_memtime %1\n\ts_waitcnt lgkmcnt(0)"
                   : "=&s"(t0), "=&s"(t1), "=&s"(v) : "s"(sb), "i"((r & 1) * 8192 + (r >> 1) * 128) : "memory");
      if (r > 1) best[r & 1] = min(best[r & 1], (unsigned)(t1 - t0));
    }
    sg[2 * wave] = best[0]; sg[2 * wave + 1] = best[1];
  } else {
    const int pc = (c & 7) * 2 + (wave >> 2), g = lane >> 4, hf = (lane >> 2) & 1, row = ((lane >> 3) & 1) * 4 + (lane & 3);
    u32x4 v = *(const u32x4*)(mem0 + row * DM + 32 * (2 * pc + hf) + 8 * g);
    v.x = nosent(v.x); v.y = nosent(v.y); v.z = nosent(v.z); v.w = nosent(v.w);
    st16_sc1(xch_base(ws) + ((size_t)3 << 21) + (size_t)(((c >> 3) & 1) * 2 + (pc >> 3)) * 16384 + (c >> 4) * 8192 + (pc & 7) * 1024 + lane * 16, v);
    const int q = c * 2 + (wave >> 2);
    if (q < 36) {
      char* p = xch_base(ws) + (q < 16 ? ((size_t)3 << 21) + 48 * 16384 + q * 4096 : ((size_t)4 << 21) + (q - 16) * 4096) + lane * 16;
#pragma unroll
      for (int i = 0; i < 4; ++i) st16_sc1(p + i * 1024, u32x4{0u, 0u, 0u, 0u});
    } else if (q == 36 && lane == 0) st4_sc1(tmo, 0u);
  }
  if (fst && wave == 0) STS(ws, 128 + (c >> 3) * 32 + 24 + (c & 7), 2);
  {
    f32x4 v;
    for (int spins = 0; spins < SPIN_CAP; ++spins) {
      u32x4 m[4];
      asm volatile("s_nop 4\n\tglobal_load_dwordx4 %0, %4, %6 sc1\n\tglobal_load_dwordx4 %1, %4, %6 offset:16 sc1\n\t"
                   "global_load_dwordx4 %2, %5, %6 sc1\n\tglobal_load_dwordx4 %3, %5, %6 offset:16 sc1\n\ts_waitcnt vmcnt(0)"
                   : "=&v"(m[0]), "=&v"(m[1]), "=&v"(m[2]), "=&v"(m[3]) : "v"((unsigned)tid * 32), "v"((unsigned)tid * 32 + 16384), "s"(mod) : "memory");
      bool ok = true;
#pragma unroll
      for (int i = 0; i < 4; ++i) ok = ok && m[i].y == (unsigned)tag && m[i].w == tag2(tag);
      v = f32x4{__uint_as_float(m[0].x) + __uint_as_float(m[2].x), __uint_as_float(m[0].z) + __uint_as_float(m[2].z),
                __uint_as_float(m[1].x) + __uint_as_float(m[3].x), __uint_as_float(m[1].z) + __uint_as_float(m[3].z)};
      if (__all(ok)) break;
      __builtin_amdgcn_s_sleep(4);
    }
    v = f32x4{bfr(v[0]), bfr(v[1]), bfr(v[2]), bfr(v[3])};
    const int a = tid >> 8, cc = tid & 255;
    if (a) v = f32x4{bfr(1.0f + v[0]), bfr(1.0f + v[1]), bfr(1.0f + v[2]), bfr(1.0f + v[3])};
    *(f32x4*)(smem + 4096 + a * 4096 + ((cc >> 7) * 2 + (cc & 1)) * 1024 + ((cc >> 1) & 63) * 16) = v;
  }
  BAR();
  if (fst && wave == 0) STS(ws, 128 + (c >> 3) * 32 + 24 + (c & 7), 3);
#pragma unroll
  for (int p = 0; p < 7; ++p) {
    if (p > 0 && fst) break;
    f32x2 v[2][4];
#pragma unroll
    for (int i = 0; i < 2; ++i)
#pragma unroll
      for (int e = 0; e < 4; ++e) v[i][e] = f32x2{bflo(xr[p][i][e]), bfhi(xr[p][i][e])};
    const f32x2 s2 = ((v[0][0] + v[0][1]) + (v[0][2] + v[0][3])) + ((v[1][0] + v[1][1]) + (v[1][2] + v[1][3]));
    const float mean = wavesum(s2[0] + s2[1]) * (1.0f / DM);
    f32x2 q2 = f32x2{0, 0};
#pragma unroll
    for (int i = 0; i < 8; ++i) { const f32x2 d = v[i >> 2][i & 3] - f32x2{mean, mean}; q2 = pkfma(d, d, q2); }
    const float q = wavesum(q2[0] + q2[1]);
    if (p == 0 && fst && wave == 0) STS((char*)tmo - OFF_TMO, 128 + (c >> 3) * 32 + 24 + (c & 7), 5);
    const float mb = bfr(mean), vb = bfr(q * (1.0f / DM));
    const float rden = 1.0f / bfr(sqrtf(bfr(vb + 1e-5f)));
    const int row = 256 * p + row0, rb = row & 31;
    char* dst = h1c + (size_t)(row >> 5) * 65536 + (lane >> 3) * 4096 + rb * 128 + (((lane & 7) ^ (rb & 7)) << 4);
    const char* ml = smem + lane * 16;
#pragma unroll
    for (int i = 0; i < 2; ++i) st16_sc1(dst + i * 32768, norm8(v[i], ml + i * 2048, mb, rden));
  }
  if (fst) {
    unsigned votes = 0, xcc; asm volatile("s_getreg_b32 %0, hwreg(HW_REG_XCC_ID)" : "=s"(xcc));
#pragma unroll
    for (int i = 0; i < 3; ++i) {
      const unsigned b0 = min(sg[2 * i], sg[2 * i + 8]), b1 = min(sg[2 * i + 1], sg[2 * i + 9]), df = max(b0, b1) - min(b0, b1);
      votes |= df < 110u || df > 400u ? 0u : (((b1 < b0) != ((xcc & 4) != 0)) ? 1u : 8u) << i;
    }
    WAIT_V(0);
    __builtin_amdgcn_s_barrier();
    if (tid == 0) st8_sc1(ws + OFF_INIT + c * 128, u32x2{(unsigned)tag << 6 | votes, (unsigned)(tag >> 26)});
  } else {
    cnt = cnt_of(ws, init_wait(ws, tag));
    WAIT_V(0);
    __builtin_amdgcn_s_barrier();
    if (tid == 0) st4_sc1(cnt + (PL1B + c) * 32, 7u);
  }
  if (wave == 0) STS(ws, 128 + (c >> 3) * 32 + 24 + (c & 7), fst ? 6 : 7);
}
__device__ __forceinline__ void arm_job(int wave_id_, int j, u64 tag, char* ws) {
  const int hb = j + 32 * WAVE_ID, b = hb >> 1;
  if (b < 80) {
    const char* p = uni(xch_base(ws) + (b < 48 ? (size_t)((b >> 4) * 32 + (b & 15)) * 16384
                                      : ((size_t)3 << 21) + (size_t)(b < 56 ? 8 + ((b - 48) >> 2) * 8 + (b & 3) : b - 56 + 24) * 16384) + (hb & 1) * 8192);
#pragma unroll
    for (int i = 0; i < 8; ++i) st16s(p, (unsigned)(i * 1024 + lane_id() * 16), u32x4{SENT, SENT, SENT, SENT});
  }
  WAIT_V(0);
  BAR();
  if (opaque_tid() == 0) st8_sc1(ws + OFF_INIT + (32 + j) * 128, u32x2{(unsigned)tag << 6, (unsigned)(tag >> 26)});
}
__device__ __forceinline__ void mod2_job(char* smem, int wave_id_, int c, const float* __restrict__ mod, const u16* __restrict__ cvec, const u16* __restrict__ wmod, unsigned* cnt) {
  const int tid = opaque_tid();
  {
    const float sva = bf2f(cvec[tid]), svb = bf2f(cvec[tid + 512]);
    float* sv = (float*)(smem + 12288);
    sv[tid] = bfr(sva / (1.0f + __expf(-sva))); sv[tid + 512] = bfr(svb / (1.0f + __expf(-svb)));
  }
  __syncthreads();
  {
    const int cg = tid & 15, r0 = tid >> 4;
    const u16* wp = wmod + (size_t)r0 * (6 * DM) + 2 * DM + 128 * c + cg * 8;
    const float* sv = (const float*)(smem + 12288) + r0;
    f32x2 acc[4] = {f32x2{0, 0}, f32x2{0, 0}, f32x2{0, 0}, f32x2{0, 0}};
#pragma unroll 1
    for (int o = 0; o < 4; ++o) {
      u32x4 w[8];
#pragma unroll
      for (int i = 0; i < 8; ++i) w[i] = ldnt<1>(wp + (size_t)(o * 8 + i) * 32 * (6 * DM));
#pragma unroll
      for (int i = 0; i < 8; ++i) {
        const float s = sv[(o * 8 + i) * 32];
#pragma unroll
        for (int e = 0; e < 4; ++e) acc[e] = pkfma(f32x2{s, s}, f32x2{bflo(w[i][e]), bfhi(w[i][e])}, acc[e]);
      }
    }
    float* red = (float*)(smem + 16384);
    *(f32x4*)(red + r0 * 128 + cg * 8) = f32x4{acc[0][0], acc[0][1], acc[1][0], acc[1][1]};
    *(f32x4*)(red + r0 * 128 + cg * 8 + 4) = f32x4{acc[2][0], acc[2][1], acc[3][0], acc[3][1]};
    __syncthreads();
    if (tid < 128) {
      float s = 0;
#pragma unroll
      for (int r = 0; r < 32; ++r) s += red[r * 128 + tid];
      st4_sc1((char*)mod + OFF_MOD2 + (128 * c + tid) * 4, __builtin_bit_cast(unsigned, s));
    }
    WAIT_V(0);
    __builtin_amdgcn_s_barrier();
    if (tid == 0) st4_sc1(cnt + (PL1 + c) * 32 + 1, 1u);
  }
}
enum { R_QKV = 0, R_PART = 1, R_F1 = 2, R_PART2 = 3 };
struct Dep {
  const char* base; unsigned voff; unsigned mul, add;
};
__device__ __forceinline__ unsigned dep_eval(unsigned v, const Dep& d) {
  v = rowmin16(v * d.mul + (d.add + 64u));
  return min(min(rdl(v, 0), rdl(v, 16)), min(rdl(v, 32), rdl(v, 48))) - 64u;
}
typedef unsigned u32x32 __attribute__((ext_vector_type(32)));
typedef unsigned u32x16 __attribute__((ext_vector_type(16)));
__device__ __forceinline__ void fast_ld0(const u32x32 (&W)[4], u32x16& pd, unsigned ea, unsigned da, int& b, int& lf, int cp, unsigned up, unsigned sa, unsigned mu, unsigned ad,
    unsigned rw, unsigned ab, const char* p1, const char* p0, unsigned l1, unsigned l0, const unsigned (&av)[8], int la) {
  asm volatile("s_setprio 0\n\t"
    "s_cmp_eq_u32 %[la], 3\n\t"
    "s_waitcnt vmcnt(12)\n\t"
    "s_cbranch_scc1 20f\n\t"
    "s_waitcnt vmcnt(4)\n\t"
    "20:\n\t"
    "s_barrier\n\t"
    "ds_read_b128 v[96:99], %[ea] offset:0\n\t"
    "ds_read_b128 v[100:103], %[ea] offset:2048\n\t"
    "ds_read_b128 v[104:107], %[da] offset:0\n\t"
    "ds_read_b128 v[108:111], %[da] offset:2048\n\t"
    "ds_read_b128 v[112:115], %[ea] offset:4096\n\t"
    "ds_read_b128 v[116:119], %[ea] offset:6144\n\t"
    "s_branch 2f\n\t"
    ".p2align 6\n\t"
    "1:\n\t"
    "s_cmp_eq_u32 %[la], 3\n\t"
    "s_waitcnt vmcnt(12)\n\t"
    "s_cbranch_scc1 21f\n\t"
    "s_waitcnt vmcnt(4)\n\t"
    "21:\n\t"
    "s_barrier\n\t"
    "2:\n\t"
    "s_waitcnt lgkmcnt(4)\n\t"
    "v_mfma_f32_16x16x32_bf16 v[64:67], v[128:131], v[96:99], 0\n\t"
    "ds_read_b128 v[120:123], %[da] offset:4096\n\t"
    "s_add_u32 m0, %[l1], 0\n\t"
    "ds_write_b128 %[rw], v[80:83] offset:0\n\t"
    "v_mfma_f32_16x16x32_bf16 v[68:71], v[132:135], v[96:99], 0\n\t"
    "ds_read_b128 v[124:127], %[da] offset:6144\n\t"
    "global_load_lds_dwordx4 %[av0], s[36:37] sc1\n\t"
    "ds_write_b128 %[rw], v[84:87] offset:1024\n\t"
    "v_mfma_f32_16x16x32_bf16 v[72:75], v[128:131], v[100:103], 0\n\t"
    "s_add_u32 m0, %[l1], 32768\n\t"
    "ds_write_b128 %[rw], v[88:91] offset:2048\n\t"
    "v_mfma_f32_16x16x32_bf16 v[76:79], v[132:135], v[100:103], 0\n\t"
    "global_load_lds_dwordx4 %[av1], s[36:37] sc1\n\t"
    "ds_write_b128 %[rw], v[92:95] offset:3072\n\t"
    "s_waitcnt lgkmcnt(8)\n\t"
    "v_mfma_f32_16x16x32_bf16 v[64:67], v[136:139], v[104:107], v[64:67]\n\t"
    "ds_read_b128 v[96:99], %[ea] offset:8192\n\t"
    "s_add_u32 m0, %[l1], 4096\n\t"
    "v_mfma_f32_16x16x32_bf16 v[68:71], v[140:143], v[104:107], v[68:71]\n\t"
    "ds_read_b128 v[100:103], %[ea] offset:10240\n\t"
    "global_load_lds_dwordx4 %[av2], s[36:37] sc1\n\t"
    "v_mfma_f32_16x16x32_bf16 v[72:75], v[136:139], v[108:111], v[72:75]\n\t"
    "s_add_u32 m0, %[l1], 36864\n\t"
    "v_mfma_f32_16x16x32_bf16 v[76:79], v[140:143], v[108:111], v[76:79]\n\t"
    "global_load_lds_dwordx4 %[av3], s[36:37] sc1\n\t"
    "s_waitcnt lgkmcnt(8)\n\t"
    "v_mfma_f32_16x16x32_bf16 v[64:67], v[144:147], v[112:115], v[64:67]\n\t"
    "ds_read_b128 v[104:107], %[da] offset:8192\n\t"
    "s_add_u32 m0, %[l1], 8192\n\t"
    "v_mfma_f32_16x16x32_bf16 v[68:71], v[148:151], v[112:115], v[68:71]\n\t"
    "ds_read_b128 v[108:111], %[da] offset:10240\n\t"
    "global_load_lds_dwordx4 %[av4], s[36:37] sc1\n\t"
    "v_mfma_f32_16x16x32_bf16 v[72:75], v[144:147], v[116:119], v[72:75]\n\t"
    "s_add_u32 m0, %[l1], 40960\n\t"
    "v_mfma_f32_16x16x32_bf16 v[76:79], v[148:151], v[116:119], v[76:79]\n\t"
    "global_load_lds_dwordx4 %[av5], s[36:37] sc1\n\t"
    "s_waitcnt lgkmcnt(7)\n\t"
    "v_mfma_f32_16x16x32_bf16 v[64:67], v[152:155], v[120:123], v[64:67]\n\t"
    "ds_read_b128 v[112:115], %[ea] offset:12288\n\t"
    "s_add_u32 m0, %[l1], 12288\n\t"
    "v_mfma_f32_16x16x32_bf16 v[68:71], v[156:159], v[120:123], v[68:71]\n\t"
    "ds_read_b128 v[116:119], %[ea] offset:14336\n\t"
    "global_load_lds_dwordx4 %[av6], s[36:37] sc1\n\t"
    "v_mfma_f32_16x16x32_bf16 v[72:75], v[152:155], v[124:127], v[72:75]\n\t"
    "s_add_u32 m0, %[l1], 45056\n\t"
    "v_mfma_f32_16x16x32_bf16 v[76:79], v[156:159], v[124:127], v[76:79]\n\t"
    "global_load_lds_dwordx4 %[av7], s[36:37] sc1\n\t"
    "s_waitcnt lgkmcnt(4)\n\t"
    "v_mfma_f32_16x16x32_bf16 v[64:67], v[160:163], v[96:99], v[64:67]\n\t"
    "ds_read_b128 v[120:123], %[da] offset:12288\n\t"
    "v_mfma_f32_16x16x32_bf16 v[68:71], v[164:167], v[96:99], v[68:71]\n\t"
    "ds_read_b128 v[124:127], %[da] offset:14336\n\t"
    "v_mfma_f32_16x16x32_bf16 v[72:75], v[160:163], v[100:103], v[72:75]\n\t"
    "v_mfma_f32_16x16x32_bf16 v[76:79], v[164:167], v[100:103], v[76:79]\n\t"
    "s_waitcnt lgkmcnt(4)\n\t"
    "v_mfma_f32_16x16x32_bf16 v[64:67], v[168:171], v[104:107], v[64:67]\n\t"
    "ds_read_b128 v[96:99], %[ea] offset:16384\n\t"
    "v_mfma_f32_16x16x32_bf16 v[68:71], v[172:175], v[104:107], v[68:71]\n\t"
    "ds_read_b128 v[100:103], %[ea] offset:18432\n\t"
    "v_mfma_f32_16x16x32_bf16 v[72:75], v[168:171], v[108:111], v[72:75]\n\t"
    "v_mfma_f32_16x16x32_bf16 v[76:79], v[172:175], v[108:111], v[76:79]\n\t"
    "s_waitcnt lgkmcnt(4)\n\t"
    "v_mfma_f32_16x16x32_bf16 v[64:67], v[176:179], v[112:115], v[64:67]\n\t"
    "ds_read_b128 v[104:107], %[da] offset:16384\n\t"
    "v_mfma_f32_16x16x32_bf16 v[68:71], v[180:183], v[112:115], v[68:71]\n\t"
    "ds_read_b128 v[108:111], %[da] offset:18432\n\t"
    "v_mfma_f32_16x16x32_bf16 v[72:75], v[176:179], v[116:119], v[72:75]\n\t"
    "v_mfma_f32_16x16x32_bf16 v[76:79], v[180:183], v[116:119], v[76:79]\n\t"
    "s_waitcnt lgkmcnt(4)\n\t"
    "v_mfma_f32_16x16x32_bf16 v[64:67], v[184:187], v[120:123], v[64:67]\n\t"
    "ds_read_b128 v[112:115], %[ea] offset:20480\n\t"
    "v_mfma_f32_16x16x32_bf16 v[68:71], v[188:191], v[120:123], v[68:71]\n\t"
    "ds_read_b128 v[116:119], %[ea] offset:22528\n\t"
    "v_mfma_f32_16x16x32_bf16 v[72:75], v[184:187], v[124:127], v[72:75]\n\t"
    "v_mfma_f32_16x16x32_bf16 v[76:79], v[188:191], v[124:127], v[76:79]\n\t"
    "s_cmp_eq_u32 %[la], 3\n\t"
    "s_waitcnt vmcnt(12)\n\t"
    "s_cbranch_scc1 22f\n\t"
    "s_waitcnt vmcnt(4)\n\t"
    "22:\n\t"
    "s_barrier\n\t"
    "s_waitcnt lgkmcnt(4)\n\t"
    "v_mfma_f32_16x16x32_bf16 v[64:67], v[192:195], v[96:99], v[64:67]\n\t"
    "ds_read_b128 v[120:123], %[da] offset:20480\n\t"
    "s_add_u32 m0, %[l0], 0\n\t"
    "v_mfma_f32_16x16x32_bf16 v[68:71], v[196:199], v[96:99], v[68:71]\n\t"
    "ds_read_b128 v[124:127], %[da] offset:22528\n\t"
    "global_load_lds_dwordx4 %[av0], s[38:39] sc1\n\t"
    "v_mfma_f32_16x16x32_bf16 v[72:75], v[192:195], v[100:103], v[72:75]\n\t"
    "s_add_u32 m0, %[l0], 32768\n\t"
    "v_mfma_f32_16x16x32_bf16 v[76:79], v[196:199], v[100:103], v[76:79]\n\t"
    "global_load_lds_dwordx4 %[av1], s[38:39] sc1\n\t"
    "s_waitcnt lgkmcnt(4)\n\t"
    "v_mfma_f32_16x16x32_bf16 v[64:67], v[200:203], v[104:107], v[64:67]\n\t"
    "ds_read_b128 v[96:99], %[ea] offset:24576\n\t"
    "s_add_u32 m0, %[l0], 4096\n\t"
    "v_mfma_f32_16x16x32_bf16 v[68:71], v[204:207], v[104:107], v[68:71]\n\t"
    "ds_read_b128 v[100:103], %[ea] offset:26624\n\t"
    "global_load_lds_dwordx4 %[av2], s[38:39] sc1\n\t"
    "v_mfma_f32_16x16x32_bf16 v[72:75], v[200:203], v[108:111], v[72:75]\n\t"
    "s_add_u32 m0, %[l0], 36864\n\t"
    "v_mfma_f32_16x16x32_bf16 v[76:79], v[204:207], v[108:111], v[76:79]\n\t"
    "global_load_lds_dwordx4 %[av3], s[38:39] sc1\n\t"
    "s_add_u32 s36, s36, %[ab]\n\t"
    "s_addc_u32 s37, s37, 0\n\t"
    "s_waitcnt lgkmcnt(4)\n\t"
    "v_mfma_f32_16x16x32_bf16 v[64:67], v[208:211], v[112:115], v[64:67]\n\t"
    "ds_read_b128 v[104:107], %[da] offset:24576\n\t"
    "s_add_u32 m0, %[l0], 8192\n\t"
    "v_mfma_f32_16x16x32_bf16 v[68:71], v[212:215], v[112:115], v[68:71]\n\t"
    "ds_read_b128 v[108:111], %[da] offset:26624\n\t"
    "global_load_lds_dwordx4 %[av4], s[38:39] sc1\n\t"
    "v_mfma_f32_16x16x32_bf16 v[72:75], v[208:211], v[116:119], v[72:75]\n\t"
    "ds_read_b32 v39, %[sa]\n\t"
    "s_add_u32 m0, %[l0], 40960\n\t"
    "v_mfma_f32_16x16x32_bf16 v[76:79], v[212:215], v[116:119], v[76:79]\n\t"
    "global_load_lds_dwordx4 %[av5], s[38:39] sc1\n\t"
    "s_xor_b32 %[l1], %[l1], 0x10000\n\t"
    "s_waitcnt lgkmcnt(5)\n\t"
    "v_mfma_f32_16x16x32_bf16 v[64:67], v[216:219], v[120:123], v[64:67]\n\t"
    "ds_read_b128 v[112:115], %[ea] offset:28672\n\t"
    "s_add_u32 m0, %[l0], 12288\n\t"
    "v_mfma_f32_16x16x32_bf16 v[68:71], v[220:223], v[120:123], v[68:71]\n\t"
    "ds_read_b128 v[116:119], %[ea] offset:30720\n\t"
    "global_load_lds_dwordx4 %[av6], s[38:39] sc1\n\t"
    "v_mfma_f32_16x16x32_bf16 v[72:75], v[216:219], v[124:127], v[72:75]\n\t"
    "v_xor_b32 %[ea], 0x10000, %[ea]\n\t"
    "s_add_u32 m0, %[l0], 45056\n\t"
    "v_mfma_f32_16x16x32_bf16 v[76:79], v[220:223], v[124:127], v[76:79]\n\t"
    "global_load_lds_dwordx4 %[av7], s[38:39] sc1\n\t"
    "s_waitcnt lgkmcnt(5)\n\t"
    "v_mfma_f32_16x16x32_bf16 v[64:67], v[224:227], v[96:99], v[64:67]\n\t"
    "ds_read_b128 v[120:123], %[da] offset:28672\n\t"
    "v_mfma_f32_16x16x32_bf16 v[68:71], v[228:231], v[96:99], v[68:71]\n\t"
    "ds_read_b128 v[124:127], %[da] offset:30720\n\t"
    "v_mfma_f32_16x16x32_bf16 v[72:75], v[224:227], v[100:103], v[72:75]\n\t"
    "v_xor_b32 %[da], 0x10000, %[da]\n\t"
    "v_mfma_f32_16x16x32_bf16 v[76:79], v[228:231], v[100:103], v[76:79]\n\t"
    "s_waitcnt lgkmcnt(5)\n\t"
    "v_mfma_f32_16x16x32_bf16 v[64:67], v[232:235], v[104:107], v[64:67]\n\t"
    "ds_read_b128 v[96:99], %[ea] offset:0\n\t"
    "v_mfma_f32_16x16x32_bf16 v[68:71], v[236:239], v[104:107], v[68:71]\n\t"
    "ds_read_b128 v[100:103], %[ea] offset:2048\n\t"
    "v_mfma_f32_16x16x32_bf16 v[72:75], v[232:235], v[108:111], v[72:75]\n\t"
    "v_mfma_f32_16x16x32_bf16 v[76:79], v[236:239], v[108:111], v[76:79]\n\t"
    "s_waitcnt lgkmcnt(4)\n\t"
    "v_mfma_f32_16x16x32_bf16 v[64:67], v[240:243], v[112:115], v[64:67]\n\t"
    "ds_read_b128 v[104:107], %[da] offset:0\n\t"
    "v_mad_u32_u24 v39, v39, %[mu], %[ad]\n\t"
    "v_mfma_f32_16x16x32_bf16 v[68:71], v[244:247], v[112:115], v[68:71]\n\t"
    "ds_read_b128 v[108:111], %[da] offset:2048\n\t"
    "v_cmp_ge_i32 vcc, %[lf], v39\n\t"
    "s_cmp_eq_u64 vcc, 0\n\t"
    "s_addc_u32 %[lf], %[lf], 0\n\t"
    "v_mfma_f32_16x16x32_bf16 v[72:75], v[240:243], v[116:119], v[72:75]\n\t"
    "v_mfma_f32_16x16x32_bf16 v[76:79], v[244:247], v[116:119], v[76:79]\n\t"
    "v_cmp_ge_i32 vcc, %[lf], v39\n\t"
    "s_cmp_eq_u64 vcc, 0\n\t"
    "s_addc_u32 %[lf], %[lf], 0\n\t"
    "s_waitcnt lgkmcnt(4)\n\t"
    "v_mfma_f32_16x16x32_bf16 v[80:83], v[248:251], v[120:123], v[64:67]\n\t"
    "ds_read_b128 v[112:115], %[ea] offset:4096\n\t"
    "s_min_i32 %[lf], %[lf], %[cp]\n\t"
    "s_add_u32 %[b], %[b], 1\n\t"
    "v_mfma_f32_16x16x32_bf16 v[84:87], v[252:255], v[120:123], v[68:71]\n\t"
    "ds_read_b128 v[116:119], %[ea] offset:6144\n\t"
    "v_mfma_f32_16x16x32_bf16 v[88:91], v[248:251], v[124:127], v[72:75]\n\t"
    "v_mfma_f32_16x16x32_bf16 v[92:95], v[252:255], v[124:127], v[76:79]\n\t"
    "s_add_u32 s38, s38, %[ab]\n\t"
    "s_addc_u32 s39, s39, 0\n\t"
    "s_xor_b32 %[l0], %[l0], 0x10000\n\t"
    "s_sub_u32 m0, %[lf], %[b]\n\t"
    "s_sub_u32 m0, m0, 1\n\t"
    "s_cmp_lt_u32 m0, %[up]\n\t"
    "s_cbranch_scc1 1b\n\t"
    "s_waitcnt lgkmcnt(0)\n\t"
    "s_nop 4"
    : "=&{v[80:95]}"(pd), [ea] "+&v"(ea), [da] "+&v"(da), [b] "+&s"(b), [lf] "+&s"(lf), "+{s[36:37]}"(p1), "+{s[38:39]}"(p0), [sa] "+&v"(sa), [l1] "+&s"(l1), [l0] "+&s"(l0)
    : [rw] "v"(rw), [ab] "s"(ab), [cp] "s"(cp), [mu] "s"(mu), [ad] "v"(ad), [up] "s"(up), "{v[128:159]}"(W[0]), "{v[160:191]}"(W[1]), "{v[192:223]}"(W[2]), "{v[224:255]}"(W[3]), [av0] "v"(av[0]), [av1] "v"(av[1]), [av2] "v"(av[2]), [av3] "v"(av[3]), [av4] "v"(av[4]), [av5] "v"(av[5]), [av6] "v"(av[6]), [av7] "v"(av[7]), [la] "s"(la)
    : "v39", "v64", "v65", "v66", "v67", "v68", "v69", "v70", "v71", "v72", "v73", "v74", "v75", "v76", "v77", "v78", "v79", "v96", "v97", "v98", "v99", "v100", "v101", "v102", "v103", "v104", "v105", "v106", "v107", "v108", "v109", "v110", "v111", "v112", "v113", "v114", "v115", "v116", "v117", "v118", "v119", "v120", "v121", "v122", "v123", "v124", "v125", "v126", "v127", "vcc", "scc", "m0", "memory");
}
__device__ __forceinline__ void fast_ld1(const u32x32 (&W)[4], u32x16& pd, unsigned ea, unsigned da, int& b, int& lf, int cp, unsigned up, unsigned sa, unsigned mu, unsigned ad,
    unsigned rw, unsigned ab, const char* p1, const char* p0, unsigned l1, unsigned l0, const unsigned (&av)[8], int la) {
  asm volatile("s_setprio 0\n\t"
    "s_cmp_eq_u32 %[la], 3\n\t"
    "s_waitcnt vmcnt(12)\n\t"
    "s_cbranch_scc1 20f\n\t"
    "s_waitcnt vmcnt(4)\n\t"
    "20:\n\t"
    "s_barrier\n\t"
    "ds_read_b128 v[96:99], %[ea] offset:0\n\t"
    "ds_read_b128 v[100:103], %[ea] offset:2048\n\t"
    "ds_read_b128 v[104:107], %[da] offset:0\n\t"
    "ds_read_b128 v[108:111], %[da] offset:2048\n\t"
    "ds_read_b128 v[112:115], %[ea] offset:4096\n\t"
    "ds_read_b128 v[116:119], %[ea] offset:6144\n\t"
    "s_branch 2f\n\t"
    ".p2align 6\n\t"
    "1:\n\t"
    "s_cmp_eq_u32 %[la], 3\n\t"
    "s_waitcnt vmcnt(12)\n\t"
    "s_cbranch_scc1 21f\n\t"
    "s_waitcnt vmcnt(4)\n\t"
    "21:\n\t"
    "s_barrier\n\t"
    "2:\n\t"
    "s_waitcnt lgkmcnt(4)\n\t"
    "v_mfma_f32_16x16x32_bf16 v[64:67], v[128:131], v[96:99], 0\n\t"
    "ds_read_b128 v[120:123], %[da] offset:4096\n\t"
    "s_add_u32 m0, %[l1], 0\n\t"
    "ds_write_b128 %[rw], v[80:83] offset:0\n\t"
    "v_mfma_f32_16x16x32_bf16 v[68:71], v[132:135], v[96:99], 0\n\t"
    "ds_read_b128 v[124:127], %[da] offset:6144\n\t"
    "global_load_lds_dwordx4 %[av0], s[36:37] sc1\n\t"
    "ds_write_b128 %[rw], v[84:87] offset:1024\n\t"
    "v_mfma_f32_16x16x32_bf16 v[72:75], v[128:131], v[100:103], 0\n\t"
    "s_add_u32 m0, %[l1], 32768\n\t"
    "ds_write_b128 %[rw], v[88:91] offset:2048\n\t"
    "v_mfma_f32_16x16x32_bf16 v[76:79], v[132:135], v[100:103], 0\n\t"
    "global_load_lds_dwordx4 %[av1], s[36:37] sc1\n\t"
    "ds_write_b128 %[rw], v[92:95] offset:3072\n\t"
    "s_waitcnt lgkmcnt(8)\n\t"
    "v_mfma_f32_16x16x32_bf16 v[64:67], v[136:139], v[104:107], v[64:67]\n\t"
    "ds_read_b128 v[96:99], %[ea] offset:8192\n\t"
    "s_add_u32 m0, %[l1], 4096\n\t"
    "v_mfma_f32_16x16x32_bf16 v[68:71], v[140:143], v[104:107], v[68:71]\n\t"
    "ds_read_b128 v[100:103], %[ea] offset:10240\n\t"
    "global_load_lds_dwordx4 %[av2], s[36:37] sc1\n\t"
    "v_mfma_f32_16x16x32_bf16 v[72:75], v[136:139], v[108:111], v[72:75]\n\t"
    "s_add_u32 m0, %[l1], 36864\n\t"
    "v_mfma_f32_16x16x32_bf16 v[76:79], v[140:143], v[108:111], v[76:79]\n\t"
    "global_load_lds_dwordx4 %[av3], s[36:37] sc1\n\t"
    "s_waitcnt lgkmcnt(8)\n\t"
    "v_mfma_f32_16x16x32_bf16 v[64:67], v[144:147], v[112:115], v[64:67]\n\t"
    "ds_read_b128 v[104:107], %[da] offset:8192\n\t"
    "s_add_u32 m0, %[l1], 8192\n\t"
    "v_mfma_f32_16x16x32_bf16 v[68:71], v[148:151], v[112:115], v[68:71]\n\t"
    "ds_read_b128 v[108:111], %[da] offset:10240\n\t"
    "global_load_lds_dwordx4 %[av4], s[36:37] sc1\n\t"
    "v_mfma_f32_16x16x32_bf16 v[72:75], v[144:147], v[116:119], v[72:75]\n\t"
    "s_add_u32 m0, %[l1], 40960\n\t"
    "v_mfma_f32_16x16x32_bf16 v[76:79], v[148:151], v[116:119], v[76:79]\n\t"
    "global_load_lds_dwordx4 %[av5], s[36:37] sc1\n\t"
    "s_waitcnt lgkmcnt(7)\n\t"
    "v_mfma_f32_16x16x32_bf16 v[64:67], v[152:155], v[120:123], v[64:67]\n\t"
    "ds_read_b128 v[112:115], %[ea] offset:12288\n\t"
    "s_add_u32 m0, %[l1], 12288\n\t"
    "v_mfma_f32_16x16x32_bf16 v[68:71], v[156:159], v[120:123], v[68:71]\n\t"
    "ds_read_b128 v[116:119], %[ea] offset:14336\n\t"
    "global_load_lds_dwordx4 %[av6], s[36:37] sc1\n\t"
    "v_mfma_f32_16x16x32_bf16 v[72:75], v[152:155], v[124:127], v[72:75]\n\t"
    "s_add_u32 m0, %[l1], 45056\n\t"
    "v_mfma_f32_16x16x32_bf16 v[76:79], v[156:159], v[124:127], v[76:79]\n\t"
    "global_load_lds_dwordx4 %[av7], s[36:37] sc1\n\t"
    "s_waitcnt lgkmcnt(4)\n\t"
    "v_mfma_f32_16x16x32_bf16 v[64:67], v[160:163], v[96:99], v[64:67]\n\t"
    "ds_read_b128 v[120:123], %[da] offset:12288\n\t"
    "v_mfma_f32_16x16x32_bf16 v[68:71], v[164:167], v[96:99], v[68:71]\n\t"
    "ds_read_b128 v[124:127], %[da] offset:14336\n\t"
    "v_mfma_f32_16x16x32_bf16 v[72:75], v[160:163], v[100:103], v[72:75]\n\t"
    "v_mfma_f32_16x16x32_bf16 v[76:79], v[164:167], v[100:103], v[76:79]\n\t"
    "s_waitcnt lgkmcnt(4)\n\t"
    "v_mfma_f32_16x16x32_bf16 v[64:67], v[168:171], v[104:107], v[64:67]\n\t"
    "ds_read_b128 v[96:99], %[ea] offset:16384\n\t"
    "v_mfma_f32_16x16x32_bf16 v[68:71], v[172:175], v[104:107], v[68:71]\n\t"
    "ds_read_b128 v[100:103], %[ea] offset:18432\n\t"
    "v_mfma_f32_16x16x32_bf16 v[72:75], v[168:171], v[108:111], v[72:75]\n\t"
    "v_mfma_f32_16x16x32_bf16 v[76:79], v[172:175], v[108:111], v[76:79]\n\t"
    "s_waitcnt lgkmcnt(4)\n\t"
    "v_mfma_f32_16x16x32_bf16 v[64:67], v[176:179], v[112:115], v[64:67]\n\t"
    "ds_read_b128 v[104:107], %[da] offset:16384\n\t"
    "v_mfma_f32_16x16x32_bf16 v[68:71], v[180:183], v[112:115], v[68:71]\n\t"
    "ds_read_b128 v[108:111], %[da] offset:18432\n\t"
    "v_mfma_f32_16x16x32_bf16 v[72:75], v[176:179], v[116:119], v[72:75]\n\t"
    "v_mfma_f32_16x16x32_bf16 v[76:79], v[180:183], v[116:119], v[76:79]\n\t"
    "s_waitcnt lgkmcnt(4)\n\t"
    "v_mfma_f32_16x16x32_bf16 v[64:67], v[184:187], v[120:123], v[64:67]\n\t"
    "ds_read_b128 v[112:115], %[ea] offset:20480\n\t"
    "v_mfma_f32_16x16x32_bf16 v[68:71], v[188:191], v[120:123], v[68:71]\n\t"
    "ds_read_b128 v[116:119], %[ea] offset:22528\n\t"
    "v_mfma_f32_16x16x32_bf16 v[72:75], v[184:187], v[124:127], v[72:75]\n\t"
    "v_mfma_f32_16x16x32_bf16 v[76:79], v[188:191], v[124:127], v[76:79]\n\t"
    "s_cmp_eq_u32 %[la], 3\n\t"
    "s_waitcnt vmcnt(12)\n\t"
    "s_cbranch_scc1 22f\n\t"
    "s_waitcnt vmcnt(4)\n\t"
    "22:\n\t"
    "s_barrier\n\t"
    "s_waitcnt lgkmcnt(4)\n\t"
    "v_mfma_f32_16x16x32_bf16 v[64:67], v[192:195], v[96:99], v[64:67]\n\t"
    "ds_read_b128 v[120:123], %[da] offset:20480\n\t"
    "s_add_u32 m0, %[l0], 0\n\t"
    "v_mfma_f32_16x16x32_bf16 v[68:71], v[196:199], v[96:99], v[68:71]\n\t"
    "ds_read_b128 v[124:127], %[da] offset:22528\n\t"
    "global_load_lds_dwordx4 %[av0], s[38:39] sc1\n\t"
    "v_mfma_f32_16x16x32_bf16 v[72:75], v[192:195], v[100:103], v[72:75]\n\t"
    "s_add_u32 m0, %[l0], 32768\n\t"
    "v_mfma_f32_16x16x32_bf16 v[76:79], v[196:199], v[100:103], v[76:79]\n\t"
    "global_load_lds_dwordx4 %[av1], s[38:39] sc1\n\t"
    "s_waitcnt lgkmcnt(4)\n\t"
    "v_mfma_f32_16x16x32_bf16 v[64:67], v[200:203], v[104:107], v[64:67]\n\t"
    "ds_read_b128 v[96:99], %[ea] offset:24576\n\t"
    "s_add_u32 m0, %[l0], 4096\n\t"
    "v_mfma_f32_16x16x32_bf16 v[68:71], v[204:207], v[104:107], v[68:71]\n\t"
    "ds_read_b128 v[100:103], %[ea] offset:26624\n\t"
    "global_load_lds_dwordx4 %[av2], s[38:39] sc1\n\t"
    "v_mfma_f32_16x16x32_bf16 v[72:75], v[200:203], v[108:111], v[72:75]\n\t"
    "s_add_u32 m0, %[l0], 36864\n\t"
    "v_mfma_f32_16x16x32_bf16 v[76:79], v[204:207], v[108:111], v[76:79]\n\t"
    "global_load_lds_dwordx4 %[av3], s[38:39] sc1\n\t"
    "s_add_u32 s36, s36, %[ab]\n\t"
    "s_addc_u32 s37, s37, 0\n\t"
    "s_waitcnt lgkmcnt(4)\n\t"
    "v_mfma_f32_16x16x32_bf16 v[64:67], v[208:211], v[112:115], v[64:67]\n\t"
    "ds_read_b128 v[104:107], %[da] offset:24576\n\t"
    "s_add_u32 m0, %[l0], 8192\n\t"
    "v_mfma_f32_16x16x32_bf16 v[68:71], v[212:215], v[112:115], v[68:71]\n\t"
    "ds_read_b128 v[108:111], %[da] offset:26624\n\t"
    "global_load_lds_dwordx4 %[av4], s[38:39] sc1\n\t"
    "v_mfma_f32_16x16x32_bf16 v[72:75], v[208:211], v[116:119], v[72:75]\n\t"
    "ds_read_b32 v39, %[sa]\n\t"
    "s_add_u32 m0, %[l0], 40960\n\t"
    "v_mfma_f32_16x16x32_bf16 v[76:79], v[212:215], v[116:119], v[76:79]\n\t"
    "global_load_lds_dwordx4 %[av5], s[38:39] sc1\n\t"
    "s_xor_b32 %[l1], %[l1], 0x10000\n\t"
    "s_waitcnt lgkmcnt(5)\n\t"
    "v_mfma_f32_16x16x32_bf16 v[64:67], v[216:219], v[120:123], v[64:67]\n\t"
    "ds_read_b128 v[112:115], %[ea] offset:28672\n\t"
    "s_add_u32 m0, %[l0], 12288\n\t"
    "v_mfma_f32_16x16x32_bf16 v[68:71], v[220:223], v[120:123], v[68:71]\n\t"
    "ds_read_b128 v[116:119], %[ea] offset:30720\n\t"
    "global_load_lds_dwordx4 %[av6], s[38:39] sc1\n\t"
    "v_mfma_f32_16x16x32_bf16 v[72:75], v[216:219], v[124:127], v[72:75]\n\t"
    "v_xor_b32 %[ea], 0x10000, %[ea]\n\t"
    "s_add_u32 m0, %[l0], 45056\n\t"
    "v_mfma_f32_16x16x32_bf16 v[76:79], v[220:223], v[124:127], v[76:79]\n\t"
    "global_load_lds_dwordx4 %[av7], s[38:39] sc1\n\t"
    "s_waitcnt lgkmcnt(5)\n\t"
    "v_mfma_f32_16x16x32_bf16 v[64:67], v[224:227], v[96:99], v[64:67]\n\t"
    "ds_read_b128 v[120:123], %[da] offset:28672\n\t"
    "v_mfma_f32_16x16x32_bf16 v[68:71], v[228:231], v[96:99], v[68:71]\n\t"
    "ds_read_b128 v[124:127], %[da] offset:30720\n\t"
    "v_mfma_f32_16x16x32_bf16 v[72:75], v[224:227], v[100:103], v[72:75]\n\t"
    "v_xor_b32 %[da], 0x10000, %[da]\n\t"
    "v_mfma_f32_16x16x32_bf16 v[76:79], v[228:231], v[100:103], v[76:79]\n\t"
    "s_waitcnt lgkmcnt(5)\n\t"
    "v_mfma_f32_16x16x32_bf16 v[64:67], v[232:235], v[104:107], v[64:67]\n\t"
    "ds_read_b128 v[96:99], %[ea] offset:0\n\t"
    "v_mfma_f32_16x16x32_bf16 v[68:71], v[236:239], v[104:107], v[68:71]\n\t"
    "ds_read_b128 v[100:103], %[ea] offset:2048\n\t"
    "v_mfma_f32_16x16x32_bf16 v[72:75], v[232:235], v[108:111], v[72:75]\n\t"
    "v_mfma_f32_16x16x32_bf16 v[76:79], v[236:239], v[108:111], v[76:79]\n\t"
    "s_waitcnt lgkmcnt(4)\n\t"
    "v_mfma_f32_16x16x32_bf16 v[64:67], v[240:243], v[112:115], v[64:67]\n\t"
    "ds_read_b128 v[104:107], %[da] offset:0\n\t"
    "v_mad_u32_u24 v39, v39, %[mu], %[ad]\n\t"
    "v_mfma_f32_16x16x32_bf16 v[68:71], v[244:247], v[112:115], v[68:71]\n\t"
    "ds_read_b128 v[108:111], %[da] offset:2048\n\t"
    "v_cmp_ge_i32 vcc, %[lf], v39\n\t"
    "s_cmp_eq_u64 vcc, 0\n\t"
    "s_addc_u32 %[lf], %[lf], 0\n\t"
    "v_mfma_f32_16x16x32_bf16 v[72:75], v[240:243], v[116:119], v[72:75]\n\t"
    "v_mfma_f32_16x16x32_bf16 v[76:79], v[244:247], v[116:119], v[76:79]\n\t"
    "v_cmp_ge_i32 vcc, %[lf], v39\n\t"
    "s_cmp_eq_u64 vcc, 0\n\t"
    "s_addc_u32 %[lf], %[lf], 0\n\t"
    "s_waitcnt lgkmcnt(4)\n\t"
    "v_mfma_f32_16x16x32_bf16 v[80:83], v[248:251], v[120:123], v[64:67]\n\t"
    "ds_read_b128 v[112:115], %[ea] offset:4096\n\t"
    "s_min_i32 %[lf], %[lf], %[cp]\n\t"
    "s_add_u32 %[b], %[b], 1\n\t"
    "v_mfma_f32_16x16x32_bf16 v[84:87], v[252:255], v[120:123], v[68:71]\n\t"
    "ds_read_b128 v[116:119], %[ea] offset:6144\n\t"
    "v_mfma_f32_16x16x32_bf16 v[88:91], v[248:251], v[124:127], v[72:75]\n\t"
    "v_mfma_f32_16x16x32_bf16 v[92:95], v[252:255], v[124:127], v[76:79]\n\t"
    "s_add_u32 s38, s38, %[ab]\n\t"
    "s_addc_u32 s39, s39, 0\n\t"
    "s_xor_b32 %[l0], %[l0], 0x10000\n\t"
    "s_sub_u32 m0, %[lf], %[b]\n\t"
    "s_sub_u32 m0, m0, 1\n\t"
    "s_cmp_lt_u32 m0, %[up]\n\t"
    "s_cbranch_scc1 1b\n\t"
    "s_waitcnt lgkmcnt(0)\n\t"
    "s_nop 4"
    : "=&{v[80:95]}"(pd), [ea] "+&v"(ea), [da] "+&v"(da), [b] "+&s"(b), [lf] "+&s"(lf), "+{s[36:37]}"(p1), "+{s[38:39]}"(p0), [sa] "+&v"(sa), [l1] "+&s"(l1), [l0] "+&s"(l0)
    : [rw] "v"(rw), [ab] "s"(ab), [cp] "s"(cp), [mu] "s"(mu), [ad] "v"(ad), [up] "s"(up), "{v[128:159]}"(W[0]), "{v[160:191]}"(W[1]), "{v[192:223]}"(W[2]), "{v[224:255]}"(W[3]), [av0] "v"(av[0]), [av1] "v"(av[1]), [av2] "v"(av[2]), [av3] "v"(av[3]), [av4] "v"(av[4]), [av5] "v"(av[5]), [av6] "v"(av[6]), [av7] "v"(av[7]), [la] "s"(la)
    : "v39", "v64", "v65", "v66", "v67", "v68", "v69", "v70", "v71", "v72", "v73", "v74", "v75", "v76", "v77", "v78", "v79", "v96", "v97", "v98", "v99", "v100", "v101", "v102", "v103", "v104", "v105", "v106", "v107", "v108", "v109", "v110", "v111", "v112", "v113", "v114", "v115", "v116", "v117", "v118", "v119", "v120", "v121", "v122", "v123", "v124", "v125", "v126", "v127", "vcc", "scc", "m0", "memory");
}
__device__ __forceinline__ void fast_ld2(const u32x32 (&W)[4], u32x16& pd, unsigned ea, unsigned da, int& b, int& lf, int cp, unsigned up, unsigned sa, unsigned mu, unsigned ad,
    unsigned rw, unsigned ab, const char* p1, const char* p0, unsigned l1, unsigned l0, const unsigned (&av)[8], int la) {
  asm volatile("s_setprio 0\n\t"
    "s_cmp_eq_u32 %[la], 3\n\t"
    "s_waitcnt vmcnt(6)\n\t"
    "s_cbranch_scc1 20f\n\t"
    "s_waitcnt vmcnt(2)\n\t"
    "20:\n\t"
    "s_barrier\n\t"
    "ds_read_b128 v[96:99], %[ea] offset:0\n\t"
    "ds_read_b128 v[100:103], %[ea] offset:2048\n\t"
    "ds_read_b128 v[104:107], %[da] offset:0\n\t"
    "ds_read_b128 v[108:111], %[da] offset:2048\n\t"
    "ds_read_b128 v[112:115], %[ea] offset:4096\n\t"
    "ds_read_b128 v[116:119], %[ea] offset:6144\n\t"
    "s_branch 2f\n\t"
    ".p2align 6\n\t"
    "1:\n\t"
    "s_cmp_eq_u32 %[la], 3\n\t"
    "s_waitcnt vmcnt(6)\n\t"
    "s_cbranch_scc1 21f\n\t"
    "s_waitcnt vmcnt(2)\n\t"
    "21:\n\t"
    "s_barrier\n\t"
    "2:\n\t"
    "s_waitcnt lgkmcnt(4)\n\t"
    "v_mfma_f32_16x16x32_bf16 v[64:67], v[128:131], v[96:99], 0\n\t"
    "ds_read_b128 v[120:123], %[da] offset:4096\n\t"
    "s_add_u32 m0, %[l1], 0\n\t"
    "ds_write_b128 %[rw], v[80:83] offset:0\n\t"
    "v_mfma_f32_16x16x32_bf16 v[68:71], v[132:135], v[96:99], 0\n\t"
    "ds_read_b128 v[124:127], %[da] offset:6144\n\t"
    "global_load_lds_dwordx4 %[av0], s[36:37] sc1\n\t"
    "ds_write_b128 %[rw], v[84:87] offset:1024\n\t"
    "v_mfma_f32_16x16x32_bf16 v[72:75], v[128:131], v[100:103], 0\n\t"
    "ds_write_b128 %[rw], v[88:91] offset:2048\n\t"
    "v_mfma_f32_16x16x32_bf16 v[76:79], v[132:135], v[100:103], 0\n\t"
    "ds_write_b128 %[rw], v[92:95] offset:3072\n\t"
    "s_waitcnt lgkmcnt(8)\n\t"
    "v_mfma_f32_16x16x32_bf16 v[64:67], v[136:139], v[104:107], v[64:67]\n\t"
    "ds_read_b128 v[96:99], %[ea] offset:8192\n\t"
    "s_add_u32 m0, %[l1], 4096\n\t"
    "v_mfma_f32_16x16x32_bf16 v[68:71], v[140:143], v[104:107], v[68:71]\n\t"
    "ds_read_b128 v[100:103], %[ea] offset:10240\n\t"
    "global_load_lds_dwordx4 %[av2], s[36:37] sc1\n\t"
    "v_mfma_f32_16x16x32_bf16 v[72:75], v[136:139], v[108:111], v[72:75]\n\t"
    "v_mfma_f32_16x16x32_bf16 v[76:79], v[140:143], v[108:111], v[76:79]\n\t"
    "s_waitcnt lgkmcnt(8)\n\t"
    "v_mfma_f32_16x16x32_bf16 v[64:67], v[144:147], v[112:115], v[64:67]\n\t"
    "ds_read_b128 v[104:107], %[da] offset:8192\n\t"
    "s_add_u32 m0, %[l1], 8192\n\t"
    "v_mfma_f32_16x16x32_bf16 v[68:71], v[148:151], v[112:115], v[68:71]\n\t"
    "ds_read_b128 v[108:111], %[da] offset:10240\n\t"
    "global_load_lds_dwordx4 %[av4], s[36:37] sc1\n\t"
    "v_mfma_f32_16x16x32_bf16 v[72:75], v[144:147], v[116:119], v[72:75]\n\t"
    "v_mfma_f32_16x16x32_bf16 v[76:79], v[148:151], v[116:119], v[76:79]\n\t"
    "s_waitcnt lgkmcnt(7)\n\t"
    "v_mfma_f32_16x16x32_bf16 v[64:67], v[152:155], v[120:123], v[64:67]\n\t"
    "ds_read_b128 v[112:115], %[ea] offset:12288\n\t"
    "s_add_u32 m0, %[l1], 12288\n\t"
    "v_mfma_f32_16x16x32_bf16 v[68:71], v[156:159], v[120:123], v[68:71]\n\t"
    "ds_read_b128 v[116:119], %[ea] offset:14336\n\t"
    "global_load_lds_dwordx4 %[av6], s[36:37] sc1\n\t"
    "v_mfma_f32_16x16x32_bf16 v[72:75], v[152:155], v[124:127], v[72:75]\n\t"
    "v_mfma_f32_16x16x32_bf16 v[76:79], v[156:159], v[124:127], v[76:79]\n\t"
    "s_waitcnt lgkmcnt(4)\n\t"
    "v_mfma_f32_16x16x32_bf16 v[64:67], v[160:163], v[96:99], v[64:67]\n\t"
    "ds_read_b128 v[120:123], %[da] offset:12288\n\t"
    "v_mfma_f32_16x16x32_bf16 v[68:71], v[164:167], v[96:99], v[68:71]\n\t"
    "ds_read_b128 v[124:127], %[da] offset:14336\n\t"
    "v_mfma_f32_16x16x32_bf16 v[72:75], v[160:163], v[100:103], v[72:75]\n\t"
    "v_mfma_f32_16x16x32_bf16 v[76:79], v[164:167], v[100:103], v[76:79]\n\t"
    "s_waitcnt lgkmcnt(4)\n\t"
    "v_mfma_f32_16x16x32_bf16 v[64:67], v[168:171], v[104:107], v[64:67]\n\t"
    "ds_read_b128 v[96:99], %[ea] offset:16384\n\t"
    "v_mfma_f32_16x16x32_bf16 v[68:71], v[172:175], v[104:107], v[68:71]\n\t"
    "ds_read_b128 v[100:103], %[ea] offset:18432\n\t"
    "v_mfma_f32_16x16x32_bf16 v[72:75], v[168:171], v[108:111], v[72:75]\n\t"
    "v_mfma_f32_16x16x32_bf16 v[76:79], v[172:175], v[108:111], v[76:79]\n\t"
    "s_waitcnt lgkmcnt(4)\n\t"
    "v_mfma_f32_16x16x32_bf16 v[64:67], v[176:179], v[112:115], v[64:67]\n\t"
    "ds_read_b128 v[104:107], %[da] offset:16384\n\t"
    "v_mfma_f32_16x16x32_bf16 v[68:71], v[180:183], v[112:115], v[68:71]\n\t"
    "ds_read_b128 v[108:111], %[da] offset:18432\n\t"
    "v_mfma_f32_16x16x32_bf16 v[72:75], v[176:179], v[116:119], v[72:75]\n\t"
    "v_mfma_f32_16x16x32_bf16 v[76:79], v[180:183], v[116:119], v[76:79]\n\t"
    "s_waitcnt lgkmcnt(4)\n\t"
    "v_mfma_f32_16x16x32_bf16 v[64:67], v[184:187], v[120:123], v[64:67]\n\t"
    "ds_read_b128 v[112:115], %[ea] offset:20480\n\t"
    "v_mfma_f32_16x16x32_bf16 v[68:71], v[188:191], v[120:123], v[68:71]\n\t"
    "ds_read_b128 v[116:119], %[ea] offset:22528\n\t"
    "v_mfma_f32_16x16x32_bf16 v[72:75], v[184:187], v[124:127], v[72:75]\n\t"
    "v_mfma_f32_16x16x32_bf16 v[76:79], v[188:191], v[124:127], v[76:79]\n\t"
    "s_cmp_eq_u32 %[la], 3\n\t"
    "s_waitcnt vmcnt(6)\n\t"
    "s_cbranch_scc1 22f\n\t"
    "s_waitcnt vmcnt(2)\n\t"
    "22:\n\t"
    "s_barrier\n\t"
    "s_waitcnt lgkmcnt(4)\n\t"
    "v_mfma_f32_16x16x32_bf16 v[64:67], v[192:195], v[96:99], v[64:67]\n\t"
    "ds_read_b128 v[120:123], %[da] offset:20480\n\t"
    "s_add_u32 m0, %[l0], 0\n\t"
    "v_mfma_f32_16x16x32_bf16 v[68:71], v[196:199], v[96:99], v[68:71]\n\t"
    "ds_read_b128 v[124:127], %[da] offset:22528\n\t"
    "global_load_lds_dwordx4 %[av0], s[38:39] sc1\n\t"
    "v_mfma_f32_16x16x32_bf16 v[72:75], v[192:195], v[100:103], v[72:75]\n\t"
    "v_mfma_f32_16x16x32_bf16 v[76:79], v[196:199], v[100:103], v[76:79]\n\t"
    "s_waitcnt lgkmcnt(4)\n\t"
    "v_mfma_f32_16x16x32_bf16 v[64:67], v[200:203], v[104:107], v[64:67]\n\t"
    "ds_read_b128 v[96:99], %[ea] offset:24576\n\t"
    "s_add_u32 m0, %[l0], 4096\n\t"
    "v_mfma_f32_16x16x32_bf16 v[68:71], v[204:207], v[104:107], v[68:71]\n\t"
    "ds_read_b128 v[100:103], %[ea] offset:26624\n\t"
    "global_load_lds_dwordx4 %[av2], s[38:39] sc1\n\t"
    "v_mfma_f32_16x16x32_bf16 v[72:75], v[200:203], v[108:111], v[72:75]\n\t"
    "v_mfma_f32_16x16x32_bf16 v[76:79], v[204:207], v[108:111], v[76:79]\n\t"
    "s_add_u32 s36, s36, %[ab]\n\t"
    "s_addc_u32 s37, s37, 0\n\t"
    "s_waitcnt lgkmcnt(4)\n\t"
    "v_mfma_f32_16x16x32_bf16 v[64:67], v[208:211], v[112:115], v[64:67]\n\t"
    "ds_read_b128 v[104:107], %[da] offset:24576\n\t"
    "s_add_u32 m0, %[l0], 8192\n\t"
    "v_mfma_f32_16x16x32_bf16 v[68:71], v[212:215], v[112:115], v[68:71]\n\t"
    "ds_read_b128 v[108:111], %[da] offset:26624\n\t"
    "global_load_lds_dwordx4 %[av4], s[38:39] sc1\n\t"
    "v_mfma_f32_16x16x32_bf16 v[72:75], v[208:211], v[116:119], v[72:75]\n\t"
    "ds_read_b32 v39, %[sa]\n\t"
    "v_mfma_f32_16x16x32_bf16 v[76:79], v[212:215], v[116:119], v[76:79]\n\t"
    "s_xor_b32 %[l1], %[l1], 0x10000\n\t"
    "s_waitcnt lgkmcnt(5)\n\t"
    "v_mfma_f32_16x16x32_bf16 v[64:67], v[216:219], v[120:123], v[64:67]\n\t"
    "ds_read_b128 v[112:115], %[ea] offset:28672\n\t"
    "s_add_u32 m0, %[l0], 12288\n\t"
    "v_mfma_f32_16x16x32_bf16 v[68:71], v[220:223], v[120:123], v[68:71]\n\t"
    "ds_read_b128 v[116:119], %[ea] offset:30720\n\t"
    "global_load_lds_dwordx4 %[av6], s[38:39] sc1\n\t"
    "v_mfma_f32_16x16x32_bf16 v[72:75], v[216:219], v[124:127], v[72:75]\n\t"
    "v_xor_b32 %[ea], 0x10000, %[ea]\n\t"
    "v_mfma_f32_16x16x32_bf16 v[76:79], v[220:223], v[124:127], v[76:79]\n\t"
    "s_waitcnt lgkmcnt(5)\n\t"
    "v_mfma_f32_16x16x32_bf16 v[64:67], v[224:227], v[96:99], v[64:67]\n\t"
    "ds_read_b128 v[120:123], %[da] offset:28672\n\t"
    "v_mfma_f32_16x16x32_bf16 v[68:71], v[228:231], v[96:99], v[68:71]\n\t"
    "ds_read_b128 v[124:127], %[da] offset:30720\n\t"
    "v_mfma_f32_16x16x32_bf16 v[72:75], v[224:227], v[100:103], v[72:75]\n\t"
    "v_xor_b32 %[da], 0x10000, %[da]\n\t"
    "v_mfma_f32_16x16x32_bf16 v[76:79], v[228:231], v[100:103], v[76:79]\n\t"
    "s_waitcnt lgkmcnt(5)\n\t"
    "v_mfma_f32_16x16x32_bf16 v[64:67], v[232:235], v[104:107], v[64:67]\n\t"
    "ds_read_b128 v[96:99], %[ea] offset:0\n\t"
    "v_mfma_f32_16x16x32_bf16 v[68:71], v[236:239], v[104:107], v[68:71]\n\t"
    "ds_read_b128 v[100:103], %[ea] offset:2048\n\t"
    "v_mfma_f32_16x16x32_bf16 v[72:75], v[232:235], v[108:111], v[72:75]\n\t"
    "v_mfma_f32_16x16x32_bf16 v[76:79], v[236:239], v[108:111], v[76:79]\n\t"
    "s_waitcnt lgkmcnt(4)\n\t"
    "v_mfma_f32_16x16x32_bf16 v[64:67], v[240:243], v[112:115], v[64:67]\n\t"
    "ds_read_b128 v[104:107], %[da] offset:0\n\t"
    "v_mad_u32_u24 v39, v39, %[mu], %[ad]\n\t"
    "v_mfma_f32_16x16x32_bf16 v[68:71], v[244:247], v[112:115], v[68:71]\n\t"
    "ds_read_b128 v[108:111], %[da] offset:2048\n\t"
    "v_cmp_ge_i32 vcc, %[lf], v39\n\t"
    "s_cmp_eq_u64 vcc, 0\n\t"
    "s_addc_u32 %[lf], %[lf], 0\n\t"
    "v_mfma_f32_16x16x32_bf16 v[72:75], v[240:243], v[116:119], v[72:75]\n\t"
    "v_mfma_f32_16x16x32_bf16 v[76:79], v[244:247], v[116:119], v[76:79]\n\t"
    "v_cmp_ge_i32 vcc, %[lf], v39\n\t"
    "s_cmp_eq_u64 vcc, 0\n\t"
    "s_addc_u32 %[lf], %[lf], 0\n\t"
    "s_waitcnt lgkmcnt(4)\n\t"
    "v_mfma_f32_16x16x32_bf16 v[80:83], v[248:251], v[120:123], v[64:67]\n\t"
    "ds_read_b128 v[112:115], %[ea] offset:4096\n\t"
    "s_min_i32 %[lf], %[lf], %[cp]\n\t"
    "s_add_u32 %[b], %[b], 1\n\t"
    "v_mfma_f32_16x16x32_bf16 v[84:87], v[252:255], v[120:123], v[68:71]\n\t"
    "ds_read_b128 v[116:119], %[ea] offset:6144\n\t"
    "v_mfma_f32_16x16x32_bf16 v[88:91], v[248:251], v[124:127], v[72:75]\n\t"
    "v_mfma_f32_16x16x32_bf16 v[92:95], v[252:255], v[124:127], v[76:79]\n\t"
    "s_add_u32 s38, s38, %[ab]\n\t"
    "s_addc_u32 s39, s39, 0\n\t"
    "s_xor_b32 %[l0], %[l0], 0x10000\n\t"
    "s_sub_u32 m0, %[lf], %[b]\n\t"
    "s_sub_u32 m0, m0, 1\n\t"
    "s_cmp_lt_u32 m0, %[up]\n\t"
    "s_cbranch_scc1 1b\n\t"
    "s_waitcnt lgkmcnt(0)\n\t"
    "s_nop 4"
    : "=&{v[80:95]}"(pd), [ea] "+&v"(ea), [da] "+&v"(da), [b] "+&s"(b), [lf] "+&s"(lf), "+{s[36:37]}"(p1), "+{s[38:39]}"(p0), [sa] "+&v"(sa), [l1] "+&s"(l1), [l0] "+&s"(l0)
    : [rw] "v"(rw), [ab] "s"(ab), [cp] "s"(cp), [mu] "s"(mu), [ad] "v"(ad), [up] "s"(up), "{v[128:159]}"(W[0]), "{v[160:191]}"(W[1]), "{v[192:223]}"(W[2]), "{v[224:255]}"(W[3]), [av0] "v"(av[0]), [av1] "v"(av[1]), [av2] "v"(av[2]), [av3] "v"(av[3]), [av4] "v"(av[4]), [av5] "v"(av[5]), [av6] "v"(av[6]), [av7] "v"(av[7]), [la] "s"(la)
    : "v39", "v64", "v65", "v66", "v67", "v68", "v69", "v70", "v71", "v72", "v73", "v74", "v75", "v76", "v77", "v78", "v79", "v96", "v97", "v98", "v99", "v100", "v101", "v102", "v103", "v104", "v105", "v106", "v107", "v108", "v109", "v110", "v111", "v112", "v113", "v114", "v115", "v116", "v117", "v118", "v119", "v120", "v121", "v122", "v123", "v124", "v125", "v126", "v127", "vcc", "scc", "m0", "memory");
}
__device__ __forceinline__ void fast_st0(const u32x32 (&W)[4], u32x16& pd, unsigned ea, unsigned da, int& b, int& lf, int cp, unsigned up, unsigned sa, unsigned mu, unsigned ad,
    unsigned rw, unsigned ab, const char* p1, const char* p0, unsigned co0, unsigned co1, int po, int pf, unsigned w0, unsigned sl, unsigned dv, const char* db, const char* pg) {
  unsigned st;
  asm volatile("s_barrier\n\t"
    "ds_read_b128 v[96:99], %[ea] offset:0\n\t"
    "ds_read_b128 v[100:103], %[ea] offset:2048\n\t"
    "ds_read_b128 v[104:107], %[da] offset:0\n\t"
    "ds_read_b128 v[108:111], %[da] offset:2048\n\t"
    "ds_read_b128 v[112:115], %[ea] offset:4096\n\t"
    "ds_read_b128 v[116:119], %[ea] offset:6144\n\t"
    "s_branch 2f\n\t"
    ".p2align 6\n\t"
    "1:\n\t"
    "s_barrier\n\t"
    "2:\n\t"
    "s_waitcnt lgkmcnt(4)\n\t"
    "v_mfma_f32_16x16x32_bf16 v[64:67], v[128:131], v[96:99], 0\n\t"
    "ds_read_b128 v[120:123], %[da] offset:4096\n\t"
    "v_mfma_f32_16x16x32_bf16 v[68:71], v[132:135], v[96:99], 0\n\t"
    "ds_read_b128 v[124:127], %[da] offset:6144\n\t"
    "v_mfma_f32_16x16x32_bf16 v[72:75], v[128:131], v[100:103], 0\n\t"
    "v_mfma_f32_16x16x32_bf16 v[76:79], v[132:135], v[100:103], 0\n\t"
    "s_waitcnt lgkmcnt(4)\n\t"
    "v_mfma_f32_16x16x32_bf16 v[64:67], v[136:139], v[104:107], v[64:67]\n\t"
    "ds_read_b128 v[96:99], %[ea] offset:8192\n\t"
    "v_mfma_f32_16x16x32_bf16 v[68:71], v[140:143], v[104:107], v[68:71]\n\t"
    "ds_read_b128 v[100:103], %[ea] offset:10240\n\t"
    "v_mfma_f32_16x16x32_bf16 v[72:75], v[136:139], v[108:111], v[72:75]\n\t"
    "v_mfma_f32_16x16x32_bf16 v[76:79], v[140:143], v[108:111], v[76:79]\n\t"
    "s_cmp_eq_u32 %[w0], 1\n\t"
    "s_cbranch_scc1 7f\n\t"
    "8:\n\t"
    "s_waitcnt lgkmcnt(4)\n\t"
    "v_mfma_f32_16x16x32_bf16 v[64:67], v[144:147], v[112:115], v[64:67]\n\t"
    "ds_read_b128 v[104:107], %[da] offset:8192\n\t"
    "v_mfma_f32_16x16x32_bf16 v[68:71], v[148:151], v[112:115], v[68:71]\n\t"
    "ds_read_b128 v[108:111], %[da] offset:10240\n\t"
    "v_mfma_f32_16x16x32_bf16 v[72:75], v[144:147], v[116:119], v[72:75]\n\t"
    "v_mfma_f32_16x16x32_bf16 v[76:79], v[148:151], v[116:119], v[76:79]\n\t"
    "s_waitcnt lgkmcnt(4)\n\t"
    "v_mfma_f32_16x16x32_bf16 v[64:67], v[152:155], v[120:123], v[64:67]\n\t"
    "ds_read_b128 v[112:115], %[ea] offset:12288\n\t"
    "v_mfma_f32_16x16x32_bf16 v[68:71], v[156:159], v[120:123], v[68:71]\n\t"
    "ds_read_b128 v[116:119], %[ea] offset:14336\n\t"
    "v_mfma_f32_16x16x32_bf16 v[72:75], v[152:155], v[124:127], v[72:75]\n\t"
    "v_mfma_f32_16x16x32_bf16 v[76:79], v[156:159], v[124:127], v[76:79]\n\t"
    "s_waitcnt lgkmcnt(4)\n\t"
    "v_mfma_f32_16x16x32_bf16 v[64:67], v[160:163], v[96:99], v[64:67]\n\t"
    "ds_read_b128 v[120:123], %[da] offset:12288\n\t"
    "v_mfma_f32_16x16x32_bf16 v[68:71], v[164:167], v[96:99], v[68:71]\n\t"
    "ds_read_b128 v[124:127], %[da] offset:14336\n\t"
    "v_mfma_f32_16x16x32_bf16 v[72:75], v[160:163], v[100:103], v[72:75]\n\t"
    "v_mfma_f32_16x16x32_bf16 v[76:79], v[164:167], v[100:103], v[76:79]\n\t"
    "s_waitcnt lgkmcnt(4)\n\t"
    "v_mfma_f32_16x16x32_bf16 v[64:67], v[168:171], v[104:107], v[64:67]\n\t"
    "ds_read_b128 v[96:99], %[ea] offset:16384\n\t"
    "v_mfma_f32_16x16x32_bf16 v[68:71], v[172:175], v[104:107], v[68:71]\n\t"
    "ds_read_b128 v[100:103], %[ea] offset:18432\n\t"
    "v_mfma_f32_16x16x32_bf16 v[72:75], v[168:171], v[108:111], v[72:75]\n\t"
    "v_mfma_f32_16x16x32_bf16 v[76:79], v[172:175], v[108:111], v[76:79]\n\t"
    "s_waitcnt lgkmcnt(4)\n\t"
    "v_mfma_f32_16x16x32_bf16 v[64:67], v[176:179], v[112:115], v[64:67]\n\t"
    "ds_read_b128 v[104:107], %[da] offset:16384\n\t"
    "v_mfma_f32_16x16x32_bf16 v[68:71], v[180:183], v[112:115], v[68:71]\n\t"
    "ds_read_b128 v[108:111], %[da] offset:18432\n\t"
    "v_mfma_f32_16x16x32_bf16 v[72:75], v[176:179], v[116:119], v[72:75]\n\t"
    "v_mfma_f32_16x16x32_bf16 v[76:79], v[180:183], v[116:119], v[76:79]\n\t"
    "s_waitcnt lgkmcnt(4)\n\t"
    "v_mfma_f32_16x16x32_bf16 v[64:67], v[184:187], v[120:123], v[64:67]\n\t"
    "ds_read_b128 v[112:115], %[ea] offset:20480\n\t"
    "v_mfma_f32_16x16x32_bf16 v[68:71], v[188:191], v[120:123], v[68:71]\n\t"
    "ds_read_b128 v[116:119], %[ea] offset:22528\n\t"
    "v_mfma_f32_16x16x32_bf16 v[72:75], v[184:187], v[124:127], v[72:75]\n\t"
    "v_mfma_f32_16x16x32_bf16 v[76:79], v[188:191], v[124:127], v[76:79]\n\t"
    "s_waitcnt vmcnt(0)\n\t"
    "s_barrier\n\t"
    "s_waitcnt lgkmcnt(4)\n\t"
    "v_mfma_f32_16x16x32_bf16 v[64:67], v[192:195], v[96:99], v[64:67]\n\t"
    "ds_read_b128 v[120:123], %[da] offset:20480\n\t"
    "v_mfma_f32_16x16x32_bf16 v[68:71], v[196:199], v[96:99], v[68:71]\n\t"
    "ds_read_b128 v[124:127], %[da] offset:22528\n\t"
    "v_mfma_f32_16x16x32_bf16 v[72:75], v[192:195], v[100:103], v[72:75]\n\t"
    "ds_read_b128 v[48:51], %[rw] offset:0\n\t"
    "v_mfma_f32_16x16x32_bf16 v[76:79], v[196:199], v[100:103], v[76:79]\n\t"
    "ds_read_b128 v[52:55], %[rw] offset:1024\n\t"
    "s_cmp_eq_u32 %[w0], 0\n\t"
    "s_cbranch_scc1 5f\n\t"
    "6:\n\t"
    "s_waitcnt lgkmcnt(6)\n\t"
    "v_mfma_f32_16x16x32_bf16 v[64:67], v[200:203], v[104:107], v[64:67]\n\t"
    "ds_read_b128 v[96:99], %[ea] offset:24576\n\t"
    "v_mfma_f32_16x16x32_bf16 v[68:71], v[204:207], v[104:107], v[68:71]\n\t"
    "ds_read_b128 v[100:103], %[ea] offset:26624\n\t"
    "v_mfma_f32_16x16x32_bf16 v[72:75], v[200:203], v[108:111], v[72:75]\n\t"
    "ds_read_b128 v[56:59], %[rw] offset:2048\n\t"
    "v_mfma_f32_16x16x32_bf16 v[76:79], v[204:207], v[108:111], v[76:79]\n\t"
    "ds_read_b128 v[60:63], %[rw] offset:3072\n\t"
    "s_waitcnt lgkmcnt(0)\n\t"
    "v_mfma_f32_16x16x32_bf16 v[64:67], v[208:211], v[112:115], v[64:67]\n\t"
    "ds_read_b128 v[104:107], %[da] offset:24576\n\t"
    "v_add_f32 v48, v48, v80\n\t"
    "v_add_f32 v49, v49, v81\n\t"
    "v_mfma_f32_16x16x32_bf16 v[68:71], v[212:215], v[112:115], v[68:71]\n\t"
    "ds_read_b128 v[108:111], %[da] offset:26624\n\t"
    "v_add_f32 v50, v50, v82\n\t"
    "v_add_f32 v51, v51, v83\n\t"
    "v_mfma_f32_16x16x32_bf16 v[72:75], v[208:211], v[116:119], v[72:75]\n\t"
    "ds_read_b32 v39, %[sa]\n\t"
    "v_add_f32 v52, v52, v84\n\t"
    "v_mfma_f32_16x16x32_bf16 v[76:79], v[212:215], v[116:119], v[76:79]\n\t"
    "v_add_f32 v53, v53, v85\n\t"
    "v_add_f32 v54, v54, v86\n\t"
    "v_mfma_f32_16x16x32_bf16 v[64:67], v[216:219], v[120:123], v[64:67]\n\t"
    "ds_read_b128 v[112:115], %[ea] offset:28672\n\t"
    "v_add_f32 v55, v55, v87\n\t"
    "v_add_f32 v56, v56, v88\n\t"
    "v_mfma_f32_16x16x32_bf16 v[68:71], v[220:223], v[120:123], v[68:71]\n\t"
    "ds_read_b128 v[116:119], %[ea] offset:30720\n\t"
    "v_add_f32 v57, v57, v89\n\t"
    "v_mfma_f32_16x16x32_bf16 v[72:75], v[216:219], v[124:127], v[72:75]\n\t"
    "v_xor_b32 %[ea], 0x10000, %[ea]\n\t"
    "v_add_f32 v58, v58, v90\n\t"
    "v_add_f32 v59, v59, v91\n\t"
    "v_mfma_f32_16x16x32_bf16 v[76:79], v[220:223], v[124:127], v[76:79]\n\t"
    "v_add_f32 v60, v60, v92\n\t"
    "v_mfma_f32_16x16x32_bf16 v[64:67], v[224:227], v[96:99], v[64:67]\n\t"
    "ds_read_b128 v[120:123], %[da] offset:28672\n\t"
    "v_add_f32 v61, v61, v93\n\t"
    "v_add_f32 v62, v62, v94\n\t"
    "v_mfma_f32_16x16x32_bf16 v[68:71], v[228:231], v[96:99], v[68:71]\n\t"
    "ds_read_b128 v[124:127], %[da] offset:30720\n\t"
    "v_add_f32 v63, v63, v95\n\t"
    "v_cvt_pk_bf16_f32 v40, v48, v49\n\t"
    "v_mfma_f32_16x16x32_bf16 v[72:75], v[224:227], v[100:103], v[72:75]\n\t"
    "v_xor_b32 %[da], 0x10000, %[da]\n\t"
    "v_cvt_pk_bf16_f32 v41, v50, v51\n\t"
    "v_mfma_f32_16x16x32_bf16 v[76:79], v[228:231], v[100:103], v[76:79]\n\t"
    "v_cvt_pk_bf16_f32 v42, v52, v53\n\t"
    "v_cvt_pk_bf16_f32 v43, v54, v55\n\t"
    "s_waitcnt lgkmcnt(5)\n\t"
    "v_mfma_f32_16x16x32_bf16 v[64:67], v[232:235], v[104:107], v[64:67]\n\t"
    "ds_read_b128 v[96:99], %[ea] offset:0\n\t"
    "global_store_dwordx4 %[co0], v[40:43], s[36:37] sc1\n\t"
    "v_cvt_pk_bf16_f32 v44, v56, v57\n\t"
    "v_mfma_f32_16x16x32_bf16 v[68:71], v[236:239], v[104:107], v[68:71]\n\t"
    "ds_read_b128 v[100:103], %[ea] offset:2048\n\t"
    "v_cvt_pk_bf16_f32 v45, v58, v59\n\t"
    "v_mfma_f32_16x16x32_bf16 v[72:75], v[232:235], v[108:111], v[72:75]\n\t"
    "v_cvt_pk_bf16_f32 v46, v60, v61\n\t"
    "v_cvt_pk_bf16_f32 v47, v62, v63\n\t"
    "v_mfma_f32_16x16x32_bf16 v[76:79], v[236:239], v[108:111], v[76:79]\n\t"
    "global_store_dwordx4 %[co1], v[44:47], s[36:37] sc1\n\t"
    "s_waitcnt lgkmcnt(4)\n\t"
    "v_mfma_f32_16x16x32_bf16 v[64:67], v[240:243], v[112:115], v[64:67]\n\t"
    "ds_read_b128 v[104:107], %[da] offset:0\n\t"
    "v_mad_u32_u24 v39, v39, %[mu], %[ad]\n\t"
    "v_mfma_f32_16x16x32_bf16 v[68:71], v[244:247], v[112:115], v[68:71]\n\t"
    "ds_read_b128 v[108:111], %[da] offset:2048\n\t"
    "v_cmp_ge_i32 vcc, %[lf], v39\n\t"
    "s_cmp_eq_u64 vcc, 0\n\t"
    "s_addc_u32 %[lf], %[lf], 0\n\t"
    "v_mfma_f32_16x16x32_bf16 v[72:75], v[240:243], v[116:119], v[72:75]\n\t"
    "v_mfma_f32_16x16x32_bf16 v[76:79], v[244:247], v[116:119], v[76:79]\n\t"
    "v_cmp_ge_i32 vcc, %[lf], v39\n\t"
    "s_cmp_eq_u64 vcc, 0\n\t"
    "s_addc_u32 %[lf], %[lf], 0\n\t"
    "s_waitcnt lgkmcnt(4)\n\t"
    "v_mfma_f32_16x16x32_bf16 v[80:83], v[248:251], v[120:123], v[64:67]\n\t"
    "ds_read_b128 v[112:115], %[ea] offset:4096\n\t"
    "s_min_i32 %[lf], %[lf], %[cp]\n\t"
    "s_add_u32 %[b], %[b], 1\n\t"
    "v_mfma_f32_16x16x32_bf16 v[84:87], v[252:255], v[120:123], v[68:71]\n\t"
    "ds_read_b128 v[116:119], %[ea] offset:6144\n\t"
    "v_mfma_f32_16x16x32_bf16 v[88:91], v[248:251], v[124:127], v[72:75]\n\t"
    "v_mfma_f32_16x16x32_bf16 v[92:95], v[252:255], v[124:127], v[76:79]\n\t"
    "s_mov_b64 s[36:37], s[38:39]\n\t"
    "s_add_u32 s38, s38, %[ab]\n\t"
    "s_addc_u32 s39, s39, 0\n\t"
    "s_sub_u32 m0, %[lf], %[b]\n\t"
    "s_sub_u32 m0, m0, 1\n\t"
    "s_cmp_lt_u32 m0, %[up]\n\t"
    "s_cbranch_scc1 1b\n\t"
    "s_waitcnt lgkmcnt(0)\n\t"
    "s_nop 4\n\t"
    "s_branch 9f\n\t"
    "5:\n\t"
    "s_add_u32 %[st], %[b], %[po]\n\t"
    "s_max_i32 %[st], %[st], %[p0]\n\t"
    "v_mov_b32 v39, %[st]\n\t"
    "v_mov_b32 v38, 0\n\t"
    "s_mov_b64 exec, 1\n\t"
    "global_store_dword v38, v39, %[pg] sc1\n\t"
    "s_mov_b64 exec, -1\n\t"
    "s_mov_b32 %[p0], %[p1]\n\t"
    "s_branch 6b\n\t"
    "7:\n\t"
    "s_cmp_ge_i32 %[lf], %[cp]\n\t"
    "s_cbranch_scc1 8b\n\t"
    "s_mov_b32 m0, %[sl]\n\t"
    "s_nop 0\n\t"
    "global_load_lds_dword %[dv], %[db] sc1\n\t"
    "s_branch 8b\n\t"
    "9:"
    : "=&{v[80:95]}"(pd), [ea] "+&v"(ea), [da] "+&v"(da), [b] "+&s"(b), [lf] "+&s"(lf), "+{s[36:37]}"(p1), "+{s[38:39]}"(p0), [sa] "+&v"(sa), [st] "=&s"(st), [sl] "+&s"(sl), [p0] "+&s"(pf)
    : [rw] "v"(rw), [ab] "s"(ab), [cp] "s"(cp), [mu] "s"(mu), [ad] "v"(ad), [up] "s"(up), "{v[128:159]}"(W[0]), "{v[160:191]}"(W[1]), "{v[192:223]}"(W[2]), "{v[224:255]}"(W[3]), [co0] "v"(co0), [w0] "s"(w0), [dv] "v"(dv), [db] "s"(db), [pg] "s"(pg), [po] "s"(po - 1), [p1] "s"(b + po), [co1] "v"(co1)
    : "v38", "v39", "v40", "v41", "v42", "v43", "v44", "v45", "v46", "v47", "v48", "v49", "v50", "v51", "v52", "v53", "v54", "v55", "v56", "v57", "v58", "v59", "v60", "v61", "v62", "v63", "v64", "v65", "v66", "v67", "v68", "v69", "v70", "v71", "v72", "v73", "v74", "v75", "v76", "v77", "v78", "v79", "v96", "v97", "v98", "v99", "v100", "v101", "v102", "v103", "v104", "v105", "v106", "v107", "v108", "v109", "v110", "v111", "v112", "v113", "v114", "v115", "v116", "v117", "v118", "v119", "v120", "v121", "v122", "v123", "v124", "v125", "v126", "v127", "vcc", "scc", "m0", "memory");
}
__device__ __forceinline__ void fast_st1(const u32x32 (&W)[4], u32x16& pd, unsigned ea, unsigned da, int& b, int& lf, int cp, unsigned up, unsigned sa, unsigned mu, unsigned ad,
    unsigned rw, unsigned ab, const char* p1, const char* p0, unsigned co0, unsigned co1, int po, int pf, unsigned w0, unsigned sl, unsigned dv, const char* db, const char* pg) {
  unsigned st;
  asm volatile("s_barrier\n\t"
    "ds_read_b128 v[96:99], %[ea] offset:0\n\t"
    "ds_read_b128 v[100:103], %[ea] offset:2048\n\t"
    "ds_read_b128 v[104:107], %[da] offset:0\n\t"
    "ds_read_b128 v[108:111], %[da] offset:2048\n\t"
    "ds_read_b128 v[112:115], %[ea] offset:4096\n\t"
    "ds_read_b128 v[116:119], %[ea] offset:6144\n\t"
    "s_branch 2f\n\t"
    ".p2align 6\n\t"
    "1:\n\t"
    "s_barrier\n\t"
    "2:\n\t"
    "s_waitcnt lgkmcnt(4)\n\t"
    "v_mfma_f32_16x16x32_bf16 v[64:67], v[128:131], v[96:99], 0\n\t"
    "ds_read_b128 v[120:123], %[da] offset:4096\n\t"
    "v_mfma_f32_16x16x32_bf16 v[68:71], v[132:135], v[96:99], 0\n\t"
    "ds_read_b128 v[124:127], %[da] offset:6144\n\t"
    "v_mfma_f32_16x16x32_bf16 v[72:75], v[128:131], v[100:103], 0\n\t"
    "v_mfma_f32_16x16x32_bf16 v[76:79], v[132:135], v[100:103], 0\n\t"
    "s_waitcnt lgkmcnt(4)\n\t"
    "v_mfma_f32_16x16x32_bf16 v[64:67], v[136:139], v[104:107], v[64:67]\n\t"
    "ds_read_b128 v[96:99], %[ea] offset:8192\n\t"
    "v_mfma_f32_16x16x32_bf16 v[68:71], v[140:143], v[104:107], v[68:71]\n\t"
    "ds_read_b128 v[100:103], %[ea] offset:10240\n\t"
    "v_mfma_f32_16x16x32_bf16 v[72:75], v[136:139], v[108:111], v[72:75]\n\t"
    "v_mfma_f32_16x16x32_bf16 v[76:79], v[140:143], v[108:111], v[76:79]\n\t"
    "s_cmp_eq_u32 %[w0], 1\n\t"
    "s_cbranch_scc1 7f\n\t"
    "8:\n\t"
    "s_waitcnt lgkmcnt(4)\n\t"
    "v_mfma_f32_16x16x32_bf16 v[64:67], v[144:147], v[112:115], v[64:67]\n\t"
    "ds_read_b128 v[104:107], %[da] offset:8192\n\t"
    "v_mfma_f32_16x16x32_bf16 v[68:71], v[148:151], v[112:115], v[68:71]\n\t"
    "ds_read_b128 v[108:111], %[da] offset:10240\n\t"
    "v_mfma_f32_16x16x32_bf16 v[72:75], v[144:147], v[116:119], v[72:75]\n\t"
    "v_mfma_f32_16x16x32_bf16 v[76:79], v[148:151], v[116:119], v[76:79]\n\t"
    "s_waitcnt lgkmcnt(4)\n\t"
    "v_mfma_f32_16x16x32_bf16 v[64:67], v[152:155], v[120:123], v[64:67]\n\t"
    "ds_read_b128 v[112:115], %[ea] offset:12288\n\t"
    "v_mfma_f32_16x16x32_bf16 v[68:71], v[156:159], v[120:123], v[68:71]\n\t"
    "ds_read_b128 v[116:119], %[ea] offset:14336\n\t"
    "v_mfma_f32_16x16x32_bf16 v[72:75], v[152:155], v[124:127], v[72:75]\n\t"
    "v_mfma_f32_16x16x32_bf16 v[76:79], v[156:159], v[124:127], v[76:79]\n\t"
    "s_waitcnt lgkmcnt(4)\n\t"
    "v_mfma_f32_16x16x32_bf16 v[64:67], v[160:163], v[96:99], v[64:67]\n\t"
    "ds_read_b128 v[120:123], %[da] offset:12288\n\t"
    "v_mfma_f32_16x16x32_bf16 v[68:71], v[164:167], v[96:99], v[68:71]\n\t"
    "ds_read_b128 v[124:127], %[da] offset:14336\n\t"
    "v_mfma_f32_16x16x32_bf16 v[72:75], v[160:163], v[100:103], v[72:75]\n\t"
    "v_mfma_f32_16x16x32_bf16 v[76:79], v[164:167], v[100:103], v[76:79]\n\t"
    "s_waitcnt lgkmcnt(4)\n\t"
    "v_mfma_f32_16x16x32_bf16 v[64:67], v[168:171], v[104:107], v[64:67]\n\t"
    "ds_read_b128 v[96:99], %[ea] offset:16384\n\t"
    "v_mfma_f32_16x16x32_bf16 v[68:71], v[172:175], v[104:107], v[68:71]\n\t"
    "ds_read_b128 v[100:103], %[ea] offset:18432\n\t"
    "v_mfma_f32_16x16x32_bf16 v[72:75], v[168:171], v[108:111], v[72:75]\n\t"
    "v_mfma_f32_16x16x32_bf16 v[76:79], v[172:175], v[108:111], v[76:79]\n\t"
    "s_waitcnt lgkmcnt(4)\n\t"
    "v_mfma_f32_16x16x32_bf16 v[64:67], v[176:179], v[112:115], v[64:67]\n\t"
    "ds_read_b128 v[104:107], %[da] offset:16384\n\t"
    "v_mfma_f32_16x16x32_bf16 v[68:71], v[180:183], v[112:115], v[68:71]\n\t"
    "ds_read_b128 v[108:111], %[da] offset:18432\n\t"
    "v_mfma_f32_16x16x32_bf16 v[72:75], v[176:179], v[116:119], v[72:75]\n\t"
    "v_mfma_f32_16x16x32_bf16 v[76:79], v[180:183], v[116:119], v[76:79]\n\t"
    "s_waitcnt lgkmcnt(4)\n\t"
    "v_mfma_f32_16x16x32_bf16 v[64:67], v[184:187], v[120:123], v[64:67]\n\t"
    "ds_read_b128 v[112:115], %[ea] offset:20480\n\t"
    "v_mfma_f32_16x16x32_bf16 v[68:71], v[188:191], v[120:123], v[68:71]\n\t"
    "ds_read_b128 v[116:119], %[ea] offset:22528\n\t"
    "v_mfma_f32_16x16x32_bf16 v[72:75], v[184:187], v[124:127], v[72:75]\n\t"
    "v_mfma_f32_16x16x32_bf16 v[76:79], v[188:191], v[124:127], v[76:79]\n\t"
    "s_waitcnt vmcnt(0)\n\t"
    "s_barrier\n\t"
    "s_waitcnt lgkmcnt(4)\n\t"
    "v_mfma_f32_16x16x32_bf16 v[64:67], v[192:195], v[96:99], v[64:67]\n\t"
    "ds_read_b128 v[120:123], %[da] offset:20480\n\t"
    "v_mfma_f32_16x16x32_bf16 v[68:71], v[196:199], v[96:99], v[68:71]\n\t"
    "ds_read_b128 v[124:127], %[da] offset:22528\n\t"
    "v_mfma_f32_16x16x32_bf16 v[72:75], v[192:195], v[100:103], v[72:75]\n\t"
    "ds_read_b128 v[48:51], %[rw] offset:0\n\t"
    "v_mfma_f32_16x16x32_bf16 v[76:79], v[196:199], v[100:103], v[76:79]\n\t"
    "ds_read_b128 v[52:55], %[rw] offset:1024\n\t"
    "s_cmp_eq_u32 %[w0], 0\n\t"
    "s_cbranch_scc1 5f\n\t"
    "6:\n\t"
    "s_waitcnt lgkmcnt(6)\n\t"
    "v_mfma_f32_16x16x32_bf16 v[64:67], v[200:203], v[104:107], v[64:67]\n\t"
    "ds_read_b128 v[96:99], %[ea] offset:24576\n\t"
    "v_mfma_f32_16x16x32_bf16 v[68:71], v[204:207], v[104:107], v[68:71]\n\t"
    "ds_read_b128 v[100:103], %[ea] offset:26624\n\t"
    "v_mfma_f32_16x16x32_bf16 v[72:75], v[200:203], v[108:111], v[72:75]\n\t"
    "ds_read_b128 v[56:59], %[rw] offset:2048\n\t"
    "v_mfma_f32_16x16x32_bf16 v[76:79], v[204:207], v[108:111], v[76:79]\n\t"
    "ds_read_b128 v[60:63], %[rw] offset:3072\n\t"
    "s_waitcnt lgkmcnt(0)\n\t"
    "v_mfma_f32_16x16x32_bf16 v[64:67], v[208:211], v[112:115], v[64:67]\n\t"
    "ds_read_b128 v[104:107], %[da] offset:24576\n\t"
    "v_add_f32 v48, v48, v80\n\t"
    "v_add_f32 v49, v49, v81\n\t"
    "v_add_f32 v50, v50, v82\n\t"
    "v_mfma_f32_16x16x32_bf16 v[68:71], v[212:215], v[112:115], v[68:71]\n\t"
    "ds_read_b128 v[108:111], %[da] offset:26624\n\t"
    "v_add_f32 v51, v51, v83\n\t"
    "v_add_f32 v52, v52, v84\n\t"
    "v_add_f32 v53, v53, v85\n\t"
    "v_mfma_f32_16x16x32_bf16 v[72:75], v[208:211], v[116:119], v[72:75]\n\t"
    "ds_read_b32 v39, %[sa]\n\t"
    "v_add_f32 v54, v54, v86\n\t"
    "v_add_f32 v55, v55, v87\n\t"
    "v_add_f32 v56, v56, v88\n\t"
    "v_mfma_f32_16x16x32_bf16 v[76:79], v[212:215], v[116:119], v[76:79]\n\t"
    "v_add_f32 v57, v57, v89\n\t"
    "v_add_f32 v58, v58, v90\n\t"
    "v_add_f32 v59, v59, v91\n\t"
    "v_mfma_f32_16x16x32_bf16 v[64:67], v[216:219], v[120:123], v[64:67]\n\t"
    "ds_read_b128 v[112:115], %[ea] offset:28672\n\t"
    "v_add_f32 v60, v60, v92\n\t"
    "v_add_f32 v61, v61, v93\n\t"
    "v_add_f32 v62, v62, v94\n\t"
    "v_mfma_f32_16x16x32_bf16 v[68:71], v[220:223], v[120:123], v[68:71]\n\t"
    "ds_read_b128 v[116:119], %[ea] offset:30720\n\t"
    "v_add_f32 v63, v63, v95\n\t"
    "v_mul_f32 v40, %[nl], v48\n\t"
    "v_mul_f32 v41, %[nl], v49\n\t"
    "v_mfma_f32_16x16x32_bf16 v[72:75], v[216:219], v[124:127], v[72:75]\n\t"
    "v_xor_b32 %[ea], 0x10000, %[ea]\n\t"
    "v_mul_f32 v42, %[nl], v50\n\t"
    "v_mul_f32 v43, %[nl], v51\n\t"
    "v_mul_f32 v44, %[nl], v56\n\t"
    "v_mfma_f32_16x16x32_bf16 v[76:79], v[220:223], v[124:127], v[76:79]\n\t"
    "v_mul_f32 v45, %[nl], v57\n\t"
    "v_mul_f32 v46, %[nl], v58\n\t"
    "v_mul_f32 v47, %[nl], v59\n\t"
    "v_mfma_f32_16x16x32_bf16 v[64:67], v[224:227], v[96:99], v[64:67]\n\t"
    "ds_read_b128 v[120:123], %[da] offset:28672\n\t"
    "v_exp_f32 v40, v40\n\t"
    "v_exp_f32 v41, v41\n\t"
    "v_exp_f32 v42, v42\n\t"
    "v_mfma_f32_16x16x32_bf16 v[68:71], v[228:231], v[96:99], v[68:71]\n\t"
    "ds_read_b128 v[124:127], %[da] offset:30720\n\t"
    "v_exp_f32 v43, v43\n\t"
    "v_exp_f32 v44, v44\n\t"
    "v_exp_f32 v45, v45\n\t"
    "v_mfma_f32_16x16x32_bf16 v[72:75], v[224:227], v[100:103], v[72:75]\n\t"
    "v_xor_b32 %[da], 0x10000, %[da]\n\t"
    "v_exp_f32 v46, v46\n\t"
    "v_exp_f32 v47, v47\n\t"
    "v_add_f32 v40, 1.0, v40\n\t"
    "v_mfma_f32_16x16x32_bf16 v[76:79], v[228:231], v[100:103], v[76:79]\n\t"
    "v_add_f32 v41, 1.0, v41\n\t"
    "v_add_f32 v42, 1.0, v42\n\t"
    "v_add_f32 v43, 1.0, v43\n\t"
    "s_waitcnt lgkmcnt(5)\n\t"
    "v_mfma_f32_16x16x32_bf16 v[64:67], v[232:235], v[104:107], v[64:67]\n\t"
    "ds_read_b128 v[96:99], %[ea] offset:0\n\t"
    "v_add_f32 v44, 1.0, v44\n\t"
    "v_add_f32 v45, 1.0, v45\n\t"
    "v_add_f32 v46, 1.0, v46\n\t"
    "v_mfma_f32_16x16x32_bf16 v[68:71], v[236:239], v[104:107], v[68:71]\n\t"
    "ds_read_b128 v[100:103], %[ea] offset:2048\n\t"
    "v_add_f32 v47, 1.0, v47\n\t"
    "v_rcp_f32 v40, v40\n\t"
    "v_rcp_f32 v41, v41\n\t"
    "v_mfma_f32_16x16x32_bf16 v[72:75], v[232:235], v[108:111], v[72:75]\n\t"
    "v_rcp_f32 v42, v42\n\t"
    "v_rcp_f32 v43, v43\n\t"
    "v_rcp_f32 v44, v44\n\t"
    "v_mfma_f32_16x16x32_bf16 v[76:79], v[236:239], v[108:111], v[76:79]\n\t"
    "v_rcp_f32 v45, v45\n\t"
    "v_rcp_f32 v46, v46\n\t"
    "v_rcp_f32 v47, v47\n\t"
    "s_waitcnt lgkmcnt(4)\n\t"
    "v_mfma_f32_16x16x32_bf16 v[64:67], v[240:243], v[112:115], v[64:67]\n\t"
    "ds_read_b128 v[104:107], %[da] offset:0\n\t"
    "v_mad_u32_u24 v39, v39, %[mu], %[ad]\n\t"
    "v_mul_f32 v40, v48, v40\n\t"
    "v_mul_f32 v41, v49, v41\n\t"
    "v_mul_f32 v42, v50, v42\n\t"
    "v_mfma_f32_16x16x32_bf16 v[68:71], v[244:247], v[112:115], v[68:71]\n\t"
    "ds_read_b128 v[108:111], %[da] offset:2048\n\t"
    "v_cmp_ge_i32 vcc, %[lf], v39\n\t"
    "s_cmp_eq_u64 vcc, 0\n\t"
    "s_addc_u32 %[lf], %[lf], 0\n\t"
    "v_mul_f32 v43, v51, v43\n\t"
    "v_mul_f32 v44, v56, v44\n\t"
    "v_mul_f32 v45, v57, v45\n\t"
    "v_mfma_f32_16x16x32_bf16 v[72:75], v[240:243], v[116:119], v[72:75]\n\t"
    "v_mul_f32 v46, v58, v46\n\t"
    "v_mul_f32 v47, v59, v47\n\t"
    "v_mul_f32 v40, v40, v52\n\t"
    "v_mfma_f32_16x16x32_bf16 v[76:79], v[244:247], v[116:119], v[76:79]\n\t"
    "v_cmp_ge_i32 vcc, %[lf], v39\n\t"
    "s_cmp_eq_u64 vcc, 0\n\t"
    "s_addc_u32 %[lf], %[lf], 0\n\t"
    "v_mul_f32 v41, v41, v53\n\t"
    "v_mul_f32 v42, v42, v54\n\t"
    "v_mul_f32 v43, v43, v55\n\t"
    "s_waitcnt lgkmcnt(4)\n\t"
    "v_mfma_f32_16x16x32_bf16 v[80:83], v[248:251], v[120:123], v[64:67]\n\t"
    "ds_read_b128 v[112:115], %[ea] offset:4096\n\t"
    "s_min_i32 %[lf], %[lf], %[cp]\n\t"
    "s_add_u32 %[b], %[b], 1\n\t"
    "v_mul_f32 v44, v44, v60\n\t"
    "v_mul_f32 v45, v45, v61\n\t"
    "v_mul_f32 v46, v46, v62\n\t"
    "v_mfma_f32_16x16x32_bf16 v[84:87], v[252:255], v[120:123], v[68:71]\n\t"
    "ds_read_b128 v[116:119], %[ea] offset:6144\n\t"
    "v_mul_f32 v47, v47, v63\n\t"
    "v_cvt_pk_bf16_f32 v48, v40, v41\n\t"
    "v_cvt_pk_bf16_f32 v49, v42, v43\n\t"
    "v_mfma_f32_16x16x32_bf16 v[88:91], v[248:251], v[124:127], v[72:75]\n\t"
    "v_cvt_pk_bf16_f32 v50, v44, v45\n\t"
    "v_cvt_pk_bf16_f32 v51, v46, v47\n\t"
    "s_nop 1\n\t"
    "v_mfma_f32_16x16x32_bf16 v[92:95], v[252:255], v[124:127], v[76:79]\n\t"
    "v_permlane16_swap_b32 v48, v50\n\t"
    "v_permlane16_swap_b32 v49, v51\n\t"
    "global_store_dwordx4 %[co0], v[48:51], s[36:37] sc1\n\t"
    "s_mov_b64 s[36:37], s[38:39]\n\t"
    "s_add_u32 s38, s38, %[ab]\n\t"
    "s_addc_u32 s39, s39, 0\n\t"
    "s_sub_u32 m0, %[lf], %[b]\n\t"
    "s_sub_u32 m0, m0, 1\n\t"
    "s_cmp_lt_u32 m0, %[up]\n\t"
    "s_cbranch_scc1 1b\n\t"
    "s_waitcnt lgkmcnt(0)\n\t"
    "s_nop 4\n\t"
    "s_branch 9f\n\t"
    "5:\n\t"
    "s_add_u32 %[st], %[b], %[po]\n\t"
    "s_max_i32 %[st], %[st], %[p0]\n\t"
    "v_mov_b32 v39, %[st]\n\t"
    "v_mov_b32 v38, 0\n\t"
    "s_mov_b64 exec, 1\n\t"
    "global_store_dword v38, v39, %[pg] sc1\n\t"
    "s_mov_b64 exec, -1\n\t"
    "s_mov_b32 %[p0], %[p1]\n\t"
    "s_branch 6b\n\t"
    "7:\n\t"
    "s_cmp_ge_i32 %[lf], %[cp]\n\t"
    "s_cbranch_scc1 8b\n\t"
    "s_mov_b32 m0, %[sl]\n\t"
    "s_nop 0\n\t"
    "global_load_lds_dword %[dv], %[db] sc1\n\t"
    "s_branch 8b\n\t"
    "9:"
    : "=&{v[80:95]}"(pd), [ea] "+&v"(ea), [da] "+&v"(da), [b] "+&s"(b), [lf] "+&s"(lf), "+{s[36:37]}"(p1), "+{s[38:39]}"(p0), [sa] "+&v"(sa), [st] "=&s"(st), [sl] "+&s"(sl), [p0] "+&s"(pf)
    : [rw] "v"(rw), [ab] "s"(ab), [cp] "s"(cp), [mu] "s"(mu), [ad] "v"(ad), [up] "s"(up), "{v[128:159]}"(W[0]), "{v[160:191]}"(W[1]), "{v[192:223]}"(W[2]), "{v[224:255]}"(W[3]), [co0] "v"(co0), [w0] "s"(w0), [dv] "v"(dv), [db] "s"(db), [pg] "s"(pg), [po] "s"(po - 1), [p1] "s"(b + po), [nl] "s"(-LOG2E)
    : "v38", "v39", "v40", "v41", "v42", "v43", "v44", "v45", "v46", "v47", "v48", "v49", "v50", "v51", "v52", "v53", "v54", "v55", "v56", "v57", "v58", "v59", "v60", "v61", "v62", "v63", "v64", "v65", "v66", "v67", "v68", "v69", "v70", "v71", "v72", "v73", "v74", "v75", "v76", "v77", "v78", "v79", "v96", "v97", "v98", "v99", "v100", "v101", "v102", "v103", "v104", "v105", "v106", "v107", "v108", "v109", "v110", "v111", "v112", "v113", "v114", "v115", "v116", "v117", "v118", "v119", "v120", "v121", "v122", "v123", "v124", "v125", "v126", "v127", "vcc", "scc", "m0", "memory");
}
__device__ __forceinline__ void fast_st2(const u32x32 (&W)[4], u32x16& pd, unsigned ea, unsigned da, int& b, int& lf, int cp, unsigned up, unsigned sa, unsigned mu, unsigned ad,
    unsigned rw, unsigned ab, const char* p1, const char* p0, unsigned co0, unsigned co1, int po, int pf, unsigned w0, unsigned sl, unsigned dv, const char* db, const char* pg) {
  unsigned st;
  asm volatile("s_barrier\n\t"
    "ds_read_b128 v[96:99], %[ea] offset:0\n\t"
    "ds_read_b128 v[100:103], %[ea] offset:2048\n\t"
    "ds_read_b128 v[104:107], %[da] offset:0\n\t"
    "ds_read_b128 v[108:111], %[da] offset:2048\n\t"
    "ds_read_b128 v[112:115], %[ea] offset:4096\n\t"
    "ds_read_b128 v[116:119], %[ea] offset:6144\n\t"
    "s_branch 2f\n\t"
    ".p2align 6\n\t"
    "1:\n\t"
    "s_barrier\n\t"
    "2:\n\t"
    "s_waitcnt lgkmcnt(4)\n\t"
    "v_mfma_f32_16x16x32_bf16 v[64:67], v[128:131], v[96:99], 0\n\t"
    "ds_read_b128 v[120:123], %[da] offset:4096\n\t"
    "v_mfma_f32_16x16x32_bf16 v[68:71], v[132:135], v[96:99], 0\n\t"
    "ds_read_b128 v[124:127], %[da] offset:6144\n\t"
    "v_mfma_f32_16x16x32_bf16 v[72:75], v[128:131], v[100:103], 0\n\t"
    "v_mfma_f32_16x16x32_bf16 v[76:79], v[132:135], v[100:103], 0\n\t"
    "s_waitcnt lgkmcnt(4)\n\t"
    "v_mfma_f32_16x16x32_bf16 v[64:67], v[136:139], v[104:107], v[64:67]\n\t"
    "ds_read_b128 v[96:99], %[ea] offset:8192\n\t"
    "v_mfma_f32_16x16x32_bf16 v[68:71], v[140:143], v[104:107], v[68:71]\n\t"
    "ds_read_b128 v[100:103], %[ea] offset:10240\n\t"
    "v_mfma_f32_16x16x32_bf16 v[72:75], v[136:139], v[108:111], v[72:75]\n\t"
    "v_mfma_f32_16x16x32_bf16 v[76:79], v[140:143], v[108:111], v[76:79]\n\t"
    "s_cmp_eq_u32 %[w0], 1\n\t"
    "s_cbranch_scc1 7f\n\t"
    "8:\n\t"
    "s_waitcnt lgkmcnt(4)\n\t"
    "v_mfma_f32_16x16x32_bf16 v[64:67], v[144:147], v[112:115], v[64:67]\n\t"
    "ds_read_b128 v[104:107], %[da] offset:8192\n\t"
    "v_mfma_f32_16x16x32_bf16 v[68:71], v[148:151], v[112:115], v[68:71]\n\t"
    "ds_read_b128 v[108:111], %[da] offset:10240\n\t"
    "v_mfma_f32_16x16x32_bf16 v[72:75], v[144:147], v[116:119], v[72:75]\n\t"
    "v_mfma_f32_16x16x32_bf16 v[76:79], v[148:151], v[116:119], v[76:79]\n\t"
    "s_waitcnt lgkmcnt(4)\n\t"
    "v_mfma_f32_16x16x32_bf16 v[64:67], v[152:155], v[120:123], v[64:67]\n\t"
    "ds_read_b128 v[112:115], %[ea] offset:12288\n\t"
    "v_mfma_f32_16x16x32_bf16 v[68:71], v[156:159], v[120:123], v[68:71]\n\t"
    "ds_read_b128 v[116:119], %[ea] offset:14336\n\t"
    "v_mfma_f32_16x16x32_bf16 v[72:75], v[152:155], v[124:127], v[72:75]\n\t"
    "v_mfma_f32_16x16x32_bf16 v[76:79], v[156:159], v[124:127], v[76:79]\n\t"
    "s_waitcnt lgkmcnt(4)\n\t"
    "v_mfma_f32_16x16x32_bf16 v[64:67], v[160:163], v[96:99], v[64:67]\n\t"
    "ds_read_b128 v[120:123], %[da] offset:12288\n\t"
    "v_mfma_f32_16x16x32_bf16 v[68:71], v[164:167], v[96:99], v[68:71]\n\t"
    "ds_read_b128 v[124:127], %[da] offset:14336\n\t"
    "v_mfma_f32_16x16x32_bf16 v[72:75], v[160:163], v[100:103], v[72:75]\n\t"
    "v_mfma_f32_16x16x32_bf16 v[76:79], v[164:167], v[100:103], v[76:79]\n\t"
    "s_waitcnt lgkmcnt(4)\n\t"
    "v_mfma_f32_16x16x32_bf16 v[64:67], v[168:171], v[104:107], v[64:67]\n\t"
    "ds_read_b128 v[96:99], %[ea] offset:16384\n\t"
    "v_mfma_f32_16x16x32_bf16 v[68:71], v[172:175], v[104:107], v[68:71]\n\t"
    "ds_read_b128 v[100:103], %[ea] offset:18432\n\t"
    "v_mfma_f32_16x16x32_bf16 v[72:75], v[168:171], v[108:111], v[72:75]\n\t"
    "v_mfma_f32_16x16x32_bf16 v[76:79], v[172:175], v[108:111], v[76:79]\n\t"
    "s_waitcnt lgkmcnt(4)\n\t"
    "v_mfma_f32_16x16x32_bf16 v[64:67], v[176:179], v[112:115], v[64:67]\n\t"
    "ds_read_b128 v[104:107], %[da] offset:16384\n\t"
    "v_mfma_f32_16x16x32_bf16 v[68:71], v[180:183], v[112:115], v[68:71]\n\t"
    "ds_read_b128 v[108:111], %[da] offset:18432\n\t"
    "v_mfma_f32_16x16x32_bf16 v[72:75], v[176:179], v[116:119], v[72:75]\n\t"
    "v_mfma_f32_16x16x32_bf16 v[76:79], v[180:183], v[116:119], v[76:79]\n\t"
    "s_waitcnt lgkmcnt(4)\n\t"
    "v_mfma_f32_16x16x32_bf16 v[64:67], v[184:187], v[120:123], v[64:67]\n\t"
    "ds_read_b128 v[112:115], %[ea] offset:20480\n\t"
    "v_mfma_f32_16x16x32_bf16 v[68:71], v[188:191], v[120:123], v[68:71]\n\t"
    "ds_read_b128 v[116:119], %[ea] offset:22528\n\t"
    "v_mfma_f32_16x16x32_bf16 v[72:75], v[184:187], v[124:127], v[72:75]\n\t"
    "v_mfma_f32_16x16x32_bf16 v[76:79], v[188:191], v[124:127], v[76:79]\n\t"
    "s_waitcnt vmcnt(0)\n\t"
    "s_barrier\n\t"
    "s_waitcnt lgkmcnt(4)\n\t"
    "v_mfma_f32_16x16x32_bf16 v[64:67], v[192:195], v[96:99], v[64:67]\n\t"
    "ds_read_b128 v[120:123], %[da] offset:20480\n\t"
    "v_mfma_f32_16x16x32_bf16 v[68:71], v[196:199], v[96:99], v[68:71]\n\t"
    "ds_read_b128 v[124:127], %[da] offset:22528\n\t"
    "v_mfma_f32_16x16x32_bf16 v[72:75], v[192:195], v[100:103], v[72:75]\n\t"
    "ds_read_b128 v[48:51], %[rw] offset:0\n\t"
    "v_mfma_f32_16x16x32_bf16 v[76:79], v[196:199], v[100:103], v[76:79]\n\t"
    "ds_read_b128 v[52:55], %[rw] offset:1024\n\t"
    "s_cmp_eq_u32 %[w0], 0\n\t"
    "s_cbranch_scc1 5f\n\t"
    "6:\n\t"
    "s_waitcnt lgkmcnt(6)\n\t"
    "v_mfma_f32_16x16x32_bf16 v[64:67], v[200:203], v[104:107], v[64:67]\n\t"
    "ds_read_b128 v[96:99], %[ea] offset:24576\n\t"
    "v_mfma_f32_16x16x32_bf16 v[68:71], v[204:207], v[104:107], v[68:71]\n\t"
    "ds_read_b128 v[100:103], %[ea] offset:26624\n\t"
    "v_mfma_f32_16x16x32_bf16 v[72:75], v[200:203], v[108:111], v[72:75]\n\t"
    "ds_read_b128 v[56:59], %[rw] offset:2048\n\t"
    "v_mfma_f32_16x16x32_bf16 v[76:79], v[204:207], v[108:111], v[76:79]\n\t"
    "ds_read_b128 v[60:63], %[rw] offset:3072\n\t"
    "s_waitcnt lgkmcnt(0)\n\t"
    "v_mfma_f32_16x16x32_bf16 v[64:67], v[208:211], v[112:115], v[64:67]\n\t"
    "ds_read_b128 v[104:107], %[da] offset:24576\n\t"
    "v_cvt_pk_bf16_f32 v40, v80, v81\n\t"
    "v_cvt_pk_bf16_f32 v41, v82, v83\n\t"
    "v_mfma_f32_16x16x32_bf16 v[68:71], v[212:215], v[112:115], v[68:71]\n\t"
    "ds_read_b128 v[108:111], %[da] offset:26624\n\t"
    "v_cvt_pk_bf16_f32 v42, v84, v85\n\t"
    "v_mfma_f32_16x16x32_bf16 v[72:75], v[208:211], v[116:119], v[72:75]\n\t"
    "ds_read_b32 v39, %[sa]\n\t"
    "v_cvt_pk_bf16_f32 v43, v86, v87\n\t"
    "v_mfma_f32_16x16x32_bf16 v[76:79], v[212:215], v[116:119], v[76:79]\n\t"
    "global_store_dwordx4 %[co0], v[40:43], s[36:37] sc1\n\t"
    "v_mfma_f32_16x16x32_bf16 v[64:67], v[216:219], v[120:123], v[64:67]\n\t"
    "ds_read_b128 v[112:115], %[ea] offset:28672\n\t"
    "v_cvt_pk_bf16_f32 v44, v88, v89\n\t"
    "v_cvt_pk_bf16_f32 v45, v90, v91\n\t"
    "v_mfma_f32_16x16x32_bf16 v[68:71], v[220:223], v[120:123], v[68:71]\n\t"
    "ds_read_b128 v[116:119], %[ea] offset:30720\n\t"
    "v_cvt_pk_bf16_f32 v46, v92, v93\n\t"
    "v_mfma_f32_16x16x32_bf16 v[72:75], v[216:219], v[124:127], v[72:75]\n\t"
    "v_xor_b32 %[ea], 0x10000, %[ea]\n\t"
    "v_cvt_pk_bf16_f32 v47, v94, v95\n\t"
    "v_mfma_f32_16x16x32_bf16 v[76:79], v[220:223], v[124:127], v[76:79]\n\t"
    "global_store_dwordx4 %[co1], v[44:47], s[36:37] sc1\n\t"
    "v_mfma_f32_16x16x32_bf16 v[64:67], v[224:227], v[96:99], v[64:67]\n\t"
    "ds_read_b128 v[120:123], %[da] offset:28672\n\t"
    "v_cvt_pk_bf16_f32 v48, v48, v49\n\t"
    "v_cvt_pk_bf16_f32 v49, v50, v51\n\t"
    "v_mfma_f32_16x16x32_bf16 v[68:71], v[228:231], v[96:99], v[68:71]\n\t"
    "ds_read_b128 v[124:127], %[da] offset:30720\n\t"
    "v_cvt_pk_bf16_f32 v50, v52, v53\n\t"
    "v_mfma_f32_16x16x32_bf16 v[72:75], v[224:227], v[100:103], v[72:75]\n\t"
    "v_xor_b32 %[da], 0x10000, %[da]\n\t"
    "v_cvt_pk_bf16_f32 v51, v54, v55\n\t"
    "v_mfma_f32_16x16x32_bf16 v[76:79], v[228:231], v[100:103], v[76:79]\n\t"
    "global_store_dwordx4 %[co0], v[48:51], s[36:37] offset:2048 sc1\n\t"
    "s_waitcnt lgkmcnt(5)\n\t"
    "v_mfma_f32_16x16x32_bf16 v[64:67], v[232:235], v[104:107], v[64:67]\n\t"
    "ds_read_b128 v[96:99], %[ea] offset:0\n\t"
    "v_cvt_pk_bf16_f32 v56, v56, v57\n\t"
    "v_cvt_pk_bf16_f32 v57, v58, v59\n\t"
    "v_mfma_f32_16x16x32_bf16 v[68:71], v[236:239], v[104:107], v[68:71]\n\t"
    "ds_read_b128 v[100:103], %[ea] offset:2048\n\t"
    "v_cvt_pk_bf16_f32 v58, v60, v61\n\t"
    "v_mfma_f32_16x16x32_bf16 v[72:75], v[232:235], v[108:111], v[72:75]\n\t"
    "v_cvt_pk_bf16_f32 v59, v62, v63\n\t"
    "v_mfma_f32_16x16x32_bf16 v[76:79], v[236:239], v[108:111], v[76:79]\n\t"
    "global_store_dwordx4 %[co1], v[56:59], s[36:37] offset:2048 sc1\n\t"
    "s_waitcnt lgkmcnt(4)\n\t"
    "v_mfma_f32_16x16x32_bf16 v[64:67], v[240:243], v[112:115], v[64:67]\n\t"
    "ds_read_b128 v[104:107], %[da] offset:0\n\t"
    "v_mad_u32_u24 v39, v39, %[mu], %[ad]\n\t"
    "v_mfma_f32_16x16x32_bf16 v[68:71], v[244:247], v[112:115], v[68:71]\n\t"
    "ds_read_b128 v[108:111], %[da] offset:2048\n\t"
    "v_cmp_ge_i32 vcc, %[lf], v39\n\t"
    "s_cmp_eq_u64 vcc, 0\n\t"
    "s_addc_u32 %[lf], %[lf], 0\n\t"
    "v_mfma_f32_16x16x32_bf16 v[72:75], v[240:243], v[116:119], v[72:75]\n\t"
    "v_mfma_f32_16x16x32_bf16 v[76:79], v[244:247], v[116:119], v[76:79]\n\t"
    "v_cmp_ge_i32 vcc, %[lf], v39\n\t"
    "s_cmp_eq_u64 vcc, 0\n\t"
    "s_addc_u32 %[lf], %[lf], 0\n\t"
    "s_waitcnt lgkmcnt(4)\n\t"
    "v_mfma_f32_16x16x32_bf16 v[80:83], v[248:251], v[120:123], v[64:67]\n\t"
    "ds_read_b128 v[112:115], %[ea] offset:4096\n\t"
    "s_min_i32 %[lf], %[lf], %[cp]\n\t"
    "s_add_u32 %[b], %[b], 1\n\t"
    "v_mfma_f32_16x16x32_bf16 v[84:87], v[252:255], v[120:123], v[68:71]\n\t"
    "ds_read_b128 v[116:119], %[ea] offset:6144\n\t"
    "v_mfma_f32_16x16x32_bf16 v[88:91], v[248:251], v[124:127], v[72:75]\n\t"
    "v_mfma_f32_16x16x32_bf16 v[92:95], v[252:255], v[124:127], v[76:79]\n\t"
    "s_mov_b64 s[36:37], s[38:39]\n\t"
    "s_add_u32 s38, s38, %[ab]\n\t"
    "s_addc_u32 s39, s39, 0\n\t"
    "s_sub_u32 m0, %[lf], %[b]\n\t"
    "s_sub_u32 m0, m0, 1\n\t"
    "s_cmp_lt_u32 m0, %[up]\n\t"
    "s_cbranch_scc1 1b\n\t"
    "s_waitcnt lgkmcnt(0)\n\t"
    "s_nop 4\n\t"
    "s_branch 9f\n\t"
    "5:\n\t"
    "s_add_u32 %[st], %[b], %[po]\n\t"
    "s_max_i32 %[st], %[st], %[p0]\n\t"
    "v_mov_b32 v39, %[st]\n\t"
    "v_mov_b32 v38, 0\n\t"
    "s_mov_b64 exec, 1\n\t"
    "global_store_dword v38, v39, %[pg] sc1\n\t"
    "s_mov_b64 exec, -1\n\t"
    "s_mov_b32 %[p0], %[p1]\n\t"
    "s_branch 6b\n\t"
    "7:\n\t"
    "s_cmp_ge_i32 %[lf], %[cp]\n\t"
    "s_cbranch_scc1 8b\n\t"
    "s_mov_b32 m0, %[sl]\n\t"
    "s_nop 0\n\t"
    "global_load_lds_dword %[dv], %[db] sc1\n\t"
    "s_branch 8b\n\t"
    "9:"
    : "=&{v[80:95]}"(pd), [ea] "+&v"(ea), [da] "+&v"(da), [b] "+&s"(b), [lf] "+&s"(lf), "+{s[36:37]}"(p1), "+{s[38:39]}"(p0), [sa] "+&v"(sa), [st] "=&s"(st), [sl] "+&s"(sl), [p0] "+&s"(pf)
    : [rw] "v"(rw), [ab] "s"(ab), [cp] "s"(cp), [mu] "s"(mu), [ad] "v"(ad), [up] "s"(up), "{v[128:159]}"(W[0]), "{v[160:191]}"(W[1]), "{v[192:223]}"(W[2]), "{v[224:255]}"(W[3]), [co0] "v"(co0), [w0] "s"(w0), [dv] "v"(dv), [db] "s"(db), [pg] "s"(pg), [po] "s"(po - 1), [p1] "s"(b + po), [co1] "v"(co1)
    : "v38", "v39", "v40", "v41", "v42", "v43", "v44", "v45", "v46", "v47", "v48", "v49", "v50", "v51", "v52", "v53", "v54", "v55", "v56", "v57", "v58", "v59", "v60", "v61", "v62", "v63", "v64", "v65", "v66", "v67", "v68", "v69", "v70", "v71", "v72", "v73", "v74", "v75", "v76", "v77", "v78", "v79", "v96", "v97", "v98", "v99", "v100", "v101", "v102", "v103", "v104", "v105", "v106", "v107", "v108", "v109", "v110", "v111", "v112", "v113", "v114", "v115", "v116", "v117", "v118", "v119", "v120", "v121", "v122", "v123", "v124", "v125", "v126", "v127", "vcc", "scc", "m0", "memory");
}
__device__ __forceinline__ void lean_ld0(const u32x32 (&W)[4], unsigned ea, unsigned da, unsigned x, unsigned lb, const char* s0, const char* s1, const unsigned (&av)[8], unsigned ps, unsigned dv, const char* db, unsigned rw, unsigned fa, unsigned fv,
    unsigned ga, unsigned mu, unsigned ad, int th, int tb, unsigned& sv, unsigned& su, unsigned& got) {
  asm volatile("s_setprio 2\n\t"
    "v_mov_b32 v76, %[ga]\n\t"
    "ds_read_b32 v76, v76 offset:24\n\t"
    "ds_read_b128 v[96:99], %[ea] offset:0\n\t"
    "ds_read_b128 v[100:103], %[ea] offset:2048\n\t"
    "ds_read_b128 v[104:107], %[da] offset:0\n\t"
    "ds_read_b128 v[108:111], %[da] offset:2048\n\t"
    "ds_read_b128 v[112:115], %[ea] offset:4096\n\t"
    "ds_read_b128 v[116:119], %[ea] offset:6144\n\t"
    "s_waitcnt lgkmcnt(6)\n\t"
    "v_cmp_ne_u32 vcc, 0, v76\n\t"
    "s_mov_b32 %[got], -1\n\t"
    "s_cbranch_vccnz 12f\n\t"
    "s_waitcnt lgkmcnt(4)\n\t"
    "v_mfma_f32_16x16x32_bf16 v[80:83], v[128:131], v[96:99], 0\n\t"
    "ds_read_b128 v[120:123], %[da] offset:4096\n\t"
    "v_mfma_f32_16x16x32_bf16 v[84:87], v[132:135], v[96:99], 0\n\t"
    "ds_read_b128 v[124:127], %[da] offset:6144\n\t"
    "v_mfma_f32_16x16x32_bf16 v[88:91], v[128:131], v[100:103], 0\n\t"
    "v_mfma_f32_16x16x32_bf16 v[92:95], v[132:135], v[100:103], 0\n\t"
    "s_waitcnt vmcnt(24)\n\t"
    "s_cmp_lt_u32 %[x], 2\n\t"
    "s_cbranch_scc0 41f\n\t"
    "s_waitcnt vmcnt(8)\n\t"
    "41:\n\t"
    "s_barrier\n\t"
    "s_waitcnt lgkmcnt(4)\n\t"
    "v_mfma_f32_16x16x32_bf16 v[80:83], v[136:139], v[104:107], v[80:83]\n\t"
    "ds_read_b128 v[96:99], %[ea] offset:8192\n\t"
    "v_mfma_f32_16x16x32_bf16 v[84:87], v[140:143], v[104:107], v[84:87]\n\t"
    "ds_read_b128 v[100:103], %[ea] offset:10240\n\t"
    "v_mfma_f32_16x16x32_bf16 v[88:91], v[136:139], v[108:111], v[88:91]\n\t"
    "v_mfma_f32_16x16x32_bf16 v[92:95], v[140:143], v[108:111], v[92:95]\n\t"
    "s_waitcnt lgkmcnt(4)\n\t"
    "v_mfma_f32_16x16x32_bf16 v[80:83], v[144:147], v[112:115], v[80:83]\n\t"
    "ds_read_b128 v[104:107], %[da] offset:8192\n\t"
    "v_mfma_f32_16x16x32_bf16 v[84:87], v[148:151], v[112:115], v[84:87]\n\t"
    "ds_read_b128 v[108:111], %[da] offset:10240\n\t"
    "v_mfma_f32_16x16x32_bf16 v[88:91], v[144:147], v[116:119], v[88:91]\n\t"
    "v_mfma_f32_16x16x32_bf16 v[92:95], v[148:151], v[116:119], v[92:95]\n\t"
    "s_waitcnt lgkmcnt(4)\n\t"
    "v_mfma_f32_16x16x32_bf16 v[80:83], v[152:155], v[120:123], v[80:83]\n\t"
    "ds_read_b128 v[112:115], %[ea] offset:12288\n\t"
    "v_mfma_f32_16x16x32_bf16 v[84:87], v[156:159], v[120:123], v[84:87]\n\t"
    "ds_read_b128 v[116:119], %[ea] offset:14336\n\t"
    "v_mfma_f32_16x16x32_bf16 v[88:91], v[152:155], v[124:127], v[88:91]\n\t"
    "v_mfma_f32_16x16x32_bf16 v[92:95], v[156:159], v[124:127], v[92:95]\n\t"
    "s_waitcnt lgkmcnt(4)\n\t"
    "v_mfma_f32_16x16x32_bf16 v[80:83], v[160:163], v[96:99], v[80:83]\n\t"
    "ds_read_b128 v[120:123], %[da] offset:12288\n\t"
    "v_mfma_f32_16x16x32_bf16 v[84:87], v[164:167], v[96:99], v[84:87]\n\t"
    "ds_read_b128 v[124:127], %[da] offset:14336\n\t"
    "v_mfma_f32_16x16x32_bf16 v[88:91], v[160:163], v[100:103], v[88:91]\n\t"
    "v_mfma_f32_16x16x32_bf16 v[92:95], v[164:167], v[100:103], v[92:95]\n\t"
    "s_waitcnt vmcnt(20)\n\t"
    "s_cmp_lt_u32 %[x], 2\n\t"
    "s_cbranch_scc0 45f\n\t"
    "s_waitcnt vmcnt(4)\n\t"
    "45:\n\t"
    "s_barrier\n\t"
    "s_waitcnt lgkmcnt(4)\n\t"
    "v_mfma_f32_16x16x32_bf16 v[80:83], v[168:171], v[104:107], v[80:83]\n\t"
    "ds_read_b128 v[96:99], %[ea] offset:16384\n\t"
    "v_mfma_f32_16x16x32_bf16 v[84:87], v[172:175], v[104:107], v[84:87]\n\t"
    "ds_read_b128 v[100:103], %[ea] offset:18432\n\t"
    "v_mfma_f32_16x16x32_bf16 v[88:91], v[168:171], v[108:111], v[88:91]\n\t"
    "v_mfma_f32_16x16x32_bf16 v[92:95], v[172:175], v[108:111], v[92:95]\n\t"
    "s_waitcnt lgkmcnt(4)\n\t"
    "v_mfma_f32_16x16x32_bf16 v[80:83], v[176:179], v[112:115], v[80:83]\n\t"
    "ds_read_b128 v[104:107], %[da] offset:16384\n\t"
    "v_mfma_f32_16x16x32_bf16 v[84:87], v[180:183], v[112:115], v[84:87]\n\t"
    "ds_read_b128 v[108:111], %[da] offset:18432\n\t"
    "v_mfma_f32_16x16x32_bf16 v[88:91], v[176:179], v[116:119], v[88:91]\n\t"
    "v_mfma_f32_16x16x32_bf16 v[92:95], v[180:183], v[116:119], v[92:95]\n\t"
    "s_waitcnt lgkmcnt(4)\n\t"
    "v_mfma_f32_16x16x32_bf16 v[80:83], v[184:187], v[120:123], v[80:83]\n\t"
    "ds_read_b128 v[112:115], %[ea] offset:20480\n\t"
    "v_mfma_f32_16x16x32_bf16 v[84:87], v[188:191], v[120:123], v[84:87]\n\t"
    "ds_read_b128 v[116:119], %[ea] offset:22528\n\t"
    "v_mfma_f32_16x16x32_bf16 v[88:91], v[184:187], v[124:127], v[88:91]\n\t"
    "v_mfma_f32_16x16x32_bf16 v[92:95], v[188:191], v[124:127], v[92:95]\n\t"
    "s_waitcnt lgkmcnt(4)\n\t"
    "v_mfma_f32_16x16x32_bf16 v[80:83], v[192:195], v[96:99], v[80:83]\n\t"
    "ds_read_b128 v[120:123], %[da] offset:20480\n\t"
    "v_mfma_f32_16x16x32_bf16 v[84:87], v[196:199], v[96:99], v[84:87]\n\t"
    "ds_read_b128 v[124:127], %[da] offset:22528\n\t"
    "v_mfma_f32_16x16x32_bf16 v[88:91], v[192:195], v[100:103], v[88:91]\n\t"
    "v_mfma_f32_16x16x32_bf16 v[92:95], v[196:199], v[100:103], v[92:95]\n\t"
    "s_waitcnt vmcnt(16)\n\t"
    "s_cmp_lt_u32 %[x], 2\n\t"
    "s_cbranch_scc0 49f\n\t"
    "s_waitcnt vmcnt(0)\n\t"
    "49:\n\t"
    "s_barrier\n\t"
    "s_waitcnt lgkmcnt(4)\n\t"
    "v_mfma_f32_16x16x32_bf16 v[80:83], v[200:203], v[104:107], v[80:83]\n\t"
    "ds_read_b128 v[96:99], %[ea] offset:24576\n\t"
    "v_mfma_f32_16x16x32_bf16 v[84:87], v[204:207], v[104:107], v[84:87]\n\t"
    "ds_read_b128 v[100:103], %[ea] offset:26624\n\t"
    "v_mfma_f32_16x16x32_bf16 v[88:91], v[200:203], v[108:111], v[88:91]\n\t"
    "v_mfma_f32_16x16x32_bf16 v[92:95], v[204:207], v[108:111], v[92:95]\n\t"
    "s_waitcnt lgkmcnt(4)\n\t"
    "v_mfma_f32_16x16x32_bf16 v[80:83], v[208:211], v[112:115], v[80:83]\n\t"
    "ds_read_b128 v[104:107], %[da] offset:24576\n\t"
    "v_mfma_f32_16x16x32_bf16 v[84:87], v[212:215], v[112:115], v[84:87]\n\t"
    "ds_read_b128 v[108:111], %[da] offset:26624\n\t"
    "v_mfma_f32_16x16x32_bf16 v[88:91], v[208:211], v[116:119], v[88:91]\n\t"
    "v_mfma_f32_16x16x32_bf16 v[92:95], v[212:215], v[116:119], v[92:95]\n\t"
    "s_cmp_eq_u32 %[ps], 0\n\t"
    "s_cbranch_scc1 10f\n\t"
    "global_load_dword %[sv], %[dv], %[db] sc1\n\t"
    "global_load_dword %[su], %[dv], %[db] offset:64 sc1\n\t"
    "10:\n\t"
    "s_waitcnt lgkmcnt(4)\n\t"
    "v_mfma_f32_16x16x32_bf16 v[80:83], v[216:219], v[120:123], v[80:83]\n\t"
    "ds_read_b128 v[112:115], %[ea] offset:28672\n\t"
    "v_mfma_f32_16x16x32_bf16 v[84:87], v[220:223], v[120:123], v[84:87]\n\t"
    "ds_read_b128 v[116:119], %[ea] offset:30720\n\t"
    "v_mfma_f32_16x16x32_bf16 v[88:91], v[216:219], v[124:127], v[88:91]\n\t"
    "v_mfma_f32_16x16x32_bf16 v[92:95], v[220:223], v[124:127], v[92:95]\n\t"
    "s_waitcnt lgkmcnt(4)\n\t"
    "v_mfma_f32_16x16x32_bf16 v[80:83], v[224:227], v[96:99], v[80:83]\n\t"
    "ds_read_b128 v[120:123], %[da] offset:28672\n\t"
    "v_mfma_f32_16x16x32_bf16 v[84:87], v[228:231], v[96:99], v[84:87]\n\t"
    "ds_read_b128 v[124:127], %[da] offset:30720\n\t"
    "v_mfma_f32_16x16x32_bf16 v[88:91], v[224:227], v[100:103], v[88:91]\n\t"
    "v_mfma_f32_16x16x32_bf16 v[92:95], v[228:231], v[100:103], v[92:95]\n\t"
    "s_waitcnt lgkmcnt(4)\n\t"
    "v_mfma_f32_16x16x32_bf16 v[80:83], v[232:235], v[104:107], v[80:83]\n\t"
    "v_mfma_f32_16x16x32_bf16 v[84:87], v[236:239], v[104:107], v[84:87]\n\t"
    "v_mfma_f32_16x16x32_bf16 v[88:91], v[232:235], v[108:111], v[88:91]\n\t"
    "v_mfma_f32_16x16x32_bf16 v[92:95], v[236:239], v[108:111], v[92:95]\n\t"
    "s_waitcnt lgkmcnt(2)\n\t"
    "v_mfma_f32_16x16x32_bf16 v[80:83], v[240:243], v[112:115], v[80:83]\n\t"
    "v_mfma_f32_16x16x32_bf16 v[84:87], v[244:247], v[112:115], v[84:87]\n\t"
    "v_mfma_f32_16x16x32_bf16 v[88:91], v[240:243], v[116:119], v[88:91]\n\t"
    "v_mfma_f32_16x16x32_bf16 v[92:95], v[244:247], v[116:119], v[92:95]\n\t"
    "s_waitcnt lgkmcnt(0)\n\t"
    "v_mfma_f32_16x16x32_bf16 v[80:83], v[248:251], v[120:123], v[80:83]\n\t"
    "s_memrealtime s[36:37]\n\t"
    "v_mfma_f32_16x16x32_bf16 v[84:87], v[252:255], v[120:123], v[84:87]\n\t"
    "v_mfma_f32_16x16x32_bf16 v[88:91], v[248:251], v[124:127], v[88:91]\n\t"
    "v_mfma_f32_16x16x32_bf16 v[92:95], v[252:255], v[124:127], v[92:95]\n\t"
    "s_mov_b32 %[got], 0\n\t"
    "v_mov_b32 v76, %[fa]\n\t"
    "v_mov_b32 v77, %[fv]\n\t"
    "v_add_u32 v78, 1, v77\n\t"
    "v_mov_b32 v79, %[ga]\n\t"
    "s_waitcnt lgkmcnt(0)\n\t"
    "ds_write_b128 %[rw], v[80:83] offset:0\n\t"
    "ds_write_b128 %[rw], v[84:87] offset:1024\n\t"
    "ds_write_b128 %[rw], v[88:91] offset:2048\n\t"
    "ds_write_b128 %[rw], v[92:95] offset:3072\n\t"
    "ds_write_b32 v76, v77\n\t"
    "s_cmp_eq_u32 %[ps], 0\n\t"
    "s_cbranch_scc1 9f\n\t"
    "s_waitcnt vmcnt(0)\n\t"
    "v_mad_u32_u24 v76, %[sv], %[mu], %[ad]\n\t"
    "v_cmp_ge_i32 vcc, %[th], v76\n\t"
    "s_cbranch_vccz 11f\n\t"
    "s_sub_u32 s36, s36, 20\n\t"
    "v_sub_u32 v76, s36, %[su]\n\t"
    "v_lshrrev_b32 v77, 24, %[su]\n\t"
    "v_lshlrev_b32 v76, 8, v76\n\t"
    "v_ashrrev_i32 v76, 31, v76\n\t"
    "v_add_u32 v77, v77, v76\n\t"
    "v_cmp_lt_i32 s[36:37], %[tb], v77\n\t"
    "s_andn2_b64 vcc, vcc, s[36:37]\n\t"
    "s_cbranch_scc1 9f\n\t"
    "11:\n\t"
    "ds_write2_b32 v79, v78, v78 offset1:1\n\t"
    "s_add_u32 m0, %[lb], 0\n\t"
    "s_nop 0\n\t"
    "global_load_lds_dwordx4 %[av0], %[s0] sc1\n\t"
    "s_add_u32 m0, %[lb], 32768\n\t"
    "s_nop 0\n\t"
    "global_load_lds_dwordx4 %[av1], %[s0] sc1\n\t"
    "s_add_u32 m0, %[lb], 4096\n\t"
    "s_nop 0\n\t"
    "global_load_lds_dwordx4 %[av2], %[s0] sc1\n\t"
    "s_add_u32 m0, %[lb], 36864\n\t"
    "s_nop 0\n\t"
    "global_load_lds_dwordx4 %[av3], %[s0] sc1\n\t"
    "s_add_u32 m0, %[lb], 8192\n\t"
    "s_nop 0\n\t"
    "global_load_lds_dwordx4 %[av4], %[s0] sc1\n\t"
    "s_add_u32 m0, %[lb], 40960\n\t"
    "s_nop 0\n\t"
    "global_load_lds_dwordx4 %[av5], %[s0] sc1\n\t"
    "s_add_u32 m0, %[lb], 12288\n\t"
    "s_nop 0\n\t"
    "global_load_lds_dwordx4 %[av6], %[s0] sc1\n\t"
    "s_add_u32 m0, %[lb], 45056\n\t"
    "s_nop 0\n\t"
    "global_load_lds_dwordx4 %[av7], %[s0] sc1\n\t"
    "s_add_u32 m0, %[lb], 16384\n\t"
    "s_nop 0\n\t"
    "global_load_lds_dwordx4 %[av0], %[s1] sc1\n\t"
    "s_add_u32 m0, %[lb], 49152\n\t"
    "s_nop 0\n\t"
    "global_load_lds_dwordx4 %[av1], %[s1] sc1\n\t"
    "s_add_u32 m0, %[lb], 20480\n\t"
    "s_nop 0\n\t"
    "global_load_lds_dwordx4 %[av2], %[s1] sc1\n\t"
    "s_add_u32 m0, %[lb], 53248\n\t"
    "s_nop 0\n\t"
    "global_load_lds_dwordx4 %[av3], %[s1] sc1\n\t"
    "s_add_u32 m0, %[lb], 24576\n\t"
    "s_nop 0\n\t"
    "global_load_lds_dwordx4 %[av4], %[s1] sc1\n\t"
    "s_add_u32 m0, %[lb], 57344\n\t"
    "s_nop 0\n\t"
    "global_load_lds_dwordx4 %[av5], %[s1] sc1\n\t"
    "s_add_u32 m0, %[lb], 28672\n\t"
    "s_nop 0\n\t"
    "global_load_lds_dwordx4 %[av6], %[s1] sc1\n\t"
    "s_add_u32 m0, %[lb], 61440\n\t"
    "s_nop 0\n\t"
    "global_load_lds_dwordx4 %[av7], %[s1] sc1\n\t"
    "s_mov_b32 %[got], 1\n\t"
    "s_branch 9f\n\t"
    "12:\n\t"
    "s_waitcnt lgkmcnt(0)\n\t"
    "9:"
    : [sv] "+&v"(sv), [su] "+&v"(su), [got] "=&s"(got)
    : [ea] "v"(ea), [da] "v"(da), [x] "s"(x), "{v[128:159]}"(W[0]), "{v[160:191]}"(W[1]), "{v[192:223]}"(W[2]), "{v[224:255]}"(W[3]), [lb] "s"(lb), [s0] "s"(s0), [s1] "s"(s1), [av0] "v"(av[0]), [av1] "v"(av[1]), [av2] "v"(av[2]), [av3] "v"(av[3]), [av4] "v"(av[4]), [av5] "v"(av[5]), [av6] "v"(av[6]), [av7] "v"(av[7]), [ps] "s"(ps), [dv] "v"(dv), [db] "s"(db), [rw] "v"(rw), [fa] "s"(fa), [fv] "s"(fv), [ga] "s"(ga), [mu] "s"(mu), [ad] "v"(ad), [th] "s"(th), [tb] "s"(tb)
    : "v96", "v97", "v98", "v99", "v100", "v101", "v102", "v103", "v104", "v105", "v106", "v107", "v108", "v109", "v110", "v111", "v112", "v113", "v114", "v115", "v116", "v117", "v118", "v119", "v120", "v121", "v122", "v123", "v124", "v125", "v126", "v127", "v76", "v77", "v78", "v79", "v80", "v81", "v82", "v83", "v84", "v85", "v86", "v87", "v88", "v89", "v90", "v91", "v92", "v93", "v94", "v95", "vcc", "scc", "m0", "memory", "s36", "s37", "s38", "s39");
}
__device__ __forceinline__ void lean_dm0(const u32x32 (&W)[4], unsigned ea, unsigned da, unsigned x, unsigned lb, const char* s0, const char* s1, const unsigned (&av)[8], unsigned ps, unsigned dv, const char* db, unsigned rw, unsigned fa, unsigned fv,
    unsigned ga, unsigned mu, unsigned ad, int th, int tb, unsigned& sv, unsigned& su, unsigned& got) {
  asm volatile("s_setprio 2\n\t"
    "v_mov_b32 v76, %[ga]\n\t"
    "ds_read_b32 v76, v76 offset:24\n\t"
    "ds_read_b128 v[96:99], %[ea] offset:0\n\t"
    "ds_read_b128 v[100:103], %[ea] offset:2048\n\t"
    "ds_read_b128 v[104:107], %[da] offset:0\n\t"
    "ds_read_b128 v[108:111], %[da] offset:2048\n\t"
    "ds_read_b128 v[112:115], %[ea] offset:4096\n\t"
    "ds_read_b128 v[116:119], %[ea] offset:6144\n\t"
    "s_waitcnt lgkmcnt(6)\n\t"
    "v_cmp_ne_u32 vcc, 0, v76\n\t"
    "s_mov_b32 %[got], -1\n\t"
    "s_cbranch_vccnz 12f\n\t"
    "s_waitcnt lgkmcnt(4)\n\t"
    "v_mfma_f32_16x16x32_bf16 v[80:83], v[128:131], v[96:99], 0\n\t"
    "ds_read_b128 v[120:123], %[da] offset:4096\n\t"
    "s_add_u32 m0, %[lb], 0\n\t"
    "v_mfma_f32_16x16x32_bf16 v[84:87], v[132:135], v[96:99], 0\n\t"
    "ds_read_b128 v[124:127], %[da] offset:6144\n\t"
    "global_load_lds_dwordx4 %[av0], %[s0] sc1\n\t"
    "v_mfma_f32_16x16x32_bf16 v[88:91], v[128:131], v[100:103], 0\n\t"
    "s_add_u32 m0, %[lb], 32768\n\t"
    "v_mfma_f32_16x16x32_bf16 v[92:95], v[132:135], v[100:103], 0\n\t"
    "global_load_lds_dwordx4 %[av1], %[s0] sc1\n\t"
    "s_waitcnt vmcnt(10)\n\t"
    "s_barrier\n\t"
    "s_waitcnt lgkmcnt(4)\n\t"
    "v_mfma_f32_16x16x32_bf16 v[80:83], v[136:139], v[104:107], v[80:83]\n\t"
    "ds_read_b128 v[96:99], %[ea] offset:8192\n\t"
    "s_add_u32 m0, %[lb], 4096\n\t"
    "v_mfma_f32_16x16x32_bf16 v[84:87], v[140:143], v[104:107], v[84:87]\n\t"
    "ds_read_b128 v[100:103], %[ea] offset:10240\n\t"
    "global_load_lds_dwordx4 %[av2], %[s0] sc1\n\t"
    "v_mfma_f32_16x16x32_bf16 v[88:91], v[136:139], v[108:111], v[88:91]\n\t"
    "s_add_u32 m0, %[lb], 36864\n\t"
    "v_mfma_f32_16x16x32_bf16 v[92:95], v[140:143], v[108:111], v[92:95]\n\t"
    "global_load_lds_dwordx4 %[av3], %[s0] sc1\n\t"
    "s_waitcnt lgkmcnt(4)\n\t"
    "v_mfma_f32_16x16x32_bf16 v[80:83], v[144:147], v[112:115], v[80:83]\n\t"
    "ds_read_b128 v[104:107], %[da] offset:8192\n\t"
    "s_add_u32 m0, %[lb], 8192\n\t"
    "v_mfma_f32_16x16x32_bf16 v[84:87], v[148:151], v[112:115], v[84:87]\n\t"
    "ds_read_b128 v[108:111], %[da] offset:10240\n\t"
    "global_load_lds_dwordx4 %[av4], %[s0] sc1\n\t"
    "v_mfma_f32_16x16x32_bf16 v[88:91], v[144:147], v[116:119], v[88:91]\n\t"
    "s_add_u32 m0, %[lb], 40960\n\t"
    "v_mfma_f32_16x16x32_bf16 v[92:95], v[148:151], v[116:119], v[92:95]\n\t"
    "global_load_lds_dwordx4 %[av5], %[s0] sc1\n\t"
    "s_waitcnt lgkmcnt(4)\n\t"
    "v_mfma_f32_16x16x32_bf16 v[80:83], v[152:155], v[120:123], v[80:83]\n\t"
    "ds_read_b128 v[112:115], %[ea] offset:12288\n\t"
    "s_add_u32 m0, %[lb], 12288\n\t"
    "v_mfma_f32_16x16x32_bf16 v[84:87], v[156:159], v[120:123], v[84:87]\n\t"
    "ds_read_b128 v[116:119], %[ea] offset:14336\n\t"
    "global_load_lds_dwordx4 %[av6], %[s0] sc1\n\t"
    "v_mfma_f32_16x16x32_bf16 v[88:91], v[152:155], v[124:127], v[88:91]\n\t"
    "s_add_u32 m0, %[lb], 45056\n\t"
    "v_mfma_f32_16x16x32_bf16 v[92:95], v[156:159], v[124:127], v[92:95]\n\t"
    "global_load_lds_dwordx4 %[av7], %[s0] sc1\n\t"
    "s_waitcnt lgkmcnt(4)\n\t"
    "v_mfma_f32_16x16x32_bf16 v[80:83], v[160:163], v[96:99], v[80:83]\n\t"
    "ds_read_b128 v[120:123], %[da] offset:12288\n\t"
    "s_add_u32 m0, %[lb], 16384\n\t"
    "v_mfma_f32_16x16x32_bf16 v[84:87], v[164:167], v[96:99], v[84:87]\n\t"
    "ds_read_b128 v[124:127], %[da] offset:14336\n\t"
    "global_load_lds_dwordx4 %[av0], %[s1] sc1\n\t"
    "v_mfma_f32_16x16x32_bf16 v[88:91], v[160:163], v[100:103], v[88:91]\n\t"
    "s_add_u32 m0, %[lb], 49152\n\t"
    "v_mfma_f32_16x16x32_bf16 v[92:95], v[164:167], v[100:103], v[92:95]\n\t"
    "global_load_lds_dwordx4 %[av1], %[s1] sc1\n\t"
    "s_waitcnt vmcnt(14)\n\t"
    "s_barrier\n\t"
    "s_waitcnt lgkmcnt(4)\n\t"
    "v_mfma_f32_16x16x32_bf16 v[80:83], v[168:171], v[104:107], v[80:83]\n\t"
    "ds_read_b128 v[96:99], %[ea] offset:16384\n\t"
    "s_add_u32 m0, %[lb], 20480\n\t"
    "v_mfma_f32_16x16x32_bf16 v[84:87], v[172:175], v[104:107], v[84:87]\n\t"
    "ds_read_b128 v[100:103], %[ea] offset:18432\n\t"
    "global_load_lds_dwordx4 %[av2], %[s1] sc1\n\t"
    "v_mfma_f32_16x16x32_bf16 v[88:91], v[168:171], v[108:111], v[88:91]\n\t"
    "s_add_u32 m0, %[lb], 53248\n\t"
    "v_mfma_f32_16x16x32_bf16 v[92:95], v[172:175], v[108:111], v[92:95]\n\t"
    "global_load_lds_dwordx4 %[av3], %[s1] sc1\n\t"
    "s_waitcnt lgkmcnt(4)\n\t"
    "v_mfma_f32_16x16x32_bf16 v[80:83], v[176:179], v[112:115], v[80:83]\n\t"
    "ds_read_b128 v[104:107], %[da] offset:16384\n\t"
    "s_add_u32 m0, %[lb], 24576\n\t"
    "v_mfma_f32_16x16x32_bf16 v[84:87], v[180:183], v[112:115], v[84:87]\n\t"
    "ds_read_b128 v[108:111], %[da] offset:18432\n\t"
    "global_load_lds_dwordx4 %[av4], %[s1] sc1\n\t"
    "v_mfma_f32_16x16x32_bf16 v[88:91], v[176:179], v[116:119], v[88:91]\n\t"
    "s_add_u32 m0, %[lb], 57344\n\t"
    "v_mfma_f32_16x16x32_bf16 v[92:95], v[180:183], v[116:119], v[92:95]\n\t"
    "global_load_lds_dwordx4 %[av5], %[s1] sc1\n\t"
    "s_waitcnt lgkmcnt(4)\n\t"
    "v_mfma_f32_16x16x32_bf16 v[80:83], v[184:187], v[120:123], v[80:83]\n\t"
    "ds_read_b128 v[112:115], %[ea] offset:20480\n\t"
    "s_add_u32 m0, %[lb], 28672\n\t"
    "v_mfma_f32_16x16x32_bf16 v[84:87], v[188:191], v[120:123], v[84:87]\n\t"
    "ds_read_b128 v[116:119], %[ea] offset:22528\n\t"
    "global_load_lds_dwordx4 %[av6], %[s1] sc1\n\t"
    "v_mfma_f32_16x16x32_bf16 v[88:91], v[184:187], v[124:127], v[88:91]\n\t"
    "s_add_u32 m0, %[lb], 61440\n\t"
    "v_mfma_f32_16x16x32_bf16 v[92:95], v[188:191], v[124:127], v[92:95]\n\t"
    "global_load_lds_dwordx4 %[av7], %[s1] sc1\n\t"
    "s_waitcnt lgkmcnt(4)\n\t"
    "v_mfma_f32_16x16x32_bf16 v[80:83], v[192:195], v[96:99], v[80:83]\n\t"
    "ds_read_b128 v[120:123], %[da] offset:20480\n\t"
    "v_mfma_f32_16x16x32_bf16 v[84:87], v[196:199], v[96:99], v[84:87]\n\t"
    "ds_read_b128 v[124:127], %[da] offset:22528\n\t"
    "v_mfma_f32_16x16x32_bf16 v[88:91], v[192:195], v[100:103], v[88:91]\n\t"
    "v_mfma_f32_16x16x32_bf16 v[92:95], v[196:199], v[100:103], v[92:95]\n\t"
    "s_waitcnt vmcnt(16)\n\t"
    "s_barrier\n\t"
    "s_waitcnt lgkmcnt(4)\n\t"
    "v_mfma_f32_16x16x32_bf16 v[80:83], v[200:203], v[104:107], v[80:83]\n\t"
    "ds_read_b128 v[96:99], %[ea] offset:24576\n\t"
    "v_mfma_f32_16x16x32_bf16 v[84:87], v[204:207], v[104:107], v[84:87]\n\t"
    "ds_read_b128 v[100:103], %[ea] offset:26624\n\t"
    "v_mfma_f32_16x16x32_bf16 v[88:91], v[200:203], v[108:111], v[88:91]\n\t"
    "v_mfma_f32_16x16x32_bf16 v[92:95], v[204:207], v[108:111], v[92:95]\n\t"
    "s_waitcnt lgkmcnt(4)\n\t"
    "v_mfma_f32_16x16x32_bf16 v[80:83], v[208:211], v[112:115], v[80:83]\n\t"
    "ds_read_b128 v[104:107], %[da] offset:24576\n\t"
    "v_mfma_f32_16x16x32_bf16 v[84:87], v[212:215], v[112:115], v[84:87]\n\t"
    "ds_read_b128 v[108:111], %[da] offset:26624\n\t"
    "v_mfma_f32_16x16x32_bf16 v[88:91], v[208:211], v[116:119], v[88:91]\n\t"
    "v_mfma_f32_16x16x32_bf16 v[92:95], v[212:215], v[116:119], v[92:95]\n\t"
    "s_waitcnt lgkmcnt(4)\n\t"
    "v_mfma_f32_16x16x32_bf16 v[80:83], v[216:219], v[120:123], v[80:83]\n\t"
    "ds_read_b128 v[112:115], %[ea] offset:28672\n\t"
    "v_mfma_f32_16x16x32_bf16 v[84:87], v[220:223], v[120:123], v[84:87]\n\t"
    "ds_read_b128 v[116:119], %[ea] offset:30720\n\t"
    "v_mfma_f32_16x16x32_bf16 v[88:91], v[216:219], v[124:127], v[88:91]\n\t"
    "v_mfma_f32_16x16x32_bf16 v[92:95], v[220:223], v[124:127], v[92:95]\n\t"
    "s_waitcnt lgkmcnt(4)\n\t"
    "v_mfma_f32_16x16x32_bf16 v[80:83], v[224:227], v[96:99], v[80:83]\n\t"
    "ds_read_b128 v[120:123], %[da] offset:28672\n\t"
    "v_mfma_f32_16x16x32_bf16 v[84:87], v[228:231], v[96:99], v[84:87]\n\t"
    "ds_read_b128 v[124:127], %[da] offset:30720\n\t"
    "v_mfma_f32_16x16x32_bf16 v[88:91], v[224:227], v[100:103], v[88:91]\n\t"
    "v_mfma_f32_16x16x32_bf16 v[92:95], v[228:231], v[100:103], v[92:95]\n\t"
    "s_waitcnt lgkmcnt(4)\n\t"
    "v_mfma_f32_16x16x32_bf16 v[80:83], v[232:235], v[104:107], v[80:83]\n\t"
    "v_mfma_f32_16x16x32_bf16 v[84:87], v[236:239], v[104:107], v[84:87]\n\t"
    "v_mfma_f32_16x16x32_bf16 v[88:91], v[232:235], v[108:111], v[88:91]\n\t"
    "v_mfma_f32_16x16x32_bf16 v[92:95], v[236:239], v[108:111], v[92:95]\n\t"
    "s_waitcnt lgkmcnt(2)\n\t"
    "v_mfma_f32_16x16x32_bf16 v[80:83], v[240:243], v[112:115], v[80:83]\n\t"
    "v_mfma_f32_16x16x32_bf16 v[84:87], v[244:247], v[112:115], v[84:87]\n\t"
    "v_mfma_f32_16x16x32_bf16 v[88:91], v[240:243], v[116:119], v[88:91]\n\t"
    "v_mfma_f32_16x16x32_bf16 v[92:95], v[244:247], v[116:119], v[92:95]\n\t"
    "s_waitcnt lgkmcnt(0)\n\t"
    "v_mfma_f32_16x16x32_bf16 v[80:83], v[248:251], v[120:123], v[80:83]\n\t"
    "v_mfma_f32_16x16x32_bf16 v[84:87], v[252:255], v[120:123], v[84:87]\n\t"
    "v_mfma_f32_16x16x32_bf16 v[88:91], v[248:251], v[124:127], v[88:91]\n\t"
    "v_mfma_f32_16x16x32_bf16 v[92:95], v[252:255], v[124:127], v[92:95]\n\t"
    "s_cmp_eq_u32 %[ps], 0\n\t"
    "s_cbranch_scc1 10f\n\t"
    "global_load_dword %[sv], %[dv], %[db] sc1\n\t"
    "global_load_dword %[su], %[dv], %[db] offset:64 sc1\n\t"
    "10:\n\t"
    "s_mov_b32 %[got], 0\n\t"
    "v_mov_b32 v76, %[fa]\n\t"
    "v_mov_b32 v77, %[fv]\n\t"
    "v_add_u32 v78, 1, v77\n\t"
    "v_mov_b32 v79, %[ga]\n\t"
    "ds_write_b128 %[rw], v[80:83] offset:0\n\t"
    "ds_write_b128 %[rw], v[84:87] offset:1024\n\t"
    "ds_write_b128 %[rw], v[88:91] offset:2048\n\t"
    "ds_write_b128 %[rw], v[92:95] offset:3072\n\t"
    "ds_write_b32 v76, v77\n\t"
    "s_cmp_eq_u32 %[ps], 0\n\t"
    "s_cbranch_scc1 9f\n\t"
    "s_waitcnt vmcnt(0)\n\t"
    "s_branch 9f\n\t"
    "12:\n\t"
    "s_waitcnt lgkmcnt(0)\n\t"
    "9:"
    : [sv] "+&v"(sv), [su] "+&v"(su), [got] "=&s"(got)
    : [ea] "v"(ea), [da] "v"(da), [x] "s"(x), "{v[128:159]}"(W[0]), "{v[160:191]}"(W[1]), "{v[192:223]}"(W[2]), "{v[224:255]}"(W[3]), [lb] "s"(lb), [s0] "s"(s0), [s1] "s"(s1), [av0] "v"(av[0]), [av1] "v"(av[1]), [av2] "v"(av[2]), [av3] "v"(av[3]), [av4] "v"(av[4]), [av5] "v"(av[5]), [av6] "v"(av[6]), [av7] "v"(av[7]), [ps] "s"(ps), [dv] "v"(dv), [db] "s"(db), [rw] "v"(rw), [fa] "s"(fa), [fv] "s"(fv), [ga] "s"(ga), [mu] "s"(mu), [ad] "v"(ad), [th] "s"(th), [tb] "s"(tb)
    : "v96", "v97", "v98", "v99", "v100", "v101", "v102", "v103", "v104", "v105", "v106", "v107", "v108", "v109", "v110", "v111", "v112", "v113", "v114", "v115", "v116", "v117", "v118", "v119", "v120", "v121", "v122", "v123", "v124", "v125", "v126", "v127", "v76", "v77", "v78", "v79", "v80", "v81", "v82", "v83", "v84", "v85", "v86", "v87", "v88", "v89", "v90", "v91", "v92", "v93", "v94", "v95", "vcc", "scc", "m0", "memory");
}
__device__ __forceinline__ void lean_ld1(const u32x32 (&W)[4], unsigned ea, unsigned da, unsigned x, unsigned lb, const char* s0, const char* s1, const unsigned (&av)[8], unsigned ps, unsigned dv, const char* db, unsigned rw, unsigned fa, unsigned fv,
    unsigned ga, unsigned mu, unsigned ad, int th, int tb, unsigned& sv, unsigned& su, unsigned& got) {
  asm volatile("s_setprio 2\n\t"
    "v_mov_b32 v76, %[ga]\n\t"
    "ds_read_b32 v76, v76 offset:24\n\t"
    "ds_read_b128 v[96:99], %[ea] offset:0\n\t"
    "ds_read_b128 v[100:103], %[ea] offset:2048\n\t"
    "ds_read_b128 v[104:107], %[da] offset:0\n\t"
    "ds_read_b128 v[108:111], %[da] offset:2048\n\t"
    "ds_read_b128 v[112:115], %[ea] offset:4096\n\t"
    "ds_read_b128 v[116:119], %[ea] offset:6144\n\t"
    "s_waitcnt lgkmcnt(6)\n\t"
    "v_cmp_ne_u32 vcc, 0, v76\n\t"
    "s_mov_b32 %[got], -1\n\t"
    "s_cbranch_vccnz 12f\n\t"
    "s_waitcnt lgkmcnt(4)\n\t"
    "v_mfma_f32_16x16x32_bf16 v[80:83], v[128:131], v[96:99], 0\n\t"
    "ds_read_b128 v[120:123], %[da] offset:4096\n\t"
    "v_mfma_f32_16x16x32_bf16 v[84:87], v[132:135], v[96:99], 0\n\t"
    "ds_read_b128 v[124:127], %[da] offset:6144\n\t"
    "v_mfma_f32_16x16x32_bf16 v[88:91], v[128:131], v[100:103], 0\n\t"
    "v_mfma_f32_16x16x32_bf16 v[92:95], v[132:135], v[100:103], 0\n\t"
    "s_waitcnt vmcnt(12)\n\t"
    "s_cmp_lt_u32 %[x], 2\n\t"
    "s_cbranch_scc0 41f\n\t"
    "s_waitcnt vmcnt(4)\n\t"
    "41:\n\t"
    "s_barrier\n\t"
    "s_waitcnt lgkmcnt(4)\n\t"
    "v_mfma_f32_16x16x32_bf16 v[80:83], v[136:139], v[104:107], v[80:83]\n\t"
    "ds_read_b128 v[96:99], %[ea] offset:8192\n\t"
    "v_mfma_f32_16x16x32_bf16 v[84:87], v[140:143], v[104:107], v[84:87]\n\t"
    "ds_read_b128 v[100:103], %[ea] offset:10240\n\t"
    "v_mfma_f32_16x16x32_bf16 v[88:91], v[136:139], v[108:111], v[88:91]\n\t"
    "v_mfma_f32_16x16x32_bf16 v[92:95], v[140:143], v[108:111], v[92:95]\n\t"
    "s_waitcnt lgkmcnt(4)\n\t"
    "v_mfma_f32_16x16x32_bf16 v[80:83], v[144:147], v[112:115], v[80:83]\n\t"
    "ds_read_b128 v[104:107], %[da] offset:8192\n\t"
    "v_mfma_f32_16x16x32_bf16 v[84:87], v[148:151], v[112:115], v[84:87]\n\t"
    "ds_read_b128 v[108:111], %[da] offset:10240\n\t"
    "v_mfma_f32_16x16x32_bf16 v[88:91], v[144:147], v[116:119], v[88:91]\n\t"
    "v_mfma_f32_16x16x32_bf16 v[92:95], v[148:151], v[116:119], v[92:95]\n\t"
    "s_waitcnt lgkmcnt(4)\n\t"
    "v_mfma_f32_16x16x32_bf16 v[80:83], v[152:155], v[120:123], v[80:83]\n\t"
    "ds_read_b128 v[112:115], %[ea] offset:12288\n\t"
    "v_mfma_f32_16x16x32_bf16 v[84:87], v[156:159], v[120:123], v[84:87]\n\t"
    "ds_read_b128 v[116:119], %[ea] offset:14336\n\t"
    "v_mfma_f32_16x16x32_bf16 v[88:91], v[152:155], v[124:127], v[88:91]\n\t"
    "v_mfma_f32_16x16x32_bf16 v[92:95], v[156:159], v[124:127], v[92:95]\n\t"
    "s_waitcnt lgkmcnt(4)\n\t"
    "v_mfma_f32_16x16x32_bf16 v[80:83], v[160:163], v[96:99], v[80:83]\n\t"
    "ds_read_b128 v[120:123], %[da] offset:12288\n\t"
    "v_mfma_f32_16x16x32_bf16 v[84:87], v[164:167], v[96:99], v[84:87]\n\t"
    "ds_read_b128 v[124:127], %[da] offset:14336\n\t"
    "v_mfma_f32_16x16x32_bf16 v[88:91], v[160:163], v[100:103], v[88:91]\n\t"
    "v_mfma_f32_16x16x32_bf16 v[92:95], v[164:167], v[100:103], v[92:95]\n\t"
    "s_waitcnt vmcnt(10)\n\t"
    "s_cmp_lt_u32 %[x], 2\n\t"
    "s_cbranch_scc0 45f\n\t"
    "s_waitcnt vmcnt(2)\n\t"
    "45:\n\t"
    "s_barrier\n\t"
    "s_waitcnt lgkmcnt(4)\n\t"
    "v_mfma_f32_16x16x32_bf16 v[80:83], v[168:171], v[104:107], v[80:83]\n\t"
    "ds_read_b128 v[96:99], %[ea] offset:16384\n\t"
    "v_mfma_f32_16x16x32_bf16 v[84:87], v[172:175], v[104:107], v[84:87]\n\t"
    "ds_read_b128 v[100:103], %[ea] offset:18432\n\t"
    "v_mfma_f32_16x16x32_bf16 v[88:91], v[168:171], v[108:111], v[88:91]\n\t"
    "v_mfma_f32_16x16x32_bf16 v[92:95], v[172:175], v[108:111], v[92:95]\n\t"
    "s_waitcnt lgkmcnt(4)\n\t"
    "v_mfma_f32_16x16x32_bf16 v[80:83], v[176:179], v[112:115], v[80:83]\n\t"
    "ds_read_b128 v[104:107], %[da] offset:16384\n\t"
    "v_mfma_f32_16x16x32_bf16 v[84:87], v[180:183], v[112:115], v[84:87]\n\t"
    "ds_read_b128 v[108:111], %[da] offset:18432\n\t"
    "v_mfma_f32_16x16x32_bf16 v[88:91], v[176:179], v[116:119], v[88:91]\n\t"
    "v_mfma_f32_16x16x32_bf16 v[92:95], v[180:183], v[116:119], v[92:95]\n\t"
    "s_waitcnt lgkmcnt(4)\n\t"
    "v_mfma_f32_16x16x32_bf16 v[80:83], v[184:187], v[120:123], v[80:83]\n\t"
    "ds_read_b128 v[112:115], %[ea] offset:20480\n\t"
    "v_mfma_f32_16x16x32_bf16 v[84:87], v[188:191], v[120:123], v[84:87]\n\t"
    "ds_read_b128 v[116:119], %[ea] offset:22528\n\t"
    "v_mfma_f32_16x16x32_bf16 v[88:91], v[184:187], v[124:127], v[88:91]\n\t"
    "v_mfma_f32_16x16x32_bf16 v[92:95], v[188:191], v[124:127], v[92:95]\n\t"
    "s_waitcnt lgkmcnt(4)\n\t"
    "v_mfma_f32_16x16x32_bf16 v[80:83], v[192:195], v[96:99], v[80:83]\n\t"
    "ds_read_b128 v[120:123], %[da] offset:20480\n\t"
    "v_mfma_f32_16x16x32_bf16 v[84:87], v[196:199], v[96:99], v[84:87]\n\t"
    "ds_read_b128 v[124:127], %[da] offset:22528\n\t"
    "v_mfma_f32_16x16x32_bf16 v[88:91], v[192:195], v[100:103], v[88:91]\n\t"
    "v_mfma_f32_16x16x32_bf16 v[92:95], v[196:199], v[100:103], v[92:95]\n\t"
    "s_waitcnt vmcnt(8)\n\t"
    "s_cmp_lt_u32 %[x], 2\n\t"
    "s_cbranch_scc0 49f\n\t"
    "s_waitcnt vmcnt(0)\n\t"
    "49:\n\t"
    "s_barrier\n\t"
    "s_waitcnt lgkmcnt(4)\n\t"
    "v_mfma_f32_16x16x32_bf16 v[80:83], v[200:203], v[104:107], v[80:83]\n\t"
    "ds_read_b128 v[96:99], %[ea] offset:24576\n\t"
    "v_mfma_f32_16x16x32_bf16 v[84:87], v[204:207], v[104:107], v[84:87]\n\t"
    "ds_read_b128 v[100:103], %[ea] offset:26624\n\t"
    "v_mfma_f32_16x16x32_bf16 v[88:91], v[200:203], v[108:111], v[88:91]\n\t"
    "v_mfma_f32_16x16x32_bf16 v[92:95], v[204:207], v[108:111], v[92:95]\n\t"
    "s_waitcnt lgkmcnt(4)\n\t"
    "v_mfma_f32_16x16x32_bf16 v[80:83], v[208:211], v[112:115], v[80:83]\n\t"
    "ds_read_b128 v[104:107], %[da] offset:24576\n\t"
    "v_mfma_f32_16x16x32_bf16 v[84:87], v[212:215], v[112:115], v[84:87]\n\t"
    "ds_read_b128 v[108:111], %[da] offset:26624\n\t"
    "v_mfma_f32_16x16x32_bf16 v[88:91], v[208:211], v[116:119], v[88:91]\n\t"
    "v_mfma_f32_16x16x32_bf16 v[92:95], v[212:215], v[116:119], v[92:95]\n\t"
    "s_cmp_eq_u32 %[ps], 0\n\t"
    "s_cbranch_scc1 10f\n\t"
    "global_load_dword %[sv], %[dv], %[db] sc1\n\t"
    "global_load_dword %[su], %[dv], %[db] offset:64 sc1\n\t"
    "10:\n\t"
    "s_waitcnt lgkmcnt(4)\n\t"
    "v_mfma_f32_16x16x32_bf16 v[80:83], v[216:219], v[120:123], v[80:83]\n\t"
    "ds_read_b128 v[112:115], %[ea] offset:28672\n\t"
    "v_mfma_f32_16x16x32_bf16 v[84:87], v[220:223], v[120:123], v[84:87]\n\t"
    "ds_read_b128 v[116:119], %[ea] offset:30720\n\t"
    "v_mfma_f32_16x16x32_bf16 v[88:91], v[216:219], v[124:127], v[88:91]\n\t"
    "v_mfma_f32_16x16x32_bf16 v[92:95], v[220:223], v[124:127], v[92:95]\n\t"
    "s_waitcnt lgkmcnt(4)\n\t"
    "v_mfma_f32_16x16x32_bf16 v[80:83], v[224:227], v[96:99], v[80:83]\n\t"
    "ds_read_b128 v[120:123], %[da] offset:28672\n\t"
    "v_mfma_f32_16x16x32_bf16 v[84:87], v[228:231], v[96:99], v[84:87]\n\t"
    "ds_read_b128 v[124:127], %[da] offset:30720\n\t"
    "v_mfma_f32_16x16x32_bf16 v[88:91], v[224:227], v[100:103], v[88:91]\n\t"
    "v_mfma_f32_16x16x32_bf16 v[92:95], v[228:231], v[100:103], v[92:95]\n\t"
    "s_waitcnt lgkmcnt(4)\n\t"
    "v_mfma_f32_16x16x32_bf16 v[80:83], v[232:235], v[104:107], v[80:83]\n\t"
    "v_mfma_f32_16x16x32_bf16 v[84:87], v[236:239], v[104:107], v[84:87]\n\t"
    "v_mfma_f32_16x16x32_bf16 v[88:91], v[232:235], v[108:111], v[88:91]\n\t"
    "v_mfma_f32_16x16x32_bf16 v[92:95], v[236:239], v[108:111], v[92:95]\n\t"
    "s_waitcnt lgkmcnt(2)\n\t"
    "v_mfma_f32_16x16x32_bf16 v[80:83], v[240:243], v[112:115], v[80:83]\n\t"
    "v_mfma_f32_16x16x32_bf16 v[84:87], v[244:247], v[112:115], v[84:87]\n\t"
    "v_mfma_f32_16x16x32_bf16 v[88:91], v[240:243], v[116:119], v[88:91]\n\t"
    "v_mfma_f32_16x16x32_bf16 v[92:95], v[244:247], v[116:119], v[92:95]\n\t"
    "s_waitcnt lgkmcnt(0)\n\t"
    "v_mfma_f32_16x16x32_bf16 v[80:83], v[248:251], v[120:123], v[80:83]\n\t"
    "s_memrealtime s[36:37]\n\t"
    "v_mfma_f32_16x16x32_bf16 v[84:87], v[252:255], v[120:123], v[84:87]\n\t"
    "v_mfma_f32_16x16x32_bf16 v[88:91], v[248:251], v[124:127], v[88:91]\n\t"
    "v_mfma_f32_16x16x32_bf16 v[92:95], v[252:255], v[124:127], v[92:95]\n\t"
    "s_mov_b32 %[got], 0\n\t"
    "v_mov_b32 v76, %[fa]\n\t"
    "v_mov_b32 v77, %[fv]\n\t"
    "v_add_u32 v78, 1, v77\n\t"
    "v_mov_b32 v79, %[ga]\n\t"
    "s_waitcnt lgkmcnt(0)\n\t"
    "ds_write_b128 %[rw], v[80:83] offset:0\n\t"
    "ds_write_b128 %[rw], v[84:87] offset:1024\n\t"
    "ds_write_b128 %[rw], v[88:91] offset:2048\n\t"
    "ds_write_b128 %[rw], v[92:95] offset:3072\n\t"
    "ds_write_b32 v76, v77\n\t"
    "s_cmp_eq_u32 %[ps], 0\n\t"
    "s_cbranch_scc1 9f\n\t"
    "s_waitcnt vmcnt(0)\n\t"
    "v_mad_u32_u24 v76, %[sv], %[mu], %[ad]\n\t"
    "v_cmp_ge_i32 vcc, %[th], v76\n\t"
    "s_cbranch_vccz 11f\n\t"
    "s_sub_u32 s36, s36, 20\n\t"
    "v_sub_u32 v76, s36, %[su]\n\t"
    "v_lshrrev_b32 v77, 24, %[su]\n\t"
    "v_lshlrev_b32 v76, 8, v76\n\t"
    "v_ashrrev_i32 v76, 31, v76\n\t"
    "v_add_u32 v77, v77, v76\n\t"
    "v_cmp_lt_i32 s[36:37], %[tb], v77\n\t"
    "s_andn2_b64 vcc, vcc, s[36:37]\n\t"
    "s_cbranch_scc1 9f\n\t"
    "11:\n\t"
    "ds_write2_b32 v79, v78, v78 offset1:1\n\t"
    "s_add_u32 m0, %[lb], 0\n\t"
    "s_nop 0\n\t"
    "global_load_lds_dwordx4 %[av0], %[s0] sc1\n\t"
    "s_add_u32 m0, %[lb], 4096\n\t"
    "s_nop 0\n\t"
    "global_load_lds_dwordx4 %[av2], %[s0] sc1\n\t"
    "s_add_u32 m0, %[lb], 8192\n\t"
    "s_nop 0\n\t"
    "global_load_lds_dwordx4 %[av4], %[s0] sc1\n\t"
    "s_add_u32 m0, %[lb], 12288\n\t"
    "s_nop 0\n\t"
    "global_load_lds_dwordx4 %[av6], %[s0] sc1\n\t"
    "s_add_u32 m0, %[lb], 16384\n\t"
    "s_nop 0\n\t"
    "global_load_lds_dwordx4 %[av0], %[s1] sc1\n\t"
    "s_add_u32 m0, %[lb], 20480\n\t"
    "s_nop 0\n\t"
    "global_load_lds_dwordx4 %[av2], %[s1] sc1\n\t"
    "s_add_u32 m0, %[lb], 24576\n\t"
    "s_nop 0\n\t"
    "global_load_lds_dwordx4 %[av4], %[s1] sc1\n\t"
    "s_add_u32 m0, %[lb], 28672\n\t"
    "s_nop 0\n\t"
    "global_load_lds_dwordx4 %[av6], %[s1] sc1\n\t"
    "s_mov_b32 %[got], 1\n\t"
    "s_branch 9f\n\t"
    "12:\n\t"
    "s_waitcnt lgkmcnt(0)\n\t"
    "9:"
    : [sv] "+&v"(sv), [su] "+&v"(su), [got] "=&s"(got)
    : [ea] "v"(ea), [da] "v"(da), [x] "s"(x), "{v[128:159]}"(W[0]), "{v[160:191]}"(W[1]), "{v[192:223]}"(W[2]), "{v[224:255]}"(W[3]), [lb] "s"(lb), [s0] "s"(s0), [s1] "s"(s1), [av0] "v"(av[0]), [av1] "v"(av[1]), [av2] "v"(av[2]), [av3] "v"(av[3]), [av4] "v"(av[4]), [av5] "v"(av[5]), [av6] "v"(av[6]), [av7] "v"(av[7]), [ps] "s"(ps), [dv] "v"(dv), [db] "s"(db), [rw] "v"(rw), [fa] "s"(fa), [fv] "s"(fv), [ga] "s"(ga), [mu] "s"(mu), [ad] "v"(ad), [th] "s"(th), [tb] "s"(tb)
    : "v96", "v97", "v98", "v99", "v100", "v101", "v102", "v103", "v104", "v105", "v106", "v107", "v108", "v109", "v110", "v111", "v112", "v113", "v114", "v115", "v116", "v117", "v118", "v119", "v120", "v121", "v122", "v123", "v124", "v125", "v126", "v127", "v76", "v77", "v78", "v79", "v80", "v81", "v82", "v83", "v84", "v85", "v86", "v87", "v88", "v89", "v90", "v91", "v92", "v93", "v94", "v95", "vcc", "scc", "m0", "memory", "s36", "s37", "s38", "s39");
}
__device__ __forceinline__ void lean_dm1(const u32x32 (&W)[4], unsigned ea, unsigned da, unsigned x, unsigned lb, const char* s0, const char* s1, const unsigned (&av)[8], unsigned ps, unsigned dv, const char* db, unsigned rw, unsigned fa, unsigned fv,
    unsigned ga, unsigned mu, unsigned ad, int th, int tb, unsigned& sv, unsigned& su, unsigned& got) {
  asm volatile("s_setprio 2\n\t"
    "v_mov_b32 v76, %[ga]\n\t"
    "ds_read_b32 v76, v76 offset:24\n\t"
    "ds_read_b128 v[96:99], %[ea] offset:0\n\t"
    "ds_read_b128 v[100:103], %[ea] offset:2048\n\t"
    "ds_read_b128 v[104:107], %[da] offset:0\n\t"
    "ds_read_b128 v[108:111], %[da] offset:2048\n\t"
    "ds_read_b128 v[112:115], %[ea] offset:4096\n\t"
    "ds_read_b128 v[116:119], %[ea] offset:6144\n\t"
    "s_waitcnt lgkmcnt(6)\n\t"
    "v_cmp_ne_u32 vcc, 0, v76\n\t"
    "s_mov_b32 %[got], -1\n\t"
    "s_cbranch_vccnz 12f\n\t"
    "s_waitcnt lgkmcnt(4)\n\t"
    "v_mfma_f32_16x16x32_bf16 v[80:83], v[128:131], v[96:99], 0\n\t"
    "ds_read_b128 v[120:123], %[da] offset:4096\n\t"
    "s_add_u32 m0, %[lb], 0\n\t"
    "v_mfma_f32_16x16x32_bf16 v[84:87], v[132:135], v[96:99], 0\n\t"
    "ds_read_b128 v[124:127], %[da] offset:6144\n\t"
    "global_load_lds_dwordx4 %[av0], %[s0] sc1\n\t"
    "v_mfma_f32_16x16x32_bf16 v[88:91], v[128:131], v[100:103], 0\n\t"
    "v_mfma_f32_16x16x32_bf16 v[92:95], v[132:135], v[100:103], 0\n\t"
    "s_waitcnt vmcnt(5)\n\t"
    "s_barrier\n\t"
    "s_waitcnt lgkmcnt(4)\n\t"
    "v_mfma_f32_16x16x32_bf16 v[80:83], v[136:139], v[104:107], v[80:83]\n\t"
    "ds_read_b128 v[96:99], %[ea] offset:8192\n\t"
    "s_add_u32 m0, %[lb], 4096\n\t"
    "v_mfma_f32_16x16x32_bf16 v[84:87], v[140:143], v[104:107], v[84:87]\n\t"
    "ds_read_b128 v[100:103], %[ea] offset:10240\n\t"
    "global_load_lds_dwordx4 %[av2], %[s0] sc1\n\t"
    "v_mfma_f32_16x16x32_bf16 v[88:91], v[136:139], v[108:111], v[88:91]\n\t"
    "v_mfma_f32_16x16x32_bf16 v[92:95], v[140:143], v[108:111], v[92:95]\n\t"
    "s_waitcnt lgkmcnt(4)\n\t"
    "v_mfma_f32_16x16x32_bf16 v[80:83], v[144:147], v[112:115], v[80:83]\n\t"
    "ds_read_b128 v[104:107], %[da] offset:8192\n\t"
    "s_add_u32 m0, %[lb], 8192\n\t"
    "v_mfma_f32_16x16x32_bf16 v[84:87], v[148:151], v[112:115], v[84:87]\n\t"
    "ds_read_b128 v[108:111], %[da] offset:10240\n\t"
    "global_load_lds_dwordx4 %[av4], %[s0] sc1\n\t"
    "v_mfma_f32_16x16x32_bf16 v[88:91], v[144:147], v[116:119], v[88:91]\n\t"
    "v_mfma_f32_16x16x32_bf16 v[92:95], v[148:151], v[116:119], v[92:95]\n\t"
    "s_waitcnt lgkmcnt(4)\n\t"
    "v_mfma_f32_16x16x32_bf16 v[80:83], v[152:155], v[120:123], v[80:83]\n\t"
    "ds_read_b128 v[112:115], %[ea] offset:12288\n\t"
    "s_add_u32 m0, %[lb], 12288\n\t"
    "v_mfma_f32_16x16x32_bf16 v[84:87], v[156:159], v[120:123], v[84:87]\n\t"
    "ds_read_b128 v[116:119], %[ea] offset:14336\n\t"
    "global_load_lds_dwordx4 %[av6], %[s0] sc1\n\t"
    "v_mfma_f32_16x16x32_bf16 v[88:91], v[152:155], v[124:127], v[88:91]\n\t"
    "v_mfma_f32_16x16x32_bf16 v[92:95], v[156:159], v[124:127], v[92:95]\n\t"
    "s_waitcnt lgkmcnt(4)\n\t"
    "v_mfma_f32_16x16x32_bf16 v[80:83], v[160:163], v[96:99], v[80:83]\n\t"
    "ds_read_b128 v[120:123], %[da] offset:12288\n\t"
    "s_add_u32 m0, %[lb], 16384\n\t"
    "v_mfma_f32_16x16x32_bf16 v[84:87], v[164:167], v[96:99], v[84:87]\n\t"
    "ds_read_b128 v[124:127], %[da] offset:14336\n\t"
    "global_load_lds_dwordx4 %[av0], %[s1] sc1\n\t"
    "v_mfma_f32_16x16x32_bf16 v[88:91], v[160:163], v[100:103], v[88:91]\n\t"
    "v_mfma_f32_16x16x32_bf16 v[92:95], v[164:167], v[100:103], v[92:95]\n\t"
    "s_waitcnt vmcnt(7)\n\t"
    "s_barrier\n\t"
    "s_waitcnt lgkmcnt(4)\n\t"
    "v_mfma_f32_16x16x32_bf16 v[80:83], v[168:171], v[104:107], v[80:83]\n\t"
    "ds_read_b128 v[96:99], %[ea] offset:16384\n\t"
    "s_add_u32 m0, %[lb], 20480\n\t"
    "v_mfma_f32_16x16x32_bf16 v[84:87], v[172:175], v[104:107], v[84:87]\n\t"
    "ds_read_b128 v[100:103], %[ea] offset:18432\n\t"
    "global_load_lds_dwordx4 %[av2], %[s1] sc1\n\t"
    "v_mfma_f32_16x16x32_bf16 v[88:91], v[168:171], v[108:111], v[88:91]\n\t"
    "v_mfma_f32_16x16x32_bf16 v[92:95], v[172:175], v[108:111], v[92:95]\n\t"
    "s_waitcnt lgkmcnt(4)\n\t"
    "v_mfma_f32_16x16x32_bf16 v[80:83], v[176:179], v[112:115], v[80:83]\n\t"
    "ds_read_b128 v[104:107], %[da] offset:16384\n\t"
    "s_add_u32 m0, %[lb], 24576\n\t"
    "v_mfma_f32_16x16x32_bf16 v[84:87], v[180:183], v[112:115], v[84:87]\n\t"
    "ds_read_b128 v[108:111], %[da] offset:18432\n\t"
    "global_load_lds_dwordx4 %[av4], %[s1] sc1\n\t"
    "v_mfma_f32_16x16x32_bf16 v[88:91], v[176:179], v[116:119], v[88:91]\n\t"
    "v_mfma_f32_16x16x32_bf16 v[92:95], v[180:183], v[116:119], v[92:95]\n\t"
    "s_waitcnt lgkmcnt(4)\n\t"
    "v_mfma_f32_16x16x32_bf16 v[80:83], v[184:187], v[120:123], v[80:83]\n\t"
    "ds_read_b128 v[112:115], %[ea] offset:20480\n\t"
    "s_add_u32 m0, %[lb], 28672\n\t"
    "v_mfma_f32_16x16x32_bf16 v[84:87], v[188:191], v[120:123], v[84:87]\n\t"
    "ds_read_b128 v[116:119], %[ea] offset:22528\n\t"
    "global_load_lds_dwordx4 %[av6], %[s1] sc1\n\t"
    "v_mfma_f32_16x16x32_bf16 v[88:91], v[184:187], v[124:127], v[88:91]\n\t"
    "v_mfma_f32_16x16x32_bf16 v[92:95], v[188:191], v[124:127], v[92:95]\n\t"
    "s_waitcnt lgkmcnt(4)\n\t"
    "v_mfma_f32_16x16x32_bf16 v[80:83], v[192:195], v[96:99], v[80:83]\n\t"
    "ds_read_b128 v[120:123], %[da] offset:20480\n\t"
    "v_mfma_f32_16x16x32_bf16 v[84:87], v[196:199], v[96:99], v[84:87]\n\t"
    "ds_read_b128 v[124:127], %[da] offset:22528\n\t"
    "v_mfma_f32_16x16x32_bf16 v[88:91], v[192:195], v[100:103], v[88:91]\n\t"
    "v_mfma_f32_16x16x32_bf16 v[92:95], v[196:199], v[100:103], v[92:95]\n\t"
    "s_waitcnt vmcnt(8)\n\t"
    "s_barrier\n\t"
    "s_waitcnt lgkmcnt(4)\n\t"
    "v_mfma_f32_16x16x32_bf16 v[80:83], v[200:203], v[104:107], v[80:83]\n\t"
    "ds_read_b128 v[96:99], %[ea] offset:24576\n\t"
    "v_mfma_f32_16x16x32_bf16 v[84:87], v[204:207], v[104:107], v[84:87]\n\t"
    "ds_read_b128 v[100:103], %[ea] offset:26624\n\t"
    "v_mfma_f32_16x16x32_bf16 v[88:91], v[200:203], v[108:111], v[88:91]\n\t"
    "v_mfma_f32_16x16x32_bf16 v[92:95], v[204:207], v[108:111], v[92:95]\n\t"
    "s_waitcnt lgkmcnt(4)\n\t"
    "v_mfma_f32_16x16x32_bf16 v[80:83], v[208:211], v[112:115], v[80:83]\n\t"
    "ds_read_b128 v[104:107], %[da] offset:24576\n\t"
    "v_mfma_f32_16x16x32_bf16 v[84:87], v[212:215], v[112:115], v[84:87]\n\t"
    "ds_read_b128 v[108:111], %[da] offset:26624\n\t"
    "v_mfma_f32_16x16x32_bf16 v[88:91], v[208:211], v[116:119], v[88:91]\n\t"
    "v_mfma_f32_16x16x32_bf16 v[92:95], v[212:215], v[116:119], v[92:95]\n\t"
    "s_waitcnt lgkmcnt(4)\n\t"
    "v_mfma_f32_16x16x32_bf16 v[80:83], v[216:219], v[120:123], v[80:83]\n\t"
    "ds_read_b128 v[112:115], %[ea] offset:28672\n\t"
    "v_mfma_f32_16x16x32_bf16 v[84:87], v[220:223], v[120:123], v[84:87]\n\t"
    "ds_read_b128 v[116:119], %[ea] offset:30720\n\t"
    "v_mfma_f32_16x16x32_bf16 v[88:91], v[216:219], v[124:127], v[88:91]\n\t"
    "v_mfma_f32_16x16x32_bf16 v[92:95], v[220:223], v[124:127], v[92:95]\n\t"
    "s_waitcnt lgkmcnt(4)\n\t"
    "v_mfma_f32_16x16x32_bf16 v[80:83], v[224:227], v[96:99], v[80:83]\n\t"
    "ds_read_b128 v[120:123], %[da] offset:28672\n\t"
    "v_mfma_f32_16x16x32_bf16 v[84:87], v[228:231], v[96:99], v[84:87]\n\t"
    "ds_read_b128 v[124:127], %[da] offset:30720\n\t"
    "v_mfma_f32_16x16x32_bf16 v[88:91], v[224:227], v[100:103], v[88:91]\n\t"
    "v_mfma_f32_16x16x32_bf16 v[92:95], v[228:231], v[100:103], v[92:95]\n\t"
    "s_waitcnt lgkmcnt(4)\n\t"
    "v_mfma_f32_16x16x32_bf16 v[80:83], v[232:235], v[104:107], v[80:83]\n\t"
    "v_mfma_f32_16x16x32_bf16 v[84:87], v[236:239], v[104:107], v[84:87]\n\t"
    "v_mfma_f32_16x16x32_bf16 v[88:91], v[232:235], v[108:111], v[88:91]\n\t"
    "v_mfma_f32_16x16x32_bf16 v[92:95], v[236:239], v[108:111], v[92:95]\n\t"
    "s_waitcnt lgkmcnt(2)\n\t"
    "v_mfma_f32_16x16x32_bf16 v[80:83], v[240:243], v[112:115], v[80:83]\n\t"
    "v_mfma_f32_16x16x32_bf16 v[84:87], v[244:247], v[112:115], v[84:87]\n\t"
    "v_mfma_f32_16x16x32_bf16 v[88:91], v[240:243], v[116:119], v[88:91]\n\t"
    "v_mfma_f32_16x16x32_bf16 v[92:95], v[244:247], v[116:119], v[92:95]\n\t"
    "s_waitcnt lgkmcnt(0)\n\t"
    "v_mfma_f32_16x16x32_bf16 v[80:83], v[248:251], v[120:123], v[80:83]\n\t"
    "v_mfma_f32_16x16x32_bf16 v[84:87], v[252:255], v[120:123], v[84:87]\n\t"
    "v_mfma_f32_16x16x32_bf16 v[88:91], v[248:251], v[124:127], v[88:91]\n\t"
    "v_mfma_f32_16x16x32_bf16 v[92:95], v[252:255], v[124:127], v[92:95]\n\t"
    "s_cmp_eq_u32 %[ps], 0\n\t"
    "s_cbranch_scc1 10f\n\t"
    "global_load_dword %[sv], %[dv], %[db] sc1\n\t"
    "global_load_dword %[su], %[dv], %[db] offset:64 sc1\n\t"
    "10:\n\t"
    "s_mov_b32 %[got], 0\n\t"
    "v_mov_b32 v76, %[fa]\n\t"
    "v_mov_b32 v77, %[fv]\n\t"
    "v_add_u32 v78, 1, v77\n\t"
    "v_mov_b32 v79, %[ga]\n\t"
    "ds_write_b128 %[rw], v[80:83] offset:0\n\t"
    "ds_write_b128 %[rw], v[84:87] offset:1024\n\t"
    "ds_write_b128 %[rw], v[88:91] offset:2048\n\t"
    "ds_write_b128 %[rw], v[92:95] offset:3072\n\t"
    "ds_write_b32 v76, v77\n\t"
    "s_cmp_eq_u32 %[ps], 0\n\t"
    "s_cbranch_scc1 9f\n\t"
    "s_waitcnt vmcnt(0)\n\t"
    "s_branch 9f\n\t"
    "12:\n\t"
    "s_waitcnt lgkmcnt(0)\n\t"
    "9:"
    : [sv] "+&v"(sv), [su] "+&v"(su), [got] "=&s"(got)
    : [ea] "v"(ea), [da] "v"(da), [x] "s"(x), "{v[128:159]}"(W[0]), "{v[160:191]}"(W[1]), "{v[192:223]}"(W[2]), "{v[224:255]}"(W[3]), [lb] "s"(lb), [s0] "s"(s0), [s1] "s"(s1), [av0] "v"(av[0]), [av1] "v"(av[1]), [av2] "v"(av[2]), [av3] "v"(av[3]), [av4] "v"(av[4]), [av5] "v"(av[5]), [av6] "v"(av[6]), [av7] "v"(av[7]), [ps] "s"(ps), [dv] "v"(dv), [db] "s"(db), [rw] "v"(rw), [fa] "s"(fa), [fv] "s"(fv), [ga] "s"(ga), [mu] "s"(mu), [ad] "v"(ad), [th] "s"(th), [tb] "s"(tb)
    : "v96", "v97", "v98", "v99", "v100", "v101", "v102", "v103", "v104", "v105", "v106", "v107", "v108", "v109", "v110", "v111", "v112", "v113", "v114", "v115", "v116", "v117", "v118", "v119", "v120", "v121", "v122", "v123", "v124", "v125", "v126", "v127", "v76", "v77", "v78", "v79", "v80", "v81", "v82", "v83", "v84", "v85", "v86", "v87", "v88", "v89", "v90", "v91", "v92", "v93", "v94", "v95", "vcc", "scc", "m0", "memory");
}
__device__ __forceinline__ void lean_st0(const u32x32 (&W)[4], unsigned ea, unsigned da, unsigned x, u32x16& pd, unsigned pv, unsigned cn, const char* pg, unsigned bl, unsigned& tk, unsigned& st) {
  asm volatile("v_mov_b32 v76, %[cn]\n\t"
    "ds_read_b32 v76, v76 offset:28\n\t"
    "ds_read_b128 v[96:99], %[ea] offset:0\n\t"
    "ds_read_b128 v[100:103], %[ea] offset:2048\n\t"
    "ds_read_b128 v[104:107], %[da] offset:0\n\t"
    "ds_read_b128 v[108:111], %[da] offset:2048\n\t"
    "ds_read_b128 v[112:115], %[ea] offset:4096\n\t"
    "ds_read_b128 v[116:119], %[ea] offset:6144\n\t"
    "s_waitcnt lgkmcnt(6)\n\t"
    "v_cmp_ne_u32 vcc, 0, v76\n\t"
    "s_mov_b32 %[st], -1\n\t"
    "s_cbranch_vccnz 12f\n\t"
    "s_waitcnt lgkmcnt(4)\n\t"
    "v_mfma_f32_16x16x32_bf16 v[80:83], v[128:131], v[96:99], 0\n\t"
    "ds_read_b128 v[120:123], %[da] offset:4096\n\t"
    "v_mfma_f32_16x16x32_bf16 v[84:87], v[132:135], v[96:99], 0\n\t"
    "ds_read_b128 v[124:127], %[da] offset:6144\n\t"
    "v_mfma_f32_16x16x32_bf16 v[88:91], v[128:131], v[100:103], 0\n\t"
    "v_mfma_f32_16x16x32_bf16 v[92:95], v[132:135], v[100:103], 0\n\t"
    "s_barrier\n\t"
    "s_bitcmp1_b32 %[x], 2\n\t"
    "s_cbranch_scc1 7f\n\t"
    "8:\n\t"
    "s_bitcmp1_b32 %[x], 3\n\t"
    "s_cbranch_scc1 13f\n\t"
    "14:\n\t"
    "s_waitcnt lgkmcnt(4)\n\t"
    "v_mfma_f32_16x16x32_bf16 v[80:83], v[136:139], v[104:107], v[80:83]\n\t"
    "ds_read_b128 v[96:99], %[ea] offset:8192\n\t"
    "v_mfma_f32_16x16x32_bf16 v[84:87], v[140:143], v[104:107], v[84:87]\n\t"
    "ds_read_b128 v[100:103], %[ea] offset:10240\n\t"
    "v_mfma_f32_16x16x32_bf16 v[88:91], v[136:139], v[108:111], v[88:91]\n\t"
    "v_mfma_f32_16x16x32_bf16 v[92:95], v[140:143], v[108:111], v[92:95]\n\t"
    "s_waitcnt lgkmcnt(4)\n\t"
    "v_mfma_f32_16x16x32_bf16 v[80:83], v[144:147], v[112:115], v[80:83]\n\t"
    "ds_read_b128 v[104:107], %[da] offset:8192\n\t"
    "v_mfma_f32_16x16x32_bf16 v[84:87], v[148:151], v[112:115], v[84:87]\n\t"
    "ds_read_b128 v[108:111], %[da] offset:10240\n\t"
    "v_mfma_f32_16x16x32_bf16 v[88:91], v[144:147], v[116:119], v[88:91]\n\t"
    "v_mfma_f32_16x16x32_bf16 v[92:95], v[148:151], v[116:119], v[92:95]\n\t"
    "s_waitcnt lgkmcnt(4)\n\t"
    "v_mfma_f32_16x16x32_bf16 v[80:83], v[152:155], v[120:123], v[80:83]\n\t"
    "ds_read_b128 v[112:115], %[ea] offset:12288\n\t"
    "v_mfma_f32_16x16x32_bf16 v[84:87], v[156:159], v[120:123], v[84:87]\n\t"
    "ds_read_b128 v[116:119], %[ea] offset:14336\n\t"
    "v_mfma_f32_16x16x32_bf16 v[88:91], v[152:155], v[124:127], v[88:91]\n\t"
    "v_mfma_f32_16x16x32_bf16 v[92:95], v[156:159], v[124:127], v[92:95]\n\t"
    "s_waitcnt lgkmcnt(4)\n\t"
    "v_mfma_f32_16x16x32_bf16 v[80:83], v[160:163], v[96:99], v[80:83]\n\t"
    "ds_read_b128 v[120:123], %[da] offset:12288\n\t"
    "v_mfma_f32_16x16x32_bf16 v[84:87], v[164:167], v[96:99], v[84:87]\n\t"
    "ds_read_b128 v[124:127], %[da] offset:14336\n\t"
    "v_mfma_f32_16x16x32_bf16 v[88:91], v[160:163], v[100:103], v[88:91]\n\t"
    "v_mfma_f32_16x16x32_bf16 v[92:95], v[164:167], v[100:103], v[92:95]\n\t"
    "s_barrier\n\t"
    "s_waitcnt lgkmcnt(4)\n\t"
    "v_mfma_f32_16x16x32_bf16 v[80:83], v[168:171], v[104:107], v[80:83]\n\t"
    "ds_read_b128 v[96:99], %[ea] offset:16384\n\t"
    "v_mfma_f32_16x16x32_bf16 v[84:87], v[172:175], v[104:107], v[84:87]\n\t"
    "ds_read_b128 v[100:103], %[ea] offset:18432\n\t"
    "v_mfma_f32_16x16x32_bf16 v[88:91], v[168:171], v[108:111], v[88:91]\n\t"
    "v_mfma_f32_16x16x32_bf16 v[92:95], v[172:175], v[108:111], v[92:95]\n\t"
    "s_waitcnt lgkmcnt(4)\n\t"
    "v_mfma_f32_16x16x32_bf16 v[80:83], v[176:179], v[112:115], v[80:83]\n\t"
    "ds_read_b128 v[104:107], %[da] offset:16384\n\t"
    "v_mfma_f32_16x16x32_bf16 v[84:87], v[180:183], v[112:115], v[84:87]\n\t"
    "ds_read_b128 v[108:111], %[da] offset:18432\n\t"
    "v_mfma_f32_16x16x32_bf16 v[88:91], v[176:179], v[116:119], v[88:91]\n\t"
    "v_mfma_f32_16x16x32_bf16 v[92:95], v[180:183], v[116:119], v[92:95]\n\t"
    "s_waitcnt lgkmcnt(4)\n\t"
    "v_mfma_f32_16x16x32_bf16 v[80:83], v[184:187], v[120:123], v[80:83]\n\t"
    "ds_read_b128 v[112:115], %[ea] offset:20480\n\t"
    "v_mfma_f32_16x16x32_bf16 v[84:87], v[188:191], v[120:123], v[84:87]\n\t"
    "ds_read_b128 v[116:119], %[ea] offset:22528\n\t"
    "v_mfma_f32_16x16x32_bf16 v[88:91], v[184:187], v[124:127], v[88:91]\n\t"
    "v_mfma_f32_16x16x32_bf16 v[92:95], v[188:191], v[124:127], v[92:95]\n\t"
    "s_waitcnt lgkmcnt(4)\n\t"
    "v_mfma_f32_16x16x32_bf16 v[80:83], v[192:195], v[96:99], v[80:83]\n\t"
    "ds_read_b128 v[120:123], %[da] offset:20480\n\t"
    "v_mfma_f32_16x16x32_bf16 v[84:87], v[196:199], v[96:99], v[84:87]\n\t"
    "ds_read_b128 v[124:127], %[da] offset:22528\n\t"
    "v_mfma_f32_16x16x32_bf16 v[88:91], v[192:195], v[100:103], v[88:91]\n\t"
    "v_mfma_f32_16x16x32_bf16 v[92:95], v[196:199], v[100:103], v[92:95]\n\t"
    "s_barrier\n\t"
    "s_and_b32 %[st], %[x], 3\n\t"
    "s_cbranch_scc1 3f\n\t"
    "4:\n\t"
    "s_waitcnt lgkmcnt(4)\n\t"
    "v_mfma_f32_16x16x32_bf16 v[80:83], v[200:203], v[104:107], v[80:83]\n\t"
    "ds_read_b128 v[96:99], %[ea] offset:24576\n\t"
    "v_mfma_f32_16x16x32_bf16 v[84:87], v[204:207], v[104:107], v[84:87]\n\t"
    "ds_read_b128 v[100:103], %[ea] offset:26624\n\t"
    "v_mfma_f32_16x16x32_bf16 v[88:91], v[200:203], v[108:111], v[88:91]\n\t"
    "v_mfma_f32_16x16x32_bf16 v[92:95], v[204:207], v[108:111], v[92:95]\n\t"
    "s_waitcnt lgkmcnt(4)\n\t"
    "v_mfma_f32_16x16x32_bf16 v[80:83], v[208:211], v[112:115], v[80:83]\n\t"
    "ds_read_b128 v[104:107], %[da] offset:24576\n\t"
    "v_mfma_f32_16x16x32_bf16 v[84:87], v[212:215], v[112:115], v[84:87]\n\t"
    "ds_read_b128 v[108:111], %[da] offset:26624\n\t"
    "v_mfma_f32_16x16x32_bf16 v[88:91], v[208:211], v[116:119], v[88:91]\n\t"
    "v_mfma_f32_16x16x32_bf16 v[92:95], v[212:215], v[116:119], v[92:95]\n\t"
    "s_waitcnt lgkmcnt(4)\n\t"
    "v_mfma_f32_16x16x32_bf16 v[80:83], v[216:219], v[120:123], v[80:83]\n\t"
    "ds_read_b128 v[112:115], %[ea] offset:28672\n\t"
    "v_mfma_f32_16x16x32_bf16 v[84:87], v[220:223], v[120:123], v[84:87]\n\t"
    "ds_read_b128 v[116:119], %[ea] offset:30720\n\t"
    "v_mfma_f32_16x16x32_bf16 v[88:91], v[216:219], v[124:127], v[88:91]\n\t"
    "v_mfma_f32_16x16x32_bf16 v[92:95], v[220:223], v[124:127], v[92:95]\n\t"
    "s_waitcnt lgkmcnt(4)\n\t"
    "v_mfma_f32_16x16x32_bf16 v[80:83], v[224:227], v[96:99], v[80:83]\n\t"
    "ds_read_b128 v[120:123], %[da] offset:28672\n\t"
    "v_mfma_f32_16x16x32_bf16 v[84:87], v[228:231], v[96:99], v[84:87]\n\t"
    "ds_read_b128 v[124:127], %[da] offset:30720\n\t"
    "v_mfma_f32_16x16x32_bf16 v[88:91], v[224:227], v[100:103], v[88:91]\n\t"
    "v_mfma_f32_16x16x32_bf16 v[92:95], v[228:231], v[100:103], v[92:95]\n\t"
    "s_waitcnt lgkmcnt(4)\n\t"
    "v_mfma_f32_16x16x32_bf16 v[80:83], v[232:235], v[104:107], v[80:83]\n\t"
    "v_mfma_f32_16x16x32_bf16 v[84:87], v[236:239], v[104:107], v[84:87]\n\t"
    "v_mfma_f32_16x16x32_bf16 v[88:91], v[232:235], v[108:111], v[88:91]\n\t"
    "v_mfma_f32_16x16x32_bf16 v[92:95], v[236:239], v[108:111], v[92:95]\n\t"
    "s_waitcnt lgkmcnt(2)\n\t"
    "v_mfma_f32_16x16x32_bf16 v[80:83], v[240:243], v[112:115], v[80:83]\n\t"
    "v_mfma_f32_16x16x32_bf16 v[84:87], v[244:247], v[112:115], v[84:87]\n\t"
    "v_mfma_f32_16x16x32_bf16 v[88:91], v[240:243], v[116:119], v[88:91]\n\t"
    "v_mfma_f32_16x16x32_bf16 v[92:95], v[244:247], v[116:119], v[92:95]\n\t"
    "s_waitcnt lgkmcnt(0)\n\t"
    "v_mfma_f32_16x16x32_bf16 v[80:83], v[248:251], v[120:123], v[80:83]\n\t"
    "v_mfma_f32_16x16x32_bf16 v[84:87], v[252:255], v[120:123], v[84:87]\n\t"
    "v_mfma_f32_16x16x32_bf16 v[88:91], v[248:251], v[124:127], v[88:91]\n\t"
    "v_mfma_f32_16x16x32_bf16 v[92:95], v[252:255], v[124:127], v[92:95]\n\t"
    "s_mov_b32 %[st], 0\n\t"
    "s_nop 5\n\t"
    "s_branch 9f\n\t"
    "13:\n\t"
    "s_memrealtime s[36:37]\n\t"
    "v_mov_b32 v79, 0\n\t"
    "s_waitcnt lgkmcnt(0)\n\t"
    "s_mov_b32 %[tk], s36\n\t"
    "s_add_u32 s36, s36, %[bl]\n\t"
    "s_and_b32 s36, s36, 0xffffff\n\t"
    "s_and_b32 s37, %[bl], 0xff000000\n\t"
    "s_or_b32 s36, s36, s37\n\t"
    "v_mov_b32 v78, s36\n\t"
    "s_mov_b64 exec, 1\n\t"
    "global_store_dword v79, v78, %[pg] offset:64 sc1\n\t"
    "s_mov_b64 exec, -1\n\t"
    "s_branch 14b\n\t"
    "3:\n\t"
    "7:\n\t"
    "s_and_b32 %[st], %[pv], 1\n\t"
    "s_mul_i32 %[st], %[st], 36\n\t"
    "s_add_u32 %[st], %[st], %[cn]\n\t"
    "v_mov_b32 v76, %[st]\n\t"
    "v_mov_b32 v77, 1\n\t"
    "v_mov_b32 v78, %[pv]\n\t"
    "v_mov_b32 v79, 0\n\t"
    "s_waitcnt vmcnt(0)\n\t"
    "s_mov_b64 exec, 1\n\t"
    "ds_add_rtn_u32 v77, v76, v77\n\t"
    "s_waitcnt lgkmcnt(0)\n\t"
    "v_readfirstlane_b32 %[st], v77\n\t"
    "s_and_b32 %[st], %[st], 3\n\t"
    "s_cmp_lg_u32 %[st], 3\n\t"
    "s_cbranch_scc1 6f\n\t"
    "global_store_dword v79, v78, %[pg] sc1\n\t"
    "6:\n\t"
    "s_mov_b64 exec, -1\n\t"
    "s_bitcmp1_b32 %[x], 2\n\t"
    "s_cbranch_scc1 8b\n\t"
    "s_branch 4b\n\t"
    "12:\n\t"
    "s_waitcnt lgkmcnt(0)\n\t"
    "9:"
    : "=&{v[80:95]}"(pd), [st] "=&s"(st), [tk] "+&s"(tk)
    : [ea] "v"(ea), [da] "v"(da), [x] "s"(x), "{v[128:159]}"(W[0]), "{v[160:191]}"(W[1]), "{v[192:223]}"(W[2]), "{v[224:255]}"(W[3]), [pv] "s"(pv), [cn] "s"(cn), [pg] "s"(pg), [bl] "s"(bl)
    : "v96", "v97", "v98", "v99", "v100", "v101", "v102", "v103", "v104", "v105", "v106", "v107", "v108", "v109", "v110", "v111", "v112", "v113", "v114", "v115", "v116", "v117", "v118", "v119", "v120", "v121", "v122", "v123", "v124", "v125", "v126", "v127", "v76", "v77", "v78", "v79", "vcc", "scc", "m0", "memory", "s36", "s37", "s38", "s39");
}
__device__ __forceinline__ void lean_ep0(const u32x32 (&W)[4], unsigned ea, unsigned da, unsigned x, unsigned pv, unsigned cn, const char* pg, unsigned bl, unsigned& tk, unsigned& st,
    unsigned rw, unsigned fa, unsigned fv, const char* cb, unsigned co0, unsigned co1, unsigned& bad, unsigned& pk) {
  asm volatile("v_mov_b32 v76, %[cn]\n\t"
    "ds_read_b32 v76, v76 offset:28\n\t"
    "ds_read_b128 v[96:99], %[ea] offset:0\n\t"
    "ds_read_b128 v[100:103], %[ea] offset:2048\n\t"
    "ds_read_b128 v[104:107], %[da] offset:0\n\t"
    "ds_read_b128 v[108:111], %[da] offset:2048\n\t"
    "ds_read_b128 v[112:115], %[ea] offset:4096\n\t"
    "ds_read_b128 v[116:119], %[ea] offset:6144\n\t"
    "s_waitcnt lgkmcnt(6)\n\t"
    "v_cmp_ne_u32 vcc, 0, v76\n\t"
    "s_mov_b32 %[st], -1\n\t"
    "s_cbranch_vccnz 12f\n\t"
    "s_waitcnt lgkmcnt(4)\n\t"
    "v_mfma_f32_16x16x32_bf16 v[80:83], v[128:131], v[96:99], 0\n\t"
    "ds_read_b128 v[120:123], %[da] offset:4096\n\t"
    "v_mfma_f32_16x16x32_bf16 v[84:87], v[132:135], v[96:99], 0\n\t"
    "ds_read_b128 v[124:127], %[da] offset:6144\n\t"
    "v_mfma_f32_16x16x32_bf16 v[88:91], v[128:131], v[100:103], 0\n\t"
    "v_mfma_f32_16x16x32_bf16 v[92:95], v[132:135], v[100:103], 0\n\t"
    "s_barrier\n\t"
    "s_bitcmp1_b32 %[x], 2\n\t"
    "s_cbranch_scc1 7f\n\t"
    "8:\n\t"
    "s_bitcmp1_b32 %[x], 3\n\t"
    "s_cbranch_scc1 13f\n\t"
    "14:\n\t"
    "s_waitcnt lgkmcnt(4)\n\t"
    "v_mfma_f32_16x16x32_bf16 v[80:83], v[136:139], v[104:107], v[80:83]\n\t"
    "ds_read_b128 v[96:99], %[ea] offset:8192\n\t"
    "v_mfma_f32_16x16x32_bf16 v[84:87], v[140:143], v[104:107], v[84:87]\n\t"
    "ds_read_b128 v[100:103], %[ea] offset:10240\n\t"
    "v_mfma_f32_16x16x32_bf16 v[88:91], v[136:139], v[108:111], v[88:91]\n\t"
    "v_mfma_f32_16x16x32_bf16 v[92:95], v[140:143], v[108:111], v[92:95]\n\t"
    "s_waitcnt lgkmcnt(4)\n\t"
    "v_mfma_f32_16x16x32_bf16 v[80:83], v[144:147], v[112:115], v[80:83]\n\t"
    "ds_read_b128 v[104:107], %[da] offset:8192\n\t"
    "v_mfma_f32_16x16x32_bf16 v[84:87], v[148:151], v[112:115], v[84:87]\n\t"
    "ds_read_b128 v[108:111], %[da] offset:10240\n\t"
    "v_mfma_f32_16x16x32_bf16 v[88:91], v[144:147], v[116:119], v[88:91]\n\t"
    "v_mfma_f32_16x16x32_bf16 v[92:95], v[148:151], v[116:119], v[92:95]\n\t"
    "s_waitcnt lgkmcnt(4)\n\t"
    "v_mfma_f32_16x16x32_bf16 v[80:83], v[152:155], v[120:123], v[80:83]\n\t"
    "ds_read_b128 v[112:115], %[ea] offset:12288\n\t"
    "v_mfma_f32_16x16x32_bf16 v[84:87], v[156:159], v[120:123], v[84:87]\n\t"
    "ds_read_b128 v[116:119], %[ea] offset:14336\n\t"
    "v_mfma_f32_16x16x32_bf16 v[88:91], v[152:155], v[124:127], v[88:91]\n\t"
    "v_mfma_f32_16x16x32_bf16 v[92:95], v[156:159], v[124:127], v[92:95]\n\t"
    "s_waitcnt lgkmcnt(4)\n\t"
    "v_mfma_f32_16x16x32_bf16 v[80:83], v[160:163], v[96:99], v[80:83]\n\t"
    "ds_read_b128 v[120:123], %[da] offset:12288\n\t"
    "v_mfma_f32_16x16x32_bf16 v[84:87], v[164:167], v[96:99], v[84:87]\n\t"
    "ds_read_b128 v[124:127], %[da] offset:14336\n\t"
    "v_mfma_f32_16x16x32_bf16 v[88:91], v[160:163], v[100:103], v[88:91]\n\t"
    "v_mfma_f32_16x16x32_bf16 v[92:95], v[164:167], v[100:103], v[92:95]\n\t"
    "s_barrier\n\t"
    "s_waitcnt lgkmcnt(4)\n\t"
    "v_mfma_f32_16x16x32_bf16 v[80:83], v[168:171], v[104:107], v[80:83]\n\t"
    "ds_read_b128 v[96:99], %[ea] offset:16384\n\t"
    "v_mfma_f32_16x16x32_bf16 v[84:87], v[172:175], v[104:107], v[84:87]\n\t"
    "ds_read_b128 v[100:103], %[ea] offset:18432\n\t"
    "v_mfma_f32_16x16x32_bf16 v[88:91], v[168:171], v[108:111], v[88:91]\n\t"
    "v_mfma_f32_16x16x32_bf16 v[92:95], v[172:175], v[108:111], v[92:95]\n\t"
    "s_waitcnt lgkmcnt(4)\n\t"
    "v_mfma_f32_16x16x32_bf16 v[80:83], v[176:179], v[112:115], v[80:83]\n\t"
    "ds_read_b128 v[104:107], %[da] offset:16384\n\t"
    "v_mfma_f32_16x16x32_bf16 v[84:87], v[180:183], v[112:115], v[84:87]\n\t"
    "ds_read_b128 v[108:111], %[da] offset:18432\n\t"
    "v_mfma_f32_16x16x32_bf16 v[88:91], v[176:179], v[116:119], v[88:91]\n\t"
    "v_mfma_f32_16x16x32_bf16 v[92:95], v[180:183], v[116:119], v[92:95]\n\t"
    "s_waitcnt lgkmcnt(4)\n\t"
    "v_mfma_f32_16x16x32_bf16 v[80:83], v[184:187], v[120:123], v[80:83]\n\t"
    "ds_read_b128 v[112:115], %[ea] offset:20480\n\t"
    "v_mfma_f32_16x16x32_bf16 v[84:87], v[188:191], v[120:123], v[84:87]\n\t"
    "ds_read_b128 v[116:119], %[ea] offset:22528\n\t"
    "v_mfma_f32_16x16x32_bf16 v[88:91], v[184:187], v[124:127], v[88:91]\n\t"
    "v_mfma_f32_16x16x32_bf16 v[92:95], v[188:191], v[124:127], v[92:95]\n\t"
    "s_waitcnt lgkmcnt(4)\n\t"
    "v_mfma_f32_16x16x32_bf16 v[80:83], v[192:195], v[96:99], v[80:83]\n\t"
    "ds_read_b128 v[120:123], %[da] offset:20480\n\t"
    "v_mfma_f32_16x16x32_bf16 v[84:87], v[196:199], v[96:99], v[84:87]\n\t"
    "ds_read_b128 v[124:127], %[da] offset:22528\n\t"
    "v_mfma_f32_16x16x32_bf16 v[88:91], v[192:195], v[100:103], v[88:91]\n\t"
    "v_mfma_f32_16x16x32_bf16 v[92:95], v[196:199], v[100:103], v[92:95]\n\t"
    "s_barrier\n\t"
    "s_and_b32 %[st], %[x], 3\n\t"
    "s_cbranch_scc1 3f\n\t"
    "4:\n\t"
    "s_waitcnt lgkmcnt(4)\n\t"
    "v_mfma_f32_16x16x32_bf16 v[80:83], v[200:203], v[104:107], v[80:83]\n\t"
    "ds_read_b128 v[96:99], %[ea] offset:24576\n\t"
    "v_mfma_f32_16x16x32_bf16 v[84:87], v[204:207], v[104:107], v[84:87]\n\t"
    "ds_read_b128 v[100:103], %[ea] offset:26624\n\t"
    "v_mfma_f32_16x16x32_bf16 v[88:91], v[200:203], v[108:111], v[88:91]\n\t"
    "v_mfma_f32_16x16x32_bf16 v[92:95], v[204:207], v[108:111], v[92:95]\n\t"
    "s_waitcnt lgkmcnt(4)\n\t"
    "v_mfma_f32_16x16x32_bf16 v[80:83], v[208:211], v[112:115], v[80:83]\n\t"
    "ds_read_b128 v[104:107], %[da] offset:24576\n\t"
    "v_mfma_f32_16x16x32_bf16 v[84:87], v[212:215], v[112:115], v[84:87]\n\t"
    "ds_read_b128 v[108:111], %[da] offset:26624\n\t"
    "v_mfma_f32_16x16x32_bf16 v[88:91], v[208:211], v[116:119], v[88:91]\n\t"
    "v_mfma_f32_16x16x32_bf16 v[92:95], v[212:215], v[116:119], v[92:95]\n\t"
    "s_waitcnt lgkmcnt(4)\n\t"
    "v_mfma_f32_16x16x32_bf16 v[80:83], v[216:219], v[120:123], v[80:83]\n\t"
    "ds_read_b128 v[112:115], %[ea] offset:28672\n\t"
    "v_mfma_f32_16x16x32_bf16 v[84:87], v[220:223], v[120:123], v[84:87]\n\t"
    "ds_read_b128 v[116:119], %[ea] offset:30720\n\t"
    "v_mfma_f32_16x16x32_bf16 v[88:91], v[216:219], v[124:127], v[88:91]\n\t"
    "v_mfma_f32_16x16x32_bf16 v[92:95], v[220:223], v[124:127], v[92:95]\n\t"
    "s_waitcnt lgkmcnt(4)\n\t"
    "v_mfma_f32_16x16x32_bf16 v[80:83], v[224:227], v[96:99], v[80:83]\n\t"
    "ds_read_b128 v[120:123], %[da] offset:28672\n\t"
    "v_mfma_f32_16x16x32_bf16 v[84:87], v[228:231], v[96:99], v[84:87]\n\t"
    "ds_read_b128 v[124:127], %[da] offset:30720\n\t"
    "v_mfma_f32_16x16x32_bf16 v[88:91], v[224:227], v[100:103], v[88:91]\n\t"
    "v_mfma_f32_16x16x32_bf16 v[92:95], v[228:231], v[100:103], v[92:95]\n\t"
    "s_waitcnt lgkmcnt(4)\n\t"
    "v_mfma_f32_16x16x32_bf16 v[80:83], v[232:235], v[104:107], v[80:83]\n\t"
    "v_mfma_f32_16x16x32_bf16 v[84:87], v[236:239], v[104:107], v[84:87]\n\t"
    "v_mfma_f32_16x16x32_bf16 v[88:91], v[232:235], v[108:111], v[88:91]\n\t"
    "v_mfma_f32_16x16x32_bf16 v[92:95], v[236:239], v[108:111], v[92:95]\n\t"
    "s_waitcnt lgkmcnt(2)\n\t"
    "v_mfma_f32_16x16x32_bf16 v[80:83], v[240:243], v[112:115], v[80:83]\n\t"
    "ds_read_b32 v39, %[fa]\n\t"
    "ds_read_b128 v[48:51], %[rw] offset:0\n\t"
    "v_mfma_f32_16x16x32_bf16 v[84:87], v[244:247], v[112:115], v[84:87]\n\t"
    "ds_read_b128 v[52:55], %[rw] offset:1024\n\t"
    "v_mfma_f32_16x16x32_bf16 v[88:91], v[240:243], v[116:119], v[88:91]\n\t"
    "ds_read_b128 v[56:59], %[rw] offset:2048\n\t"
    "v_mfma_f32_16x16x32_bf16 v[92:95], v[244:247], v[116:119], v[92:95]\n\t"
    "ds_read_b128 v[60:63], %[rw] offset:3072\n\t"
    "s_waitcnt lgkmcnt(5)\n\t"
    "v_mfma_f32_16x16x32_bf16 v[80:83], v[248:251], v[120:123], v[80:83]\n\t"
    "v_mfma_f32_16x16x32_bf16 v[84:87], v[252:255], v[120:123], v[84:87]\n\t"
    "v_mfma_f32_16x16x32_bf16 v[88:91], v[248:251], v[124:127], v[88:91]\n\t"
    "v_mfma_f32_16x16x32_bf16 v[92:95], v[252:255], v[124:127], v[92:95]\n\t"
    "s_mov_b32 %[st], 0\n\t"
    "21:\n\t"
    "s_waitcnt lgkmcnt(0)\n\t"
    "v_cmp_ne_u32 vcc, %[fv], v39\n\t"
    "s_cbranch_vccnz 20f\n\t"
    "v_mov_b32 v39, %[cn]\n\t"
    "s_nop 0\n\t"
    "v_pk_add_f32 v[48:49], v[48:49], v[80:81]\n\t"
    "v_pk_add_f32 v[50:51], v[50:51], v[82:83]\n\t"
    "v_pk_add_f32 v[52:53], v[52:53], v[84:85]\n\t"
    "v_pk_add_f32 v[54:55], v[54:55], v[86:87]\n\t"
    "v_pk_add_f32 v[56:57], v[56:57], v[88:89]\n\t"
    "v_pk_add_f32 v[58:59], v[58:59], v[90:91]\n\t"
    "v_pk_add_f32 v[60:61], v[60:61], v[92:93]\n\t"
    "v_pk_add_f32 v[62:63], v[62:63], v[94:95]\n\t"
    "v_cvt_pk_bf16_f32 v40, v48, v49\n\t"
    "v_cvt_pk_bf16_f32 v41, v50, v51\n\t"
    "v_cvt_pk_bf16_f32 v42, v52, v53\n\t"
    "v_cvt_pk_bf16_f32 v43, v54, v55\n\t"
    "global_store_dwordx4 %[co0], v[40:43], %[cb] sc1\n\t"
    "ds_read_b32 v38, v39 offset:4\n\t"
    "v_cvt_pk_bf16_f32 v44, v56, v57\n\t"
    "v_cvt_pk_bf16_f32 v45, v58, v59\n\t"
    "v_cvt_pk_bf16_f32 v46, v60, v61\n\t"
    "v_cvt_pk_bf16_f32 v47, v62, v63\n\t"
    "global_store_dwordx4 %[co1], v[44:47], %[cb] sc1\n\t"
    "s_memrealtime s[36:37]\n\t"
    "v_cmp_u_f32 vcc, v48, v56\n\t"
    "s_waitcnt lgkmcnt(0)\n\t"
    "s_or_b32 %[bad], vcc_lo, vcc_hi\n\t"
    "v_readfirstlane_b32 %[pk], v38\n\t"
    "s_sub_u32 %[tk], s36, %[tk]\n\t"
    "s_branch 9f\n\t"
    "20:\n\t"
    "ds_read_b32 v39, %[fa]\n\t"
    "ds_read_b128 v[48:51], %[rw] offset:0\n\t"
    "ds_read_b128 v[52:55], %[rw] offset:1024\n\t"
    "ds_read_b128 v[56:59], %[rw] offset:2048\n\t"
    "ds_read_b128 v[60:63], %[rw] offset:3072\n\t"
    "s_branch 21b\n\t"
    "13:\n\t"
    "s_memrealtime s[36:37]\n\t"
    "v_mov_b32 v79, 0\n\t"
    "s_waitcnt lgkmcnt(0)\n\t"
    "s_mov_b32 %[tk], s36\n\t"
    "s_add_u32 s36, s36, %[bl]\n\t"
    "s_and_b32 s36, s36, 0xffffff\n\t"
    "s_and_b32 s37, %[bl], 0xff000000\n\t"
    "s_or_b32 s36, s36, s37\n\t"
    "v_mov_b32 v78, s36\n\t"
    "s_mov_b64 exec, 1\n\t"
    "global_store_dword v79, v78, %[pg] offset:64 sc1\n\t"
    "s_mov_b64 exec, -1\n\t"
    "s_branch 14b\n\t"
    "3:\n\t"
    "7:\n\t"
    "s_and_b32 %[st], %[pv], 1\n\t"
    "s_mul_i32 %[st], %[st], 36\n\t"
    "s_add_u32 %[st], %[st], %[cn]\n\t"
    "v_mov_b32 v76, %[st]\n\t"
    "v_mov_b32 v77, 1\n\t"
    "v_mov_b32 v78, %[pv]\n\t"
    "v_mov_b32 v79, 0\n\t"
    "s_waitcnt vmcnt(0)\n\t"
    "s_mov_b64 exec, 1\n\t"
    "ds_add_rtn_u32 v77, v76, v77\n\t"
    "s_waitcnt lgkmcnt(0)\n\t"
    "v_readfirstlane_b32 %[st], v77\n\t"
    "s_and_b32 %[st], %[st], 3\n\t"
    "s_cmp_lg_u32 %[st], 3\n\t"
    "s_cbranch_scc1 6f\n\t"
    "global_store_dword v79, v78, %[pg] sc1\n\t"
    "6:\n\t"
    "s_mov_b64 exec, -1\n\t"
    "s_bitcmp1_b32 %[x], 2\n\t"
    "s_cbranch_scc1 8b\n\t"
    "s_branch 4b\n\t"
    "12:\n\t"
    "s_waitcnt lgkmcnt(0)\n\t"
    "9:"
    : [st] "=&s"(st), [tk] "+&s"(tk), [bad] "=&s"(bad), [pk] "=&s"(pk)
    : [ea] "v"(ea), [da] "v"(da), [x] "s"(x), "{v[128:159]}"(W[0]), "{v[160:191]}"(W[1]), "{v[192:223]}"(W[2]), "{v[224:255]}"(W[3]), [pv] "s"(pv), [cn] "s"(cn), [pg] "s"(pg), [bl] "s"(bl), [rw] "v"(rw), [fa] "v"(fa), [fv] "s"(fv), [cb] "s"(cb), [co0] "v"(co0), [co1] "v"(co1)
    : "v96", "v97", "v98", "v99", "v100", "v101", "v102", "v103", "v104", "v105", "v106", "v107", "v108", "v109", "v110", "v111", "v112", "v113", "v114", "v115", "v116", "v117", "v118", "v119", "v120", "v121", "v122", "v123", "v124", "v125", "v126", "v127", "v76", "v77", "v78", "v79", "v38", "v39", "v40", "v41", "v42", "v43", "v44", "v45", "v46", "v47", "v48", "v49", "v50", "v51", "v52", "v53", "v54", "v55", "v56", "v57", "v58", "v59", "v60", "v61", "v62", "v63", "v80", "v81", "v82", "v83", "v84", "v85", "v86", "v87", "v88", "v89", "v90", "v91", "v92", "v93", "v94", "v95", "vcc", "scc", "m0", "memory", "s36", "s37", "s38", "s39");
}
__device__ __forceinline__ void lean_ep1(const u32x32 (&W)[4], unsigned ea, unsigned da, unsigned x, unsigned pv, unsigned cn, const char* pg, unsigned bl, unsigned& tk, unsigned& st,
    unsigned rw, unsigned fa, unsigned fv, const char* cb, unsigned co0, unsigned co1, unsigned& bad, unsigned& pk) {
  asm volatile("v_mov_b32 v76, %[cn]\n\t"
    "ds_read_b32 v76, v76 offset:28\n\t"
    "ds_read_b128 v[96:99], %[ea] offset:0\n\t"
    "ds_read_b128 v[100:103], %[ea] offset:2048\n\t"
    "ds_read_b128 v[104:107], %[da] offset:0\n\t"
    "ds_read_b128 v[108:111], %[da] offset:2048\n\t"
    "ds_read_b128 v[112:115], %[ea] offset:4096\n\t"
    "ds_read_b128 v[116:119], %[ea] offset:6144\n\t"
    "s_waitcnt lgkmcnt(6)\n\t"
    "v_cmp_ne_u32 vcc, 0, v76\n\t"
    "s_mov_b32 %[st], -1\n\t"
    "s_cbranch_vccnz 12f\n\t"
    "s_waitcnt lgkmcnt(4)\n\t"
    "v_mfma_f32_16x16x32_bf16 v[80:83], v[128:131], v[96:99], 0\n\t"
    "ds_read_b128 v[120:123], %[da] offset:4096\n\t"
    "v_mfma_f32_16x16x32_bf16 v[84:87], v[132:135], v[96:99], 0\n\t"
    "ds_read_b128 v[124:127], %[da] offset:6144\n\t"
    "v_mfma_f32_16x16x32_bf16 v[88:91], v[128:131], v[100:103], 0\n\t"
    "v_mfma_f32_16x16x32_bf16 v[92:95], v[132:135], v[100:103], 0\n\t"
    "s_barrier\n\t"
    "s_bitcmp1_b32 %[x], 2\n\t"
    "s_cbranch_scc1 7f\n\t"
    "8:\n\t"
    "s_bitcmp1_b32 %[x], 3\n\t"
    "s_cbranch_scc1 13f\n\t"
    "14:\n\t"
    "s_waitcnt lgkmcnt(4)\n\t"
    "v_mfma_f32_16x16x32_bf16 v[80:83], v[136:139], v[104:107], v[80:83]\n\t"
    "ds_read_b128 v[96:99], %[ea] offset:8192\n\t"
    "v_mfma_f32_16x16x32_bf16 v[84:87], v[140:143], v[104:107], v[84:87]\n\t"
    "ds_read_b128 v[100:103], %[ea] offset:10240\n\t"
    "v_mfma_f32_16x16x32_bf16 v[88:91], v[136:139], v[108:111], v[88:91]\n\t"
    "v_mfma_f32_16x16x32_bf16 v[92:95], v[140:143], v[108:111], v[92:95]\n\t"
    "s_waitcnt lgkmcnt(4)\n\t"
    "v_mfma_f32_16x16x32_bf16 v[80:83], v[144:147], v[112:115], v[80:83]\n\t"
    "ds_read_b128 v[104:107], %[da] offset:8192\n\t"
    "v_mfma_f32_16x16x32_bf16 v[84:87], v[148:151], v[112:115], v[84:87]\n\t"
    "ds_read_b128 v[108:111], %[da] offset:10240\n\t"
    "v_mfma_f32_16x16x32_bf16 v[88:91], v[144:147], v[116:119], v[88:91]\n\t"
    "v_mfma_f32_16x16x32_bf16 v[92:95], v[148:151], v[116:119], v[92:95]\n\t"
    "s_waitcnt lgkmcnt(4)\n\t"
    "v_mfma_f32_16x16x32_bf16 v[80:83], v[152:155], v[120:123], v[80:83]\n\t"
    "ds_read_b128 v[112:115], %[ea] offset:12288\n\t"
    "v_mfma_f32_16x16x32_bf16 v[84:87], v[156:159], v[120:123], v[84:87]\n\t"
    "ds_read_b128 v[116:119], %[ea] offset:14336\n\t"
    "v_mfma_f32_16x16x32_bf16 v[88:91], v[152:155], v[124:127], v[88:91]\n\t"
    "v_mfma_f32_16x16x32_bf16 v[92:95], v[156:159], v[124:127], v[92:95]\n\t"
    "s_waitcnt lgkmcnt(4)\n\t"
    "v_mfma_f32_16x16x32_bf16 v[80:83], v[160:163], v[96:99], v[80:83]\n\t"
    "ds_read_b128 v[120:123], %[da] offset:12288\n\t"
    "v_mfma_f32_16x16x32_bf16 v[84:87], v[164:167], v[96:99], v[84:87]\n\t"
    "ds_read_b128 v[124:127], %[da] offset:14336\n\t"
    "v_mfma_f32_16x16x32_bf16 v[88:91], v[160:163], v[100:103], v[88:91]\n\t"
    "v_mfma_f32_16x16x32_bf16 v[92:95], v[164:167], v[100:103], v[92:95]\n\t"
    "s_barrier\n\t"
    "s_waitcnt lgkmcnt(4)\n\t"
    "v_mfma_f32_16x16x32_bf16 v[80:83], v[168:171], v[104:107], v[80:83]\n\t"
    "ds_read_b128 v[96:99], %[ea] offset:16384\n\t"
    "v_mfma_f32_16x16x32_bf16 v[84:87], v[172:175], v[104:107], v[84:87]\n\t"
    "ds_read_b128 v[100:103], %[ea] offset:18432\n\t"
    "v_mfma_f32_16x16x32_bf16 v[88:91], v[168:171], v[108:111], v[88:91]\n\t"
    "v_mfma_f32_16x16x32_bf16 v[92:95], v[172:175], v[108:111], v[92:95]\n\t"
    "s_waitcnt lgkmcnt(4)\n\t"
    "v_mfma_f32_16x16x32_bf16 v[80:83], v[176:179], v[112:115], v[80:83]\n\t"
    "ds_read_b128 v[104:107], %[da] offset:16384\n\t"
    "v_mfma_f32_16x16x32_bf16 v[84:87], v[180:183], v[112:115], v[84:87]\n\t"
    "ds_read_b128 v[108:111], %[da] offset:18432\n\t"
    "v_mfma_f32_16x16x32_bf16 v[88:91], v[176:179], v[116:119], v[88:91]\n\t"
    "v_mfma_f32_16x16x32_bf16 v[92:95], v[180:183], v[116:119], v[92:95]\n\t"
    "s_waitcnt lgkmcnt(4)\n\t"
    "v_mfma_f32_16x16x32_bf16 v[80:83], v[184:187], v[120:123], v[80:83]\n\t"
    "ds_read_b128 v[112:115], %[ea] offset:20480\n\t"
    "v_mfma_f32_16x16x32_bf16 v[84:87], v[188:191], v[120:123], v[84:87]\n\t"
    "ds_read_b128 v[116:119], %[ea] offset:22528\n\t"
    "v_mfma_f32_16x16x32_bf16 v[88:91], v[184:187], v[124:127], v[88:91]\n\t"
    "v_mfma_f32_16x16x32_bf16 v[92:95], v[188:191], v[124:127], v[92:95]\n\t"
    "s_waitcnt lgkmcnt(4)\n\t"
    "v_mfma_f32_16x16x32_bf16 v[80:83], v[192:195], v[96:99], v[80:83]\n\t"
    "ds_read_b128 v[120:123], %[da] offset:20480\n\t"
    "v_mfma_f32_16x16x32_bf16 v[84:87], v[196:199], v[96:99], v[84:87]\n\t"
    "ds_read_b128 v[124:127], %[da] offset:22528\n\t"
    "v_mfma_f32_16x16x32_bf16 v[88:91], v[192:195], v[100:103], v[88:91]\n\t"
    "v_mfma_f32_16x16x32_bf16 v[92:95], v[196:199], v[100:103], v[92:95]\n\t"
    "s_barrier\n\t"
    "s_and_b32 %[st], %[x], 3\n\t"
    "s_cbranch_scc1 3f\n\t"
    "4:\n\t"
    "s_waitcnt lgkmcnt(4)\n\t"
    "v_mfma_f32_16x16x32_bf16 v[80:83], v[200:203], v[104:107], v[80:83]\n\t"
    "ds_read_b128 v[96:99], %[ea] offset:24576\n\t"
    "v_mfma_f32_16x16x32_bf16 v[84:87], v[204:207], v[104:107], v[84:87]\n\t"
    "ds_read_b128 v[100:103], %[ea] offset:26624\n\t"
    "v_mfma_f32_16x16x32_bf16 v[88:91], v[200:203], v[108:111], v[88:91]\n\t"
    "v_mfma_f32_16x16x32_bf16 v[92:95], v[204:207], v[108:111], v[92:95]\n\t"
    "s_waitcnt lgkmcnt(4)\n\t"
    "v_mfma_f32_16x16x32_bf16 v[80:83], v[208:211], v[112:115], v[80:83]\n\t"
    "ds_read_b128 v[104:107], %[da] offset:24576\n\t"
    "v_mfma_f32_16x16x32_bf16 v[84:87], v[212:215], v[112:115], v[84:87]\n\t"
    "ds_read_b128 v[108:111], %[da] offset:26624\n\t"
    "v_mfma_f32_16x16x32_bf16 v[88:91], v[208:211], v[116:119], v[88:91]\n\t"
    "v_mfma_f32_16x16x32_bf16 v[92:95], v[212:215], v[116:119], v[92:95]\n\t"
    "s_waitcnt lgkmcnt(4)\n\t"
    "v_mfma_f32_16x16x32_bf16 v[80:83], v[216:219], v[120:123], v[80:83]\n\t"
    "ds_read_b128 v[112:115], %[ea] offset:28672\n\t"
    "v_mfma_f32_16x16x32_bf16 v[84:87], v[220:223], v[120:123], v[84:87]\n\t"
    "ds_read_b128 v[116:119], %[ea] offset:30720\n\t"
    "v_mfma_f32_16x16x32_bf16 v[88:91], v[216:219], v[124:127], v[88:91]\n\t"
    "v_mfma_f32_16x16x32_bf16 v[92:95], v[220:223], v[124:127], v[92:95]\n\t"
    "s_waitcnt lgkmcnt(4)\n\t"
    "v_mfma_f32_16x16x32_bf16 v[80:83], v[224:227], v[96:99], v[80:83]\n\t"
    "ds_read_b128 v[120:123], %[da] offset:28672\n\t"
    "v_mfma_f32_16x16x32_bf16 v[84:87], v[228:231], v[96:99], v[84:87]\n\t"
    "ds_read_b128 v[124:127], %[da] offset:30720\n\t"
    "v_mfma_f32_16x16x32_bf16 v[88:91], v[224:227], v[100:103], v[88:91]\n\t"
    "v_mfma_f32_16x16x32_bf16 v[92:95], v[228:231], v[100:103], v[92:95]\n\t"
    "s_waitcnt lgkmcnt(4)\n\t"
    "v_mfma_f32_16x16x32_bf16 v[80:83], v[232:235], v[104:107], v[80:83]\n\t"
    "v_mfma_f32_16x16x32_bf16 v[84:87], v[236:239], v[104:107], v[84:87]\n\t"
    "v_mfma_f32_16x16x32_bf16 v[88:91], v[232:235], v[108:111], v[88:91]\n\t"
    "v_mfma_f32_16x16x32_bf16 v[92:95], v[236:239], v[108:111], v[92:95]\n\t"
    "s_waitcnt lgkmcnt(2)\n\t"
    "v_mfma_f32_16x16x32_bf16 v[80:83], v[240:243], v[112:115], v[80:83]\n\t"
    "ds_read_b32 v39, %[fa]\n\t"
    "ds_read_b128 v[48:51], %[rw] offset:0\n\t"
    "v_mfma_f32_16x16x32_bf16 v[84:87], v[244:247], v[112:115], v[84:87]\n\t"
    "ds_read_b128 v[52:55], %[rw] offset:1024\n\t"
    "v_mfma_f32_16x16x32_bf16 v[88:91], v[240:243], v[116:119], v[88:91]\n\t"
    "ds_read_b128 v[56:59], %[rw] offset:2048\n\t"
    "v_mfma_f32_16x16x32_bf16 v[92:95], v[244:247], v[116:119], v[92:95]\n\t"
    "ds_read_b128 v[60:63], %[rw] offset:3072\n\t"
    "s_waitcnt lgkmcnt(5)\n\t"
    "v_mfma_f32_16x16x32_bf16 v[80:83], v[248:251], v[120:123], v[80:83]\n\t"
    "v_mfma_f32_16x16x32_bf16 v[84:87], v[252:255], v[120:123], v[84:87]\n\t"
    "v_mfma_f32_16x16x32_bf16 v[88:91], v[248:251], v[124:127], v[88:91]\n\t"
    "v_mfma_f32_16x16x32_bf16 v[92:95], v[252:255], v[124:127], v[92:95]\n\t"
    "s_mov_b32 %[st], 0\n\t"
    "21:\n\t"
    "s_waitcnt lgkmcnt(0)\n\t"
    "v_cmp_ne_u32 vcc, %[fv], v39\n\t"
    "s_cbranch_vccnz 20f\n\t"
    "v_mov_b32 v39, %[cn]\n\t"
    "s_nop 0\n\t"
    "v_pk_add_f32 v[48:49], v[48:49], v[80:81]\n\t"
    "v_pk_add_f32 v[50:51], v[50:51], v[82:83]\n\t"
    "v_pk_add_f32 v[52:53], v[52:53], v[84:85]\n\t"
    "v_pk_add_f32 v[54:55], v[54:55], v[86:87]\n\t"
    "v_pk_add_f32 v[56:57], v[56:57], v[88:89]\n\t"
    "v_pk_add_f32 v[58:59], v[58:59], v[90:91]\n\t"
    "v_pk_add_f32 v[60:61], v[60:61], v[92:93]\n\t"
    "v_pk_add_f32 v[62:63], v[62:63], v[94:95]\n\t"
    "v_mul_f32 v40, %[nl], v48\n\t"
    "v_mul_f32 v41, %[nl], v49\n\t"
    "v_mul_f32 v42, %[nl], v50\n\t"
    "v_mul_f32 v43, %[nl], v51\n\t"
    "v_mul_f32 v44, %[nl], v56\n\t"
    "v_mul_f32 v45, %[nl], v57\n\t"
    "v_mul_f32 v46, %[nl], v58\n\t"
    "v_mul_f32 v47, %[nl], v59\n\t"
    "v_exp_f32 v40, v40\n\t"
    "v_exp_f32 v41, v41\n\t"
    "v_exp_f32 v42, v42\n\t"
    "v_exp_f32 v43, v43\n\t"
    "v_exp_f32 v44, v44\n\t"
    "v_exp_f32 v45, v45\n\t"
    "v_exp_f32 v46, v46\n\t"
    "v_exp_f32 v47, v47\n\t"
    "v_pk_add_f32 v[40:41], v[40:41], 1.0 op_sel_hi:[1,0]\n\t"
    "v_pk_add_f32 v[42:43], v[42:43], 1.0 op_sel_hi:[1,0]\n\t"
    "v_pk_add_f32 v[44:45], v[44:45], 1.0 op_sel_hi:[1,0]\n\t"
    "v_pk_add_f32 v[46:47], v[46:47], 1.0 op_sel_hi:[1,0]\n\t"
    "v_rcp_f32 v40, v40\n\t"
    "v_rcp_f32 v41, v41\n\t"
    "v_rcp_f32 v42, v42\n\t"
    "v_rcp_f32 v43, v43\n\t"
    "v_rcp_f32 v44, v44\n\t"
    "v_rcp_f32 v45, v45\n\t"
    "v_rcp_f32 v46, v46\n\t"
    "v_rcp_f32 v47, v47\n\t"
    "v_pk_mul_f32 v[40:41], v[48:49], v[40:41]\n\t"
    "v_pk_mul_f32 v[42:43], v[50:51], v[42:43]\n\t"
    "v_pk_mul_f32 v[44:45], v[56:57], v[44:45]\n\t"
    "v_pk_mul_f32 v[46:47], v[58:59], v[46:47]\n\t"
    "v_pk_mul_f32 v[40:41], v[40:41], v[52:53]\n\t"
    "v_pk_mul_f32 v[42:43], v[42:43], v[54:55]\n\t"
    "v_pk_mul_f32 v[44:45], v[44:45], v[60:61]\n\t"
    "v_pk_mul_f32 v[46:47], v[46:47], v[62:63]\n\t"
    "v_cvt_pk_bf16_f32 v48, v40, v41\n\t"
    "v_cvt_pk_bf16_f32 v49, v42, v43\n\t"
    "v_cvt_pk_bf16_f32 v50, v44, v45\n\t"
    "ds_read_b32 v38, v39 offset:4\n\t"
    "v_cvt_pk_bf16_f32 v51, v46, v47\n\t"
    "s_nop 1\n\t"
    "v_permlane16_swap_b32 v48, v50\n\t"
    "v_permlane16_swap_b32 v49, v51\n\t"
    "global_store_dwordx4 %[co0], v[48:51], %[cb] sc1\n\t"
    "s_memrealtime s[36:37]\n\t"
    "v_cmp_u_f32 vcc, v52, v60\n\t"
    "s_waitcnt lgkmcnt(0)\n\t"
    "s_or_b32 %[bad], vcc_lo, vcc_hi\n\t"
    "v_readfirstlane_b32 %[pk], v38\n\t"
    "s_sub_u32 %[tk], s36, %[tk]\n\t"
    "s_branch 9f\n\t"
    "20:\n\t"
    "ds_read_b32 v39, %[fa]\n\t"
    "ds_read_b128 v[48:51], %[rw] offset:0\n\t"
    "ds_read_b128 v[52:55], %[rw] offset:1024\n\t"
    "ds_read_b128 v[56:59], %[rw] offset:2048\n\t"
    "ds_read_b128 v[60:63], %[rw] offset:3072\n\t"
    "s_branch 21b\n\t"
    "13:\n\t"
    "s_memrealtime s[36:37]\n\t"
    "v_mov_b32 v79, 0\n\t"
    "s_waitcnt lgkmcnt(0)\n\t"
    "s_mov_b32 %[tk], s36\n\t"
    "s_add_u32 s36, s36, %[bl]\n\t"
    "s_and_b32 s36, s36, 0xffffff\n\t"
    "s_and_b32 s37, %[bl], 0xff000000\n\t"
    "s_or_b32 s36, s36, s37\n\t"
    "v_mov_b32 v78, s36\n\t"
    "s_mov_b64 exec, 1\n\t"
    "global_store_dword v79, v78, %[pg] offset:64 sc1\n\t"
    "s_mov_b64 exec, -1\n\t"
    "s_branch 14b\n\t"
    "3:\n\t"
    "7:\n\t"
    "s_and_b32 %[st], %[pv], 1\n\t"
    "s_mul_i32 %[st], %[st], 36\n\t"
    "s_add_u32 %[st], %[st], %[cn]\n\t"
    "v_mov_b32 v76, %[st]\n\t"
    "v_mov_b32 v77, 1\n\t"
    "v_mov_b32 v78, %[pv]\n\t"
    "v_mov_b32 v79, 0\n\t"
    "s_waitcnt vmcnt(0)\n\t"
    "s_mov_b64 exec, 1\n\t"
    "ds_add_rtn_u32 v77, v76, v77\n\t"
    "s_waitcnt lgkmcnt(0)\n\t"
    "v_readfirstlane_b32 %[st], v77\n\t"
    "s_and_b32 %[st], %[st], 3\n\t"
    "s_cmp_lg_u32 %[st], 3\n\t"
    "s_cbranch_scc1 6f\n\t"
    "global_store_dword v79, v78, %[pg] sc1\n\t"
    "6:\n\t"
    "s_mov_b64 exec, -1\n\t"
    "s_bitcmp1_b32 %[x], 2\n\t"
    "s_cbranch_scc1 8b\n\t"
    "s_branch 4b\n\t"
    "12:\n\t"
    "s_waitcnt lgkmcnt(0)\n\t"
    "9:"
    : [st] "=&s"(st), [tk] "+&s"(tk), [bad] "=&s"(bad), [pk] "=&s"(pk)
    : [ea] "v"(ea), [da] "v"(da), [x] "s"(x), "{v[128:159]}"(W[0]), "{v[160:191]}"(W[1]), "{v[192:223]}"(W[2]), "{v[224:255]}"(W[3]), [pv] "s"(pv), [cn] "s"(cn), [pg] "s"(pg), [bl] "s"(bl), [rw] "v"(rw), [fa] "v"(fa), [fv] "s"(fv), [cb] "s"(cb), [co0] "v"(co0), [nl] "s"(-LOG2E)
    : "v96", "v97", "v98", "v99", "v100", "v101", "v102", "v103", "v104", "v105", "v106", "v107", "v108", "v109", "v110", "v111", "v112", "v113", "v114", "v115", "v116", "v117", "v118", "v119", "v120", "v121", "v122", "v123", "v124", "v125", "v126", "v127", "v76", "v77", "v78", "v79", "v38", "v39", "v40", "v41", "v42", "v43", "v44", "v45", "v46", "v47", "v48", "v49", "v50", "v51", "v52", "v53", "v54", "v55", "v56", "v57", "v58", "v59", "v60", "v61", "v62", "v63", "v80", "v81", "v82", "v83", "v84", "v85", "v86", "v87", "v88", "v89", "v90", "v91", "v92", "v93", "v94", "v95", "vcc", "scc", "m0", "memory", "s36", "s37", "s38", "s39");
}
__device__ __forceinline__ void lean_ep2(const u32x32 (&W)[4], unsigned ea, unsigned da, unsigned x, unsigned pv, unsigned cn, const char* pg, unsigned bl, unsigned& tk, unsigned& st,
    unsigned rw, unsigned fa, unsigned fv, const char* cb, unsigned co0, unsigned co1, unsigned& bad, unsigned& pk) {
  asm volatile("v_mov_b32 v76, %[cn]\n\t"
    "ds_read_b32 v76, v76 offset:28\n\t"
    "ds_read_b128 v[96:99], %[ea] offset:0\n\t"
    "ds_read_b128 v[100:103], %[ea] offset:2048\n\t"
    "ds_read_b128 v[104:107], %[da] offset:0\n\t"
    "ds_read_b128 v[108:111], %[da] offset:2048\n\t"
    "ds_read_b128 v[112:115], %[ea] offset:4096\n\t"
    "ds_read_b128 v[116:119], %[ea] offset:6144\n\t"
    "s_waitcnt lgkmcnt(6)\n\t"
    "v_cmp_ne_u32 vcc, 0, v76\n\t"
    "s_mov_b32 %[st], -1\n\t"
    "s_cbranch_vccnz 12f\n\t"
    "s_waitcnt lgkmcnt(4)\n\t"
    "v_mfma_f32_16x16x32_bf16 v[80:83], v[128:131], v[96:99], 0\n\t"
    "ds_read_b128 v[120:123], %[da] offset:4096\n\t"
    "v_mfma_f32_16x16x32_bf16 v[84:87], v[132:135], v[96:99], 0\n\t"
    "ds_read_b128 v[124:127], %[da] offset:6144\n\t"
    "v_mfma_f32_16x16x32_bf16 v[88:91], v[128:131], v[100:103], 0\n\t"
    "v_mfma_f32_16x16x32_bf16 v[92:95], v[132:135], v[100:103], 0\n\t"
    "s_barrier\n\t"
    "s_bitcmp1_b32 %[x], 2\n\t"
    "s_cbranch_scc1 7f\n\t"
    "8:\n\t"
    "s_bitcmp1_b32 %[x], 3\n\t"
    "s_cbranch_scc1 13f\n\t"
    "14:\n\t"
    "s_waitcnt lgkmcnt(4)\n\t"
    "v_mfma_f32_16x16x32_bf16 v[80:83], v[136:139], v[104:107], v[80:83]\n\t"
    "ds_read_b128 v[96:99], %[ea] offset:8192\n\t"
    "v_mfma_f32_16x16x32_bf16 v[84:87], v[140:143], v[104:107], v[84:87]\n\t"
    "ds_read_b128 v[100:103], %[ea] offset:10240\n\t"
    "v_mfma_f32_16x16x32_bf16 v[88:91], v[136:139], v[108:111], v[88:91]\n\t"
    "v_mfma_f32_16x16x32_bf16 v[92:95], v[140:143], v[108:111], v[92:95]\n\t"
    "s_waitcnt lgkmcnt(4)\n\t"
    "v_mfma_f32_16x16x32_bf16 v[80:83], v[144:147], v[112:115], v[80:83]\n\t"
    "ds_read_b128 v[104:107], %[da] offset:8192\n\t"
    "v_mfma_f32_16x16x32_bf16 v[84:87], v[148:151], v[112:115], v[84:87]\n\t"
    "ds_read_b128 v[108:111], %[da] offset:10240\n\t"
    "v_mfma_f32_16x16x32_bf16 v[88:91], v[144:147], v[116:119], v[88:91]\n\t"
    "v_mfma_f32_16x16x32_bf16 v[92:95], v[148:151], v[116:119], v[92:95]\n\t"
    "s_waitcnt lgkmcnt(4)\n\t"
    "v_mfma_f32_16x16x32_bf16 v[80:83], v[152:155], v[120:123], v[80:83]\n\t"
    "ds_read_b128 v[112:115], %[ea] offset:12288\n\t"
    "v_mfma_f32_16x16x32_bf16 v[84:87], v[156:159], v[120:123], v[84:87]\n\t"
    "ds_read_b128 v[116:119], %[ea] offset:14336\n\t"
    "v_mfma_f32_16x16x32_bf16 v[88:91], v[152:155], v[124:127], v[88:91]\n\t"
    "v_mfma_f32_16x16x32_bf16 v[92:95], v[156:159], v[124:127], v[92:95]\n\t"
    "s_waitcnt lgkmcnt(4)\n\t"
    "v_mfma_f32_16x16x32_bf16 v[80:83], v[160:163], v[96:99], v[80:83]\n\t"
    "ds_read_b128 v[120:123], %[da] offset:12288\n\t"
    "v_mfma_f32_16x16x32_bf16 v[84:87], v[164:167], v[96:99], v[84:87]\n\t"
    "ds_read_b128 v[124:127], %[da] offset:14336\n\t"
    "v_mfma_f32_16x16x32_bf16 v[88:91], v[160:163], v[100:103], v[88:91]\n\t"
    "v_mfma_f32_16x16x32_bf16 v[92:95], v[164:167], v[100:103], v[92:95]\n\t"
    "s_barrier\n\t"
    "s_waitcnt lgkmcnt(4)\n\t"
    "v_mfma_f32_16x16x32_bf16 v[80:83], v[168:171], v[104:107], v[80:83]\n\t"
    "ds_read_b128 v[96:99], %[ea] offset:16384\n\t"
    "v_mfma_f32_16x16x32_bf16 v[84:87], v[172:175], v[104:107], v[84:87]\n\t"
    "ds_read_b128 v[100:103], %[ea] offset:18432\n\t"
    "v_mfma_f32_16x16x32_bf16 v[88:91], v[168:171], v[108:111], v[88:91]\n\t"
    "v_mfma_f32_16x16x32_bf16 v[92:95], v[172:175], v[108:111], v[92:95]\n\t"
    "s_waitcnt lgkmcnt(4)\n\t"
    "v_mfma_f32_16x16x32_bf16 v[80:83], v[176:179], v[112:115], v[80:83]\n\t"
    "ds_read_b128 v[104:107], %[da] offset:16384\n\t"
    "v_mfma_f32_16x16x32_bf16 v[84:87], v[180:183], v[112:115], v[84:87]\n\t"
    "ds_read_b128 v[108:111], %[da] offset:18432\n\t"
    "v_mfma_f32_16x16x32_bf16 v[88:91], v[176:179], v[116:119], v[88:91]\n\t"
    "v_mfma_f32_16x16x32_bf16 v[92:95], v[180:183], v[116:119], v[92:95]\n\t"
    "s_waitcnt lgkmcnt(4)\n\t"
    "v_mfma_f32_16x16x32_bf16 v[80:83], v[184:187], v[120:123], v[80:83]\n\t"
    "ds_read_b128 v[112:115], %[ea] offset:20480\n\t"
    "v_mfma_f32_16x16x32_bf16 v[84:87], v[188:191], v[120:123], v[84:87]\n\t"
    "ds_read_b128 v[116:119], %[ea] offset:22528\n\t"
    "v_mfma_f32_16x16x32_bf16 v[88:91], v[184:187], v[124:127], v[88:91]\n\t"
    "v_mfma_f32_16x16x32_bf16 v[92:95], v[188:191], v[124:127], v[92:95]\n\t"
    "s_waitcnt lgkmcnt(4)\n\t"
    "v_mfma_f32_16x16x32_bf16 v[80:83], v[192:195], v[96:99], v[80:83]\n\t"
    "ds_read_b128 v[120:123], %[da] offset:20480\n\t"
    "v_mfma_f32_16x16x32_bf16 v[84:87], v[196:199], v[96:99], v[84:87]\n\t"
    "ds_read_b128 v[124:127], %[da] offset:22528\n\t"
    "v_mfma_f32_16x16x32_bf16 v[88:91], v[192:195], v[100:103], v[88:91]\n\t"
    "v_mfma_f32_16x16x32_bf16 v[92:95], v[196:199], v[100:103], v[92:95]\n\t"
    "s_barrier\n\t"
    "s_and_b32 %[st], %[x], 3\n\t"
    "s_cbranch_scc1 3f\n\t"
    "4:\n\t"
    "s_waitcnt lgkmcnt(4)\n\t"
    "v_mfma_f32_16x16x32_bf16 v[80:83], v[200:203], v[104:107], v[80:83]\n\t"
    "ds_read_b128 v[96:99], %[ea] offset:24576\n\t"
    "v_mfma_f32_16x16x32_bf16 v[84:87], v[204:207], v[104:107], v[84:87]\n\t"
    "ds_read_b128 v[100:103], %[ea] offset:26624\n\t"
    "v_mfma_f32_16x16x32_bf16 v[88:91], v[200:203], v[108:111], v[88:91]\n\t"
    "v_mfma_f32_16x16x32_bf16 v[92:95], v[204:207], v[108:111], v[92:95]\n\t"
    "s_waitcnt lgkmcnt(4)\n\t"
    "v_mfma_f32_16x16x32_bf16 v[80:83], v[208:211], v[112:115], v[80:83]\n\t"
    "ds_read_b128 v[104:107], %[da] offset:24576\n\t"
    "v_mfma_f32_16x16x32_bf16 v[84:87], v[212:215], v[112:115], v[84:87]\n\t"
    "ds_read_b128 v[108:111], %[da] offset:26624\n\t"
    "v_mfma_f32_16x16x32_bf16 v[88:91], v[208:211], v[116:119], v[88:91]\n\t"
    "v_mfma_f32_16x16x32_bf16 v[92:95], v[212:215], v[116:119], v[92:95]\n\t"
    "s_waitcnt lgkmcnt(4)\n\t"
    "v_mfma_f32_16x16x32_bf16 v[80:83], v[216:219], v[120:123], v[80:83]\n\t"
    "ds_read_b128 v[112:115], %[ea] offset:28672\n\t"
    "v_mfma_f32_16x16x32_bf16 v[84:87], v[220:223], v[120:123], v[84:87]\n\t"
    "ds_read_b128 v[116:119], %[ea] offset:30720\n\t"
    "v_mfma_f32_16x16x32_bf16 v[88:91], v[216:219], v[124:127], v[88:91]\n\t"
    "v_mfma_f32_16x16x32_bf16 v[92:95], v[220:223], v[124:127], v[92:95]\n\t"
    "s_waitcnt lgkmcnt(4)\n\t"
    "v_mfma_f32_16x16x32_bf16 v[80:83], v[224:227], v[96:99], v[80:83]\n\t"
    "ds_read_b128 v[120:123], %[da] offset:28672\n\t"
    "v_mfma_f32_16x16x32_bf16 v[84:87], v[228:231], v[96:99], v[84:87]\n\t"
    "ds_read_b128 v[124:127], %[da] offset:30720\n\t"
    "v_mfma_f32_16x16x32_bf16 v[88:91], v[224:227], v[100:103], v[88:91]\n\t"
    "v_mfma_f32_16x16x32_bf16 v[92:95], v[228:231], v[100:103], v[92:95]\n\t"
    "s_waitcnt lgkmcnt(4)\n\t"
    "v_mfma_f32_16x16x32_bf16 v[80:83], v[232:235], v[104:107], v[80:83]\n\t"
    "v_mfma_f32_16x16x32_bf16 v[84:87], v[236:239], v[104:107], v[84:87]\n\t"
    "v_mfma_f32_16x16x32_bf16 v[88:91], v[232:235], v[108:111], v[88:91]\n\t"
    "v_mfma_f32_16x16x32_bf16 v[92:95], v[236:239], v[108:111], v[92:95]\n\t"
    "s_waitcnt lgkmcnt(2)\n\t"
    "v_mfma_f32_16x16x32_bf16 v[80:83], v[240:243], v[112:115], v[80:83]\n\t"
    "ds_read_b32 v39, %[fa]\n\t"
    "ds_read_b128 v[48:51], %[rw] offset:0\n\t"
    "v_mfma_f32_16x16x32_bf16 v[84:87], v[244:247], v[112:115], v[84:87]\n\t"
    "ds_read_b128 v[52:55], %[rw] offset:1024\n\t"
    "v_mfma_f32_16x16x32_bf16 v[88:91], v[240:243], v[116:119], v[88:91]\n\t"
    "ds_read_b128 v[56:59], %[rw] offset:2048\n\t"
    "v_mfma_f32_16x16x32_bf16 v[92:95], v[244:247], v[116:119], v[92:95]\n\t"
    "ds_read_b128 v[60:63], %[rw] offset:3072\n\t"
    "s_waitcnt lgkmcnt(5)\n\t"
    "v_mfma_f32_16x16x32_bf16 v[80:83], v[248:251], v[120:123], v[80:83]\n\t"
    "v_mfma_f32_16x16x32_bf16 v[84:87], v[252:255], v[120:123], v[84:87]\n\t"
    "v_mfma_f32_16x16x32_bf16 v[88:91], v[248:251], v[124:127], v[88:91]\n\t"
    "v_mfma_f32_16x16x32_bf16 v[92:95], v[252:255], v[124:127], v[92:95]\n\t"
    "s_mov_b32 %[st], 0\n\t"
    "21:\n\t"
    "s_waitcnt lgkmcnt(0)\n\t"
    "v_cmp_ne_u32 vcc, %[fv], v39\n\t"
    "s_cbranch_vccnz 20f\n\t"
    "v_mov_b32 v39, %[cn]\n\t"
    "s_nop 0\n\t"
    "v_cvt_pk_bf16_f32 v40, v80, v81\n\t"
    "v_cvt_pk_bf16_f32 v41, v82, v83\n\t"
    "v_cvt_pk_bf16_f32 v42, v84, v85\n\t"
    "v_cvt_pk_bf16_f32 v43, v86, v87\n\t"
    "global_store_dwordx4 %[co0], v[40:43], %[cb] sc1\n\t"
    "v_cvt_pk_bf16_f32 v44, v88, v89\n\t"
    "v_cvt_pk_bf16_f32 v45, v90, v91\n\t"
    "v_cvt_pk_bf16_f32 v46, v92, v93\n\t"
    "v_cvt_pk_bf16_f32 v47, v94, v95\n\t"
    "global_store_dwordx4 %[co1], v[44:47], %[cb] sc1\n\t"
    "v_cvt_pk_bf16_f32 v48, v48, v49\n\t"
    "v_cvt_pk_bf16_f32 v49, v50, v51\n\t"
    "v_cvt_pk_bf16_f32 v50, v52, v53\n\t"
    "v_cvt_pk_bf16_f32 v51, v54, v55\n\t"
    "global_store_dwordx4 %[co0], v[48:51], %[cb] offset:2048 sc1\n\t"
    "ds_read_b32 v38, v39 offset:4\n\t"
    "v_cvt_pk_bf16_f32 v56, v56, v57\n\t"
    "v_cvt_pk_bf16_f32 v57, v58, v59\n\t"
    "v_cvt_pk_bf16_f32 v58, v60, v61\n\t"
    "v_cvt_pk_bf16_f32 v59, v62, v63\n\t"
    "global_store_dwordx4 %[co1], v[56:59], %[cb] offset:2048 sc1\n\t"
    "s_memrealtime s[36:37]\n\t"
    "v_cmp_u_f32 vcc, v80, v88\n\t"
    "s_waitcnt lgkmcnt(0)\n\t"
    "s_or_b32 %[bad], vcc_lo, vcc_hi\n\t"
    "v_readfirstlane_b32 %[pk], v38\n\t"
    "s_sub_u32 %[tk], s36, %[tk]\n\t"
    "s_branch 9f\n\t"
    "20:\n\t"
    "ds_read_b32 v39, %[fa]\n\t"
    "ds_read_b128 v[48:51], %[rw] offset:0\n\t"
    "ds_read_b128 v[52:55], %[rw] offset:1024\n\t"
    "ds_read_b128 v[56:59], %[rw] offset:2048\n\t"
    "ds_read_b128 v[60:63], %[rw] offset:3072\n\t"
    "s_branch 21b\n\t"
    "13:\n\t"
    "s_memrealtime s[36:37]\n\t"
    "v_mov_b32 v79, 0\n\t"
    "s_waitcnt lgkmcnt(0)\n\t"
    "s_mov_b32 %[tk], s36\n\t"
    "s_add_u32 s36, s36, %[bl]\n\t"
    "s_and_b32 s36, s36, 0xffffff\n\t"
    "s_and_b32 s37, %[bl], 0xff000000\n\t"
    "s_or_b32 s36, s36, s37\n\t"
    "v_mov_b32 v78, s36\n\t"
    "s_mov_b64 exec, 1\n\t"
    "global_store_dword v79, v78, %[pg] offset:64 sc1\n\t"
    "s_mov_b64 exec, -1\n\t"
    "s_branch 14b\n\t"
    "3:\n\t"
    "7:\n\t"
    "s_and_b32 %[st], %[pv], 1\n\t"
    "s_mul_i32 %[st], %[st], 36\n\t"
    "s_add_u32 %[st], %[st], %[cn]\n\t"
    "v_mov_b32 v76, %[st]\n\t"
    "v_mov_b32 v77, 1\n\t"
    "v_mov_b32 v78, %[pv]\n\t"
    "v_mov_b32 v79, 0\n\t"
    "s_waitcnt vmcnt(0)\n\t"
    "s_mov_b64 exec, 1\n\t"
    "ds_add_rtn_u32 v77, v76, v77\n\t"
    "s_waitcnt lgkmcnt(0)\n\t"
    "v_readfirstlane_b32 %[st], v77\n\t"
    "s_and_b32 %[st], %[st], 3\n\t"
    "s_cmp_lg_u32 %[st], 3\n\t"
    "s_cbranch_scc1 6f\n\t"
    "global_store_dword v79, v78, %[pg] sc1\n\t"
    "6:\n\t"
    "s_mov_b64 exec, -1\n\t"
    "s_bitcmp1_b32 %[x], 2\n\t"
    "s_cbranch_scc1 8b\n\t"
    "s_branch 4b\n\t"
    "12:\n\t"
    "s_waitcnt lgkmcnt(0)\n\t"
    "9:"
    : [st] "=&s"(st), [tk] "+&s"(tk), [bad] "=&s"(bad), [pk] "=&s"(pk)
    : [ea] "v"(ea), [da] "v"(da), [x] "s"(x), "{v[128:159]}"(W[0]), "{v[160:191]}"(W[1]), "{v[192:223]}"(W[2]), "{v[224:255]}"(W[3]), [pv] "s"(pv), [cn] "s"(cn), [pg] "s"(pg), [bl] "s"(bl), [rw] "v"(rw), [fa] "v"(fa), [fv] "s"(fv), [cb] "s"(cb), [co0] "v"(co0), [co1] "v"(co1)
    : "v96", "v97", "v98", "v99", "v100", "v101", "v102", "v103", "v104", "v105", "v106", "v107", "v108", "v109", "v110", "v111", "v112", "v113", "v114", "v115", "v116", "v117", "v118", "v119", "v120", "v121", "v122", "v123", "v124", "v125", "v126", "v127", "v76", "v77", "v78", "v79", "v38", "v39", "v40", "v41", "v42", "v43", "v44", "v45", "v46", "v47", "v48", "v49", "v50", "v51", "v52", "v53", "v54", "v55", "v56", "v57", "v58", "v59", "v60", "v61", "v62", "v63", "v80", "v81", "v82", "v83", "v84", "v85", "v86", "v87", "v88", "v89", "v90", "v91", "v92", "v93", "v94", "v95", "vcc", "scc", "m0", "memory", "s36", "s37", "s38", "s39");
}
#define LEAN_QB 1
#define PREK 10
#define NEARLY 16
__device__ __forceinline__ void fin_ld(const u32x16& pd, unsigned rw, unsigned fa, unsigned fv) {
  asm volatile("ds_write_b128 %[rw], v[80:83] offset:0\n\t"
    "ds_write_b128 %[rw], v[84:87] offset:1024\n\t"
    "ds_write_b128 %[rw], v[88:91] offset:2048\n\t"
    "ds_write_b128 %[rw], v[92:95] offset:3072\n\t"
    "ds_write_b32 %[fa], %[fv]"
    :
    : [rw] "v"(rw), "{v[80:95]}"(pd), [fa] "v"(fa), [fv] "v"(fv)
    : "memory");
}
__device__ __forceinline__ void fin_st0(const u32x16& pd, unsigned rw, unsigned fa, unsigned fv, const char* cb, unsigned co0, unsigned co1, unsigned pa, unsigned& bad, unsigned& pk) {
  asm volatile("1:\n\t"
    "ds_read_b32 v39, %[fa]\n\t"
    "ds_read_b128 v[48:51], %[rw] offset:0\n\t"
    "ds_read_b128 v[52:55], %[rw] offset:1024\n\t"
    "ds_read_b128 v[56:59], %[rw] offset:2048\n\t"
    "ds_read_b128 v[60:63], %[rw] offset:3072\n\t"
    "s_waitcnt lgkmcnt(2)\n\t"
    "v_cmp_ne_u32 vcc, v39, %[fv]\n\t"
    "s_cbranch_vccz 2f\n\t"
    "s_waitcnt lgkmcnt(0)\n\t"
    "s_branch 1b\n\t"
    "2:\n\t"
    "v_pk_add_f32 v[48:49], v[48:49], v[80:81]\n\t"
    "v_pk_add_f32 v[50:51], v[50:51], v[82:83]\n\t"
    "v_pk_add_f32 v[52:53], v[52:53], v[84:85]\n\t"
    "v_pk_add_f32 v[54:55], v[54:55], v[86:87]\n\t"
    "v_cvt_pk_bf16_f32 v40, v48, v49\n\t"
    "v_cvt_pk_bf16_f32 v41, v50, v51\n\t"
    "v_cvt_pk_bf16_f32 v42, v52, v53\n\t"
    "v_cvt_pk_bf16_f32 v43, v54, v55\n\t"
    "global_store_dwordx4 %[co0], v[40:43], %[cb] sc1\n\t"
    "s_waitcnt lgkmcnt(0)\n\t"
    "v_mov_b32 v39, %[pa]\n\t"
    "ds_read_b32 v39, v39\n\t"
    "v_pk_add_f32 v[56:57], v[56:57], v[88:89]\n\t"
    "v_pk_add_f32 v[58:59], v[58:59], v[90:91]\n\t"
    "v_pk_add_f32 v[60:61], v[60:61], v[92:93]\n\t"
    "v_pk_add_f32 v[62:63], v[62:63], v[94:95]\n\t"
    "v_cvt_pk_bf16_f32 v44, v56, v57\n\t"
    "v_cvt_pk_bf16_f32 v45, v58, v59\n\t"
    "v_cvt_pk_bf16_f32 v46, v60, v61\n\t"
    "v_cvt_pk_bf16_f32 v47, v62, v63\n\t"
    "global_store_dwordx4 %[co1], v[44:47], %[cb] sc1\n\t"
    "v_cmp_u_f32 vcc, v48, v56\n\t"
    "s_waitcnt lgkmcnt(0)\n\t"
    "s_or_b32 %[bad], vcc_lo, vcc_hi\n\t"
    "v_readfirstlane_b32 %[pk], v39"
    : [bad] "=&s"(bad), [pk] "=&s"(pk)
    : [rw] "v"(rw), "{v[80:95]}"(pd), [fa] "v"(fa), [fv] "v"(fv), [cb] "s"(cb), [co0] "v"(co0), [co1] "v"(co1), [pa] "s"(pa)
    : "v39", "v40", "v41", "v42", "v43", "v44", "v45", "v46", "v47", "v48", "v49", "v50", "v51", "v52", "v53", "v54", "v55", "v56", "v57", "v58", "v59", "v60", "v61", "v62", "v63", "vcc", "scc", "memory");
}
__device__ __forceinline__ void fin_st1(const u32x16& pd, unsigned rw, unsigned fa, unsigned fv, const char* cb, unsigned co0, unsigned co1, unsigned pa, unsigned& bad, unsigned& pk) {
  asm volatile("1:\n\t"
    "ds_read_b32 v39, %[fa]\n\t"
    "ds_read_b128 v[48:51], %[rw] offset:0\n\t"
    "ds_read_b128 v[52:55], %[rw] offset:1024\n\t"
    "ds_read_b128 v[56:59], %[rw] offset:2048\n\t"
    "ds_read_b128 v[60:63], %[rw] offset:3072\n\t"
    "s_waitcnt lgkmcnt(2)\n\t"
    "v_cmp_ne_u32 vcc, v39, %[fv]\n\t"
    "s_cbranch_vccz 2f\n\t"
    "s_waitcnt lgkmcnt(0)\n\t"
    "s_branch 1b\n\t"
    "2:\n\t"
    "v_pk_add_f32 v[48:49], v[48:49], v[80:81]\n\t"
    "v_pk_add_f32 v[50:51], v[50:51], v[82:83]\n\t"
    "v_pk_add_f32 v[52:53], v[52:53], v[84:85]\n\t"
    "v_pk_add_f32 v[54:55], v[54:55], v[86:87]\n\t"
    "s_waitcnt lgkmcnt(0)\n\t"
    "v_mov_b32 v39, %[pa]\n\t"
    "ds_read_b32 v39, v39\n\t"
    "v_pk_add_f32 v[56:57], v[56:57], v[88:89]\n\t"
    "v_pk_add_f32 v[58:59], v[58:59], v[90:91]\n\t"
    "v_pk_add_f32 v[60:61], v[60:61], v[92:93]\n\t"
    "v_pk_add_f32 v[62:63], v[62:63], v[94:95]\n\t"
    "v_mul_f32 v40, %[nl], v48\n\t"
    "v_mul_f32 v41, %[nl], v49\n\t"
    "v_mul_f32 v42, %[nl], v50\n\t"
    "v_mul_f32 v43, %[nl], v51\n\t"
    "v_mul_f32 v44, %[nl], v56\n\t"
    "v_mul_f32 v45, %[nl], v57\n\t"
    "v_mul_f32 v46, %[nl], v58\n\t"
    "v_mul_f32 v47, %[nl], v59\n\t"
    "v_exp_f32 v40, v40\n\t"
    "v_exp_f32 v41, v41\n\t"
    "v_exp_f32 v42, v42\n\t"
    "v_exp_f32 v43, v43\n\t"
    "v_exp_f32 v44, v44\n\t"
    "v_exp_f32 v45, v45\n\t"
    "v_exp_f32 v46, v46\n\t"
    "v_exp_f32 v47, v47\n\t"
    "v_pk_add_f32 v[40:41], v[40:41], 1.0 op_sel_hi:[1,0]\n\t"
    "v_pk_add_f32 v[42:43], v[42:43], 1.0 op_sel_hi:[1,0]\n\t"
    "v_pk_add_f32 v[44:45], v[44:45], 1.0 op_sel_hi:[1,0]\n\t"
    "v_pk_add_f32 v[46:47], v[46:47], 1.0 op_sel_hi:[1,0]\n\t"
    "v_rcp_f32 v40, v40\n\t"
    "v_rcp_f32 v41, v41\n\t"
    "v_rcp_f32 v42, v42\n\t"
    "v_rcp_f32 v43, v43\n\t"
    "v_rcp_f32 v44, v44\n\t"
    "v_rcp_f32 v45, v45\n\t"
    "v_rcp_f32 v46, v46\n\t"
    "v_rcp_f32 v47, v47\n\t"
    "v_pk_mul_f32 v[40:41], v[48:49], v[40:41]\n\t"
    "v_pk_mul_f32 v[42:43], v[50:51], v[42:43]\n\t"
    "v_pk_mul_f32 v[44:45], v[56:57], v[44:45]\n\t"
    "v_pk_mul_f32 v[46:47], v[58:59], v[46:47]\n\t"
    "v_pk_mul_f32 v[40:41], v[40:41], v[52:53]\n\t"
    "v_pk_mul_f32 v[42:43], v[42:43], v[54:55]\n\t"
    "v_pk_mul_f32 v[44:45], v[44:45], v[60:61]\n\t"
    "v_pk_mul_f32 v[46:47], v[46:47], v[62:63]\n\t"
    "v_cvt_pk_bf16_f32 v48, v40, v41\n\t"
    "v_cvt_pk_bf16_f32 v49, v42, v43\n\t"
    "v_cvt_pk_bf16_f32 v50, v44, v45\n\t"
    "v_cvt_pk_bf16_f32 v51, v46, v47\n\t"
    "s_nop 1\n\t"
    "v_permlane16_swap_b32 v48, v50\n\t"
    "v_permlane16_swap_b32 v49, v51\n\t"
    "global_store_dwordx4 %[co0], v[48:51], %[cb] sc1\n\t"
    "v_cmp_u_f32 vcc, v52, v60\n\t"
    "s_waitcnt lgkmcnt(0)\n\t"
    "s_or_b32 %[bad], vcc_lo, vcc_hi\n\t"
    "v_readfirstlane_b32 %[pk], v39"
    : [bad] "=&s"(bad), [pk] "=&s"(pk)
    : [rw] "v"(rw), "{v[80:95]}"(pd), [fa] "v"(fa), [fv] "v"(fv), [cb] "s"(cb), [co0] "v"(co0), [nl] "s"(-LOG2E), [pa] "s"(pa)
    : "v39", "v40", "v41", "v42", "v43", "v44", "v45", "v46", "v47", "v48", "v49", "v50", "v51", "v52", "v53", "v54", "v55", "v56", "v57", "v58", "v59", "v60", "v61", "v62", "v63", "vcc", "scc", "memory");
}
__device__ __forceinline__ void fin_st2(const u32x16& pd, unsigned rw, unsigned fa, unsigned fv, const char* cb, unsigned co0, unsigned co1, unsigned pa, unsigned& bad, unsigned& pk) {
  asm volatile("s_nop 1\n\t"
    "v_cvt_pk_bf16_f32 v40, v80, v81\n\t"
    "v_cvt_pk_bf16_f32 v41, v82, v83\n\t"
    "v_cvt_pk_bf16_f32 v42, v84, v85\n\t"
    "v_cvt_pk_bf16_f32 v43, v86, v87\n\t"
    "global_store_dwordx4 %[co0], v[40:43], %[cb] sc1\n\t"
    "v_cvt_pk_bf16_f32 v44, v88, v89\n\t"
    "v_cvt_pk_bf16_f32 v45, v90, v91\n\t"
    "v_cvt_pk_bf16_f32 v46, v92, v93\n\t"
    "v_cvt_pk_bf16_f32 v47, v94, v95\n\t"
    "global_store_dwordx4 %[co1], v[44:47], %[cb] sc1\n\t"
    "1:\n\t"
    "ds_read_b32 v39, %[fa]\n\t"
    "ds_read_b128 v[48:51], %[rw] offset:0\n\t"
    "ds_read_b128 v[52:55], %[rw] offset:1024\n\t"
    "ds_read_b128 v[56:59], %[rw] offset:2048\n\t"
    "ds_read_b128 v[60:63], %[rw] offset:3072\n\t"
    "s_waitcnt lgkmcnt(2)\n\t"
    "v_cmp_ne_u32 vcc, v39, %[fv]\n\t"
    "s_cbranch_vccz 2f\n\t"
    "s_waitcnt lgkmcnt(0)\n\t"
    "s_branch 1b\n\t"
    "2:\n\t"
    "v_cvt_pk_bf16_f32 v48, v48, v49\n\t"
    "v_cvt_pk_bf16_f32 v49, v50, v51\n\t"
    "v_cvt_pk_bf16_f32 v50, v52, v53\n\t"
    "v_cvt_pk_bf16_f32 v51, v54, v55\n\t"
    "global_store_dwordx4 %[co0], v[48:51], %[cb] offset:2048 sc1\n\t"
    "s_waitcnt lgkmcnt(0)\n\t"
    "v_mov_b32 v39, %[pa]\n\t"
    "ds_read_b32 v39, v39\n\t"
    "v_cvt_pk_bf16_f32 v56, v56, v57\n\t"
    "v_cvt_pk_bf16_f32 v57, v58, v59\n\t"
    "v_cvt_pk_bf16_f32 v58, v60, v61\n\t"
    "v_cvt_pk_bf16_f32 v59, v62, v63\n\t"
    "global_store_dwordx4 %[co1], v[56:59], %[cb] offset:2048 sc1\n\t"
    "v_cmp_u_f32 vcc, v80, v88\n\t"
    "s_waitcnt lgkmcnt(0)\n\t"
    "s_or_b32 %[bad], vcc_lo, vcc_hi\n\t"
    "v_readfirstlane_b32 %[pk], v39"
    : [bad] "=&s"(bad), [pk] "=&s"(pk)
    : [rw] "v"(rw), "{v[80:95]}"(pd), [fa] "v"(fa), [fv] "v"(fv), [cb] "s"(cb), [co0] "v"(co0), [co1] "v"(co1), [pa] "s"(pa)
    : "v39", "v40", "v41", "v42", "v43", "v44", "v45", "v46", "v47", "v48", "v49", "v50", "v51", "v52", "v53", "v54", "v55", "v56", "v57", "v58", "v59", "v60", "v61", "v62", "v63", "vcc", "scc", "memory");
}
template <int R> __device__ __forceinline__ void gw_load(u32x32& w, unsigned a0, unsigned a1);
template <> __device__ __forceinline__ void gw_load<0>(u32x32& w, unsigned a0, unsigned a1) {
  asm volatile("ds_read_b64_tr_b16 v[128:129], %1 offset:0\n\t"
    "ds_read_b64_tr_b16 v[130:131], %1 offset:512\n\t"
    "ds_read_b64_tr_b16 v[132:133], %2 offset:0\n\t"
    "ds_read_b64_tr_b16 v[134:135], %2 offset:512\n\t"
    "ds_read_b64_tr_b16 v[136:137], %1 offset:4096\n\t"
    "ds_read_b64_tr_b16 v[138:139], %1 offset:4608\n\t"
    "ds_read_b64_tr_b16 v[140:141], %2 offset:4096\n\t"
    "ds_read_b64_tr_b16 v[142:143], %2 offset:4608\n\t"
    "s_waitcnt lgkmcnt(0)\n\t"
    "ds_read_b64_tr_b16 v[144:145], %1 offset:16384\n\t"
    "ds_read_b64_tr_b16 v[146:147], %1 offset:16896\n\t"
    "ds_read_b64_tr_b16 v[148:149], %2 offset:16384\n\t"
    "ds_read_b64_tr_b16 v[150:151], %2 offset:16896\n\t"
    "ds_read_b64_tr_b16 v[152:153], %1 offset:20480\n\t"
    "ds_read_b64_tr_b16 v[154:155], %1 offset:20992\n\t"
    "ds_read_b64_tr_b16 v[156:157], %2 offset:20480\n\t"
    "ds_read_b64_tr_b16 v[158:159], %2 offset:20992\n\t"
    "s_waitcnt lgkmcnt(0)"
    : "=&{v[128:159]}"(w) : "v"(a0), "v"(a1) : "memory");
}
template <> __device__ __forceinline__ void gw_load<1>(u32x32& w, unsigned a0, unsigned a1) {
  asm volatile("ds_read_b64_tr_b16 v[160:161], %1 offset:0\n\t"
    "ds_read_b64_tr_b16 v[162:163], %1 offset:512\n\t"
    "ds_read_b64_tr_b16 v[164:165], %2 offset:0\n\t"
    "ds_read_b64_tr_b16 v[166:167], %2 offset:512\n\t"
    "ds_read_b64_tr_b16 v[168:169], %1 offset:4096\n\t"
    "ds_read_b64_tr_b16 v[170:171], %1 offset:4608\n\t"
    "ds_read_b64_tr_b16 v[172:173], %2 offset:4096\n\t"
    "ds_read_b64_tr_b16 v[174:175], %2 offset:4608\n\t"
    "s_waitcnt lgkmcnt(0)\n\t"
    "ds_read_b64_tr_b16 v[176:177], %1 offset:16384\n\t"
    "ds_read_b64_tr_b16 v[178:179], %1 offset:16896\n\t"
    "ds_read_b64_tr_b16 v[180:181], %2 offset:16384\n\t"
    "ds_read_b64_tr_b16 v[182:183], %2 offset:16896\n\t"
    "ds_read_b64_tr_b16 v[184:185], %1 offset:20480\n\t"
    "ds_read_b64_tr_b16 v[186:187], %1 offset:20992\n\t"
    "ds_read_b64_tr_b16 v[188:189], %2 offset:20480\n\t"
    "ds_read_b64_tr_b16 v[190:191], %2 offset:20992\n\t"
    "s_waitcnt lgkmcnt(0)"
    : "=&{v[160:191]}"(w) : "v"(a0), "v"(a1) : "memory");
}
template <> __device__ __forceinline__ void gw_load<2>(u32x32& w, unsigned a0, unsigned a1) {
  asm volatile("ds_read_b64_tr_b16 v[192:193], %1 offset:0\n\t"
    "ds_read_b64_tr_b16 v[194:195], %1 offset:512\n\t"
    "ds_read_b64_tr_b16 v[196:197], %2 offset:0\n\t"
    "ds_read_b64_tr_b16 v[198:199], %2 offset:512\n\t"
    "ds_read_b64_tr_b16 v[200:201], %1 offset:4096\n\t"
    "ds_read_b64_tr_b16 v[202:203], %1 offset:4608\n\t"
    "ds_read_b64_tr_b16 v[204:205], %2 offset:4096\n\t"
    "ds_read_b64_tr_b16 v[206:207], %2 offset:4608\n\t"
    "s_waitcnt lgkmcnt(0)\n\t"
    "ds_read_b64_tr_b16 v[208:209], %1 offset:16384\n\t"
    "ds_read_b64_tr_b16 v[210:211], %1 offset:16896\n\t"
    "ds_read_b64_tr_b16 v[212:213], %2 offset:16384\n\t"
    "ds_read_b64_tr_b16 v[214:215], %2 offset:16896\n\t"
    "ds_read_b64_tr_b16 v[216:217], %1 offset:20480\n\t"
    "ds_read_b64_tr_b16 v[218:219], %1 offset:20992\n\t"
    "ds_read_b64_tr_b16 v[220:221], %2 offset:20480\n\t"
    "ds_read_b64_tr_b16 v[222:223], %2 offset:20992\n\t"
    "s_waitcnt lgkmcnt(0)"
    : "=&{v[192:223]}"(w) : "v"(a0), "v"(a1) : "memory");
}
template <> __device__ __forceinline__ void gw_load<3>(u32x32& w, unsigned a0, unsigned a1) {
  asm volatile("ds_read_b64_tr_b16 v[224:225], %1 offset:0\n\t"
    "ds_read_b64_tr_b16 v[226:227], %1 offset:512\n\t"
    "ds_read_b64_tr_b16 v[228:229], %2 offset:0\n\t"
    "ds_read_b64_tr_b16 v[230:231], %2 offset:512\n\t"
    "ds_read_b64_tr_b16 v[232:233], %1 offset:4096\n\t"
    "ds_read_b64_tr_b16 v[234:235], %1 offset:4608\n\t"
    "ds_read_b64_tr_b16 v[236:237], %2 offset:4096\n\t"
    "ds_read_b64_tr_b16 v[238:239], %2 offset:4608\n\t"
    "s_waitcnt lgkmcnt(0)\n\t"
    "ds_read_b64_tr_b16 v[240:241], %1 offset:16384\n\t"
    "ds_read_b64_tr_b16 v[242:243], %1 offset:16896\n\t"
    "ds_read_b64_tr_b16 v[244:245], %2 offset:16384\n\t"
    "ds_read_b64_tr_b16 v[246:247], %2 offset:16896\n\t"
    "ds_read_b64_tr_b16 v[248:249], %1 offset:20480\n\t"
    "ds_read_b64_tr_b16 v[250:251], %1 offset:20992\n\t"
    "ds_read_b64_tr_b16 v[252:253], %2 offset:20480\n\t"
    "ds_read_b64_tr_b16 v[254:255], %2 offset:20992\n\t"
    "s_waitcnt lgkmcnt(0)"
    : "=&{v[224:255]}"(w) : "v"(a0), "v"(a1) : "memory");
}
template <int I, int E, class F> __device__ __forceinline__ void sfor(F&& f) { if constexpr (I < E) { f(std::integral_constant<int, I>{}); sfor<I + 1, E>(f); } }
#define W2C 0
#define FL63 0
template <int ROLE, int KQ, class F>
__device__ __forceinline__ void wload(char* smem, int wave_id_, const u16* W0, const u16* W1, int ldb, u32x32 (&bw)[4], int rot, int mode, F&& hook) {
  constexpr int ABUF = 65536;
  const int tid = opaque_tid(), lane = tid & 63, wave = WAVE_ID;
  const int kq = KQ == 2 ? wave >> 2 : 0, nq = KQ == 2 ? wave & 3 : wave;
  const int g = lane >> 4, r16 = lane & 15, q4 = r16 >> 2, p4 = lane & 3;
  const unsigned ldsb = __builtin_amdgcn_readfirstlane((unsigned)(size_t)(LAS char*)smem);
  {
    const int srow = 8 * wave + (lane >> 3), ch = (lane & 7) ^ bswz(srow);
    const unsigned voff = (unsigned)(srow * ldb + ch * 8) * 2;
    const char* w0 = uni((const char*)W0); const char* w1 = uni((const char*)(ROLE == R_F1 ? W1 : W0 + 64));
    const size_t kb = (size_t)64 * ldb * 2;
    auto wstage = [&](int r) {
      const unsigned ld = ldsb + (r & 1) * ABUF + wave * 1024;
#pragma unroll
      for (int i = 0; i < 8; ++i) {
        const int kbi = KQ == 2 ? (ROLE == R_PART2 ? 0 : (i >> 2) * 8) + ((2 * r + ((i >> 1) & 1)) ^ rot) : 2 * r + (i >> 2);
        const char* src = KQ == 2 ? ((i & 1) ? w1 : w0) + kbi * kb + (ROLE == R_PART2 ? (i >> 2) * 256 : 0) : w0 + kbi * kb + (i & 3) * 128;
        unsigned keep;
        if (W2C && (ROLE == R_PART || ROLE == R_PART2)) glds16s<0>(src, voff, ld + i * 8192);
        else
        asm volatile("s_mov_b32 %0, m0\n\ts_mov_b32 m0, %3\n\ts_nop 2\n\tglobal_load_lds_dwordx4 %1, %2" WNT_ "\n\ts_mov_b32 m0, %0"
                     : "=&s"(keep) : "v"(voff), "s"(src), "s"(ld + i * 8192) : "memory");
      }
    };
    if (mode == 1) { wstage(0); wstage(1); return; }
    const int brow = 8 * g + q4, bs = bswz(brow);
    unsigned la[2];
#pragma unroll
    for (int j = 0; j < 2; ++j) {
      int blk, c16, sub;
      if (ROLE == R_F1) { blk = j; c16 = nq * 2 + (p4 >> 1); sub = (p4 & 1) * 8; }
      else { blk = nq >> 1; c16 = (nq & 1) * 4 + p4; sub = j * 8; }
      int set = kq;
      if (ROLE == R_PART2 && TLP) { set = nq >> 1; blk = nq & 1; c16 = kq * 4 + p4; }
      la[j] = (unsigned)((KQ == 2 ? set * 4 + blk : blk) * 8192 + brow * 128 + ((c16 ^ bs) << 4) + sub);
    }
    hook(4);
    if (mode != 2) { wstage(0); wstage(1); }
    sfor<0, 4>([&](auto r_c) __attribute__((always_inline)) {
      constexpr int r = decltype(r_c)::value;
      if (r < 3) WAIT_V(8); else WAIT_V(0);
      __builtin_amdgcn_s_barrier(); asm volatile("" ::: "memory");
      gw_load<r>(bw[r], ldsb + (r & 1) * ABUF + la[0], ldsb + (r & 1) * ABUF + la[1]);
      BAR();
      hook(r);
      if (r + 2 < 4) wstage(r + 2);
    });
  }
}
#define DSR2(d0, d1, a0_, a1_, off) asm volatile("ds_read_b128 %0, %2 offset:%4\n\tds_read_b128 %1, %3 offset:%4" : "=&v"(d0), "=&v"(d1) : "v"(a0_), "v"(a1_), "i"(off))
template <int N> __device__ __forceinline__ void waitl(u32x4& a, u32x4& b) { asm volatile("s_waitcnt lgkmcnt(%2)" : "+v"(a), "+v"(b) : "i"(N)); }
__device__ __forceinline__ void glds1(const char* sbase, unsigned voff, unsigned lds) {
  unsigned keep;
  asm volatile("s_mov_b32 %0, m0\n\ts_mov_b32 m0, %3\n\ts_nop 2\n\tglobal_load_lds_dwordx4 %1, %2 sc1\n\ts_mov_b32 m0, %0"
               : "=&s"(keep) : "v"(voff), "s"(sbase), "s"(lds) : "memory");
}
#define XS(i) do { } while (0)
#define XFL(b_) do { } while (0)
#define XA_ 0
#define XF_ 0
#define XE_ 0
#define XP_ 0
#define PUBK 9
#define LATE 0
#define NOWP 0
#define TB 48
#define TDM 1
#define TDA 15
#define TPUB 1
#define DUR0 160
#define PUP 4
#define PDQ 5
#define PDM 2
#define PDEEP 5
template <int ROLE>
__device__ __forceinline__ void gemm7(char* smem, int wave_id_, const u16* W0, const u16* W1, int ldb, const char* A_, size_t a_blk, Dep dep,
                                      char* C_, size_t c_blk, unsigned*& cnt, int myw, unsigned* tmo, int slot, unsigned long long* mts,
                                      bool gl, int nimm = 0, int nseg = NSEG, int wm0 = 0, int pofs = 0, u64 tag = 0, int b0 = 0) {
  dep.base = (const char*)cnt;
  unsigned* myprog = cnt + myw;
  volatile LAS unsigned* wsl = (volatile LAS unsigned*)(smem + L_SLOT + 512);
  if (opaque_tid() < 144) *(volatile LAS unsigned*)(smem + L_SLOT + opaque_tid() * 4) = 0u;
  const int tid = opaque_tid(), lane = tid & 63, wave = WAVE_ID;
  const int kq = wave >> 2, nq = wave & 3;
  const unsigned ldsb = __builtin_amdgcn_readfirstlane((unsigned)(size_t)(LAS char*)smem);
  constexpr int rot = 0;
  const char* A = uni(A_) + nq * 1024;
  const char* Cu = uni(C_);
  constexpr int PD = PDM;
  constexpr bool P2C = ROLE == R_PART2;
  int pw = 0;
  int wm = wm0, nh = 2 * b0;
  constexpr bool SPEC = ROLE != R_QKV;
  const int sbi = gl ? SC : SB, sbo = ROLE == R_F1 || gl ? SC : SB;
  auto lgo = [&](int b, unsigned& v, unsigned& u, bool pre, bool force) {
    if (wave == 4) {
      for (int spins = 0; __builtin_amdgcn_readfirstlane(wsl[2]) != (unsigned)(b + 1); ++spins) {
        if (!pre) ld_fb(v, u, dep.base, dep.voff);
        const bool ok = (int)(v * dep.mul + dep.add) > b;
        if (__all(ok)) break;
        if (SPEC && !force && bells(ok, u, b, b0 ? MARH : MARG, sbi)) break;
        if (!pre) { if (gl) __builtin_amdgcn_s_sleep(GSL); else __builtin_amdgcn_s_sleep(XSL); }
        pre = false;
        if (give_up(spins, tmo, 9)) { v = 255u; break; }
      }
      wsl[2] = (unsigned)(b + 1);
    } else {
      while (__builtin_amdgcn_readfirstlane(wsl[2]) != (unsigned)(b + 1)) __builtin_amdgcn_s_sleep(1);
    }
  };
  auto lwm = [&](int b, unsigned v) {
    unsigned w;
    if (wave == 4) {
      const int w2 = (int)dep_eval(v, dep);
      w = (unsigned)min(max(max(w2, wm), b + 1), 255);
      wsl[0] = ((unsigned)(b + 1) << 8) | w;
    } else {
      for (;;) {
        w = __builtin_amdgcn_readfirstlane(wsl[0]);
        if ((w >> 8) == (unsigned)(b + 1)) break;
        __builtin_amdgcn_s_sleep(1);
      }
      w &= 255u;
    }
    return (int)w;
  };
  auto pubw = [&](int n) {
    WAIT_V(0);
    if (lane == 0) {
      const unsigned o = __hip_atomic_fetch_add((LAS unsigned*)(smem + L_SLOT + 516 + ((n + pofs) & 1) * 36), 1u, __ATOMIC_RELAXED, __HIP_MEMORY_SCOPE_WORKGROUP);
      if ((o & 3u) == 3u) {
        st4_sc1(myprog, (unsigned)(n + pofs));
      }
    }
    asm volatile("s_waitcnt lgkmcnt(0)" ::: "memory");
  };
  auto hsrc = [&](int hh) { return A + (size_t)(hh >> 1) * a_blk + ((hh ^ (rot >> 2)) & 1) * 16384; };
  auto hlds = [&](int hh) { return ldsb + (unsigned)(((hh >> 1) & 1) * 65536 + (hh & 1) * 16384 + nq * 1024); };
#define POFF(i_) (((i_) & 1) * 32768 + ((i_) >> 1) * 4096)
#define PIECE(s_, l_, i_) asm volatile("s_add_u32 m0, %2, %3\n\ts_nop 0\n\tglobal_load_lds_dwordx4 %0, %1 sc1" :: "v"(av[i_]), "s"(s_), "s"(l_), "i"(POFF(i_)) : "memory", "scc", "m0")
  int na = max(sbo, b0);
  auto arm = [&](int bb) {
    unsigned z = SENT; asm volatile("" : "+v"(z));
    constexpr int N = ROLE == R_F1 ? 1 : (P2C ? 4 : 2);
    const unsigned o = nq * N * 1024 + lane_id() * 16;
    const char* p = Cu + (size_t)bb * c_blk;
#pragma unroll
    for (int i = 0; i < N; ++i) st16s(p, o + i * 1024, u32x4{z, z, z, z});
  };
  u32x32 BW[4];
  wload<ROLE, 2>(smem, wave_id_, W0, W1, ldb, BW, rot, SPRO && ROLE == R_QKV && nimm > 0 ? 2 : 0, [&](int r) {
    if (ROLE == R_QKV && nimm > 0 && wave == 0) STS((char*)tmo - OFF_TMO, 128 + slot, 3 + r);
    if (SPEC && r == 2 && (ROLE != R_PART || gl)) { for (na += kq; na < nseg; na += 2) arm(na); na = nseg + 1; }
    if (ROLE == R_QKV && nimm > 0 && r == 1 && wave == 4) init_issue((char*)tmo - OFF_TMO, ldsb + L_RED);
    if (ROLE == R_QKV && nimm > 0 && r == 2) {
      cnt = cnt_of((char*)tmo - OFF_TMO, init_get((char*)tmo - OFF_TMO, tag, smem + L_RED));
      dep.base = (const char*)cnt; myprog = cnt + myw;
      if (kq) {
        const unsigned l16 = lane_id() * 16;
#pragma unroll
        for (int i = 0; i < 16; ++i) glds1(hsrc(i >> 3) + (i & 1) * 32768 + ((((i & 7) >> 1) ^ rot) & 3) * 4096, l16, hlds(i >> 3) + POFF(i & 7));
      }
      nh = 2; wm = 8;
    }
  });
  if (ROLE == R_QKV && nimm > 0 && opaque_tid() == 0) STS((char*)tmo - OFF_TMO, 128 + slot, 0);
  if (wave == 0) WFB();
  auto body = [&](auto ldr_c) __attribute__((always_inline)) {
    constexpr bool LDR = decltype(ldr_c)::value;
    if (XP_ == 0 ? !LDR : (XP_ == 1 ? LDR : false)) __builtin_amdgcn_s_setprio(1);
    unsigned av[8];
#pragma unroll
    for (int i = 0; i < 8; ++i) av[i] = lane * 16 + (P2C ? 0 : (i & 1) * 32768) + (((i >> 1) ^ rot) & 3) * 4096;
    auto issue_all = [&](int hh) {
      if (LDR) {
        const char* s_ = hsrc(hh); const unsigned l_ = hlds(hh);
#pragma unroll
        for (int i = 0; i < 8; ++i) if (!(P2C && (i & 1))) PIECE(s_, l_, i);
      }
    };
    int b = b0;
    bool pre = false;
    unsigned smu = 0, tk = 0;
    unsigned smp = 0, got = 0;
    while (b < nseg) {
      const bool fast = (wm < nseg ? wm : nseg) >= b + PD && b >= nimm && nh <= 2 * b + 2;
      if (SPEC && b >= TB && !fast) break;
      const bool pl = wm <= b || pre, cold = nh < 2 * b + 2, nh3 = nh == 2 * b + 3;
      if (!fast) {
        XS(0);
        if (LDR && wm <= b && !got) lgo(b, smp, smu, false, true);
        XS(1);
        if (got) nh += NEARLY / 8;
        { const int upto = nh3 ? 2 * b + 4 : 2 * b + 2; for (; nh < upto; ++nh) issue_all(nh); }
        if (LDR && pl) wm = lwm(b, smp);
        XS(2);
      }
      const int ln = lane_id(), g = ln >> 4, r16 = ln & 15;
      const unsigned e0 = ldsb + (unsigned)((P2C ? 0 : kq * 32768) + (b & 1) * 65536 + (gl ? (g >> 1) * 512 + r16 * 32 + (g & 1) * 16 : r16 * 128 + ((g ^ (r16 & 7)) << 4)));
      const unsigned d0 = e0 ^ (gl ? 1024u : 64u);
      const unsigned rw = ldsb + L_RED + nq * 4096 + ln * 16;
      const unsigned co0 = ROLE == R_F1 ? (unsigned)((g & 1) * 2048 + nq * 512 + r16 * 32 + (g >> 1) * 16)
                         : ((ROLE == R_QKV ? TLQ : TLP) ? (unsigned)(nq * (P2C ? 4096 : 2048) + r16 * 64 + g * 16)
                         : (ROLE == R_QKV ? (unsigned)(r16 * 256 + (((4 * nq + g) ^ r16) << 4)) : (unsigned)(r16 * PROW + (32 * nq + 8 * g) * 2)));
      const unsigned co1 = co0 + ((ROLE == R_QKV ? TLQ : TLP) ? 1024u : (ROLE == R_QKV ? 4096u : 16u * PROW));
      u32x16 pv_;
      bool pr2 = false;

      if (fast) {
        __builtin_amdgcn_s_barrier(); asm volatile("" ::: "memory");
        const int LA = (wm < nseg ? wm : nseg) >= b + (ROLE == R_QKV ? PDQ : PDEEP) ? 3 : 2;
        for (; nh < 2 * b + LA; ++nh) issue_all(nh);
        int lf = (wm < nseg ? wm : nseg) - (LA - 1), cp = nseg - (LA - 1);
        const unsigned sa = ldsb + L_SLOT + ln * 4, ad = dep.add - (unsigned)(LA - 1);
        const unsigned up = LA == 3 ? 1000u : (unsigned)PUP;
#define FARGS BW, pv_, e0, d0, b, lf, cp, up, sa, dep.mul, ad, rw, (unsigned)a_blk, hsrc(2 * b + LA), hsrc(2 * b + LA + 1), hlds(2 * b + LA), hlds(2 * b + LA + 1), av
        if constexpr (LDR) (P2C ? fast_ld2 : (ROLE == R_F1 ? fast_ld1 : fast_ld0))(FARGS, LA);
#undef FARGS
        else {
          const char* c0_ = uni((const char*)tmo - OFF_TMO + OFF_DUMP); const char* c1_ = Cu + (size_t)b * c_blk;
          const int sf = b + pofs;
          const char* pg = uni((const char*)myprog);
          (ROLE == R_F1 ? fast_st1 : (P2C ? fast_st2 : fast_st0))(BW, pv_, e0, d0, b, lf, cp, up, sa, dep.mul, ad, rw, (unsigned)c_blk, c0_, c1_, co0, co1, pofs, sf, (unsigned)wave,
                                                                   ldsb + L_SLOT, dep.voff, dep.base, pg);
        }
        wm = lf + LA - 1 > wm ? lf + LA - 1 : wm;
        nh = 2 * b + 2;
        --b;
        __builtin_amdgcn_s_barrier(); asm volatile("" ::: "memory");
      } else {
        if (LDR) { if (nh >= 2 * b + 4) { if (P2C) WAIT_V(14); else WAIT_V(28); } else { if (P2C) WAIT_V(6); else WAIT_V(12); } }
        BAR();
        XS(3);
        if (ROLE == R_QKV && nimm > 0 && b < 2 && opaque_tid() == 0) STS((char*)tmo - OFF_TMO, 128 + slot, 1 + b);
        if constexpr (!LDR) {
          const unsigned wv = wsl[0];
          unsigned st;
          lean_st0(BW, e0, d0, __builtin_amdgcn_readfirstlane(pw > 0 ? (cold ? 4u : 1u) : 0u), pv_, (unsigned)(pw + pofs), ldsb + L_SLOT + 516, uni((const char*)myprog), 0u, tk, st);
          if (pl) wm = (int)(__builtin_amdgcn_readfirstlane(wv) & 255u);
        }
        const bool dm = nh == 2 * b + 2 && wm > b + 1 && b + 1 < nseg;
        pr2 = b + 1 < nseg && wm < nseg;
        if constexpr (LDR) {
          const bool er = nh == 2 * b + 2 && !dm;
          const unsigned ps = pr2 && (wave == 4 || er) ? (unsigned)(b + 1) : 0u;
#define LARGS BW, e0, d0, (unsigned)(nh - 2 * b - 2), hlds(nh), hsrc(nh), hsrc(nh + 1), av, ps, dep.voff, dep.base, \
              rw, ldsb + L_SLOT + 528 + nq * 4, (unsigned)(b + 1), ldsb + L_SLOT + 520, dep.mul, dep.add, er ? b + 1 : 0x7FFFFFFF, 0x7FFFFFFF, smp, smu, got
          if (dm) (P2C ? lean_dm1 : lean_dm0)(LARGS); else (P2C ? lean_ld1 : lean_ld0)(LARGS);
#undef LARGS
          got = __builtin_amdgcn_readfirstlane(got);
        }
        if (dm) nh += 2;
        XS(5);
      }
      unsigned bad = 0, pk = 0;
      if constexpr (LDR) { if (fast) fin_ld(pv_, rw, ldsb + L_SLOT + 528 + nq * 4, (unsigned)(b + 1)); }
      else (ROLE == R_F1 ? fin_st1 : (P2C ? fin_st2 : fin_st0))(pv_, rw, ldsb + L_SLOT + 528 + nq * 4, (unsigned)(b + 1), Cu + (size_t)b * c_blk, co0, co1, ldsb + L_SLOT + 524, bad, pk);
      XS(6);
      pre = pr2;
      bool now = (nh < 2 * b + 4 && wm < b + 1 + PD) || b + 1 >= nseg || b < nimm;
      if (!LDR && now) {
        if (NOWP == 0 && b >= nimm && pk == (unsigned)(b + 2)) now = false;
        else pubw(b + 1);
      }
      XS(7);
      XFL(b);
      pw = now ? 0 : b + 1;
      if (SPEC && !LDR && !fast && b >= 16 && na < nseg && na > b + 1) { arm(na); arm(na + 1); na += 2; }
      ++b;
    }
    if (SPEC && b < nseg) {
      static_assert(!SPEC || (NEARLY == 16 && TLP), "");
      const int ln = lane_id(), g = ln >> 4, r16 = ln & 15;
      const unsigned e0 = ldsb + (unsigned)((P2C ? 0 : kq * 32768) + (gl ? (g >> 1) * 512 + r16 * 32 + (g & 1) * 16 : r16 * 128 + ((g ^ (r16 & 7)) << 4)));
      const unsigned rw = ldsb + L_RED + nq * 4096 + ln * 16;
      const unsigned co0 = ROLE == R_F1 ? (unsigned)((g & 1) * 2048 + nq * 512 + r16 * 32 + (g >> 1) * 16) : (unsigned)(nq * (P2C ? 4096 : 2048) + r16 * 64 + g * 16);
      const char* pg = uni((const char*)myprog);
      unsigned fa = ldsb + L_SLOT + 528 + nq * 4; asm volatile("" : "+v"(fa));
      bool force = false, dmx = false;
      unsigned dur = DUR0, dp = 0, x8 = __builtin_amdgcn_readfirstlane(wave == 0 ? 8u : 0u);
      if (nh >= 2 * b + 2) got = 1;
      pre = false;
      for (;;) {
        unsigned rd;
        XS(0);
        if (LDR && b < nseg) {
          if (!got) { lgo(b, smp, smu, pre, force); issue_all(2 * b); issue_all(2 * b + 1); }
          XS(2);
          if (P2C) WAIT_V(6); else WAIT_V(12);
        }
        if (!LDR && b >= sbo && na <= nseg) { for (na = max(na, b); na < nseg; ++na) arm(na); WAIT_V(0); ++na; }
        const unsigned ea = e0 + (b & 1) * 65536;
        BAR();
        XS(3);
        if (b >= nseg) rd = __builtin_amdgcn_readfirstlane(wsl[8]);
        else if constexpr (LDR) {
          const int th = b + 1 < nseg ? b + 1 : 0;
#define LARGS BW, ea, ea ^ (gl ? 1024u : 64u), 0u, hlds(2 * b + 2), hsrc(2 * b + 2), hsrc(2 * b + 3), av, (unsigned)th, dep.voff, dep.base, \
              rw, ldsb + L_SLOT + 528 + nq * 4, (unsigned)(b + 1), ldsb + L_SLOT + 520, dep.mul, dep.add, th, th >= sbi ? th : 0x7FFFFFFF, smp, smu, got
          if (dmx) {
            *(volatile LAS u32x2*)(wsl + 2) = u32x2{(unsigned)(b + 2), (unsigned)(b + 2)};
            (P2C ? lean_dm1 : lean_dm0)(LARGS);
          } else (P2C ? lean_ld1 : lean_ld0)(LARGS);
#undef LARGS
          got = __builtin_amdgcn_readfirstlane(got) | (dmx ? 1u : 0u);
          rd = got >> 1;
          dmx = TDM && got == 1u && b + 2 < nseg
             && __all((int)(smp * dep.mul + dep.add) > b + 2 || (b + 2 >= sbi && (int)(smu >> 24) + ((int)((rt_now() + (dmx ? TDA : 0) - MARG - smu) << 8) >> 31) > b + 2));
          pre = true;
        } else {
          unsigned bad, pk;
          (ROLE == R_F1 ? lean_ep1 : (P2C ? lean_ep2 : lean_ep0))(BW, ea, ea ^ (gl ? 1024u : 64u), (unsigned)min(pw, 1) | (b >= sbo - 2 ? x8 : 0u), (unsigned)pw, ldsb + L_SLOT + 516, pg,
              (unsigned)(b + 1) << 24 | dur, tk, rd, rw, fa, (unsigned)(b + 1), Cu + (size_t)b * c_blk, co0, co0 + 1024u, bad, pk);
          XS(6);
          if (!rd) {
            pw = b + 1;
            { const unsigned d = min(tk, 300u); dur = max(d, dp); dp = d; }
            if (bad && !force && b >= sbi) wsl[8] = 1u;
            else if ((ROLE == R_PART && !gl && b == sbo - 1) || pk != (unsigned)(b + 2)) {
              pubw(b + 1);
              pw = 0;
            }
            if (na < nseg && na > b + 1) { arm(na); arm(na + 1); na += 2; }
          }
        }
        XS(7);
        if (rd) {
          WAIT_V(0);
          BAR();
          if (wave == 4 && ln < 11) wsl[ln] = 0u;
          BAR();
          --b; pw = 0; pre = false; got = 0; force = true;
          continue;
        }
        if (b >= nseg) break;
        force = false;
        ++b;
      }
    }
    __builtin_amdgcn_s_setprio(0);
  };
  if (kq) body(std::true_type{}); else body(std::false_type{});
#undef PIECE
#undef POFF
  WAIT_V(0);
  BAR();
}
__device__ __forceinline__ void fin_role(char* smem, int wave_id_, int fidx, const float* mod, const char* P2, const u16* x1, u16* out, unsigned* cnt, unsigned* tmo,
                                         unsigned long long* mts) {
  const int tid = opaque_tid(), lane = tid & 63, wave = WAVE_ID;
  row_table(smem + L_GB, mod, cnt, tmo, tid);
  volatile LAS unsigned* wsl = (volatile LAS unsigned*)(smem + L_SLOT + 512);
  int wf = 0, par = 0;
  const unsigned pf2 = (unsigned)(PF2 + ((lane & 31) < 28 ? (lane & 31) : 0)) * 128, pln = (unsigned)((PLN + (lane >> 3)) * 128 + (lane & 7) * 4);
  for (int nf = fidx >> 3; nf < NSEG; nf += !FL63 || nf < NSEG - 3 ? 2 : (nf == NSEG - 2 ? 1 : 3)) {
    if (wf <= nf) {
      if (wave == 0) {
        unsigned w;
        for (int spins = 0;; ++spins) {
          unsigned v, u;
          ld_fb(v, u, cnt, pf2 + (nf >= HC ? 4 : (nf >= HB ? 8 : 0)));
          if (!__all(v > (unsigned)nf) && bells(v > (unsigned)nf, u, nf, nf >= HB ? MARX : MARF, SC)) { w = 0; break; }
          v = rowmin16(v);
          w = min(rdl(v, 0), rdl(v, 16));
          if ((int)w > nf) break;
          __builtin_amdgcn_s_sleep(FSL);
          if (give_up(spins, tmo, 9)) { w = NSEG; break; }
        }
        if (lane == 0) wsl[par] = w;
      }
      BAR();
      wf = (int)__builtin_amdgcn_readfirstlane(wsl[par]);
      par ^= 1;
    }
    int ln = lane; asm volatile("" : "+v"(ln));
    if (wave == 0) WTS(256 + 8 + fidx, nf / 2, 0);
    bool fin = nf < SB;
    for (int spins = 0; !fin_half(fin, P2, x1, smem + L_GB + 12288, out, nf * 32 + 4 * (fidx & 7) + (wave >> 1), wave & 1, ln); ++spins)
      if (spins > 1) fin = __all(ld4_sc1((const char*)cnt + pf2 + (nf >= HC ? 4 : (nf >= HB ? 8 : 0))) > (unsigned)nf && ld4_sc1((const char*)cnt + pln) > (unsigned)nf) || give_up(spins, tmo, 9);
    if (wave == 0) WTS(256 + 8 + fidx, nf / 2, 2);
  }
}
#define LD8(r, vo, sb) asm volatile("global_load_dwordx4 %0, %8, %12 sc1\n\tglobal_load_dwordx4 %1, %9, %12 sc1\n\t" \
    "global_load_dwordx4 %2, %10, %12 sc1\n\tglobal_load_dwordx4 %3, %11, %12 sc1\n\t" \
    "global_load_dwordx4 %4, %8, %13 sc1\n\tglobal_load_dwordx4 %5, %9, %13 sc1\n\t" \
    "global_load_dwordx4 %6, %10, %13 sc1\n\tglobal_load_dwordx4 %7, %11, %13 sc1" \
    : "=&v"(r[0][0]), "=&v"(r[0][1]), "=&v"(r[0][2]), "=&v"(r[0][3]), "=&v"(r[1][0]), "=&v"(r[1][1]), "=&v"(r[1][2]), "=&v"(r[1][3]) \
    : "v"(vo), "v"(vo ^ (TLQ ? 2048u : 64u)), "v"(vo ^ (TLQ ? 4096u : 128u)), "v"(vo ^ (TLQ ? 6144u : 192u)), "s"(sb), "s"((sb) + (TLQ ? 1024 : 4096)) : "memory")
#define TIE8(r) "+v"(r[0][0]), "+v"(r[0][1]), "+v"(r[0][2]), "+v"(r[0][3]), "+v"(r[1][0]), "+v"(r[1][1]), "+v"(r[1][2]), "+v"(r[1][3])
#define SADL 255
#define SAE 110
#define DSA 20
#define SASL 8
__device__ __forceinline__ void sa_role(char* smem, int wave_id_, int hbase, const char* __restrict__ Q, const char* __restrict__ Kx, const char* __restrict__ V,
                                        const char* kvm, char* __restrict__ O, unsigned* cnt, unsigned* tmo, int slot, unsigned long long* mts) {
  constexpr float LS = LOG2E * SCALE;
  const int tid = opaque_tid(), wave = WAVE_ID;
  const int h = hbase + (wave & 1), s0 = wave >> 1;
  char* vl = smem + wave * 17408;
  const unsigned ldsw = __builtin_amdgcn_readfirstlane((unsigned)(size_t)(LAS char*)vl);
  unsigned avail = 0;
  {
    static_assert(SB % 4 == 0, "");
    unsigned z = SENT; asm volatile("" : "+v"(z));
    for (int s = SB + s0; s < NSEG; s += 4)
#pragma unroll
      for (int i = 0; i < 8; ++i) st16s(uni(O + ((size_t)s * 64 + 2 * h) * 4096), (unsigned)(i * 1024 + (tid & 63) * 16), u32x4{z, z, z, z});
  }
  for (int s = s0, it = 1; s < NSEG; s += 4, ++it) {
    u32x4 qv[2][4], kv[2][4];
    int lane = tid & 63; asm volatile("" : "+v"(lane));
    const int g = lane >> 4, r16 = lane & 15, q4 = r16 >> 2, p4 = lane & 3;
    auto request = [&](int sr) {
      const size_t o = ((size_t)sr * NH + h) * 8192;
      const char* qs = uni(Q + o); const char* ks = uni(Kx + o); const char* vs = uni(V + o);
      const unsigned fo = TLQ ? (unsigned)(r16 * 64 + g * 16) : (unsigned)(r16 * 256 + ((g ^ r16) << 4));
      LD8(qv, fo, qs); LD8(kv, fo, ks);
      head_to_lds(vs, lane, ldsw);
    };
    {
      for (int spins = 0; avail <= (unsigned)s; ++spins) {
        const unsigned p = ld4_sc1(cnt + (PQKV + h) * 32 + (lane & 15));
        avail = min(min(qkv_avail(p, 0), qkv_avail(p, 1)), qkv_avail(p, 2));
        if (avail > (unsigned)s) break;
        __builtin_amdgcn_s_sleep(32);
        if (give_up(spins, tmo, 10)) break;
      }
      request(s);
      asm volatile("s_waitcnt vmcnt(0)" : TIE8(qv), TIE8(kv) :: "memory");
    }
    WTM(slot, s, 0);
    f32x4 s00 = f32x4{0, 0, 0, 0}, s01 = s00, s11 = s00;
#pragma unroll
    for (int ks = 0; ks < 4; ++ks) {
      s00 = MFMA32(asbf8(kv[0][ks]), asbf8(qv[0][ks]), s00);
      s01 = MFMA32(asbf8(kv[0][ks]), asbf8(qv[1][ks]), s01);
      s11 = MFMA32(asbf8(kv[1][ks]), asbf8(qv[1][ks]), s11);
    }
    float mx[2], sum[2];
    u32x4 pseg[2];
    {
#pragma unroll
      for (int r = 0; r < 4; ++r) { const bool ok = 4 * g + r <= r16; s00[r] = ok ? s00[r] : -3.0e38f; s11[r] = ok ? s11[r] : -3.0e38f; }
      mx[0] = red16_32_max(max3(max3(s00[0], s00[1], s00[2]), s00[3], -1.0e38f));
      mx[1] = red16_32_max(max3(max3(fmaxf(fmaxf(s01[0], s01[1]), fmaxf(s01[2], s01[3])), s11[0], -1.0e38f), max3(s11[1], s11[2], s11[3]), -1.0e38f));
      const float n0 = -mx[0] * LS, n1 = -mx[1] * LS;
#pragma unroll
      for (int r = 0; r < 4; ++r) {
        s00[r] = __builtin_amdgcn_exp2f(s00[r] * LS + n0); s01[r] = __builtin_amdgcn_exp2f(s01[r] * LS + n1); s11[r] = __builtin_amdgcn_exp2f(s11[r] * LS + n1);
      }
      sum[0] = (s00[0] + s00[1]) + (s00[2] + s00[3]);
      sum[1] = ((s01[0] + s01[1]) + (s01[2] + s01[3])) + ((s11[0] + s11[1]) + (s11[2] + s11[3]));
      pseg[0] = u32x4{pack2(s00[0], s00[1]), pack2(s00[2], s00[3]), 0u, 0u};
      pseg[1] = u32x4{pack2(s01[0], s01[1]), pack2(s01[2], s01[3]), pack2(s11[0], s11[1]), pack2(s11[2], s11[3])};
    }
    f32x4 o[8][2];
#pragma unroll
    for (int dt = 0; dt < 8; ++dt) {
      const int row0 = 4 * g + q4;
      const unsigned xo = (unsigned)(row0 * 256 + (((4 * (dt >> 1) + p4) ^ row0) << 4) + (dt & 1) * 8);
      const u32x2 lo = trread(vl, xo), hi = trread(vl, xo + 4096);
      o[dt][0] = MFMA32(asbf8(u32x4{lo.x, lo.y, 0u, 0u}), asbf8(pseg[0]), (f32x4{0, 0, 0, 0}));
      o[dt][1] = MFMA32(asbf8(u32x4{lo.x, lo.y, hi.x, hi.y}), asbf8(pseg[1]), (f32x4{0, 0, 0, 0}));
    }
    float n0[2]; f32x4 sm[2];
#pragma unroll
    for (int b = 0; b < 2; ++b) { const float t = red16_32_add(sum[b]); sm[b] = f32x4{t, t, t, t}; n0[b] = -mx[b] * LS; }
    unsigned oa = (unsigned)(r16 * 128 + ((g ^ (r16 & 7)) << 4)), ka = (unsigned)(g * 256 + (r16 & 7) * 16), va = (unsigned)lane * 16;
    unsigned one = g < 2 ? 0x3F803F80u : 0u;
    u32x4 kmv[4], vmv[4];
#pragma unroll
    for (int i = 0; i < 4; ++i) vmv[i] = u32x4{0u, 0u, 0u, 0u};
#pragma unroll
    for (int dt = 0; dt < 8; ++dt) asm volatile("" : "+v"(o[dt][0]), "+v"(o[dt][1]), "+v"(vmv[dt >> 1]));
    asm volatile("s_waitcnt lgkmcnt(0)" : "+v"(sm[0]), "+v"(sm[1]), "+v"(n0[0]), "+v"(n0[1]), "+v"(oa), "+v"(ka), "+v"(va), "+v"(one) :: "memory");
    unsigned pg;
    for (int spins = 0;; ++spins) {
      pg = ld4_sc1(cnt + (PROG + h) * 32 + ((lane & 4) ? 5 : (lane & 3)));
      if (__all((pg >> 24) + (s >= SDIR ? 1u : 0u) > (unsigned)s)) break;
      __builtin_amdgcn_s_sleep(SASL);
      if (give_up(spins, tmo, 6)) break;
    }
    if (__all((pg >> 24) == (unsigned)s)) wait_rt(latest(pg), s == NSEG - 1 ? SADL - LAST : SADL);
    const char* mk = uni(kvm + ((size_t)s * NH + h) * 4096);
    for (int spins = 0;; ++spins) {
      asm volatile("global_load_dwordx4 %0, %8, %10 sc1\n\tglobal_load_dwordx4 %1, %8, %10 offset:128 sc1\n\t"
                   "global_load_dwordx4 %2, %8, %10 offset:1024 sc1\n\tglobal_load_dwordx4 %3, %8, %10 offset:1152 sc1\n\ts_mov_b32 exec_hi, 0\n\t"
                   "global_load_dwordx4 %4, %9, %10 offset:2048 sc1\n\tglobal_load_dwordx4 %5, %9, %10 offset:2560 sc1\n\t"
                   "global_load_dwordx4 %6, %9, %10 offset:3072 sc1\n\tglobal_load_dwordx4 %7, %9, %10 offset:3584 sc1\n\ts_mov_b32 exec_hi, -1\n\ts_waitcnt vmcnt(0)"
                   : "=&v"(kmv[0]), "=&v"(kmv[1]), "=&v"(kmv[2]), "=&v"(kmv[3]), "+v"(vmv[0]), "+v"(vmv[1]), "+v"(vmv[2]), "+v"(vmv[3])
                   : "v"(ka), "v"(va), "s"(mk) : "memory");
      unsigned mx_ = 0;
#pragma unroll
      for (int i = 0; i < 4; ++i) mx_ = max(max(max(max(mx_, kmv[i].x), kmv[i].y), max(kmv[i].z, kmv[i].w)), max(max(vmv[i].x, vmv[i].y), max(vmv[i].z, vmv[i].w)));
      if (__all(mx_ != SENT)) break;
      __builtin_amdgcn_s_sleep(2);
      if (give_up(spins, tmo, 6)) break;
    }
    if (SAE && s >= SB && lane == 0) st4_sc1(cnt + (PSA + (hbase >> 1)) * 32 + 16 + wave, bell(s, rt_now() + SAE));
    WTM(slot, s, 1);
    f32x4 tt[2] = {f32x4{0, 0, 0, 0}, f32x4{0, 0, 0, 0}};
#pragma unroll
    for (int ks = 0; ks < 4; ++ks) { tt[0] = MFMA32(asbf8(kmv[ks]), asbf8(qv[0][ks]), tt[0]); tt[1] = MFMA32(asbf8(kmv[ks]), asbf8(qv[1][ks]), tt[1]); }
#pragma unroll
    for (int b = 0; b < 2; ++b)
#pragma unroll
      for (int r = 0; r < 4; ++r) tt[b][r] = tt[b][r] * LS + n0[b];
    if (!__all(max3(max3(tt[0][0], tt[0][1], tt[0][2]), max3(tt[0][3], tt[1][0], tt[1][1]), max2(tt[1][2], tt[1][3])) <= 40.0f)) {
#pragma unroll
      for (int b = 0; b < 2; ++b) {
        float m = max3(max3(tt[b][0], tt[b][1], tt[b][2]), tt[b][3], 0.0f);
        { float x, y; sw16(m, x, y); m = max2(x, y); }
        const float f = __builtin_amdgcn_exp2f(-m);
        tt[b] -= m; sm[b] *= f;
#pragma unroll
        for (int dt = 0; dt < 8; ++dt) o[dt][b] *= f;
      }
    }
    u32x2 pm[2]; float inv[2];
#pragma unroll
    for (int b = 0; b < 2; ++b) {
      pm[b] = u32x2{pack2(__builtin_amdgcn_exp2f(tt[b][0]), __builtin_amdgcn_exp2f(tt[b][1])), pack2(__builtin_amdgcn_exp2f(tt[b][2]), __builtin_amdgcn_exp2f(tt[b][3]))};
      sm[b] = __builtin_amdgcn_mfma_f32_16x16x16bf16_1k(ass4(u32x2{one, one}), ass4(pm[b]), sm[b], 0, 0, 0);
    }
#pragma unroll
    for (int dt = 0; dt < 8; ++dt)
#pragma unroll
      for (int b = 0; b < 2; ++b) o[dt][b] = __builtin_amdgcn_mfma_f32_16x16x16bf16_1k(ass4((dt & 1) ? u32x2{vmv[dt >> 1].z, vmv[dt >> 1].w} : u32x2{vmv[dt >> 1].x, vmv[dt >> 1].y}), ass4(pm[b]), o[dt][b], 0, 0, 0);
    inv[0] = __builtin_amdgcn_rcpf(sm[0][0]); inv[1] = __builtin_amdgcn_rcpf(sm[1][0]);
    const char* od = uni(O + ((size_t)s * 64 + 2 * h) * 4096);
#pragma unroll
    for (int j = 0; j < 4; ++j) {
#pragma unroll
      for (int b = 0; b < 2; ++b) {
        const f32x4 r0 = o[2 * j][b] * inv[b], r1 = o[2 * j + 1][b] * inv[b];
        st16s(od + (j >> 1) * 4096 + b * 2048, oa ^ ((j & 1) * 64u), u32x4{pack2(r0[0], r0[1]), pack2(r0[2], r0[3]), pack2(r1[0], r1[1]), pack2(r1[2], r1[3])});
      }
    }
    if (!SAE && s >= SB && lane == 0) st4_sc1(cnt + (PSA + (hbase >> 1)) * 32 + 16 + wave, bell(s, rt_now() + DSA));
    WTM(slot, s, 3);
    WAIT_V(0);
    if (lane == 0) st4_sc1(cnt + (PSA + (hbase >> 1)) * 32 + wave, (unsigned)it);
    WTM(slot, s, 2);
  }
}
#undef LD8
#undef TIE8

constexpr int Q_MEM = 0;
constexpr int Q_K   = 16384;
constexpr int Q_V   = 24576;
constexpr int Q_QK  = 32768;
constexpr int Q_TM  = 36864;
constexpr int Q_CB  = 40960;
constexpr int Q_RING = 32768;
constexpr int V_P1  = 0;
constexpr int V_P2  = 16384;
constexpr int NCHAIN = 96;
#define EKV 1
#define VHN 2
#define ROTH 0
#define NQQ 6
#define NQK 4
#define E_AG 9
#define RDYI -2
#define ROTW 2
#define DQU 6
#define DQM 18
#define DQD 1
#define ARSEL (xg >> 1)
#define ORSEL (xg >> 1)
#define D_Q 105
#define D_O 4
#define E_B 6
#define E_O 1900
#define KDEL 5
#define VPOS 0

typedef __bf16 bf16x16 __attribute__((ext_vector_type(16)));
typedef unsigned u32x8 __attribute__((ext_vector_type(8)));
__device__ __forceinline__ f32x4 SMF(u32x4 a, u32x8 b, f32x4 c, int idx) {
  return __builtin_amdgcn_smfmac_f32_16x16x64_bf16(__builtin_bit_cast(bf16x8, a), __builtin_bit_cast(bf16x16, b), c, idx, 0, 0);
}
__device__ __forceinline__ float fold32(float a, float b) {
  const auto p = __builtin_amdgcn_permlane32_swap(__float_as_uint(a), __float_as_uint(b), false, false);
  return __uint_as_float(p[0]) + __uint_as_float(p[1]);
}
__device__ __forceinline__ f32x2 rowsum(f32x4 x) { return f32x2{fold32(x[0], x[2]), fold32(x[1], x[3])}; }
__device__ __forceinline__ unsigned nosent1(unsigned v) { return v < 0xFFFFFFFEu ? v : 0xFFFFFFFEu; }
__device__ __forceinline__ void st4s(const char* sb, unsigned vo, unsigned v) { asm volatile("global_store_dword %0, %1, %2 sc1\n\ts_nop 0" :: "v"(vo), "v"(v), "s"(sb) : "memory"); }
__device__ __forceinline__ void st8s(const char* sb, unsigned vo, u32x2 v) { asm volatile("global_store_dwordx2 %0, %1, %2 sc1\n\ts_nop 1" :: "v"(vo), "v"(v), "s"(sb) : "memory"); }
__device__ __forceinline__ void gldsw(const char* sbase, unsigned voff, unsigned lds) {
  unsigned keep;
  asm volatile("s_mov_b32 %0, m0\n\ts_mov_b32 m0, %3\n\ts_nop 2\n\tglobal_load_lds_dwordx4 %1, %2" CNT_ "\n\ts_mov_b32 m0, %0"
               : "=&s"(keep) : "v"(voff), "s"(sbase), "s"(lds) : "memory");
}
__device__ __forceinline__ void wait_clk(unsigned t0, unsigned d) {
  while ((unsigned)__builtin_amdgcn_s_memtime() - t0 < d) __builtin_amdgcn_s_sleep(1);
}
__device__ __forceinline__ unsigned bell_time(const char* a, unsigned vo, unsigned tag, unsigned cap) {
  unsigned v;
  for (unsigned spins = 0;; ++spins) {
    asm volatile("global_load_dword %0, %1, %2 sc1\n\ts_waitcnt vmcnt(0)" : "=&v"(v) : "v"(vo), "s"(a) : "memory");
    if (__all((v >> 24) >= tag) || spins >= cap) break;
  }
  const unsigned now = (unsigned)__builtin_amdgcn_s_memrealtime() + 250u;
  const unsigned age = rowmin16((now - v) & 0xFFFFFFu);
  return now - min(min(rdl(age, 0), rdl(age, 16)), 500u);
}
#define WBAR() do { __builtin_amdgcn_s_barrier(); asm volatile("" ::: "memory"); } while (0)
#define TN(k, v) do { } while (0)
__device__ __forceinline__ void stage_proj(const char* src, unsigned lds, int tid, int wave) {
  const int row = tid >> 4, f = 2 * ((row & 1) | (((row >> 2) & 1) << 1));
#pragma unroll
  for (int j = 0; j < 4; ++j)
    gldsw(src + (size_t)j * 32 * HD * 2, (unsigned)(row * HD * 2 + (((tid & 15) ^ f ^ ((j & 1) << 3)) << 4)), lds + (wave * 64 + 512 * j) * 16);
}
__device__ __forceinline__ unsigned proj_base(int T, int lane) {
  const int g = lane >> 4, q4 = (lane & 15) >> 2, p4 = lane & 3, fs = 2 * ((q4 & 1) | ((g & 1) << 1) | ((q4 >> 1) << 2));
  return (unsigned)((4 * g + (q4 & 1) + 32 * (q4 >> 1)) * 256 + (((2 * T) ^ fs) << 4) + p4 * 8);
}
__device__ __forceinline__ u32x8 proj_frag(const char* smem, unsigned a) {
  const u32x2 t0 = trread(smem, a), t1 = trread(smem, a + 512), t2 = trread(smem, a + 4096), t3 = trread(smem, a + 4608);
  return u32x8{t0.x, t0.y, t1.x, t1.y, t2.x, t2.y, t3.x, t3.y};
}
__device__ __forceinline__ void flag_frags(unsigned fa, unsigned a, unsigned want, bf16x8 (&f)[4]) {
  u32x2 t[8];
  for (;;) {
    u32x4 fl;
    asm volatile("ds_read_b128 %0, %9\n\t"
                 "ds_read_b64_tr_b16 %1, %10\n\tds_read_b64_tr_b16 %2, %10 offset:64\n\t"
                 "ds_read_b64_tr_b16 %3, %10 offset:512\n\tds_read_b64_tr_b16 %4, %10 offset:576\n\t"
                 "ds_read_b64_tr_b16 %5, %10 offset:1024\n\tds_read_b64_tr_b16 %6, %10 offset:1088\n\t"
                 "ds_read_b64_tr_b16 %7, %10 offset:1536\n\tds_read_b64_tr_b16 %8, %10 offset:1600\n\ts_waitcnt lgkmcnt(0)"
                 : "=&v"(fl), "=&v"(t[0]), "=&v"(t[1]), "=&v"(t[2]), "=&v"(t[3]), "=&v"(t[4]), "=&v"(t[5]), "=&v"(t[6]), "=&v"(t[7])
                 : "v"(fa), "v"(a) : "memory");
    if (__builtin_amdgcn_readfirstlane(min(min(fl.x, fl.y), min(fl.z, fl.w))) >= want) break;
  }
#pragma unroll
  for (int ks = 0; ks < 4; ++ks) f[ks] = asbf8(u32x4{t[2 * ks].x, t[2 * ks].y, t[2 * ks + 1].x, t[2 * ks + 1].y});
}
__device__ __forceinline__ void wait_flags(const char* p, unsigned want) {
  for (;;) {
    const u32x4 fl = *(volatile LAS u32x4*)p;
    if (__builtin_amdgcn_readfirstlane(min(min(fl.x, fl.y), min(fl.z, fl.w))) >= want) break;
  }
}
__device__ __forceinline__ f32x4 zero4() { float z = 0.0f; asm volatile("" : "+v"(z)); return f32x4{z, z, z, z}; }
__device__ __forceinline__ unsigned lds_word(const char* p) { return __builtin_amdgcn_readfirstlane(*(volatile LAS unsigned*)p); }

#define MFMA16(a, b, c) __builtin_amdgcn_mfma_f32_16x16x16bf16_1k((a), (b), (c), 0, 0, 0)
#define UF(x) __uint_as_float(x)
__device__ __forceinline__ unsigned umax3(unsigned a, unsigned b, unsigned c) { return max(max(a, b), c); }
__device__ __forceinline__ f32x2 logit2(float a, float b) {
  return f32x2{a, b};
}
constexpr float LSC = LOG2E * SCALE;

struct QKW { u32x32 w0, w1, w2, w3, w4, w5; u32x8 x; };
template <int W_> __device__ __forceinline__ void qk_step(const QKW& W, const char* ag, const char* vs, const char* od, const char* kv, unsigned& cap, unsigned& sp,
                                                          unsigned long long& tc, unsigned long long* ts, unsigned& d0, unsigned& d1);
template <> __device__ __forceinline__ void qk_step<0>(const QKW& W, const char* ag, const char* vs, const char* od, const char* kv, unsigned& cap, unsigned& sp,
                                                        unsigned long long& tc, unsigned long long* ts, unsigned& d0, unsigned& d1) {
  unsigned st, bg;
  asm volatile("v_mbcnt_lo_u32_b32 v60, -1, 0\n\t"
    "v_mbcnt_hi_u32_b32 v60, -1, v60\n\t"
    "v_lshlrev_b32 v6, 4, v60\n\t"
    "s_mov_b32 %[sp], 0\n\t"
    "s_setprio 3\n\t"
    "global_load_dwordx4 v[40:43], v6, %[ag] sc1\n\t"
    "global_load_dwordx4 v[44:47], v6, %[ag] offset:1024 sc1\n\t"
    "s_memtime %[tc]\n\t"
    "v_add_u32 v7, 40960, v6\n\t"
    "v_add_u32 v8, 98304, v6\n\t"
    "ds_read_b128 v[24:27], v7 offset:0\n\t"
    "ds_read_b128 v[28:31], v7 offset:1024\n\t"
    "ds_read_b128 v[32:35], v7 offset:8192\n\t"
    "ds_read_b128 v[36:39], v7 offset:9216\n\t"
    "v_lshrrev_b32 v48, 4, v60\n\t"
    "v_and_b32 v49, 15, v60\n\t"
    "v_and_b32 v61, 4, v60\n\t"
    "v_mul_u32_u24 v61, 0x2aaa, v61\n\t"
    "v_lshrrev_b32 v9, 1, v60\n\t"
    "v_and_b32 v9, 2, v9\n\t"
    "v_or3_b32 v9, v9, v61, %[c44]\n\t"
    "v_add_u32 v14, 0, v6\n\t"
    "v_lshlrev_b32 v61, 2, v48\n\t"
    "v_lshl_or_b32 v10, v49, 4, v61\n\t"
    "v_lshrrev_b32 v50, 2, v49\n\t"
    "v_and_b32 v51, 3, v60\n\t"
    "v_and_b32 v61, 1, v60\n\t"
    "v_lshlrev_b32 v61, 3, v61\n\t"
    "v_lshl_or_b32 v11, v50, 4, v61\n\t"
    "v_lshl_or_b32 v11, v48, 7, v11\n\t"
    "v_xor_b32 v12, v48, v49\n\t"
    "v_lshlrev_b32 v12, 4, v12\n\t"
    "v_lshl_or_b32 v12, v49, 8, v12\n\t"
    "v_lshl_or_b32 v50, v48, 2, v50\n\t"
    "v_or_b32 v51, 0, v51\n\t"
    "v_xor_b32 v51, v51, v50\n\t"
    "v_lshlrev_b32 v13, 4, v51\n\t"
    "v_lshl_or_b32 v13, v50, 8, v13\n\t"
    "v_and_b32 v61, 3, v60\n\t"
    "v_lshlrev_b32 v61, 4, v61\n\t"
    "v_lshl_or_b32 v15, v48, 6, v61\n\t"
    "v_and_b32 v61, 8, v60\n\t"
    "v_lshl_or_b32 v15, v61, 14, v15\n\t"
    "v_and_b32 v61, 4, v60\n\t"
    "v_lshl_or_b32 v15, v61, 8, v15\n\t"
    "v_add_u32 v15, 0, v15\n\t"
    "3:\n\t"
    "v_mov_b32 v16, 0\n\t"
    "v_mov_b32 v17, 0\n\t"
    "v_mov_b32 v18, 0\n\t"
    "v_mov_b32 v19, 0\n\t"
    "v_mov_b32 v20, 0\n\t"
    "v_mov_b32 v21, 0\n\t"
    "v_mov_b32 v22, 0\n\t"
    "v_mov_b32 v23, 0\n\t"
    "s_waitcnt vmcnt(0) lgkmcnt(0)\n\t"
    "14:\n\t"
    "ds_write_b128 v6, v[40:43] offset:0\n\t"
    "ds_write_b128 v6, v[44:47] offset:1024\n\t"
    "v_smfmac_f32_16x16x64_bf16 v[16:19], v[40:43], v[64:71], v9\n\t"
    "v_max3_u32 v60, v40, v41, v42\n\t"
    "v_max3_u32 v61, v43, v44, v45\n\t"
    "v_smfmac_f32_16x16x64_bf16 v[20:23], v[40:43], v[72:79], v9\n\t"
    "v_max3_u32 v60, v60, v46, v47\n\t"
    "v_max_u32 v60, v60, v61\n\t"
    "v_cmp_eq_u32 vcc, -1, v60\n\t"
    "s_cbranch_vccnz 1f\n\t"
    "2:\n\t"
    "s_waitcnt lgkmcnt(0)\n\t"
    "s_cmp_eq_u64 %[kv], 0\n\t"
    "s_barrier\n\t"
    "s_cbranch_scc1 20f\n\t"
    "ds_read_b128 v[40:43], v6 offset:12288\n\t"
    "ds_read_b128 v[48:51], v6 offset:2048\n\t"
    "v_smfmac_f32_16x16x64_bf16 v[16:19], v[44:47], v[80:87], v9\n\t"
    "ds_read_b128 v[52:55], v6 offset:3072\n\t"
    "ds_read_b128 v[56:59], v6 offset:4096\n\t"
    "v_smfmac_f32_16x16x64_bf16 v[20:23], v[44:47], v[88:95], v9\n\t"
    "ds_read_b128 v[60:63], v6 offset:13312\n\t"
    "ds_read_b128 v[44:47], v6 offset:5120\n\t"
    "s_waitcnt lgkmcnt(5)\n\t"
    "v_smfmac_f32_16x16x64_bf16 v[16:19], v[40:43], v[24:31], v9\n\t"
    "ds_read_b128 v[24:27], v7 offset:16384\n\t"
    "ds_read_b128 v[28:31], v7 offset:17408\n\t"
    "v_smfmac_f32_16x16x64_bf16 v[20:23], v[40:43], v[32:39], v9\n\t"
    "ds_read_b128 v[32:35], v7 offset:24576\n\t"
    "ds_read_b128 v[36:39], v7 offset:25600\n\t"
    "ds_read_b128 v[40:43], v6 offset:6144\n\t"
    "s_waitcnt lgkmcnt(9)\n\t"
    "v_smfmac_f32_16x16x64_bf16 v[16:19], v[48:51], v[96:103], v9\n\t"
    "v_smfmac_f32_16x16x64_bf16 v[20:23], v[48:51], v[104:111], v9\n\t"
    "ds_read_b128 v[48:51], v6 offset:7168\n\t"
    "s_waitcnt lgkmcnt(9)\n\t"
    "v_smfmac_f32_16x16x64_bf16 v[16:19], v[52:55], v[112:119], v9\n\t"
    "v_smfmac_f32_16x16x64_bf16 v[20:23], v[52:55], v[120:127], v9\n\t"
    "ds_read_b128 v[52:55], v6 offset:14336\n\t"
    "s_waitcnt lgkmcnt(9)\n\t"
    "v_smfmac_f32_16x16x64_bf16 v[16:19], v[56:59], v[128:135], v9\n\t"
    "v_smfmac_f32_16x16x64_bf16 v[20:23], v[56:59], v[136:143], v9\n\t"
    "ds_read_b128 v[56:59], v6 offset:8192\n\t"
    "s_waitcnt lgkmcnt(6)\n\t"
    "v_smfmac_f32_16x16x64_bf16 v[16:19], v[60:63], v[24:31], v9\n\t"
    "ds_read_b128 v[24:27], v8 offset:0\n\t"
    "ds_read_b128 v[28:31], v8 offset:1024\n\t"
    "s_waitcnt lgkmcnt(6)\n\t"
    "v_smfmac_f32_16x16x64_bf16 v[20:23], v[60:63], v[32:39], v9\n\t"
    "ds_read_b128 v[32:35], v8 offset:8192\n\t"
    "ds_read_b128 v[36:39], v8 offset:9216\n\t"
    "ds_read_b128 v[60:63], v6 offset:9216\n\t"
    "v_smfmac_f32_16x16x64_bf16 v[16:19], v[44:47], v[144:151], v9\n\t"
    "v_smfmac_f32_16x16x64_bf16 v[20:23], v[44:47], v[152:159], v9\n\t"
    "ds_read_b128 v[44:47], v6 offset:10240\n\t"
    "s_waitcnt lgkmcnt(9)\n\t"
    "v_smfmac_f32_16x16x64_bf16 v[16:19], v[40:43], v[160:167], v9\n\t"
    "v_smfmac_f32_16x16x64_bf16 v[20:23], v[40:43], v[168:175], v9\n\t"
    "ds_read_b128 v[40:43], v6 offset:15360\n\t"
    "s_waitcnt lgkmcnt(9)\n\t"
    "v_smfmac_f32_16x16x64_bf16 v[16:19], v[48:51], v[176:183], v9\n\t"
    "v_smfmac_f32_16x16x64_bf16 v[20:23], v[48:51], v[184:191], v9\n\t"
    "ds_read_b128 v[48:51], v6 offset:11264\n\t"
    "s_waitcnt lgkmcnt(6)\n\t"
    "v_smfmac_f32_16x16x64_bf16 v[16:19], v[52:55], v[24:31], v9\n\t"
    "ds_read_b128 v[24:27], v8 offset:16384\n\t"
    "ds_read_b128 v[28:31], v8 offset:17408\n\t"
    "s_waitcnt lgkmcnt(6)\n\t"
    "v_smfmac_f32_16x16x64_bf16 v[20:23], v[52:55], v[32:39], v9\n\t"
    "ds_read_b128 v[32:35], v8 offset:24576\n\t"
    "ds_read_b128 v[36:39], v8 offset:25600\n\t"
    "v_smfmac_f32_16x16x64_bf16 v[16:19], v[56:59], v[192:199], v9\n\t"
    "v_smfmac_f32_16x16x64_bf16 v[20:23], v[56:59], v[200:207], v9\n\t"
    "s_waitcnt lgkmcnt(7)\n\t"
    "v_smfmac_f32_16x16x64_bf16 v[16:19], v[60:63], v[208:215], v9\n\t"
    "v_smfmac_f32_16x16x64_bf16 v[20:23], v[60:63], v[216:223], v9\n\t"
    "s_waitcnt lgkmcnt(6)\n\t"
    "v_smfmac_f32_16x16x64_bf16 v[16:19], v[44:47], v[224:231], v9\n\t"
    "v_smfmac_f32_16x16x64_bf16 v[20:23], v[44:47], v[232:239], v9\n\t"
    "s_waitcnt lgkmcnt(2)\n\t"
    "v_smfmac_f32_16x16x64_bf16 v[16:19], v[40:43], v[24:31], v9\n\t"
    "s_waitcnt lgkmcnt(0)\n\t"
    "v_smfmac_f32_16x16x64_bf16 v[20:23], v[40:43], v[32:39], v9\n\t"
    "v_smfmac_f32_16x16x64_bf16 v[16:19], v[48:51], v[240:247], v9\n\t"
    "v_smfmac_f32_16x16x64_bf16 v[20:23], v[48:51], v[248:255], v9\n\t"
    "v_xor_b32 v7, 64, v12\n\t"
    "v_xor_b32 v8, 0x80, v12\n\t"
    "v_xor_b32 v6, 0xc0, v12\n\t"
    "ds_read_b128 v[24:27], v12 offset:16384\n\t"
    "ds_read_b128 v[28:31], v12 offset:20480\n\t"
    "ds_read_b128 v[32:35], v7 offset:16384\n\t"
    "ds_read_b128 v[36:39], v7 offset:20480\n\t"
    "v_permlane16_swap_b32 v16, v18\n\t"
    "v_permlane16_swap_b32 v17, v19\n\t"
    "v_permlane16_swap_b32 v20, v22\n\t"
    "v_permlane16_swap_b32 v21, v23\n\t"
    "v_add_f32 v16, v16, v18\n\t"
    "v_add_f32 v17, v17, v19\n\t"
    "v_add_f32 v20, v20, v22\n\t"
    "v_mul_f32 v16, %[lsc], v16\n\t"
    "v_mul_f32 v17, %[lsc], v17\n\t"
    "v_cvt_pk_bf16_f32 v16, v16, v17\n\t"
    "v_add_f32 v21, v21, v23\n\t"
    "ds_write_b32 v10, v16 offset:32768\n\t"
    "v_mul_f32 v20, %[lsc], v20\n\t"
    "v_mul_f32 v21, %[lsc], v21\n\t"
    "v_cvt_pk_bf16_f32 v20, v20, v21\n\t"
    "ds_write_b32 v10, v20 offset:33024\n\t"
    "ds_read_b128 v[40:43], v8 offset:16384\n\t"
    "ds_read_b128 v[44:47], v8 offset:20480\n\t"
    "ds_read_b128 v[48:51], v6 offset:16384\n\t"
    "ds_read_b128 v[52:55], v6 offset:20480\n\t"
    "s_waitcnt lgkmcnt(4)\n\t"
    "s_barrier\n\t"
    "ds_read_b64_tr_b16 v[16:17], v11 offset:32768\n\t"
    "ds_read_b64_tr_b16 v[18:19], v11 offset:32832\n\t"
    "ds_read_b64_tr_b16 v[20:21], v11 offset:33280\n\t"
    "ds_read_b64_tr_b16 v[22:23], v11 offset:33344\n\t"
    "ds_read_b64_tr_b16 v[56:57], v11 offset:33792\n\t"
    "ds_read_b64_tr_b16 v[58:59], v11 offset:33856\n\t"
    "ds_read_b64_tr_b16 v[60:61], v11 offset:34304\n\t"
    "ds_read_b64_tr_b16 v[62:63], v11 offset:34368\n\t"
    "v_mov_b32 v6, 0\n\t"
    "v_mov_b32 v7, 0\n\t"
    "v_mov_b32 v8, 0\n\t"
    "v_mov_b32 v9, 0\n\t"
    "s_mov_b32 exec_hi, 0\n\t"
    "global_load_dwordx4 v[6:9], v14, %[vs] sc1\n\t"
    "s_mov_b32 exec_hi, -1\n\t"
    "s_waitcnt lgkmcnt(6)\n\t"
    "v_mfma_f32_16x16x32_bf16 v[24:27], v[24:27], v[16:19], 0\n\t"
    "v_mfma_f32_16x16x32_bf16 v[28:31], v[28:31], v[16:19], 0\n\t"
    "s_waitcnt lgkmcnt(4)\n\t"
    "v_mfma_f32_16x16x32_bf16 v[24:27], v[32:35], v[20:23], v[24:27]\n\t"
    "v_mfma_f32_16x16x32_bf16 v[28:31], v[36:39], v[20:23], v[28:31]\n\t"
    "ds_read_b64_tr_b16 v[32:33], v13 offset:24576\n\t"
    "ds_read_b64_tr_b16 v[34:35], v13 offset:28672\n\t"
    "ds_read_b64_tr_b16 v[36:37], v13 offset:24584\n\t"
    "ds_read_b64_tr_b16 v[38:39], v13 offset:28680\n\t"
    "s_waitcnt lgkmcnt(6)\n\t"
    "v_mfma_f32_16x16x32_bf16 v[24:27], v[40:43], v[56:59], v[24:27]\n\t"
    "v_mfma_f32_16x16x32_bf16 v[28:31], v[44:47], v[56:59], v[28:31]\n\t"
    "s_waitcnt lgkmcnt(4)\n\t"
    "v_mfma_f32_16x16x32_bf16 v[24:27], v[48:51], v[60:63], v[24:27]\n\t"
    "v_mfma_f32_16x16x32_bf16 v[28:31], v[52:55], v[60:63], v[28:31]\n\t"
    "s_setprio 1\n\t"
    "v_mov_b32 v52, %[one]\n\t"
    "v_mov_b32 v53, %[one]\n\t"
    "v_mov_b32 v54, %[one]\n\t"
    "v_mov_b32 v55, %[one]\n\t"
    "s_nop 2\n\t"
    "v_max3_f32 v10, |v24|, |v25|, |v26|\n\t"
    "v_max3_f32 v40, |v27|, |v28|, |v29|\n\t"
    "v_max3_f32 v10, v10, |v30|, |v31|\n\t"
    "v_max_f32 v10, v10, v40\n\t"
    "v_cmp_nge_f32 vcc, %[big], v10\n\t"
    "s_mov_b32 %[bg], %[big]\n\t"
    "v_mov_b32 v10, 0\n\t"
    "s_cbranch_vccnz 12f\n\t"
    "13:\n\t"
    "v_exp_f32 v24, v24\n\t"
    "v_exp_f32 v25, v25\n\t"
    "v_exp_f32 v26, v26\n\t"
    "v_exp_f32 v27, v27\n\t"
    "v_exp_f32 v28, v28\n\t"
    "v_exp_f32 v29, v29\n\t"
    "v_exp_f32 v30, v30\n\t"
    "v_exp_f32 v31, v31\n\t"
    "v_cvt_pk_bf16_f32 v40, v24, v25\n\t"
    "v_cvt_pk_bf16_f32 v41, v26, v27\n\t"
    "v_cvt_pk_bf16_f32 v42, v28, v29\n\t"
    "v_cvt_pk_bf16_f32 v43, v30, v31\n\t"
    "s_setprio 3\n\t"
    "s_waitcnt lgkmcnt(0)\n\t"
    "s_nop 0\n\t"
    "v_mfma_f32_16x16x32_bf16 v[44:47], v[32:35], v[40:43], 0\n\t"
    "v_mfma_f32_16x16x32_bf16 v[48:51], v[36:39], v[40:43], 0\n\t"
    "v_mfma_f32_16x16x32_bf16 v[52:55], v[52:55], v[40:43], 0\n\t"
    "v_mov_b32 v12, 0\n\t"
    "v_mov_b32 v13, 0\n\t"
    "s_mov_b32 exec_hi, 0\n\t"
    "v_mov_b32 v12, %[one]\n\t"
    "v_mov_b32 v13, %[one]\n\t"
    "s_mov_b32 exec_hi, -1\n\t"
    "s_barrier\n\t"
    "ds_read_b64_tr_b16 v[24:25], v11 offset:34816\n\t"
    "ds_read_b64_tr_b16 v[26:27], v11 offset:34880\n\t"
    "ds_read_b64_tr_b16 v[28:29], v11 offset:35328\n\t"
    "ds_read_b64_tr_b16 v[30:31], v11 offset:35392\n\t"
    "ds_read_b64_tr_b16 v[32:33], v11 offset:35840\n\t"
    "ds_read_b64_tr_b16 v[34:35], v11 offset:35904\n\t"
    "ds_read_b64_tr_b16 v[36:37], v11 offset:36352\n\t"
    "ds_read_b64_tr_b16 v[38:39], v11 offset:36416\n\t"
    "s_waitcnt lgkmcnt(6)\n\t"
    "v_mfma_f32_16x16x32_bf16 v[40:43], v[24:27], v[16:19], 0\n\t"
    "s_waitcnt lgkmcnt(4)\n\t"
    "v_mfma_f32_16x16x32_bf16 v[40:43], v[28:31], v[20:23], v[40:43]\n\t"
    "s_waitcnt lgkmcnt(2)\n\t"
    "v_mfma_f32_16x16x32_bf16 v[40:43], v[32:35], v[56:59], v[40:43]\n\t"
    "s_waitcnt lgkmcnt(0)\n\t"
    "v_mfma_f32_16x16x32_bf16 v[40:43], v[36:39], v[60:63], v[40:43]\n\t"
    "5:\n\t"
    "s_waitcnt vmcnt(0)\n\t"
    "v_max3_u32 v11, v6, v7, v8\n\t"
    "v_max_u32 v11, v11, v9\n\t"
    "v_cmp_eq_u32 vcc, -1, v11\n\t"
    "s_cbranch_vccnz 4f\n\t"
    "6:\n\t"
    "s_nop 2\n\t"
    "v_exp_f32 v28, v40\n\t"
    "v_max3_f32 v11, v40, v41, v42\n\t"
    "v_exp_f32 v29, v41\n\t"
    "v_max_f32 v11, v11, v43\n\t"
    "v_exp_f32 v30, v42\n\t"
    "v_cmp_nge_f32 vcc, %[bg], v11\n\t"
    "v_exp_f32 v31, v43\n\t"
    "s_cbranch_vccnz 10f\n\t"
    "11:\n\t"
    "v_cvt_pk_bf16_f32 v24, v28, v29\n\t"
    "v_cvt_pk_bf16_f32 v25, v30, v31\n\t"
    "s_nop 1\n\t"
    "v_mfma_f32_16x16x16_bf16 v[52:55], v[12:13], v[24:25], v[52:55]\n\t"
    "v_mfma_f32_16x16x16_bf16 v[44:47], v[6:7], v[24:25], v[44:47]\n\t"
    "v_mfma_f32_16x16x16_bf16 v[48:51], v[8:9], v[24:25], v[48:51]\n\t"
    "s_nop 5\n\t"
    "v_rcp_f32 v26, v52\n\t"
    "s_nop 0\n\t"
    "v_pk_mul_f32 v[44:45], v[44:45], v[26:27] op_sel_hi:[1,0]\n\t"
    "v_pk_mul_f32 v[46:47], v[46:47], v[26:27] op_sel_hi:[1,0]\n\t"
    "v_pk_mul_f32 v[48:49], v[48:49], v[26:27] op_sel_hi:[1,0]\n\t"
    "v_pk_mul_f32 v[50:51], v[50:51], v[26:27] op_sel_hi:[1,0]\n\t"
    "v_cvt_pk_bf16_f32 v36, v44, v45\n\t"
    "v_cvt_pk_bf16_f32 v37, v46, v47\n\t"
    "v_cvt_pk_bf16_f32 v38, v48, v49\n\t"
    "v_cvt_pk_bf16_f32 v39, v50, v51\n\t"
    "v_min_u32 v36, -2, v36\n\t"
    "v_min_u32 v37, -2, v37\n\t"
    "v_min_u32 v38, -2, v38\n\t"
    "v_min_u32 v39, -2, v39\n\t"
    "global_store_dwordx4 v15, v[36:39], %[od] sc1\n\t"
    "s_barrier\n\t"
    "s_mov_b32 exec_hi, 0\n\t"
    "global_store_dwordx4 v14, v[6:9], %[kv] sc1\n\t"
    "s_mov_b32 exec_hi, -1\n\t"
    "s_nop 0\n\t"
    "s_waitcnt lgkmcnt(0)\n\t"
    "s_branch 9f\n\t"
    "1:\n\t"
    "s_add_u32 %[sp], %[sp], 1\n\t"
    "s_cmp_ge_u32 %[sp], %[cap]\n\t"
    "s_cbranch_scc1 7f\n\t"
    "global_load_dwordx4 v[40:43], v6, %[ag] sc1\n\t"
    "global_load_dwordx4 v[44:47], v6, %[ag] offset:1024 sc1\n\t"
    "s_waitcnt lgkmcnt(0)\n\t"
    "s_memtime %[tc]\n\t"
    "s_nop 7\n\t"
    "s_branch 3b\n\t"
    "7:\n\t"
    "s_mov_b32 %[cap], 0\n\t"
    "s_branch 2b\n\t"
    "20:\n\t"
    "s_barrier\n\t"
    "s_barrier\n\t"
    "s_barrier\n\t"
    "s_branch 9f\n\t"
    "4:\n\t"
    "s_add_u32 %[sp], %[sp], 0x10000\n\t"
    "s_lshr_b32 %[st], %[sp], 16\n\t"
    "s_cmp_ge_u32 %[st], %[cap]\n\t"
    "s_cbranch_scc1 8f\n\t"
    "s_mov_b32 exec_hi, 0\n\t"
    "global_load_dwordx4 v[6:9], v14, %[vs] sc1\n\t"
    "s_mov_b32 exec_hi, -1\n\t"
    "s_branch 5b\n\t"
    "8:\n\t"
    "s_mov_b32 %[cap], 0\n\t"
    "s_branch 6b\n\t"
    "10:\n\t"
    "v_max_f32 v11, v11, v10\n\t"
    "v_mov_b32 v26, v11\n\t"
    "s_nop 1\n\t"
    "v_permlane16_swap_b32 v11, v26\n\t"
    "v_max_f32 v11, v11, v26\n\t"
    "v_sub_f32 v28, v40, v11\n\t"
    "v_sub_f32 v29, v41, v11\n\t"
    "v_sub_f32 v30, v42, v11\n\t"
    "v_sub_f32 v31, v43, v11\n\t"
    "v_sub_f32 v11, v10, v11\n\t"
    "v_exp_f32 v11, v11\n\t"
    "v_exp_f32 v28, v28\n\t"
    "v_exp_f32 v29, v29\n\t"
    "v_exp_f32 v30, v30\n\t"
    "v_exp_f32 v31, v31\n\t"
    "v_mul_f32 v44, v44, v11\n\t"
    "v_mul_f32 v45, v45, v11\n\t"
    "v_mul_f32 v46, v46, v11\n\t"
    "v_mul_f32 v47, v47, v11\n\t"
    "v_mul_f32 v48, v48, v11\n\t"
    "v_mul_f32 v49, v49, v11\n\t"
    "v_mul_f32 v50, v50, v11\n\t"
    "v_mul_f32 v51, v51, v11\n\t"
    "v_mul_f32 v52, v52, v11\n\t"
    "v_mul_f32 v53, v53, v11\n\t"
    "v_mul_f32 v54, v54, v11\n\t"
    "v_mul_f32 v55, v55, v11\n\t"
    "s_branch 11b\n\t"
    "12:\n\t"
    "v_max3_f32 v10, v24, v25, v26\n\t"
    "v_max3_f32 v40, v27, v28, v29\n\t"
    "v_max3_f32 v10, v10, v30, v31\n\t"
    "v_max3_f32 v10, v10, v40, %[neg]\n\t"
    "v_mov_b32 v40, v10\n\t"
    "s_nop 1\n\t"
    "v_permlane16_swap_b32 v10, v40\n\t"
    "v_max_f32 v10, v10, v40\n\t"
    "v_mov_b32 v40, v10\n\t"
    "s_nop 1\n\t"
    "v_permlane32_swap_b32 v10, v40\n\t"
    "v_max_f32 v10, v10, v40\n\t"
    "v_sub_f32 v24, v24, v10\n\t"
    "v_sub_f32 v25, v25, v10\n\t"
    "v_sub_f32 v26, v26, v10\n\t"
    "v_sub_f32 v27, v27, v10\n\t"
    "v_sub_f32 v28, v28, v10\n\t"
    "v_sub_f32 v29, v29, v10\n\t"
    "v_sub_f32 v30, v30, v10\n\t"
    "v_sub_f32 v31, v31, v10\n\t"
    "s_mov_b32 %[bg], 0x7fc00000\n\t"
    "s_branch 13b\n\t"
    "9:"
    : [tc] "=&s"(tc), [sp] "=&s"(sp), [cap] "+s"(cap), [st] "=&s"(st), [bg] "=&s"(bg)
    : [ag] "s"(ag), [c44] "s"(0x4444u), [kv] "s"(kv), [vs] "s"(vs), [od] "s"(od), [od2] "s"(od + 262144), [neg] "s"(-1.0e38f), [lsc] "s"(LSC), [one] "s"(0x3F803F80u), [big] "s"(24.0f), "{v[64:95]}"(W.w0), "{v[96:127]}"(W.w1), "{v[128:159]}"(W.w2), "{v[160:191]}"(W.w3), "{v[192:223]}"(W.w4), "{v[224:255]}"(W.w5)
    : "v6", "v7", "v8", "v9", "v10", "v11", "v12", "v13", "v14", "v15", "v16", "v17", "v18", "v19", "v20", "v21", "v22", "v23", "v24", "v25", "v26", "v27", "v28", "v29", "v30", "v31", "v32", "v33", "v34", "v35", "v36", "v37", "v38", "v39", "v40", "v41", "v42", "v43", "v44", "v45", "v46", "v47", "v48", "v49", "v50", "v51", "v52", "v53", "v54", "v55", "v56", "v57", "v58", "v59", "v60", "v61", "v62", "v63", "vcc", "scc", "memory");
}
template <> __device__ __forceinline__ void qk_step<1>(const QKW& W, const char* ag, const char* vs, const char* od, const char* kv, unsigned& cap, unsigned& sp,
                                                        unsigned long long& tc, unsigned long long* ts, unsigned& d0, unsigned& d1) {
  unsigned st, bg;
  asm volatile("v_mbcnt_lo_u32_b32 v60, -1, 0\n\t"
    "v_mbcnt_hi_u32_b32 v60, -1, v60\n\t"
    "v_lshlrev_b32 v6, 4, v60\n\t"
    "s_mov_b32 %[sp], 0\n\t"
    "s_setprio 3\n\t"
    "global_load_dwordx4 v[40:43], v6, %[ag] sc1\n\t"
    "global_load_dwordx4 v[44:47], v6, %[ag] offset:1024 sc1\n\t"
    "s_memtime %[tc]\n\t"
    "v_add_u32 v7, 43008, v6\n\t"
    "v_add_u32 v8, 100352, v6\n\t"
    "ds_read_b128 v[24:27], v7 offset:0\n\t"
    "ds_read_b128 v[28:31], v7 offset:1024\n\t"
    "ds_read_b128 v[32:35], v7 offset:8192\n\t"
    "ds_read_b128 v[36:39], v7 offset:9216\n\t"
    "v_lshrrev_b32 v48, 4, v60\n\t"
    "v_and_b32 v49, 15, v60\n\t"
    "v_and_b32 v61, 4, v60\n\t"
    "v_mul_u32_u24 v61, 0x2aaa, v61\n\t"
    "v_lshrrev_b32 v9, 1, v60\n\t"
    "v_and_b32 v9, 2, v9\n\t"
    "v_or3_b32 v9, v9, v61, %[c44]\n\t"
    "v_add_u32 v14, 512, v6\n\t"
    "v_lshlrev_b32 v61, 2, v48\n\t"
    "v_lshl_or_b32 v10, v49, 4, v61\n\t"
    "v_lshrrev_b32 v50, 2, v49\n\t"
    "v_and_b32 v51, 3, v60\n\t"
    "v_and_b32 v61, 1, v60\n\t"
    "v_lshlrev_b32 v61, 3, v61\n\t"
    "v_lshl_or_b32 v11, v50, 4, v61\n\t"
    "v_lshl_or_b32 v11, v48, 7, v11\n\t"
    "v_xor_b32 v12, v48, v49\n\t"
    "v_lshlrev_b32 v12, 4, v12\n\t"
    "v_lshl_or_b32 v12, v49, 8, v12\n\t"
    "v_lshl_or_b32 v50, v48, 2, v50\n\t"
    "v_or_b32 v51, 4, v51\n\t"
    "v_xor_b32 v51, v51, v50\n\t"
    "v_lshlrev_b32 v13, 4, v51\n\t"
    "v_lshl_or_b32 v13, v50, 8, v13\n\t"
    "v_and_b32 v61, 3, v60\n\t"
    "v_lshlrev_b32 v61, 4, v61\n\t"
    "v_lshl_or_b32 v15, v48, 6, v61\n\t"
    "v_and_b32 v61, 8, v60\n\t"
    "v_lshl_or_b32 v15, v61, 14, v15\n\t"
    "v_and_b32 v61, 4, v60\n\t"
    "v_lshl_or_b32 v15, v61, 8, v15\n\t"
    "v_add_u32 v15, 256, v15\n\t"
    "3:\n\t"
    "v_mov_b32 v16, 0\n\t"
    "v_mov_b32 v17, 0\n\t"
    "v_mov_b32 v18, 0\n\t"
    "v_mov_b32 v19, 0\n\t"
    "v_mov_b32 v20, 0\n\t"
    "v_mov_b32 v21, 0\n\t"
    "v_mov_b32 v22, 0\n\t"
    "v_mov_b32 v23, 0\n\t"
    "s_waitcnt vmcnt(0) lgkmcnt(0)\n\t"
    "14:\n\t"
    "ds_write_b128 v6, v[40:43] offset:2048\n\t"
    "ds_write_b128 v6, v[44:47] offset:3072\n\t"
    "v_smfmac_f32_16x16x64_bf16 v[16:19], v[40:43], v[96:103], v9\n\t"
    "v_max3_u32 v60, v40, v41, v42\n\t"
    "v_max3_u32 v61, v43, v44, v45\n\t"
    "v_smfmac_f32_16x16x64_bf16 v[20:23], v[40:43], v[104:111], v9\n\t"
    "v_max3_u32 v60, v60, v46, v47\n\t"
    "v_max_u32 v60, v60, v61\n\t"
    "v_cmp_eq_u32 vcc, -1, v60\n\t"
    "s_cbranch_vccnz 1f\n\t"
    "2:\n\t"
    "s_waitcnt lgkmcnt(0)\n\t"
    "s_cmp_eq_u64 %[kv], 0\n\t"
    "s_barrier\n\t"
    "s_cbranch_scc1 20f\n\t"
    "ds_read_b128 v[40:43], v6 offset:12288\n\t"
    "ds_read_b128 v[48:51], v6 offset:0\n\t"
    "v_smfmac_f32_16x16x64_bf16 v[16:19], v[44:47], v[112:119], v9\n\t"
    "ds_read_b128 v[52:55], v6 offset:1024\n\t"
    "ds_read_b128 v[56:59], v6 offset:4096\n\t"
    "v_smfmac_f32_16x16x64_bf16 v[20:23], v[44:47], v[120:127], v9\n\t"
    "ds_read_b128 v[60:63], v6 offset:13312\n\t"
    "ds_read_b128 v[44:47], v6 offset:5120\n\t"
    "s_waitcnt lgkmcnt(5)\n\t"
    "v_smfmac_f32_16x16x64_bf16 v[16:19], v[40:43], v[24:31], v9\n\t"
    "ds_read_b128 v[24:27], v7 offset:16384\n\t"
    "ds_read_b128 v[28:31], v7 offset:17408\n\t"
    "v_smfmac_f32_16x16x64_bf16 v[20:23], v[40:43], v[32:39], v9\n\t"
    "ds_read_b128 v[32:35], v7 offset:24576\n\t"
    "ds_read_b128 v[36:39], v7 offset:25600\n\t"
    "ds_read_b128 v[40:43], v6 offset:6144\n\t"
    "s_waitcnt lgkmcnt(9)\n\t"
    "v_smfmac_f32_16x16x64_bf16 v[16:19], v[48:51], v[64:71], v9\n\t"
    "v_smfmac_f32_16x16x64_bf16 v[20:23], v[48:51], v[72:79], v9\n\t"
    "ds_read_b128 v[48:51], v6 offset:7168\n\t"
    "s_waitcnt lgkmcnt(9)\n\t"
    "v_smfmac_f32_16x16x64_bf16 v[16:19], v[52:55], v[80:87], v9\n\t"
    "v_smfmac_f32_16x16x64_bf16 v[20:23], v[52:55], v[88:95], v9\n\t"
    "ds_read_b128 v[52:55], v6 offset:14336\n\t"
    "s_waitcnt lgkmcnt(9)\n\t"
    "v_smfmac_f32_16x16x64_bf16 v[16:19], v[56:59], v[128:135], v9\n\t"
    "v_smfmac_f32_16x16x64_bf16 v[20:23], v[56:59], v[136:143], v9\n\t"
    "ds_read_b128 v[56:59], v6 offset:8192\n\t"
    "s_waitcnt lgkmcnt(6)\n\t"
    "v_smfmac_f32_16x16x64_bf16 v[16:19], v[60:63], v[24:31], v9\n\t"
    "ds_read_b128 v[24:27], v8 offset:0\n\t"
    "ds_read_b128 v[28:31], v8 offset:1024\n\t"
    "s_waitcnt lgkmcnt(6)\n\t"
    "v_smfmac_f32_16x16x64_bf16 v[20:23], v[60:63], v[32:39], v9\n\t"
    "ds_read_b128 v[32:35], v8 offset:8192\n\t"
    "ds_read_b128 v[36:39], v8 offset:9216\n\t"
    "ds_read_b128 v[60:63], v6 offset:9216\n\t"
    "v_smfmac_f32_16x16x64_bf16 v[16:19], v[44:47], v[144:151], v9\n\t"
    "v_smfmac_f32_16x16x64_bf16 v[20:23], v[44:47], v[152:159], v9\n\t"
    "ds_read_b128 v[44:47], v6 offset:10240\n\t"
    "s_waitcnt lgkmcnt(9)\n\t"
    "v_smfmac_f32_16x16x64_bf16 v[16:19], v[40:43], v[160:167], v9\n\t"
    "v_smfmac_f32_16x16x64_bf16 v[20:23], v[40:43], v[168:175], v9\n\t"
    "ds_read_b128 v[40:43], v6 offset:15360\n\t"
    "s_waitcnt lgkmcnt(9)\n\t"
    "v_smfmac_f32_16x16x64_bf16 v[16:19], v[48:51], v[176:183], v9\n\t"
    "v_smfmac_f32_16x16x64_bf16 v[20:23], v[48:51], v[184:191], v9\n\t"
    "ds_read_b128 v[48:51], v6 offset:11264\n\t"
    "s_waitcnt lgkmcnt(6)\n\t"
    "v_smfmac_f32_16x16x64_bf16 v[16:19], v[52:55], v[24:31], v9\n\t"
    "ds_read_b128 v[24:27], v8 offset:16384\n\t"
    "ds_read_b128 v[28:31], v8 offset:17408\n\t"
    "s_waitcnt lgkmcnt(6)\n\t"
    "v_smfmac_f32_16x16x64_bf16 v[20:23], v[52:55], v[32:39], v9\n\t"
    "ds_read_b128 v[32:35], v8 offset:24576\n\t"
    "ds_read_b128 v[36:39], v8 offset:25600\n\t"
    "v_smfmac_f32_16x16x64_bf16 v[16:19], v[56:59], v[192:199], v9\n\t"
    "v_smfmac_f32_16x16x64_bf16 v[20:23], v[56:59], v[200:207], v9\n\t"
    "s_waitcnt lgkmcnt(7)\n\t"
    "v_smfmac_f32_16x16x64_bf16 v[16:19], v[60:63], v[208:215], v9\n\t"
    "v_smfmac_f32_16x16x64_bf16 v[20:23], v[60:63], v[216:223], v9\n\t"
    "s_waitcnt lgkmcnt(6)\n\t"
    "v_smfmac_f32_16x16x64_bf16 v[16:19], v[44:47], v[224:231], v9\n\t"
    "v_smfmac_f32_16x16x64_bf16 v[20:23], v[44:47], v[232:239], v9\n\t"
    "s_waitcnt lgkmcnt(2)\n\t"
    "v_smfmac_f32_16x16x64_bf16 v[16:19], v[40:43], v[24:31], v9\n\t"
    "s_waitcnt lgkmcnt(0)\n\t"
    "v_smfmac_f32_16x16x64_bf16 v[20:23], v[40:43], v[32:39], v9\n\t"
    "v_smfmac_f32_16x16x64_bf16 v[16:19], v[48:51], v[240:247], v9\n\t"
    "v_smfmac_f32_16x16x64_bf16 v[20:23], v[48:51], v[248:255], v9\n\t"
    "v_xor_b32 v7, 64, v12\n\t"
    "v_xor_b32 v8, 0x80, v12\n\t"
    "v_xor_b32 v6, 0xc0, v12\n\t"
    "ds_read_b128 v[24:27], v12 offset:16384\n\t"
    "ds_read_b128 v[28:31], v12 offset:20480\n\t"
    "ds_read_b128 v[32:35], v7 offset:16384\n\t"
    "ds_read_b128 v[36:39], v7 offset:20480\n\t"
    "v_permlane16_swap_b32 v16, v18\n\t"
    "v_permlane16_swap_b32 v17, v19\n\t"
    "v_permlane16_swap_b32 v20, v22\n\t"
    "v_permlane16_swap_b32 v21, v23\n\t"
    "v_add_f32 v16, v16, v18\n\t"
    "v_add_f32 v17, v17, v19\n\t"
    "v_add_f32 v20, v20, v22\n\t"
    "v_mul_f32 v16, %[lsc], v16\n\t"
    "v_mul_f32 v17, %[lsc], v17\n\t"
    "v_cvt_pk_bf16_f32 v16, v16, v17\n\t"
    "v_add_f32 v21, v21, v23\n\t"
    "ds_write_b32 v10, v16 offset:33280\n\t"
    "v_mul_f32 v20, %[lsc], v20\n\t"
    "v_mul_f32 v21, %[lsc], v21\n\t"
    "v_cvt_pk_bf16_f32 v20, v20, v21\n\t"
    "ds_write_b32 v10, v20 offset:33536\n\t"
    "ds_read_b128 v[40:43], v8 offset:16384\n\t"
    "ds_read_b128 v[44:47], v8 offset:20480\n\t"
    "ds_read_b128 v[48:51], v6 offset:16384\n\t"
    "ds_read_b128 v[52:55], v6 offset:20480\n\t"
    "s_waitcnt lgkmcnt(4)\n\t"
    "s_barrier\n\t"
    "ds_read_b64_tr_b16 v[16:17], v11 offset:32768\n\t"
    "ds_read_b64_tr_b16 v[18:19], v11 offset:32832\n\t"
    "ds_read_b64_tr_b16 v[20:21], v11 offset:33280\n\t"
    "ds_read_b64_tr_b16 v[22:23], v11 offset:33344\n\t"
    "ds_read_b64_tr_b16 v[56:57], v11 offset:33792\n\t"
    "ds_read_b64_tr_b16 v[58:59], v11 offset:33856\n\t"
    "ds_read_b64_tr_b16 v[60:61], v11 offset:34304\n\t"
    "ds_read_b64_tr_b16 v[62:63], v11 offset:34368\n\t"
    "v_mov_b32 v6, 0\n\t"
    "v_mov_b32 v7, 0\n\t"
    "v_mov_b32 v8, 0\n\t"
    "v_mov_b32 v9, 0\n\t"
    "s_mov_b32 exec_hi, 0\n\t"
    "global_load_dwordx4 v[6:9], v14, %[vs] sc1\n\t"
    "s_mov_b32 exec_hi, -1\n\t"
    "s_waitcnt lgkmcnt(6)\n\t"
    "v_mfma_f32_16x16x32_bf16 v[24:27], v[24:27], v[16:19], 0\n\t"
    "v_mfma_f32_16x16x32_bf16 v[28:31], v[28:31], v[16:19], 0\n\t"
    "s_waitcnt lgkmcnt(4)\n\t"
    "v_mfma_f32_16x16x32_bf16 v[24:27], v[32:35], v[20:23], v[24:27]\n\t"
    "v_mfma_f32_16x16x32_bf16 v[28:31], v[36:39], v[20:23], v[28:31]\n\t"
    "ds_read_b64_tr_b16 v[32:33], v13 offset:24576\n\t"
    "ds_read_b64_tr_b16 v[34:35], v13 offset:28672\n\t"
    "ds_read_b64_tr_b16 v[36:37], v13 offset:24584\n\t"
    "ds_read_b64_tr_b16 v[38:39], v13 offset:28680\n\t"
    "s_waitcnt lgkmcnt(6)\n\t"
    "v_mfma_f32_16x16x32_bf16 v[24:27], v[40:43], v[56:59], v[24:27]\n\t"
    "v_mfma_f32_16x16x32_bf16 v[28:31], v[44:47], v[56:59], v[28:31]\n\t"
    "s_waitcnt lgkmcnt(4)\n\t"
    "v_mfma_f32_16x16x32_bf16 v[24:27], v[48:51], v[60:63], v[24:27]\n\t"
    "v_mfma_f32_16x16x32_bf16 v[28:31], v[52:55], v[60:63], v[28:31]\n\t"
    "s_setprio 1\n\t"
    "v_mov_b32 v52, %[one]\n\t"
    "v_mov_b32 v53, %[one]\n\t"
    "v_mov_b32 v54, %[one]\n\t"
    "v_mov_b32 v55, %[one]\n\t"
    "s_nop 2\n\t"
    "v_max3_f32 v10, |v24|, |v25|, |v26|\n\t"
    "v_max3_f32 v40, |v27|, |v28|, |v29|\n\t"
    "v_max3_f32 v10, v10, |v30|, |v31|\n\t"
    "v_max_f32 v10, v10, v40\n\t"
    "v_cmp_nge_f32 vcc, %[big], v10\n\t"
    "s_mov_b32 %[bg], %[big]\n\t"
    "v_mov_b32 v10, 0\n\t"
    "s_cbranch_vccnz 12f\n\t"
    "13:\n\t"
    "v_exp_f32 v24, v24\n\t"
    "v_exp_f32 v25, v25\n\t"
    "v_exp_f32 v26, v26\n\t"
    "v_exp_f32 v27, v27\n\t"
    "v_exp_f32 v28, v28\n\t"
    "v_exp_f32 v29, v29\n\t"
    "v_exp_f32 v30, v30\n\t"
    "v_exp_f32 v31, v31\n\t"
    "v_cvt_pk_bf16_f32 v40, v24, v25\n\t"
    "v_cvt_pk_bf16_f32 v41, v26, v27\n\t"
    "v_cvt_pk_bf16_f32 v42, v28, v29\n\t"
    "v_cvt_pk_bf16_f32 v43, v30, v31\n\t"
    "s_setprio 3\n\t"
    "s_waitcnt lgkmcnt(0)\n\t"
    "s_nop 0\n\t"
    "v_mfma_f32_16x16x32_bf16 v[44:47], v[32:35], v[40:43], 0\n\t"
    "v_mfma_f32_16x16x32_bf16 v[48:51], v[36:39], v[40:43], 0\n\t"
    "v_mfma_f32_16x16x32_bf16 v[52:55], v[52:55], v[40:43], 0\n\t"
    "v_mov_b32 v12, 0\n\t"
    "v_mov_b32 v13, 0\n\t"
    "s_mov_b32 exec_hi, 0\n\t"
    "v_mov_b32 v12, %[one]\n\t"
    "v_mov_b32 v13, %[one]\n\t"
    "s_mov_b32 exec_hi, -1\n\t"
    "s_barrier\n\t"
    "ds_read_b64_tr_b16 v[24:25], v11 offset:34816\n\t"
    "ds_read_b64_tr_b16 v[26:27], v11 offset:34880\n\t"
    "ds_read_b64_tr_b16 v[28:29], v11 offset:35328\n\t"
    "ds_read_b64_tr_b16 v[30:31], v11 offset:35392\n\t"
    "ds_read_b64_tr_b16 v[32:33], v11 offset:35840\n\t"
    "ds_read_b64_tr_b16 v[34:35], v11 offset:35904\n\t"
    "ds_read_b64_tr_b16 v[36:37], v11 offset:36352\n\t"
    "ds_read_b64_tr_b16 v[38:39], v11 offset:36416\n\t"
    "s_waitcnt lgkmcnt(6)\n\t"
    "v_mfma_f32_16x16x32_bf16 v[40:43], v[24:27], v[16:19], 0\n\t"
    "s_waitcnt lgkmcnt(4)\n\t"
    "v_mfma_f32_16x16x32_bf16 v[40:43], v[28:31], v[20:23], v[40:43]\n\t"
    "s_waitcnt lgkmcnt(2)\n\t"
    "v_mfma_f32_16x16x32_bf16 v[40:43], v[32:35], v[56:59], v[40:43]\n\t"
    "s_waitcnt lgkmcnt(0)\n\t"
    "v_mfma_f32_16x16x32_bf16 v[40:43], v[36:39], v[60:63], v[40:43]\n\t"
    "5:\n\t"
    "s_waitcnt vmcnt(0)\n\t"
    "v_max3_u32 v11, v6, v7, v8\n\t"
    "v_max_u32 v11, v11, v9\n\t"
    "v_cmp_eq_u32 vcc, -1, v11\n\t"
    "s_cbranch_vccnz 4f\n\t"
    "6:\n\t"
    "s_nop 2\n\t"
    "v_exp_f32 v28, v40\n\t"
    "v_max3_f32 v11, v40, v41, v42\n\t"
    "v_exp_f32 v29, v41\n\t"
    "v_max_f32 v11, v11, v43\n\t"
    "v_exp_f32 v30, v42\n\t"
    "v_cmp_nge_f32 vcc, %[bg], v11\n\t"
    "v_exp_f32 v31, v43\n\t"
    "s_cbranch_vccnz 10f\n\t"
    "11:\n\t"
    "v_cvt_pk_bf16_f32 v24, v28, v29\n\t"
    "v_cvt_pk_bf16_f32 v25, v30, v31\n\t"
    "s_nop 1\n\t"
    "v_mfma_f32_16x16x16_bf16 v[52:55], v[12:13], v[24:25], v[52:55]\n\t"
    "v_mfma_f32_16x16x16_bf16 v[44:47], v[6:7], v[24:25], v[44:47]\n\t"
    "v_mfma_f32_16x16x16_bf16 v[48:51], v[8:9], v[24:25], v[48:51]\n\t"
    "s_nop 5\n\t"
    "v_rcp_f32 v26, v52\n\t"
    "s_nop 0\n\t"
    "v_pk_mul_f32 v[44:45], v[44:45], v[26:27] op_sel_hi:[1,0]\n\t"
    "v_pk_mul_f32 v[46:47], v[46:47], v[26:27] op_sel_hi:[1,0]\n\t"
    "v_pk_mul_f32 v[48:49], v[48:49], v[26:27] op_sel_hi:[1,0]\n\t"
    "v_pk_mul_f32 v[50:51], v[50:51], v[26:27] op_sel_hi:[1,0]\n\t"
    "v_cvt_pk_bf16_f32 v36, v44, v45\n\t"
    "v_cvt_pk_bf16_f32 v37, v46, v47\n\t"
    "v_cvt_pk_bf16_f32 v38, v48, v49\n\t"
    "v_cvt_pk_bf16_f32 v39, v50, v51\n\t"
    "v_min_u32 v36, -2, v36\n\t"
    "v_min_u32 v37, -2, v37\n\t"
    "v_min_u32 v38, -2, v38\n\t"
    "v_min_u32 v39, -2, v39\n\t"
    "global_store_dwordx4 v15, v[36:39], %[od] sc1\n\t"
    "s_barrier\n\t"
    "s_mov_b32 exec_hi, 0\n\t"
    "global_store_dwordx4 v14, v[6:9], %[kv] sc1\n\t"
    "s_mov_b32 exec_hi, -1\n\t"
    "s_nop 0\n\t"
    "s_waitcnt lgkmcnt(0)\n\t"
    "s_branch 9f\n\t"
    "1:\n\t"
    "s_add_u32 %[sp], %[sp], 1\n\t"
    "s_cmp_ge_u32 %[sp], %[cap]\n\t"
    "s_cbranch_scc1 7f\n\t"
    "global_load_dwordx4 v[40:43], v6, %[ag] sc1\n\t"
    "global_load_dwordx4 v[44:47], v6, %[ag] offset:1024 sc1\n\t"
    "s_waitcnt lgkmcnt(0)\n\t"
    "s_memtime %[tc]\n\t"
    "s_nop 7\n\t"
    "s_branch 3b\n\t"
    "7:\n\t"
    "s_mov_b32 %[cap], 0\n\t"
    "s_branch 2b\n\t"
    "20:\n\t"
    "s_barrier\n\t"
    "s_barrier\n\t"
    "s_barrier\n\t"
    "s_branch 9f\n\t"
    "4:\n\t"
    "s_add_u32 %[sp], %[sp], 0x10000\n\t"
    "s_lshr_b32 %[st], %[sp], 16\n\t"
    "s_cmp_ge_u32 %[st], %[cap]\n\t"
    "s_cbranch_scc1 8f\n\t"
    "s_mov_b32 exec_hi, 0\n\t"
    "global_load_dwordx4 v[6:9], v14, %[vs] sc1\n\t"
    "s_mov_b32 exec_hi, -1\n\t"
    "s_branch 5b\n\t"
    "8:\n\t"
    "s_mov_b32 %[cap], 0\n\t"
    "s_branch 6b\n\t"
    "10:\n\t"
    "v_max_f32 v11, v11, v10\n\t"
    "v_mov_b32 v26, v11\n\t"
    "s_nop 1\n\t"
    "v_permlane16_swap_b32 v11, v26\n\t"
    "v_max_f32 v11, v11, v26\n\t"
    "v_sub_f32 v28, v40, v11\n\t"
    "v_sub_f32 v29, v41, v11\n\t"
    "v_sub_f32 v30, v42, v11\n\t"
    "v_sub_f32 v31, v43, v11\n\t"
    "v_sub_f32 v11, v10, v11\n\t"
    "v_exp_f32 v11, v11\n\t"
    "v_exp_f32 v28, v28\n\t"
    "v_exp_f32 v29, v29\n\t"
    "v_exp_f32 v30, v30\n\t"
    "v_exp_f32 v31, v31\n\t"
    "v_mul_f32 v44, v44, v11\n\t"
    "v_mul_f32 v45, v45, v11\n\t"
    "v_mul_f32 v46, v46, v11\n\t"
    "v_mul_f32 v47, v47, v11\n\t"
    "v_mul_f32 v48, v48, v11\n\t"
    "v_mul_f32 v49, v49, v11\n\t"
    "v_mul_f32 v50, v50, v11\n\t"
    "v_mul_f32 v51, v51, v11\n\t"
    "v_mul_f32 v52, v52, v11\n\t"
    "v_mul_f32 v53, v53, v11\n\t"
    "v_mul_f32 v54, v54, v11\n\t"
    "v_mul_f32 v55, v55, v11\n\t"
    "s_branch 11b\n\t"
    "12:\n\t"
    "v_max3_f32 v10, v24, v25, v26\n\t"
    "v_max3_f32 v40, v27, v28, v29\n\t"
    "v_max3_f32 v10, v10, v30, v31\n\t"
    "v_max3_f32 v10, v10, v40, %[neg]\n\t"
    "v_mov_b32 v40, v10\n\t"
    "s_nop 1\n\t"
    "v_permlane16_swap_b32 v10, v40\n\t"
    "v_max_f32 v10, v10, v40\n\t"
    "v_mov_b32 v40, v10\n\t"
    "s_nop 1\n\t"
    "v_permlane32_swap_b32 v10, v40\n\t"
    "v_max_f32 v10, v10, v40\n\t"
    "v_sub_f32 v24, v24, v10\n\t"
    "v_sub_f32 v25, v25, v10\n\t"
    "v_sub_f32 v26, v26, v10\n\t"
    "v_sub_f32 v27, v27, v10\n\t"
    "v_sub_f32 v28, v28, v10\n\t"
    "v_sub_f32 v29, v29, v10\n\t"
    "v_sub_f32 v30, v30, v10\n\t"
    "v_sub_f32 v31, v31, v10\n\t"
    "s_mov_b32 %[bg], 0x7fc00000\n\t"
    "s_branch 13b\n\t"
    "9:"
    : [tc] "=&s"(tc), [sp] "=&s"(sp), [cap] "+s"(cap), [st] "=&s"(st), [bg] "=&s"(bg)
    : [ag] "s"(ag), [c44] "s"(0x4444u), [kv] "s"(kv), [vs] "s"(vs), [od] "s"(od), [od2] "s"(od + 262144), [neg] "s"(-1.0e38f), [lsc] "s"(LSC), [one] "s"(0x3F803F80u), [big] "s"(24.0f), "{v[64:95]}"(W.w0), "{v[96:127]}"(W.w1), "{v[128:159]}"(W.w2), "{v[160:191]}"(W.w3), "{v[192:223]}"(W.w4), "{v[224:255]}"(W.w5)
    : "v6", "v7", "v8", "v9", "v10", "v11", "v12", "v13", "v14", "v15", "v16", "v17", "v18", "v19", "v20", "v21", "v22", "v23", "v24", "v25", "v26", "v27", "v28", "v29", "v30", "v31", "v32", "v33", "v34", "v35", "v36", "v37", "v38", "v39", "v40", "v41", "v42", "v43", "v44", "v45", "v46", "v47", "v48", "v49", "v50", "v51", "v52", "v53", "v54", "v55", "v56", "v57", "v58", "v59", "v60", "v61", "v62", "v63", "vcc", "scc", "memory");
}
template <> __device__ __forceinline__ void qk_step<2>(const QKW& W, const char* ag, const char* vs, const char* od, const char* kv, unsigned& cap, unsigned& sp,
                                                        unsigned long long& tc, unsigned long long* ts, unsigned& d0, unsigned& d1) {
  unsigned st, bg;
  asm volatile("v_mbcnt_lo_u32_b32 v60, -1, 0\n\t"
    "v_mbcnt_hi_u32_b32 v60, -1, v60\n\t"
    "v_lshlrev_b32 v6, 4, v60\n\t"
    "s_mov_b32 %[sp], 0\n\t"
    "s_setprio 3\n\t"
    "global_load_dwordx4 v[40:43], v6, %[ag] sc1\n\t"
    "global_load_dwordx4 v[44:47], v6, %[ag] offset:1024 sc1\n\t"
    "s_memtime %[tc]\n\t"
    "v_add_u32 v7, 45056, v6\n\t"
    "v_add_u32 v8, 102400, v6\n\t"
    "ds_read_b128 v[24:27], v7 offset:0\n\t"
    "ds_read_b128 v[28:31], v7 offset:1024\n\t"
    "ds_read_b128 v[32:35], v7 offset:8192\n\t"
    "ds_read_b128 v[36:39], v7 offset:9216\n\t"
    "v_lshrrev_b32 v48, 4, v60\n\t"
    "v_and_b32 v49, 15, v60\n\t"
    "v_and_b32 v61, 4, v60\n\t"
    "v_mul_u32_u24 v61, 0x2aaa, v61\n\t"
    "v_lshrrev_b32 v9, 1, v60\n\t"
    "v_and_b32 v9, 2, v9\n\t"
    "v_or3_b32 v9, v9, v61, %[c44]\n\t"
    "v_add_u32 v14, 1024, v6\n\t"
    "v_lshlrev_b32 v61, 2, v48\n\t"
    "v_lshl_or_b32 v10, v49, 4, v61\n\t"
    "v_lshrrev_b32 v50, 2, v49\n\t"
    "v_and_b32 v51, 3, v60\n\t"
    "v_and_b32 v61, 1, v60\n\t"
    "v_lshlrev_b32 v61, 3, v61\n\t"
    "v_lshl_or_b32 v11, v50, 4, v61\n\t"
    "v_lshl_or_b32 v11, v48, 7, v11\n\t"
    "v_xor_b32 v12, v48, v49\n\t"
    "v_lshlrev_b32 v12, 4, v12\n\t"
    "v_lshl_or_b32 v12, v49, 8, v12\n\t"
    "v_lshl_or_b32 v50, v48, 2, v50\n\t"
    "v_or_b32 v51, 8, v51\n\t"
    "v_xor_b32 v51, v51, v50\n\t"
    "v_lshlrev_b32 v13, 4, v51\n\t"
    "v_lshl_or_b32 v13, v50, 8, v13\n\t"
    "v_and_b32 v61, 3, v60\n\t"
    "v_lshlrev_b32 v61, 4, v61\n\t"
    "v_lshl_or_b32 v15, v48, 6, v61\n\t"
    "v_and_b32 v61, 8, v60\n\t"
    "v_lshl_or_b32 v15, v61, 14, v15\n\t"
    "v_and_b32 v61, 4, v60\n\t"
    "v_lshl_or_b32 v15, v61, 8, v15\n\t"
    "v_add_u32 v15, 512, v15\n\t"
    "3:\n\t"
    "v_mov_b32 v16, 0\n\t"
    "v_mov_b32 v17, 0\n\t"
    "v_mov_b32 v18, 0\n\t"
    "v_mov_b32 v19, 0\n\t"
    "v_mov_b32 v20, 0\n\t"
    "v_mov_b32 v21, 0\n\t"
    "v_mov_b32 v22, 0\n\t"
    "v_mov_b32 v23, 0\n\t"
    "s_waitcnt vmcnt(0) lgkmcnt(0)\n\t"
    "14:\n\t"
    "ds_write_b128 v6, v[40:43] offset:4096\n\t"
    "ds_write_b128 v6, v[44:47] offset:5120\n\t"
    "v_smfmac_f32_16x16x64_bf16 v[16:19], v[40:43], v[128:135], v9\n\t"
    "v_max3_u32 v60, v40, v41, v42\n\t"
    "v_max3_u32 v61, v43, v44, v45\n\t"
    "v_smfmac_f32_16x16x64_bf16 v[20:23], v[40:43], v[136:143], v9\n\t"
    "v_max3_u32 v60, v60, v46, v47\n\t"
    "v_max_u32 v60, v60, v61\n\t"
    "v_cmp_eq_u32 vcc, -1, v60\n\t"
    "s_cbranch_vccnz 1f\n\t"
    "2:\n\t"
    "s_waitcnt lgkmcnt(0)\n\t"
    "s_cmp_eq_u64 %[kv], 0\n\t"
    "s_barrier\n\t"
    "s_cbranch_scc1 20f\n\t"
    "ds_read_b128 v[40:43], v6 offset:12288\n\t"
    "ds_read_b128 v[48:51], v6 offset:0\n\t"
    "v_smfmac_f32_16x16x64_bf16 v[16:19], v[44:47], v[144:151], v9\n\t"
    "ds_read_b128 v[52:55], v6 offset:1024\n\t"
    "ds_read_b128 v[56:59], v6 offset:2048\n\t"
    "v_smfmac_f32_16x16x64_bf16 v[20:23], v[44:47], v[152:159], v9\n\t"
    "ds_read_b128 v[60:63], v6 offset:13312\n\t"
    "ds_read_b128 v[44:47], v6 offset:3072\n\t"
    "s_waitcnt lgkmcnt(5)\n\t"
    "v_smfmac_f32_16x16x64_bf16 v[16:19], v[40:43], v[24:31], v9\n\t"
    "ds_read_b128 v[24:27], v7 offset:16384\n\t"
    "ds_read_b128 v[28:31], v7 offset:17408\n\t"
    "v_smfmac_f32_16x16x64_bf16 v[20:23], v[40:43], v[32:39], v9\n\t"
    "ds_read_b128 v[32:35], v7 offset:24576\n\t"
    "ds_read_b128 v[36:39], v7 offset:25600\n\t"
    "ds_read_b128 v[40:43], v6 offset:6144\n\t"
    "s_waitcnt lgkmcnt(9)\n\t"
    "v_smfmac_f32_16x16x64_bf16 v[16:19], v[48:51], v[64:71], v9\n\t"
    "v_smfmac_f32_16x16x64_bf16 v[20:23], v[48:51], v[72:79], v9\n\t"
    "ds_read_b128 v[48:51], v6 offset:7168\n\t"
    "s_waitcnt lgkmcnt(9)\n\t"
    "v_smfmac_f32_16x16x64_bf16 v[16:19], v[52:55], v[80:87], v9\n\t"
    "v_smfmac_f32_16x16x64_bf16 v[20:23], v[52:55], v[88:95], v9\n\t"
    "ds_read_b128 v[52:55], v6 offset:14336\n\t"
    "s_waitcnt lgkmcnt(9)\n\t"
    "v_smfmac_f32_16x16x64_bf16 v[16:19], v[56:59], v[96:103], v9\n\t"
    "v_smfmac_f32_16x16x64_bf16 v[20:23], v[56:59], v[104:111], v9\n\t"
    "ds_read_b128 v[56:59], v6 offset:8192\n\t"
    "s_waitcnt lgkmcnt(6)\n\t"
    "v_smfmac_f32_16x16x64_bf16 v[16:19], v[60:63], v[24:31], v9\n\t"
    "ds_read_b128 v[24:27], v8 offset:0\n\t"
    "ds_read_b128 v[28:31], v8 offset:1024\n\t"
    "s_waitcnt lgkmcnt(6)\n\t"
    "v_smfmac_f32_16x16x64_bf16 v[20:23], v[60:63], v[32:39], v9\n\t"
    "ds_read_b128 v[32:35], v8 offset:8192\n\t"
    "ds_read_b128 v[36:39], v8 offset:9216\n\t"
    "ds_read_b128 v[60:63], v6 offset:9216\n\t"
    "v_smfmac_f32_16x16x64_bf16 v[16:19], v[44:47], v[112:119], v9\n\t"
    "v_smfmac_f32_16x16x64_bf16 v[20:23], v[44:47], v[120:127], v9\n\t"
    "ds_read_b128 v[44:47], v6 offset:10240\n\t"
    "s_waitcnt lgkmcnt(9)\n\t"
    "v_smfmac_f32_16x16x64_bf16 v[16:19], v[40:43], v[160:167], v9\n\t"
    "v_smfmac_f32_16x16x64_bf16 v[20:23], v[40:43], v[168:175], v9\n\t"
    "ds_read_b128 v[40:43], v6 offset:15360\n\t"
    "s_waitcnt lgkmcnt(9)\n\t"
    "v_smfmac_f32_16x16x64_bf16 v[16:19], v[48:51], v[176:183], v9\n\t"
    "v_smfmac_f32_16x16x64_bf16 v[20:23], v[48:51], v[184:191], v9\n\t"
    "ds_read_b128 v[48:51], v6 offset:11264\n\t"
    "s_waitcnt lgkmcnt(6)\n\t"
    "v_smfmac_f32_16x16x64_bf16 v[16:19], v[52:55], v[24:31], v9\n\t"
    "ds_read_b128 v[24:27], v8 offset:16384\n\t"
    "ds_read_b128 v[28:31], v8 offset:17408\n\t"
    "s_waitcnt lgkmcnt(6)\n\t"
    "v_smfmac_f32_16x16x64_bf16 v[20:23], v[52:55], v[32:39], v9\n\t"
    "ds_read_b128 v[32:35], v8 offset:24576\n\t"
    "ds_read_b128 v[36:39], v8 offset:25600\n\t"
    "v_smfmac_f32_16x16x64_bf16 v[16:19], v[56:59], v[192:199], v9\n\t"
    "v_smfmac_f32_16x16x64_bf16 v[20:23], v[56:59], v[200:207], v9\n\t"
    "s_waitcnt lgkmcnt(7)\n\t"
    "v_smfmac_f32_16x16x64_bf16 v[16:19], v[60:63], v[208:215], v9\n\t"
    "v_smfmac_f32_16x16x64_bf16 v[20:23], v[60:63], v[216:223], v9\n\t"
    "s_waitcnt lgkmcnt(6)\n\t"
    "v_smfmac_f32_16x16x64_bf16 v[16:19], v[44:47], v[224:231], v9\n\t"
    "v_smfmac_f32_16x16x64_bf16 v[20:23], v[44:47], v[232:239], v9\n\t"
    "s_waitcnt lgkmcnt(2)\n\t"
    "v_smfmac_f32_16x16x64_bf16 v[16:19], v[40:43], v[24:31], v9\n\t"
    "s_waitcnt lgkmcnt(0)\n\t"
    "v_smfmac_f32_16x16x64_bf16 v[20:23], v[40:43], v[32:39], v9\n\t"
    "v_smfmac_f32_16x16x64_bf16 v[16:19], v[48:51], v[240:247], v9\n\t"
    "v_smfmac_f32_16x16x64_bf16 v[20:23], v[48:51], v[248:255], v9\n\t"
    "v_xor_b32 v7, 64, v12\n\t"
    "v_xor_b32 v8, 0x80, v12\n\t"
    "v_xor_b32 v6, 0xc0, v12\n\t"
    "ds_read_b128 v[24:27], v12 offset:16384\n\t"
    "ds_read_b128 v[28:31], v12 offset:20480\n\t"
    "ds_read_b128 v[32:35], v7 offset:16384\n\t"
    "ds_read_b128 v[36:39], v7 offset:20480\n\t"
    "v_permlane16_swap_b32 v16, v18\n\t"
    "v_permlane16_swap_b32 v17, v19\n\t"
    "v_permlane16_swap_b32 v20, v22\n\t"
    "v_permlane16_swap_b32 v21, v23\n\t"
    "v_add_f32 v16, v16, v18\n\t"
    "v_add_f32 v17, v17, v19\n\t"
    "v_add_f32 v20, v20, v22\n\t"
    "v_mul_f32 v16, %[lsc], v16\n\t"
    "v_mul_f32 v17, %[lsc], v17\n\t"
    "v_cvt_pk_bf16_f32 v16, v16, v17\n\t"
    "v_add_f32 v21, v21, v23\n\t"
    "ds_write_b32 v10, v16 offset:33792\n\t"
    "v_mul_f32 v20, %[lsc], v20\n\t"
    "v_mul_f32 v21, %[lsc], v21\n\t"
    "v_cvt_pk_bf16_f32 v20, v20, v21\n\t"
    "ds_write_b32 v10, v20 offset:34048\n\t"
    "ds_read_b128 v[40:43], v8 offset:16384\n\t"
    "ds_read_b128 v[44:47], v8 offset:20480\n\t"
    "ds_read_b128 v[48:51], v6 offset:16384\n\t"
    "ds_read_b128 v[52:55], v6 offset:20480\n\t"
    "s_waitcnt lgkmcnt(4)\n\t"
    "s_barrier\n\t"
    "ds_read_b64_tr_b16 v[16:17], v11 offset:32768\n\t"
    "ds_read_b64_tr_b16 v[18:19], v11 offset:32832\n\t"
    "ds_read_b64_tr_b16 v[20:21], v11 offset:33280\n\t"
    "ds_read_b64_tr_b16 v[22:23], v11 offset:33344\n\t"
    "ds_read_b64_tr_b16 v[56:57], v11 offset:33792\n\t"
    "ds_read_b64_tr_b16 v[58:59], v11 offset:33856\n\t"
    "ds_read_b64_tr_b16 v[60:61], v11 offset:34304\n\t"
    "ds_read_b64_tr_b16 v[62:63], v11 offset:34368\n\t"
    "v_mov_b32 v6, 0\n\t"
    "v_mov_b32 v7, 0\n\t"
    "v_mov_b32 v8, 0\n\t"
    "v_mov_b32 v9, 0\n\t"
    "s_mov_b32 exec_hi, 0\n\t"
    "global_load_dwordx4 v[6:9], v14, %[vs] sc1\n\t"
    "s_mov_b32 exec_hi, -1\n\t"
    "s_waitcnt lgkmcnt(6)\n\t"
    "v_mfma_f32_16x16x32_bf16 v[24:27], v[24:27], v[16:19], 0\n\t"
    "v_mfma_f32_16x16x32_bf16 v[28:31], v[28:31], v[16:19], 0\n\t"
    "s_waitcnt lgkmcnt(4)\n\t"
    "v_mfma_f32_16x16x32_bf16 v[24:27], v[32:35], v[20:23], v[24:27]\n\t"
    "v_mfma_f32_16x16x32_bf16 v[28:31], v[36:39], v[20:23], v[28:31]\n\t"
    "ds_read_b64_tr_b16 v[32:33], v13 offset:24576\n\t"
    "ds_read_b64_tr_b16 v[34:35], v13 offset:28672\n\t"
    "ds_read_b64_tr_b16 v[36:37], v13 offset:24584\n\t"
    "ds_read_b64_tr_b16 v[38:39], v13 offset:28680\n\t"
    "s_waitcnt lgkmcnt(6)\n\t"
    "v_mfma_f32_16x16x32_bf16 v[24:27], v[40:43], v[56:59], v[24:27]\n\t"
    "v_mfma_f32_16x16x32_bf16 v[28:31], v[44:47], v[56:59], v[28:31]\n\t"
    "s_waitcnt lgkmcnt(4)\n\t"
    "v_mfma_f32_16x16x32_bf16 v[24:27], v[48:51], v[60:63], v[24:27]\n\t"
    "v_mfma_f32_16x16x32_bf16 v[28:31], v[52:55], v[60:63], v[28:31]\n\t"
    "s_setprio 1\n\t"
    "v_mov_b32 v52, %[one]\n\t"
    "v_mov_b32 v53, %[one]\n\t"
    "v_mov_b32 v54, %[one]\n\t"
    "v_mov_b32 v55, %[one]\n\t"
    "s_nop 2\n\t"
    "v_max3_f32 v10, |v24|, |v25|, |v26|\n\t"
    "v_max3_f32 v40, |v27|, |v28|, |v29|\n\t"
    "v_max3_f32 v10, v10, |v30|, |v31|\n\t"
    "v_max_f32 v10, v10, v40\n\t"
    "v_cmp_nge_f32 vcc, %[big], v10\n\t"
    "s_mov_b32 %[bg], %[big]\n\t"
    "v_mov_b32 v10, 0\n\t"
    "s_cbranch_vccnz 12f\n\t"
    "13:\n\t"
    "v_exp_f32 v24, v24\n\t"
    "v_exp_f32 v25, v25\n\t"
    "v_exp_f32 v26, v26\n\t"
    "v_exp_f32 v27, v27\n\t"
    "v_exp_f32 v28, v28\n\t"
    "v_exp_f32 v29, v29\n\t"
    "v_exp_f32 v30, v30\n\t"
    "v_exp_f32 v31, v31\n\t"
    "v_cvt_pk_bf16_f32 v40, v24, v25\n\t"
    "v_cvt_pk_bf16_f32 v41, v26, v27\n\t"
    "v_cvt_pk_bf16_f32 v42, v28, v29\n\t"
    "v_cvt_pk_bf16_f32 v43, v30, v31\n\t"
    "s_setprio 3\n\t"
    "s_waitcnt lgkmcnt(0)\n\t"
    "s_nop 0\n\t"
    "v_mfma_f32_16x16x32_bf16 v[44:47], v[32:35], v[40:43], 0\n\t"
    "v_mfma_f32_16x16x32_bf16 v[48:51], v[36:39], v[40:43], 0\n\t"
    "v_mfma_f32_16x16x32_bf16 v[52:55], v[52:55], v[40:43], 0\n\t"
    "v_mov_b32 v12, 0\n\t"
    "v_mov_b32 v13, 0\n\t"
    "s_mov_b32 exec_hi, 0\n\t"
    "v_mov_b32 v12, %[one]\n\t"
    "v_mov_b32 v13, %[one]\n\t"
    "s_mov_b32 exec_hi, -1\n\t"
    "s_barrier\n\t"
    "ds_read_b64_tr_b16 v[24:25], v11 offset:34816\n\t"
    "ds_read_b64_tr_b16 v[26:27], v11 offset:34880\n\t"
    "ds_read_b64_tr_b16 v[28:29], v11 offset:35328\n\t"
    "ds_read_b64_tr_b16 v[30:31], v11 offset:35392\n\t"
    "ds_read_b64_tr_b16 v[32:33], v11 offset:35840\n\t"
    "ds_read_b64_tr_b16 v[34:35], v11 offset:35904\n\t"
    "ds_read_b64_tr_b16 v[36:37], v11 offset:36352\n\t"
    "ds_read_b64_tr_b16 v[38:39], v11 offset:36416\n\t"
    "s_waitcnt lgkmcnt(6)\n\t"
    "v_mfma_f32_16x16x32_bf16 v[40:43], v[24:27], v[16:19], 0\n\t"
    "s_waitcnt lgkmcnt(4)\n\t"
    "v_mfma_f32_16x16x32_bf16 v[40:43], v[28:31], v[20:23], v[40:43]\n\t"
    "s_waitcnt lgkmcnt(2)\n\t"
    "v_mfma_f32_16x16x32_bf16 v[40:43], v[32:35], v[56:59], v[40:43]\n\t"
    "s_waitcnt lgkmcnt(0)\n\t"
    "v_mfma_f32_16x16x32_bf16 v[40:43], v[36:39], v[60:63], v[40:43]\n\t"
    "5:\n\t"
    "s_waitcnt vmcnt(0)\n\t"
    "v_max3_u32 v11, v6, v7, v8\n\t"
    "v_max_u32 v11, v11, v9\n\t"
    "v_cmp_eq_u32 vcc, -1, v11\n\t"
    "s_cbranch_vccnz 4f\n\t"
    "6:\n\t"
    "s_nop 2\n\t"
    "v_exp_f32 v28, v40\n\t"
    "v_max3_f32 v11, v40, v41, v42\n\t"
    "v_exp_f32 v29, v41\n\t"
    "v_max_f32 v11, v11, v43\n\t"
    "v_exp_f32 v30, v42\n\t"
    "v_cmp_nge_f32 vcc, %[bg], v11\n\t"
    "v_exp_f32 v31, v43\n\t"
    "s_cbranch_vccnz 10f\n\t"
    "11:\n\t"
    "v_cvt_pk_bf16_f32 v24, v28, v29\n\t"
    "v_cvt_pk_bf16_f32 v25, v30, v31\n\t"
    "s_nop 1\n\t"
    "v_mfma_f32_16x16x16_bf16 v[52:55], v[12:13], v[24:25], v[52:55]\n\t"
    "v_mfma_f32_16x16x16_bf16 v[44:47], v[6:7], v[24:25], v[44:47]\n\t"
    "v_mfma_f32_16x16x16_bf16 v[48:51], v[8:9], v[24:25], v[48:51]\n\t"
    "s_nop 5\n\t"
    "v_rcp_f32 v26, v52\n\t"
    "s_nop 0\n\t"
    "v_pk_mul_f32 v[44:45], v[44:45], v[26:27] op_sel_hi:[1,0]\n\t"
    "v_pk_mul_f32 v[46:47], v[46:47], v[26:27] op_sel_hi:[1,0]\n\t"
    "v_pk_mul_f32 v[48:49], v[48:49], v[26:27] op_sel_hi:[1,0]\n\t"
    "v_pk_mul_f32 v[50:51], v[50:51], v[26:27] op_sel_hi:[1,0]\n\t"
    "v_cvt_pk_bf16_f32 v36, v44, v45\n\t"
    "v_cvt_pk_bf16_f32 v37, v46, v47\n\t"
    "v_cvt_pk_bf16_f32 v38, v48, v49\n\t"
    "v_cvt_pk_bf16_f32 v39, v50, v51\n\t"
    "v_min_u32 v36, -2, v36\n\t"
    "v_min_u32 v37, -2, v37\n\t"
    "v_min_u32 v38, -2, v38\n\t"
    "v_min_u32 v39, -2, v39\n\t"
    "global_store_dwordx4 v15, v[36:39], %[od] sc1\n\t"
    "s_barrier\n\t"
    "s_mov_b32 exec_hi, 0\n\t"
    "global_store_dwordx4 v14, v[6:9], %[kv] sc1\n\t"
    "s_mov_b32 exec_hi, -1\n\t"
    "s_nop 0\n\t"
    "s_waitcnt lgkmcnt(0)\n\t"
    "s_branch 9f\n\t"
    "1:\n\t"
    "s_add_u32 %[sp], %[sp], 1\n\t"
    "s_cmp_ge_u32 %[sp], %[cap]\n\t"
    "s_cbranch_scc1 7f\n\t"
    "global_load_dwordx4 v[40:43], v6, %[ag] sc1\n\t"
    "global_load_dwordx4 v[44:47], v6, %[ag] offset:1024 sc1\n\t"
    "s_waitcnt lgkmcnt(0)\n\t"
    "s_memtime %[tc]\n\t"
    "s_nop 7\n\t"
    "s_branch 3b\n\t"
    "7:\n\t"
    "s_mov_b32 %[cap], 0\n\t"
    "s_branch 2b\n\t"
    "20:\n\t"
    "s_barrier\n\t"
    "s_barrier\n\t"
    "s_barrier\n\t"
    "s_branch 9f\n\t"
    "4:\n\t"
    "s_add_u32 %[sp], %[sp], 0x10000\n\t"
    "s_lshr_b32 %[st], %[sp], 16\n\t"
    "s_cmp_ge_u32 %[st], %[cap]\n\t"
    "s_cbranch_scc1 8f\n\t"
    "s_mov_b32 exec_hi, 0\n\t"
    "global_load_dwordx4 v[6:9], v14, %[vs] sc1\n\t"
    "s_mov_b32 exec_hi, -1\n\t"
    "s_branch 5b\n\t"
    "8:\n\t"
    "s_mov_b32 %[cap], 0\n\t"
    "s_branch 6b\n\t"
    "10:\n\t"
    "v_max_f32 v11, v11, v10\n\t"
    "v_mov_b32 v26, v11\n\t"
    "s_nop 1\n\t"
    "v_permlane16_swap_b32 v11, v26\n\t"
    "v_max_f32 v11, v11, v26\n\t"
    "v_sub_f32 v28, v40, v11\n\t"
    "v_sub_f32 v29, v41, v11\n\t"
    "v_sub_f32 v30, v42, v11\n\t"
    "v_sub_f32 v31, v43, v11\n\t"
    "v_sub_f32 v11, v10, v11\n\t"
    "v_exp_f32 v11, v11\n\t"
    "v_exp_f32 v28, v28\n\t"
    "v_exp_f32 v29, v29\n\t"
    "v_exp_f32 v30, v30\n\t"
    "v_exp_f32 v31, v31\n\t"
    "v_mul_f32 v44, v44, v11\n\t"
    "v_mul_f32 v45, v45, v11\n\t"
    "v_mul_f32 v46, v46, v11\n\t"
    "v_mul_f32 v47, v47, v11\n\t"
    "v_mul_f32 v48, v48, v11\n\t"
    "v_mul_f32 v49, v49, v11\n\t"
    "v_mul_f32 v50, v50, v11\n\t"
    "v_mul_f32 v51, v51, v11\n\t"
    "v_mul_f32 v52, v52, v11\n\t"
    "v_mul_f32 v53, v53, v11\n\t"
    "v_mul_f32 v54, v54, v11\n\t"
    "v_mul_f32 v55, v55, v11\n\t"
    "s_branch 11b\n\t"
    "12:\n\t"
    "v_max3_f32 v10, v24, v25, v26\n\t"
    "v_max3_f32 v40, v27, v28, v29\n\t"
    "v_max3_f32 v10, v10, v30, v31\n\t"
    "v_max3_f32 v10, v10, v40, %[neg]\n\t"
    "v_mov_b32 v40, v10\n\t"
    "s_nop 1\n\t"
    "v_permlane16_swap_b32 v10, v40\n\t"
    "v_max_f32 v10, v10, v40\n\t"
    "v_mov_b32 v40, v10\n\t"
    "s_nop 1\n\t"
    "v_permlane32_swap_b32 v10, v40\n\t"
    "v_max_f32 v10, v10, v40\n\t"
    "v_sub_f32 v24, v24, v10\n\t"
    "v_sub_f32 v25, v25, v10\n\t"
    "v_sub_f32 v26, v26, v10\n\t"
    "v_sub_f32 v27, v27, v10\n\t"
    "v_sub_f32 v28, v28, v10\n\t"
    "v_sub_f32 v29, v29, v10\n\t"
    "v_sub_f32 v30, v30, v10\n\t"
    "v_sub_f32 v31, v31, v10\n\t"
    "s_mov_b32 %[bg], 0x7fc00000\n\t"
    "s_branch 13b\n\t"
    "9:"
    : [tc] "=&s"(tc), [sp] "=&s"(sp), [cap] "+s"(cap), [st] "=&s"(st), [bg] "=&s"(bg)
    : [ag] "s"(ag), [c44] "s"(0x4444u), [kv] "s"(kv), [vs] "s"(vs), [od] "s"(od), [od2] "s"(od + 262144), [neg] "s"(-1.0e38f), [lsc] "s"(LSC), [one] "s"(0x3F803F80u), [big] "s"(24.0f), "{v[64:95]}"(W.w0), "{v[96:127]}"(W.w1), "{v[128:159]}"(W.w2), "{v[160:191]}"(W.w3), "{v[192:223]}"(W.w4), "{v[224:255]}"(W.w5)
    : "v6", "v7", "v8", "v9", "v10", "v11", "v12", "v13", "v14", "v15", "v16", "v17", "v18", "v19", "v20", "v21", "v22", "v23", "v24", "v25", "v26", "v27", "v28", "v29", "v30", "v31", "v32", "v33", "v34", "v35", "v36", "v37", "v38", "v39", "v40", "v41", "v42", "v43", "v44", "v45", "v46", "v47", "v48", "v49", "v50", "v51", "v52", "v53", "v54", "v55", "v56", "v57", "v58", "v59", "v60", "v61", "v62", "v63", "vcc", "scc", "memory");
}
template <> __device__ __forceinline__ void qk_step<3>(const QKW& W, const char* ag, const char* vs, const char* od, const char* kv, unsigned& cap, unsigned& sp,
                                                        unsigned long long& tc, unsigned long long* ts, unsigned& d0, unsigned& d1) {
  unsigned st, bg;
  asm volatile("v_mbcnt_lo_u32_b32 v60, -1, 0\n\t"
    "v_mbcnt_hi_u32_b32 v60, -1, v60\n\t"
    "v_lshlrev_b32 v6, 4, v60\n\t"
    "s_mov_b32 %[sp], 0\n\t"
    "s_setprio 3\n\t"
    "global_load_dwordx4 v[40:43], v6, %[ag] sc1\n\t"
    "global_load_dwordx4 v[44:47], v6, %[ag] offset:1024 sc1\n\t"
    "s_memtime %[tc]\n\t"
    "v_add_u32 v7, 47104, v6\n\t"
    "v_add_u32 v8, 104448, v6\n\t"
    "ds_read_b128 v[24:27], v7 offset:0\n\t"
    "ds_read_b128 v[28:31], v7 offset:1024\n\t"
    "ds_read_b128 v[32:35], v7 offset:8192\n\t"
    "ds_read_b128 v[36:39], v7 offset:9216\n\t"
    "v_lshrrev_b32 v48, 4, v60\n\t"
    "v_and_b32 v49, 15, v60\n\t"
    "v_and_b32 v61, 4, v60\n\t"
    "v_mul_u32_u24 v61, 0x2aaa, v61\n\t"
    "v_lshrrev_b32 v9, 1, v60\n\t"
    "v_and_b32 v9, 2, v9\n\t"
    "v_or3_b32 v9, v9, v61, %[c44]\n\t"
    "v_add_u32 v14, 1536, v6\n\t"
    "v_lshlrev_b32 v61, 2, v48\n\t"
    "v_lshl_or_b32 v10, v49, 4, v61\n\t"
    "v_lshrrev_b32 v50, 2, v49\n\t"
    "v_and_b32 v51, 3, v60\n\t"
    "v_and_b32 v61, 1, v60\n\t"
    "v_lshlrev_b32 v61, 3, v61\n\t"
    "v_lshl_or_b32 v11, v50, 4, v61\n\t"
    "v_lshl_or_b32 v11, v48, 7, v11\n\t"
    "v_xor_b32 v12, v48, v49\n\t"
    "v_lshlrev_b32 v12, 4, v12\n\t"
    "v_lshl_or_b32 v12, v49, 8, v12\n\t"
    "v_lshl_or_b32 v50, v48, 2, v50\n\t"
    "v_or_b32 v51, 12, v51\n\t"
    "v_xor_b32 v51, v51, v50\n\t"
    "v_lshlrev_b32 v13, 4, v51\n\t"
    "v_lshl_or_b32 v13, v50, 8, v13\n\t"
    "v_and_b32 v61, 3, v60\n\t"
    "v_lshlrev_b32 v61, 4, v61\n\t"
    "v_lshl_or_b32 v15, v48, 6, v61\n\t"
    "v_and_b32 v61, 8, v60\n\t"
    "v_lshl_or_b32 v15, v61, 14, v15\n\t"
    "v_and_b32 v61, 4, v60\n\t"
    "v_lshl_or_b32 v15, v61, 8, v15\n\t"
    "v_add_u32 v15, 768, v15\n\t"
    "3:\n\t"
    "v_mov_b32 v16, 0\n\t"
    "v_mov_b32 v17, 0\n\t"
    "v_mov_b32 v18, 0\n\t"
    "v_mov_b32 v19, 0\n\t"
    "v_mov_b32 v20, 0\n\t"
    "v_mov_b32 v21, 0\n\t"
    "v_mov_b32 v22, 0\n\t"
    "v_mov_b32 v23, 0\n\t"
    "s_waitcnt vmcnt(0) lgkmcnt(0)\n\t"
    "14:\n\t"
    "ds_write_b128 v6, v[40:43] offset:6144\n\t"
    "ds_write_b128 v6, v[44:47] offset:7168\n\t"
    "v_smfmac_f32_16x16x64_bf16 v[16:19], v[40:43], v[160:167], v9\n\t"
    "v_max3_u32 v60, v40, v41, v42\n\t"
    "v_max3_u32 v61, v43, v44, v45\n\t"
    "v_smfmac_f32_16x16x64_bf16 v[20:23], v[40:43], v[168:175], v9\n\t"
    "v_max3_u32 v60, v60, v46, v47\n\t"
    "v_max_u32 v60, v60, v61\n\t"
    "v_cmp_eq_u32 vcc, -1, v60\n\t"
    "s_cbranch_vccnz 1f\n\t"
    "2:\n\t"
    "s_waitcnt lgkmcnt(0)\n\t"
    "s_cmp_eq_u64 %[kv], 0\n\t"
    "s_barrier\n\t"
    "s_cbranch_scc1 20f\n\t"
    "ds_read_b128 v[40:43], v6 offset:12288\n\t"
    "ds_read_b128 v[48:51], v6 offset:0\n\t"
    "v_smfmac_f32_16x16x64_bf16 v[16:19], v[44:47], v[176:183], v9\n\t"
    "ds_read_b128 v[52:55], v6 offset:1024\n\t"
    "ds_read_b128 v[56:59], v6 offset:2048\n\t"
    "v_smfmac_f32_16x16x64_bf16 v[20:23], v[44:47], v[184:191], v9\n\t"
    "ds_read_b128 v[60:63], v6 offset:13312\n\t"
    "ds_read_b128 v[44:47], v6 offset:3072\n\t"
    "s_waitcnt lgkmcnt(5)\n\t"
    "v_smfmac_f32_16x16x64_bf16 v[16:19], v[40:43], v[24:31], v9\n\t"
    "ds_read_b128 v[24:27], v7 offset:16384\n\t"
    "ds_read_b128 v[28:31], v7 offset:17408\n\t"
    "v_smfmac_f32_16x16x64_bf16 v[20:23], v[40:43], v[32:39], v9\n\t"
    "ds_read_b128 v[32:35], v7 offset:24576\n\t"
    "ds_read_b128 v[36:39], v7 offset:25600\n\t"
    "ds_read_b128 v[40:43], v6 offset:4096\n\t"
    "s_waitcnt lgkmcnt(9)\n\t"
    "v_smfmac_f32_16x16x64_bf16 v[16:19], v[48:51], v[64:71], v9\n\t"
    "v_smfmac_f32_16x16x64_bf16 v[20:23], v[48:51], v[72:79], v9\n\t"
    "ds_read_b128 v[48:51], v6 offset:5120\n\t"
    "s_waitcnt lgkmcnt(9)\n\t"
    "v_smfmac_f32_16x16x64_bf16 v[16:19], v[52:55], v[80:87], v9\n\t"
    "v_smfmac_f32_16x16x64_bf16 v[20:23], v[52:55], v[88:95], v9\n\t"
    "ds_read_b128 v[52:55], v6 offset:14336\n\t"
    "s_waitcnt lgkmcnt(9)\n\t"
    "v_smfmac_f32_16x16x64_bf16 v[16:19], v[56:59], v[96:103], v9\n\t"
    "v_smfmac_f32_16x16x64_bf16 v[20:23], v[56:59], v[104:111], v9\n\t"
    "ds_read_b128 v[56:59], v6 offset:8192\n\t"
    "s_waitcnt lgkmcnt(6)\n\t"
    "v_smfmac_f32_16x16x64_bf16 v[16:19], v[60:63], v[24:31], v9\n\t"
    "ds_read_b128 v[24:27], v8 offset:0\n\t"
    "ds_read_b128 v[28:31], v8 offset:1024\n\t"
    "s_waitcnt lgkmcnt(6)\n\t"
    "v_smfmac_f32_16x16x64_bf16 v[20:23], v[60:63], v[32:39], v9\n\t"
    "ds_read_b128 v[32:35], v8 offset:8192\n\t"
    "ds_read_b128 v[36:39], v8 offset:9216\n\t"
    "ds_read_b128 v[60:63], v6 offset:9216\n\t"
    "v_smfmac_f32_16x16x64_bf16 v[16:19], v[44:47], v[112:119], v9\n\t"
    "v_smfmac_f32_16x16x64_bf16 v[20:23], v[44:47], v[120:127], v9\n\t"
    "ds_read_b128 v[44:47], v6 offset:10240\n\t"
    "s_waitcnt lgkmcnt(9)\n\t"
    "v_smfmac_f32_16x16x64_bf16 v[16:19], v[40:43], v[128:135], v9\n\t"
    "v_smfmac_f32_16x16x64_bf16 v[20:23], v[40:43], v[136:143], v9\n\t"
    "ds_read_b128 v[40:43], v6 offset:15360\n\t"
    "s_waitcnt lgkmcnt(9)\n\t"
    "v_smfmac_f32_16x16x64_bf16 v[16:19], v[48:51], v[144:151], v9\n\t"
    "v_smfmac_f32_16x16x64_bf16 v[20:23], v[48:51], v[152:159], v9\n\t"
    "ds_read_b128 v[48:51], v6 offset:11264\n\t"
    "s_waitcnt lgkmcnt(6)\n\t"
    "v_smfmac_f32_16x16x64_bf16 v[16:19], v[52:55], v[24:31], v9\n\t"
    "ds_read_b128 v[24:27], v8 offset:16384\n\t"
    "ds_read_b128 v[28:31], v8 offset:17408\n\t"
    "s_waitcnt lgkmcnt(6)\n\t"
    "v_smfmac_f32_16x16x64_bf16 v[20:23], v[52:55], v[32:39], v9\n\t"
    "ds_read_b128 v[32:35], v8 offset:24576\n\t"
    "ds_read_b128 v[36:39], v8 offset:25600\n\t"
    "v_smfmac_f32_16x16x64_bf16 v[16:19], v[56:59], v[192:199], v9\n\t"
    "v_smfmac_f32_16x16x64_bf16 v[20:23], v[56:59], v[200:207], v9\n\t"
    "s_waitcnt lgkmcnt(7)\n\t"
    "v_smfmac_f32_16x16x64_bf16 v[16:19], v[60:63], v[208:215], v9\n\t"
    "v_smfmac_f32_16x16x64_bf16 v[20:23], v[60:63], v[216:223], v9\n\t"
    "s_waitcnt lgkmcnt(6)\n\t"
    "v_smfmac_f32_16x16x64_bf16 v[16:19], v[44:47], v[224:231], v9\n\t"
    "v_smfmac_f32_16x16x64_bf16 v[20:23], v[44:47], v[232:239], v9\n\t"
    "s_waitcnt lgkmcnt(2)\n\t"
    "v_smfmac_f32_16x16x64_bf16 v[16:19], v[40:43], v[24:31], v9\n\t"
    "s_waitcnt lgkmcnt(0)\n\t"
    "v_smfmac_f32_16x16x64_bf16 v[20:23], v[40:43], v[32:39], v9\n\t"
    "v_smfmac_f32_16x16x64_bf16 v[16:19], v[48:51], v[240:247], v9\n\t"
    "v_smfmac_f32_16x16x64_bf16 v[20:23], v[48:51], v[248:255], v9\n\t"
    "v_xor_b32 v7, 64, v12\n\t"
    "v_xor_b32 v8, 0x80, v12\n\t"
    "v_xor_b32 v6, 0xc0, v12\n\t"
    "ds_read_b128 v[24:27], v12 offset:16384\n\t"
    "ds_read_b128 v[28:31], v12 offset:20480\n\t"
    "ds_read_b128 v[32:35], v7 offset:16384\n\t"
    "ds_read_b128 v[36:39], v7 offset:20480\n\t"
    "v_permlane16_swap_b32 v16, v18\n\t"
    "v_permlane16_swap_b32 v17, v19\n\t"
    "v_permlane16_swap_b32 v20, v22\n\t"
    "v_permlane16_swap_b32 v21, v23\n\t"
    "v_add_f32 v16, v16, v18\n\t"
    "v_add_f32 v17, v17, v19\n\t"
    "v_add_f32 v20, v20, v22\n\t"
    "v_mul_f32 v16, %[lsc], v16\n\t"
    "v_mul_f32 v17, %[lsc], v17\n\t"
    "v_cvt_pk_bf16_f32 v16, v16, v17\n\t"
    "v_add_f32 v21, v21, v23\n\t"
    "ds_write_b32 v10, v16 offset:34304\n\t"
    "v_mul_f32 v20, %[lsc], v20\n\t"
    "v_mul_f32 v21, %[lsc], v21\n\t"
    "v_cvt_pk_bf16_f32 v20, v20, v21\n\t"
    "ds_write_b32 v10, v20 offset:34560\n\t"
    "ds_read_b128 v[40:43], v8 offset:16384\n\t"
    "ds_read_b128 v[44:47], v8 offset:20480\n\t"
    "ds_read_b128 v[48:51], v6 offset:16384\n\t"
    "ds_read_b128 v[52:55], v6 offset:20480\n\t"
    "s_waitcnt lgkmcnt(4)\n\t"
    "s_barrier\n\t"
    "ds_read_b64_tr_b16 v[16:17], v11 offset:32768\n\t"
    "ds_read_b64_tr_b16 v[18:19], v11 offset:32832\n\t"
    "ds_read_b64_tr_b16 v[20:21], v11 offset:33280\n\t"
    "ds_read_b64_tr_b16 v[22:23], v11 offset:33344\n\t"
    "ds_read_b64_tr_b16 v[56:57], v11 offset:33792\n\t"
    "ds_read_b64_tr_b16 v[58:59], v11 offset:33856\n\t"
    "ds_read_b64_tr_b16 v[60:61], v11 offset:34304\n\t"
    "ds_read_b64_tr_b16 v[62:63], v11 offset:34368\n\t"
    "v_mov_b32 v6, 0\n\t"
    "v_mov_b32 v7, 0\n\t"
    "v_mov_b32 v8, 0\n\t"
    "v_mov_b32 v9, 0\n\t"
    "s_mov_b32 exec_hi, 0\n\t"
    "global_load_dwordx4 v[6:9], v14, %[vs] sc1\n\t"
    "s_mov_b32 exec_hi, -1\n\t"
    "s_waitcnt lgkmcnt(6)\n\t"
    "v_mfma_f32_16x16x32_bf16 v[24:27], v[24:27], v[16:19], 0\n\t"
    "v_mfma_f32_16x16x32_bf16 v[28:31], v[28:31], v[16:19], 0\n\t"
    "s_waitcnt lgkmcnt(4)\n\t"
    "v_mfma_f32_16x16x32_bf16 v[24:27], v[32:35], v[20:23], v[24:27]\n\t"
    "v_mfma_f32_16x16x32_bf16 v[28:31], v[36:39], v[20:23], v[28:31]\n\t"
    "ds_read_b64_tr_b16 v[32:33], v13 offset:24576\n\t"
    "ds_read_b64_tr_b16 v[34:35], v13 offset:28672\n\t"
    "ds_read_b64_tr_b16 v[36:37], v13 offset:24584\n\t"
    "ds_read_b64_tr_b16 v[38:39], v13 offset:28680\n\t"
    "s_waitcnt lgkmcnt(6)\n\t"
    "v_mfma_f32_16x16x32_bf16 v[24:27], v[40:43], v[56:59], v[24:27]\n\t"
    "v_mfma_f32_16x16x32_bf16 v[28:31], v[44:47], v[56:59], v[28:31]\n\t"
    "s_waitcnt lgkmcnt(4)\n\t"
    "v_mfma_f32_16x16x32_bf16 v[24:27], v[48:51], v[60:63], v[24:27]\n\t"
    "v_mfma_f32_16x16x32_bf16 v[28:31], v[52:55], v[60:63], v[28:31]\n\t"
    "s_setprio 1\n\t"
    "v_mov_b32 v52, %[one]\n\t"
    "v_mov_b32 v53, %[one]\n\t"
    "v_mov_b32 v54, %[one]\n\t"
    "v_mov_b32 v55, %[one]\n\t"
    "s_nop 2\n\t"
    "v_max3_f32 v10, |v24|, |v25|, |v26|\n\t"
    "v_max3_f32 v40, |v27|, |v28|, |v29|\n\t"
    "v_max3_f32 v10, v10, |v30|, |v31|\n\t"
    "v_max_f32 v10, v10, v40\n\t"
    "v_cmp_nge_f32 vcc, %[big], v10\n\t"
    "s_mov_b32 %[bg], %[big]\n\t"
    "v_mov_b32 v10, 0\n\t"
    "s_cbranch_vccnz 12f\n\t"
    "13:\n\t"
    "v_exp_f32 v24, v24\n\t"
    "v_exp_f32 v25, v25\n\t"
    "v_exp_f32 v26, v26\n\t"
    "v_exp_f32 v27, v27\n\t"
    "v_exp_f32 v28, v28\n\t"
    "v_exp_f32 v29, v29\n\t"
    "v_exp_f32 v30, v30\n\t"
    "v_exp_f32 v31, v31\n\t"
    "v_cvt_pk_bf16_f32 v40, v24, v25\n\t"
    "v_cvt_pk_bf16_f32 v41, v26, v27\n\t"
    "v_cvt_pk_bf16_f32 v42, v28, v29\n\t"
    "v_cvt_pk_bf16_f32 v43, v30, v31\n\t"
    "s_setprio 3\n\t"
    "s_waitcnt lgkmcnt(0)\n\t"
    "s_nop 0\n\t"
    "v_mfma_f32_16x16x32_bf16 v[44:47], v[32:35], v[40:43], 0\n\t"
    "v_mfma_f32_16x16x32_bf16 v[48:51], v[36:39], v[40:43], 0\n\t"
    "v_mfma_f32_16x16x32_bf16 v[52:55], v[52:55], v[40:43], 0\n\t"
    "v_mov_b32 v12, 0\n\t"
    "v_mov_b32 v13, 0\n\t"
    "s_mov_b32 exec_hi, 0\n\t"
    "v_mov_b32 v12, %[one]\n\t"
    "v_mov_b32 v13, %[one]\n\t"
    "s_mov_b32 exec_hi, -1\n\t"
    "s_barrier\n\t"
    "ds_read_b64_tr_b16 v[24:25], v11 offset:34816\n\t"
    "ds_read_b64_tr_b16 v[26:27], v11 offset:34880\n\t"
    "ds_read_b64_tr_b16 v[28:29], v11 offset:35328\n\t"
    "ds_read_b64_tr_b16 v[30:31], v11 offset:35392\n\t"
    "ds_read_b64_tr_b16 v[32:33], v11 offset:35840\n\t"
    "ds_read_b64_tr_b16 v[34:35], v11 offset:35904\n\t"
    "ds_read_b64_tr_b16 v[36:37], v11 offset:36352\n\t"
    "ds_read_b64_tr_b16 v[38:39], v11 offset:36416\n\t"
    "s_waitcnt lgkmcnt(6)\n\t"
    "v_mfma_f32_16x16x32_bf16 v[40:43], v[24:27], v[16:19], 0\n\t"
    "s_waitcnt lgkmcnt(4)\n\t"
    "v_mfma_f32_16x16x32_bf16 v[40:43], v[28:31], v[20:23], v[40:43]\n\t"
    "s_waitcnt lgkmcnt(2)\n\t"
    "v_mfma_f32_16x16x32_bf16 v[40:43], v[32:35], v[56:59], v[40:43]\n\t"
    "s_waitcnt lgkmcnt(0)\n\t"
    "v_mfma_f32_16x16x32_bf16 v[40:43], v[36:39], v[60:63], v[40:43]\n\t"
    "5:\n\t"
    "s_waitcnt vmcnt(0)\n\t"
    "v_max3_u32 v11, v6, v7, v8\n\t"
    "v_max_u32 v11, v11, v9\n\t"
    "v_cmp_eq_u32 vcc, -1, v11\n\t"
    "s_cbranch_vccnz 4f\n\t"
    "6:\n\t"
    "s_nop 2\n\t"
    "v_exp_f32 v28, v40\n\t"
    "v_max3_f32 v11, v40, v41, v42\n\t"
    "v_exp_f32 v29, v41\n\t"
    "v_max_f32 v11, v11, v43\n\t"
    "v_exp_f32 v30, v42\n\t"
    "v_cmp_nge_f32 vcc, %[bg], v11\n\t"
    "v_exp_f32 v31, v43\n\t"
    "s_cbranch_vccnz 10f\n\t"
    "11:\n\t"
    "v_cvt_pk_bf16_f32 v24, v28, v29\n\t"
    "v_cvt_pk_bf16_f32 v25, v30, v31\n\t"
    "s_nop 1\n\t"
    "v_mfma_f32_16x16x16_bf16 v[52:55], v[12:13], v[24:25], v[52:55]\n\t"
    "v_mfma_f32_16x16x16_bf16 v[44:47], v[6:7], v[24:25], v[44:47]\n\t"
    "v_mfma_f32_16x16x16_bf16 v[48:51], v[8:9], v[24:25], v[48:51]\n\t"
    "s_nop 5\n\t"
    "v_rcp_f32 v26, v52\n\t"
    "s_nop 0\n\t"
    "v_pk_mul_f32 v[44:45], v[44:45], v[26:27] op_sel_hi:[1,0]\n\t"
    "v_pk_mul_f32 v[46:47], v[46:47], v[26:27] op_sel_hi:[1,0]\n\t"
    "v_pk_mul_f32 v[48:49], v[48:49], v[26:27] op_sel_hi:[1,0]\n\t"
    "v_pk_mul_f32 v[50:51], v[50:51], v[26:27] op_sel_hi:[1,0]\n\t"
    "v_cvt_pk_bf16_f32 v36, v44, v45\n\t"
    "v_cvt_pk_bf16_f32 v37, v46, v47\n\t"
    "v_cvt_pk_bf16_f32 v38, v48, v49\n\t"
    "v_cvt_pk_bf16_f32 v39, v50, v51\n\t"
    "v_min_u32 v36, -2, v36\n\t"
    "v_min_u32 v37, -2, v37\n\t"
    "v_min_u32 v38, -2, v38\n\t"
    "v_min_u32 v39, -2, v39\n\t"
    "global_store_dwordx4 v15, v[36:39], %[od] sc1\n\t"
    "s_barrier\n\t"
    "s_mov_b32 exec_hi, 0\n\t"
    "global_store_dwordx4 v14, v[6:9], %[kv] sc1\n\t"
    "s_mov_b32 exec_hi, -1\n\t"
    "s_nop 0\n\t"
    "s_waitcnt lgkmcnt(0)\n\t"
    "s_branch 9f\n\t"
    "1:\n\t"
    "s_add_u32 %[sp], %[sp], 1\n\t"
    "s_cmp_ge_u32 %[sp], %[cap]\n\t"
    "s_cbranch_scc1 7f\n\t"
    "global_load_dwordx4 v[40:43], v6, %[ag] sc1\n\t"
    "global_load_dwordx4 v[44:47], v6, %[ag] offset:1024 sc1\n\t"
    "s_waitcnt lgkmcnt(0)\n\t"
    "s_memtime %[tc]\n\t"
    "s_nop 7\n\t"
    "s_branch 3b\n\t"
    "7:\n\t"
    "s_mov_b32 %[cap], 0\n\t"
    "s_branch 2b\n\t"
    "20:\n\t"
    "s_barrier\n\t"
    "s_barrier\n\t"
    "s_barrier\n\t"
    "s_branch 9f\n\t"
    "4:\n\t"
    "s_add_u32 %[sp], %[sp], 0x10000\n\t"
    "s_lshr_b32 %[st], %[sp], 16\n\t"
    "s_cmp_ge_u32 %[st], %[cap]\n\t"
    "s_cbranch_scc1 8f\n\t"
    "s_mov_b32 exec_hi, 0\n\t"
    "global_load_dwordx4 v[6:9], v14, %[vs] sc1\n\t"
    "s_mov_b32 exec_hi, -1\n\t"
    "s_branch 5b\n\t"
    "8:\n\t"
    "s_mov_b32 %[cap], 0\n\t"
    "s_branch 6b\n\t"
    "10:\n\t"
    "v_max_f32 v11, v11, v10\n\t"
    "v_mov_b32 v26, v11\n\t"
    "s_nop 1\n\t"
    "v_permlane16_swap_b32 v11, v26\n\t"
    "v_max_f32 v11, v11, v26\n\t"
    "v_sub_f32 v28, v40, v11\n\t"
    "v_sub_f32 v29, v41, v11\n\t"
    "v_sub_f32 v30, v42, v11\n\t"
    "v_sub_f32 v31, v43, v11\n\t"
    "v_sub_f32 v11, v10, v11\n\t"
    "v_exp_f32 v11, v11\n\t"
    "v_exp_f32 v28, v28\n\t"
    "v_exp_f32 v29, v29\n\t"
    "v_exp_f32 v30, v30\n\t"
    "v_exp_f32 v31, v31\n\t"
    "v_mul_f32 v44, v44, v11\n\t"
    "v_mul_f32 v45, v45, v11\n\t"
    "v_mul_f32 v46, v46, v11\n\t"
    "v_mul_f32 v47, v47, v11\n\t"
    "v_mul_f32 v48, v48, v11\n\t"
    "v_mul_f32 v49, v49, v11\n\t"
    "v_mul_f32 v50, v50, v11\n\t"
    "v_mul_f32 v51, v51, v11\n\t"
    "v_mul_f32 v52, v52, v11\n\t"
    "v_mul_f32 v53, v53, v11\n\t"
    "v_mul_f32 v54, v54, v11\n\t"
    "v_mul_f32 v55, v55, v11\n\t"
    "s_branch 11b\n\t"
    "12:\n\t"
    "v_max3_f32 v10, v24, v25, v26\n\t"
    "v_max3_f32 v40, v27, v28, v29\n\t"
    "v_max3_f32 v10, v10, v30, v31\n\t"
    "v_max3_f32 v10, v10, v40, %[neg]\n\t"
    "v_mov_b32 v40, v10\n\t"
    "s_nop 1\n\t"
    "v_permlane16_swap_b32 v10, v40\n\t"
    "v_max_f32 v10, v10, v40\n\t"
    "v_mov_b32 v40, v10\n\t"
    "s_nop 1\n\t"
    "v_permlane32_swap_b32 v10, v40\n\t"
    "v_max_f32 v10, v10, v40\n\t"
    "v_sub_f32 v24, v24, v10\n\t"
    "v_sub_f32 v25, v25, v10\n\t"
    "v_sub_f32 v26, v26, v10\n\t"
    "v_sub_f32 v27, v27, v10\n\t"
    "v_sub_f32 v28, v28, v10\n\t"
    "v_sub_f32 v29, v29, v10\n\t"
    "v_sub_f32 v30, v30, v10\n\t"
    "v_sub_f32 v31, v31, v10\n\t"
    "s_mov_b32 %[bg], 0x7fc00000\n\t"
    "s_branch 13b\n\t"
    "9:"
    : [tc] "=&s"(tc), [sp] "=&s"(sp), [cap] "+s"(cap), [st] "=&s"(st), [bg] "=&s"(bg)
    : [ag] "s"(ag), [c44] "s"(0x4444u), [kv] "s"(kv), [vs] "s"(vs), [od] "s"(od), [od2] "s"(od + 262144), [neg] "s"(-1.0e38f), [lsc] "s"(LSC), [one] "s"(0x3F803F80u), [big] "s"(24.0f), "{v[64:95]}"(W.w0), "{v[96:127]}"(W.w1), "{v[128:159]}"(W.w2), "{v[160:191]}"(W.w3), "{v[192:223]}"(W.w4), "{v[224:255]}"(W.w5)
    : "v6", "v7", "v8", "v9", "v10", "v11", "v12", "v13", "v14", "v15", "v16", "v17", "v18", "v19", "v20", "v21", "v22", "v23", "v24", "v25", "v26", "v27", "v28", "v29", "v30", "v31", "v32", "v33", "v34", "v35", "v36", "v37", "v38", "v39", "v40", "v41", "v42", "v43", "v44", "v45", "v46", "v47", "v48", "v49", "v50", "v51", "v52", "v53", "v54", "v55", "v56", "v57", "v58", "v59", "v60", "v61", "v62", "v63", "vcc", "scc", "memory");
}
template <> __device__ __forceinline__ void qk_step<4>(const QKW& W, const char* ag, const char* vs, const char* od, const char* kv, unsigned& cap, unsigned& sp,
                                                        unsigned long long& tc, unsigned long long* ts, unsigned& d0, unsigned& d1) {
  unsigned st, bg;
  asm volatile("v_mbcnt_lo_u32_b32 v52, -1, 0\n\t"
    "v_mbcnt_hi_u32_b32 v52, -1, v52\n\t"
    "v_lshlrev_b32 v6, 4, v52\n\t"
    "s_mov_b32 %[sp], 0\n\t"
    "global_load_dwordx4 v[36:39], v6, %[ag] sc1\n\t"
    "global_load_dwordx4 v[40:43], v6, %[ag] offset:1024 sc1\n\t"
    "s_memtime %[tc]\n\t"
    "v_add_u32 v7, 73728, v6\n\t"
    "v_add_u32 v8, 131072, v6\n\t"
    "ds_read_b128 v[20:23], v7 offset:8192\n\t"
    "ds_read_b128 v[24:27], v7 offset:9216\n\t"
    "ds_read_b128 v[28:31], v7 offset:0\n\t"
    "ds_read_b128 v[32:35], v7 offset:1024\n\t"
    "v_lshrrev_b32 v44, 4, v52\n\t"
    "v_and_b32 v45, 15, v52\n\t"
    "v_and_b32 v53, 4, v52\n\t"
    "v_mul_u32_u24 v53, 0x2aaa, v53\n\t"
    "v_lshrrev_b32 v9, 1, v52\n\t"
    "v_and_b32 v9, 2, v9\n\t"
    "v_or3_b32 v9, v9, v53, %[c44]\n\t"
    "v_lshlrev_b32 v53, 2, v44\n\t"
    "v_lshl_or_b32 v10, v45, 4, v53\n\t"
    "3:\n\t"
    "v_mov_b32 v12, 0\n\t"
    "v_mov_b32 v13, 0\n\t"
    "v_mov_b32 v14, 0\n\t"
    "v_mov_b32 v15, 0\n\t"
    "v_mov_b32 v16, 0\n\t"
    "v_mov_b32 v17, 0\n\t"
    "v_mov_b32 v18, 0\n\t"
    "v_mov_b32 v19, 0\n\t"
    "s_waitcnt vmcnt(0) lgkmcnt(0)\n\t"
    "14:\n\t"
    "ds_write_b128 v6, v[36:39] offset:8192\n\t"
    "ds_write_b128 v6, v[40:43] offset:9216\n\t"
    "v_smfmac_f32_16x16x64_bf16 v[12:15], v[36:39], v[128:135], v9\n\t"
    "v_max3_u32 v52, v36, v37, v38\n\t"
    "v_max3_u32 v53, v39, v40, v41\n\t"
    "v_smfmac_f32_16x16x64_bf16 v[16:19], v[36:39], v[136:143], v9\n\t"
    "v_max3_u32 v52, v52, v42, v43\n\t"
    "v_max_u32 v52, v52, v53\n\t"
    "v_cmp_eq_u32 vcc, -1, v52\n\t"
    "s_cbranch_vccnz 1f\n\t"
    "2:\n\t"
    "s_waitcnt lgkmcnt(0)\n\t"
    "s_barrier\n\t"
    "s_memrealtime s[36:37]\n\t"
    "ds_read_b128 v[36:39], v6 offset:0\n\t"
    "ds_read_b128 v[44:47], v6 offset:4096\n\t"
    "v_smfmac_f32_16x16x64_bf16 v[12:15], v[40:43], v[144:151], v9\n\t"
    "ds_read_b128 v[48:51], v6 offset:5120\n\t"
    "ds_read_b128 v[52:55], v6 offset:6144\n\t"
    "v_smfmac_f32_16x16x64_bf16 v[16:19], v[40:43], v[152:159], v9\n\t"
    "ds_read_b128 v[40:43], v6 offset:1024\n\t"
    "s_waitcnt lgkmcnt(0)\n\t"
    "s_mov_b32 s38, s36\n\t"
    "s_min_u32 s37, %[d0], %[d1]\n\t"
    "s_add_u32 s36, s36, s37\n\t"
    "s_and_b32 s36, s36, 0xffffff\n\t"
    "s_or_b32 s36, s36, %[sn]\n\t"
    "v_lshlrev_b32 v11, 10, v6\n\t"
    "v_mov_b32 v5, s36\n\t"
    "s_mov_b64 exec, 15\n\t"
    "global_store_dword v11, v5, %[vs] offset:-4096 sc1\n\t"
    "s_mov_b64 exec, -1\n\t"
    "s_sleep 0\n\t"
    "v_smfmac_f32_16x16x64_bf16 v[12:15], v[36:39], v[56:63], v9\n\t"
    "v_smfmac_f32_16x16x64_bf16 v[16:19], v[36:39], v[28:35], v9\n\t"
    "ds_read_b128 v[28:31], v7 offset:16384\n\t"
    "ds_read_b128 v[32:35], v7 offset:17408\n\t"
    "ds_read_b128 v[36:39], v6 offset:7168\n\t"
    "v_smfmac_f32_16x16x64_bf16 v[12:15], v[44:47], v[64:71], v9\n\t"
    "v_smfmac_f32_16x16x64_bf16 v[16:19], v[44:47], v[72:79], v9\n\t"
    "ds_read_b128 v[44:47], v6 offset:10240\n\t"
    "v_smfmac_f32_16x16x64_bf16 v[12:15], v[48:51], v[80:87], v9\n\t"
    "v_smfmac_f32_16x16x64_bf16 v[16:19], v[48:51], v[88:95], v9\n\t"
    "ds_read_b128 v[48:51], v6 offset:11264\n\t"
    "v_smfmac_f32_16x16x64_bf16 v[12:15], v[52:55], v[96:103], v9\n\t"
    "v_smfmac_f32_16x16x64_bf16 v[16:19], v[52:55], v[104:111], v9\n\t"
    "ds_read_b128 v[52:55], v6 offset:2048\n\t"
    "v_smfmac_f32_16x16x64_bf16 v[12:15], v[40:43], v[20:27], v9\n\t"
    "ds_read_b128 v[20:23], v8 offset:0\n\t"
    "ds_read_b128 v[24:27], v8 offset:1024\n\t"
    "s_waitcnt lgkmcnt(6)\n\t"
    "v_smfmac_f32_16x16x64_bf16 v[16:19], v[40:43], v[28:35], v9\n\t"
    "ds_read_b128 v[28:31], v8 offset:8192\n\t"
    "ds_read_b128 v[32:35], v8 offset:9216\n\t"
    "ds_read_b128 v[40:43], v6 offset:12288\n\t"
    "s_waitcnt lgkmcnt(8)\n\t"
    "v_smfmac_f32_16x16x64_bf16 v[12:15], v[36:39], v[112:119], v9\n\t"
    "v_smfmac_f32_16x16x64_bf16 v[16:19], v[36:39], v[120:127], v9\n\t"
    "ds_read_b128 v[36:39], v6 offset:13312\n\t"
    "s_waitcnt lgkmcnt(8)\n\t"
    "v_smfmac_f32_16x16x64_bf16 v[12:15], v[44:47], v[160:167], v9\n\t"
    "v_smfmac_f32_16x16x64_bf16 v[16:19], v[44:47], v[168:175], v9\n\t"
    "ds_read_b128 v[44:47], v6 offset:14336\n\t"
    "s_barrier\n\t"
    "s_setprio 2\n\t"
    "s_waitcnt lgkmcnt(8)\n\t"
    "v_smfmac_f32_16x16x64_bf16 v[12:15], v[48:51], v[176:183], v9\n\t"
    "v_smfmac_f32_16x16x64_bf16 v[16:19], v[48:51], v[184:191], v9\n\t"
    "ds_read_b128 v[48:51], v6 offset:3072\n\t"
    "s_waitcnt lgkmcnt(6)\n\t"
    "v_smfmac_f32_16x16x64_bf16 v[12:15], v[52:55], v[20:27], v9\n\t"
    "ds_read_b128 v[20:23], v8 offset:16384\n\t"
    "ds_read_b128 v[24:27], v8 offset:17408\n\t"
    "s_waitcnt lgkmcnt(6)\n\t"
    "v_smfmac_f32_16x16x64_bf16 v[16:19], v[52:55], v[28:35], v9\n\t"
    "ds_read_b128 v[28:31], v8 offset:24576\n\t"
    "ds_read_b128 v[32:35], v8 offset:25600\n\t"
    "ds_read_b128 v[52:55], v6 offset:15360\n\t"
    "s_waitcnt lgkmcnt(8)\n\t"
    "v_smfmac_f32_16x16x64_bf16 v[12:15], v[40:43], v[192:199], v9\n\t"
    "v_smfmac_f32_16x16x64_bf16 v[16:19], v[40:43], v[200:207], v9\n\t"
    "s_waitcnt lgkmcnt(7)\n\t"
    "v_smfmac_f32_16x16x64_bf16 v[12:15], v[36:39], v[208:215], v9\n\t"
    "v_smfmac_f32_16x16x64_bf16 v[16:19], v[36:39], v[216:223], v9\n\t"
    "s_waitcnt lgkmcnt(6)\n\t"
    "v_smfmac_f32_16x16x64_bf16 v[12:15], v[44:47], v[224:231], v9\n\t"
    "v_smfmac_f32_16x16x64_bf16 v[16:19], v[44:47], v[232:239], v9\n\t"
    "s_waitcnt lgkmcnt(3)\n\t"
    "v_smfmac_f32_16x16x64_bf16 v[12:15], v[48:51], v[20:27], v9\n\t"
    "s_waitcnt lgkmcnt(1)\n\t"
    "v_smfmac_f32_16x16x64_bf16 v[16:19], v[48:51], v[28:35], v9\n\t"
    "s_waitcnt lgkmcnt(0)\n\t"
    "v_smfmac_f32_16x16x64_bf16 v[12:15], v[52:55], v[240:247], v9\n\t"
    "v_smfmac_f32_16x16x64_bf16 v[16:19], v[52:55], v[248:255], v9\n\t"
    "s_nop 6\n\t"
    "v_permlane16_swap_b32 v12, v14\n\t"
    "v_permlane16_swap_b32 v13, v15\n\t"
    "v_permlane16_swap_b32 v16, v18\n\t"
    "v_permlane16_swap_b32 v17, v19\n\t"
    "v_add_f32 v12, v12, v14\n\t"
    "v_add_f32 v13, v13, v15\n\t"
    "v_add_f32 v16, v16, v18\n\t"
    "v_cvt_pk_bf16_f32 v12, v12, v13\n\t"
    "v_add_f32 v17, v17, v19\n\t"
    "ds_write_b32 v10, v12 offset:34816\n\t"
    "v_cvt_pk_bf16_f32 v16, v16, v17\n\t"
    "ds_write_b32 v10, v16 offset:35072\n\t"
    "s_waitcnt lgkmcnt(0)\n\t"
    "s_setprio 0\n\t"
    "s_barrier\n\t"
    "s_memrealtime s[36:37]\n\t"
    "s_waitcnt lgkmcnt(0)\n\t"
    "s_and_b32 s36, s36, 0xffffff\n\t"
    "s_or_b32 s36, s36, %[sn]\n\t"
    "v_lshlrev_b32 v7, 10, v6\n\t"
    "v_mov_b32 v8, s36\n\t"
    "s_mov_b64 exec, 15\n\t"
    "global_store_dword v7, v8, %[vs] offset:0 sc1\n\t"
    "s_mov_b64 exec, -1\n\t"
    "s_barrier\n\t"
    "s_memrealtime s[36:37]\n\t"
    "s_mov_b32 %[d1], %[d0]\n\t"
    "s_waitcnt lgkmcnt(0)\n\t"
    "s_sub_u32 %[d0], s36, s38\n\t"
    "s_min_u32 %[d0], %[d0], 200\n\t"
    "s_waitcnt lgkmcnt(0)\n\t"
    "s_branch 9f\n\t"
    "1:\n\t"
    "s_add_u32 %[sp], %[sp], 1\n\t"
    "s_cmp_ge_u32 %[sp], %[cap]\n\t"
    "s_cbranch_scc1 7f\n\t"
    "global_load_dwordx4 v[36:39], v6, %[ag] sc1\n\t"
    "global_load_dwordx4 v[40:43], v6, %[ag] offset:1024 sc1\n\t"
    "s_waitcnt lgkmcnt(0)\n\t"
    "s_memtime %[tc]\n\t"
    "s_nop 7\n\t"
    "s_branch 3b\n\t"
    "7:\n\t"
    "s_mov_b32 %[cap], 0\n\t"
    "s_branch 2b\n\t"
    "9:"
    : [tc] "=&s"(tc), [sp] "=&s"(sp), [cap] "+s"(cap), [d0] "+s"(d0), [d1] "+s"(d1)
    : [ag] "s"(ag), [c44] "s"(0x4444u), [vs] "s"(vs), [sn] "s"((unsigned)(size_t)od), "{v[64:95]}"(W.w0), "{v[96:127]}"(W.w1), "{v[128:159]}"(W.w2), "{v[160:191]}"(W.w3), "{v[192:223]}"(W.w4), "{v[224:255]}"(W.w5), "{v[56:63]}"(W.x)
    : "v5", "v6", "v7", "v8", "v9", "v10", "v11", "v12", "v13", "v14", "v15", "v16", "v17", "v18", "v19", "v20", "v21", "v22", "v23", "v24", "v25", "v26", "v27", "v28", "v29", "v30", "v31", "v32", "v33", "v34", "v35", "v36", "v37", "v38", "v39", "v40", "v41", "v42", "v43", "v44", "v45", "v46", "v47", "v48", "v49", "v50", "v51", "v52", "v53", "v54", "v55", "vcc", "scc", "memory", "s36", "s37", "s38");
}
template <> __device__ __forceinline__ void qk_step<5>(const QKW& W, const char* ag, const char* vs, const char* od, const char* kv, unsigned& cap, unsigned& sp,
                                                        unsigned long long& tc, unsigned long long* ts, unsigned& d0, unsigned& d1) {
  unsigned st, bg;
  asm volatile("v_mbcnt_lo_u32_b32 v52, -1, 0\n\t"
    "v_mbcnt_hi_u32_b32 v52, -1, v52\n\t"
    "v_lshlrev_b32 v6, 4, v52\n\t"
    "s_mov_b32 %[sp], 0\n\t"
    "global_load_dwordx4 v[36:39], v6, %[ag] sc1\n\t"
    "global_load_dwordx4 v[40:43], v6, %[ag] offset:1024 sc1\n\t"
    "s_memtime %[tc]\n\t"
    "v_add_u32 v7, 75776, v6\n\t"
    "v_add_u32 v8, 133120, v6\n\t"
    "ds_read_b128 v[20:23], v7 offset:8192\n\t"
    "ds_read_b128 v[24:27], v7 offset:9216\n\t"
    "ds_read_b128 v[28:31], v7 offset:0\n\t"
    "ds_read_b128 v[32:35], v7 offset:1024\n\t"
    "v_lshrrev_b32 v44, 4, v52\n\t"
    "v_and_b32 v45, 15, v52\n\t"
    "v_and_b32 v53, 4, v52\n\t"
    "v_mul_u32_u24 v53, 0x2aaa, v53\n\t"
    "v_lshrrev_b32 v9, 1, v52\n\t"
    "v_and_b32 v9, 2, v9\n\t"
    "v_or3_b32 v9, v9, v53, %[c44]\n\t"
    "v_lshlrev_b32 v53, 2, v44\n\t"
    "v_lshl_or_b32 v10, v45, 4, v53\n\t"
    "v_and_b32 v53, 1, v52\n\t"
    "v_lshlrev_b32 v53, 3, v53\n\t"
    "v_lshl_or_b32 v11, v44, 7, v53\n\t"
    "v_and_b32 v53, 12, v52\n\t"
    "v_lshl_or_b32 v11, v53, 2, v11\n\t"
    "v_and_b32 v53, 2, v52\n\t"
    "v_lshl_or_b32 v11, v53, 8, v11\n\t"
    "3:\n\t"
    "v_mov_b32 v12, 0\n\t"
    "v_mov_b32 v13, 0\n\t"
    "v_mov_b32 v14, 0\n\t"
    "v_mov_b32 v15, 0\n\t"
    "v_mov_b32 v16, 0\n\t"
    "v_mov_b32 v17, 0\n\t"
    "v_mov_b32 v18, 0\n\t"
    "v_mov_b32 v19, 0\n\t"
    "s_waitcnt vmcnt(0) lgkmcnt(0)\n\t"
    "14:\n\t"
    "ds_write_b128 v6, v[36:39] offset:10240\n\t"
    "ds_write_b128 v6, v[40:43] offset:11264\n\t"
    "v_smfmac_f32_16x16x64_bf16 v[12:15], v[36:39], v[160:167], v9\n\t"
    "v_max3_u32 v52, v36, v37, v38\n\t"
    "v_max3_u32 v53, v39, v40, v41\n\t"
    "v_smfmac_f32_16x16x64_bf16 v[16:19], v[36:39], v[168:175], v9\n\t"
    "v_max3_u32 v52, v52, v42, v43\n\t"
    "v_max_u32 v52, v52, v53\n\t"
    "v_cmp_eq_u32 vcc, -1, v52\n\t"
    "s_cbranch_vccnz 1f\n\t"
    "2:\n\t"
    "s_waitcnt lgkmcnt(0)\n\t"
    "s_barrier\n\t"
    "ds_read_b128 v[36:39], v6 offset:0\n\t"
    "ds_read_b128 v[44:47], v6 offset:4096\n\t"
    "v_smfmac_f32_16x16x64_bf16 v[12:15], v[40:43], v[176:183], v9\n\t"
    "ds_read_b128 v[48:51], v6 offset:5120\n\t"
    "ds_read_b128 v[52:55], v6 offset:6144\n\t"
    "v_smfmac_f32_16x16x64_bf16 v[16:19], v[40:43], v[184:191], v9\n\t"
    "ds_read_b128 v[40:43], v6 offset:1024\n\t"
    "s_sleep 0\n\t"
    "s_waitcnt lgkmcnt(4)\n\t"
    "v_smfmac_f32_16x16x64_bf16 v[12:15], v[36:39], v[56:63], v9\n\t"
    "v_smfmac_f32_16x16x64_bf16 v[16:19], v[36:39], v[28:35], v9\n\t"
    "ds_read_b128 v[28:31], v7 offset:16384\n\t"
    "ds_read_b128 v[32:35], v7 offset:17408\n\t"
    "ds_read_b128 v[36:39], v6 offset:7168\n\t"
    "s_waitcnt lgkmcnt(6)\n\t"
    "v_smfmac_f32_16x16x64_bf16 v[12:15], v[44:47], v[64:71], v9\n\t"
    "v_smfmac_f32_16x16x64_bf16 v[16:19], v[44:47], v[72:79], v9\n\t"
    "ds_read_b128 v[44:47], v6 offset:8192\n\t"
    "s_waitcnt lgkmcnt(6)\n\t"
    "v_smfmac_f32_16x16x64_bf16 v[12:15], v[48:51], v[80:87], v9\n\t"
    "v_smfmac_f32_16x16x64_bf16 v[16:19], v[48:51], v[88:95], v9\n\t"
    "ds_read_b128 v[48:51], v6 offset:9216\n\t"
    "s_waitcnt lgkmcnt(6)\n\t"
    "v_smfmac_f32_16x16x64_bf16 v[12:15], v[52:55], v[96:103], v9\n\t"
    "v_smfmac_f32_16x16x64_bf16 v[16:19], v[52:55], v[104:111], v9\n\t"
    "ds_read_b128 v[52:55], v6 offset:2048\n\t"
    "s_waitcnt lgkmcnt(6)\n\t"
    "v_smfmac_f32_16x16x64_bf16 v[12:15], v[40:43], v[20:27], v9\n\t"
    "ds_read_b128 v[20:23], v8 offset:0\n\t"
    "ds_read_b128 v[24:27], v8 offset:1024\n\t"
    "s_waitcnt lgkmcnt(6)\n\t"
    "v_smfmac_f32_16x16x64_bf16 v[16:19], v[40:43], v[28:35], v9\n\t"
    "ds_read_b128 v[28:31], v8 offset:8192\n\t"
    "ds_read_b128 v[32:35], v8 offset:9216\n\t"
    "ds_read_b128 v[40:43], v6 offset:12288\n\t"
    "s_waitcnt lgkmcnt(8)\n\t"
    "v_smfmac_f32_16x16x64_bf16 v[12:15], v[36:39], v[112:119], v9\n\t"
    "v_smfmac_f32_16x16x64_bf16 v[16:19], v[36:39], v[120:127], v9\n\t"
    "ds_read_b128 v[36:39], v6 offset:13312\n\t"
    "s_waitcnt lgkmcnt(8)\n\t"
    "v_smfmac_f32_16x16x64_bf16 v[12:15], v[44:47], v[128:135], v9\n\t"
    "v_smfmac_f32_16x16x64_bf16 v[16:19], v[44:47], v[136:143], v9\n\t"
    "ds_read_b128 v[44:47], v6 offset:14336\n\t"
    "s_barrier\n\t"
    "s_setprio 2\n\t"
    "s_waitcnt lgkmcnt(8)\n\t"
    "v_smfmac_f32_16x16x64_bf16 v[12:15], v[48:51], v[144:151], v9\n\t"
    "v_smfmac_f32_16x16x64_bf16 v[16:19], v[48:51], v[152:159], v9\n\t"
    "ds_read_b128 v[48:51], v6 offset:3072\n\t"
    "s_waitcnt lgkmcnt(6)\n\t"
    "v_smfmac_f32_16x16x64_bf16 v[12:15], v[52:55], v[20:27], v9\n\t"
    "ds_read_b128 v[20:23], v8 offset:16384\n\t"
    "ds_read_b128 v[24:27], v8 offset:17408\n\t"
    "s_waitcnt lgkmcnt(6)\n\t"
    "v_smfmac_f32_16x16x64_bf16 v[16:19], v[52:55], v[28:35], v9\n\t"
    "ds_read_b128 v[28:31], v8 offset:24576\n\t"
    "ds_read_b128 v[32:35], v8 offset:25600\n\t"
    "ds_read_b128 v[52:55], v6 offset:15360\n\t"
    "s_waitcnt lgkmcnt(8)\n\t"
    "v_smfmac_f32_16x16x64_bf16 v[12:15], v[40:43], v[192:199], v9\n\t"
    "v_smfmac_f32_16x16x64_bf16 v[16:19], v[40:43], v[200:207], v9\n\t"
    "s_waitcnt lgkmcnt(7)\n\t"
    "v_smfmac_f32_16x16x64_bf16 v[12:15], v[36:39], v[208:215], v9\n\t"
    "v_smfmac_f32_16x16x64_bf16 v[16:19], v[36:39], v[216:223], v9\n\t"
    "s_waitcnt lgkmcnt(6)\n\t"
    "v_smfmac_f32_16x16x64_bf16 v[12:15], v[44:47], v[224:231], v9\n\t"
    "v_smfmac_f32_16x16x64_bf16 v[16:19], v[44:47], v[232:239], v9\n\t"
    "s_waitcnt lgkmcnt(3)\n\t"
    "v_smfmac_f32_16x16x64_bf16 v[12:15], v[48:51], v[20:27], v9\n\t"
    "s_waitcnt lgkmcnt(1)\n\t"
    "v_smfmac_f32_16x16x64_bf16 v[16:19], v[48:51], v[28:35], v9\n\t"
    "s_waitcnt lgkmcnt(0)\n\t"
    "v_smfmac_f32_16x16x64_bf16 v[12:15], v[52:55], v[240:247], v9\n\t"
    "v_smfmac_f32_16x16x64_bf16 v[16:19], v[52:55], v[248:255], v9\n\t"
    "s_nop 6\n\t"
    "v_permlane16_swap_b32 v12, v14\n\t"
    "v_permlane16_swap_b32 v13, v15\n\t"
    "v_permlane16_swap_b32 v16, v18\n\t"
    "v_permlane16_swap_b32 v17, v19\n\t"
    "v_add_f32 v12, v12, v14\n\t"
    "v_add_f32 v13, v13, v15\n\t"
    "v_add_f32 v16, v16, v18\n\t"
    "v_cvt_pk_bf16_f32 v12, v12, v13\n\t"
    "v_add_f32 v17, v17, v19\n\t"
    "ds_write_b32 v10, v12 offset:35328\n\t"
    "v_cvt_pk_bf16_f32 v16, v16, v17\n\t"
    "ds_write_b32 v10, v16 offset:35584\n\t"
    "s_waitcnt lgkmcnt(0)\n\t"
    "s_setprio 0\n\t"
    "s_barrier\n\t"
    "ds_read_b64_tr_b16 v[36:37], v11 offset:34816\n\t"
    "ds_read_b64_tr_b16 v[38:39], v11 offset:34880\n\t"
    "ds_read_b64_tr_b16 v[40:41], v11 offset:35840\n\t"
    "ds_read_b64_tr_b16 v[42:43], v11 offset:35904\n\t"
    "s_waitcnt lgkmcnt(0)\n\t"
    "v_min_u32 v36, -2, v36\n\t"
    "v_min_u32 v37, -2, v37\n\t"
    "v_min_u32 v38, -2, v38\n\t"
    "v_min_u32 v39, -2, v39\n\t"
    "v_min_u32 v40, -2, v40\n\t"
    "v_min_u32 v41, -2, v41\n\t"
    "v_min_u32 v42, -2, v42\n\t"
    "v_min_u32 v43, -2, v43\n\t"
    "global_store_dwordx4 v6, v[36:39], %[vs] sc1\n\t"
    "global_store_dwordx4 v6, v[40:43], %[vs] offset:1024 sc1\n\t"
    "s_nop 1\n\t"
    "s_barrier\n\t"
    "s_waitcnt lgkmcnt(0)\n\t"
    "s_branch 9f\n\t"
    "1:\n\t"
    "s_add_u32 %[sp], %[sp], 1\n\t"
    "s_cmp_ge_u32 %[sp], %[cap]\n\t"
    "s_cbranch_scc1 7f\n\t"
    "global_load_dwordx4 v[36:39], v6, %[ag] sc1\n\t"
    "global_load_dwordx4 v[40:43], v6, %[ag] offset:1024 sc1\n\t"
    "s_waitcnt lgkmcnt(0)\n\t"
    "s_memtime %[tc]\n\t"
    "s_nop 7\n\t"
    "s_branch 3b\n\t"
    "7:\n\t"
    "s_mov_b32 %[cap], 0\n\t"
    "s_branch 2b\n\t"
    "9:"
    : [tc] "=&s"(tc), [sp] "=&s"(sp), [cap] "+s"(cap)
    : [ag] "s"(ag), [c44] "s"(0x4444u), [vs] "s"(vs), "{v[64:95]}"(W.w0), "{v[96:127]}"(W.w1), "{v[128:159]}"(W.w2), "{v[160:191]}"(W.w3), "{v[192:223]}"(W.w4), "{v[224:255]}"(W.w5), "{v[56:63]}"(W.x)
    : "v6", "v7", "v8", "v9", "v10", "v11", "v12", "v13", "v14", "v15", "v16", "v17", "v18", "v19", "v20", "v21", "v22", "v23", "v24", "v25", "v26", "v27", "v28", "v29", "v30", "v31", "v32", "v33", "v34", "v35", "v36", "v37", "v38", "v39", "v40", "v41", "v42", "v43", "v44", "v45", "v46", "v47", "v48", "v49", "v50", "v51", "v52", "v53", "v54", "v55", "vcc", "scc", "memory");
}
template <> __device__ __forceinline__ void qk_step<6>(const QKW& W, const char* ag, const char* vs, const char* od, const char* kv, unsigned& cap, unsigned& sp,
                                                        unsigned long long& tc, unsigned long long* ts, unsigned& d0, unsigned& d1) {
  unsigned st, bg;
  asm volatile("v_mbcnt_lo_u32_b32 v52, -1, 0\n\t"
    "v_mbcnt_hi_u32_b32 v52, -1, v52\n\t"
    "v_lshlrev_b32 v6, 4, v52\n\t"
    "s_mov_b32 %[sp], 0\n\t"
    "global_load_dwordx4 v[36:39], v6, %[ag] sc1\n\t"
    "global_load_dwordx4 v[40:43], v6, %[ag] offset:1024 sc1\n\t"
    "s_memtime %[tc]\n\t"
    "v_add_u32 v7, 77824, v6\n\t"
    "v_add_u32 v8, 135168, v6\n\t"
    "ds_read_b128 v[20:23], v7 offset:8192\n\t"
    "ds_read_b128 v[24:27], v7 offset:9216\n\t"
    "ds_read_b128 v[28:31], v7 offset:0\n\t"
    "ds_read_b128 v[32:35], v7 offset:1024\n\t"
    "v_lshrrev_b32 v44, 4, v52\n\t"
    "v_and_b32 v45, 15, v52\n\t"
    "v_and_b32 v53, 4, v52\n\t"
    "v_mul_u32_u24 v53, 0x2aaa, v53\n\t"
    "v_lshrrev_b32 v9, 1, v52\n\t"
    "v_and_b32 v9, 2, v9\n\t"
    "v_or3_b32 v9, v9, v53, %[c44]\n\t"
    "v_lshlrev_b32 v53, 2, v44\n\t"
    "v_lshl_or_b32 v10, v45, 4, v53\n\t"
    "3:\n\t"
    "v_mov_b32 v12, 0\n\t"
    "v_mov_b32 v13, 0\n\t"
    "v_mov_b32 v14, 0\n\t"
    "v_mov_b32 v15, 0\n\t"
    "v_mov_b32 v16, 0\n\t"
    "v_mov_b32 v17, 0\n\t"
    "v_mov_b32 v18, 0\n\t"
    "v_mov_b32 v19, 0\n\t"
    "s_waitcnt vmcnt(0) lgkmcnt(0)\n\t"
    "14:\n\t"
    "ds_write_b128 v6, v[36:39] offset:12288\n\t"
    "ds_write_b128 v6, v[40:43] offset:13312\n\t"
    "v_smfmac_f32_16x16x64_bf16 v[12:15], v[36:39], v[192:199], v9\n\t"
    "v_max3_u32 v52, v36, v37, v38\n\t"
    "v_max3_u32 v53, v39, v40, v41\n\t"
    "v_smfmac_f32_16x16x64_bf16 v[16:19], v[36:39], v[200:207], v9\n\t"
    "v_max3_u32 v52, v52, v42, v43\n\t"
    "v_max_u32 v52, v52, v53\n\t"
    "v_cmp_eq_u32 vcc, -1, v52\n\t"
    "s_cbranch_vccnz 1f\n\t"
    "2:\n\t"
    "s_waitcnt lgkmcnt(0)\n\t"
    "s_barrier\n\t"
    "ds_read_b128 v[36:39], v6 offset:0\n\t"
    "ds_read_b128 v[44:47], v6 offset:4096\n\t"
    "v_smfmac_f32_16x16x64_bf16 v[12:15], v[40:43], v[208:215], v9\n\t"
    "ds_read_b128 v[48:51], v6 offset:5120\n\t"
    "ds_read_b128 v[52:55], v6 offset:6144\n\t"
    "v_smfmac_f32_16x16x64_bf16 v[16:19], v[40:43], v[216:223], v9\n\t"
    "ds_read_b128 v[40:43], v6 offset:1024\n\t"
    "s_sleep 0\n\t"
    "s_waitcnt lgkmcnt(4)\n\t"
    "v_smfmac_f32_16x16x64_bf16 v[12:15], v[36:39], v[56:63], v9\n\t"
    "v_smfmac_f32_16x16x64_bf16 v[16:19], v[36:39], v[28:35], v9\n\t"
    "ds_read_b128 v[28:31], v7 offset:16384\n\t"
    "ds_read_b128 v[32:35], v7 offset:17408\n\t"
    "ds_read_b128 v[36:39], v6 offset:7168\n\t"
    "s_waitcnt lgkmcnt(6)\n\t"
    "v_smfmac_f32_16x16x64_bf16 v[12:15], v[44:47], v[64:71], v9\n\t"
    "v_smfmac_f32_16x16x64_bf16 v[16:19], v[44:47], v[72:79], v9\n\t"
    "ds_read_b128 v[44:47], v6 offset:8192\n\t"
    "s_waitcnt lgkmcnt(6)\n\t"
    "v_smfmac_f32_16x16x64_bf16 v[12:15], v[48:51], v[80:87], v9\n\t"
    "v_smfmac_f32_16x16x64_bf16 v[16:19], v[48:51], v[88:95], v9\n\t"
    "ds_read_b128 v[48:51], v6 offset:9216\n\t"
    "s_waitcnt lgkmcnt(6)\n\t"
    "v_smfmac_f32_16x16x64_bf16 v[12:15], v[52:55], v[96:103], v9\n\t"
    "v_smfmac_f32_16x16x64_bf16 v[16:19], v[52:55], v[104:111], v9\n\t"
    "ds_read_b128 v[52:55], v6 offset:2048\n\t"
    "s_waitcnt lgkmcnt(6)\n\t"
    "v_smfmac_f32_16x16x64_bf16 v[12:15], v[40:43], v[20:27], v9\n\t"
    "ds_read_b128 v[20:23], v8 offset:0\n\t"
    "ds_read_b128 v[24:27], v8 offset:1024\n\t"
    "s_waitcnt lgkmcnt(6)\n\t"
    "v_smfmac_f32_16x16x64_bf16 v[16:19], v[40:43], v[28:35], v9\n\t"
    "ds_read_b128 v[28:31], v8 offset:8192\n\t"
    "ds_read_b128 v[32:35], v8 offset:9216\n\t"
    "ds_read_b128 v[40:43], v6 offset:10240\n\t"
    "s_waitcnt lgkmcnt(8)\n\t"
    "v_smfmac_f32_16x16x64_bf16 v[12:15], v[36:39], v[112:119], v9\n\t"
    "v_smfmac_f32_16x16x64_bf16 v[16:19], v[36:39], v[120:127], v9\n\t"
    "ds_read_b128 v[36:39], v6 offset:11264\n\t"
    "s_waitcnt lgkmcnt(8)\n\t"
    "v_smfmac_f32_16x16x64_bf16 v[12:15], v[44:47], v[128:135], v9\n\t"
    "v_smfmac_f32_16x16x64_bf16 v[16:19], v[44:47], v[136:143], v9\n\t"
    "ds_read_b128 v[44:47], v6 offset:14336\n\t"
    "s_barrier\n\t"
    "s_setprio 2\n\t"
    "s_waitcnt lgkmcnt(8)\n\t"
    "v_smfmac_f32_16x16x64_bf16 v[12:15], v[48:51], v[144:151], v9\n\t"
    "v_smfmac_f32_16x16x64_bf16 v[16:19], v[48:51], v[152:159], v9\n\t"
    "ds_read_b128 v[48:51], v6 offset:3072\n\t"
    "s_waitcnt lgkmcnt(6)\n\t"
    "v_smfmac_f32_16x16x64_bf16 v[12:15], v[52:55], v[20:27], v9\n\t"
    "ds_read_b128 v[20:23], v8 offset:16384\n\t"
    "ds_read_b128 v[24:27], v8 offset:17408\n\t"
    "s_waitcnt lgkmcnt(6)\n\t"
    "v_smfmac_f32_16x16x64_bf16 v[16:19], v[52:55], v[28:35], v9\n\t"
    "ds_read_b128 v[28:31], v8 offset:24576\n\t"
    "ds_read_b128 v[32:35], v8 offset:25600\n\t"
    "ds_read_b128 v[52:55], v6 offset:15360\n\t"
    "s_waitcnt lgkmcnt(8)\n\t"
    "v_smfmac_f32_16x16x64_bf16 v[12:15], v[40:43], v[160:167], v9\n\t"
    "v_smfmac_f32_16x16x64_bf16 v[16:19], v[40:43], v[168:175], v9\n\t"
    "s_waitcnt lgkmcnt(7)\n\t"
    "v_smfmac_f32_16x16x64_bf16 v[12:15], v[36:39], v[176:183], v9\n\t"
    "v_smfmac_f32_16x16x64_bf16 v[16:19], v[36:39], v[184:191], v9\n\t"
    "s_waitcnt lgkmcnt(6)\n\t"
    "v_smfmac_f32_16x16x64_bf16 v[12:15], v[44:47], v[224:231], v9\n\t"
    "v_smfmac_f32_16x16x64_bf16 v[16:19], v[44:47], v[232:239], v9\n\t"
    "s_waitcnt lgkmcnt(3)\n\t"
    "v_smfmac_f32_16x16x64_bf16 v[12:15], v[48:51], v[20:27], v9\n\t"
    "s_waitcnt lgkmcnt(1)\n\t"
    "v_smfmac_f32_16x16x64_bf16 v[16:19], v[48:51], v[28:35], v9\n\t"
    "s_waitcnt lgkmcnt(0)\n\t"
    "v_smfmac_f32_16x16x64_bf16 v[12:15], v[52:55], v[240:247], v9\n\t"
    "v_smfmac_f32_16x16x64_bf16 v[16:19], v[52:55], v[248:255], v9\n\t"
    "s_nop 6\n\t"
    "v_permlane16_swap_b32 v12, v14\n\t"
    "v_permlane16_swap_b32 v13, v15\n\t"
    "v_permlane16_swap_b32 v16, v18\n\t"
    "v_permlane16_swap_b32 v17, v19\n\t"
    "v_add_f32 v12, v12, v14\n\t"
    "v_add_f32 v13, v13, v15\n\t"
    "v_add_f32 v16, v16, v18\n\t"
    "v_cvt_pk_bf16_f32 v12, v12, v13\n\t"
    "v_add_f32 v17, v17, v19\n\t"
    "ds_write_b32 v10, v12 offset:35840\n\t"
    "v_cvt_pk_bf16_f32 v16, v16, v17\n\t"
    "ds_write_b32 v10, v16 offset:36096\n\t"
    "s_waitcnt lgkmcnt(0)\n\t"
    "s_setprio 0\n\t"
    "s_barrier\n\t"
    "s_barrier\n\t"
    "s_waitcnt lgkmcnt(0)\n\t"
    "s_branch 9f\n\t"
    "1:\n\t"
    "s_add_u32 %[sp], %[sp], 1\n\t"
    "s_cmp_ge_u32 %[sp], %[cap]\n\t"
    "s_cbranch_scc1 7f\n\t"
    "global_load_dwordx4 v[36:39], v6, %[ag] sc1\n\t"
    "global_load_dwordx4 v[40:43], v6, %[ag] offset:1024 sc1\n\t"
    "s_waitcnt lgkmcnt(0)\n\t"
    "s_memtime %[tc]\n\t"
    "s_nop 7\n\t"
    "s_branch 3b\n\t"
    "7:\n\t"
    "s_mov_b32 %[cap], 0\n\t"
    "s_branch 2b\n\t"
    "9:"
    : [tc] "=&s"(tc), [sp] "=&s"(sp), [cap] "+s"(cap)
    : [ag] "s"(ag), [c44] "s"(0x4444u), "{v[64:95]}"(W.w0), "{v[96:127]}"(W.w1), "{v[128:159]}"(W.w2), "{v[160:191]}"(W.w3), "{v[192:223]}"(W.w4), "{v[224:255]}"(W.w5), "{v[56:63]}"(W.x)
    : "v6", "v7", "v8", "v9", "v10", "v11", "v12", "v13", "v14", "v15", "v16", "v17", "v18", "v19", "v20", "v21", "v22", "v23", "v24", "v25", "v26", "v27", "v28", "v29", "v30", "v31", "v32", "v33", "v34", "v35", "v36", "v37", "v38", "v39", "v40", "v41", "v42", "v43", "v44", "v45", "v46", "v47", "v48", "v49", "v50", "v51", "v52", "v53", "v54", "v55", "vcc", "scc", "memory");
}
template <> __device__ __forceinline__ void qk_step<7>(const QKW& W, const char* ag, const char* vs, const char* od, const char* kv, unsigned& cap, unsigned& sp,
                                                        unsigned long long& tc, unsigned long long* ts, unsigned& d0, unsigned& d1) {
  unsigned st, bg;
  asm volatile("v_mbcnt_lo_u32_b32 v52, -1, 0\n\t"
    "v_mbcnt_hi_u32_b32 v52, -1, v52\n\t"
    "v_lshlrev_b32 v6, 4, v52\n\t"
    "s_mov_b32 %[sp], 0\n\t"
    "global_load_dwordx4 v[36:39], v6, %[ag] sc1\n\t"
    "global_load_dwordx4 v[40:43], v6, %[ag] offset:1024 sc1\n\t"
    "s_memtime %[tc]\n\t"
    "v_add_u32 v7, 79872, v6\n\t"
    "v_add_u32 v8, 137216, v6\n\t"
    "ds_read_b128 v[20:23], v7 offset:8192\n\t"
    "ds_read_b128 v[24:27], v7 offset:9216\n\t"
    "ds_read_b128 v[28:31], v7 offset:0\n\t"
    "ds_read_b128 v[32:35], v7 offset:1024\n\t"
    "v_lshrrev_b32 v44, 4, v52\n\t"
    "v_and_b32 v45, 15, v52\n\t"
    "v_and_b32 v53, 4, v52\n\t"
    "v_mul_u32_u24 v53, 0x2aaa, v53\n\t"
    "v_lshrrev_b32 v9, 1, v52\n\t"
    "v_and_b32 v9, 2, v9\n\t"
    "v_or3_b32 v9, v9, v53, %[c44]\n\t"
    "v_lshlrev_b32 v53, 2, v44\n\t"
    "v_lshl_or_b32 v10, v45, 4, v53\n\t"
    "3:\n\t"
    "v_mov_b32 v12, 0\n\t"
    "v_mov_b32 v13, 0\n\t"
    "v_mov_b32 v14, 0\n\t"
    "v_mov_b32 v15, 0\n\t"
    "v_mov_b32 v16, 0\n\t"
    "v_mov_b32 v17, 0\n\t"
    "v_mov_b32 v18, 0\n\t"
    "v_mov_b32 v19, 0\n\t"
    "s_waitcnt vmcnt(0) lgkmcnt(0)\n\t"
    "14:\n\t"
    "ds_write_b128 v6, v[36:39] offset:14336\n\t"
    "ds_write_b128 v6, v[40:43] offset:15360\n\t"
    "v_smfmac_f32_16x16x64_bf16 v[12:15], v[36:39], v[224:231], v9\n\t"
    "v_max3_u32 v52, v36, v37, v38\n\t"
    "v_max3_u32 v53, v39, v40, v41\n\t"
    "v_smfmac_f32_16x16x64_bf16 v[16:19], v[36:39], v[232:239], v9\n\t"
    "v_max3_u32 v52, v52, v42, v43\n\t"
    "v_max_u32 v52, v52, v53\n\t"
    "v_cmp_eq_u32 vcc, -1, v52\n\t"
    "s_cbranch_vccnz 1f\n\t"
    "2:\n\t"
    "s_waitcnt lgkmcnt(0)\n\t"
    "s_barrier\n\t"
    "ds_read_b128 v[36:39], v6 offset:0\n\t"
    "ds_read_b128 v[44:47], v6 offset:4096\n\t"
    "v_smfmac_f32_16x16x64_bf16 v[12:15], v[40:43], v[240:247], v9\n\t"
    "ds_read_b128 v[48:51], v6 offset:5120\n\t"
    "ds_read_b128 v[52:55], v6 offset:6144\n\t"
    "v_smfmac_f32_16x16x64_bf16 v[16:19], v[40:43], v[248:255], v9\n\t"
    "ds_read_b128 v[40:43], v6 offset:1024\n\t"
    "s_sleep 0\n\t"
    "s_waitcnt lgkmcnt(4)\n\t"
    "v_smfmac_f32_16x16x64_bf16 v[12:15], v[36:39], v[56:63], v9\n\t"
    "v_smfmac_f32_16x16x64_bf16 v[16:19], v[36:39], v[28:35], v9\n\t"
    "ds_read_b128 v[28:31], v7 offset:16384\n\t"
    "ds_read_b128 v[32:35], v7 offset:17408\n\t"
    "ds_read_b128 v[36:39], v6 offset:7168\n\t"
    "s_waitcnt lgkmcnt(6)\n\t"
    "v_smfmac_f32_16x16x64_bf16 v[12:15], v[44:47], v[64:71], v9\n\t"
    "v_smfmac_f32_16x16x64_bf16 v[16:19], v[44:47], v[72:79], v9\n\t"
    "ds_read_b128 v[44:47], v6 offset:8192\n\t"
    "s_waitcnt lgkmcnt(6)\n\t"
    "v_smfmac_f32_16x16x64_bf16 v[12:15], v[48:51], v[80:87], v9\n\t"
    "v_smfmac_f32_16x16x64_bf16 v[16:19], v[48:51], v[88:95], v9\n\t"
    "ds_read_b128 v[48:51], v6 offset:9216\n\t"
    "s_waitcnt lgkmcnt(6)\n\t"
    "v_smfmac_f32_16x16x64_bf16 v[12:15], v[52:55], v[96:103], v9\n\t"
    "v_smfmac_f32_16x16x64_bf16 v[16:19], v[52:55], v[104:111], v9\n\t"
    "ds_read_b128 v[52:55], v6 offset:2048\n\t"
    "s_waitcnt lgkmcnt(6)\n\t"
    "v_smfmac_f32_16x16x64_bf16 v[12:15], v[40:43], v[20:27], v9\n\t"
    "ds_read_b128 v[20:23], v8 offset:0\n\t"
    "ds_read_b128 v[24:27], v8 offset:1024\n\t"
    "s_waitcnt lgkmcnt(6)\n\t"
    "v_smfmac_f32_16x16x64_bf16 v[16:19], v[40:43], v[28:35], v9\n\t"
    "ds_read_b128 v[28:31], v8 offset:8192\n\t"
    "ds_read_b128 v[32:35], v8 offset:9216\n\t"
    "ds_read_b128 v[40:43], v6 offset:10240\n\t"
    "s_waitcnt lgkmcnt(8)\n\t"
    "v_smfmac_f32_16x16x64_bf16 v[12:15], v[36:39], v[112:119], v9\n\t"
    "v_smfmac_f32_16x16x64_bf16 v[16:19], v[36:39], v[120:127], v9\n\t"
    "ds_read_b128 v[36:39], v6 offset:11264\n\t"
    "s_waitcnt lgkmcnt(8)\n\t"
    "v_smfmac_f32_16x16x64_bf16 v[12:15], v[44:47], v[128:135], v9\n\t"
    "v_smfmac_f32_16x16x64_bf16 v[16:19], v[44:47], v[136:143], v9\n\t"
    "ds_read_b128 v[44:47], v6 offset:12288\n\t"
    "s_barrier\n\t"
    "s_setprio 2\n\t"
    "s_waitcnt lgkmcnt(8)\n\t"
    "v_smfmac_f32_16x16x64_bf16 v[12:15], v[48:51], v[144:151], v9\n\t"
    "v_smfmac_f32_16x16x64_bf16 v[16:19], v[48:51], v[152:159], v9\n\t"
    "ds_read_b128 v[48:51], v6 offset:3072\n\t"
    "s_waitcnt lgkmcnt(6)\n\t"
    "v_smfmac_f32_16x16x64_bf16 v[12:15], v[52:55], v[20:27], v9\n\t"
    "ds_read_b128 v[20:23], v8 offset:16384\n\t"
    "ds_read_b128 v[24:27], v8 offset:17408\n\t"
    "s_waitcnt lgkmcnt(6)\n\t"
    "v_smfmac_f32_16x16x64_bf16 v[16:19], v[52:55], v[28:35], v9\n\t"
    "ds_read_b128 v[28:31], v8 offset:24576\n\t"
    "ds_read_b128 v[32:35], v8 offset:25600\n\t"
    "ds_read_b128 v[52:55], v6 offset:13312\n\t"
    "s_waitcnt lgkmcnt(8)\n\t"
    "v_smfmac_f32_16x16x64_bf16 v[12:15], v[40:43], v[160:167], v9\n\t"
    "v_smfmac_f32_16x16x64_bf16 v[16:19], v[40:43], v[168:175], v9\n\t"
    "s_waitcnt lgkmcnt(7)\n\t"
    "v_smfmac_f32_16x16x64_bf16 v[12:15], v[36:39], v[176:183], v9\n\t"
    "v_smfmac_f32_16x16x64_bf16 v[16:19], v[36:39], v[184:191], v9\n\t"
    "s_waitcnt lgkmcnt(6)\n\t"
    "v_smfmac_f32_16x16x64_bf16 v[12:15], v[44:47], v[192:199], v9\n\t"
    "v_smfmac_f32_16x16x64_bf16 v[16:19], v[44:47], v[200:207], v9\n\t"
    "s_waitcnt lgkmcnt(3)\n\t"
    "v_smfmac_f32_16x16x64_bf16 v[12:15], v[48:51], v[20:27], v9\n\t"
    "s_waitcnt lgkmcnt(1)\n\t"
    "v_smfmac_f32_16x16x64_bf16 v[16:19], v[48:51], v[28:35], v9\n\t"
    "s_waitcnt lgkmcnt(0)\n\t"
    "v_smfmac_f32_16x16x64_bf16 v[12:15], v[52:55], v[208:215], v9\n\t"
    "v_smfmac_f32_16x16x64_bf16 v[16:19], v[52:55], v[216:223], v9\n\t"
    "s_nop 6\n\t"
    "v_permlane16_swap_b32 v12, v14\n\t"
    "v_permlane16_swap_b32 v13, v15\n\t"
    "v_permlane16_swap_b32 v16, v18\n\t"
    "v_permlane16_swap_b32 v17, v19\n\t"
    "v_add_f32 v12, v12, v14\n\t"
    "v_add_f32 v13, v13, v15\n\t"
    "v_add_f32 v16, v16, v18\n\t"
    "v_cvt_pk_bf16_f32 v12, v12, v13\n\t"
    "v_add_f32 v17, v17, v19\n\t"
    "ds_write_b32 v10, v12 offset:36352\n\t"
    "v_cvt_pk_bf16_f32 v16, v16, v17\n\t"
    "ds_write_b32 v10, v16 offset:36608\n\t"
    "s_waitcnt lgkmcnt(0)\n\t"
    "s_setprio 0\n\t"
    "s_barrier\n\t"
    "s_barrier\n\t"
    "s_waitcnt lgkmcnt(0)\n\t"
    "s_branch 9f\n\t"
    "1:\n\t"
    "s_add_u32 %[sp], %[sp], 1\n\t"
    "s_cmp_ge_u32 %[sp], %[cap]\n\t"
    "s_cbranch_scc1 7f\n\t"
    "global_load_dwordx4 v[36:39], v6, %[ag] sc1\n\t"
    "global_load_dwordx4 v[40:43], v6, %[ag] offset:1024 sc1\n\t"
    "s_waitcnt lgkmcnt(0)\n\t"
    "s_memtime %[tc]\n\t"
    "s_nop 7\n\t"
    "s_branch 3b\n\t"
    "7:\n\t"
    "s_mov_b32 %[cap], 0\n\t"
    "s_branch 2b\n\t"
    "9:"
    : [tc] "=&s"(tc), [sp] "=&s"(sp), [cap] "+s"(cap)
    : [ag] "s"(ag), [c44] "s"(0x4444u), "{v[64:95]}"(W.w0), "{v[96:127]}"(W.w1), "{v[128:159]}"(W.w2), "{v[160:191]}"(W.w3), "{v[192:223]}"(W.w4), "{v[224:255]}"(W.w5), "{v[56:63]}"(W.x)
    : "v6", "v7", "v8", "v9", "v10", "v11", "v12", "v13", "v14", "v15", "v16", "v17", "v18", "v19", "v20", "v21", "v22", "v23", "v24", "v25", "v26", "v27", "v28", "v29", "v30", "v31", "v32", "v33", "v34", "v35", "v36", "v37", "v38", "v39", "v40", "v41", "v42", "v43", "v44", "v45", "v46", "v47", "v48", "v49", "v50", "v51", "v52", "v53", "v54", "v55", "vcc", "scc", "memory");
}
template <int I> __device__ __forceinline__ void qk_wload(u32x32& w, unsigned a0, unsigned a1);
template <> __device__ __forceinline__ void qk_wload<0>(u32x32& w, unsigned a0, unsigned a1) {
  asm volatile("ds_read_b64_tr_b16 v[64:65], %1 offset:0\n\t"
    "ds_read_b64_tr_b16 v[66:67], %1 offset:512\n\t"
    "ds_read_b64_tr_b16 v[68:69], %1 offset:4096\n\t"
    "ds_read_b64_tr_b16 v[70:71], %1 offset:4608\n\t"
    "ds_read_b64_tr_b16 v[72:73], %2 offset:0\n\t"
    "ds_read_b64_tr_b16 v[74:75], %2 offset:512\n\t"
    "ds_read_b64_tr_b16 v[76:77], %2 offset:4096\n\t"
    "ds_read_b64_tr_b16 v[78:79], %2 offset:4608\n\t"
    "s_waitcnt lgkmcnt(0)\n\t"
    "ds_read_b64_tr_b16 v[80:81], %1 offset:16384\n\t"
    "ds_read_b64_tr_b16 v[82:83], %1 offset:16896\n\t"
    "ds_read_b64_tr_b16 v[84:85], %1 offset:20480\n\t"
    "ds_read_b64_tr_b16 v[86:87], %1 offset:20992\n\t"
    "ds_read_b64_tr_b16 v[88:89], %2 offset:16384\n\t"
    "ds_read_b64_tr_b16 v[90:91], %2 offset:16896\n\t"
    "ds_read_b64_tr_b16 v[92:93], %2 offset:20480\n\t"
    "ds_read_b64_tr_b16 v[94:95], %2 offset:20992\n\t"
    "s_waitcnt lgkmcnt(0)"
    : "=&{v[64:95]}"(w) : "v"(a0), "v"(a1) : "memory");
}
template <> __device__ __forceinline__ void qk_wload<1>(u32x32& w, unsigned a0, unsigned a1) {
  asm volatile("ds_read_b64_tr_b16 v[96:97], %1 offset:0\n\t"
    "ds_read_b64_tr_b16 v[98:99], %1 offset:512\n\t"
    "ds_read_b64_tr_b16 v[100:101], %1 offset:4096\n\t"
    "ds_read_b64_tr_b16 v[102:103], %1 offset:4608\n\t"
    "ds_read_b64_tr_b16 v[104:105], %2 offset:0\n\t"
    "ds_read_b64_tr_b16 v[106:107], %2 offset:512\n\t"
    "ds_read_b64_tr_b16 v[108:109], %2 offset:4096\n\t"
    "ds_read_b64_tr_b16 v[110:111], %2 offset:4608\n\t"
    "s_waitcnt lgkmcnt(0)\n\t"
    "ds_read_b64_tr_b16 v[112:113], %1 offset:16384\n\t"
    "ds_read_b64_tr_b16 v[114:115], %1 offset:16896\n\t"
    "ds_read_b64_tr_b16 v[116:117], %1 offset:20480\n\t"
    "ds_read_b64_tr_b16 v[118:119], %1 offset:20992\n\t"
    "ds_read_b64_tr_b16 v[120:121], %2 offset:16384\n\t"
    "ds_read_b64_tr_b16 v[122:123], %2 offset:16896\n\t"
    "ds_read_b64_tr_b16 v[124:125], %2 offset:20480\n\t"
    "ds_read_b64_tr_b16 v[126:127], %2 offset:20992\n\t"
    "s_waitcnt lgkmcnt(0)"
    : "=&{v[96:127]}"(w) : "v"(a0), "v"(a1) : "memory");
}
template <> __device__ __forceinline__ void qk_wload<2>(u32x32& w, unsigned a0, unsigned a1) {
  asm volatile("ds_read_b64_tr_b16 v[128:129], %1 offset:0\n\t"
    "ds_read_b64_tr_b16 v[130:131], %1 offset:512\n\t"
    "ds_read_b64_tr_b16 v[132:133], %1 offset:4096\n\t"
    "ds_read_b64_tr_b16 v[134:135], %1 offset:4608\n\t"
    "ds_read_b64_tr_b16 v[136:137], %2 offset:0\n\t"
    "ds_read_b64_tr_b16 v[138:139], %2 offset:512\n\t"
    "ds_read_b64_tr_b16 v[140:141], %2 offset:4096\n\t"
    "ds_read_b64_tr_b16 v[142:143], %2 offset:4608\n\t"
    "s_waitcnt lgkmcnt(0)\n\t"
    "ds_read_b64_tr_b16 v[144:145], %1 offset:16384\n\t"
    "ds_read_b64_tr_b16 v[146:147], %1 offset:16896\n\t"
    "ds_read_b64_tr_b16 v[148:149], %1 offset:20480\n\t"
    "ds_read_b64_tr_b16 v[150:151], %1 offset:20992\n\t"
    "ds_read_b64_tr_b16 v[152:153], %2 offset:16384\n\t"
    "ds_read_b64_tr_b16 v[154:155], %2 offset:16896\n\t"
    "ds_read_b64_tr_b16 v[156:157], %2 offset:20480\n\t"
    "ds_read_b64_tr_b16 v[158:159], %2 offset:20992\n\t"
    "s_waitcnt lgkmcnt(0)"
    : "=&{v[128:159]}"(w) : "v"(a0), "v"(a1) : "memory");
}
template <> __device__ __forceinline__ void qk_wload<3>(u32x32& w, unsigned a0, unsigned a1) {
  asm volatile("ds_read_b64_tr_b16 v[160:161], %1 offset:0\n\t"
    "ds_read_b64_tr_b16 v[162:163], %1 offset:512\n\t"
    "ds_read_b64_tr_b16 v[164:165], %1 offset:4096\n\t"
    "ds_read_b64_tr_b16 v[166:167], %1 offset:4608\n\t"
    "ds_read_b64_tr_b16 v[168:169], %2 offset:0\n\t"
    "ds_read_b64_tr_b16 v[170:171], %2 offset:512\n\t"
    "ds_read_b64_tr_b16 v[172:173], %2 offset:4096\n\t"
    "ds_read_b64_tr_b16 v[174:175], %2 offset:4608\n\t"
    "s_waitcnt lgkmcnt(0)\n\t"
    "ds_read_b64_tr_b16 v[176:177], %1 offset:16384\n\t"
    "ds_read_b64_tr_b16 v[178:179], %1 offset:16896\n\t"
    "ds_read_b64_tr_b16 v[180:181], %1 offset:20480\n\t"
    "ds_read_b64_tr_b16 v[182:183], %1 offset:20992\n\t"
    "ds_read_b64_tr_b16 v[184:185], %2 offset:16384\n\t"
    "ds_read_b64_tr_b16 v[186:187], %2 offset:16896\n\t"
    "ds_read_b64_tr_b16 v[188:189], %2 offset:20480\n\t"
    "ds_read_b64_tr_b16 v[190:191], %2 offset:20992\n\t"
    "s_waitcnt lgkmcnt(0)"
    : "=&{v[160:191]}"(w) : "v"(a0), "v"(a1) : "memory");
}
template <> __device__ __forceinline__ void qk_wload<4>(u32x32& w, unsigned a0, unsigned a1) {
  asm volatile("ds_read_b64_tr_b16 v[192:193], %1 offset:0\n\t"
    "ds_read_b64_tr_b16 v[194:195], %1 offset:512\n\t"
    "ds_read_b64_tr_b16 v[196:197], %1 offset:4096\n\t"
    "ds_read_b64_tr_b16 v[198:199], %1 offset:4608\n\t"
    "ds_read_b64_tr_b16 v[200:201], %2 offset:0\n\t"
    "ds_read_b64_tr_b16 v[202:203], %2 offset:512\n\t"
    "ds_read_b64_tr_b16 v[204:205], %2 offset:4096\n\t"
    "ds_read_b64_tr_b16 v[206:207], %2 offset:4608\n\t"
    "s_waitcnt lgkmcnt(0)\n\t"
    "ds_read_b64_tr_b16 v[208:209], %1 offset:16384\n\t"
    "ds_read_b64_tr_b16 v[210:211], %1 offset:16896\n\t"
    "ds_read_b64_tr_b16 v[212:213], %1 offset:20480\n\t"
    "ds_read_b64_tr_b16 v[214:215], %1 offset:20992\n\t"
    "ds_read_b64_tr_b16 v[216:217], %2 offset:16384\n\t"
    "ds_read_b64_tr_b16 v[218:219], %2 offset:16896\n\t"
    "ds_read_b64_tr_b16 v[220:221], %2 offset:20480\n\t"
    "ds_read_b64_tr_b16 v[222:223], %2 offset:20992\n\t"
    "s_waitcnt lgkmcnt(0)"
    : "=&{v[192:223]}"(w) : "v"(a0), "v"(a1) : "memory");
}
template <> __device__ __forceinline__ void qk_wload<5>(u32x32& w, unsigned a0, unsigned a1) {
  asm volatile("ds_read_b64_tr_b16 v[224:225], %1 offset:0\n\t"
    "ds_read_b64_tr_b16 v[226:227], %1 offset:512\n\t"
    "ds_read_b64_tr_b16 v[228:229], %1 offset:4096\n\t"
    "ds_read_b64_tr_b16 v[230:231], %1 offset:4608\n\t"
    "ds_read_b64_tr_b16 v[232:233], %2 offset:0\n\t"
    "ds_read_b64_tr_b16 v[234:235], %2 offset:512\n\t"
    "ds_read_b64_tr_b16 v[236:237], %2 offset:4096\n\t"
    "ds_read_b64_tr_b16 v[238:239], %2 offset:4608\n\t"
    "s_waitcnt lgkmcnt(0)\n\t"
    "ds_read_b64_tr_b16 v[240:241], %1 offset:16384\n\t"
    "ds_read_b64_tr_b16 v[242:243], %1 offset:16896\n\t"
    "ds_read_b64_tr_b16 v[244:245], %1 offset:20480\n\t"
    "ds_read_b64_tr_b16 v[246:247], %1 offset:20992\n\t"
    "ds_read_b64_tr_b16 v[248:249], %2 offset:16384\n\t"
    "ds_read_b64_tr_b16 v[250:251], %2 offset:16896\n\t"
    "ds_read_b64_tr_b16 v[252:253], %2 offset:20480\n\t"
    "ds_read_b64_tr_b16 v[254:255], %2 offset:20992\n\t"
    "s_waitcnt lgkmcnt(0)"
    : "=&{v[224:255]}"(w) : "v"(a0), "v"(a1) : "memory");
}
#define QK_OWN 0
template <int I> __device__ __forceinline__ void qk_swap(u32x32& w, unsigned a);
template <> __device__ __forceinline__ void qk_swap<0>(u32x32& w, unsigned a) {
  asm volatile("ds_read_b128 v[8:11], %1 offset:0\n\t"
    "ds_read_b128 v[12:15], %1 offset:1024\n\t"
    "ds_read_b128 v[16:19], %1 offset:8192\n\t"
    "ds_read_b128 v[20:23], %1 offset:9216\n\t"
    "ds_read_b128 v[24:27], %1 offset:16384\n\t"
    "ds_read_b128 v[28:31], %1 offset:17408\n\t"
    "ds_read_b128 v[32:35], %1 offset:24576\n\t"
    "ds_read_b128 v[36:39], %1 offset:25600\n\t"
    "ds_write_b128 %1, v[64:67] offset:0\n\t"
    "ds_write_b128 %1, v[68:71] offset:1024\n\t"
    "ds_write_b128 %1, v[72:75] offset:8192\n\t"
    "ds_write_b128 %1, v[76:79] offset:9216\n\t"
    "ds_write_b128 %1, v[80:83] offset:16384\n\t"
    "ds_write_b128 %1, v[84:87] offset:17408\n\t"
    "ds_write_b128 %1, v[88:91] offset:24576\n\t"
    "ds_write_b128 %1, v[92:95] offset:25600\n\t"
    "s_waitcnt lgkmcnt(0)\n\t"
    "v_mov_b32 v64, v8\n\t"
    "v_mov_b32 v65, v9\n\t"
    "v_mov_b32 v66, v10\n\t"
    "v_mov_b32 v67, v11\n\t"
    "v_mov_b32 v68, v12\n\t"
    "v_mov_b32 v69, v13\n\t"
    "v_mov_b32 v70, v14\n\t"
    "v_mov_b32 v71, v15\n\t"
    "v_mov_b32 v72, v16\n\t"
    "v_mov_b32 v73, v17\n\t"
    "v_mov_b32 v74, v18\n\t"
    "v_mov_b32 v75, v19\n\t"
    "v_mov_b32 v76, v20\n\t"
    "v_mov_b32 v77, v21\n\t"
    "v_mov_b32 v78, v22\n\t"
    "v_mov_b32 v79, v23\n\t"
    "v_mov_b32 v80, v24\n\t"
    "v_mov_b32 v81, v25\n\t"
    "v_mov_b32 v82, v26\n\t"
    "v_mov_b32 v83, v27\n\t"
    "v_mov_b32 v84, v28\n\t"
    "v_mov_b32 v85, v29\n\t"
    "v_mov_b32 v86, v30\n\t"
    "v_mov_b32 v87, v31\n\t"
    "v_mov_b32 v88, v32\n\t"
    "v_mov_b32 v89, v33\n\t"
    "v_mov_b32 v90, v34\n\t"
    "v_mov_b32 v91, v35\n\t"
    "v_mov_b32 v92, v36\n\t"
    "v_mov_b32 v93, v37\n\t"
    "v_mov_b32 v94, v38\n\t"
    "v_mov_b32 v95, v39"
    :: "{v[64:95]}"(w), "v"(a) : "v8", "v9", "v10", "v11", "v12", "v13", "v14", "v15", "v16", "v17", "v18", "v19", "v20", "v21", "v22", "v23", "v24", "v25", "v26", "v27", "v28", "v29", "v30", "v31", "v32", "v33", "v34", "v35", "v36", "v37", "v38", "v39", "memory");
}
template <> __device__ __forceinline__ void qk_swap<1>(u32x32& w, unsigned a) {
  asm volatile("ds_read_b128 v[8:11], %1 offset:0\n\t"
    "ds_read_b128 v[12:15], %1 offset:1024\n\t"
    "ds_read_b128 v[16:19], %1 offset:8192\n\t"
    "ds_read_b128 v[20:23], %1 offset:9216\n\t"
    "ds_read_b128 v[24:27], %1 offset:16384\n\t"
    "ds_read_b128 v[28:31], %1 offset:17408\n\t"
    "ds_read_b128 v[32:35], %1 offset:24576\n\t"
    "ds_read_b128 v[36:39], %1 offset:25600\n\t"
    "ds_write_b128 %1, v[96:99] offset:0\n\t"
    "ds_write_b128 %1, v[100:103] offset:1024\n\t"
    "ds_write_b128 %1, v[104:107] offset:8192\n\t"
    "ds_write_b128 %1, v[108:111] offset:9216\n\t"
    "ds_write_b128 %1, v[112:115] offset:16384\n\t"
    "ds_write_b128 %1, v[116:119] offset:17408\n\t"
    "ds_write_b128 %1, v[120:123] offset:24576\n\t"
    "ds_write_b128 %1, v[124:127] offset:25600\n\t"
    "s_waitcnt lgkmcnt(0)\n\t"
    "v_mov_b32 v96, v8\n\t"
    "v_mov_b32 v97, v9\n\t"
    "v_mov_b32 v98, v10\n\t"
    "v_mov_b32 v99, v11\n\t"
    "v_mov_b32 v100, v12\n\t"
    "v_mov_b32 v101, v13\n\t"
    "v_mov_b32 v102, v14\n\t"
    "v_mov_b32 v103, v15\n\t"
    "v_mov_b32 v104, v16\n\t"
    "v_mov_b32 v105, v17\n\t"
    "v_mov_b32 v106, v18\n\t"
    "v_mov_b32 v107, v19\n\t"
    "v_mov_b32 v108, v20\n\t"
    "v_mov_b32 v109, v21\n\t"
    "v_mov_b32 v110, v22\n\t"
    "v_mov_b32 v111, v23\n\t"
    "v_mov_b32 v112, v24\n\t"
    "v_mov_b32 v113, v25\n\t"
    "v_mov_b32 v114, v26\n\t"
    "v_mov_b32 v115, v27\n\t"
    "v_mov_b32 v116, v28\n\t"
    "v_mov_b32 v117, v29\n\t"
    "v_mov_b32 v118, v30\n\t"
    "v_mov_b32 v119, v31\n\t"
    "v_mov_b32 v120, v32\n\t"
    "v_mov_b32 v121, v33\n\t"
    "v_mov_b32 v122, v34\n\t"
    "v_mov_b32 v123, v35\n\t"
    "v_mov_b32 v124, v36\n\t"
    "v_mov_b32 v125, v37\n\t"
    "v_mov_b32 v126, v38\n\t"
    "v_mov_b32 v127, v39"
    :: "{v[96:127]}"(w), "v"(a) : "v8", "v9", "v10", "v11", "v12", "v13", "v14", "v15", "v16", "v17", "v18", "v19", "v20", "v21", "v22", "v23", "v24", "v25", "v26", "v27", "v28", "v29", "v30", "v31", "v32", "v33", "v34", "v35", "v36", "v37", "v38", "v39", "memory");
}
template <> __device__ __forceinline__ void qk_swap<2>(u32x32& w, unsigned a) {
  asm volatile("ds_read_b128 v[8:11], %1 offset:0\n\t"
    "ds_read_b128 v[12:15], %1 offset:1024\n\t"
    "ds_read_b128 v[16:19], %1 offset:8192\n\t"
    "ds_read_b128 v[20:23], %1 offset:9216\n\t"
    "ds_read_b128 v[24:27], %1 offset:16384\n\t"
    "ds_read_b128 v[28:31], %1 offset:17408\n\t"
    "ds_read_b128 v[32:35], %1 offset:24576\n\t"
    "ds_read_b128 v[36:39], %1 offset:25600\n\t"
    "ds_write_b128 %1, v[128:131] offset:0\n\t"
    "ds_write_b128 %1, v[132:135] offset:1024\n\t"
    "ds_write_b128 %1, v[136:139] offset:8192\n\t"
    "ds_write_b128 %1, v[140:143] offset:9216\n\t"
    "ds_write_b128 %1, v[144:147] offset:16384\n\t"
    "ds_write_b128 %1, v[148:151] offset:17408\n\t"
    "ds_write_b128 %1, v[152:155] offset:24576\n\t"
    "ds_write_b128 %1, v[156:159] offset:25600\n\t"
    "s_waitcnt lgkmcnt(0)\n\t"
    "v_mov_b32 v128, v8\n\t"
    "v_mov_b32 v129, v9\n\t"
    "v_mov_b32 v130, v10\n\t"
    "v_mov_b32 v131, v11\n\t"
    "v_mov_b32 v132, v12\n\t"
    "v_mov_b32 v133, v13\n\t"
    "v_mov_b32 v134, v14\n\t"
    "v_mov_b32 v135, v15\n\t"
    "v_mov_b32 v136, v16\n\t"
    "v_mov_b32 v137, v17\n\t"
    "v_mov_b32 v138, v18\n\t"
    "v_mov_b32 v139, v19\n\t"
    "v_mov_b32 v140, v20\n\t"
    "v_mov_b32 v141, v21\n\t"
    "v_mov_b32 v142, v22\n\t"
    "v_mov_b32 v143, v23\n\t"
    "v_mov_b32 v144, v24\n\t"
    "v_mov_b32 v145, v25\n\t"
    "v_mov_b32 v146, v26\n\t"
    "v_mov_b32 v147, v27\n\t"
    "v_mov_b32 v148, v28\n\t"
    "v_mov_b32 v149, v29\n\t"
    "v_mov_b32 v150, v30\n\t"
    "v_mov_b32 v151, v31\n\t"
    "v_mov_b32 v152, v32\n\t"
    "v_mov_b32 v153, v33\n\t"
    "v_mov_b32 v154, v34\n\t"
    "v_mov_b32 v155, v35\n\t"
    "v_mov_b32 v156, v36\n\t"
    "v_mov_b32 v157, v37\n\t"
    "v_mov_b32 v158, v38\n\t"
    "v_mov_b32 v159, v39"
    :: "{v[128:159]}"(w), "v"(a) : "v8", "v9", "v10", "v11", "v12", "v13", "v14", "v15", "v16", "v17", "v18", "v19", "v20", "v21", "v22", "v23", "v24", "v25", "v26", "v27", "v28", "v29", "v30", "v31", "v32", "v33", "v34", "v35", "v36", "v37", "v38", "v39", "memory");
}
template <> __device__ __forceinline__ void qk_swap<3>(u32x32& w, unsigned a) {
  asm volatile("ds_read_b128 v[8:11], %1 offset:0\n\t"
    "ds_read_b128 v[12:15], %1 offset:1024\n\t"
    "ds_read_b128 v[16:19], %1 offset:8192\n\t"
    "ds_read_b128 v[20:23], %1 offset:9216\n\t"
    "ds_read_b128 v[24:27], %1 offset:16384\n\t"
    "ds_read_b128 v[28:31], %1 offset:17408\n\t"
    "ds_read_b128 v[32:35], %1 offset:24576\n\t"
    "ds_read_b128 v[36:39], %1 offset:25600\n\t"
    "ds_write_b128 %1, v[160:163] offset:0\n\t"
    "ds_write_b128 %1, v[164:167] offset:1024\n\t"
    "ds_write_b128 %1, v[168:171] offset:8192\n\t"
    "ds_write_b128 %1, v[172:175] offset:9216\n\t"
    "ds_write_b128 %1, v[176:179] offset:16384\n\t"
    "ds_write_b128 %1, v[180:183] offset:17408\n\t"
    "ds_write_b128 %1, v[184:187] offset:24576\n\t"
    "ds_write_b128 %1, v[188:191] offset:25600\n\t"
    "s_waitcnt lgkmcnt(0)\n\t"
    "v_mov_b32 v160, v8\n\t"
    "v_mov_b32 v161, v9\n\t"
    "v_mov_b32 v162, v10\n\t"
    "v_mov_b32 v163, v11\n\t"
    "v_mov_b32 v164, v12\n\t"
    "v_mov_b32 v165, v13\n\t"
    "v_mov_b32 v166, v14\n\t"
    "v_mov_b32 v167, v15\n\t"
    "v_mov_b32 v168, v16\n\t"
    "v_mov_b32 v169, v17\n\t"
    "v_mov_b32 v170, v18\n\t"
    "v_mov_b32 v171, v19\n\t"
    "v_mov_b32 v172, v20\n\t"
    "v_mov_b32 v173, v21\n\t"
    "v_mov_b32 v174, v22\n\t"
    "v_mov_b32 v175, v23\n\t"
    "v_mov_b32 v176, v24\n\t"
    "v_mov_b32 v177, v25\n\t"
    "v_mov_b32 v178, v26\n\t"
    "v_mov_b32 v179, v27\n\t"
    "v_mov_b32 v180, v28\n\t"
    "v_mov_b32 v181, v29\n\t"
    "v_mov_b32 v182, v30\n\t"
    "v_mov_b32 v183, v31\n\t"
    "v_mov_b32 v184, v32\n\t"
    "v_mov_b32 v185, v33\n\t"
    "v_mov_b32 v186, v34\n\t"
    "v_mov_b32 v187, v35\n\t"
    "v_mov_b32 v188, v36\n\t"
    "v_mov_b32 v189, v37\n\t"
    "v_mov_b32 v190, v38\n\t"
    "v_mov_b32 v191, v39"
    :: "{v[160:191]}"(w), "v"(a) : "v8", "v9", "v10", "v11", "v12", "v13", "v14", "v15", "v16", "v17", "v18", "v19", "v20", "v21", "v22", "v23", "v24", "v25", "v26", "v27", "v28", "v29", "v30", "v31", "v32", "v33", "v34", "v35", "v36", "v37", "v38", "v39", "memory");
}
template <> __device__ __forceinline__ void qk_swap<4>(u32x32& w, unsigned a) {
  asm volatile("ds_read_b128 v[8:11], %1 offset:0\n\t"
    "ds_read_b128 v[12:15], %1 offset:1024\n\t"
    "ds_read_b128 v[16:19], %1 offset:8192\n\t"
    "ds_read_b128 v[20:23], %1 offset:9216\n\t"
    "ds_read_b128 v[24:27], %1 offset:16384\n\t"
    "ds_read_b128 v[28:31], %1 offset:17408\n\t"
    "ds_read_b128 v[32:35], %1 offset:24576\n\t"
    "ds_read_b128 v[36:39], %1 offset:25600\n\t"
    "ds_write_b128 %1, v[192:195] offset:0\n\t"
    "ds_write_b128 %1, v[196:199] offset:1024\n\t"
    "ds_write_b128 %1, v[200:203] offset:8192\n\t"
    "ds_write_b128 %1, v[204:207] offset:9216\n\t"
    "ds_write_b128 %1, v[208:211] offset:16384\n\t"
    "ds_write_b128 %1, v[212:215] offset:17408\n\t"
    "ds_write_b128 %1, v[216:219] offset:24576\n\t"
    "ds_write_b128 %1, v[220:223] offset:25600\n\t"
    "s_waitcnt lgkmcnt(0)\n\t"
    "v_mov_b32 v192, v8\n\t"
    "v_mov_b32 v193, v9\n\t"
    "v_mov_b32 v194, v10\n\t"
    "v_mov_b32 v195, v11\n\t"
    "v_mov_b32 v196, v12\n\t"
    "v_mov_b32 v197, v13\n\t"
    "v_mov_b32 v198, v14\n\t"
    "v_mov_b32 v199, v15\n\t"
    "v_mov_b32 v200, v16\n\t"
    "v_mov_b32 v201, v17\n\t"
    "v_mov_b32 v202, v18\n\t"
    "v_mov_b32 v203, v19\n\t"
    "v_mov_b32 v204, v20\n\t"
    "v_mov_b32 v205, v21\n\t"
    "v_mov_b32 v206, v22\n\t"
    "v_mov_b32 v207, v23\n\t"
    "v_mov_b32 v208, v24\n\t"
    "v_mov_b32 v209, v25\n\t"
    "v_mov_b32 v210, v26\n\t"
    "v_mov_b32 v211, v27\n\t"
    "v_mov_b32 v212, v28\n\t"
    "v_mov_b32 v213, v29\n\t"
    "v_mov_b32 v214, v30\n\t"
    "v_mov_b32 v215, v31\n\t"
    "v_mov_b32 v216, v32\n\t"
    "v_mov_b32 v217, v33\n\t"
    "v_mov_b32 v218, v34\n\t"
    "v_mov_b32 v219, v35\n\t"
    "v_mov_b32 v220, v36\n\t"
    "v_mov_b32 v221, v37\n\t"
    "v_mov_b32 v222, v38\n\t"
    "v_mov_b32 v223, v39"
    :: "{v[192:223]}"(w), "v"(a) : "v8", "v9", "v10", "v11", "v12", "v13", "v14", "v15", "v16", "v17", "v18", "v19", "v20", "v21", "v22", "v23", "v24", "v25", "v26", "v27", "v28", "v29", "v30", "v31", "v32", "v33", "v34", "v35", "v36", "v37", "v38", "v39", "memory");
}
template <> __device__ __forceinline__ void qk_swap<5>(u32x32& w, unsigned a) {
  asm volatile("ds_read_b128 v[8:11], %1 offset:0\n\t"
    "ds_read_b128 v[12:15], %1 offset:1024\n\t"
    "ds_read_b128 v[16:19], %1 offset:8192\n\t"
    "ds_read_b128 v[20:23], %1 offset:9216\n\t"
    "ds_read_b128 v[24:27], %1 offset:16384\n\t"
    "ds_read_b128 v[28:31], %1 offset:17408\n\t"
    "ds_read_b128 v[32:35], %1 offset:24576\n\t"
    "ds_read_b128 v[36:39], %1 offset:25600\n\t"
    "ds_write_b128 %1, v[224:227] offset:0\n\t"
    "ds_write_b128 %1, v[228:231] offset:1024\n\t"
    "ds_write_b128 %1, v[232:235] offset:8192\n\t"
    "ds_write_b128 %1, v[236:239] offset:9216\n\t"
    "ds_write_b128 %1, v[240:243] offset:16384\n\t"
    "ds_write_b128 %1, v[244:247] offset:17408\n\t"
    "ds_write_b128 %1, v[248:251] offset:24576\n\t"
    "ds_write_b128 %1, v[252:255] offset:25600\n\t"
    "s_waitcnt lgkmcnt(0)\n\t"
    "v_mov_b32 v224, v8\n\t"
    "v_mov_b32 v225, v9\n\t"
    "v_mov_b32 v226, v10\n\t"
    "v_mov_b32 v227, v11\n\t"
    "v_mov_b32 v228, v12\n\t"
    "v_mov_b32 v229, v13\n\t"
    "v_mov_b32 v230, v14\n\t"
    "v_mov_b32 v231, v15\n\t"
    "v_mov_b32 v232, v16\n\t"
    "v_mov_b32 v233, v17\n\t"
    "v_mov_b32 v234, v18\n\t"
    "v_mov_b32 v235, v19\n\t"
    "v_mov_b32 v236, v20\n\t"
    "v_mov_b32 v237, v21\n\t"
    "v_mov_b32 v238, v22\n\t"
    "v_mov_b32 v239, v23\n\t"
    "v_mov_b32 v240, v24\n\t"
    "v_mov_b32 v241, v25\n\t"
    "v_mov_b32 v242, v26\n\t"
    "v_mov_b32 v243, v27\n\t"
    "v_mov_b32 v244, v28\n\t"
    "v_mov_b32 v245, v29\n\t"
    "v_mov_b32 v246, v30\n\t"
    "v_mov_b32 v247, v31\n\t"
    "v_mov_b32 v248, v32\n\t"
    "v_mov_b32 v249, v33\n\t"
    "v_mov_b32 v250, v34\n\t"
    "v_mov_b32 v251, v35\n\t"
    "v_mov_b32 v252, v36\n\t"
    "v_mov_b32 v253, v37\n\t"
    "v_mov_b32 v254, v38\n\t"
    "v_mov_b32 v255, v39"
    :: "{v[224:255]}"(w), "v"(a) : "v8", "v9", "v10", "v11", "v12", "v13", "v14", "v15", "v16", "v17", "v18", "v19", "v20", "v21", "v22", "v23", "v24", "v25", "v26", "v27", "v28", "v29", "v30", "v31", "v32", "v33", "v34", "v35", "v36", "v37", "v38", "v39", "memory");
}
template <int I> __device__ __forceinline__ void qk_swapk(u32x32& w, u32x8& x, unsigned a);
template <> __device__ __forceinline__ void qk_swapk<2>(u32x32& w, u32x8& x, unsigned a) {
  asm volatile("ds_read_b128 v[16:19], %2 offset:0\n\t"
    "ds_read_b128 v[20:23], %2 offset:1024\n\t"
    "ds_read_b128 v[24:27], %2 offset:8192\n\t"
    "ds_read_b128 v[28:31], %2 offset:9216\n\t"
    "ds_read_b128 v[32:35], %2 offset:16384\n\t"
    "ds_read_b128 v[36:39], %2 offset:17408\n\t"
    "ds_write_b128 %2, v[136:139] offset:0\n\t"
    "ds_write_b128 %2, v[140:143] offset:1024\n\t"
    "ds_write_b128 %2, v[144:147] offset:8192\n\t"
    "ds_write_b128 %2, v[148:151] offset:9216\n\t"
    "ds_write_b128 %2, v[152:155] offset:16384\n\t"
    "ds_write_b128 %2, v[156:159] offset:17408\n\t"
    "v_swap_b32 v128, v56\n\t"
    "v_swap_b32 v129, v57\n\t"
    "v_swap_b32 v130, v58\n\t"
    "v_swap_b32 v131, v59\n\t"
    "v_swap_b32 v132, v60\n\t"
    "v_swap_b32 v133, v61\n\t"
    "v_swap_b32 v134, v62\n\t"
    "v_swap_b32 v135, v63\n\t"
    "s_waitcnt lgkmcnt(0)\n\t"
    "v_mov_b32 v136, v16\n\t"
    "v_mov_b32 v137, v17\n\t"
    "v_mov_b32 v138, v18\n\t"
    "v_mov_b32 v139, v19\n\t"
    "v_mov_b32 v140, v20\n\t"
    "v_mov_b32 v141, v21\n\t"
    "v_mov_b32 v142, v22\n\t"
    "v_mov_b32 v143, v23\n\t"
    "v_mov_b32 v144, v24\n\t"
    "v_mov_b32 v145, v25\n\t"
    "v_mov_b32 v146, v26\n\t"
    "v_mov_b32 v147, v27\n\t"
    "v_mov_b32 v148, v28\n\t"
    "v_mov_b32 v149, v29\n\t"
    "v_mov_b32 v150, v30\n\t"
    "v_mov_b32 v151, v31\n\t"
    "v_mov_b32 v152, v32\n\t"
    "v_mov_b32 v153, v33\n\t"
    "v_mov_b32 v154, v34\n\t"
    "v_mov_b32 v155, v35\n\t"
    "v_mov_b32 v156, v36\n\t"
    "v_mov_b32 v157, v37\n\t"
    "v_mov_b32 v158, v38\n\t"
    "v_mov_b32 v159, v39"
    :: "{v[128:159]}"(w), "{v[56:63]}"(x), "v"(a) : "v16", "v17", "v18", "v19", "v20", "v21", "v22", "v23", "v24", "v25", "v26", "v27", "v28", "v29", "v30", "v31", "v32", "v33", "v34", "v35", "v36", "v37", "v38", "v39", "memory");
}
template <> __device__ __forceinline__ void qk_swapk<3>(u32x32& w, u32x8& x, unsigned a) {
  asm volatile("ds_read_b128 v[16:19], %2 offset:0\n\t"
    "ds_read_b128 v[20:23], %2 offset:1024\n\t"
    "ds_read_b128 v[24:27], %2 offset:8192\n\t"
    "ds_read_b128 v[28:31], %2 offset:9216\n\t"
    "ds_read_b128 v[32:35], %2 offset:16384\n\t"
    "ds_read_b128 v[36:39], %2 offset:17408\n\t"
    "ds_write_b128 %2, v[168:171] offset:0\n\t"
    "ds_write_b128 %2, v[172:175] offset:1024\n\t"
    "ds_write_b128 %2, v[176:179] offset:8192\n\t"
    "ds_write_b128 %2, v[180:183] offset:9216\n\t"
    "ds_write_b128 %2, v[184:187] offset:16384\n\t"
    "ds_write_b128 %2, v[188:191] offset:17408\n\t"
    "v_swap_b32 v160, v56\n\t"
    "v_swap_b32 v161, v57\n\t"
    "v_swap_b32 v162, v58\n\t"
    "v_swap_b32 v163, v59\n\t"
    "v_swap_b32 v164, v60\n\t"
    "v_swap_b32 v165, v61\n\t"
    "v_swap_b32 v166, v62\n\t"
    "v_swap_b32 v167, v63\n\t"
    "s_waitcnt lgkmcnt(0)\n\t"
    "v_mov_b32 v168, v16\n\t"
    "v_mov_b32 v169, v17\n\t"
    "v_mov_b32 v170, v18\n\t"
    "v_mov_b32 v171, v19\n\t"
    "v_mov_b32 v172, v20\n\t"
    "v_mov_b32 v173, v21\n\t"
    "v_mov_b32 v174, v22\n\t"
    "v_mov_b32 v175, v23\n\t"
    "v_mov_b32 v176, v24\n\t"
    "v_mov_b32 v177, v25\n\t"
    "v_mov_b32 v178, v26\n\t"
    "v_mov_b32 v179, v27\n\t"
    "v_mov_b32 v180, v28\n\t"
    "v_mov_b32 v181, v29\n\t"
    "v_mov_b32 v182, v30\n\t"
    "v_mov_b32 v183, v31\n\t"
    "v_mov_b32 v184, v32\n\t"
    "v_mov_b32 v185, v33\n\t"
    "v_mov_b32 v186, v34\n\t"
    "v_mov_b32 v187, v35\n\t"
    "v_mov_b32 v188, v36\n\t"
    "v_mov_b32 v189, v37\n\t"
    "v_mov_b32 v190, v38\n\t"
    "v_mov_b32 v191, v39"
    :: "{v[160:191]}"(w), "{v[56:63]}"(x), "v"(a) : "v16", "v17", "v18", "v19", "v20", "v21", "v22", "v23", "v24", "v25", "v26", "v27", "v28", "v29", "v30", "v31", "v32", "v33", "v34", "v35", "v36", "v37", "v38", "v39", "memory");
}
template <> __device__ __forceinline__ void qk_swapk<4>(u32x32& w, u32x8& x, unsigned a) {
  asm volatile("ds_read_b128 v[16:19], %2 offset:0\n\t"
    "ds_read_b128 v[20:23], %2 offset:1024\n\t"
    "ds_read_b128 v[24:27], %2 offset:8192\n\t"
    "ds_read_b128 v[28:31], %2 offset:9216\n\t"
    "ds_read_b128 v[32:35], %2 offset:16384\n\t"
    "ds_read_b128 v[36:39], %2 offset:17408\n\t"
    "ds_write_b128 %2, v[200:203] offset:0\n\t"
    "ds_write_b128 %2, v[204:207] offset:1024\n\t"
    "ds_write_b128 %2, v[208:211] offset:8192\n\t"
    "ds_write_b128 %2, v[212:215] offset:9216\n\t"
    "ds_write_b128 %2, v[216:219] offset:16384\n\t"
    "ds_write_b128 %2, v[220:223] offset:17408\n\t"
    "v_swap_b32 v192, v56\n\t"
    "v_swap_b32 v193, v57\n\t"
    "v_swap_b32 v194, v58\n\t"
    "v_swap_b32 v195, v59\n\t"
    "v_swap_b32 v196, v60\n\t"
    "v_swap_b32 v197, v61\n\t"
    "v_swap_b32 v198, v62\n\t"
    "v_swap_b32 v199, v63\n\t"
    "s_waitcnt lgkmcnt(0)\n\t"
    "v_mov_b32 v200, v16\n\t"
    "v_mov_b32 v201, v17\n\t"
    "v_mov_b32 v202, v18\n\t"
    "v_mov_b32 v203, v19\n\t"
    "v_mov_b32 v204, v20\n\t"
    "v_mov_b32 v205, v21\n\t"
    "v_mov_b32 v206, v22\n\t"
    "v_mov_b32 v207, v23\n\t"
    "v_mov_b32 v208, v24\n\t"
    "v_mov_b32 v209, v25\n\t"
    "v_mov_b32 v210, v26\n\t"
    "v_mov_b32 v211, v27\n\t"
    "v_mov_b32 v212, v28\n\t"
    "v_mov_b32 v213, v29\n\t"
    "v_mov_b32 v214, v30\n\t"
    "v_mov_b32 v215, v31\n\t"
    "v_mov_b32 v216, v32\n\t"
    "v_mov_b32 v217, v33\n\t"
    "v_mov_b32 v218, v34\n\t"
    "v_mov_b32 v219, v35\n\t"
    "v_mov_b32 v220, v36\n\t"
    "v_mov_b32 v221, v37\n\t"
    "v_mov_b32 v222, v38\n\t"
    "v_mov_b32 v223, v39"
    :: "{v[192:223]}"(w), "{v[56:63]}"(x), "v"(a) : "v16", "v17", "v18", "v19", "v20", "v21", "v22", "v23", "v24", "v25", "v26", "v27", "v28", "v29", "v30", "v31", "v32", "v33", "v34", "v35", "v36", "v37", "v38", "v39", "memory");
}
template <> __device__ __forceinline__ void qk_swapk<5>(u32x32& w, u32x8& x, unsigned a) {
  asm volatile("ds_read_b128 v[16:19], %2 offset:0\n\t"
    "ds_read_b128 v[20:23], %2 offset:1024\n\t"
    "ds_read_b128 v[24:27], %2 offset:8192\n\t"
    "ds_read_b128 v[28:31], %2 offset:9216\n\t"
    "ds_read_b128 v[32:35], %2 offset:16384\n\t"
    "ds_read_b128 v[36:39], %2 offset:17408\n\t"
    "ds_write_b128 %2, v[232:235] offset:0\n\t"
    "ds_write_b128 %2, v[236:239] offset:1024\n\t"
    "ds_write_b128 %2, v[240:243] offset:8192\n\t"
    "ds_write_b128 %2, v[244:247] offset:9216\n\t"
    "ds_write_b128 %2, v[248:251] offset:16384\n\t"
    "ds_write_b128 %2, v[252:255] offset:17408\n\t"
    "v_swap_b32 v224, v56\n\t"
    "v_swap_b32 v225, v57\n\t"
    "v_swap_b32 v226, v58\n\t"
    "v_swap_b32 v227, v59\n\t"
    "v_swap_b32 v228, v60\n\t"
    "v_swap_b32 v229, v61\n\t"
    "v_swap_b32 v230, v62\n\t"
    "v_swap_b32 v231, v63\n\t"
    "s_waitcnt lgkmcnt(0)\n\t"
    "v_mov_b32 v232, v16\n\t"
    "v_mov_b32 v233, v17\n\t"
    "v_mov_b32 v234, v18\n\t"
    "v_mov_b32 v235, v19\n\t"
    "v_mov_b32 v236, v20\n\t"
    "v_mov_b32 v237, v21\n\t"
    "v_mov_b32 v238, v22\n\t"
    "v_mov_b32 v239, v23\n\t"
    "v_mov_b32 v240, v24\n\t"
    "v_mov_b32 v241, v25\n\t"
    "v_mov_b32 v242, v26\n\t"
    "v_mov_b32 v243, v27\n\t"
    "v_mov_b32 v244, v28\n\t"
    "v_mov_b32 v245, v29\n\t"
    "v_mov_b32 v246, v30\n\t"
    "v_mov_b32 v247, v31\n\t"
    "v_mov_b32 v248, v32\n\t"
    "v_mov_b32 v249, v33\n\t"
    "v_mov_b32 v250, v34\n\t"
    "v_mov_b32 v251, v35\n\t"
    "v_mov_b32 v252, v36\n\t"
    "v_mov_b32 v253, v37\n\t"
    "v_mov_b32 v254, v38\n\t"
    "v_mov_b32 v255, v39"
    :: "{v[224:255]}"(w), "{v[56:63]}"(x), "v"(a) : "v16", "v17", "v18", "v19", "v20", "v21", "v22", "v23", "v24", "v25", "v26", "v27", "v28", "v29", "v30", "v31", "v32", "v33", "v34", "v35", "v36", "v37", "v38", "v39", "memory");
}
template <int I> __device__ __forceinline__ void vr_wload(u32x32& w, unsigned a);
template <> __device__ __forceinline__ void vr_wload<0>(u32x32& w, unsigned a) {
  asm volatile("ds_read_b64_tr_b16 v[64:65], %1 offset:0\n\t"
    "ds_read_b64_tr_b16 v[66:67], %1 offset:256\n\t"
    "ds_read_b64_tr_b16 v[68:69], %1 offset:2048\n\t"
    "ds_read_b64_tr_b16 v[70:71], %1 offset:2304\n\t"
    "ds_read_b64_tr_b16 v[72:73], %1 offset:32\n\t"
    "ds_read_b64_tr_b16 v[74:75], %1 offset:288\n\t"
    "ds_read_b64_tr_b16 v[76:77], %1 offset:2080\n\t"
    "ds_read_b64_tr_b16 v[78:79], %1 offset:2336\n\t"
    "s_waitcnt lgkmcnt(0)\n\t"
    "ds_read_b64_tr_b16 v[80:81], %1 offset:64\n\t"
    "ds_read_b64_tr_b16 v[82:83], %1 offset:320\n\t"
    "ds_read_b64_tr_b16 v[84:85], %1 offset:2112\n\t"
    "ds_read_b64_tr_b16 v[86:87], %1 offset:2368\n\t"
    "ds_read_b64_tr_b16 v[88:89], %1 offset:96\n\t"
    "ds_read_b64_tr_b16 v[90:91], %1 offset:352\n\t"
    "ds_read_b64_tr_b16 v[92:93], %1 offset:2144\n\t"
    "ds_read_b64_tr_b16 v[94:95], %1 offset:2400\n\t"
    "s_waitcnt lgkmcnt(0)"
    : "=&{v[64:95]}"(w) : "v"(a) : "memory");
}
template <> __device__ __forceinline__ void vr_wload<1>(u32x32& w, unsigned a) {
  asm volatile("ds_read_b64_tr_b16 v[96:97], %1 offset:8192\n\t"
    "ds_read_b64_tr_b16 v[98:99], %1 offset:8448\n\t"
    "ds_read_b64_tr_b16 v[100:101], %1 offset:10240\n\t"
    "ds_read_b64_tr_b16 v[102:103], %1 offset:10496\n\t"
    "ds_read_b64_tr_b16 v[104:105], %1 offset:8224\n\t"
    "ds_read_b64_tr_b16 v[106:107], %1 offset:8480\n\t"
    "ds_read_b64_tr_b16 v[108:109], %1 offset:10272\n\t"
    "ds_read_b64_tr_b16 v[110:111], %1 offset:10528\n\t"
    "s_waitcnt lgkmcnt(0)\n\t"
    "ds_read_b64_tr_b16 v[112:113], %1 offset:8256\n\t"
    "ds_read_b64_tr_b16 v[114:115], %1 offset:8512\n\t"
    "ds_read_b64_tr_b16 v[116:117], %1 offset:10304\n\t"
    "ds_read_b64_tr_b16 v[118:119], %1 offset:10560\n\t"
    "ds_read_b64_tr_b16 v[120:121], %1 offset:8288\n\t"
    "ds_read_b64_tr_b16 v[122:123], %1 offset:8544\n\t"
    "ds_read_b64_tr_b16 v[124:125], %1 offset:10336\n\t"
    "ds_read_b64_tr_b16 v[126:127], %1 offset:10592\n\t"
    "s_waitcnt lgkmcnt(0)"
    : "=&{v[96:127]}"(w) : "v"(a) : "memory");
}
template <> __device__ __forceinline__ void vr_wload<2>(u32x32& w, unsigned a) {
  asm volatile("ds_read_b64_tr_b16 v[128:129], %1 offset:0\n\t"
    "ds_read_b64_tr_b16 v[130:131], %1 offset:128\n\t"
    "ds_read_b64_tr_b16 v[132:133], %1 offset:1024\n\t"
    "ds_read_b64_tr_b16 v[134:135], %1 offset:1152\n\t"
    "ds_read_b64_tr_b16 v[136:137], %1 offset:32\n\t"
    "ds_read_b64_tr_b16 v[138:139], %1 offset:160\n\t"
    "ds_read_b64_tr_b16 v[140:141], %1 offset:1056\n\t"
    "ds_read_b64_tr_b16 v[142:143], %1 offset:1184\n\t"
    "s_waitcnt lgkmcnt(0)\n\t"
    "ds_read_b64_tr_b16 v[144:145], %1 offset:4096\n\t"
    "ds_read_b64_tr_b16 v[146:147], %1 offset:4224\n\t"
    "ds_read_b64_tr_b16 v[148:149], %1 offset:5120\n\t"
    "ds_read_b64_tr_b16 v[150:151], %1 offset:5248\n\t"
    "ds_read_b64_tr_b16 v[152:153], %1 offset:4128\n\t"
    "ds_read_b64_tr_b16 v[154:155], %1 offset:4256\n\t"
    "ds_read_b64_tr_b16 v[156:157], %1 offset:5152\n\t"
    "ds_read_b64_tr_b16 v[158:159], %1 offset:5280\n\t"
    "s_waitcnt lgkmcnt(0)"
    : "=&{v[128:159]}"(w) : "v"(a) : "memory");
}
template <> __device__ __forceinline__ void vr_wload<3>(u32x32& w, unsigned a) {
  asm volatile("ds_read_b64_tr_b16 v[160:161], %1 offset:8192\n\t"
    "ds_read_b64_tr_b16 v[162:163], %1 offset:8320\n\t"
    "ds_read_b64_tr_b16 v[164:165], %1 offset:9216\n\t"
    "ds_read_b64_tr_b16 v[166:167], %1 offset:9344\n\t"
    "ds_read_b64_tr_b16 v[168:169], %1 offset:8224\n\t"
    "ds_read_b64_tr_b16 v[170:171], %1 offset:8352\n\t"
    "ds_read_b64_tr_b16 v[172:173], %1 offset:9248\n\t"
    "ds_read_b64_tr_b16 v[174:175], %1 offset:9376\n\t"
    "s_waitcnt lgkmcnt(0)\n\t"
    "ds_read_b64_tr_b16 v[176:177], %1 offset:12288\n\t"
    "ds_read_b64_tr_b16 v[178:179], %1 offset:12416\n\t"
    "ds_read_b64_tr_b16 v[180:181], %1 offset:13312\n\t"
    "ds_read_b64_tr_b16 v[182:183], %1 offset:13440\n\t"
    "ds_read_b64_tr_b16 v[184:185], %1 offset:12320\n\t"
    "ds_read_b64_tr_b16 v[186:187], %1 offset:12448\n\t"
    "ds_read_b64_tr_b16 v[188:189], %1 offset:13344\n\t"
    "ds_read_b64_tr_b16 v[190:191], %1 offset:13472\n\t"
    "s_waitcnt lgkmcnt(0)"
    : "=&{v[160:191]}"(w) : "v"(a) : "memory");
}
template <> __device__ __forceinline__ void vr_wload<4>(u32x32& w, unsigned a) {
  asm volatile("ds_read_b64_tr_b16 v[192:193], %1 offset:0\n\t"
    "ds_read_b64_tr_b16 v[194:195], %1 offset:128\n\t"
    "ds_read_b64_tr_b16 v[196:197], %1 offset:1024\n\t"
    "ds_read_b64_tr_b16 v[198:199], %1 offset:1152\n\t"
    "ds_read_b64_tr_b16 v[200:201], %1 offset:32\n\t"
    "ds_read_b64_tr_b16 v[202:203], %1 offset:160\n\t"
    "ds_read_b64_tr_b16 v[204:205], %1 offset:1056\n\t"
    "ds_read_b64_tr_b16 v[206:207], %1 offset:1184\n\t"
    "s_waitcnt lgkmcnt(0)\n\t"
    "ds_read_b64_tr_b16 v[208:209], %1 offset:4096\n\t"
    "ds_read_b64_tr_b16 v[210:211], %1 offset:4224\n\t"
    "ds_read_b64_tr_b16 v[212:213], %1 offset:5120\n\t"
    "ds_read_b64_tr_b16 v[214:215], %1 offset:5248\n\t"
    "ds_read_b64_tr_b16 v[216:217], %1 offset:4128\n\t"
    "ds_read_b64_tr_b16 v[218:219], %1 offset:4256\n\t"
    "ds_read_b64_tr_b16 v[220:221], %1 offset:5152\n\t"
    "ds_read_b64_tr_b16 v[222:223], %1 offset:5280\n\t"
    "s_waitcnt lgkmcnt(0)"
    : "=&{v[192:223]}"(w) : "v"(a) : "memory");
}
template <> __device__ __forceinline__ void vr_wload<5>(u32x32& w, unsigned a) {
  asm volatile("ds_read_b64_tr_b16 v[224:225], %1 offset:8192\n\t"
    "ds_read_b64_tr_b16 v[226:227], %1 offset:8320\n\t"
    "ds_read_b64_tr_b16 v[228:229], %1 offset:9216\n\t"
    "ds_read_b64_tr_b16 v[230:231], %1 offset:9344\n\t"
    "ds_read_b64_tr_b16 v[232:233], %1 offset:8224\n\t"
    "ds_read_b64_tr_b16 v[234:235], %1 offset:8352\n\t"
    "ds_read_b64_tr_b16 v[236:237], %1 offset:9248\n\t"
    "ds_read_b64_tr_b16 v[238:239], %1 offset:9376\n\t"
    "s_waitcnt lgkmcnt(0)\n\t"
    "ds_read_b64_tr_b16 v[240:241], %1 offset:12288\n\t"
    "ds_read_b64_tr_b16 v[242:243], %1 offset:12416\n\t"
    "ds_read_b64_tr_b16 v[244:245], %1 offset:13312\n\t"
    "ds_read_b64_tr_b16 v[246:247], %1 offset:13440\n\t"
    "ds_read_b64_tr_b16 v[248:249], %1 offset:12320\n\t"
    "ds_read_b64_tr_b16 v[250:251], %1 offset:12448\n\t"
    "ds_read_b64_tr_b16 v[252:253], %1 offset:13344\n\t"
    "ds_read_b64_tr_b16 v[254:255], %1 offset:13472\n\t"
    "s_waitcnt lgkmcnt(0)"
    : "=&{v[224:255]}"(w) : "v"(a) : "memory");
}
#define OREP 2
#define AREP 2
struct VRW { u32x32 v0, v1, o0, o1, o2, o3; };
__device__ __forceinline__ void vr_v(const VRW& W, const char* ag, const char* vd, unsigned wa, unsigned ra, unsigned& cap, unsigned& sp, unsigned long long& tc, unsigned long long* ts) {
  asm volatile("v_mbcnt_lo_u32_b32 v16, -1, 0\n\t"
    "v_mbcnt_hi_u32_b32 v16, -1, v16\n\t"
    "v_lshlrev_b32 v17, 4, v16\n\t"
    "s_mov_b32 %[sp], 0\n\t"
    "global_load_dwordx4 v[24:27], v17, %[ag] sc1\n\t"
    "global_load_dwordx4 v[28:31], v17, %[ag] offset:1024 sc1\n\t"
    "s_memtime %[tc]\n\t"
    "v_and_b32 v22, 4, v16\n\t"
    "v_mul_u32_u24 v22, 0x2aaa, v22\n\t"
    "v_lshrrev_b32 v18, 1, v16\n\t"
    "v_and_b32 v18, 2, v18\n\t"
    "v_or3_b32 v18, v18, v22, %[c44]\n\t"
    "v_lshl_add_u32 v19, v16, 3, %[wa]\n\t"
    "v_lshl_add_u32 v20, v16, 3, %[ra]\n\t"
    "v_and_b32 v22, 40, v16\n\t"
    "v_lshlrev_b32 v21, 3, v22\n\t"
    "v_and_b32 v22, 3, v16\n\t"
    "v_lshl_or_b32 v21, v22, 4, v21\n\t"
    "v_and_b32 v22, 4, v16\n\t"
    "v_lshl_or_b32 v21, v22, 1, v21\n\t"
    "v_lshrrev_b32 v22, 2, v16\n\t"
    "v_and_or_b32 v21, v22, 4, v21\n\t"
    "3:\n\t"
    "v_mov_b32 v32, 0\n\t"
    "v_mov_b32 v33, 0\n\t"
    "v_mov_b32 v34, 0\n\t"
    "v_mov_b32 v35, 0\n\t"
    "v_mov_b32 v36, 0\n\t"
    "v_mov_b32 v37, 0\n\t"
    "v_mov_b32 v38, 0\n\t"
    "v_mov_b32 v39, 0\n\t"
    "v_mov_b32 v40, 0\n\t"
    "v_mov_b32 v41, 0\n\t"
    "v_mov_b32 v42, 0\n\t"
    "v_mov_b32 v43, 0\n\t"
    "v_mov_b32 v44, 0\n\t"
    "v_mov_b32 v45, 0\n\t"
    "v_mov_b32 v46, 0\n\t"
    "v_mov_b32 v47, 0\n\t"
    "s_waitcnt vmcnt(0) lgkmcnt(0)\n\t"
    "v_smfmac_f32_16x16x64_bf16 v[32:35], v[24:27], v[64:71], v18\n\t"
    "v_smfmac_f32_16x16x64_bf16 v[32:35], v[28:31], v[96:103], v18\n\t"
    "v_max3_u32 v22, v24, v25, v26\n\t"
    "v_max3_u32 v23, v27, v28, v29\n\t"
    "v_smfmac_f32_16x16x64_bf16 v[36:39], v[24:27], v[72:79], v18\n\t"
    "v_max3_u32 v22, v22, v30, v31\n\t"
    "v_max_u32 v22, v22, v23\n\t"
    "v_smfmac_f32_16x16x64_bf16 v[36:39], v[28:31], v[104:111], v18\n\t"
    "v_cmp_eq_u32 vcc, -1, v22\n\t"
    "s_cbranch_vccnz 1f\n\t"
    "2:\n\t"
    "v_smfmac_f32_16x16x64_bf16 v[40:43], v[24:27], v[80:87], v18\n\t"
    "v_permlane16_swap_b32 v32, v34\n\t"
    "v_permlane16_swap_b32 v33, v35\n\t"
    "v_smfmac_f32_16x16x64_bf16 v[40:43], v[28:31], v[112:119], v18\n\t"
    "v_pk_add_f32 v[32:33], v[32:33], v[34:35]\n\t"
    "ds_write_b64 v19, v[32:33] offset:0\n\t"
    "v_smfmac_f32_16x16x64_bf16 v[44:47], v[24:27], v[88:95], v18\n\t"
    "s_nop 0\n\t"
    "v_permlane16_swap_b32 v36, v38\n\t"
    "v_permlane16_swap_b32 v37, v39\n\t"
    "v_smfmac_f32_16x16x64_bf16 v[44:47], v[28:31], v[120:127], v18\n\t"
    "v_pk_add_f32 v[36:37], v[36:37], v[38:39]\n\t"
    "ds_write_b64 v19, v[36:37] offset:512\n\t"
    "v_permlane16_swap_b32 v40, v42\n\t"
    "v_permlane16_swap_b32 v41, v43\n\t"
    "v_pk_add_f32 v[40:41], v[40:41], v[42:43]\n\t"
    "ds_write_b64 v19, v[40:41] offset:1024\n\t"
    "s_nop 1\n\t"
    "v_permlane16_swap_b32 v44, v46\n\t"
    "v_permlane16_swap_b32 v45, v47\n\t"
    "v_pk_add_f32 v[44:45], v[44:45], v[46:47]\n\t"
    "ds_write_b64 v19, v[44:45] offset:1536\n\t"
    "s_cmp_ge_u32 %[wa], 8192\n\t"
    "s_waitcnt lgkmcnt(0)\n\t"
    "s_barrier\n\t"
    "s_cbranch_scc1 9f\n\t"
    "ds_read_b64 v[48:49], v20 offset:0\n\t"
    "ds_read_b64 v[50:51], v20 offset:2048\n\t"
    "ds_read_b64 v[52:53], v20 offset:4096\n\t"
    "ds_read_b64 v[54:55], v20 offset:6144\n\t"
    "ds_read_b64 v[56:57], v20 offset:8192\n\t"
    "ds_read_b64 v[58:59], v20 offset:10240\n\t"
    "ds_read_b64 v[60:61], v20 offset:12288\n\t"
    "ds_read_b64 v[62:63], v20 offset:14336\n\t"
    "s_waitcnt lgkmcnt(6)\n\t"
    "v_pk_add_f32 v[48:49], v[48:49], v[50:51]\n\t"
    "s_waitcnt lgkmcnt(4)\n\t"
    "v_pk_add_f32 v[52:53], v[52:53], v[54:55]\n\t"
    "v_pk_add_f32 v[48:49], v[48:49], v[52:53]\n\t"
    "s_waitcnt lgkmcnt(2)\n\t"
    "v_pk_add_f32 v[56:57], v[56:57], v[58:59]\n\t"
    "s_waitcnt lgkmcnt(0)\n\t"
    "v_pk_add_f32 v[60:61], v[60:61], v[62:63]\n\t"
    "v_pk_add_f32 v[56:57], v[56:57], v[60:61]\n\t"
    "v_pk_add_f32 v[48:49], v[48:49], v[56:57]\n\t"
    "v_cvt_pk_bf16_f32 v22, v48, v49\n\t"
    "v_min_u32 v22, -2, v22\n\t"
    "global_store_dword v21, v22, %[vd] sc1\n\t"
    "s_waitcnt lgkmcnt(0)\n\t"
    "s_branch 9f\n\t"
    "1:\n\t"
    "s_add_u32 %[sp], %[sp], 1\n\t"
    "s_cmp_ge_u32 %[sp], %[cap]\n\t"
    "s_cbranch_scc1 7f\n\t"
    "global_load_dwordx4 v[24:27], v17, %[ag] sc1\n\t"
    "global_load_dwordx4 v[28:31], v17, %[ag] offset:1024 sc1\n\t"
    "s_waitcnt lgkmcnt(0)\n\t"
    "s_memtime %[tc]\n\t"
    "s_nop 7\n\t"
    "s_branch 3b\n\t"
    "7:\n\t"
    "s_mov_b32 %[cap], 0\n\t"
    "s_branch 2b\n\t"
    "9:"
    : [tc] "=&s"(tc), [sp] "=&s"(sp), [cap] "+s"(cap)
    : [ag] "s"(ag), [vd] "s"(vd), [wa] "s"(wa), [ra] "s"(ra), [c44] "s"(0x4444u), "{v[64:95]}"(W.v0), "{v[96:127]}"(W.v1), "{v[128:159]}"(W.o0), "{v[160:191]}"(W.o1), "{v[192:223]}"(W.o2), "{v[224:255]}"(W.o3)
    : "v16", "v17", "v18", "v19", "v20", "v21", "v22", "v23", "v24", "v25", "v26", "v27", "v28", "v29", "v30", "v31", "v32", "v33", "v34", "v35", "v36", "v37", "v38", "v39", "v40", "v41", "v42", "v43", "v44", "v45", "v46", "v47", "v48", "v49", "v50", "v51", "v52", "v53", "v54", "v55", "v56", "v57", "v58", "v59", "v60", "v61", "v62", "v63", "vcc", "scc", "memory");
}
__device__ __forceinline__ void vr_r(const VRW& W, const char* os, const char* ad, unsigned wa, unsigned jx, unsigned& cap, unsigned& sp, unsigned long long* ts) {
  asm volatile("v_mbcnt_lo_u32_b32 v16, -1, 0\n\t"
    "v_mbcnt_hi_u32_b32 v16, -1, v16\n\t"
    "v_lshl_or_b32 v17, v16, 4, %[jx]\n\t"
    "global_load_dwordx4 v[24:27], v17, %[os] sc1\n\t"
    "v_xor_b32 v18, 2048, v17\n\t"
    "global_load_dwordx4 v[28:31], v18, %[os] sc1\n\t"
    "v_xor_b32 v57, 4096, v17\n\t"
    "global_load_dwordx4 v[32:35], v57, %[os] sc1\n\t"
    "v_xor_b32 v58, 6144, v17\n\t"
    "global_load_dwordx4 v[36:39], v58, %[os] sc1\n\t"
    "s_mov_b32 %[sp], 0\n\t"
    "v_and_b32 v23, 4, v16\n\t"
    "v_mul_u32_u24 v23, 0x2aaa, v23\n\t"
    "v_lshrrev_b32 v19, 1, v16\n\t"
    "v_and_b32 v19, 2, v19\n\t"
    "v_or3_b32 v19, v19, v23, %[c44]\n\t"
    "v_and_b32 v23, 15, v16\n\t"
    "v_lshl_add_u32 v20, v23, 2, %[wa]\n\t"
    "v_and_b32 v23, 48, v16\n\t"
    "v_lshl_add_u32 v20, v23, 3, v20\n\t"
    "v_and_b32 v23, 32, v16\n\t"
    "v_and_b32 v21, 28, v16\n\t"
    "v_lshlrev_b32 v21, 4, v21\n\t"
    "v_or_b32 v21, v21, v23\n\t"
    "v_lshlrev_b32 v22, 3, v23\n\t"
    "v_and_b32 v23, 3, v16\n\t"
    "v_lshl_or_b32 v21, v23, 3, v21\n\t"
    "v_and_b32 v23, 31, v16\n\t"
    "v_lshl_or_b32 v22, v23, 2, v22\n\t"
    "3:\n\t"
    "v_mov_b32 v40, 0\n\t"
    "v_mov_b32 v41, 0\n\t"
    "v_mov_b32 v42, 0\n\t"
    "v_mov_b32 v43, 0\n\t"
    "v_mov_b32 v44, 0\n\t"
    "v_mov_b32 v45, 0\n\t"
    "v_mov_b32 v46, 0\n\t"
    "v_mov_b32 v47, 0\n\t"
    "v_mov_b32 v48, 0\n\t"
    "v_mov_b32 v49, 0\n\t"
    "v_mov_b32 v50, 0\n\t"
    "v_mov_b32 v51, 0\n\t"
    "v_mov_b32 v52, 0\n\t"
    "v_mov_b32 v53, 0\n\t"
    "v_mov_b32 v54, 0\n\t"
    "v_mov_b32 v55, 0\n\t"
    "v_mov_b32 v56, 0\n\t"
    "s_waitcnt vmcnt(3)\n\t"
    "v_smfmac_f32_16x16x64_bf16 v[44:47], v[24:27], v[144:151], v19\n\t"
    "v_max3_u32 v56, v56, v24, v25\n\t"
    "v_smfmac_f32_16x16x64_bf16 v[48:51], v[24:27], v[136:143], v19\n\t"
    "v_max3_u32 v56, v56, v26, v27\n\t"
    "v_smfmac_f32_16x16x64_bf16 v[40:43], v[24:27], v[128:135], v19\n\t"
    "v_smfmac_f32_16x16x64_bf16 v[52:55], v[24:27], v[152:159], v19\n\t"
    "s_waitcnt vmcnt(2)\n\t"
    "v_smfmac_f32_16x16x64_bf16 v[44:47], v[28:31], v[176:183], v19\n\t"
    "v_max3_u32 v56, v56, v28, v29\n\t"
    "v_smfmac_f32_16x16x64_bf16 v[48:51], v[28:31], v[168:175], v19\n\t"
    "v_max3_u32 v56, v56, v30, v31\n\t"
    "v_smfmac_f32_16x16x64_bf16 v[40:43], v[28:31], v[160:167], v19\n\t"
    "v_smfmac_f32_16x16x64_bf16 v[52:55], v[28:31], v[184:191], v19\n\t"
    "s_waitcnt vmcnt(1)\n\t"
    "v_smfmac_f32_16x16x64_bf16 v[44:47], v[32:35], v[208:215], v19\n\t"
    "v_max3_u32 v56, v56, v32, v33\n\t"
    "v_smfmac_f32_16x16x64_bf16 v[48:51], v[32:35], v[200:207], v19\n\t"
    "v_max3_u32 v56, v56, v34, v35\n\t"
    "v_smfmac_f32_16x16x64_bf16 v[40:43], v[32:35], v[192:199], v19\n\t"
    "v_smfmac_f32_16x16x64_bf16 v[52:55], v[32:35], v[216:223], v19\n\t"
    "s_waitcnt vmcnt(0)\n\t"
    "v_smfmac_f32_16x16x64_bf16 v[44:47], v[36:39], v[240:247], v19\n\t"
    "v_max3_u32 v56, v56, v36, v37\n\t"
    "v_smfmac_f32_16x16x64_bf16 v[48:51], v[36:39], v[232:239], v19\n\t"
    "v_max3_u32 v56, v56, v38, v39\n\t"
    "v_smfmac_f32_16x16x64_bf16 v[40:43], v[36:39], v[224:231], v19\n\t"
    "v_cmp_eq_u32 vcc, -1, v56\n\t"
    "v_smfmac_f32_16x16x64_bf16 v[52:55], v[36:39], v[248:255], v19\n\t"
    "s_cbranch_vccnz 4f\n\t"
    "5:\n\t"
    "s_nop 2\n\t"
    "v_permlane32_swap_b32 v44, v48\n\t"
    "v_permlane32_swap_b32 v45, v49\n\t"
    "v_permlane32_swap_b32 v46, v50\n\t"
    "v_permlane32_swap_b32 v47, v51\n\t"
    "s_mov_b32 exec_hi, 0\n\t"
    "v_pk_add_f32 v[40:41], v[40:41], v[48:49]\n\t"
    "v_pk_add_f32 v[42:43], v[42:43], v[50:51]\n\t"
    "s_mov_b64 exec, %[hi]\n\t"
    "v_pk_add_f32 v[40:41], v[44:45], v[52:53]\n\t"
    "v_pk_add_f32 v[42:43], v[46:47], v[54:55]\n\t"
    "s_mov_b32 exec_lo, -1\n\t"
    "s_nop 0\n\t"
    "v_permlane16_swap_b32 v40, v42\n\t"
    "v_permlane16_swap_b32 v41, v43\n\t"
    "v_add_f32 v40, v40, v42\n\t"
    "v_add_f32 v41, v41, v43\n\t"
    "ds_write_b32 v20, v40 offset:16384\n\t"
    "ds_write_b32 v20, v41 offset:16448\n\t"
    "s_cmp_ge_u32 %[wa], 1024\n\t"
    "s_waitcnt lgkmcnt(0)\n\t"
    "s_barrier\n\t"
    "s_cbranch_scc1 9f\n\t"
    "ds_read_b64 v[24:25], v21 offset:16384\n\t"
    "ds_read_b64 v[26:27], v21 offset:16896\n\t"
    "ds_read_b64 v[28:29], v21 offset:17408\n\t"
    "ds_read_b64 v[30:31], v21 offset:17920\n\t"
    "ds_read_b64 v[32:33], v21 offset:18432\n\t"
    "ds_read_b64 v[34:35], v21 offset:18944\n\t"
    "ds_read_b64 v[36:37], v21 offset:19456\n\t"
    "ds_read_b64 v[38:39], v21 offset:19968\n\t"
    "s_waitcnt lgkmcnt(6)\n\t"
    "v_pk_add_f32 v[24:25], v[24:25], v[26:27]\n\t"
    "s_waitcnt lgkmcnt(4)\n\t"
    "v_pk_add_f32 v[28:29], v[28:29], v[30:31]\n\t"
    "v_pk_add_f32 v[24:25], v[24:25], v[28:29]\n\t"
    "s_waitcnt lgkmcnt(2)\n\t"
    "v_pk_add_f32 v[32:33], v[32:33], v[34:35]\n\t"
    "s_waitcnt lgkmcnt(0)\n\t"
    "v_pk_add_f32 v[36:37], v[36:37], v[38:39]\n\t"
    "v_pk_add_f32 v[32:33], v[32:33], v[36:37]\n\t"
    "v_pk_add_f32 v[24:25], v[24:25], v[32:33]\n\t"
    "v_cvt_pk_bf16_f32 v23, v24, v25\n\t"
    "v_min_u32 v23, -2, v23\n\t"
    "global_store_dword v22, v23, %[ad] sc1\n\t"
    "s_waitcnt lgkmcnt(0)\n\t"
    "s_branch 9f\n\t"
    "4:\n\t"
    "s_add_u32 %[sp], %[sp], 1\n\t"
    "s_cmp_ge_u32 %[sp], %[cap]\n\t"
    "s_cbranch_scc1 7f\n\t"
    "global_load_dwordx4 v[24:27], v17, %[os] sc1\n\t"
    "global_load_dwordx4 v[28:31], v18, %[os] sc1\n\t"
    "global_load_dwordx4 v[32:35], v57, %[os] sc1\n\t"
    "global_load_dwordx4 v[36:39], v58, %[os] sc1\n\t"
    "s_nop 7\n\t"
    "s_branch 3b\n\t"
    "7:\n\t"
    "s_mov_b32 %[cap], 0\n\t"
    "s_branch 5b\n\t"
    "9:"
    : [sp] "=&s"(sp), [cap] "+s"(cap)
    : [os] "s"(os), [ad] "s"(ad), [wa] "s"(wa), [jx] "s"(jx), [c44] "s"(0x4444u), [hi] "s"(0xFFFFFFFF00000000ull), "{v[64:95]}"(W.v0), "{v[96:127]}"(W.v1), "{v[128:159]}"(W.o0), "{v[160:191]}"(W.o1), "{v[192:223]}"(W.o2), "{v[224:255]}"(W.o3)
    : "v16", "v17", "v18", "v19", "v20", "v21", "v22", "v23", "v24", "v25", "v26", "v27", "v28", "v29", "v30", "v31", "v32", "v33", "v34", "v35", "v36", "v37", "v38", "v39", "v40", "v41", "v42", "v43", "v44", "v45", "v46", "v47", "v48", "v49", "v50", "v51", "v52", "v53", "v54", "v55", "v56", "v57", "v58", "v59", "v60", "v61", "v62", "v63", "vcc", "scc", "memory");
}
__device__ __forceinline__ void qk_stage(const char* wq, const char* wk, int u, int wave) {
  stage_proj((u & 1) ? wk + (size_t)(((u >> 1) + 2) & 7) * 128 * HD * 2 : wq + (size_t)(u >> 1) * 128 * HD * 2, ((u + 4) % 5) * 32768, wave * 64 + lane_id(), wave);
}
__device__ __forceinline__ void vr_first(const u16* Wv, int r, int wave) {
  const char* sv_ = uni((const char*)(Wv + (size_t)(128 * wave) * HD + 64 * r));
  const int lane = lane_id();
#pragma unroll
  for (int i = 0; i < 16; ++i) gldsw(sv_ + (size_t)i * 8 * HD * 2, (unsigned)((lane >> 3) * HD * 2 + (lane & 7) * 16), wave * 16384 + i * 1024);
}
__device__ __forceinline__ void qk_body(char* smem, int wave_id_, int xg, int h, int wg, const u16* __restrict__ Wq, const u16* __restrict__ Wk,
                                        const char* __restrict__ Kx, const char* __restrict__ V, char* ws, unsigned* tmo, u64 tag) {
  const int wave = WAVE_ID;
  const int ty = wave >> 2, w4 = wave & 3;
  const char* wq = uni((const char*)(Wq + h * DH)); const char* wk = uni((const char*)(Wk + h * DH));
  auto stage = [&](int u) { qk_stage(wq, wk, u, wave); };
  QKW W;
  if (wave == 0) STS(ws, wg, 7);
  if (!SPRO) { stage(0); stage(1); stage(2); stage(3); }
#define WLD(i, w) do { WAIT_V(12); WBAR(); stage(2 * (i) + 4); if (ty) { WAIT_V(12); WBAR(); stage(2 * (i) + 5); } \
    { const int l_ = lane_id(); const unsigned o_ = ty ? ((2 * (i) + 5) % 5) * 32768 : ((2 * (i) + 4) % 5) * 32768; qk_wload<i>(w, proj_base(2 * w4, l_) + o_, proj_base(2 * w4 + 1, l_) + o_); } \
    if (!ty) { WAIT_V(12); WBAR(); stage(2 * (i) + 5); } if (wave == 0) STS(ws, wg, 1 + (i)); } while (0)
  WLD(0, W.w0); WLD(1, W.w1); WLD(2, W.w2); WLD(3, W.w3); WLD(4, W.w4); WLD(5, W.w5);
#undef WLD
  unsigned in = 0, avail = 0;
#pragma unroll
  for (int i = 0; i < 2; ++i) {
    if (i == 0) WAIT_V(8); else WAIT_V(0);
    WBAR();
    if (i == 0 && wave == 1) init_issue(ws, Q_MEM);
    if (i == 1) in = init_get(ws, tag, smem + Q_MEM);
    if (EKV && i == 1 && wave == 6)
      asm volatile("s_mov_b32 m0, %0\n\ts_nop 4\n\tglobal_load_lds_dword %1, %2 sc1" :: "s"(Q_K), "v"((unsigned)((PQKV + h) * 128 + (lane_id() & 15) * 4)), "s"(cnt_of(ws, in)) : "memory", "m0");
    const int lane = lane_id();
    const unsigned o = Q_RING + ty * 32768 + i * 65536, f0 = proj_base(2 * w4, lane) + o, f1 = proj_base(2 * w4 + 1, lane) + o;
    u32x8 x[4] = {proj_frag(smem, f0), proj_frag(smem, f1), proj_frag(smem, f0 + 16384), proj_frag(smem, f1 + 16384)};
    asm volatile("" : "+v"(x[0]), "+v"(x[1]), "+v"(x[2]), "+v"(x[3]));
    BAR();
    char* d = smem + Q_CB + i * 57344 + ty * (i ? 32768 : 24576) + w4 * 2048 + lane * 16;
#pragma unroll
    for (int j = 0; j < 4; ++j)
      if (i || j || !ty) { *(u32x4*)(d + j * 8192) = u32x4{x[j][0], x[j][1], x[j][2], x[j][3]}; *(u32x4*)(d + j * 8192 + 1024) = u32x4{x[j][4], x[j][5], x[j][6], x[j][7]}; }
    if (i == 0 && ty) { *(u32x4*)(smem + Q_MEM + wave * 2048 + lane * 16) = u32x4{x[0][0], x[0][1], x[0][2], x[0][3]}; *(u32x4*)(smem + Q_MEM + wave * 2048 + 1024 + lane * 16) = u32x4{x[0][4], x[0][5], x[0][6], x[0][7]}; }
  }
  if (wave == 4) *(volatile LAS u32x2*)(smem + Q_TM) = u32x2{0u, 0u};
  BAR();
  if (wave == 0) STS(ws, wg, 0);
  unsigned* cnt = cnt_of(ws, in);
  const char* xb = uni(xch_base(ws) + ((size_t)3 << 21) + (size_t)((in & 2u) << 12));
  const char* oh = uni(xch_base(ws) + (size_t)((in & 1u) << 13) + (size_t)(h >> 2) * 16384 + (h & 3) * 2048);
  auto rearm = [&](int ps) {
    unsigned z = SENT; asm volatile("" : "+v"(z));
    const unsigned l16 = lane_id() * 16;
#pragma unroll
    for (int r = 0; r < 2 * OREP; ++r) st16s(oh + (size_t)(ps * 4 + (r >> 1)) * 131072, (r & 1) * 1024u + l16, u32x4{z, z, z, z});
  };
  const char* dbw = xb + (size_t)48 * 16384 + 4096 + h * 128;
  auto stage_kv = [&](int s) {
    if (avail <= (unsigned)s) {
      for (int spins = 0;; ++spins) {
        const unsigned p = ld4_sc1(cnt + (PQKV + h) * 32 + (lane_id() & 15));
        avail = min(qkv_avail(p, 1), qkv_avail(p, 2));
        if (avail > (unsigned)s) break;
        __builtin_amdgcn_s_sleep(8);
        if (give_up(spins, tmo, 5)) break;
      }
    }
    const char* ks = uni(Kx + ((size_t)s * NH + h) * 8192); const char* vs = uni(V + ((size_t)s * NH + h) * 8192);
    const int lane = lane_id();
    head_to_lds(ks, lane, Q_K); head_to_lds(vs, lane, Q_V);
  };
  auto loop = [&](auto w_c) __attribute__((always_inline)) {
    constexpr int WV = decltype(w_c)::value;
    if (WV >= 4) asm volatile("ds_read_b128 v[56:59], %1\n\tds_read_b128 v[60:63], %1 offset:1024\n\ts_waitcnt lgkmcnt(0)" : "=&{v[56:63]}"(W.x) : "v"(Q_MEM + WV * 2048 + lane_id() * 16) : "memory");
    if (WV == 6) {
      if (EKV) { WAIT_V(0); const unsigned p = *(volatile LAS unsigned*)(smem + Q_K + lane_id() * 4); avail = min(qkv_avail(p, 1), qkv_avail(p, 2)); }
      stage_kv(0);
    }
    if (QK_OWN) {
      constexpr int PI = WV < 4 ? WV : WV - 2;
      if constexpr (QK_OWN == 3 && WV >= 4) qk_swapk<PI>(PI == 2 ? W.w2 : PI == 3 ? W.w3 : PI == 4 ? W.w4 : W.w5, W.x, Q_CB + 32768 + (WV & 3) * 2048 + lane_id() * 16);
      else qk_swap<PI>(PI == 0 ? W.w0 : PI == 1 ? W.w1 : PI == 2 ? W.w2 : PI == 3 ? W.w3 : PI == 4 ? W.w4 : W.w5, Q_CB + 57344 + (WV >> 2) * 32768 + (WV & 3) * 2048 + lane_id() * 16);
    }
    if (WV < 4) __builtin_amdgcn_s_setprio(2);
    unsigned t0 = 0, cap = 60000, d0 = 105, d1 = 200;
    int dq = D_Q;
    int sl = 0;
    for (int s = 0; s < NSEG; ++s) {
      TSDECL;
      const char* ag = xb + (size_t)((sl * 4 + (AREP == 4 ? xg : (AREP == 2 ? ARSEL : 0))) * 2 + (WV >> 2)) * 16384 + (WV & 3) * 2048;
      const char* vs = WV == 4 ? dbw : WV == 5 ? ws + OFF_KVM + ((size_t)s * NH + h) * 4096 : xb + (size_t)(24 + sl * 8 + (h >> 2)) * 16384 + (h & 3) * 2048;
      const char* od = WV == 4 ? (const char*)(size_t)((unsigned)(s + 1) << 24) : oh + (size_t)sl * 524288;
      if (s > 0) wait_rt(t0, dq);
      TS(0);
      unsigned long long tc; unsigned sp;
      qk_step<WV>(W, ag, vs, od, LAST && s == NSEG - 1 ? nullptr : ws + OFF_KVM + ((size_t)s * NH + h) * 4096 + 2048, cap, sp, tc, nullptr, d0, d1);
      dq = (sp & 0xFFFFu) ? min(dq + DQU, D_Q + DQM) : max(dq - DQD, D_Q);
      if ((WV < 4 || WV == 5) && !(LAST && s == NSEG - 1)) {
        const unsigned pv = (unsigned)(s + 1) << 24 | (rt_now() & 0xFFFFFFu);
        WAIT_V(0);
        if (lane_id() == 0) st4_sc1(cnt + (PROG + h) * 32 + WV, pv);
      }
      if (WV == 6 && s + 1 < NSEG - 1) stage_kv(s + 1);
      if (WV == 7 && s > 0 && s < NSEG - 1) rearm(sl == 0 ? 2 : sl - 1);
      if (WV == 7 && s >= 8 && s < 8 + NSEG - SDIR) {
        unsigned z = SENT; asm volatile("" : "+v"(z));
        const char* kd = ws + OFF_KVM + ((size_t)(s - 8 + SDIR) * NH + h) * 4096;
#pragma unroll
        for (int r = 0; r < 4; ++r) st16s(kd, r * 1024u + lane_id() * 16, u32x4{z, z, z, z});
      }
      if (s < NSEG - 1) {
        if (WV == 4) {
          delay64<E_B>();
          t0 = bell_time(xb + (size_t)(48 + xg) * 16384 + 4096, (lane_id() & 31) * 128, (unsigned)(s + 1), cap);
          *(volatile LAS u32x2*)(smem + Q_TM) = u32x2{(unsigned)(s + 1), t0};
        } else {
          for (;;) {
            const u32x2 v = *(volatile LAS u32x2*)(smem + Q_TM);
            t0 = __builtin_amdgcn_readfirstlane(v.y);
            if (__builtin_amdgcn_readfirstlane(v.x) == (unsigned)(s + 1)) break;
            __builtin_amdgcn_s_sleep(1);
          }
        }
      }
      sl = sl == 2 ? 0 : sl + 1;
    }
    if (cap == 0 && lane_id() == 0) st4_sc1(tmo, 1u);
  };
  switch (wave) {
    case 0: loop(std::integral_constant<int, 0>{}); break;
    case 1: loop(std::integral_constant<int, 1>{}); break;
    case 2: loop(std::integral_constant<int, 2>{}); break;
    case 3: loop(std::integral_constant<int, 3>{}); break;
    case 4: loop(std::integral_constant<int, 4>{}); break;
    case 5: loop(std::integral_constant<int, 5>{}); break;
    case 6: loop(std::integral_constant<int, 6>{}); break;
    default: loop(std::integral_constant<int, 7>{}); break;
  }
  __builtin_amdgcn_s_setprio(0);
}

__device__ __forceinline__ unsigned vr_body(char* smem, int wave_id_, int xg, int h, int c, int r, int wg, const u16* __restrict__ Wv, const u16* __restrict__ Wo,
                                        const u16* __restrict__ Wq, const u16* __restrict__ Wk, char* ws, unsigned* tmo, u64 tag, const char* code) {
  const int wave = WAVE_ID;
  const unsigned po = (unsigned)(r >> 5) * 16384u + ((r >> 2) & 7) * 1024 + (r & 1) * 512 + ((r >> 1) & 1) * 128;
  const unsigned vo_ = (unsigned)(24 + (h >> 2)) * 16384u + (h & 3) * 2048 + c * 1024;
  const int hg = (wave + ROTW * (r & 3)) & 7;
  const int jr = ROTH ? r & 3 : 0;
  VRW W;
  {
    const int lane = lane_id();
    const int g = lane >> 4, q4 = (lane & 15) >> 2, p4 = lane & 3, fr = 4 * g + (q4 & 1) + 32 * (q4 >> 1);
    const unsigned lw = wave * 16384;
    const char* sv_ = uni((const char*)(Wv + (size_t)(128 * wave) * HD + h * DH + 64 * c));
    const char* so = uni((const char*)(Wo + (size_t)(512 * hg) * DM + 64 * (r >> 2) + 16 * (r & 1)));
    const unsigned vo = (unsigned)((32 * (lane >> 5) + ((lane >> 2) & 7)) * DM * 2 + ((lane >> 1) & 1) * 64 + (lane & 1) * 16);
    auto issue = [&](int p) {
#pragma unroll
      for (int i = 0; i < 8; ++i) {
        if (p >= 2 && (NTM & 8)) gldsw(so + (size_t)(128 * ((p - 2) ^ jr) + 64 * (i & 1) + 8 * (i >> 1)) * DM * 2, vo, lw + (p & 1) * 8192 + i * 1024);
        else if (p >= 2) glds16s<0>(so + (size_t)(128 * ((p - 2) ^ jr) + 64 * (i & 1) + 8 * (i >> 1)) * DM * 2, vo, lw + (p & 1) * 8192 + i * 1024);
        else gldsw(sv_ + (size_t)(8 * p + i) * 8 * HD * 2, (unsigned)((lane >> 3) * HD * 2 + (lane & 7) * 16), lw + (p & 1) * 8192 + i * 1024);
      }
    };
#define VTS(p) do { } while (0)
    if (wave == 0) STS(ws, wg, 7);
    if (!SPRO) { issue(0); issue(1); }
    const unsigned av = lw + (unsigned)(fr * 128 + p4 * 8), ao = lw + (unsigned)(fr * 64 + p4 * 8);
    WAIT_V(8); VTS(0); vr_wload<0>(W.v0, av); issue(2);
    WAIT_V(8); VTS(1); vr_wload<1>(W.v1, av); issue(3);
    WAIT_V(8); VTS(2); vr_wload<2>(W.o0, ao); issue(4);
    WAIT_V(8); vr_wload<3>(W.o1, ao); issue(5);
    if (wave == 0) init_issue(ws, 131072);
    WAIT_V(8); vr_wload<4>(W.o2, ao);
    WAIT_V(0); VTS(3);
    if (VHN) {
      const int rl = r - 20 * xg, hn = ((xg == 1 ? rl : rl - (rl >= (xg ? 8 : 16) ? 4 : 0)) * 8 + wave) * (6 * VHN) + (lane >> 1);
      const int pm = hn / 384, hr = hn - pm * 384, p = 8 - VHN + (pm >> 1), hh = 12 * xg + 1 + 4 * (hr >> 7);
      const u16* hp = ((pm & 1) ? Wk : Wq) + (size_t)(((pm & 1) ? (p + 2) & 7 : p) * 128 + (hr & 127)) * HD + min(hh, 31) * DH + (lane & 1) * 64;
      if (((r >> 2) & 7) != 3 && lane < 12 * VHN && pm < 2 * VHN) asm volatile("s_mov_b32 m0, %0\n\ts_nop 0\n\tglobal_load_lds_dword %1, off" :: "s"(155648 + wave * 256), "v"(hp) : "memory", "m0");
    }
    vr_wload<5>(W.o3, ao);
#undef VTS
  }
  if (wave == 0) STS(ws, wg, 5);
  if (wave == 0) STS(ws, wg, 6);
  BAR();
  if (wave == 0) STS(ws, wg, 0);
  const unsigned in = init_get(ws, tag, smem + 131072);
  const char* xb = uni(xch_base(ws) + ((size_t)3 << 21) + (size_t)((in & 2u) << 12));
  const char* ob = uni(xch_base(ws) + (size_t)((in & 1u) << 13));
  auto rearm = [&](int ps, bool ag) {
    const int lane = lane_id();
    unsigned z = SENT; asm volatile("" : "+v"(z));
    const u32x4 sv = u32x4{z, z, z, z};
    if (ag && lane < 16 * AREP) st16s(xb, (unsigned)(ps * 8 + (lane >> 4) * 2) * 16384u + po + ((lane >> 3) & 1) * 256 + (lane & 7) * 16, sv);
    st16s(xb, (unsigned)ps * 131072u + vo_ + lane * 16, sv);
  };
  __builtin_amdgcn_s_setprio(2);
  unsigned cap = 60000;
  int sl = 0;
  for (int s = 0; s < NSEG; ++s) {
    TSDECL;
    const int sn = sl == 2 ? 0 : sl + 1;
    const char* ag = xb + (size_t)((sl * 4 + (AREP == 4 ? xg : (AREP == 2 ? ARSEL : 0))) * 2 + (wave >> 2)) * 16384 + (wave & 3) * 2048;
    const char* vd = (LAST && s == NSEG - 1 ? ws + OFF_KVM + ((size_t)s * NH + h) * 4096 + 2048 + c * 1024 : xb + (size_t)sl * 131072 + vo_) + (wave & 2) * 256 + (wave & 1) * 128;
    const char* os = ob + (size_t)((sl * 4 + (OREP == 4 ? xg : ORSEL)) * 8 + hg) * 16384 + ((r >> 1) & 1) * 1024;
    const char* ad = xb + (size_t)((sn * 4 + (wave & 3)) * 2) * 16384 + po;
    if (s > 0) delay64<E_AG>();
    TS(0);
    unsigned long long tc; unsigned sp;
    unsigned long long* tsp = nullptr;
    vr_v(W, ag, vd, wave * 2048, (wave & 3) * 512, cap, sp, tc, tsp);
    TN(12, sp + 1);
    if (s == NSEG - 1) break;
    if (wave == 7 && s > 0) rearm(sl == 0 ? 2 : sl - 1, true);
    if (wave == 6 && s == NSEG - 4 && code) touch(code, (unsigned)lane_id() * 128, 155648);
    wait_clk((unsigned)tc, E_O);
    TS(3);
    const unsigned tb = bell_time(xb + (size_t)(48 + xg) * 16384 + hg * 512, (lane_id() & 3) * 128, (unsigned)(s + 1), cap);
    wait_rt(tb, D_O);
    TS(4);
    vr_r(W, os, ad, wave * 512, jr * 2048, cap, sp, tsp);
    TN(14, (sp & 0xFFFF) + 1);
    if (wave == 1) TSFLUSH(0x6078);
    sl = sn;
  }
  __builtin_amdgcn_s_setprio(0);
  if (cap == 0 && lane_id() == 0) st4_sc1(tmo, 2u);
  return in;
}
struct ModIn { u32x4 w[8]; unsigned cv; };
__device__ __forceinline__ void mod_issue(ModIn& I, int wave_id_, int j, const u16* __restrict__ c, const u16* __restrict__ wmod) {
  const int tid = opaque_tid(), k0 = (j >> 5) * 512 + (tid >> 3);
  I.cv = c[k0 + 64 * (tid & 7)];
#pragma unroll
  for (int i = 0; i < 8; ++i) I.w[i] = ldnt<1>(wmod + (size_t)(k0 + 64 * i) * (6 * DM) + (j & 31) * 64 + (tid & 7) * 8);
}
__device__ __forceinline__ void mod_job(char* smem, int wave_id_, int j, u64 tag, const ModIn& I, char* ws) {
  const int tid = opaque_tid(), lane = tid & 63, wave = WAVE_ID, cg = tid & 7;
  const float cv = bflo(I.cv), sl = bfr(cv / (1.0f + __expf(-cv)));
  float acc[8] = {0, 0, 0, 0, 0, 0, 0, 0};
#pragma unroll
  for (int i = 0; i < 8; ++i) {
    const float sv = __shfl(sl, (lane & 56) + i);
#pragma unroll
    for (int e = 0; e < 4; ++e) { acc[2 * e] += sv * bflo(I.w[i][e]); acc[2 * e + 1] += sv * bfhi(I.w[i][e]); }
  }
#pragma unroll
  for (int e = 0; e < 8; ++e) acc[e] = red16_32_add(add_ror8(acc[e]));
  float* red = (float*)(smem + L_GB);
  if (lane < 8) { *(f32x4*)(red + wave * 64 + cg * 8) = f32x4{acc[0], acc[1], acc[2], acc[3]}; *(f32x4*)(red + wave * 64 + cg * 8 + 4) = f32x4{acc[4], acc[5], acc[6], acc[7]}; }
  __syncthreads();
  if (tid < 32) {
    f32x2 s = f32x2{0, 0};
#pragma unroll
    for (int r = 0; r < 8; ++r) s += *(const f32x2*)(red + r * 64 + tid * 2);
    st16_sc1(ws + OFF_MOD + (j * 32 + tid) * 16, u32x4{__float_as_uint(s[0]), (unsigned)tag, __float_as_uint(s[1]), tag2(tag)});
  }
  BAR();
}
#define NIMM 3
constexpr unsigned TK(int m, int hl, int part, int bo, int be) { return (unsigned)(m | hl << 2 | part << 5 | bo << 8 | be << 16); }
#define QX 30
#define CA 36
#define CB 42
#define C2 46
constexpr unsigned task_tab(int t, int it) {
  return t >= 8 && t < 13 ? (it == 0 ? TK(1, t - 8, 0, 0, 64) : it == 1 ? (t < 12 ? TK(2, t - 4, 1, C2, 64) : TK(1, 5, 1, C2, 64)) : 0)
       : it > 0 ? 0
       : t < 8 ? TK(0, t, 1, QX, 64)
       : t < 16 ? TK(1, t - 8, 0, 0, C2)
       : t < 24 ? TK(2, t - 16, 0, 0, t < 18 ? CA : (t < 20 ? CB : C2))
       : t < 26 ? TK(2, t - 24, 1, CA, 64)
       : t == 26 ? TK(1, 6, 1, C2, (C2 + 64) / 2) : t == 27 ? TK(1, 6, 2, (C2 + 64) / 2, 64)
       : t < 30 ? TK(2, t - 26, 1, CB, 64)
       : t == 30 ? TK(1, 7, 1, C2, (C2 + 64) / 2) : t == 31 ? TK(1, 7, 2, (C2 + 64) / 2, 64)
       : TK(0, t - 32, 0, 0, QX);
}
template <int IT, int I = 0> __device__ __forceinline__ unsigned task_sel(int t) {
  if constexpr (I == 40) return 0u;
  else { unsigned r = task_sel<IT, I + 1>(t); constexpr unsigned c = task_tab(I, IT); if (c) asm volatile("s_cmp_eq_u32 %1, %2\n\ts_cselect_b32 %0, %3, %0" : "+s"(r) : "s"(t), "i"(I), "i"(c) : "scc"); return r; }
}
__global__ void __launch_bounds__(512, 2) k_mega(const u16* __restrict__ Wq, const u16* __restrict__ Wk, const u16* __restrict__ Wv,
                                                 const u16* __restrict__ Wo, const u16* __restrict__ w1, const u16* __restrict__ w3,
                                                 const u16* __restrict__ w2, const u16* __restrict__ x, const u16* __restrict__ cvec,
                                                 const u16* __restrict__ wmod, const u16* __restrict__ mem0, char* ws, u16* out) {
  extern __shared__ __attribute__((aligned(16))) char smem[];
  int wave_id_ = __builtin_amdgcn_readfirstlane((int)threadIdx.x >> 6);
  unsigned bid = blockIdx.x;
  asm volatile("" : "+s"(wave_id_), "+s"(bid));
  unsigned* tmo = (unsigned*)(ws + OFF_TMO);
  const unsigned tst = (unsigned)__builtin_amdgcn_s_memrealtime();
  unsigned long long* mts = nullptr;
  const float* mod = (const float*)(ws + OFF_MOD);
  volatile LAS unsigned* tslot = (volatile LAS unsigned*)(smem + L_SLOT + 768);
#define CPF 131072
  u64 pc; asm volatile("s_getpc_b64 %0" : "=s"(pc));
  const u64 pc0 = pc & ~(u64)127;
  if (CPF && wave_id_ == 1) {
    const unsigned o = (((bid >> 3) & 31) * 64 + lane_id()) * 64;
    if (o < CPF) asm volatile("s_mov_b32 m0, %0\n\ts_nop 0\n\tglobal_load_lds_dword %1, %2" :: "s"(155648 + 1024), "v"(o), "s"(pc & ~(u64)127) : "memory", "m0");
  }
  unsigned xcc; asm volatile("s_getreg_b32 %0, hwreg(HW_REG_XCC_ID)" : "=s"(xcc));
  xcc &= 7;
  const unsigned pred = xcc * 32 + ((bid >> 3) & 31);
  ModIn mi;
  auto modj = [](unsigned sl) { return sl >= 128 && ((sl + 8) & 31) < 16 ? (int)(((sl >> 5) & 3) * 16 + (sl & 15)) : -1; };
  if (modj(pred) >= 0) mod_issue(mi, wave_id_, modj(pred), cvec, wmod);
  u64 tag;
  asm volatile("s_load_dwordx2 %0, %1, 0x0\n\ts_waitcnt lgkmcnt(0)" : "=s"(tag) : "s"(ws + OFF_TAG) : "memory");
  tag += 1;
  auto first = [&](unsigned sl) __attribute__((always_inline)) {
    const int g_ = sl >> 5, t_ = sl & 31, nq_ = g_ == 2 ? 8 : 12;
    if (g_ < 3 && t_ < nq_) {
      const char* wq = uni((const char*)(Wq + (g_ * 12 + t_) * DH)); const char* wk = uni((const char*)(Wk + (g_ * 12 + t_) * DH));
#pragma unroll
      for (int u = 0; u < 4; ++u) qk_stage(wq, wk, u, wave_id_);
    } else if (g_ < 3) vr_first(Wv, g_ * 20 + t_ - nq_, wave_id_);
    else if (g_ > 3 && g_ < 8 && t_ >= 8 && t_ < 24) {
      u64 pk = (u64)Wk, pv = (u64)Wv; asm volatile("" : "+s"(pk), "+s"(pv));
      u32x32 none[4];
      wload<R_QKV, 2>(smem, wave_id_, (const u16*)(t_ < 16 ? pk : pv) + (8 * (g_ & 3) + (t_ & 7)) * DH, nullptr, HD, none, 0, 1, [](int) {});
    }
  };
  if (pred < 72 && (pred & 31) < 12) tslot = (volatile LAS unsigned*)(smem + 100000);
  int slot;
  for (unsigned sf = pred, rep = 0;; ++rep) {
    if (rep == 0 && opaque_tid() == 0) {
      unsigned sl = pred;
      const unsigned s0 = sl;
      for (unsigned i = 1; i <= 256; ++i) {
        if (__hip_atomic_exchange((u64*)(ws + OFF_CLAIM + sl * 128), tag, __ATOMIC_RELAXED, __HIP_MEMORY_SCOPE_AGENT) != tag) break;
        sl = i == 256 ? 0xFFFFu : (((s0 + (i & ~31u)) & 0xE0u) | ((s0 + i) & 31u));
      }
      *tslot = sl;
    }
    if (SPRO) first(sf);
    if (rep) break;
    BAR();
    slot = (int)__builtin_amdgcn_readfirstlane(*tslot);
    if (slot != (int)pred) WAIT_V(0);
    BAR();
    if (slot == (int)pred) break;
    sf = (unsigned)slot;
  }
  if (slot >= 256) return;
  if (modj(slot) >= 0) {
    if (slot != (int)pred) mod_issue(mi, wave_id_, modj(slot), cvec, wmod);
    mod_job(smem, wave_id_, modj(slot), tag, mi, ws);
  }
  unsigned* cnt = nullptr;
  const int xg = slot >> 5;
  int t = xg == 3 ? 24 + (slot & 7) : slot & 31;
  int hx = -1; bool hs = false;
  if (xg < 3) {
    const int nq = xg == 2 ? 8 : 12;
#define HPF 1
    if (t < nq) {
      const int h = xg * 12 + t;
      qk_body(smem, wave_id_, xg, h, (h >> 3) * 32 + (h & 7), Wq, Wk, ws + OFF_K, ws + OFF_V, ws, tmo, tag);
      if (HPF && HB < NSEG && t < 8) {
        const int lane = lane_id(), wv = t * 8 + wave_id_;
#pragma unroll
        for (int j = 0; j < 2; ++j) touch(w2 + (size_t)(xg * 1024 + 256 + j * 128 + (wv & 32) * 16 + (wv & 31) * 4) * DM, (unsigned)lane * 128, wave_id_ * 256);
        if (wv < (xg == 2 ? 32 : 16)) touch(w2 + (size_t)(3072 + 256 + (wv & 15) * 16) * DM + (wv >= 16 ? 3 : xg) * 256, (unsigned)((lane >> 2) * DM * 2 + (lane & 3) * 128), wave_id_ * 256);
        WAIT_V(0);
      }
    } else {
      const int u = t - nq, r = xg * 20 + u, uu = u % 10;
      cnt = cnt_of(ws, vr_body(smem, wave_id_, xg, r >> 1, r & 1, r, (r >> 4) * 32 + 8 + (r & 15), Wv, Wo, Wq, Wk, ws, tmo, tag, CPF && HB < NSEG && u < 16 ? (const char*)pc0 + u * 8192 : nullptr));
      if (HB < NSEG && u < (HB < HC ? 20 : 10)) hx = uu < 8 ? xg * 8 + uu : (uu == 8 ? 24 + xg : (xg == 2 ? 27 : -1));
      hs = u >= 10;
    }
    if (hx < 0) return;
    t = 8;
  }
  const bool cx = xg == 3;
  const int xq = cx ? (slot >> 3) & 3 : xg & 3, wsl = xq * 32 + t;
#define GDEL 900
#define GDX 1000
  if (cx || t < 8) l1_role(smem, wave_id_, cx, xq * 8 + (t & 7), x, mod, mem0, ws + OFF_H1, cnt, tag, tmo, mts);
#define KH 0
  if (KH && !cx && t >= 24) {
    const int it = (t & 7) * 8 + wave_id_, lane = lane_id();
    touch(((it & 16) ? Wk : Wv) + (size_t)(256 + (it & 8) * 64 + (it & 7) * 32) * HD + (8 * xq + 1 + 4 * (it >> 5)) * DH,
          (unsigned)((lane >> 1) * HD * 2 + (lane & 1) * 128), 155648 + wave_id_ * 256);
  }
  if (t < 8 || t >= 24) {
    if (!cx && t >= 24) arm_job(wave_id_, xq * 8 + t - 24, tag, ws);
    if (!cx && t >= 24 && wave_id_ == 0) STS(ws, 128 + xq * 32 + t - 24, 4);
    while ((unsigned)__builtin_amdgcn_s_memrealtime() - tst < (cx ? GDX : GDEL)) __builtin_amdgcn_s_sleep(8);
    if (t >= 24) cnt = cnt_of(ws, init_wait(ws, tag));
    if (!cx && t >= 24) {
      mod2_job(smem, wave_id_, xq * 8 + t - 24, mod, cvec, wmod, cnt);
    }
  }
  auto u2_of = [&]() { return cx ? -1 : (t >= 13 && t < 16 ? t - 13 : (t >= 20 && t < 24 ? t - 17 : -1)); };
  auto f_of = [&]() {
    int fl = t < 8 ? t : (t < 18 ? t - 4 : (t < 20 ? t - 10 : t - 14));
    const int sa = (3 + 2 * xq) & 7, sb = sa + 8 < 14 ? sa + 8 : 13;
    if (fl == 12) fl = sa; else if (fl == sa) fl = 12;
    else if (fl == 13) fl = sb; else if (fl == sb) fl = 13;
    return (int)__builtin_amdgcn_readfirstlane(14 * xq + fl);
  };
  unsigned tk = task_sel<0>(hx >= 0 ? 99 : (cx ? t + 8 : t)), t2 = task_sel<1>(cx ? t + 8 : t), t3 = task_sel<2>(cx ? t + 8 : t);
  for (int it = 0; tk != 0; ++it, tk = t2, t2 = t3, t3 = 0) {
    const int m = tk & 3, hl = (tk >> 2) & 7, part = (tk >> 5) & 3, bo = (tk >> 8) & 255, ns = (int)(tk >> 16) - bo, h = 8 * xq + hl;
    const bool first = m != 0 && bo == 0, late = it > 0;
    const int lane = lane_id();
    const Dep d{nullptr, (unsigned)(PL1B + (lane & 31)) * 128, 8u, (unsigned)(8 - bo)};
    if (wave_id_ == 0 && late) wait_l1(cnt + (PL1B - PL1) * 32, 7u, tmo);
    BAR();
    gemm7<R_QKV>(smem, wave_id_, (m == 0 ? Wq : (m == 1 ? Wk : Wv)) + h * DH, nullptr, HD, ws + OFF_H1 + (size_t)bo * 65536, 65536, d,
                    ws + (m == 0 ? OFF_Q : (m == 1 ? OFF_K : OFF_V)) + h * 8192 + (size_t)bo * 32 * 8192, 32 * 8192,
                    cnt, (PQKV + h) * 32 + 4 * part + m, tmo, wsl, mts, false, first ? NIMM : 0, ns, late ? ns : 0, bo * 257, tag);
  }
  __syncthreads();
  const int u2 = u2_of();
  const int f2 = hx >= 0 ? hx : (u2 < 0 ? -1 : (xq < 3 ? xq * 8 + u2 : (u2 < 3 ? u2 * 8 + 7 : 21 + u2)));
  const int fb = hx >= 0 ? (hs ? HB : HC) : 0, fe = hx >= 0 ? (hs ? HC : NSEG) : HB;
  if (cx || (f2 >= 0 && f2 < 24)) {
    const int ks = cx ? xq : f2 >> 3, cn = cx ? (t + 1) & 7 : f2 & 7;
    const int lane = lane_id();
    const Dep d{nullptr, (unsigned)(cx ? (PSA + 4 * xq + ((lane >> 3) & 3)) * 128 + (lane & 7) * 4 : (PF1 + 16 * ks + (lane & 15)) * 128), cx ? 4u : 1u, cx ? (unsigned)((lane >> 1) & 3) : 0u};
    gemm7<R_PART>(smem, wave_id_, (cx ? Wo : w2) + (size_t)ks * 1024 * DM + cn * 128, nullptr, DM, ws + (cx ? OFF_O : OFF_G) + ks * 16 * 4096, (cx ? 64 : 56) * 4096, d,
                  ws + (cx ? OFF_P : OFF_P2) + (ks * DM + cn * 128) * (TLP ? 64 : 2), (size_t)32 * PROW, cnt, ((cx ? PWO : PF2) + ks * 8 + cn) * 32 + (hx >= 0 ? (hs ? 2 : 1) : 0), tmo,
                  hx >= 0 ? 280 + hx : 128 + xq * 32 + (cx ? cn : 22 + u2), mts, !cx, 0, cx ? NSEG : fe, 0, 0, 0, fb);
  } else if (f2 >= 0) {
    const int c4 = f2 - 24;
    const int lane = lane_id();
    const Dep d{nullptr, (unsigned)(PF1 + 48 + (lane & 7)) * 128, 1u, 0u};
    gemm7<R_PART2>(smem, wave_id_, w2 + (size_t)3072 * DM + c4 * 256, nullptr, DM, ws + OFF_G + 48 * 4096, 56 * 4096, d,
                   ws + OFF_P2 + (3 * DM + c4 * 256) * (TLP ? 64 : 2), (size_t)32 * PROW, cnt, (PF2 + 24 + c4) * 32 + (hx >= 0 ? (hs ? 2 : 1) : 0), tmo, hx >= 0 ? 280 + hx : 128 + xq * 32 + 22 + u2, mts, true, 0, fe, 0, 0, 0, fb);
  } else if (t < 8 || (t >= 16 && t < 20) || t == 24 || t == 25) {
    int f = f_of();
    asm volatile("" : "+s"(f));
    const int lane = lane_id();
    const Dep d{nullptr, (unsigned)((PLN + (lane >> 3)) * 128 + (lane & 7) * 4), 1u, 0u};
    gemm7<R_F1>(smem, wave_id_, w1 + 64 * f, w3 + 64 * f, FF, ws + OFF_H2, 65536, d, ws + OFF_G + f * 4096, 56 * 4096, cnt, (PF1 + f) * 32, tmo, 128 + xq * 18 + 8 + f, mts, false);
  } else if (t >= 9 && t < 13) {
    fin_role(smem, wave_id_, xq * 4 + t - 9, mod, ws + OFF_P2, (const u16*)(ws + OFF_X1), out, cnt, tmo, mts);
    if (xq == (FL63 ? 0 : 3) && t == 12 && opaque_tid() == 0) {
      const u32x4 v = ld16_sc1(ws + OFF_TAG);
      const u64 tn = (((u64)v.y << 32) | v.x) + 1;
      st8_sc1(ws + OFF_TAG, u32x2{(unsigned)tn, (unsigned)(tn >> 32)});
    }
  } else if (t == 28 || t == 29) {
    row_role(smem, wave_id_, 8 * xq, t - 28, mod, ws + OFF_P, x, (u16*)(ws + OFF_X1), ws + OFF_H2, cnt, tmo, 256 + xq, mts);
  } else if (t >= 26) {
    sa_role(smem, wave_id_, 8 * xq + 2 * (t >= 30 ? t - 30 : t - 24), ws + OFF_Q, ws + OFF_K, ws + OFF_V, ws + OFF_KVM, ws + OFF_O, cnt, tmo, 128 + xq * 32 + 30 + (t & 1), mts);
  }
}

extern "C" void kernel_launch(void* const* d_in, const int* in_sizes, int n_in, void* d_out, int out_size, void* d_ws, size_t ws_size,
                              hipStream_t stream) {
  const u16* x    = (const u16*)d_in[0];
  const u16* mem0 = (const u16*)d_in[1];
  const u16* Wq   = (const u16*)d_in[2];
  const u16* Wk   = (const u16*)d_in[3];
  const u16* Wv   = (const u16*)d_in[4];
  const u16* Wo   = (const u16*)d_in[5];
  const u16* w1   = (const u16*)d_in[8];
  const u16* w3   = (const u16*)d_in[9];
  const u16* w2   = (const u16*)d_in[10];
  const u16* c    = (const u16*)d_in[11];
  const u16* wmod = (const u16*)d_in[12];
#define WSHIFT 0
  char* ws = (char*)d_ws + ((size_t)WSHIFT << 20);
  hipFuncSetAttribute((const void*)k_mega, hipFuncAttributeMaxDynamicSharedMemorySize, MEGA_LDS);
  k_mega<<<256, 512, MEGA_LDS, stream>>>(Wq, Wk, Wv, Wo, w1, w3, w2, x, c, wmod, mem0, ws, (u16*)d_out);
}
```

```cpp
#include <hip/hip_runtime.h>
#include <hip/hip_bf16.h>
#include <stdint.h>
#include <type_traits>
#pragma clang diagnostic ignored "-Winline-asm"
#pragma clang diagnostic ignored "-Wunused-value"

typedef unsigned short u16;
typedef short s16x4 __attribute__((ext_vector_type(4)));
typedef __bf16 bf16x8 __attribute__((ext_vector_type(8)));
typedef float f32x4 __attribute__((ext_vector_type(4)));
typedef float f32x2 __attribute__((ext_vector_type(2)));
typedef unsigned u32x4 __attribute__((ext_vector_type(4)));
typedef unsigned u32x2 __attribute__((ext_vector_type(2)));
#define LAS __attribute__((address_space(3)))

constexpr int SEQ = 2048, DM = 1024, NH = 32, DH = 128, HD = 4096, NMEM = 8, SEG = 32, NSEG = 64, FF = 3584;
constexpr unsigned SENT = 0xFFFFFFFFu;

__device__ __forceinline__ int lane_id() { int l; asm volatile("v_mbcnt_lo_u32_b32 %0, -1, 0\n\tv_mbcnt_hi_u32_b32 %0, -1, %0" : "=v"(l)); return l; }
#define WAVE_ID wave_id_
#define opaque_tid() (WAVE_ID * 64 + lane_id())
__device__ __forceinline__ float bf2f(u16 v) { return __builtin_bit_cast(float, (unsigned)v << 16); }
__device__ __forceinline__ float bflo(unsigned v) { return __builtin_bit_cast(float, v << 16); }
__device__ __forceinline__ float bfhi(unsigned v) { return __builtin_bit_cast(float, v & 0xffff0000u); }
__device__ __forceinline__ float bfr(float x) { return (float)(__bf16)x; }
__device__ __forceinline__ unsigned pack2(float a, float b) {
  typedef __bf16 bf16x2 __attribute__((ext_vector_type(2)));
  bf16x2 v; v[0] = (__bf16)a; v[1] = (__bf16)b; return __builtin_bit_cast(unsigned, v);
}
__device__ __forceinline__ unsigned nosent(unsigned v) { return v == SENT ? 0x7FC07FC0u : v; }
__device__ __forceinline__ bf16x8 asbf8(u32x4 v) { return __builtin_bit_cast(bf16x8, v); }
__device__ __forceinline__ s16x4 ass4(u32x2 v) { return __builtin_bit_cast(s16x4, v); }
__device__ __forceinline__ u32x2 trread(const char* lds_base, unsigned off) {
  s16x4 v = __builtin_amdgcn_ds_read_tr16_b64_v4i16((LAS s16x4*)(lds_base + off));
  return __builtin_bit_cast(u32x2, v);
}
__device__ __forceinline__ void glds16(const void* gsrc, unsigned lds_dst) {
  unsigned keep;
  asm volatile("s_mov_b32 %0, m0\n\ts_mov_b32 m0, %2\n\ts_nop 0\n\tglobal_load_lds_dwordx4 %1, off\n\ts_mov_b32 m0, %0"
               : "=&s"(keep) : "v"(gsrc), "s"(lds_dst) : "memory");
}
#define CNT_ " nt"
#define WNT_ " nt"
__device__ __forceinline__ void glds16_w(const void* gsrc, unsigned lds_dst) {
  unsigned keep;
  asm volatile("s_mov_b32 %0, m0\n\ts_mov_b32 m0, %2\n\ts_nop 0\n\tglobal_load_lds_dwordx4 %1, off" CNT_ "\n\ts_mov_b32 m0, %0"
               : "=&s"(keep) : "v"(gsrc), "s"(lds_dst) : "memory");
}
__device__ __forceinline__ void glds16_sc1(const void* gsrc, unsigned lds_dst) {
  unsigned keep;
  asm volatile("s_mov_b32 %0, m0\n\ts_mov_b32 m0, %2\n\ts_nop 0\n\tglobal_load_lds_dwordx4 %1, off sc1\n\ts_mov_b32 m0, %0"
               : "=&s"(keep) : "v"(gsrc), "s"(lds_dst) : "memory");
}
#define NTM 1
template <int B> __device__ __forceinline__ u32x4 ldnt(const void* p) { if (NTM & B) return __builtin_nontemporal_load((const u32x4*)p); else return *(const u32x4*)p; }
#define MFMA32(a, b, c) __builtin_amdgcn_mfma_f32_16x16x32_bf16((a), (b), (c), 0, 0, 0)

__device__ __forceinline__ void st16_sc1(void* p, u32x4 v) { asm volatile("global_store_dwordx4 %0, %1, off sc1\n\ts_nop 1" :: "v"(p), "v"(v) : "memory"); }
__device__ __forceinline__ void st8_sc1(void* p, u32x2 v) { asm volatile("global_store_dwordx2 %0, %1, off sc1\n\ts_nop 1" :: "v"(p), "v"(v) : "memory"); }
template <bool WT> __device__ __forceinline__ void st8(void* p, u32x2 v) { if (WT) st8_sc1(p, v); else *(u32x2*)p = v; }
template <bool WT> __device__ __forceinline__ void st16(void* p, u32x4 v) { if (WT) st16_sc1(p, v); else *(u32x4*)p = v; }
__device__ __forceinline__ u32x4 ld16_sc1(const void* p) {
  u32x4 r; asm volatile("global_load_dwordx4 %0, %1, off sc1\n\ts_waitcnt vmcnt(0)" : "=&v"(r) : "v"(p) : "memory"); return r;
}
__device__ __forceinline__ void ld16x4_sc1(const void* p, u32x4& a, u32x4& b, u32x4& c, u32x4& d) {
  asm volatile("global_load_dwordx4 %0, %4, off sc1\n\tglobal_load_dwordx4 %1, %4, off offset:1024 sc1\n\t"
               "global_load_dwordx4 %2, %4, off offset:2048 sc1\n\tglobal_load_dwordx4 %3, %4, off offset:3072 sc1\n\ts_waitcnt vmcnt(0)"
               : "=&v"(a), "=&v"(b), "=&v"(c), "=&v"(d) : "v"(p) : "memory");
}
typedef __amdgpu_buffer_rsrc_t rsrc_t;
__device__ __forceinline__ rsrc_t mkrs(const void* p) { return __builtin_amdgcn_make_buffer_rsrc((void*)p, 0, 0x7FFFFFFF, 0x00020000); }
__device__ __forceinline__ u32x4 bld16(rsrc_t r, unsigned off) { return __builtin_amdgcn_raw_buffer_load_b128(r, off, 0, 16); }
__device__ __forceinline__ u32x2 bld8(rsrc_t r, unsigned off) { return __builtin_amdgcn_raw_buffer_load_b64(r, off, 0, 16); }
__device__ __forceinline__ bool ok4(u32x4 v) { return v.x != SENT && v.y != SENT && v.z != SENT && v.w != SENT; }
constexpr int SPIN_CAP = 2000000;
__device__ __forceinline__ bool give_up(int spins, unsigned* tmo, unsigned code) {
  if ((spins & 4095) != 4095) return false;
  if (spins > SPIN_CAP) { __hip_atomic_store(tmo, code, __ATOMIC_RELAXED, __HIP_MEMORY_SCOPE_AGENT); return true; }
  return __hip_atomic_load(tmo, __ATOMIC_RELAXED, __HIP_MEMORY_SCOPE_AGENT) != 0u;
}
template <bool ACQ = true>
__device__ __forceinline__ void wait_cnt(unsigned* c, unsigned n, unsigned* tmo) {
  for (int spins = 0;; ++spins) {
    const unsigned v = __hip_atomic_load(c, __ATOMIC_RELAXED, __HIP_MEMORY_SCOPE_AGENT);
    if (v >= n) break;
    __builtin_amdgcn_s_sleep(8);
    if (give_up(spins, tmo, 5)) break;
  }
  if (ACQ) {
    __builtin_amdgcn_fence(__ATOMIC_ACQUIRE, "agent");
    asm volatile("s_waitcnt vmcnt(0)" ::: "memory");
  }
}

__device__ __forceinline__ const char* uni(const char* p) {
  const unsigned long long v = (unsigned long long)p;
  const unsigned lo = __builtin_amdgcn_readfirstlane((unsigned)v), hi = __builtin_amdgcn_readfirstlane((unsigned)(v >> 32));
  return (const char*)(((unsigned long long)hi << 32) | lo);
}
#define TSCLK "s_memrealtime"
#define TC(k) do { } while (0)
#define TS(k) do { } while (0)
#define TSDECL do { } while (0)
#define TSFLUSH(mask) do { } while (0)

constexpr size_t OFF_MOD  = 0;
constexpr size_t OFF_MOD2 = 65536;
constexpr size_t OFF_TMO  = 98304;
constexpr size_t OFF_TAG  = 131072;
constexpr size_t OFF_CLAIM = 135168;
constexpr size_t OFF_INIT = 196608;
constexpr size_t OFF_H1   = 262144;
constexpr size_t OFF_Q    = OFF_H1 + (size_t)SEQ * DM * 2;
constexpr size_t OFF_K    = OFF_Q + (size_t)SEQ * HD * 2;
constexpr size_t OFF_V    = OFF_K + (size_t)SEQ * HD * 2;
constexpr size_t OFF_O    = OFF_V + (size_t)SEQ * HD * 2;
constexpr size_t OFF_X1   = OFF_O + (size_t)SEQ * HD * 2;
constexpr size_t OFF_H2   = OFF_X1 + (size_t)SEQ * DM * 2;
constexpr size_t OFF_G    = OFF_H2 + (size_t)SEQ * DM * 2;
constexpr size_t OFF_KVM  = (size_t)92 << 20;
constexpr size_t OFF_XCH  = (size_t)100 << 20;
constexpr size_t OFF_P    = (size_t)112 << 20;
constexpr size_t OFF_P2   = (size_t)128 << 20;
constexpr size_t OFF_DUMP = (size_t)144 << 20;
constexpr int PROW = 4 * DM * 2;
#define TLQ 1
#define TLP 1
static_assert(OFF_G + (size_t)SEQ * FF * 2 <= OFF_KVM, "layout");
__device__ __forceinline__ char* xch_base(char* ws) { return (char*)(((size_t)(ws + OFF_XCH) + ((2u << 20) - 1)) & ~(size_t)((2u << 20) - 1)); }
#define XPAR 0
constexpr int PROG = 16;
constexpr int PQKV = 64;
constexpr int PL1 = 96;
constexpr int PL1B = 350;
constexpr int PSA = 128;
constexpr int PWO = 192;
constexpr int PLN = 224;
constexpr int PF2 = 320;
constexpr int PF1 = 448;
#define SDIR 40
#define SB 52
#define SC 60
#define MARG 20
#define MARL 15
#define MARF 8
#define SPRO 1
#define LAST 52
#define HB 60
#define HC 62
#define HF 63
#define MARX 10
#define MARH 35
#define SAW 0
constexpr int L_RED = 131072;
constexpr int L_SLOT = L_RED + 16384;
constexpr int L_GB = L_SLOT + 1024;
constexpr int MEGA_LDS = 163840;
static_assert(L_GB + 12288 + 2048 <= MEGA_LDS, "lds");
#define XPARC 0

__device__ __forceinline__ int bswz(int row) { return (((row >> 1) & 1) | (((row >> 3) & 1) << 1)) << 1; }
template <int OFF, bool SC1 = false>
__device__ __forceinline__ void glds16s(const char* sbase, unsigned voff, unsigned lds_base) {
  unsigned keep;
  if (SC1)
    asm volatile("s_mov_b32 %0, m0\n\ts_add_u32 m0, %3, %4\n\ts_nop 2\n\tglobal_load_lds_dwordx4 %1, %2 sc1\n\ts_mov_b32 m0, %0"
                 : "=&s"(keep) : "v"(voff), "s"(sbase), "s"(lds_base), "i"(OFF) : "memory", "scc");
  else
    asm volatile("s_mov_b32 %0, m0\n\ts_add_u32 m0, %3, %4\n\ts_nop 2\n\tglobal_load_lds_dwordx4 %1, %2\n\ts_mov_b32 m0, %0"
                 : "=&s"(keep) : "v"(voff), "s"(sbase), "s"(lds_base), "i"(OFF) : "memory", "scc");
}
template <int OFF>
__device__ __forceinline__ void glds16x2s(const char* sbase, unsigned voff, unsigned lds_base) {
  unsigned keep;
  asm volatile("s_mov_b32 %0, m0\n\ts_add_u32 m0, %3, %4\n\ts_nop 2\n\tglobal_load_lds_dwordx4 %1, %2 sc1\n\tglobal_load_lds_dwordx4 %1, %2 offset:1024 sc1\n\ts_mov_b32 m0, %0"
               : "=&s"(keep) : "v"(voff), "s"(sbase), "s"(lds_base), "i"(OFF) : "memory", "scc");
}
__device__ __forceinline__ void st16s(const char* sb, unsigned vo, u32x4 v) { asm volatile("global_store_dwordx4 %0, %1, %2 sc1\n\ts_nop 1" :: "v"(vo), "v"(v), "s"(sb) : "memory"); }
__device__ __forceinline__ void head_to_lds(const char* src, int lane, unsigned lds) {
  if (TLQ) {
    const int c = (lane & 15) ^ (lane >> 4);
    const unsigned v0 = (unsigned)((c >> 2) * 2048 + (lane >> 4) * 64 + (c & 3) * 16);
#pragma unroll
    for (int i = 0; i < 8; ++i) glds16s<0, true>(src + i * 256, v0 ^ ((i & 3) * 2048), lds + i * 1024);
  } else {
#pragma unroll
    for (int i = 0; i < 4; ++i) glds16x2s<0>(src + i * 2048, (unsigned)lane * 16, lds + i * 2048);
  }
}
#define TCHP ""
template <bool NT = false> __device__ __forceinline__ void touch(const void* sb, unsigned vo, unsigned lds) {
  if (NT) asm volatile("s_mov_b32 m0, %0\n\ts_nop 0\n\tglobal_load_lds_dword %1, %2 nt" :: "s"(lds), "v"(vo), "s"(sb) : "memory", "m0");
  else
  asm volatile("s_mov_b32 m0, %0\n\ts_nop 0\n\tglobal_load_lds_dword %1, %2" TCHP :: "s"(lds), "v"(vo), "s"(sb) : "memory", "m0");
}
#define WAIT_V(n) asm volatile("s_waitcnt vmcnt(" #n ")" ::: "memory")
#define BAR() do { asm volatile("s_waitcnt lgkmcnt(0)" ::: "memory"); __builtin_amdgcn_s_barrier(); asm volatile("" ::: "memory"); } while (0)
__device__ __forceinline__ void st4_sc1(void* p, unsigned v) { asm volatile("global_store_dword %0, %1, off sc1" :: "v"(p), "v"(v) : "memory"); }
__device__ __forceinline__ unsigned ld4_sc1(const void* p) {
  unsigned r; asm volatile("global_load_dword %0, %1, off sc1\n\ts_waitcnt vmcnt(0)" : "=&v"(r) : "v"(p) : "memory"); return r;
}
template <int CTRL> __device__ __forceinline__ unsigned dppu(unsigned x) { return (unsigned)__builtin_amdgcn_update_dpp(0, (int)x, CTRL, 0xf, 0xf, false); }
__device__ __forceinline__ unsigned rowmin16(unsigned v) {
  v = min(v, dppu<0x128>(v)); v = min(v, dppu<0x124>(v)); v = min(v, dppu<0x122>(v)); v = min(v, dppu<0x121>(v));
  return v;
}
__device__ __forceinline__ unsigned rdl(unsigned v, int l) { return (unsigned)__builtin_amdgcn_readlane((int)v, l); }
__device__ __forceinline__ unsigned qkv_avail(unsigned v, int m) {
  unsigned a = rdl(v, m);
#pragma unroll
  for (int j = 1; j < 4; ++j) { const unsigned p = rdl(v, 4 * j + m); if ((p >> 8) <= a) a = max(a, p & 255u); }
  return a;
}
constexpr float SCALE = 0.08838834764831845f;
constexpr float LOG2E = 1.4426950408889634f;
template <int N> __device__ __forceinline__ void delay64() {
  if (N >= 16) { __builtin_amdgcn_s_sleep(15); delay64<(N >= 16 ? N - 15 : 0)>(); } else if (N > 0) __builtin_amdgcn_s_sleep(N);
}
__device__ __forceinline__ void sw16(float x, float& a, float& b) {
  a = x; b = x; asm volatile("s_nop 1\n\tv_permlane16_swap_b32 %0, %1" : "+v"(a), "+v"(b));
}
__device__ __forceinline__ void sw32(float x, float& a, float& b) {
  a = x; b = x; asm volatile("s_nop 1\n\tv_permlane32_swap_b32 %0, %1" : "+v"(a), "+v"(b));
}
__device__ __forceinline__ float red16_32_max(float x) {
  float a, b; sw16(x, a, b); x = fmaxf(a, b); sw32(x, a, b); return fmaxf(a, b);
}
__device__ __forceinline__ float red16_32_add(float x) {
  float a, b; sw16(x, a, b); x = a + b; sw32(x, a, b); return a + b;
}
__device__ __forceinline__ unsigned cvtpk(float a, float b) { unsigned r; asm("v_cvt_pk_bf16_f32 %0, %1, %2" : "=v"(r) : "v"(a), "v"(b)); return r; }
__device__ __forceinline__ float max3(float a, float b, float c) { float r; asm("v_max3_f32 %0, %1, %2, %3" : "=v"(r) : "v"(a), "v"(b), "v"(c)); return r; }
__device__ __forceinline__ float max2(float a, float b) { float r; asm("v_max_f32 %0, %1, %2" : "=v"(r) : "v"(a), "v"(b)); return r; }
__device__ __forceinline__ unsigned ror8u(unsigned x) { return (unsigned)__builtin_amdgcn_update_dpp(0, (int)x, 0x128, 0xf, 0xf, false); }
__device__ __forceinline__ float ror8(float x) { return __builtin_bit_cast(float, ror8u(__builtin_bit_cast(unsigned, x))); }
__device__ __forceinline__ float add_ror8(float x) {
  return x + __builtin_bit_cast(float, __builtin_amdgcn_update_dpp(0, __builtin_bit_cast(int, x), 0x128, 0xf, 0xf, false));
}

__device__ __forceinline__ unsigned rt_now() { return (unsigned)__builtin_amdgcn_s_memrealtime(); }
__device__ __forceinline__ unsigned latest(unsigned v) {
  const unsigned now = rt_now() + 400u, age = rowmin16((now - v) & 0xFFFFFFu);
  return now - min(min(min(rdl(age, 0), rdl(age, 16)), min(rdl(age, 32), rdl(age, 48))), 900u);
}
__device__ __forceinline__ void wait_rt(unsigned t0, int d) {
  while ((int)(rt_now() - t0) < d) { }
}
__device__ __forceinline__ bool bells(bool ok, unsigned u, int b, int marg, int sb = SB) {
  if (b < sb || !__all(ok || (int)(u >> 24) > b)) return false;
  wait_rt(latest(!ok && (int)(u >> 24) == b + 1 ? u : rt_now() - 400u), marg);
  return true;
}
__device__ __forceinline__ void ld_fb(unsigned& v, unsigned& u, const void* sb, unsigned vo) {
  asm volatile("global_load_dword %0, %2, %3 sc1\n\tglobal_load_dword %1, %2, %3 offset:64 sc1\n\ts_waitcnt vmcnt(0)" : "=&v"(v), "=&v"(u) : "v"(vo), "s"(sb) : "memory");
}
__device__ __forceinline__ unsigned bell(int b, unsigned t) { return (unsigned)(b + 1) << 24 | (t & 0xFFFFFFu); }
typedef unsigned long long u64;
__device__ __forceinline__ unsigned tag2(u64 tag) { return (unsigned)(tag >> 32) ^ ((unsigned)tag * 0x9E3779B1u); }
__device__ __forceinline__ unsigned init_eval(u32x4 v, u64 tag) {
  if (!__all((v.x & ~63u) == (unsigned)tag << 6 && v.y == (unsigned)(tag >> 26))) return 0u;
  unsigned r = 8u;
#pragma unroll
  for (int c = 0; c < 3; ++c) r |= __popcll(__ballot(v.x & (1u << c))) > __popcll(__ballot(v.x & (8u << c))) ? 1u << c : 0u;
  return r ^ (XPAR * 3u + XPARC * 4u);
}
__device__ __forceinline__ unsigned init_wait(const char* ws, u64 tag) {
  for (int spins = 0; spins < SPIN_CAP; ++spins) {
    const unsigned r = init_eval(ld16_sc1(ws + OFF_INIT + lane_id() * 128), tag);
    if (r) return r;
    __builtin_amdgcn_s_sleep(4);
  }
  return 8u;
}
__device__ __forceinline__ void init_issue(const char* ws, unsigned lds) { glds16s<0, true>(uni(ws + OFF_INIT), (unsigned)lane_id() * 128u, lds); }
__device__ __forceinline__ unsigned init_get(const char* ws, u64 tag, const char* copy) {
  const unsigned r = init_eval(*(volatile LAS u32x4*)(copy + lane_id() * 16), tag);
  return r ? r : init_wait(ws, tag);
}
__device__ __forceinline__ unsigned* cnt_of(char* ws, unsigned in) { return (unsigned*)uni(xch_base(ws) + ((size_t)4 << 21) + (size_t)((in & 4u) << 11)); }
__device__ __forceinline__ void wait_l1(const unsigned* cnt, unsigned n, unsigned* tmo) {
  const int lane = lane_id();
  for (int spins = 0;; ++spins) {
    const unsigned v = ld4_sc1(cnt + (PL1 + (lane & 31)) * 32);
    if (__all(v >= n)) break;
    __builtin_amdgcn_s_sleep(8);
    if (give_up(spins, tmo, 12)) break;
  }
}
#define GKV 1
__device__ __forceinline__ void wait_k0(const unsigned* cnt, int h, unsigned* tmo) {
  for (int spins = 0;; ++spins) {
    const int l = lane_id();
    const unsigned v = ld4_sc1(cnt + (PQKV + h) * 32 + 1 + (l & 1));
    if (__all(v >= (unsigned)GKV)) break;
    __builtin_amdgcn_s_sleep(32);
    if (give_up(spins, tmo, 13)) break;
  }
}
#define WCLK(slot, b) do { } while (0)
#define WTM(slot, b, k) do { } while (0)
#define WTS(slot, b, k) do { } while (0)
#define WFB() do { } while (0)
#define STS(ws_, slot_, k_) do { } while (0)
typedef __bf16 bf16x2 __attribute__((ext_vector_type(2)));
__device__ __forceinline__ f32x2 pkfma(f32x2 a, f32x2 b, f32x2 c) { return __builtin_elementwise_fma(a, b, c); }
__device__ __forceinline__ float wavesum(float v) {
  v += __builtin_bit_cast(float, dppu<0x128>(__builtin_bit_cast(unsigned, v))); v += __builtin_bit_cast(float, dppu<0x124>(__builtin_bit_cast(unsigned, v)));
  v += __builtin_bit_cast(float, dppu<0x122>(__builtin_bit_cast(unsigned, v))); v += __builtin_bit_cast(float, dppu<0x121>(__builtin_bit_cast(unsigned, v)));
  return red16_32_add(v);
}
__device__ __forceinline__ float adds(unsigned v, unsigned sel, float c) { return __builtin_amdgcn_fdot2_f32_bf16(__builtin_bit_cast(bf16x2, v), __builtin_bit_cast(bf16x2, sel), c, false); }
__device__ __forceinline__ void sum4(const u32x4 (&p)[4], f32x2 (&a)[4]) {
  unsigned slo = 0x00003F80u, shi = 0x3F800000u;
  asm("" : "+s"(slo), "+s"(shi));
#pragma unroll
  for (int e = 0; e < 4; ++e) {
    float lo = bflo(p[0][e]), hi = bfhi(p[0][e]);
#pragma unroll
    for (int q = 1; q < 4; ++q) { lo = adds(p[q][e], slo, lo); hi = adds(p[q][e], shi, hi); }
    a[e] = f32x2{lo, hi};
  }
}
__device__ __forceinline__ u32x4 norm8(const f32x2 (&v)[4], const char* mt, float mb, float rden) {
  const f32x4 h0 = *(const f32x4*)(mt + 4096), h1 = *(const f32x4*)(mt + 4096 + 1024), c0 = *(const f32x4*)(mt + 8192), c1 = *(const f32x4*)(mt + 8192 + 1024);
  const f32x2 m2 = f32x2{mb, mb}, d2 = f32x2{rden, rden};
  const f32x2 r0 = pkfma(v[0] - m2, f32x2{c0[0], c0[1]} * d2, f32x2{h0[0], h0[1]}), r1 = pkfma(v[1] - m2, f32x2{c0[2], c0[3]} * d2, f32x2{h0[2], h0[3]});
  const f32x2 r2 = pkfma(v[2] - m2, f32x2{c1[0], c1[1]} * d2, f32x2{h1[0], h1[1]}), r3 = pkfma(v[3] - m2, f32x2{c1[2], c1[3]} * d2, f32x2{h1[2], h1[3]});
  return u32x4{pack2(r0[0], r0[1]), pack2(r1[0], r1[1]), pack2(r2[0], r2[1]), pack2(r3[0], r3[1])};
}
__device__ __forceinline__ void half_load(const char* P, const u16* res, int row, int hf, int lane, f32x2 (&a)[4], u32x4& rv) {
  const rsrc_t rp = mkrs(P), rr = mkrs(res);
  const unsigned po = TLP ? (unsigned)((row >> 5) * 32 * PROW + (hf * 16 + (lane >> 2)) * 2048 + (row & 31) * 64 + (lane & 3) * 16) : (unsigned)(row * PROW + hf * 1024 + lane * 16), ro = (unsigned)(row * DM * 2 + hf * 1024 + lane * 16);
  u32x4 p[4];
#pragma unroll
  for (int q = 0; q < 4; ++q) p[q] = bld16(rp, po + q * (TLP ? 65536 : 2048));
  if (res) rv = bld16(rr, ro);
  sum4(p, a);
}
__device__ __forceinline__ bool fin_half(bool fin, const char* P, const u16* res, const char* gbt, u16* __restrict__ o1, int row, int hf, int lane) {
  f32x2 a[4]; u32x4 rv;
  half_load(P, res, row, hf, lane, a, rv);
  const u32x4 gv = *(const u32x4*)(gbt + hf * 1024 + lane * 16);
  u32x4 o; f32x2 t = f32x2{0, 0};
#pragma unroll
  for (int e = 0; e < 4; ++e) { const f32x2 s = pkfma(f32x2{bflo(gv[e]), bfhi(gv[e])}, a[e], f32x2{bflo(rv[e]), bfhi(rv[e])}); o[e] = pack2(s[0], s[1]); t += s; }
  if (!fin && !__all(t[0] == t[0] && t[1] == t[1])) return false;
  st16_sc1(o1 + (size_t)row * DM + hf * 512 + lane * 8, o);
  return true;
}
#define LSL 2
#define FSH 4
#define FSL 2
#define GSL 2
#define XSL 2
#define DLN 10
#define LEB 64
#define MARE 25
#define LNE 1
template <int CTRL> __device__ __forceinline__ float dppf(float x) { return __builtin_bit_cast(float, dppu<CTRL>(__builtin_bit_cast(unsigned, x))); }
__device__ __forceinline__ bool ln_row(bool fin, int bb, unsigned* bl, const f32x2 (&a)[2][4], const f32x2 (&xf)[8], const f32x2 (&tg)[8], const f32x2 (&th)[8], const f32x2 (&tc)[8],
                                       const char* x1, const char* h2, unsigned xo, unsigned ho) {
  f32x2 v[8]; u32x4 o[2];
#pragma unroll
  for (int e = 0; e < 8; ++e) { const f32x2 t = pkfma(tg[e], a[e >> 2][e & 3], xf[e]); const unsigned u = pack2(t[0], t[1]); o[e >> 2][e & 3] = u; v[e] = f32x2{bflo(u), bfhi(u)}; }
  const float pv = __builtin_bit_cast(float, __builtin_amdgcn_readfirstlane(__float_as_int(v[0][0])));
  f32x2 d[8];
#pragma unroll
  for (int e = 0; e < 8; ++e) d[e] = v[e] - f32x2{pv, pv};
  const f32x2 s2 = ((d[0] + d[1]) + (d[2] + d[3])) + ((d[4] + d[5]) + (d[6] + d[7]));
  const f32x2 q2 = pkfma(d[3], d[3], pkfma(d[2], d[2], pkfma(d[1], d[1], d[0] * d[0]))) + pkfma(d[7], d[7], pkfma(d[6], d[6], pkfma(d[5], d[5], d[4] * d[4])));
  float s = s2[0] + s2[1], q = q2[0] + q2[1];
  if (!fin && !__all(s == s)) return false;
  if (LNE && bb >= SB && bl && lane_id() == 0) st4_sc1(bl, bell(bb, rt_now() + DLN + 30));
  s += dppf<0x128>(s); q += dppf<0x128>(q); s += dppf<0x124>(s); q += dppf<0x124>(q);
  s += dppf<0x122>(s); q += dppf<0x122>(q); s += dppf<0x121>(s); q += dppf<0x121>(q);
  asm volatile("s_nop 1\n\tv_permlane16_swap_b32 %0, %1" : "+v"(s), "+v"(q));
  s += q;
  { float x, y; sw32(s, x, y); s = x + y; }
  const float sh = __builtin_bit_cast(float, rdl(__builtin_bit_cast(unsigned, s), 0)) * (1.0f / DM), sq = __builtin_bit_cast(float, rdl(__builtin_bit_cast(unsigned, s), 16));
  const float mb = bfr(pv + sh), vb = bfr(fmaxf(sq * (1.0f / DM) - sh * sh, 0.0f));
  const float rden = __builtin_amdgcn_rcpf(bfr(__builtin_amdgcn_sqrtf(bfr(vb + 1e-5f))));
#pragma unroll
  for (int i = 0; i < 2; ++i) {
    u32x4 r;
#pragma unroll
    for (int e = 0; e < 4; ++e) { const f32x2 t = pkfma(v[4 * i + e] - f32x2{mb, mb}, tc[4 * i + e] * f32x2{rden, rden}, th[4 * i + e]); r[e] = pack2(t[0], t[1]); }
    st16s(h2, ho + i * 32768, r);
  }
  if (!LNE && bb >= SB && lane_id() == 0) st4_sc1(bl, bell(bb, rt_now() + DLN));
  st16s(x1, xo, o[0]); st16s(x1, xo + 1024, o[1]);
  return true;
}
__device__ __forceinline__ void row_table(char* tab, const float* mod, const unsigned* cnt, unsigned* tmo, int tid) {
  wait_l1(cnt + 1, 1u, tmo);
  const rsrc_t rm = mkrs((const char*)mod + OFF_MOD2);
#pragma unroll
  for (int i = 0; i < 2; ++i) {
    const int u = tid + 512 * i, a = u >> 8, c = u & 255;
    f32x4 v = __builtin_bit_cast(f32x4, bld16(rm, (unsigned)u * 16));
    v = f32x4{bfr(v[0]), bfr(v[1]), bfr(v[2]), bfr(v[3])};
    if (a == 2) v = f32x4{bfr(1.0f + v[0]), bfr(1.0f + v[1]), bfr(1.0f + v[2]), bfr(1.0f + v[3])};
    if (a < 3) *(f32x4*)(tab + a * 4096 + ((c >> 7) * 2 + (c & 1)) * 1024 + ((c >> 1) & 63) * 16) = v;
    else *(u32x2*)(tab + 12288 + c * 8) = u32x2{pack2(v[0], v[1]), pack2(v[2], v[3])};
  }
  __syncthreads();
}
__device__ __forceinline__ void row_role(char* smem, int wave_id_, int rbase, int par, const float* mod, const char* P, const u16* x, u16* x1, char* h2c,
                                         unsigned* cnt, unsigned* tmo, int slot, unsigned long long* mts) {
  const int tid = opaque_tid(), lane = tid & 63, wave = WAVE_ID;
  if (tid < 80) *(volatile LAS unsigned*)(smem + 16384 + tid * 4) = 0u;
  {
    static_assert(TLP && SB % 2 == 0, "");
    unsigned z = SENT; asm volatile("" : "+v"(z));
    const int r4 = rbase + 4 * par + (wave & 3);
    for (int bb = SB + (wave >> 2); bb < NSEG; bb += 2)
#pragma unroll
      for (int i = 0; i < 2; ++i) {
        st16s(uni((const char*)x1 + (size_t)bb * 65536), (unsigned)(r4 * DM * 2 + i * 1024 + lane * 16), u32x4{z, z, z, z});
        st16s(uni(h2c + (size_t)bb * 65536), (unsigned)(r4 * 128 + (lane & 7) * 16 + (lane >> 3) * 4096 + i * 32768), u32x4{z, z, z, z});
      }
    WAIT_V(0);
  }
  row_table(smem, mod, cnt, tmo, tid);
  const int rb = rbase + wave + 32 * par;
  const rsrc_t rp = mkrs(P), rr = mkrs(x);
  const unsigned l16 = lane * 16;
  const unsigned pwo = (unsigned)(PWO + (lane & 31)) * 128;
  unsigned* myp = cnt + (PLN + (rb >> 3)) * 32 + wave;
  f32x2 a[2][4];
  const int gp = wave >> 2, r4 = rbase + 4 * par + (wave & 3);
  volatile LAS unsigned* lwb = (volatile LAS unsigned*)(smem + 16384 + gp * 8);
  if (gp && lane == 0) st4_sc1(myp, 1u);
  f32x2 tg[8], th[8], tc[8];
#pragma unroll
  for (int i = 0; i < 4; ++i) {
    const char* mt = smem + lane * 16 + i * 1024;
    const f32x4 g_ = *(const f32x4*)mt, h_ = *(const f32x4*)(mt + 4096), c_ = *(const f32x4*)(mt + 8192);
    tg[2 * i] = f32x2{g_[0], g_[1]}; tg[2 * i + 1] = f32x2{g_[2], g_[3]}; th[2 * i] = f32x2{h_[0], h_[1]}; th[2 * i + 1] = f32x2{h_[2], h_[3]};
    tc[2 * i] = f32x2{c_[0], c_[1]}; tc[2 * i + 1] = f32x2{c_[2], c_[3]};
  }
  const unsigned xo = (unsigned)(r4 * DM * 2 + lane * 16), ho = (unsigned)(r4 * 128 + (((lane & 7) ^ (r4 & 7)) << 4) + (lane >> 3) * 4096);
  const unsigned po = (l16 >> 6) * 2048 + (l16 & 48);
  u32x4 rv[2];
#pragma unroll
  for (int i = 0; i < 2; ++i) rv[i] = __builtin_amdgcn_raw_buffer_load_b128(rr, xo + i * 1024, (unsigned)gp * 65536u, 0);
  unsigned wb = 0, d0 = 150, d1 = 150;
  for (int bb = gp; bb < NSEG; bb += 2) {
    f32x2 xf[8];
#pragma unroll
    for (int e = 0; e < 8; ++e) xf[e] = f32x2{bflo(rv[e >> 2][e & 3]), bfhi(rv[e >> 2][e & 3])};
    asm volatile("" : "+v"(xf[0]), "+v"(xf[1]), "+v"(xf[2]), "+v"(xf[3]), "+v"(xf[4]), "+v"(xf[5]), "+v"(xf[6]), "+v"(xf[7]));
    if (wb <= (unsigned)bb) {
      if ((wave & 3) == 0) {
        wb = (unsigned)(bb + 1);
        for (int spins = 0;; ++spins) {
          unsigned v, u;
          ld_fb(v, u, cnt, pwo);
          if (__all(v > (unsigned)bb + 2u)) { v = rowmin16(v); wb = min(rdl(v, 0), rdl(v, 16)); break; }
          if (__all(v > (unsigned)bb)) break;
          if (bb >= LEB && __all(v > (unsigned)bb || (u >> 24) > (unsigned)bb)) {
            const unsigned now = rt_now(), g0 = latest(v <= (unsigned)bb && (u >> 24) == (unsigned)bb + 1u ? u : now - 400u) + MARE, go = (int)(g0 - now) > 0 ? g0 : now;
            if (lane < 4) st4_sc1(myp + 16 + lane, bell(bb, go + max(d0, d1)));
            wait_rt(go, 0);
            wb |= 256u;
            break;
          }
          if (bells(v > (unsigned)bb, u, bb, MARL)) break;
          __builtin_amdgcn_s_sleep(LSL);
          if (give_up(spins, tmo, 11)) break;
        }
        lwb[0] = wb;
      } else {
        while (((wb = __builtin_amdgcn_readfirstlane(lwb[0])) & 255u) <= (unsigned)bb) __builtin_amdgcn_s_sleep(1);
      }
    }
    const bool rung = wb == (unsigned)bb + 257u;
    wb &= 255u;
    const unsigned t0 = rt_now();
    WTM(slot, bb, 0);
    bool fin = bb < SB;
    for (int spins = 0;; ++spins) {
      u32x4 pp[2][4];
#pragma unroll
      for (int i = 0; i < 2; ++i)
#pragma unroll
        for (int q = 0; q < 4; ++q) pp[i][q] = __builtin_amdgcn_raw_buffer_load_b128(rp, po + i * 32768, (unsigned)(bb * 32 * PROW + q * 65536 + r4 * 64), 16);
      sum4(pp[0], a[0]); sum4(pp[1], a[1]);
      if (ln_row(fin, bb, rung && spins == 0 ? nullptr : myp + 16, a, xf, tg, th, tc, uni((const char*)x1 + (size_t)bb * 65536), uni(h2c + (size_t)bb * 65536), xo, ho)) break;
      if (spins > 1) fin = __all(ld4_sc1((const char*)cnt + pwo) > (unsigned)bb) || give_up(spins, tmo, 11);
    }
    d1 = d0; d0 = min(rt_now() - t0 + 12u, 200u);
    WTM(slot, bb, 3);
#pragma unroll
    for (int i = 0; i < 2; ++i) rv[i] = __builtin_amdgcn_raw_buffer_load_b128(rr, xo + i * 1024, (unsigned)min(bb + 2, NSEG - 1) * 65536u, 0);
    WAIT_V(2);
    unsigned o = 0;
    if (lane == 0) o = __hip_atomic_fetch_add((LAS unsigned*)(smem + 16448 + bb * 4), 1u, __ATOMIC_RELAXED, __HIP_MEMORY_SCOPE_WORKGROUP);
    if (__builtin_amdgcn_readfirstlane(o) == 3u) {
      if (lane < 4) st4_sc1(myp - (wave & 3) + lane, (unsigned)(bb + 2));
      WTM(slot, bb, 2);
    }
  }
}
#define NCS 8
__device__ __forceinline__ void l1_role(char* smem, int wave_id_, bool fst, int c, const u16* __restrict__ x, const float* __restrict__ mod,
                                        const u16* __restrict__ mem0, char* __restrict__ h1c, unsigned*& cnt, u64 tag, unsigned* tmo, unsigned long long* mts) {
  const int tid = opaque_tid(), lane = tid & 63, wave = WAVE_ID;
  char* ws = (char*)mod - OFF_MOD;
  u32x4 xr[7][2];
  const int row0 = (fst ? 0 : 256) + 8 * c + wave;
  const u16* src = x + (size_t)row0 * DM + lane * 8;
  xr[0][0] = ldnt<2>(src); xr[0][1] = ldnt<2>(src + 512);
  volatile LAS unsigned* sg = (volatile LAS unsigned*)(smem + L_SLOT + 784);
  if (!fst) {
#pragma unroll
    for (int pp = 1; pp < 7; ++pp) { xr[pp][0] = ldnt<2>(src + (size_t)pp * 256 * DM); xr[pp][1] = ldnt<2>(src + (size_t)pp * 256 * DM + 512); }
  } else if ((wave & 3) != 3) {
    char* cal = xch_base(ws) + ((size_t)((wave & 3) == 0 ? 0 : (wave & 3) + 2) << 21) + (124 + (wave >> 2) * 2 + (c >> 4)) * 16384 + (c & 15) * 512;
    st16_sc1(cal + (lane >> 5) * 8192 + (lane & 31) * 16, u32x4{SENT, SENT, SENT, SENT});
    WAIT_V(0);
    unsigned best[2] = {1u << 30, 1u << 30};
    const char* sb = uni(cal);
#pragma unroll
    for (int r = 1; r < NCS; ++r) {
      unsigned long long t0, t1; unsigned v;
      asm volatile("s_memtime %0\n\ts_waitcnt lgkmcnt(0)\n\ts_load_dword %2, %3, %4 glc\n\ts_waitcnt lgkmcnt(0)\n\ts_memtime %1\n\ts_waitcnt lgkmcnt(0)"
                   : "=&s"(t0), "=&s"(t1), "=&s"(v) : "s"(sb), "i"((r & 1) * 8192 + (r >> 1) * 128) : "memory");
      if (r > 1) best[r & 1] = min(best[r & 1], (unsigned)(t1 - t0));
    }
    sg[2 * wave] = best[0]; sg[2 * wave + 1] = best[1];
  } else {
    const int pc = (c & 7) * 2 + (wave >> 2), g = lane >> 4, hf = (lane >> 2) & 1, row = ((lane >> 3) & 1) * 4 + (lane & 3);
    u32x4 v = *(const u32x4*)(mem0 + row * DM + 32 * (2 * pc + hf) + 8 * g);
    v.x = nosent(v.x); v.y = nosent(v.y); v.z = nosent(v.z); v.w = nosent(v.w);
    st16_sc1(xch_base(ws) + ((size_t)3 << 21) + (size_t)(((c >> 3) & 1) * 2 + (pc >> 3)) * 16384 + (c >> 4) * 8192 + (pc & 7) * 1024 + lane * 16, v);
    const int q = c * 2 + (wave >> 2);
    if (q < 36) {
      char* p = xch_base(ws) + (q < 16 ? ((size_t)3 << 21) + 48 * 16384 + q * 4096 : ((size_t)4 << 21) + (q - 16) * 4096) + lane * 16;
#pragma unroll
      for (int i = 0; i < 4; ++i) st16_sc1(p + i * 1024, u32x4{0u, 0u, 0u, 0u});
    } else if (q == 36 && lane == 0) st4_sc1(tmo, 0u);
  }
  if (fst && wave == 0) STS(ws, 128 + (c >> 3) * 32 + 24 + (c & 7), 2);
  {
    f32x4 v;
    for (int spins = 0; spins < SPIN_CAP; ++spins) {
      u32x4 m[4];
      asm volatile("s_nop 4\n\tglobal_load_dwordx4 %0, %4, %6 sc1\n\tglobal_load_dwordx4 %1, %4, %6 offset:16 sc1\n\t"
                   "global_load_dwordx4 %2, %5, %6 sc1\n\tglobal_load_dwordx4 %3, %5, %6 offset:16 sc1\n\ts_waitcnt vmcnt(0)"
                   : "=&v"(m[0]), "=&v"(m[1]), "=&v"(m[2]), "=&v"(m[3]) : "v"((unsigned)tid * 32), "v"((unsigned)tid * 32 + 16384), "s"(mod) : "memory");
      bool ok = true;
#pragma unroll
      for (int i = 0; i < 4; ++i) ok = ok && m[i].y == (unsigned)tag && m[i].w == tag2(tag);
      v = f32x4{__uint_as_float(m[0].x) + __uint_as_float(m[2].x), __uint_as_float(m[0].z) + __uint_as_float(m[2].z),
                __uint_as_float(m[1].x) + __uint_as_float(m[3].x), __uint_as_float(m[1].z) + __uint_as_float(m[3].z)};
      if (__all(ok)) break;
      __builtin_amdgcn_s_sleep(4);
    }
    v = f32x4{bfr(v[0]), bfr(v[1]), bfr(v[2]), bfr(v[3])};
    const int a = tid >> 8, cc = tid & 255;
    if (a) v = f32x4{bfr(1.0f + v[0]), bfr(1.0f + v[1]), bfr(1.0f + v[2]), bfr(1.0f + v[3])};
    *(f32x4*)(smem + 4096 + a * 4096 + ((cc >> 7) * 2 + (cc & 1)) * 1024 + ((cc >> 1) & 63) * 16) = v;
  }
  BAR();
  if (fst && wave == 0) STS(ws, 128 + (c >> 3) * 32 + 24 + (c & 7), 3);
#pragma unroll
  for (int p = 0; p < 7; ++p) {
    if (p > 0 && fst) break;
    f32x2 v[2][4];
#pragma unroll
    for (int i = 0; i < 2; ++i)
#pragma unroll
      for (int e = 0; e < 4; ++e) v[i][e] = f32x2{bflo(xr[p][i][e]), bfhi(xr[p][i][e])};
    const f32x2 s2 = ((v[0][0] + v[0][1]) + (v[0][2] + v[0][3])) + ((v[1][0] + v[1][1]) + (v[1][2] + v[1][3]));
    const float mean = wavesum(s2[0] + s2[1]) * (1.0f / DM);
    f32x2 q2 = f32x2{0, 0};
#pragma unroll
    for (int i = 0; i < 8; ++i) { const f32x2 d = v[i >> 2][i & 3] - f32x2{mean, mean}; q2 = pkfma(d, d, q2); }
    const float q = wavesum(q2[0] + q2[1]);
    if (p == 0 && fst && wave == 0) STS((char*)tmo - OFF_TMO, 128 + (c >> 3) * 32 + 24 + (c & 7), 5);
    const float mb = bfr(mean), vb = bfr(q * (1.0f / DM));
    const float rden = 1.0f / bfr(sqrtf(bfr(vb + 1e-5f)));
    const int row = 256 * p + row0, rb = row & 31;
    char* dst = h1c + (size_t)(row >> 5) * 65536 + (lane >> 3) * 4096 + rb * 128 + (((lane & 7) ^ (rb & 7)) << 4);
    const char* ml = smem + lane * 16;
#pragma unroll
    for (int i = 0; i < 2; ++i) st16_sc1(dst + i * 32768, norm8(v[i], ml + i * 2048, mb, rden));
  }
  if (fst) {
    unsigned votes = 0, xcc; asm volatile("s_getreg_b32 %0, hwreg(HW_REG_XCC_ID)" : "=s"(xcc));
#pragma unroll
    for (int i = 0; i < 3; ++i) {
      const unsigned b0 = min(sg[2 * i], sg[2 * i + 8]), b1 = min(sg[2 * i + 1], sg[2 * i + 9]), df = max(b0, b1) - min(b0, b1);
      votes |= df < 110u || df > 400u ? 0u : (((b1 < b0) != ((xcc & 4) != 0)) ? 1u : 8u) << i;
    }
    WAIT_V(0);
    __builtin_amdgcn_s_barrier();
    if (tid == 0) st8_sc1(ws + OFF_INIT + c * 128, u32x2{(unsigned)tag << 6 | votes, (unsigned)(tag >> 26)});
  } else {
    cnt = cnt_of(ws, init_wait(ws, tag));
    WAIT_V(0);
    __builtin_amdgcn_s_barrier();
    if (tid == 0) st4_sc1(cnt + (PL1B + c) * 32, 7u);
  }
  if (wave == 0) STS(ws, 128 + (c >> 3) * 32 + 24 + (c & 7), fst ? 6 : 7);
}
__device__ __forceinline__ void arm_job(int wave_id_, int j, u64 tag, char* ws) {
  const int hb = j + 32 * WAVE_ID, b = hb >> 1;
  if (b < 80) {
    const char* p = uni(xch_base(ws) + (b < 48 ? (size_t)((b >> 4) * 32 + (b & 15)) * 16384
                                      : ((size_t)3 << 21) + (size_t)(b < 56 ? 8 + ((b - 48) >> 2) * 8 + (b & 3) : b - 56 + 24) * 16384) + (hb & 1) * 8192);
#pragma unroll
    for (int i = 0; i < 8; ++i) st16s(p, (unsigned)(i * 1024 + lane_id() * 16), u32x4{SENT, SENT, SENT, SENT});
  }
  WAIT_V(0);
  BAR();
  if (opaque_tid() == 0) st8_sc1(ws + OFF_INIT + (32 + j) * 128, u32x2{(unsigned)tag << 6, (unsigned)(tag >> 26)});
}
__device__ __forceinline__ void mod2_job(char* smem, int wave_id_, int c, const float* __restrict__ mod, const u16* __restrict__ cvec, const u16* __restrict__ wmod, unsigned* cnt) {
  const int tid = opaque_tid();
  {
    const float sva = bf2f(cvec[tid]), svb = bf2f(cvec[tid + 512]);
    float* sv = (float*)(smem + 12288);
    sv[tid] = bfr(sva / (1.0f + __expf(-sva))); sv[tid + 512] = bfr(svb / (1.0f + __expf(-svb)));
  }
  __syncthreads();
  {
    const int cg = tid & 15, r0 = tid >> 4;
    const u16* wp = wmod + (size_t)r0 * (6 * DM) + 2 * DM + 128 * c + cg * 8;
    const float* sv = (const float*)(smem + 12288) + r0;
    f32x2 acc[4] = {f32x2{0, 0}, f32x2{0, 0}, f32x2{0, 0}, f32x2{0, 0}};
#pragma unroll 1
    for (int o = 0; o < 4; ++o) {
      u32x4 w[8];
#pragma unroll
      for (int i = 0; i < 8; ++i) w[i] = ldnt<1>(wp + (size_t)(o * 8 + i) * 32 * (6 * DM));
#pragma unroll
      for (int i = 0; i < 8; ++i) {
        const float s = sv[(o * 8 + i) * 32];
#pragma unroll
        for (int e = 0; e < 4; ++e) acc[e] = pkfma(f32x2{s, s}, f32x2{bflo(w[i][e]), bfhi(w[i][e])}, acc[e]);
      }
    }
    float* red = (float*)(smem + 16384);
    *(f32x4*)(red + r0 * 128 + cg * 8) = f32x4{acc[0][0], acc[0][1], acc[1][0], acc[1][1]};
    *(f32x4*)(red + r0 * 128 + cg * 8 + 4) = f32x4{acc[2][0], acc[2][1], acc[3][0], acc[3][1]};
    __syncthreads();
    if (tid < 128) {
      float s = 0;
#pragma unroll
      for (int r = 0; r < 32; ++r) s += red[r * 128 + tid];
      st4_sc1((char*)mod + OFF_MOD2 + (128 * c + tid) * 4, __builtin_bit_cast(unsigned, s));
    }
    WAIT_V(0);
    __builtin_amdgcn_s_barrier();
    if (tid == 0) st4_sc1(cnt + (PL1 + c) * 32 + 1, 1u);
  }
}
enum { R_QKV = 0, R_PART = 1, R_F1 = 2, R_PART2 = 3 };
struct Dep {
  const char* base; unsigned voff; unsigned mul, add;
};
__device__ __forceinline__ unsigned dep_eval(unsigned v, const Dep& d) {
  v = rowmin16(v * d.mul + (d.add + 64u));
  return min(min(rdl(v, 0), rdl(v, 16)), min(rdl(v, 32), rdl(v, 48))) - 64u;
}
typedef unsigned u32x32 __attribute__((ext_vector_type(32)));
typedef unsigned u32x16 __attribute__((ext_vector_type(16)));
__device__ __forceinline__ void fast_ld0(const u32x32 (&W)[4], u32x16& pd, unsigned ea, unsigned da, int& b, int& lf, int cp, unsigned up, unsigned sa, unsigned mu, unsigned ad,
    unsigned rw, unsigned ab, const char* p1, const char* p0, unsigned l1, unsigned l0, const unsigned (&av)[8], int la) {
  asm volatile("s_setprio 0\n\t"
    "s_cmp_eq_u32 %[la], 3\n\t"
    "s_waitcnt vmcnt(12)\n\t"
    "s_cbranch_scc1 20f\n\t"
    "s_waitcnt vmcnt(4)\n\t"
    "20:\n\t"
    "s_barrier\n\t"
    "ds_read_b128 v[96:99], %[ea] offset:0\n\t"
    "ds_read_b128 v[100:103], %[ea] offset:2048\n\t"
    "ds_read_b128 v[104:107], %[da] offset:0\n\t"
    "ds_read_b128 v[108:111], %[da] offset:2048\n\t"
    "ds_read_b128 v[112:115], %[ea] offset:4096\n\t"
    "ds_read_b128 v[116:119], %[ea] offset:6144\n\t"
    "s_branch 2f\n\t"
    ".p2align 6\n\t"
    "1:\n\t"
    "s_cmp_eq_u32 %[la], 3\n\t"
    "s_waitcnt vmcnt(12)\n\t"
    "s_cbranch_scc1 21f\n\t"
    "s_waitcnt vmcnt(4)\n\t"
    "21:\n\t"
    "s_barrier\n\t"
    "2:\n\t"
    "s_waitcnt lgkmcnt(4)\n\t"
    "v_mfma_f32_16x16x32_bf16 v[64:67], v[128:131], v[96:99], 0\n\t"
    "ds_read_b128 v[120:123], %[da] offset:4096\n\t"
    "s_add_u32 m0, %[l1], 0\n\t"
    "ds_write_b128 %[rw], v[80:83] offset:0\n\t"
    "v_mfma_f32_16x16x32_bf16 v[68:71], v[132:135], v[96:99], 0\n\t"
    "ds_read_b128 v[124:127], %[da] offset:6144\n\t"
    "global_load_lds_dwordx4 %[av0], s[36:37] sc1\n\t"
    "ds_write_b128 %[rw], v[84:87] offset:1024\n\t"
    "v_mfma_f32_16x16x32_bf16 v[72:75], v[128:131], v[100:103], 0\n\t"
    "s_add_u32 m0, %[l1], 32768\n\t"
    "ds_write_b128 %[rw], v[88:91] offset:2048\n\t"
    "v_mfma_f32_16x16x32_bf16 v[76:79], v[132:135], v[100:103], 0\n\t"
    "global_load_lds_dwordx4 %[av1], s[36:37] sc1\n\t"
    "ds_write_b128 %[rw], v[92:95] offset:3072\n\t"
    "s_waitcnt lgkmcnt(8)\n\t"
    "v_mfma_f32_16x16x32_bf16 v[64:67], v[136:139], v[104:107], v[64:67]\n\t"
    "ds_read_b128 v[96:99], %[ea] offset:8192\n\t"
    "s_add_u32 m0, %[l1], 4096\n\t"
    "v_mfma_f32_16x16x32_bf16 v[68:71], v[140:143], v[104:107], v[68:71]\n\t"
    "ds_read_b128 v[100:103], %[ea] offset:10240\n\t"
    "global_load_lds_dwordx4 %[av2], s[36:37] sc1\n\t"
    "v_mfma_f32_16x16x32_bf16 v[72:75], v[136:139], v[108:111], v[72:75]\n\t"
    "s_add_u32 m0, %[l1], 36864\n\t"
    "v_mfma_f32_16x16x32_bf16 v[76:79], v[140:143], v[108:111], v[76:79]\n\t"
    "global_load_lds_dwordx4 %[av3], s[36:37] sc1\n\t"
    "s_waitcnt lgkmcnt(8)\n\t"
    "v_mfma_f32_16x16x32_bf16 v[64:67], v[144:147], v[112:115], v[64:67]\n\t"
    "ds_read_b128 v[104:107], %[da] offset:8192\n\t"
    "s_add_u32 m0, %[l1], 8192\n\t"
    "v_mfma_f32_16x16x32_bf16 v[68:71], v[148:151], v[112:115], v[68:71]\n\t"
    "ds_read_b128 v[108:111], %[da] offset:10240\n\t"
    "global_load_lds_dwordx4 %[av4], s[36:37] sc1\n\t"
    "v_mfma_f32_16x16x32_bf16 v[72:75], v[144:147], v[116:119], v[72:75]\n\t"
    "s_add_u32 m0, %[l1], 40960\n\t"
    "v_mfma_f32_16x16x32_bf16 v[76:79], v[148:151], v[116:119], v[76:79]\n\t"
    "global_load_lds_dwordx4 %[av5], s[36:37] sc1\n\t"
    "s_waitcnt lgkmcnt(7)\n\t"
    "v_mfma_f32_16x16x32_bf16 v[64:67], v[152:155], v[120:123], v[64:67]\n\t"
    "ds_read_b128 v[112:115], %[ea] offset:12288\n\t"
    "s_add_u32 m0, %[l1], 12288\n\t"
    "v_mfma_f32_16x16x32_bf16 v[68:71], v[156:159], v[120:123], v[68:71]\n\t"
    "ds_read_b128 v[116:119], %[ea] offset:14336\n\t"
    "global_load_lds_dwordx4 %[av6], s[36:37] sc1\n\t"
    "v_mfma_f32_16x16x32_bf16 v[72:75], v[152:155], v[124:127], v[72:75]\n\t"
    "s_add_u32 m0, %[l1], 45056\n\t"
    "v_mfma_f32_16x16x32_bf16 v[76:79], v[156:159], v[124:127], v[76:79]\n\t"
    "global_load_lds_dwordx4 %[av7], s[36:37] sc1\n\t"
    "s_waitcnt lgkmcnt(4)\n\t"
    "v_mfma_f32_16x16x32_bf16 v[64:67], v[160:163], v[96:99], v[64:67]\n\t"
    "ds_read_b128 v[120:123], %[da] offset:12288\n\t"
    "v_mfma_f32_16x16x32_bf16 v[68:71], v[164:167], v[96:99], v[68:71]\n\t"
    "ds_read_b128 v[124:127], %[da] offset:14336\n\t"
    "v_mfma_f32_16x16x32_bf16 v[72:75], v[160:163], v[100:103], v[72:75]\n\t"
    "v_mfma_f32_16x16x32_bf16 v[76:79], v[164:167], v[100:103], v[76:79]\n\t"
    "s_waitcnt lgkmcnt(4)\n\t"
    "v_mfma_f32_16x16x32_bf16 v[64:67], v[168:171], v[104:107], v[64:67]\n\t"
    "ds_read_b128 v[96:99], %[ea] offset:16384\n\t"
    "v_mfma_f32_16x16x32_bf16 v[68:71], v[172:175], v[104:107], v[68:71]\n\t"
    "ds_read_b128 v[100:103], %[ea] offset:18432\n\t"
    "v_mfma_f32_16x16x32_bf16 v[72:75], v[168:171], v[108:111], v[72:75]\n\t"
    "v_mfma_f32_16x16x32_bf16 v[76:79], v[172:175], v[108:111], v[76:79]\n\t"
    "s_waitcnt lgkmcnt(4)\n\t"
    "v_mfma_f32_16x16x32_bf16 v[64:67], v[176:179], v[112:115], v[64:67]\n\t"
    "ds_read_b128 v[104:107], %[da] offset:16384\n\t"
    "v_mfma_f32_16x16x32_bf16 v[68:71], v[180:183], v[112:115], v[68:71]\n\t"
    "ds_read_b128 v[108:111], %[da] offset:18432\n\t"
    "v_mfma_f32_16x16x32_bf16 v[72:75], v[176:179], v[116:119], v[72:75]\n\t"
    "v_mfma_f32_16x16x32_bf16 v[76:79], v[180:183], v[116:119], v[76:79]\n\t"
    "s_waitcnt lgkmcnt(4)\n\t"
    "v_mfma_f32_16x16x32_bf16 v[64:67], v[184:187], v[120:123], v[64:67]\n\t"
    "ds_read_b128 v[112:115], %[ea] offset:20480\n\t"
    "v_mfma_f32_16x16x32_bf16 v[68:71], v[188:191], v[120:123], v[68:71]\n\t"
    "ds_read_b128 v[116:119], %[ea] offset:22528\n\t"
    "v_mfma_f32_16x16x32_bf16 v[72:75], v[184:187], v[124:127], v[72:75]\n\t"
    "v_mfma_f32_16x16x32_bf16 v[76:79], v[188:191], v[124:127], v[76:79]\n\t"
    "s_cmp_eq_u32 %[la], 3\n\t"
    "s_waitcnt vmcnt(12)\n\t"
    "s_cbranch_scc1 22f\n\t"
    "s_waitcnt vmcnt(4)\n\t"
    "22:\n\t"
    "s_barrier\n\t"
    "s_waitcnt lgkmcnt(4)\n\t"
    "v_mfma_f32_16x16x32_bf16 v[64:67], v[192:195], v[96:99], v[64:67]\n\t"
    "ds_read_b128 v[120:123], %[da] offset:20480\n\t"
    "s_add_u32 m0, %[l0], 0\n\t"
    "v_mfma_f32_16x16x32_bf16 v[68:71], v[196:199], v[96:99], v[68:71]\n\t"
    "ds_read_b128 v[124:127], %[da] offset:22528\n\t"
    "global_load_lds_dwordx4 %[av0], s[38:39] sc1\n\t"
    "v_mfma_f32_16x16x32_bf16 v[72:75], v[192:195], v[100:103], v[72:75]\n\t"
    "s_add_u32 m0, %[l0], 32768\n\t"
    "v_mfma_f32_16x16x32_bf16 v[76:79], v[196:199], v[100:103], v[76:79]\n\t"
    "global_load_lds_dwordx4 %[av1], s[38:39] sc1\n\t"
    "s_waitcnt lgkmcnt(4)\n\t"
    "v_mfma_f32_16x16x32_bf16 v[64:67], v[200:203], v[104:107], v[64:67]\n\t"
    "ds_read_b128 v[96:99], %[ea] offset:24576\n\t"
    "s_add_u32 m0, %[l0], 4096\n\t"
    "v_mfma_f32_16x16x32_bf16 v[68:71], v[204:207], v[104:107], v[68:71]\n\t"
    "ds_read_b128 v[100:103], %[ea] offset:26624\n\t"
    "global_load_lds_dwordx4 %[av2], s[38:39] sc1\n\t"
    "v_mfma_f32_16x16x32_bf16 v[72:75], v[200:203], v[108:111], v[72:75]\n\t"
    "s_add_u32 m0, %[l0], 36864\n\t"
    "v_mfma_f32_16x16x32_bf16 v[76:79], v[204:207], v[108:111], v[76:79]\n\t"
    "global_load_lds_dwordx4 %[av3], s[38:39] sc1\n\t"
    "s_add_u32 s36, s36, %[ab]\n\t"
    "s_addc_u32 s37, s37, 0\n\t"
    "s_waitcnt lgkmcnt(4)\n\t"
    "v_mfma_f32_16x16x32_bf16 v[64:67], v[208:211], v[112:115], v[64:67]\n\t"
    "ds_read_b128 v[104:107], %[da] offset:24576\n\t"
    "s_add_u32 m0, %[l0], 8192\n\t"
    "v_mfma_f32_16x16x32_bf16 v[68:71], v[212:215], v[112:115], v[68:71]\n\t"
    "ds_read_b128 v[108:111], %[da] offset:26624\n\t"
    "global_load_lds_dwordx4 %[av4], s[38:39] sc1\n\t"
    "v_mfma_f32_16x16x32_bf16 v[72:75], v[208:211], v[116:119], v[72:75]\n\t"
    "ds_read_b32 v39, %[sa]\n\t"
    "s_add_u32 m0, %[l0], 40960\n\t"
    "v_mfma_f32_16x16x32_bf16 v[76:79], v[212:215], v[116:119], v[76:79]\n\t"
    "global_load_lds_dwordx4 %[av5], s[38:39] sc1\n\t"
    "s_xor_b32 %[l1], %[l1], 0x10000\n\t"
    "s_waitcnt lgkmcnt(5)\n\t"
    "v_mfma_f32_16x16x32_bf16 v[64:67], v[216:219], v[120:123], v[64:67]\n\t"
    "ds_read_b128 v[112:115], %[ea] offset:28672\n\t"
    "s_add_u32 m0, %[l0], 12288\n\t"
    "v_mfma_f32_16x16x32_bf16 v[68:71], v[220:223], v[120:123], v[68:71]\n\t"
    "ds_read_b128 v[116:119], %[ea] offset:30720\n\t"
    "global_load_lds_dwordx4 %[av6], s[38:39] sc1\n\t"
    "v_mfma_f32_16x16x32_bf16 v[72:75], v[216:219], v[124:127], v[72:75]\n\t"
    "v_xor_b32 %[ea], 0x10000, %[ea]\n\t"
    "s_add_u32 m0, %[l0], 45056\n\t"
    "v_mfma_f32_16x16x32_bf16 v[76:79], v[220:223], v[124:127], v[76:79]\n\t"
    "global_load_lds_dwordx4 %[av7], s[38:39] sc1\n\t"
    "s_waitcnt lgkmcnt(5)\n\t"
    "v_mfma_f32_16x16x32_bf16 v[64:67], v[224:227], v[96:99], v[64:67]\n\t"
    "ds_read_b128 v[120:123], %[da] offset:28672\n\t"
    "v_mfma_f32_16x16x32_bf16 v[68:71], v[228:231], v[96:99], v[68:71]\n\t"
    "ds_read_b128 v[124:127], %[da] offset:30720\n\t"
    "v_mfma_f32_16x16x32_bf16 v[72:75], v[224:227], v[100:103], v[72:75]\n\t"
    "v_xor_b32 %[da], 0x10000, %[da]\n\t"
    "v_mfma_f32_16x16x32_bf16 v[76:79], v[228:231], v[100:103], v[76:79]\n\t"
    "s_waitcnt lgkmcnt(5)\n\t"
    "v_mfma_f32_16x16x32_bf16 v[64:67], v[232:235], v[104:107], v[64:67]\n\t"
    "ds_read_b128 v[96:99], %[ea] offset:0\n\t"
    "v_mfma_f32_16x16x32_bf16 v[68:71], v[236:239], v[104:107], v[68:71]\n\t"
    "ds_read_b128 v[100:103], %[ea] offset:2048\n\t"
    "v_mfma_f32_16x16x32_bf16 v[72:75], v[232:235], v[108:111], v[72:75]\n\t"
    "v_mfma_f32_16x16x32_bf16 v[76:79], v[236:239], v[108:111], v[76:79]\n\t"
    "s_waitcnt lgkmcnt(4)\n\t"
    "v_mfma_f32_16x16x32_bf16 v[64:67], v[240:243], v[112:115], v[64:67]\n\t"
    "ds_read_b128 v[104:107], %[da] offset:0\n\t"
    "v_mad_u32_u24 v39, v39, %[mu], %[ad]\n\t"
    "v_mfma_f32_16x16x32_bf16 v[68:71], v[244:247], v[112:115], v[68:71]\n\t"
    "ds_read_b128 v[108:111], %[da] offset:2048\n\t"
    "v_cmp_ge_i32 vcc, %[lf], v39\n\t"
    "s_cmp_eq_u64 vcc, 0\n\t"
    "s_addc_u32 %[lf], %[lf], 0\n\t"
    "v_mfma_f32_16x16x32_bf16 v[72:75], v[240:243], v[116:119], v[72:75]\n\t"
    "v_mfma_f32_16x16x32_bf16 v[76:79], v[244:247], v[116:119], v[76:79]\n\t"
    "v_cmp_ge_i32 vcc, %[lf], v39\n\t"
    "s_cmp_eq_u64 vcc, 0\n\t"
    "s_addc_u32 %[lf], %[lf], 0\n\t"
    "s_waitcnt lgkmcnt(4)\n\t"
    "v_mfma_f32_16x16x32_bf16 v[80:83], v[248:251], v[120:123], v[64:67]\n\t"
    "ds_read_b128 v[112:115], %[ea] offset:4096\n\t"
    "s_min_i32 %[lf], %[lf], %[cp]\n\t"
    "s_add_u32 %[b], %[b], 1\n\t"
    "v_mfma_f32_16x16x32_bf16 v[84:87], v[252:255], v[120:123], v[68:71]\n\t"
    "ds_read_b128 v[116:119], %[ea] offset:6144\n\t"
    "v_mfma_f32_16x16x32_bf16 v[88:91], v[248:251], v[124:127], v[72:75]\n\t"
    "v_mfma_f32_16x16x32_bf16 v[92:95], v[252:255], v[124:127], v[76:79]\n\t"
    "s_add_u32 s38, s38, %[ab]\n\t"
    "s_addc_u32 s39, s39, 0\n\t"
    "s_xor_b32 %[l0], %[l0], 0x10000\n\t"
    "s_sub_u32 m0, %[lf], %[b]\n\t"
    "s_sub_u32 m0, m0, 1\n\t"
    "s_cmp_lt_u32 m0, %[up]\n\t"
    "s_cbranch_scc1 1b\n\t"
    "s_waitcnt lgkmcnt(0)\n\t"
    "s_nop 4"
    : "=&{v[80:95]}"(pd), [ea] "+&v"(ea), [da] "+&v"(da), [b] "+&s"(b), [lf] "+&s"(lf), "+{s[36:37]}"(p1), "+{s[38:39]}"(p0), [sa] "+&v"(sa), [l1] "+&s"(l1), [l0] "+&s"(l0)
    : [rw] "v"(rw), [ab] "s"(ab), [cp] "s"(cp), [mu] "s"(mu), [ad] "v"(ad), [up] "s"(up), "{v[128:159]}"(W[0]), "{v[160:191]}"(W[1]), "{v[192:223]}"(W[2]), "{v[224:255]}"(W[3]), [av0] "v"(av[0]), [av1] "v"(av[1]), [av2] "v"(av[2]), [av3] "v"(av[3]), [av4] "v"(av[4]), [av5] "v"(av[5]), [av6] "v"(av[6]), [av7] "v"(av[7]), [la] "s"(la)
    : "v39", "v64", "v65", "v66", "v67", "v68", "v69", "v70", "v71", "v72", "v73", "v74", "v75", "v76", "v77", "v78", "v79", "v96", "v97", "v98", "v99", "v100", "v101", "v102", "v103", "v104", "v105", "v106", "v107", "v108", "v109", "v110", "v111", "v112", "v113", "v114", "v115", "v116", "v117", "v118", "v119", "v120", "v121", "v122", "v123", "v124", "v125", "v126", "v127", "vcc", "scc", "m0", "memory");
}
__device__ __forceinline__ void fast_ld1(const u32x32 (&W)[4], u32x16& pd, unsigned ea, unsigned da, int& b, int& lf, int cp, unsigned up, unsigned sa, unsigned mu, unsigned ad,
    unsigned rw, unsigned ab, const char* p1, const char* p0, unsigned l1, unsigned l0, const unsigned (&av)[8], int la) {
  asm volatile("s_setprio 0\n\t"
    "s_cmp_eq_u32 %[la], 3\n\t"
    "s_waitcnt vmcnt(12)\n\t"
    "s_cbranch_scc1 20f\n\t"
    "s_waitcnt vmcnt(4)\n\t"
    "20:\n\t"
    "s_barrier\n\t"
    "ds_read_b128 v[96:99], %[ea] offset:0\n\t"
    "ds_read_b128 v[100:103], %[ea] offset:2048\n\t"
    "ds_read_b128 v[104:107], %[da] offset:0\n\t"
    "ds_read_b128 v[108:111], %[da] offset:2048\n\t"
    "ds_read_b128 v[112:115], %[ea] offset:4096\n\t"
    "ds_read_b128 v[116:119], %[ea] offset:6144\n\t"
    "s_branch 2f\n\t"
    ".p2align 6\n\t"
    "1:\n\t"
    "s_cmp_eq_u32 %[la], 3\n\t"
    "s_waitcnt vmcnt(12)\n\t"
    "s_cbranch_scc1 21f\n\t"
    "s_waitcnt vmcnt(4)\n\t"
    "21:\n\t"
    "s_barrier\n\t"
    "2:\n\t"
    "s_waitcnt lgkmcnt(4)\n\t"
    "v_mfma_f32_16x16x32_bf16 v[64:67], v[128:131], v[96:99], 0\n\t"
    "ds_read_b128 v[120:123], %[da] offset:4096\n\t"
    "s_add_u32 m0, %[l1], 0\n\t"
    "ds_write_b128 %[rw], v[80:83] offset:0\n\t"
    "v_mfma_f32_16x16x32_bf16 v[68:71], v[132:135], v[96:99], 0\n\t"
    "ds_read_b128 v[124:127], %[da] offset:6144\n\t"
    "global_load_lds_dwordx4 %[av0], s[36:37] sc1\n\t"
    "ds_write_b128 %[rw], v[84:87] offset:1024\n\t"
    "v_mfma_f32_16x16x32_bf16 v[72:75], v[128:131], v[100:103], 0\n\t"
    "s_add_u32 m0, %[l1], 32768\n\t"
    "ds_write_b128 %[rw], v[88:91] offset:2048\n\t"
    "v_mfma_f32_16x16x32_bf16 v[76:79], v[132:135], v[100:103], 0\n\t"
    "global_load_lds_dwordx4 %[av1], s[36:37] sc1\n\t"
    "ds_write_b128 %[rw], v[92:95] offset:3072\n\t"
    "s_waitcnt lgkmcnt(8)\n\t"
    "v_mfma_f32_16x16x32_bf16 v[64:67], v[136:139], v[104:107], v[64:67]\n\t"
    "ds_read_b128 v[96:99], %[ea] offset:8192\n\t"
    "s_add_u32 m0, %[l1], 4096\n\t"
    "v_mfma_f32_16x16x32_bf16 v[68:71], v[140:143], v[104:107], v[68:71]\n\t"
    "ds_read_b128 v[100:103], %[ea] offset:10240\n\t"
    "global_load_lds_dwordx4 %[av2], s[36:37] sc1\n\t"
    "v_mfma_f32_16x16x32_bf16 v[72:75], v[136:139], v[108:111], v[72:75]\n\t"
    "s_add_u32 m0, %[l1], 36864\n\t"
    "v_mfma_f32_16x16x32_bf16 v[76:79], v[140:143], v[108:111], v[76:79]\n\t"
    "global_load_lds_dwordx4 %[av3], s[36:37] sc1\n\t"
    "s_waitcnt lgkmcnt(8)\n\t"
    "v_mfma_f32_16x16x32_bf16 v[64:67], v[144:147], v[112:115], v[64:67]\n\t"
    "ds_read_b128 v[104:107], %[da] offset:8192\n\t"
    "s_add_u32 m0, %[l1], 8192\n\t"
    "v_mfma_f32_16x16x32_bf16 v[68:71], v[148:151], v[112:115], v[68:71]\n\t"
    "ds_read_b128 v[108:111], %[da] offset:10240\n\t"
    "global_load_lds_dwordx4 %[av4], s[36:37] sc1\n\t"
    "v_mfma_f32_16x16x32_bf16 v[72:75], v[144:147], v[116:119], v[72:75]\n\t"
    "s_add_u32 m0, %[l1], 40960\n\t"
    "v_mfma_f32_16x16x32_bf16 v[76:79], v[148:151], v[116:119], v[76:79]\n\t"
    "global_load_lds_dwordx4 %[av5], s[36:37] sc1\n\t"
    "s_waitcnt lgkmcnt(7)\n\t"
    "v_mfma_f32_16x16x32_bf16 v[64:67], v[152:155], v[120:123], v[64:67]\n\t"
    "ds_read_b128 v[112:115], %[ea] offset:12288\n\t"
    "s_add_u32 m0, %[l1], 12288\n\t"
    "v_mfma_f32_16x16x32_bf16 v[68:71], v[156:159], v[120:123], v[68:71]\n\t"
    "ds_read_b128 v[116:119], %[ea] offset:14336\n\t"
    "global_load_lds_dwordx4 %[av6], s[36:37] sc1\n\t"
    "v_mfma_f32_16x16x32_bf16 v[72:75], v[152:155], v[124:127], v[72:75]\n\t"
    "s_add_u32 m0, %[l1], 45056\n\t"
    "v_mfma_f32_16x16x32_bf16 v[76:79], v[156:159], v[124:127], v[76:79]\n\t"
    "global_load_lds_dwordx4 %[av7], s[36:37] sc1\n\t"
    "s_waitcnt lgkmcnt(4)\n\t"
    "v_mfma_f32_16x16x32_bf16 v[64:67], v[160:163], v[96:99], v[64:67]\n\t"
    "ds_read_b128 v[120:123], %[da] offset:12288\n\t"
    "v_mfma_f32_16x16x32_bf16 v[68:71], v[164:167], v[96:99], v[68:71]\n\t"
    "ds_read_b128 v[124:127], %[da] offset:14336\n\t"
    "v_mfma_f32_16x16x32_bf16 v[72:75], v[160:163], v[100:103], v[72:75]\n\t"
    "v_mfma_f32_16x16x32_bf16 v[76:79], v[164:167], v[100:103], v[76:79]\n\t"
    "s_waitcnt lgkmcnt(4)\n\t"
    "v_mfma_f32_16x16x32_bf16 v[64:67], v[168:171], v[104:107], v[64:67]\n\t"
    "ds_read_b128 v[96:99], %[ea] offset:16384\n\t"
    "v_mfma_f32_16x16x32_bf16 v[68:71], v[172:175], v[104:107], v[68:71]\n\t"
    "ds_read_b128 v[100:103], %[ea] offset:18432\n\t"
    "v_mfma_f32_16x16x32_bf16 v[72:75], v[168:171], v[108:111], v[72:75]\n\t"
    "v_mfma_f32_16x16x32_bf16 v[76:79], v[172:175], v[108:111], v[76:79]\n\t"
    "s_waitcnt lgkmcnt(4)\n\t"
    "v_mfma_f32_16x16x32_bf16 v[64:67], v[176:179], v[112:115], v[64:67]\n\t"
    "ds_read_b128 v[104:107], %[da] offset:16384\n\t"
    "v_mfma_f32_16x16x32_bf16 v[68:71], v[180:183], v[112:115], v[68:71]\n\t"
    "ds_read_b128 v[108:111], %[da] offset:18432\n\t"
    "v_mfma_f32_16x16x32_bf16 v[72:75], v[176:179], v[116:119], v[72:75]\n\t"
    "v_mfma_f32_16x16x32_bf16 v[76:79], v[180:183], v[116:119], v[76:79]\n\t"
    "s_waitcnt lgkmcnt(4)\n\t"
    "v_mfma_f32_16x16x32_bf16 v[64:67], v[184:187], v[120:123], v[64:67]\n\t"
    "ds_read_b128 v[112:115], %[ea] offset:20480\n\t"
    "v_mfma_f32_16x16x32_bf16 v[68:71], v[188:191], v[120:123], v[68:71]\n\t"
    "ds_read_b128 v[116:119], %[ea] offset:22528\n\t"
    "v_mfma_f32_16x16x32_bf16 v[72:75], v[184:187], v[124:127], v[72:75]\n\t"
    "v_mfma_f32_16x16x32_bf16 v[76:79], v[188:191], v[124:127], v[76:79]\n\t"
    "s_cmp_eq_u32 %[la], 3\n\t"
    "s_waitcnt vmcnt(12)\n\t"
    "s_cbranch_scc1 22f\n\t"
    "s_waitcnt vmcnt(4)\n\t"
    "22:\n\t"
    "s_barrier\n\t"
    "s_waitcnt lgkmcnt(4)\n\t"
    "v_mfma_f32_16x16x32_bf16 v[64:67], v[192:195], v[96:99], v[64:67]\n\t"
    "ds_read_b128 v[120:123], %[da] offset:20480\n\t"
    "s_add_u32 m0, %[l0], 0\n\t"
    "v_mfma_f32_16x16x32_bf16 v[68:71], v[196:199], v[96:99], v[68:71]\n\t"
    "ds_read_b128 v[124:127], %[da] offset:22528\n\t"
    "global_load_lds_dwordx4 %[av0], s[38:39] sc1\n\t"
    "v_mfma_f32_16x16x32_bf16 v[72:75], v[192:195], v[100:103], v[72:75]\n\t"
    "s_add_u32 m0, %[l0], 32768\n\t"
    "v_mfma_f32_16x16x32_bf16 v[76:79], v[196:199], v[100:103], v[76:79]\n\t"
    "global_load_lds_dwordx4 %[av1], s[38:39] sc1\n\t"
    "s_waitcnt lgkmcnt(4)\n\t"
    "v_mfma_f32_16x16x32_bf16 v[64:67], v[200:203], v[104:107], v[64:67]\n\t"
    "ds_read_b128 v[96:99], %[ea] offset:24576\n\t"
    "s_add_u32 m0, %[l0], 4096\n\t"
    "v_mfma_f32_16x16x32_bf16 v[68:71], v[204:207], v[104:107], v[68:71]\n\t"
    "ds_read_b128 v[100:103], %[ea] offset:26624\n\t"
    "global_load_lds_dwordx4 %[av2], s[38:39] sc1\n\t"
    "v_mfma_f32_16x16x32_bf16 v[72:75], v[200:203], v[108:111], v[72:75]\n\t"
    "s_add_u32 m0, %[l0], 36864\n\t"
    "v_mfma_f32_16x16x32_bf16 v[76:79], v[204:207], v[108:111], v[76:79]\n\t"
    "global_load_lds_dwordx4 %[av3], s[38:39] sc1\n\t"
    "s_add_u32 s36, s36, %[ab]\n\t"
    "s_addc_u32 s37, s37, 0\n\t"
    "s_waitcnt lgkmcnt(4)\n\t"
    "v_mfma_f32_16x16x32_bf16 v[64:67], v[208:211], v[112:115], v[64:67]\n\t"
    "ds_read_b128 v[104:107], %[da] offset:24576\n\t"
    "s_add_u32 m0, %[l0], 8192\n\t"
    "v_mfma_f32_16x16x32_bf16 v[68:71], v[212:215], v[112:115], v[68:71]\n\t"
    "ds_read_b128 v[108:111], %[da] offset:26624\n\t"
    "global_load_lds_dwordx4 %[av4], s[38:39] sc1\n\t"
    "v_mfma_f32_16x16x32_bf16 v[72:75], v[208:211], v[116:119], v[72:75]\n\t"
    "ds_read_b32 v39, %[sa]\n\t"
    "s_add_u32 m0, %[l0], 40960\n\t"
    "v_mfma_f32_16x16x32_bf16 v[76:79], v[212:215], v[116:119], v[76:79]\n\t"
    "global_load_lds_dwordx4 %[av5], s[38:39] sc1\n\t"
    "s_xor_b32 %[l1], %[l1], 0x10000\n\t"
    "s_waitcnt lgkmcnt(5)\n\t"
    "v_mfma_f32_16x16x32_bf16 v[64:67], v[216:219], v[120:123], v[64:67]\n\t"
    "ds_read_b128 v[112:115], %[ea] offset:28672\n\t"
    "s_add_u32 m0, %[l0], 12288\n\t"
    "v_mfma_f32_16x16x32_bf16 v[68:71], v[220:223], v[120:123], v[68:71]\n\t"
    "ds_read_b128 v[116:119], %[ea] offset:30720\n\t"
    "global_load_lds_dwordx4 %[av6], s[38:39] sc1\n\t"
    "v_mfma_f32_16x16x32_bf16 v[72:75], v[216:219], v[124:127], v[72:75]\n\t"
    "v_xor_b32 %[ea], 0x10000, %[ea]\n\t"
    "s_add_u32 m0, %[l0], 45056\n\t"
    "v_mfma_f32_16x16x32_bf16 v[76:79], v[220:223], v[124:127], v[76:79]\n\t"
    "global_load_lds_dwordx4 %[av7], s[38:39] sc1\n\t"
    "s_waitcnt lgkmcnt(5)\n\t"
    "v_mfma_f32_16x16x32_bf16 v[64:67], v[224:227], v[96:99], v[64:67]\n\t"
    "ds_read_b128 v[120:123], %[da] offset:28672\n\t"
    "v_mfma_f32_16x16x32_bf16 v[68:71], v[228:231], v[96:99], v[68:71]\n\t"
    "ds_read_b128 v[124:127], %[da] offset:30720\n\t"
    "v_mfma_f32_16x16x32_bf16 v[72:75], v[224:227], v[100:103], v[72:75]\n\t"
    "v_xor_b32 %[da], 0x10000, %[da]\n\t"
    "v_mfma_f32_16x16x32_bf16 v[76:79], v[228:231], v[100:103], v[76:79]\n\t"
    "s_waitcnt lgkmcnt(5)\n\t"
    "v_mfma_f32_16x16x32_bf16 v[64:67], v[232:235], v[104:107], v[64:67]\n\t"
    "ds_read_b128 v[96:99], %[ea] offset:0\n\t"
    "v_mfma_f32_16x16x32_bf16 v[68:71], v[236:239], v[104:107], v[68:71]\n\t"
    "ds_read_b128 v[100:103], %[ea] offset:2048\n\t"
    "v_mfma_f32_16x16x32_bf16 v[72:75], v[232:235], v[108:111], v[72:75]\n\t"
    "v_mfma_f32_16x16x32_bf16 v[76:79], v[236:239], v[108:111], v[76:79]\n\t"
    "s_waitcnt lgkmcnt(4)\n\t"
    "v_mfma_f32_16x16x32_bf16 v[64:67], v[240:243], v[112:115], v[64:67]\n\t"
    "ds_read_b128 v[104:107], %[da] offset:0\n\t"
    "v_mad_u32_u24 v39, v39, %[mu], %[ad]\n\t"
    "v_mfma_f32_16x16x32_bf16 v[68:71], v[244:247], v[112:115], v[68:71]\n\t"
    "ds_read_b128 v[108:111], %[da] offset:2048\n\t"
    "v_cmp_ge_i32 vcc, %[lf], v39\n\t"
    "s_cmp_eq_u64 vcc, 0\n\t"
    "s_addc_u32 %[lf], %[lf], 0\n\t"
    "v_mfma_f32_16x16x32_bf16 v[72:75], v[240:243], v[116:119], v[72:75]\n\t"
    "v_mfma_f32_16x16x32_bf16 v[76:79], v[244:247], v[116:119], v[76:79]\n\t"
    "v_cmp_ge_i32 vcc, %[lf], v39\n\t"
    "s_cmp_eq_u64 vcc, 0\n\t"
    "s_addc_u32 %[lf], %[lf], 0\n\t"
    "s_waitcnt lgkmcnt(4)\n\t"
    "v_mfma_f32_16x16x32_bf16 v[80:83], v[248:251], v[120:123], v[64:67]\n\t"
    "ds_read_b128 v[112:115], %[ea] offset:4096\n\t"
    "s_min_i32 %[lf], %[lf], %[cp]\n\t"
    "s_add_u32 %[b], %[b], 1\n\t"
    "v_mfma_f32_16x16x32_bf16 v[84:87], v[252:255], v[120:123], v[68:71]\n\t"
    "ds_read_b128 v[116:119], %[ea] offset:6144\n\t"
    "v_mfma_f32_16x16x32_bf16 v[88:91], v[248:251], v[124:127], v[72:75]\n\t"
    "v_mfma_f32_16x16x32_bf16 v[92:95], v[252:255], v[124:127], v[76:79]\n\t"
    "s_add_u32 s38, s38, %[ab]\n\t"
    "s_addc_u32 s39, s39, 0\n\t"
    "s_xor_b32 %[l0], %[l0], 0x10000\n\t"
    "s_sub_u32 m0, %[lf], %[b]\n\t"
    "s_sub_u32 m0, m0, 1\n\t"
    "s_cmp_lt_u32 m0, %[up]\n\t"
    "s_cbranch_scc1 1b\n\t"
    "s_waitcnt lgkmcnt(0)\n\t"
    "s_nop 4"
    : "=&{v[80:95]}"(pd), [ea] "+&v"(ea), [da] "+&v"(da), [b] "+&s"(b), [lf] "+&s"(lf), "+{s[36:37]}"(p1), "+{s[38:39]}"(p0), [sa] "+&v"(sa), [l1] "+&s"(l1), [l0] "+&s"(l0)
    : [rw] "v"(rw), [ab] "s"(ab), [cp] "s"(cp), [mu] "s"(mu), [ad] "v"(ad), [up] "s"(up), "{v[128:159]}"(W[0]), "{v[160:191]}"(W[1]), "{v[192:223]}"(W[2]), "{v[224:255]}"(W[3]), [av0] "v"(av[0]), [av1] "v"(av[1]), [av2] "v"(av[2]), [av3] "v"(av[3]), [av4] "v"(av[4]), [av5] "v"(av[5]), [av6] "v"(av[6]), [av7] "v"(av[7]), [la] "s"(la)
    : "v39", "v64", "v65", "v66", "v67", "v68", "v69", "v70", "v71", "v72", "v73", "v74", "v75", "v76", "v77", "v78", "v79", "v96", "v97", "v98", "v99", "v100", "v101", "v102", "v103", "v104", "v105", "v106", "v107", "v108", "v109", "v110", "v111", "v112", "v113", "v114", "v115", "v116", "v117", "v118", "v119", "v120", "v121", "v122", "v123", "v124", "v125", "v126", "v127", "vcc", "scc", "m0", "memory");
}
__device__ __forceinline__ void fast_ld2(const u32x32 (&W)[4], u32x16& pd, unsigned ea, unsigned da, int& b, int& lf, int cp, unsigned up, unsigned sa, unsigned mu, unsigned ad,
    unsigned rw, unsigned ab, const char* p1, const char* p0, unsigned l1, unsigned l0, const unsigned (&av)[8], int la) {
  asm volatile("s_setprio 0\n\t"
    "s_cmp_eq_u32 %[la], 3\n\t"
    "s_waitcnt vmcnt(6)\n\t"
    "s_cbranch_scc1 20f\n\t"
    "s_waitcnt vmcnt(2)\n\t"
    "20:\n\t"
    "s_barrier\n\t"
    "ds_read_b128 v[96:99], %[ea] offset:0\n\t"
    "ds_read_b128 v[100:103], %[ea] offset:2048\n\t"
    "ds_read_b128 v[104:107], %[da] offset:0\n\t"
    "ds_read_b128 v[108:111], %[da] offset:2048\n\t"
    "ds_read_b128 v[112:115], %[ea] offset:4096\n\t"
    "ds_read_b128 v[116:119], %[ea] offset:6144\n\t"
    "s_branch 2f\n\t"
    ".p2align 6\n\t"
    "1:\n\t"
    "s_cmp_eq_u32 %[la], 3\n\t"
    "s_waitcnt vmcnt(6)\n\t"
    "s_cbranch_scc1 21f\n\t"
    "s_waitcnt vmcnt(2)\n\t"
    "21:\n\t"
    "s_barrier\n\t"
    "2:\n\t"
    "s_waitcnt lgkmcnt(4)\n\t"
    "v_mfma_f32_16x16x32_bf16 v[64:67], v[128:131], v[96:99], 0\n\t"
    "ds_read_b128 v[120:123], %[da] offset:4096\n\t"
    "s_add_u32 m0, %[l1], 0\n\t"
    "ds_write_b128 %[rw], v[80:83] offset:0\n\t"
    "v_mfma_f32_16x16x32_bf16 v[68:71], v[132:135], v[96:99], 0\n\t"
    "ds_read_b128 v[124:127], %[da] offset:6144\n\t"
    "global_load_lds_dwordx4 %[av0], s[36:37] sc1\n\t"
    "ds_write_b128 %[rw], v[84:87] offset:1024\n\t"
    "v_mfma_f32_16x16x32_bf16 v[72:75], v[128:131], v[100:103], 0\n\t"
    "ds_write_b128 %[rw], v[88:91] offset:2048\n\t"
    "v_mfma_f32_16x16x32_bf16 v[76:79], v[132:135], v[100:103], 0\n\t"
    "ds_write_b128 %[rw], v[92:95] offset:3072\n\t"
    "s_waitcnt lgkmcnt(8)\n\t"
    "v_mfma_f32_16x16x32_bf16 v[64:67], v[136:139], v[104:107], v[64:67]\n\t"
    "ds_read_b128 v[96:99], %[ea] offset:8192\n\t"
    "s_add_u32 m0, %[l1], 4096\n\t"
    "v_mfma_f32_16x16x32_bf16 v[68:71], v[140:143], v[104:107], v[68:71]\n\t"
    "ds_read_b128 v[100:103], %[ea] offset:10240\n\t"
    "global_load_lds_dwordx4 %[av2], s[36:37] sc1\n\t"
    "v_mfma_f32_16x16x32_bf16 v[72:75], v[136:139], v[108:111], v[72:75]\n\t"
    "v_mfma_f32_16x16x32_bf16 v[76:79], v[140:143], v[108:111], v[76:79]\n\t"
    "s_waitcnt lgkmcnt(8)\n\t"
    "v_mfma_f32_16x16x32_bf16 v[64:67], v[144:147], v[112:115], v[64:67]\n\t"
    "ds_read_b128 v[104:107], %[da] offset:8192\n\t"
    "s_add_u32 m0, %[l1], 8192\n\t"
    "v_mfma_f32_16x16x32_bf16 v[68:71], v[148:151], v[112:115], v[68:71]\n\t"
    "ds_read_b128 v[108:111], %[da] offset:10240\n\t"
    "global_load_lds_dwordx4 %[av4], s[36:37] sc1\n\t"
    "v_mfma_f32_16x16x32_bf16 v[72:75], v[144:147], v[116:119], v[72:75]\n\t"
    "v_mfma_f32_16x16x32_bf16 v[76:79], v[148:151], v[116:119], v[76:79]\n\t"
    "s_waitcnt lgkmcnt(7)\n\t"
    "v_mfma_f32_16x16x32_bf16 v[64:67], v[152:155], v[120:123], v[64:67]\n\t"
    "ds_read_b128 v[112:115], %[ea] offset:12288\n\t"
    "s_add_u32 m0, %[l1], 12288\n\t"
    "v_mfma_f32_16x16x32_bf16 v[68:71], v[156:159], v[120:123], v[68:71]\n\t"
    "ds_read_b128 v[116:119], %[ea] offset:14336\n\t"
    "global_load_lds_dwordx4 %[av6], s[36:37] sc1\n\t"
    "v_mfma_f32_16x16x32_bf16 v[72:75], v[152:155], v[124:127], v[72:75]\n\t"
    "v_mfma_f32_16x16x32_bf16 v[76:79], v[156:159], v[124:127], v[76:79]\n\t"
    "s_waitcnt lgkmcnt(4)\n\t"
    "v_mfma_f32_16x16x32_bf16 v[64:67], v[160:163], v[96:99], v[64:67]\n\t"
    "ds_read_b128 v[120:123], %[da] offset:12288\n\t"
    "v_mfma_f32_16x16x32_bf16 v[68:71], v[164:167], v[96:99], v[68:71]\n\t"
    "ds_read_b128 v[124:127], %[da] offset:14336\n\t"
    "v_mfma_f32_16x16x32_bf16 v[72:75], v[160:163], v[100:103], v[72:75]\n\t"
    "v_mfma_f32_16x16x32_bf16 v[76:79], v[164:167], v[100:103], v[76:79]\n\t"
    "s_waitcnt lgkmcnt(4)\n\t"
    "v_mfma_f32_16x16x32_bf16 v[64:67], v[168:171], v[104:107], v[64:67]\n\t"
    "ds_read_b128 v[96:99], %[ea] offset:16384\n\t"
    "v_mfma_f32_16x16x32_bf16 v[68:71], v[172:175], v[104:107], v[68:71]\n\t"
    "ds_read_b128 v[100:103], %[ea] offset:18432\n\t"
    "v_mfma_f32_16x16x32_bf16 v[72:75], v[168:171], v[108:111], v[72:75]\n\t"
    "v_mfma_f32_16x16x32_bf16 v[76:79], v[172:175], v[108:111], v[76:79]\n\t"
    "s_waitcnt lgkmcnt(4)\n\t"
    "v_mfma_f32_16x16x32_bf16 v[64:67], v[176:179], v[112:115], v[64:67]\n\t"
    "ds_read_b128 v[104:107], %[da] offset:16384\n\t"
    "v_mfma_f32_16x16x32_bf16 v[68:71], v[180:183], v[112:115], v[68:71]\n\t"
    "ds_read_b128 v[108:111], %[da] offset:18432\n\t"
    "v_mfma_f32_16x16x32_bf16 v[72:75], v[176:179], v[116:119], v[72:75]\n\t"
    "v_mfma_f32_16x16x32_bf16 v[76:79], v[180:183], v[116:119], v[76:79]\n\t"
    "s_waitcnt lgkmcnt(4)\n\t"
    "v_mfma_f32_16x16x32_bf16 v[64:67], v[184:187], v[120:123], v[64:67]\n\t"
    "ds_read_b128 v[112:115], %[ea] offset:20480\n\t"
    "v_mfma_f32_16x16x32_bf16 v[68:71], v[188:191], v[120:123], v[68:71]\n\t"
    "ds_read_b128 v[116:119], %[ea] offset:22528\n\t"
    "v_mfma_f32_16x16x32_bf16 v[72:75], v[184:187], v[124:127], v[72:75]\n\t"
    "v_mfma_f32_16x16x32_bf16 v[76:79], v[188:191], v[124:127], v[76:79]\n\t"
    "s_cmp_eq_u32 %[la], 3\n\t"
    "s_waitcnt vmcnt(6)\n\t"
    "s_cbranch_scc1 22f\n\t"
    "s_waitcnt vmcnt(2)\n\t"
    "22:\n\t"
    "s_barrier\n\t"
    "s_waitcnt lgkmcnt(4)\n\t"
    "v_mfma_f32_16x16x32_bf16 v[64:67], v[192:195], v[96:99], v[64:67]\n\t"
    "ds_read_b128 v[120:123], %[da] offset:20480\n\t"
    "s_add_u32 m0, %[l0], 0\n\t"
    "v_mfma_f32_16x16x32_bf16 v[68:71], v[196:199], v[96:99], v[68:71]\n\t"
    "ds_read_b128 v[124:127], %[da] offset:22528\n\t"
    "global_load_lds_dwordx4 %[av0], s[38:39] sc1\n\t"
    "v_mfma_f32_16x16x32_bf16 v[72:75], v[192:195], v[100:103], v[72:75]\n\t"
    "v_mfma_f32_16x16x32_bf16 v[76:79], v[196:199], v[100:103], v[76:79]\n\t"
    "s_waitcnt lgkmcnt(4)\n\t"
    "v_mfma_f32_16x16x32_bf16 v[64:67], v[200:203], v[104:107], v[64:67]\n\t"
    "ds_read_b128 v[96:99], %[ea] offset:24576\n\t"
    "s_add_u32 m0, %[l0], 4096\n\t"
    "v_mfma_f32_16x16x32_bf16 v[68:71], v[204:207], v[104:107], v[68:71]\n\t"
    "ds_read_b128 v[100:103], %[ea] offset:26624\n\t"
    "global_load_lds_dwordx4 %[av2], s[38:39] sc1\n\t"
    "v_mfma_f32_16x16x32_bf16 v[72:75], v[200:203], v[108:111], v[72:75]\n\t"
    "v_mfma_f32_16x16x32_bf16 v[76:79], v[204:207], v[108:111], v[76:79]\n\t"
    "s_add_u32 s36, s36, %[ab]\n\t"
    "s_addc_u32 s37, s37, 0\n\t"
    "s_waitcnt lgkmcnt(4)\n\t"
    "v_mfma_f32_16x16x32_bf16 v[64:67], v[208:211], v[112:115], v[64:67]\n\t"
    "ds_read_b128 v[104:107], %[da] offset:24576\n\t"
    "s_add_u32 m0, %[l0], 8192\n\t"
    "v_mfma_f32_16x16x32_bf16 v[68:71], v[212:215], v[112:115], v[68:71]\n\t"
    "ds_read_b128 v[108:111], %[da] offset:26624\n\t"
    "global_load_lds_dwordx4 %[av4], s[38:39] sc1\n\t"
    "v_mfma_f32_16x16x32_bf16 v[72:75], v[208:211], v[116:119], v[72:75]\n\t"
    "ds_read_b32 v39, %[sa]\n\t"
    "v_mfma_f32_16x16x32_bf16 v[76:79], v[212:215], v[116:119], v[76:79]\n\t"
    "s_xor_b32 %[l1], %[l1], 0x10000\n\t"
    "s_waitcnt lgkmcnt(5)\n\t"
    "v_mfma_f32_16x16x32_bf16 v[64:67], v[216:219], v[120:123], v[64:67]\n\t"
    "ds_read_b128 v[112:115], %[ea] offset:28672\n\t"
    "s_add_u32 m0, %[l0], 12288\n\t"
    "v_mfma_f32_16x16x32_bf16 v[68:71], v[220:223], v[120:123], v[68:71]\n\t"
    "ds_read_b128 v[116:119], %[ea] offset:30720\n\t"
    "global_load_lds_dwordx4 %[av6], s[38:39] sc1\n\t"
    "v_mfma_f32_16x16x32_bf16 v[72:75], v[216:219], v[124:127], v[72:75]\n\t"
    "v_xor_b32 %[ea], 0x10000, %[ea]\n\t"
    "v_mfma_f32_16x16x32_bf16 v[76:79], v[220:223], v[124:127], v[76:79]\n\t"
    "s_waitcnt lgkmcnt(5)\n\t"
    "v_mfma_f32_16x16x32_bf16 v[64:67], v[224:227], v[96:99], v[64:67]\n\t"
    "ds_read_b128 v[120:123], %[da] offset:28672\n\t"
    "v_mfma_f32_16x16x32_bf16 v[68:71], v[228:231], v[96:99], v[68:71]\n\t"
    "ds_read_b128 v[124:127], %[da] offset:30720\n\t"
    "v_mfma_f32_16x16x32_bf16 v[72:75], v[224:227], v[100:103], v[72:75]\n\t"
    "v_xor_b32 %[da], 0x10000, %[da]\n\t"
    "v_mfma_f32_16x16x32_bf16 v[76:79], v[228:231], v[100:103], v[76:79]\n\t"
    "s_waitcnt lgkmcnt(5)\n\t"
    "v_mfma_f32_16x16x32_bf16 v[64:67], v[232:235], v[104:107], v[64:67]\n\t"
    "ds_read_b128 v[96:99], %[ea] offset:0\n\t"
    "v_mfma_f32_16x16x32_bf16 v[68:71], v[236:239], v[104:107], v[68:71]\n\t"
    "ds_read_b128 v[100:103], %[ea] offset:2048\n\t"
    "v_mfma_f32_16x16x32_bf16 v[72:75], v[232:235], v[108:111], v[72:75]\n\t"
    "v_mfma_f32_16x16x32_bf16 v[76:79], v[236:239], v[108:111], v[76:79]\n\t"
    "s_waitcnt lgkmcnt(4)\n\t"
    "v_mfma_f32_16x16x32_bf16 v[64:67], v[240:243], v[112:115], v[64:67]\n\t"
    "ds_read_b128 v[104:107], %[da] offset:0\n\t"
    "v_mad_u32_u24 v39, v39, %[mu], %[ad]\n\t"
    "v_mfma_f32_16x16x32_bf16 v[68:71], v[244:247], v[112:115], v[68:71]\n\t"
    "ds_read_b128 v[108:111], %[da] offset:2048\n\t"
    "v_cmp_ge_i32 vcc, %[lf], v39\n\t"
    "s_cmp_eq_u64 vcc, 0\n\t"
    "s_addc_u32 %[lf], %[lf], 0\n\t"
    "v_mfma_f32_16x16x32_bf16 v[72:75], v[240:243], v[116:119], v[72:75]\n\t"
    "v_mfma_f32_16x16x32_bf16 v[76:79], v[244:247], v[116:119], v[76:79]\n\t"
    "v_cmp_ge_i32 vcc, %[lf], v39\n\t"
    "s_cmp_eq_u64 vcc, 0\n\t"
    "s_addc_u32 %[lf], %[lf], 0\n\t"
    "s_waitcnt lgkmcnt(4)\n\t"
    "v_mfma_f32_16x16x32_bf16 v[80:83], v[248:251], v[120:123], v[64:67]\n\t"
    "ds_read_b128 v[112:115], %[ea] offset:4096\n\t"
    "s_min_i32 %[lf], %[lf], %[cp]\n\t"
    "s_add_u32 %[b], %[b], 1\n\t"
    "v_mfma_f32_16x16x32_bf16 v[84:87], v[252:255], v[120:123], v[68:71]\n\t"
    "ds_read_b128 v[116:119], %[ea] offset:6144\n\t"
    "v_mfma_f32_16x16x32_bf16 v[88:91], v[248:251], v[124:127], v[72:75]\n\t"
    "v_mfma_f32_16x16x32_bf16 v[92:95], v[252:255], v[124:127], v[76:79]\n\t"
    "s_add_u32 s38, s38, %[ab]\n\t"
    "s_addc_u32 s39, s39, 0\n\t"
    "s_xor_b32 %[l0], %[l0], 0x10000\n\t"
    "s_sub_u32 m0, %[lf], %[b]\n\t"
    "s_sub_u32 m0, m0, 1\n\t"
    "s_cmp_lt_u32 m0, %[up]\n\t"
    "s_cbranch_scc1 1b\n\t"
    "s_waitcnt lgkmcnt(0)\n\t"
    "s_nop 4"
    : "=&{v[80:95]}"(pd), [ea] "+&v"(ea), [da] "+&v"(da), [b] "+&s"(b), [lf] "+&s"(lf), "+{s[36:37]}"(p1), "+{s[38:39]}"(p0), [sa] "+&v"(sa), [l1] "+&s"(l1), [l0] "+&s"(l0)
    : [rw] "v"(rw), [ab] "s"(ab), [cp] "s"(cp), [mu] "s"(mu), [ad] "v"(ad), [up] "s"(up), "{v[128:159]}"(W[0]), "{v[160:191]}"(W[1]), "{v[192:223]}"(W[2]), "{v[224:255]}"(W[3]), [av0] "v"(av[0]), [av1] "v"(av[1]), [av2] "v"(av[2]), [av3] "v"(av[3]), [av4] "v"(av[4]), [av5] "v"(av[5]), [av6] "v"(av[6]), [av7] "v"(av[7]), [la] "s"(la)
    : "v39", "v64", "v65", "v66", "v67", "v68", "v69", "v70", "v71", "v72", "v73", "v74", "v75", "v76", "v77", "v78", "v79", "v96", "v97", "v98", "v99", "v100", "v101", "v102", "v103", "v104", "v105", "v106", "v107", "v108", "v109", "v110", "v111", "v112", "v113", "v114", "v115", "v116", "v117", "v118", "v119", "v120", "v121", "v122", "v123", "v124", "v125", "v126", "v127", "vcc", "scc", "m0", "memory");
}
__device__ __forceinline__ void fast_st0(const u32x32 (&W)[4], u32x16& pd, unsigned ea, unsigned da, int& b, int& lf, int cp, unsigned up, unsigned sa, unsigned mu, unsigned ad,
    unsigned rw, unsigned ab, const char* p1, const char* p0, unsigned co0, unsigned co1, int po, int pf, unsigned w0, unsigned sl, unsigned dv, const char* db, const char* pg) {
  unsigned st;
  asm volatile("s_barrier\n\t"
    "ds_read_b128 v[96:99], %[ea] offset:0\n\t"
    "ds_read_b128 v[100:103], %[ea] offset:2048\n\t"
    "ds_read_b128 v[104:107], %[da] offset:0\n\t"
    "ds_read_b128 v[108:111], %[da] offset:2048\n\t"
    "ds_read_b128 v[112:115], %[ea] offset:4096\n\t"
    "ds_read_b128 v[116:119], %[ea] offset:6144\n\t"
    "s_branch 2f\n\t"
    ".p2align 6\n\t"
    "1:\n\t"
    "s_barrier\n\t"
    "2:\n\t"
    "s_waitcnt lgkmcnt(4)\n\t"
    "v_mfma_f32_16x16x32_bf16 v[64:67], v[128:131], v[96:99], 0\n\t"
    "ds_read_b128 v[120:123], %[da] offset:4096\n\t"
    "v_mfma_f32_16x16x32_bf16 v[68:71], v[132:135], v[96:99], 0\n\t"
    "ds_read_b128 v[124:127], %[da] offset:6144\n\t"
    "v_mfma_f32_16x16x32_bf16 v[72:75], v[128:131], v[100:103], 0\n\t"
    "v_mfma_f32_16x16x32_bf16 v[76:79], v[132:135], v[100:103], 0\n\t"
    "s_waitcnt lgkmcnt(4)\n\t"
    "v_mfma_f32_16x16x32_bf16 v[64:67], v[136:139], v[104:107], v[64:67]\n\t"
    "ds_read_b128 v[96:99], %[ea] offset:8192\n\t"
    "v_mfma_f32_16x16x32_bf16 v[68:71], v[140:143], v[104:107], v[68:71]\n\t"
    "ds_read_b128 v[100:103], %[ea] offset:10240\n\t"
    "v_mfma_f32_16x16x32_bf16 v[72:75], v[136:139], v[108:111], v[72:75]\n\t"
    "v_mfma_f32_16x16x32_bf16 v[76:79], v[140:143], v[108:111], v[76:79]\n\t"
    "s_cmp_eq_u32 %[w0], 1\n\t"
    "s_cbranch_scc1 7f\n\t"
    "8:\n\t"
    "s_waitcnt lgkmcnt(4)\n\t"
    "v_mfma_f32_16x16x32_bf16 v[64:67], v[144:147], v[112:115], v[64:67]\n\t"
    "ds_read_b128 v[104:107], %[da] offset:8192\n\t"
    "v_mfma_f32_16x16x32_bf16 v[68:71], v[148:151], v[112:115], v[68:71]\n\t"
    "ds_read_b128 v[108:111], %[da] offset:10240\n\t"
    "v_mfma_f32_16x16x32_bf16 v[72:75], v[144:147], v[116:119], v[72:75]\n\t"
    "v_mfma_f32_16x16x32_bf16 v[76:79], v[148:151], v[116:119], v[76:79]\n\t"
    "s_waitcnt lgkmcnt(4)\n\t"
    "v_mfma_f32_16x16x32_bf16 v[64:67], v[152:155], v[120:123], v[64:67]\n\t"
    "ds_read_b128 v[112:115], %[ea] offset:12288\n\t"
    "v_mfma_f32_16x16x32_bf16 v[68:71], v[156:159], v[120:123], v[68:71]\n\t"
    "ds_read_b128 v[116:119], %[ea] offset:14336\n\t"
    "v_mfma_f32_16x16x32_bf16 v[72:75], v[152:155], v[124:127], v[72:75]\n\t"
    "v_mfma_f32_16x16x32_bf16 v[76:79], v[156:159], v[124:127], v[76:79]\n\t"
    "s_waitcnt lgkmcnt(4)\n\t"
    "v_mfma_f32_16x16x32_bf16 v[64:67], v[160:163], v[96:99], v[64:67]\n\t"
    "ds_read_b128 v[120:123], %[da] offset:12288\n\t"
    "v_mfma_f32_16x16x32_bf16 v[68:71], v[164:167], v[96:99], v[68:71]\n\t"
    "ds_read_b128 v[124:127], %[da] offset:14336\n\t"
    "v_mfma_f32_16x16x32_bf16 v[72:75], v[160:163], v[100:103], v[72:75]\n\t"
    "v_mfma_f32_16x16x32_bf16 v[76:79], v[164:167], v[100:103], v[76:79]\n\t"
    "s_waitcnt lgkmcnt(4)\n\t"
    "v_mfma_f32_16x16x32_bf16 v[64:67], v[168:171], v[104:107], v[64:67]\n\t"
    "ds_read_b128 v[96:99], %[ea] offset:16384\n\t"
    "v_mfma_f32_16x16x32_bf16 v[68:71], v[172:175], v[104:107], v[68:71]\n\t"
    "ds_read_b128 v[100:103], %[ea] offset:18432\n\t"
    "v_mfma_f32_16x16x32_bf16 v[72:75], v[168:171], v[108:111], v[72:75]\n\t"
    "v_mfma_f32_16x16x32_bf16 v[76:79], v[172:175], v[108:111], v[76:79]\n\t"
    "s_waitcnt lgkmcnt(4)\n\t"
    "v_mfma_f32_16x16x32_bf16 v[64:67], v[176:179], v[112:115], v[64:67]\n\t"
    "ds_read_b128 v[104:107], %[da] offset:16384\n\t"
    "v_mfma_f32_16x16x32_bf16 v[68:71], v[180:183], v[112:115], v[68:71]\n\t"
    "ds_read_b128 v[108:111], %[da] offset:18432\n\t"
    "v_mfma_f32_16x16x32_bf16 v[72:75], v[176:179], v[116:119], v[72:75]\n\t"
    "v_mfma_f32_16x16x32_bf16 v[76:79], v[180:183], v[116:119], v[76:79]\n\t"
    "s_waitcnt lgkmcnt(4)\n\t"
    "v_mfma_f32_16x16x32_bf16 v[64:67], v[184:187], v[120:123], v[64:67]\n\t"
    "ds_read_b128 v[112:115], %[ea] offset:20480\n\t"
    "v_mfma_f32_16x16x32_bf16 v[68:71], v[188:191], v[120:123], v[68:71]\n\t"
    "ds_read_b128 v[116:119], %[ea] offset:22528\n\t"
    "v_mfma_f32_16x16x32_bf16 v[72:75], v[184:187], v[124:127], v[72:75]\n\t"
    "v_mfma_f32_16x16x32_bf16 v[76:79], v[188:191], v[124:127], v[76:79]\n\t"
    "s_waitcnt vmcnt(0)\n\t"
    "s_barrier\n\t"
    "s_waitcnt lgkmcnt(4)\n\t"
    "v_mfma_f32_16x16x32_bf16 v[64:67], v[192:195], v[96:99], v[64:67]\n\t"
    "ds_read_b128 v[120:123], %[da] offset:20480\n\t"
    "v_mfma_f32_16x16x32_bf16 v[68:71], v[196:199], v[96:99], v[68:71]\n\t"
    "ds_read_b128 v[124:127], %[da] offset:22528\n\t"
    "v_mfma_f32_16x16x32_bf16 v[72:75], v[192:195], v[100:103], v[72:75]\n\t"
    "ds_read_b128 v[48:51], %[rw] offset:0\n\t"
    "v_mfma_f32_16x16x32_bf16 v[76:79], v[196:199], v[100:103], v[76:79]\n\t"
    "ds_read_b128 v[52:55], %[rw] offset:1024\n\t"
    "s_cmp_eq_u32 %[w0], 0\n\t"
    "s_cbranch_scc1 5f\n\t"
    "6:\n\t"
    "s_waitcnt lgkmcnt(6)\n\t"
    "v_mfma_f32_16x16x32_bf16 v[64:67], v[200:203], v[104:107], v[64:67]\n\t"
    "ds_read_b128 v[96:99], %[ea] offset:24576\n\t"
    "v_mfma_f32_16x16x32_bf16 v[68:71], v[204:207], v[104:107], v[68:71]\n\t"
    "ds_read_b128 v[100:103], %[ea] offset:26624\n\t"
    "v_mfma_f32_16x16x32_bf16 v[72:75], v[200:203], v[108:111], v[72:75]\n\t"
    "ds_read_b128 v[56:59], %[rw] offset:2048\n\t"
    "v_mfma_f32_16x16x32_bf16 v[76:79], v[204:207], v[108:111], v[76:79]\n\t"
    "ds_read_b128 v[60:63], %[rw] offset:3072\n\t"
    "s_waitcnt lgkmcnt(0)\n\t"
    "v_mfma_f32_16x16x32_bf16 v[64:67], v[208:211], v[112:115], v[64:67]\n\t"
    "ds_read_b128 v[104:107], %[da] offset:24576\n\t"
    "v_add_f32 v48, v48, v80\n\t"
    "v_add_f32 v49, v49, v81\n\t"
    "v_mfma_f32_16x16x32_bf16 v[68:71], v[212:215], v[112:115], v[68:71]\n\t"
    "ds_read_b128 v[108:111], %[da] offset:26624\n\t"
    "v_add_f32 v50, v50, v82\n\t"
    "v_add_f32 v51, v51, v83\n\t"
    "v_mfma_f32_16x16x32_bf16 v[72:75], v[208:211], v[116:119], v[72:75]\n\t"
    "ds_read_b32 v39, %[sa]\n\t"
    "v_add_f32 v52, v52, v84\n\t"
    "v_mfma_f32_16x16x32_bf16 v[76:79], v[212:215], v[116:119], v[76:79]\n\t"
    "v_add_f32 v53, v53, v85\n\t"
    "v_add_f32 v54, v54, v86\n\t"
    "v_mfma_f32_16x16x32_bf16 v[64:67], v[216:219], v[120:123], v[64:67]\n\t"
    "ds_read_b128 v[112:115], %[ea] offset:28672\n\t"
    "v_add_f32 v55, v55, v87\n\t"
    "v_add_f32 v56, v56, v88\n\t"
    "v_mfma_f32_16x16x32_bf16 v[68:71], v[220:223], v[120:123], v[68:71]\n\t"
    "ds_read_b128 v[116:119], %[ea] offset:30720\n\t"
    "v_add_f32 v57, v57, v89\n\t"
    "v_mfma_f32_16x16x32_bf16 v[72:75], v[216:219], v[124:127], v[72:75]\n\t"
    "v_xor_b32 %[ea], 0x10000, %[ea]\n\t"
    "v_add_f32 v58, v58, v90\n\t"
    "v_add_f32 v59, v59, v91\n\t"
    "v_mfma_f32_16x16x32_bf16 v[76:79], v[220:223], v[124:127], v[76:79]\n\t"
    "v_add_f32 v60, v60, v92\n\t"
    "v_mfma_f32_16x16x32_bf16 v[64:67], v[224:227], v[96:99], v[64:67]\n\t"
    "ds_read_b128 v[120:123], %[da] offset:28672\n\t"
    "v_add_f32 v61, v61, v93\n\t"
    "v_add_f32 v62, v62, v94\n\t"
    "v_mfma_f32_16x16x32_bf16 v[68:71], v[228:231], v[96:99], v[68:71]\n\t"
    "ds_read_b128 v[124:127], %[da] offset:30720\n\t"
    "v_add_f32 v63, v63, v95\n\t"
    "v_cvt_pk_bf16_f32 v40, v48, v49\n\t"
    "v_mfma_f32_16x16x32_bf16 v[72:75], v[224:227], v[100:103], v[72:75]\n\t"
    "v_xor_b32 %[da], 0x10000, %[da]\n\t"
    "v_cvt_pk_bf16_f32 v41, v50, v51\n\t"
    "v_mfma_f32_16x16x32_bf16 v[76:79], v[228:231], v[100:103], v[76:79]\n\t"
    "v_cvt_pk_bf16_f32 v42, v52, v53\n\t"
    "v_cvt_pk_bf16_f32 v43, v54, v55\n\t"
    "s_waitcnt lgkmcnt(5)\n\t"
    "v_mfma_f32_16x16x32_bf16 v[64:67], v[232:235], v[104:107], v[64:67]\n\t"
    "ds_read_b128 v[96:99], %[ea] offset:0\n\t"
    "global_store_dwordx4 %[co0], v[40:43], s[36:37] sc1\n\t"
    "v_cvt_pk_bf16_f32 v44, v56, v57\n\t"
    "v_mfma_f32_16x16x32_bf16 v[68:71], v[236:239], v[104:107], v[68:71]\n\t"
    "ds_read_b128 v[100:103], %[ea] offset:2048\n\t"
    "v_cvt_pk_bf16_f32 v45, v58, v59\n\t"
    "v_mfma_f32_16x16x32_bf16 v[72:75], v[232:235], v[108:111], v[72:75]\n\t"
    "v_cvt_pk_bf16_f32 v46, v60, v61\n\t"
    "v_cvt_pk_bf16_f32 v47, v62, v63\n\t"
    "v_mfma_f32_16x16x32_bf16 v[76:79], v[236:239], v[108:111], v[76:79]\n\t"
    "global_store_dwordx4 %[co1], v[44:47], s[36:37] sc1\n\t"
    "s_waitcnt lgkmcnt(4)\n\t"
    "v_mfma_f32_16x16x32_bf16 v[64:67], v[240:243], v[112:115], v[64:67]\n\t"
    "ds_read_b128 v[104:107], %[da] offset:0\n\t"
    "v_mad_u32_u24 v39, v39, %[mu], %[ad]\n\t"
    "v_mfma_f32_16x16x32_bf16 v[68:71], v[244:247], v[112:115], v[68:71]\n\t"
    "ds_read_b128 v[108:111], %[da] offset:2048\n\t"
    "v_cmp_ge_i32 vcc, %[lf], v39\n\t"
    "s_cmp_eq_u64 vcc, 0\n\t"
    "s_addc_u32 %[lf], %[lf], 0\n\t"
    "v_mfma_f32_16x16x32_bf16 v[72:75], v[240:243], v[116:119], v[72:75]\n\t"
    "v_mfma_f32_16x16x32_bf16 v[76:79], v[244:247], v[116:119], v[76:79]\n\t"
    "v_cmp_ge_i32 vcc, %[lf], v39\n\t"
    "s_cmp_eq_u64 vcc, 0\n\t"
    "s_addc_u32 %[lf], %[lf], 0\n\t"
    "s_waitcnt lgkmcnt(4)\n\t"
    "v_mfma_f32_16x16x32_bf16 v[80:83], v[248:251], v[120:123], v[64:67]\n\t"
    "ds_read_b128 v[112:115], %[ea] offset:4096\n\t"
    "s_min_i32 %[lf], %[lf], %[cp]\n\t"
    "s_add_u32 %[b], %[b], 1\n\t"
    "v_mfma_f32_16x16x32_bf16 v[84:87], v[252:255], v[120:123], v[68:71]\n\t"
    "ds_read_b128 v[116:119], %[ea] offset:6144\n\t"
    "v_mfma_f32_16x16x32_bf16 v[88:91], v[248:251], v[124:127], v[72:75]\n\t"
    "v_mfma_f32_16x16x32_bf16 v[92:95], v[252:255], v[124:127], v[76:79]\n\t"
    "s_mov_b64 s[36:37], s[38:39]\n\t"
    "s_add_u32 s38, s38, %[ab]\n\t"
    "s_addc_u32 s39, s39, 0\n\t"
    "s_sub_u32 m0, %[lf], %[b]\n\t"
    "s_sub_u32 m0, m0, 1\n\t"
    "s_cmp_lt_u32 m0, %[up]\n\t"
    "s_cbranch_scc1 1b\n\t"
    "s_waitcnt lgkmcnt(0)\n\t"
    "s_nop 4\n\t"
    "s_branch 9f\n\t"
    "5:\n\t"
    "s_add_u32 %[st], %[b], %[po]\n\t"
    "s_max_i32 %[st], %[st], %[p0]\n\t"
    "v_mov_b32 v39, %[st]\n\t"
    "v_mov_b32 v38, 0\n\t"
    "s_mov_b64 exec, 1\n\t"
    "global_store_dword v38, v39, %[pg] sc1\n\t"
    "s_mov_b64 exec, -1\n\t"
    "s_mov_b32 %[p0], %[p1]\n\t"
    "s_branch 6b\n\t"
    "7:\n\t"
    "s_cmp_ge_i32 %[lf], %[cp]\n\t"
    "s_cbranch_scc1 8b\n\t"
    "s_mov_b32 m0, %[sl]\n\t"
    "s_nop 0\n\t"
    "global_load_lds_dword %[dv], %[db] sc1\n\t"
    "s_branch 8b\n\t"
    "9:"
    : "=&{v[80:95]}"(pd), [ea] "+&v"(ea), [da] "+&v"(da), [b] "+&s"(b), [lf] "+&s"(lf), "+{s[36:37]}"(p1), "+{s[38:39]}"(p0), [sa] "+&v"(sa), [st] "=&s"(st), [sl] "+&s"(sl), [p0] "+&s"(pf)
    : [rw] "v"(rw), [ab] "s"(ab), [cp] "s"(cp), [mu] "s"(mu), [ad] "v"(ad), [up] "s"(up), "{v[128:159]}"(W[0]), "{v[160:191]}"(W[1]), "{v[192:223]}"(W[2]), "{v[224:255]}"(W[3]), [co0] "v"(co0), [w0] "s"(w0), [dv] "v"(dv), [db] "s"(db), [pg] "s"(pg), [po] "s"(po - 1), [p1] "s"(b + po), [co1] "v"(co1)
    : "v38", "v39", "v40", "v41", "v42", "v43", "v44", "v45", "v46", "v47", "v48", "v49", "v50", "v51", "v52", "v53", "v54", "v55", "v56", "v57", "v58", "v59", "v60", "v61", "v62", "v63", "v64", "v65", "v66", "v67", "v68", "v69", "v70", "v71", "v72", "v73", "v74", "v75", "v76", "v77", "v78", "v79", "v96", "v97", "v98", "v99", "v100", "v101", "v102", "v103", "v104", "v105", "v106", "v107", "v108", "v109", "v110", "v111", "v112", "v113", "v114", "v115", "v116", "v117", "v118", "v119", "v120", "v121", "v122", "v123", "v124", "v125", "v126", "v127", "vcc", "scc", "m0", "memory");
}
__device__ __forceinline__ void fast_st1(const u32x32 (&W)[4], u32x16& pd, unsigned ea, unsigned da, int& b, int& lf, int cp, unsigned up, unsigned sa, unsigned mu, unsigned ad,
    unsigned rw, unsigned ab, const char* p1, const char* p0, unsigned co0, unsigned co1, int po, int pf, unsigned w0, unsigned sl, unsigned dv, const char* db, const char* pg) {
  unsigned st;
  asm volatile("s_barrier\n\t"
    "ds_read_b128 v[96:99], %[ea] offset:0\n\t"
    "ds_read_b128 v[100:103], %[ea] offset:2048\n\t"
    "ds_read_b128 v[104:107], %[da] offset:0\n\t"
    "ds_read_b128 v[108:111], %[da] offset:2048\n\t"
    "ds_read_b128 v[112:115], %[ea] offset:4096\n\t"
    "ds_read_b128 v[116:119], %[ea] offset:6144\n\t"
    "s_branch 2f\n\t"
    ".p2align 6\n\t"
    "1:\n\t"
    "s_barrier\n\t"
    "2:\n\t"
    "s_waitcnt lgkmcnt(4)\n\t"
    "v_mfma_f32_16x16x32_bf16 v[64:67], v[128:131], v[96:99], 0\n\t"
    "ds_read_b128 v[120:123], %[da] offset:4096\n\t"
    "v_mfma_f32_16x16x32_bf16 v[68:71], v[132:135], v[96:99], 0\n\t"
    "ds_read_b128 v[124:127], %[da] offset:6144\n\t"
    "v_mfma_f32_16x16x32_bf16 v[72:75], v[128:131], v[100:103], 0\n\t"
    "v_mfma_f32_16x16x32_bf16 v[76:79], v[132:135], v[100:103], 0\n\t"
    "s_waitcnt lgkmcnt(4)\n\t"
    "v_mfma_f32_16x16x32_bf16 v[64:67], v[136:139], v[104:107], v[64:67]\n\t"
    "ds_read_b128 v[96:99], %[ea] offset:8192\n\t"
    "v_mfma_f32_16x16x32_bf16 v[68:71], v[140:143], v[104:107], v[68:71]\n\t"
    "ds_read_b128 v[100:103], %[ea] offset:10240\n\t"
    "v_mfma_f32_16x16x32_bf16 v[72:75], v[136:139], v[108:111], v[72:75]\n\t"
    "v_mfma_f32_16x16x32_bf16 v[76:79], v[140:143], v[108:111], v[76:79]\n\t"
    "s_cmp_eq_u32 %[w0], 1\n\t"
    "s_cbranch_scc1 7f\n\t"
    "8:\n\t"
    "s_waitcnt lgkmcnt(4)\n\t"
    "v_mfma_f32_16x16x32_bf16 v[64:67], v[144:147], v[112:115], v[64:67]\n\t"
    "ds_read_b128 v[104:107], %[da] offset:8192\n\t"
    "v_mfma_f32_16x16x32_bf16 v[68:71], v[148:151], v[112:115], v[68:71]\n\t"
    "ds_read_b128 v[108:111], %[da] offset:10240\n\t"
    "v_mfma_f32_16x16x32_bf16 v[72:75], v[144:147], v[116:119], v[72:75]\n\t"
    "v_mfma_f32_16x16x32_bf16 v[76:79], v[148:151], v[116:119], v[76:79]\n\t"
    "s_waitcnt lgkmcnt(4)\n\t"
    "v_mfma_f32_16x16x32_bf16 v[64:67], v[152:155], v[120:123], v[64:67]\n\t"
    "ds_read_b128 v[112:115], %[ea] offset:12288\n\t"
    "v_mfma_f32_16x16x32_bf16 v[68:71], v[156:159], v[120:123], v[68:71]\n\t"
    "ds_read_b128 v[116:119], %[ea] offset:14336\n\t"
    "v_mfma_f32_16x16x32_bf16 v[72:75], v[152:155], v[124:127], v[72:75]\n\t"
    "v_mfma_f32_16x16x32_bf16 v[76:79], v[156:159], v[124:127], v[76:79]\n\t"
    "s_waitcnt lgkmcnt(4)\n\t"
    "v_mfma_f32_16x16x32_bf16 v[64:67], v[160:163], v[96:99], v[64:67]\n\t"
    "ds_read_b128 v[120:123], %[da] offset:12288\n\t"
    "v_mfma_f32_16x16x32_bf16 v[68:71], v[164:167], v[96:99], v[68:71]\n\t"
    "ds_read_b128 v[124:127], %[da] offset:14336\n\t"
    "v_mfma_f32_16x16x32_bf16 v[72:75], v[160:163], v[100:103], v[72:75]\n\t"
    "v_mfma_f32_16x16x32_bf16 v[76:79], v[164:167], v[100:103], v[76:79]\n\t"
    "s_waitcnt lgkmcnt(4)\n\t"
    "v_mfma_f32_16x16x32_bf16 v[64:67], v[168:171], v[104:107], v[64:67]\n\t"
    "ds_read_b128 v[96:99], %[ea] offset:16384\n\t"
    "v_mfma_f32_16x16x32_bf16 v[68:71], v[172:175], v[104:107], v[68:71]\n\t"
    "ds_read_b128 v[100:103], %[ea] offset:18432\n\t"
    "v_mfma_f32_16x16x32_bf16 v[72:75], v[168:171], v[108:111], v[72:75]\n\t"
    "v_mfma_f32_16x16x32_bf16 v[76:79], v[172:175], v[108:111], v[76:79]\n\t"
    "s_waitcnt lgkmcnt(4)\n\t"
    "v_mfma_f32_16x16x32_bf16 v[64:67], v[176:179], v[112:115], v[64:67]\n\t"
    "ds_read_b128 v[104:107], %[da] offset:16384\n\t"
    "v_mfma_f32_16x16x32_bf16 v[68:71], v[180:183], v[112:115], v[68:71]\n\t"
    "ds_read_b128 v[108:111], %[da] offset:18432\n\t"
    "v_mfma_f32_16x16x32_bf16 v[72:75], v[176:179], v[116:119], v[72:75]\n\t"
    "v_mfma_f32_16x16x32_bf16 v[76:79], v[180:183], v[116:119], v[76:79]\n\t"
    "s_waitcnt lgkmcnt(4)\n\t"
    "v_mfma_f32_16x16x32_bf16 v[64:67], v[184:187], v[120:123], v[64:67]\n\t"
    "ds_read_b128 v[112:115], %[ea] offset:20480\n\t"
    "v_mfma_f32_16x16x32_bf16 v[68:71], v[188:191], v[120:123], v[68:71]\n\t"
    "ds_read_b128 v[116:119], %[ea] offset:22528\n\t"
    "v_mfma_f32_16x16x32_bf16 v[72:75], v[184:187], v[124:127], v[72:75]\n\t"
    "v_mfma_f32_16x16x32_bf16 v[76:79], v[188:191], v[124:127], v[76:79]\n\t"
    "s_waitcnt vmcnt(0)\n\t"
    "s_barrier\n\t"
    "s_waitcnt lgkmcnt(4)\n\t"
    "v_mfma_f32_16x16x32_bf16 v[64:67], v[192:195], v[96:99], v[64:67]\n\t"
    "ds_read_b128 v[120:123], %[da] offset:20480\n\t"
    "v_mfma_f32_16x16x32_bf16 v[68:71], v[196:199], v[96:99], v[68:71]\n\t"
    "ds_read_b128 v[124:127], %[da] offset:22528\n\t"
    "v_mfma_f32_16x16x32_bf16 v[72:75], v[192:195], v[100:103], v[72:75]\n\t"
    "ds_read_b128 v[48:51], %[rw] offset:0\n\t"
    "v_mfma_f32_16x16x32_bf16 v[76:79], v[196:199], v[100:103], v[76:79]\n\t"
    "ds_read_b128 v[52:55], %[rw] offset:1024\n\t"
    "s_cmp_eq_u32 %[w0], 0\n\t"
    "s_cbranch_scc1 5f\n\t"
    "6:\n\t"
    "s_waitcnt lgkmcnt(6)\n\t"
    "v_mfma_f32_16x16x32_bf16 v[64:67], v[200:203], v[104:107], v[64:67]\n\t"
    "ds_read_b128 v[96:99], %[ea] offset:24576\n\t"
    "v_mfma_f32_16x16x32_bf16 v[68:71], v[204:207], v[104:107], v[68:71]\n\t"
    "ds_read_b128 v[100:103], %[ea] offset:26624\n\t"
    "v_mfma_f32_16x16x32_bf16 v[72:75], v[200:203], v[108:111], v[72:75]\n\t"
    "ds_read_b128 v[56:59], %[rw] offset:2048\n\t"
    "v_mfma_f32_16x16x32_bf16 v[76:79], v[204:207], v[108:111], v[76:79]\n\t"
    "ds_read_b128 v[60:63], %[rw] offset:3072\n\t"
    "s_waitcnt lgkmcnt(0)\n\t"
    "v_mfma_f32_16x16x32_bf16 v[64:67], v[208:211], v[112:115], v[64:67]\n\t"
    "ds_read_b128 v[104:107], %[da] offset:24576\n\t"
    "v_add_f32 v48, v48, v80\n\t"
    "v_add_f32 v49, v49, v81\n\t"
    "v_add_f32 v50, v50, v82\n\t"
    "v_mfma_f32_16x16x32_bf16 v[68:71], v[212:215], v[112:115], v[68:71]\n\t"
    "ds_read_b128 v[108:111], %[da] offset:26624\n\t"
    "v_add_f32 v51, v51, v83\n\t"
    "v_add_f32 v52, v52, v84\n\t"
    "v_add_f32 v53, v53, v85\n\t"
    "v_mfma_f32_16x16x32_bf16 v[72:75], v[208:211], v[116:119], v[72:75]\n\t"
    "ds_read_b32 v39, %[sa]\n\t"
    "v_add_f32 v54, v54, v86\n\t"
    "v_add_f32 v55, v55, v87\n\t"
    "v_add_f32 v56, v56, v88\n\t"
    "v_mfma_f32_16x16x32_bf16 v[76:79], v[212:215], v[116:119], v[76:79]\n\t"
    "v_add_f32 v57, v57, v89\n\t"
    "v_add_f32 v58, v58, v90\n\t"
    "v_add_f32 v59, v59, v91\n\t"
    "v_mfma_f32_16x16x32_bf16 v[64:67], v[216:219], v[120:123], v[64:67]\n\t"
    "ds_read_b128 v[112:115], %[ea] offset:28672\n\t"
    "v_add_f32 v60, v60, v92\n\t"
    "v_add_f32 v61, v61, v93\n\t"
    "v_add_f32 v62, v62, v94\n\t"
    "v_mfma_f32_16x16x32_bf16 v[68:71], v[220:223], v[120:123], v[68:71]\n\t"
    "ds_read_b128 v[116:119], %[ea] offset:30720\n\t"
    "v_add_f32 v63, v63, v95\n\t"
    "v_mul_f32 v40, %[nl], v48\n\t"
    "v_mul_f32 v41, %[nl], v49\n\t"
    "v_mfma_f32_16x16x32_bf16 v[72:75], v[216:219], v[124:127], v[72:75]\n\t"
    "v_xor_b32 %[ea], 0x10000, %[ea]\n\t"
    "v_mul_f32 v42, %[nl], v50\n\t"
    "v_mul_f32 v43, %[nl], v51\n\t"
    "v_mul_f32 v44, %[nl], v56\n\t"
    "v_mfma_f32_16x16x32_bf16 v[76:79], v[220:223], v[124:127], v[76:79]\n\t"
    "v_mul_f32 v45, %[nl], v57\n\t"
    "v_mul_f32 v46, %[nl], v58\n\t"
    "v_mul_f32 v47, %[nl], v59\n\t"
    "v_mfma_f32_16x16x32_bf16 v[64:67], v[224:227], v[96:99], v[64:67]\n\t"
    "ds_read_b128 v[120:123], %[da] offset:28672\n\t"
    "v_exp_f32 v40, v40\n\t"
    "v_exp_f32 v41, v41\n\t"
    "v_exp_f32 v42, v42\n\t"
    "v_mfma_f32_16x16x32_bf16 v[68:71], v[228:231], v[96:99], v[68:71]\n\t"
    "ds_read_b128 v[124:127], %[da] offset:30720\n\t"
    "v_exp_f32 v43, v43\n\t"
    "v_exp_f32 v44, v44\n\t"
    "v_exp_f32 v45, v45\n\t"
    "v_mfma_f32_16x16x32_bf16 v[72:75], v[224:227], v[100:103], v[72:75]\n\t"
    "v_xor_b32 %[da], 0x10000, %[da]\n\t"
    "v_exp_f32 v46, v46\n\t"
    "v_exp_f32 v47, v47\n\t"
    "v_add_f32 v40, 1.0, v40\n\t"
    "v_mfma_f32_16x16x32_bf16 v[76:79], v[228:231], v[100:103], v[76:79]\n\t"
    "v_add_f32 v41, 1.0, v41\n\t"
    "v_add_f32 v42, 1.0, v42\n\t"
    "v_add_f32 v43, 1.0, v43\n\t"
    "s_waitcnt lgkmcnt(5)\n\t"
    "v_mfma_f32_16x16x32_bf16 v[64:67], v[232:235], v[104:107], v[64:67]\n\t"
    "ds_read_b128 v[96:99], %[ea] offset:0\n\t"
    "v_add_f32 v44, 1.0, v44\n\t"
    "v_add_f32 v45, 1.0, v45\n\t"
    "v_add_f32 v46, 1.0, v46\n\t"
    "v_mfma_f32_16x16x32_bf16 v[68:71], v[236:239], v[104:107], v[68:71]\n\t"
    "ds_read_b128 v[100:103], %[ea] offset:2048\n\t"
    "v_add_f32 v47, 1.0, v47\n\t"
    "v_rcp_f32 v40, v40\n\t"
    "v_rcp_f32 v41, v41\n\t"
    "v_mfma_f32_16x16x32_bf16 v[72:75], v[232:235], v[108:111], v[72:75]\n\t"
    "v_rcp_f32 v42, v42\n\t"
    "v_rcp_f32 v43, v43\n\t"
    "v_rcp_f32 v44, v44\n\t"
    "v_mfma_f32_16x16x32_bf16 v[76:79], v[236:239], v[108:111], v[76:79]\n\t"
    "v_rcp_f32 v45, v45\n\t"
    "v_rcp_f32 v46, v46\n\t"
    "v_rcp_f32 v47, v47\n\t"
    "s_waitcnt lgkmcnt(4)\n\t"
    "v_mfma_f32_16x16x32_bf16 v[64:67], v[240:243], v[112:115], v[64:67]\n\t"
    "ds_read_b128 v[104:107], %[da] offset:0\n\t"
    "v_mad_u32_u24 v39, v39, %[mu], %[ad]\n\t"
    "v_mul_f32 v40, v48, v40\n\t"
    "v_mul_f32 v41, v49, v41\n\t"
    "v_mul_f32 v42, v50, v42\n\t"
    "v_mfma_f32_16x16x32_bf16 v[68:71], v[244:247], v[112:115], v[68:71]\n\t"
    "ds_read_b128 v[108:111], %[da] offset:2048\n\t"
    "v_cmp_ge_i32 vcc, %[lf], v39\n\t"
    "s_cmp_eq_u64 vcc, 0\n\t"
    "s_addc_u32 %[lf], %[lf], 0\n\t"
    "v_mul_f32 v43, v51, v43\n\t"
    "v_mul_f32 v44, v56, v44\n\t"
    "v_mul_f32 v45, v57, v45\n\t"
    "v_mfma_f32_16x16x32_bf16 v[72:75], v[240:243], v[116:119], v[72:75]\n\t"
    "v_mul_f32 v46, v58, v46\n\t"
    "v_mul_f32 v47, v59, v47\n\t"
    "v_mul_f32 v40, v40, v52\n\t"
    "v_mfma_f32_16x16x32_bf16 v[76:79], v[244:247], v[116:119], v[76:79]\n\t"
    "v_cmp_ge_i32 vcc, %[lf], v39\n\t"
    "s_cmp_eq_u64 vcc, 0\n\t"
    "s_addc_u32 %[lf], %[lf], 0\n\t"
    "v_mul_f32 v41, v41, v53\n\t"
    "v_mul_f32 v42, v42, v54\n\t"
    "v_mul_f32 v43, v43, v55\n\t"
    "s_waitcnt lgkmcnt(4)\n\t"
    "v_mfma_f32_16x16x32_bf16 v[80:83], v[248:251], v[120:123], v[64:67]\n\t"
    "ds_read_b128 v[112:115], %[ea] offset:4096\n\t"
    "s_min_i32 %[lf], %[lf], %[cp]\n\t"
    "s_add_u32 %[b], %[b], 1\n\t"
    "v_mul_f32 v44, v44, v60\n\t"
    "v_mul_f32 v45, v45, v61\n\t"
    "v_mul_f32 v46, v46, v62\n\t"
    "v_mfma_f32_16x16x32_bf16 v[84:87], v[252:255], v[120:123], v[68:71]\n\t"
    "ds_read_b128 v[116:119], %[ea] offset:6144\n\t"
    "v_mul_f32 v47, v47, v63\n\t"
    "v_cvt_pk_bf16_f32 v48, v40, v41\n\t"
    "v_cvt_pk_bf16_f32 v49, v42, v43\n\t"
    "v_mfma_f32_16x16x32_bf16 v[88:91], v[248:251], v[124:127], v[72:75]\n\t"
    "v_cvt_pk_bf16_f32 v50, v44, v45\n\t"
    "v_cvt_pk_bf16_f32 v51, v46, v47\n\t"
    "s_nop 1\n\t"
    "v_mfma_f32_16x16x32_bf16 v[92:95], v[252:255], v[124:127], v[76:79]\n\t"
    "v_permlane16_swap_b32 v48, v50\n\t"
    "v_permlane16_swap_b32 v49, v51\n\t"
    "global_store_dwordx4 %[co0], v[48:51], s[36:37] sc1\n\t"
    "s_mov_b64 s[36:37], s[38:39]\n\t"
    "s_add_u32 s38, s38, %[ab]\n\t"
    "s_addc_u32 s39, s39, 0\n\t"
    "s_sub_u32 m0, %[lf], %[b]\n\t"
    "s_sub_u32 m0, m0, 1\n\t"
    "s_cmp_lt_u32 m0, %[up]\n\t"
    "s_cbranch_scc1 1b\n\t"
    "s_waitcnt lgkmcnt(0)\n\t"
    "s_nop 4\n\t"
    "s_branch 9f\n\t"
    "5:\n\t"
    "s_add_u32 %[st], %[b], %[po]\n\t"
    "s_max_i32 %[st], %[st], %[p0]\n\t"
    "v_mov_b32 v39, %[st]\n\t"
    "v_mov_b32 v38, 0\n\t"
    "s_mov_b64 exec, 1\n\t"
    "global_store_dword v38, v39, %[pg] sc1\n\t"
    "s_mov_b64 exec, -1\n\t"
    "s_mov_b32 %[p0], %[p1]\n\t"
    "s_branch 6b\n\t"
    "7:\n\t"
    "s_cmp_ge_i32 %[lf], %[cp]\n\t"
    "s_cbranch_scc1 8b\n\t"
    "s_mov_b32 m0, %[sl]\n\t"
    "s_nop 0\n\t"
    "global_load_lds_dword %[dv], %[db] sc1\n\t"
    "s_branch 8b\n\t"
    "9:"
    : "=&{v[80:95]}"(pd), [ea] "+&v"(ea), [da] "+&v"(da), [b] "+&s"(b), [lf] "+&s"(lf), "+{s[36:37]}"(p1), "+{s[38:39]}"(p0), [sa] "+&v"(sa), [st] "=&s"(st), [sl] "+&s"(sl), [p0] "+&s"(pf)
    : [rw] "v"(rw), [ab] "s"(ab), [cp] "s"(cp), [mu] "s"(mu), [ad] "v"(ad), [up] "s"(up), "{v[128:159]}"(W[0]), "{v[160:191]}"(W[1]), "{v[192:223]}"(W[2]), "{v[224:255]}"(W[3]), [co0] "v"(co0), [w0] "s"(w0), [dv] "v"(dv), [db] "s"(db), [pg] "s"(pg), [po] "s"(po - 1), [p1] "s"(b + po), [nl] "s"(-LOG2E)
    : "v38", "v39", "v40", "v41", "v42", "v43", "v44", "v45", "v46", "v47", "v48", "v49", "v50", "v51", "v52", "v53", "v54", "v55", "v56", "v57", "v58", "v59", "v60", "v61", "v62", "v63", "v64", "v65", "v66", "v67", "v68", "v69", "v70", "v71", "v72", "v73", "v74", "v75", "v76", "v77", "v78", "v79", "v96", "v97", "v98", "v99", "v100", "v101", "v102", "v103", "v104", "v105", "v106", "v107", "v108", "v109", "v110", "v111", "v112", "v113", "v114", "v115", "v116", "v117", "v118", "v119", "v120", "v121", "v122", "v123", "v124", "v125", "v126", "v127", "vcc", "scc", "m0", "memory");
}
__device__ __forceinline__ void fast_st2(const u32x32 (&W)[4], u32x16& pd, unsigned ea, unsigned da, int& b, int& lf, int cp, unsigned up, unsigned sa, unsigned mu, unsigned ad,
    unsigned rw, unsigned ab, const char* p1, const char* p0, unsigned co0, unsigned co1, int po, int pf, unsigned w0, unsigned sl, unsigned dv, const char* db, const char* pg) {
  unsigned st;
  asm volatile("s_barrier\n\t"
    "ds_read_b128 v[96:99], %[ea] offset:0\n\t"
    "ds_read_b128 v[100:103], %[ea] offset:2048\n\t"
    "ds_read_b128 v[104:107], %[da] offset:0\n\t"
    "ds_read_b128 v[108:111], %[da] offset:2048\n\t"
    "ds_read_b128 v[112:115], %[ea] offset:4096\n\t"
    "ds_read_b128 v[116:119], %[ea] offset:6144\n\t"
    "s_branch 2f\n\t"
    ".p2align 6\n\t"
    "1:\n\t"
    "s_barrier\n\t"
    "2:\n\t"
    "s_waitcnt lgkmcnt(4)\n\t"
    "v_mfma_f32_16x16x32_bf16 v[64:67], v[128:131], v[96:99], 0\n\t"
    "ds_read_b128 v[120:123], %[da] offset:4096\n\t"
    "v_mfma_f32_16x16x32_bf16 v[68:71], v[132:135], v[96:99], 0\n\t"
    "ds_read_b128 v[124:127], %[da] offset:6144\n\t"
    "v_mfma_f32_16x16x32_bf16 v[72:75], v[128:131], v[100:103], 0\n\t"
    "v_mfma_f32_16x16x32_bf16 v[76:79], v[132:135], v[100:103], 0\n\t"
    "s_waitcnt lgkmcnt(4)\n\t"
    "v_mfma_f32_16x16x32_bf16 v[64:67], v[136:139], v[104:107], v[64:67]\n\t"
    "ds_read_b128 v[96:99], %[ea] offset:8192\n\t"
    "v_mfma_f32_16x16x32_bf16 v[68:71], v[140:143], v[104:107], v[68:71]\n\t"
    "ds_read_b128 v[100:103], %[ea] offset:10240\n\t"
    "v_mfma_f32_16x16x32_bf16 v[72:75], v[136:139], v[108:111], v[72:75]\n\t"
    "v_mfma_f32_16x16x32_bf16 v[76:79], v[140:143], v[108:111], v[76:79]\n\t"
    "s_cmp_eq_u32 %[w0], 1\n\t"
    "s_cbranch_scc1 7f\n\t"
    "8:\n\t"
    "s_waitcnt lgkmcnt(4)\n\t"
    "v_mfma_f32_16x16x32_bf16 v[64:67], v[144:147], v[112:115], v[64:67]\n\t"
    "ds_read_b128 v[104:107], %[da] offset:8192\n\t"
    "v_mfma_f32_16x16x32_bf16 v[68:71], v[148:151], v[112:115], v[68:71]\n\t"
    "ds_read_b128 v[108:111], %[da] offset:10240\n\t"
    "v_mfma_f32_16x16x32_bf16 v[72:75], v[144:147], v[116:119], v[72:75]\n\t"
    "v_mfma_f32_16x16x32_bf16 v[76:79], v[148:151], v[116:119], v[76:79]\n\t"
    "s_waitcnt lgkmcnt(4)\n\t"
    "v_mfma_f32_16x16x32_bf16 v[64:67], v[152:155], v[120:123], v[64:67]\n\t"
    "ds_read_b128 v[112:115], %[ea] offset:12288\n\t"
    "v_mfma_f32_16x16x32_bf16 v[68:71], v[156:159], v[120:123], v[68:71]\n\t"
    "ds_read_b128 v[116:119], %[ea] offset:14336\n\t"
    "v_mfma_f32_16x16x32_bf16 v[72:75], v[152:155], v[124:127], v[72:75]\n\t"
    "v_mfma_f32_16x16x32_bf16 v[76:79], v[156:159], v[124:127], v[76:79]\n\t"
    "s_waitcnt lgkmcnt(4)\n\t"
    "v_mfma_f32_16x16x32_bf16 v[64:67], v[160:163], v[96:99], v[64:67]\n\t"
    "ds_read_b128 v[120:123], %[da] offset:12288\n\t"
    "v_mfma_f32_16x16x32_bf16 v[68:71], v[164:167], v[96:99], v[68:71]\n\t"
    "ds_read_b128 v[124:127], %[da] offset:14336\n\t"
    "v_mfma_f32_16x16x32_bf16 v[72:75], v[160:163], v[100:103], v[72:75]\n\t"
    "v_mfma_f32_16x16x32_bf16 v[76:79], v[164:167], v[100:103], v[76:79]\n\t"
    "s_waitcnt lgkmcnt(4)\n\t"
    "v_mfma_f32_16x16x32_bf16 v[64:67], v[168:171], v[104:107], v[64:67]\n\t"
    "ds_read_b128 v[96:99], %[ea] offset:16384\n\t"
    "v_mfma_f32_16x16x32_bf16 v[68:71], v[172:175], v[104:107], v[68:71]\n\t"
    "ds_read_b128 v[100:103], %[ea] offset:18432\n\t"
    "v_mfma_f32_16x16x32_bf16 v[72:75], v[168:171], v[108:111], v[72:75]\n\t"
    "v_mfma_f32_16x16x32_bf16 v[76:79], v[172:175], v[108:111], v[76:79]\n\t"
    "s_waitcnt lgkmcnt(4)\n\t"
    "v_mfma_f32_16x16x32_bf16 v[64:67], v[176:179], v[112:115], v[64:67]\n\t"
    "ds_read_b128 v[104:107], %[da] offset:16384\n\t"
    "v_mfma_f32_16x16x32_bf16 v[68:71], v[180:183], v[112:115], v[68:71]\n\t"
    "ds_read_b128 v[108:111], %[da] offset:18432\n\t"
    "v_mfma_f32_16x16x32_bf16 v[72:75], v[176:179], v[116:119], v[72:75]\n\t"
    "v_mfma_f32_16x16x32_bf16 v[76:79], v[180:183], v[116:119], v[76:79]\n\t"
    "s_waitcnt lgkmcnt(4)\n\t"
    "v_mfma_f32_16x16x32_bf16 v[64:67], v[184:187], v[120:123], v[64:67]\n\t"
    "ds_read_b128 v[112:115], %[ea] offset:20480\n\t"
    "v_mfma_f32_16x16x32_bf16 v[68:71], v[188:191], v[120:123], v[68:71]\n\t"
    "ds_read_b128 v[116:119], %[ea] offset:22528\n\t"
    "v_mfma_f32_16x16x32_bf16 v[72:75], v[184:187], v[124:127], v[72:75]\n\t"
    "v_mfma_f32_16x16x32_bf16 v[76:79], v[188:191], v[124:127], v[76:79]\n\t"
    "s_waitcnt vmcnt(0)\n\t"
    "s_barrier\n\t"
    "s_waitcnt lgkmcnt(4)\n\t"
    "v_mfma_f32_16x16x32_bf16 v[64:67], v[192:195], v[96:99], v[64:67]\n\t"
    "ds_read_b128 v[120:123], %[da] offset:20480\n\t"
    "v_mfma_f32_16x16x32_bf16 v[68:71], v[196:199], v[96:99], v[68:71]\n\t"
    "ds_read_b128 v[124:127], %[da] offset:22528\n\t"
    "v_mfma_f32_16x16x32_bf16 v[72:75], v[192:195], v[100:103], v[72:75]\n\t"
    "ds_read_b128 v[48:51], %[rw] offset:0\n\t"
    "v_mfma_f32_16x16x32_bf16 v[76:79], v[196:199], v[100:103], v[76:79]\n\t"
    "ds_read_b128 v[52:55], %[rw] offset:1024\n\t"
    "s_cmp_eq_u32 %[w0], 0\n\t"
    "s_cbranch_scc1 5f\n\t"
    "6:\n\t"
    "s_waitcnt lgkmcnt(6)\n\t"
    "v_mfma_f32_16x16x32_bf16 v[64:67], v[200:203], v[104:107], v[64:67]\n\t"
    "ds_read_b128 v[96:99], %[ea] offset:24576\n\t"
    "v_mfma_f32_16x16x32_bf16 v[68:71], v[204:207], v[104:107], v[68:71]\n\t"
    "ds_read_b128 v[100:103], %[ea] offset:26624\n\t"
    "v_mfma_f32_16x16x32_bf16 v[72:75], v[200:203], v[108:111], v[72:75]\n\t"
    "ds_read_b128 v[56:59], %[rw] offset:2048\n\t"
    "v_mfma_f32_16x16x32_bf16 v[76:79], v[204:207], v[108:111], v[76:79]\n\t"
    "ds_read_b128 v[60:63], %[rw] offset:3072\n\t"
    "s_waitcnt lgkmcnt(0)\n\t"
    "v_mfma_f32_16x16x32_bf16 v[64:67], v[208:211], v[112:115], v[64:67]\n\t"
    "ds_read_b128 v[104:107], %[da] offset:24576\n\t"
    "v_cvt_pk_bf16_f32 v40, v80, v81\n\t"
    "v_cvt_pk_bf16_f32 v41, v82, v83\n\t"
    "v_mfma_f32_16x16x32_bf16 v[68:71], v[212:215], v[112:115], v[68:71]\n\t"
    "ds_read_b128 v[108:111], %[da] offset:26624\n\t"
    "v_cvt_pk_bf16_f32 v42, v84, v85\n\t"
    "v_mfma_f32_16x16x32_bf16 v[72:75], v[208:211], v[116:119], v[72:75]\n\t"
    "ds_read_b32 v39, %[sa]\n\t"
    "v_cvt_pk_bf16_f32 v43, v86, v87\n\t"
    "v_mfma_f32_16x16x32_bf16 v[76:79], v[212:215], v[116:119], v[76:79]\n\t"
    "global_store_dwordx4 %[co0], v[40:43], s[36:37] sc1\n\t"
    "v_mfma_f32_16x16x32_bf16 v[64:67], v[216:219], v[120:123], v[64:67]\n\t"
    "ds_read_b128 v[112:115], %[ea] offset:28672\n\t"
    "v_cvt_pk_bf16_f32 v44, v88, v89\n\t"
    "v_cvt_pk_bf16_f32 v45, v90, v91\n\t"
    "v_mfma_f32_16x16x32_bf16 v[68:71], v[220:223], v[120:123], v[68:71]\n\t"
    "ds_read_b128 v[116:119], %[ea] offset:30720\n\t"
    "v_cvt_pk_bf16_f32 v46, v92, v93\n\t"
    "v_mfma_f32_16x16x32_bf16 v[72:75], v[216:219], v[124:127], v[72:75]\n\t"
    "v_xor_b32 %[ea], 0x10000, %[ea]\n\t"
    "v_cvt_pk_bf16_f32 v47, v94, v95\n\t"
    "v_mfma_f32_16x16x32_bf16 v[76:79], v[220:223], v[124:127], v[76:79]\n\t"
    "global_store_dwordx4 %[co1], v[44:47], s[36:37] sc1\n\t"
    "v_mfma_f32_16x16x32_bf16 v[64:67], v[224:227], v[96:99], v[64:67]\n\t"
    "ds_read_b128 v[120:123], %[da] offset:28672\n\t"
    "v_cvt_pk_bf16_f32 v48, v48, v49\n\t"
    "v_cvt_pk_bf16_f32 v49, v50, v51\n\t"
    "v_mfma_f32_16x16x32_bf16 v[68:71], v[228:231], v[96:99], v[68:71]\n\t"
    "ds_read_b128 v[124:127], %[da] offset:30720\n\t"
    "v_cvt_pk_bf16_f32 v50, v52, v53\n\t"
    "v_mfma_f32_16x16x32_bf16 v[72:75], v[224:227], v[100:103], v[72:75]\n\t"
    "v_xor_b32 %[da], 0x10000, %[da]\n\t"
    "v_cvt_pk_bf16_f32 v51, v54, v55\n\t"
    "v_mfma_f32_16x16x32_bf16 v[76:79], v[228:231], v[100:103], v[76:79]\n\t"
    "global_store_dwordx4 %[co0], v[48:51], s[36:37] offset:2048 sc1\n\t"
    "s_waitcnt lgkmcnt(5)\n\t"
    "v_mfma_f32_16x16x32_bf16 v[64:67], v[232:235], v[104:107], v[64:67]\n\t"
    "ds_read_b128 v[96:99], %[ea] offset:0\n\t"
    "v_cvt_pk_bf16_f32 v56, v56, v57\n\t"
    "v_cvt_pk_bf16_f32 v57, v58, v59\n\t"
    "v_mfma_f32_16x16x32_bf16 v[68:71], v[236:239], v[104:107], v[68:71]\n\t"
    "ds_read_b128 v[100:103], %[ea] offset:2048\n\t"
    "v_cvt_pk_bf16_f32 v58, v60, v61\n\t"
    "v_mfma_f32_16x16x32_bf16 v[72:75], v[232:235], v[108:111], v[72:75]\n\t"
    "v_cvt_pk_bf16_f32 v59, v62, v63\n\t"
    "v_mfma_f32_16x16x32_bf16 v[76:79], v[236:239], v[108:111], v[76:79]\n\t"
    "global_store_dwordx4 %[co1], v[56:59], s[36:37] offset:2048 sc1\n\t"
    "s_waitcnt lgkmcnt(4)\n\t"
    "v_mfma_f32_16x16x32_bf16 v[64:67], v[240:243], v[112:115], v[64:67]\n\t"
    "ds_read_b128 v[104:107], %[da] offset:0\n\t"
    "v_mad_u32_u24 v39, v39, %[mu], %[ad]\n\t"
    "v_mfma_f32_16x16x32_bf16 v[68:71], v[244:247], v[112:115], v[68:71]\n\t"
    "ds_read_b128 v[108:111], %[da] offset:2048\n\t"
    "v_cmp_ge_i32 vcc, %[lf], v39\n\t"
    "s_cmp_eq_u64 vcc, 0\n\t"
    "s_addc_u32 %[lf], %[lf], 0\n\t"
    "v_mfma_f32_16x16x32_bf16 v[72:75], v[240:243], v[116:119], v[72:75]\n\t"
    "v_mfma_f32_16x16x32_bf16 v[76:79], v[244:247], v[116:119], v[76:79]\n\t"
    "v_cmp_ge_i32 vcc, %[lf], v39\n\t"
    "s_cmp_eq_u64 vcc, 0\n\t"
    "s_addc_u32 %[lf], %[lf], 0\n\t"
    "s_waitcnt lgkmcnt(4)\n\t"
    "v_mfma_f32_16x16x32_bf16 v[80:83], v[248:251], v[120:123], v[64:67]\n\t"
    "ds_read_b128 v[112:115], %[ea] offset:4096\n\t"
    "s_min_i32 %[lf], %[lf], %[cp]\n\t"
    "s_add_u32 %[b], %[b], 1\n\t"
    "v_mfma_f32_16x16x32_bf16 v[84:87], v[252:255], v[120:123], v[68:71]\n\t"
    "ds_read_b128 v[116:119], %[ea] offset:6144\n\t"
    "v_mfma_f32_16x16x32_bf16 v[88:91], v[248:251], v[124:127], v[72:75]\n\t"
    "v_mfma_f32_16x16x32_bf16 v[92:95], v[252:255], v[124:127], v[76:79]\n\t"
    "s_mov_b64 s[36:37], s[38:39]\n\t"
    "s_add_u32 s38, s38, %[ab]\n\t"
    "s_addc_u32 s39, s39, 0\n\t"
    "s_sub_u32 m0, %[lf], %[b]\n\t"
    "s_sub_u32 m0, m0, 1\n\t"
    "s_cmp_lt_u32 m0, %[up]\n\t"
    "s_cbranch_scc1 1b\n\t"
    "s_waitcnt lgkmcnt(0)\n\t"
    "s_nop 4\n\t"
    "s_branch 9f\n\t"
    "5:\n\t"
    "s_add_u32 %[st], %[b], %[po]\n\t"
    "s_max_i32 %[st], %[st], %[p0]\n\t"
    "v_mov_b32 v39, %[st]\n\t"
    "v_mov_b32 v38, 0\n\t"
    "s_mov_b64 exec, 1\n\t"
    "global_store_dword v38, v39, %[pg] sc1\n\t"
    "s_mov_b64 exec, -1\n\t"
    "s_mov_b32 %[p0], %[p1]\n\t"
    "s_branch 6b\n\t"
    "7:\n\t"
    "s_cmp_ge_i32 %[lf], %[cp]\n\t"
    "s_cbranch_scc1 8b\n\t"
    "s_mov_b32 m0, %[sl]\n\t"
    "s_nop 0\n\t"
    "global_load_lds_dword %[dv], %[db] sc1\n\t"
    "s_branch 8b\n\t"
    "9:"
    : "=&{v[80:95]}"(pd), [ea] "+&v"(ea), [da] "+&v"(da), [b] "+&s"(b), [lf] "+&s"(lf), "+{s[36:37]}"(p1), "+{s[38:39]}"(p0), [sa] "+&v"(sa), [st] "=&s"(st), [sl] "+&s"(sl), [p0] "+&s"(pf)
    : [rw] "v"(rw), [ab] "s"(ab), [cp] "s"(cp), [mu] "s"(mu), [ad] "v"(ad), [up] "s"(up), "{v[128:159]}"(W[0]), "{v[160:191]}"(W[1]), "{v[192:223]}"(W[2]), "{v[224:255]}"(W[3]), [co0] "v"(co0), [w0] "s"(w0), [dv] "v"(dv), [db] "s"(db), [pg] "s"(pg), [po] "s"(po - 1), [p1] "s"(b + po), [co1] "v"(co1)
    : "v38", "v39", "v40", "v41", "v42", "v43", "v44", "v45", "v46", "v47", "v48", "v49", "v50", "v51", "v52", "v53", "v54", "v55", "v56", "v57", "v58", "v59", "v60", "v61", "v62", "v63", "v64", "v65", "v66", "v67", "v68", "v69", "v70", "v71", "v72", "v73", "v74", "v75", "v76", "v77", "v78", "v79", "v96", "v97", "v98", "v99", "v100", "v101", "v102", "v103", "v104", "v105", "v106", "v107", "v108", "v109", "v110", "v111", "v112", "v113", "v114", "v115", "v116", "v117", "v118", "v119", "v120", "v121", "v122", "v123", "v124", "v125", "v126", "v127", "vcc", "scc", "m0", "memory");
}
__device__ __forceinline__ void lean_ld0(const u32x32 (&W)[4], unsigned ea, unsigned da, unsigned x, unsigned lb, const char* s0, const char* s1, const unsigned (&av)[8], unsigned ps, unsigned dv, const char* db, unsigned rw, unsigned fa, unsigned fv,
    unsigned ga, unsigned mu, unsigned ad, int th, int tb, unsigned& sv, unsigned& su, unsigned& got) {
  asm volatile("s_setprio 2\n\t"
    "v_mov_b32 v76, %[ga]\n\t"
    "ds_read_b32 v76, v76 offset:24\n\t"
    "ds_read_b128 v[96:99], %[ea] offset:0\n\t"
    "ds_read_b128 v[100:103], %[ea] offset:2048\n\t"
    "ds_read_b128 v[104:107], %[da] offset:0\n\t"
    "ds_read_b128 v[108:111], %[da] offset:2048\n\t"
    "ds_read_b128 v[112:115], %[ea] offset:4096\n\t"
    "ds_read_b128 v[116:119], %[ea] offset:6144\n\t"
    "s_waitcnt lgkmcnt(6)\n\t"
    "v_cmp_ne_u32 vcc, 0, v76\n\t"
    "s_mov_b32 %[got], -1\n\t"
    "s_cbranch_vccnz 12f\n\t"
    "s_waitcnt lgkmcnt(4)\n\t"
    "v_mfma_f32_16x16x32_bf16 v[80:83], v[128:131], v[96:99], 0\n\t"
    "ds_read_b128 v[120:123], %[da] offset:4096\n\t"
    "v_mfma_f32_16x16x32_bf16 v[84:87], v[132:135], v[96:99], 0\n\t"
    "ds_read_b128 v[124:127], %[da] offset:6144\n\t"
    "v_mfma_f32_16x16x32_bf16 v[88:91], v[128:131], v[100:103], 0\n\t"
    "v_mfma_f32_16x16x32_bf16 v[92:95], v[132:135], v[100:103], 0\n\t"
    "s_waitcnt vmcnt(24)\n\t"
    "s_cmp_lt_u32 %[x], 2\n\t"
    "s_cbranch_scc0 41f\n\t"
    "s_waitcnt vmcnt(8)\n\t"
    "41:\n\t"
    "s_barrier\n\t"
    "s_waitcnt lgkmcnt(4)\n\t"
    "v_mfma_f32_16x16x32_bf16 v[80:83], v[136:139], v[104:107], v[80:83]\n\t"
    "ds_read_b128 v[96:99], %[ea] offset:8192\n\t"
    "v_mfma_f32_16x16x32_bf16 v[84:87], v[140:143], v[104:107], v[84:87]\n\t"
    "ds_read_b128 v[100:103], %[ea] offset:10240\n\t"
    "v_mfma_f32_16x16x32_bf16 v[88:91], v[136:139], v[108:111], v[88:91]\n\t"
    "v_mfma_f32_16x16x32_bf16 v[92:95], v[140:143], v[108:111], v[92:95]\n\t"
    "s_waitcnt lgkmcnt(4)\n\t"
    "v_mfma_f32_16x16x32_bf16 v[80:83], v[144:147], v[112:115], v[80:83]\n\t"
    "ds_read_b128 v[104:107], %[da] offset:8192\n\t"
    "v_mfma_f32_16x16x32_bf16 v[84:87], v[148:151], v[112:115], v[84:87]\n\t"
    "ds_read_b128 v[108:111], %[da] offset:10240\n\t"
    "v_mfma_f32_16x16x32_bf16 v[88:91], v[144:147], v[116:119], v[88:91]\n\t"
    "v_mfma_f32_16x16x32_bf16 v[92:95], v[148:151], v[116:119], v[92:95]\n\t"
    "s_waitcnt lgkmcnt(4)\n\t"
    "v_mfma_f32_16x16x32_bf16 v[80:83], v[152:155], v[120:123], v[80:83]\n\t"
    "ds_read_b128 v[112:115], %[ea] offset:12288\n\t"
    "v_mfma_f32_16x16x32_bf16 v[84:87], v[156:159], v[120:123], v[84:87]\n\t"
    "ds_read_b128 v[116:119], %[ea] offset:14336\n\t"
    "v_mfma_f32_16x16x32_bf16 v[88:91], v[152:155], v[124:127], v[88:91]\n\t"
    "v_mfma_f32_16x16x32_bf16 v[92:95], v[156:159], v[124:127], v[92:95]\n\t"
    "s_waitcnt lgkmcnt(4)\n\t"
    "v_mfma_f32_16x16x32_bf16 v[80:83], v[160:163], v[96:99], v[80:83]\n\t"
    "ds_read_b128 v[120:123], %[da] offset:12288\n\t"
    "v_mfma_f32_16x16x32_bf16 v[84:87], v[164:167], v[96:99], v[84:87]\n\t"
    "ds_read_b128 v[124:127], %[da] offset:14336\n\t"
    "v_mfma_f32_16x16x32_bf16 v[88:91], v[160:163], v[100:103], v[88:91]\n\t"
    "v_mfma_f32_16x16x32_bf16 v[92:95], v[164:167], v[100:103], v[92:95]\n\t"
    "s_waitcnt vmcnt(20)\n\t"
    "s_cmp_lt_u32 %[x], 2\n\t"
    "s_cbranch_scc0 45f\n\t"
    "s_waitcnt vmcnt(4)\n\t"
    "45:\n\t"
    "s_barrier\n\t"
    "s_waitcnt lgkmcnt(4)\n\t"
    "v_mfma_f32_16x16x32_bf16 v[80:83], v[168:171], v[104:107], v[80:83]\n\t"
    "ds_read_b128 v[96:99], %[ea] offset:16384\n\t"
    "v_mfma_f32_16x16x32_bf16 v[84:87], v[172:175], v[104:107], v[84:87]\n\t"
    "ds_read_b128 v[100:103], %[ea] offset:18432\n\t"
    "v_mfma_f32_16x16x32_bf16 v[88:91], v[168:171], v[108:111], v[88:91]\n\t"
    "v_mfma_f32_16x16x32_bf16 v[92:95], v[172:175], v[108:111], v[92:95]\n\t"
    "s_waitcnt lgkmcnt(4)\n\t"
    "v_mfma_f32_16x16x32_bf16 v[80:83], v[176:179], v[112:115], v[80:83]\n\t"
    "ds_read_b128 v[104:107], %[da] offset:16384\n\t"
    "v_mfma_f32_16x16x32_bf16 v[84:87], v[180:183], v[112:115], v[84:87]\n\t"
    "ds_read_b128 v[108:111], %[da] offset:18432\n\t"
    "v_mfma_f32_16x16x32_bf16 v[88:91], v[176:179], v[116:119], v[88:91]\n\t"
    "v_mfma_f32_16x16x32_bf16 v[92:95], v[180:183], v[116:119], v[92:95]\n\t"
    "s_waitcnt lgkmcnt(4)\n\t"
    "v_mfma_f32_16x16x32_bf16 v[80:83], v[184:187], v[120:123], v[80:83]\n\t"
    "ds_read_b128 v[112:115], %[ea] offset:20480\n\t"
    "v_mfma_f32_16x16x32_bf16 v[84:87], v[188:191], v[120:123], v[84:87]\n\t"
    "ds_read_b128 v[116:119], %[ea] offset:22528\n\t"
    "v_mfma_f32_16x16x32_bf16 v[88:91], v[184:187], v[124:127], v[88:91]\n\t"
    "v_mfma_f32_16x16x32_bf16 v[92:95], v[188:191], v[124:127], v[92:95]\n\t"
    "s_waitcnt lgkmcnt(4)\n\t"
    "v_mfma_f32_16x16x32_bf16 v[80:83], v[192:195], v[96:99], v[80:83]\n\t"
    "ds_read_b128 v[120:123], %[da] offset:20480\n\t"
    "v_mfma_f32_16x16x32_bf16 v[84:87], v[196:199], v[96:99], v[84:87]\n\t"
    "ds_read_b128 v[124:127], %[da] offset:22528\n\t"
    "v_mfma_f32_16x16x32_bf16 v[88:91], v[192:195], v[100:103], v[88:91]\n\t"
    "v_mfma_f32_16x16x32_bf16 v[92:95], v[196:199], v[100:103], v[92:95]\n\t"
    "s_waitcnt vmcnt(16)\n\t"
    "s_cmp_lt_u32 %[x], 2\n\t"
    "s_cbranch_scc0 49f\n\t"
    "s_waitcnt vmcnt(0)\n\t"
    "49:\n\t"
    "s_barrier\n\t"
    "s_waitcnt lgkmcnt(4)\n\t"
    "v_mfma_f32_16x16x32_bf16 v[80:83], v[200:203], v[104:107], v[80:83]\n\t"
    "ds_read_b128 v[96:99], %[ea] offset:24576\n\t"
    "v_mfma_f32_16x16x32_bf16 v[84:87], v[204:207], v[104:107], v[84:87]\n\t"
    "ds_read_b128 v[100:103], %[ea] offset:26624\n\t"
    "v_mfma_f32_16x16x32_bf16 v[88:91], v[200:203], v[108:111], v[88:91]\n\t"
    "v_mfma_f32_16x16x32_bf16 v[92:95], v[204:207], v[108:111], v[92:95]\n\t"
    "s_waitcnt lgkmcnt(4)\n\t"
    "v_mfma_f32_16x16x32_bf16 v[80:83], v[208:211], v[112:115], v[80:83]\n\t"
    "ds_read_b128 v[104:107], %[da] offset:24576\n\t"
    "v_mfma_f32_16x16x32_bf16 v[84:87], v[212:215], v[112:115], v[84:87]\n\t"
    "ds_read_b128 v[108:111], %[da] offset:26624\n\t"
    "v_mfma_f32_16x16x32_bf16 v[88:91], v[208:211], v[116:119], v[88:91]\n\t"
    "v_mfma_f32_16x16x32_bf16 v[92:95], v[212:215], v[116:119], v[92:95]\n\t"
    "s_cmp_eq_u32 %[ps], 0\n\t"
    "s_cbranch_scc1 10f\n\t"
    "global_load_dword %[sv], %[dv], %[db] sc1\n\t"
    "global_load_dword %[su], %[dv], %[db] offset:64 sc1\n\t"
    "10:\n\t"
    "s_waitcnt lgkmcnt(4)\n\t"
    "v_mfma_f32_16x16x32_bf16 v[80:83], v[216:219], v[120:123], v[80:83]\n\t"
    "ds_read_b128 v[112:115], %[ea] offset:28672\n\t"
    "v_mfma_f32_16x16x32_bf16 v[84:87], v[220:223], v[120:123], v[84:87]\n\t"
    "ds_read_b128 v[116:119], %[ea] offset:30720\n\t"
    "v_mfma_f32_16x16x32_bf16 v[88:91], v[216:219], v[124:127], v[88:91]\n\t"
    "v_mfma_f32_16x16x32_bf16 v[92:95], v[220:223], v[124:127], v[92:95]\n\t"
    "s_waitcnt lgkmcnt(4)\n\t"
    "v_mfma_f32_16x16x32_bf16 v[80:83], v[224:227], v[96:99], v[80:83]\n\t"
    "ds_read_b128 v[120:123], %[da] offset:28672\n\t"
    "v_mfma_f32_16x16x32_bf16 v[84:87], v[228:231], v[96:99], v[84:87]\n\t"
    "ds_read_b128 v[124:127], %[da] offset:30720\n\t"
    "v_mfma_f32_16x16x32_bf16 v[88:91], v[224:227], v[100:103], v[88:91]\n\t"
    "v_mfma_f32_16x16x32_bf16 v[92:95], v[228:231], v[100:103], v[92:95]\n\t"
    "s_waitcnt lgkmcnt(4)\n\t"
    "v_mfma_f32_16x16x32_bf16 v[80:83], v[232:235], v[104:107], v[80:83]\n\t"
    "v_mfma_f32_16x16x32_bf16 v[84:87], v[236:239], v[104:107], v[84:87]\n\t"
    "v_mfma_f32_16x16x32_bf16 v[88:91], v[232:235], v[108:111], v[88:91]\n\t"
    "v_mfma_f32_16x16x32_bf16 v[92:95], v[236:239], v[108:111], v[92:95]\n\t"
    "s_waitcnt lgkmcnt(2)\n\t"
    "v_mfma_f32_16x16x32_bf16 v[80:83], v[240:243], v[112:115], v[80:83]\n\t"
    "v_mfma_f32_16x16x32_bf16 v[84:87], v[244:247], v[112:115], v[84:87]\n\t"
    "v_mfma_f32_16x16x32_bf16 v[88:91], v[240:243], v[116:119], v[88:91]\n\t"
    "v_mfma_f32_16x16x32_bf16 v[92:95], v[244:247], v[116:119], v[92:95]\n\t"
    "s_waitcnt lgkmcnt(0)\n\t"
    "v_mfma_f32_16x16x32_bf16 v[80:83], v[248:251], v[120:123], v[80:83]\n\t"
    "s_memrealtime s[36:37]\n\t"
    "v_mfma_f32_16x16x32_bf16 v[84:87], v[252:255], v[120:123], v[84:87]\n\t"
    "v_mfma_f32_16x16x32_bf16 v[88:91], v[248:251], v[124:127], v[88:91]\n\t"
    "v_mfma_f32_16x16x32_bf16 v[92:95], v[252:255], v[124:127], v[92:95]\n\t"
    "s_mov_b32 %[got], 0\n\t"
    "v_mov_b32 v76, %[fa]\n\t"
    "v_mov_b32 v77, %[fv]\n\t"
    "v_add_u32 v78, 1, v77\n\t"
    "v_mov_b32 v79, %[ga]\n\t"
    "s_waitcnt lgkmcnt(0)\n\t"
    "ds_write_b128 %[rw], v[80:83] offset:0\n\t"
    "ds_write_b128 %[rw], v[84:87] offset:1024\n\t"
    "ds_write_b128 %[rw], v[88:91] offset:2048\n\t"
    "ds_write_b128 %[rw], v[92:95] offset:3072\n\t"
    "ds_write_b32 v76, v77\n\t"
    "s_cmp_eq_u32 %[ps], 0\n\t"
    "s_cbranch_scc1 9f\n\t"
    "s_waitcnt vmcnt(0)\n\t"
    "v_mad_u32_u24 v76, %[sv], %[mu], %[ad]\n\t"
    "v_cmp_ge_i32 vcc, %[th], v76\n\t"
    "s_cbranch_vccz 11f\n\t"
    "s_sub_u32 s36, s36, 20\n\t"
    "v_sub_u32 v76, s36, %[su]\n\t"
    "v_lshrrev_b32 v77, 24, %[su]\n\t"
    "v_lshlrev_b32 v76, 8, v76\n\t"
    "v_ashrrev_i32 v76, 31, v76\n\t"
    "v_add_u32 v77, v77, v76\n\t"
    "v_cmp_lt_i32 s[36:37], %[tb], v77\n\t"
    "s_andn2_b64 vcc, vcc, s[36:37]\n\t"
    "s_cbranch_scc1 9f\n\t"
    "11:\n\t"
    "ds_write2_b32 v79, v78, v78 offset1:1\n\t"
    "s_add_u32 m0, %[lb], 0\n\t"
    "s_nop 0\n\t"
    "global_load_lds_dwordx4 %[av0], %[s0] sc1\n\t"
    "s_add_u32 m0, %[lb], 32768\n\t"
    "s_nop 0\n\t"
    "global_load_lds_dwordx4 %[av1], %[s0] sc1\n\t"
    "s_add_u32 m0, %[lb], 4096\n\t"
    "s_nop 0\n\t"
    "global_load_lds_dwordx4 %[av2], %[s0] sc1\n\t"
    "s_add_u32 m0, %[lb], 36864\n\t"
    "s_nop 0\n\t"
    "global_load_lds_dwordx4 %[av3], %[s0] sc1\n\t"
    "s_add_u32 m0, %[lb], 8192\n\t"
    "s_nop 0\n\t"
    "global_load_lds_dwordx4 %[av4], %[s0] sc1\n\t"
    "s_add_u32 m0, %[lb], 40960\n\t"
    "s_nop 0\n\t"
    "global_load_lds_dwordx4 %[av5], %[s0] sc1\n\t"
    "s_add_u32 m0, %[lb], 12288\n\t"
    "s_nop 0\n\t"
    "global_load_lds_dwordx4 %[av6], %[s0] sc1\n\t"
    "s_add_u32 m0, %[lb], 45056\n\t"
    "s_nop 0\n\t"
    "global_load_lds_dwordx4 %[av7], %[s0] sc1\n\t"
    "s_add_u32 m0, %[lb], 16384\n\t"
    "s_nop 0\n\t"
    "global_load_lds_dwordx4 %[av0], %[s1] sc1\n\t"
    "s_add_u32 m0, %[lb], 49152\n\t"
    "s_nop 0\n\t"
    "global_load_lds_dwordx4 %[av1], %[s1] sc1\n\t"
    "s_add_u32 m0, %[lb], 20480\n\t"
    "s_nop 0\n\t"
    "global_load_lds_dwordx4 %[av2], %[s1] sc1\n\t"
    "s_add_u32 m0, %[lb], 53248\n\t"
    "s_nop 0\n\t"
    "global_load_lds_dwordx4 %[av3], %[s1] sc1\n\t"
    "s_add_u32 m0, %[lb], 24576\n\t"
    "s_nop 0\n\t"
    "global_load_lds_dwordx4 %[av4], %[s1] sc1\n\t"
    "s_add_u32 m0, %[lb], 57344\n\t"
    "s_nop 0\n\t"
    "global_load_lds_dwordx4 %[av5], %[s1] sc1\n\t"
    "s_add_u32 m0, %[lb], 28672\n\t"
    "s_nop 0\n\t"
    "global_load_lds_dwordx4 %[av6], %[s1] sc1\n\t"
    "s_add_u32 m0, %[lb], 61440\n\t"
    "s_nop 0\n\t"
    "global_load_lds_dwordx4 %[av7], %[s1] sc1\n\t"
    "s_mov_b32 %[got], 1\n\t"
    "s_branch 9f\n\t"
    "12:\n\t"
    "s_waitcnt lgkmcnt(0)\n\t"
    "9:"
    : [sv] "+&v"(sv), [su] "+&v"(su), [got] "=&s"(got)
    : [ea] "v"(ea), [da] "v"(da), [x] "s"(x), "{v[128:159]}"(W[0]), "{v[160:191]}"(W[1]), "{v[192:223]}"(W[2]), "{v[224:255]}"(W[3]), [lb] "s"(lb), [s0] "s"(s0), [s1] "s"(s1), [av0] "v"(av[0]), [av1] "v"(av[1]), [av2] "v"(av[2]), [av3] "v"(av[3]), [av4] "v"(av[4]), [av5] "v"(av[5]), [av6] "v"(av[6]), [av7] "v"(av[7]), [ps] "s"(ps), [dv] "v"(dv), [db] "s"(db), [rw] "v"(rw), [fa] "s"(fa), [fv] "s"(fv), [ga] "s"(ga), [mu] "s"(mu), [ad] "v"(ad), [th] "s"(th), [tb] "s"(tb)
    : "v96", "v97", "v98", "v99", "v100", "v101", "v102", "v103", "v104", "v105", "v106", "v107", "v108", "v109", "v110", "v111", "v112", "v113", "v114", "v115", "v116", "v117", "v118", "v119", "v120", "v121", "v122", "v123", "v124", "v125", "v126", "v127", "v76", "v77", "v78", "v79", "v80", "v81", "v82", "v83", "v84", "v85", "v86", "v87", "v88", "v89", "v90", "v91", "v92", "v93", "v94", "v95", "vcc", "scc", "m0", "memory", "s36", "s37", "s38", "s39");
}
__device__ __forceinline__ void lean_dm0(const u32x32 (&W)[4], unsigned ea, unsigned da, unsigned x, unsigned lb, const char* s0, const char* s1, const unsigned (&av)[8], unsigned ps, unsigned dv, const char* db, unsigned rw, unsigned fa, unsigned fv,
    unsigned ga, unsigned mu, unsigned ad, int th, int tb, unsigned& sv, unsigned& su, unsigned& got) {
  asm volatile("s_setprio 2\n\t"
    "v_mov_b32 v76, %[ga]\n\t"
    "ds_read_b32 v76, v76 offset:24\n\t"
    "ds_read_b128 v[96:99], %[ea] offset:0\n\t"
    "ds_read_b128 v[100:103], %[ea] offset:2048\n\t"
    "ds_read_b128 v[104:107], %[da] offset:0\n\t"
    "ds_read_b128 v[108:111], %[da] offset:2048\n\t"
    "ds_read_b128 v[112:115], %[ea] offset:4096\n\t"
    "ds_read_b128 v[116:119], %[ea] offset:6144\n\t"
    "s_waitcnt lgkmcnt(6)\n\t"
    "v_cmp_ne_u32 vcc, 0, v76\n\t"
    "s_mov_b32 %[got], -1\n\t"
    "s_cbranch_vccnz 12f\n\t"
    "s_waitcnt lgkmcnt(4)\n\t"
    "v_mfma_f32_16x16x32_bf16 v[80:83], v[128:131], v[96:99], 0\n\t"
    "ds_read_b128 v[120:123], %[da] offset:4096\n\t"
    "s_add_u32 m0, %[lb], 0\n\t"
    "v_mfma_f32_16x16x32_bf16 v[84:87], v[132:135], v[96:99], 0\n\t"
    "ds_read_b128 v[124:127], %[da] offset:6144\n\t"
    "global_load_lds_dwordx4 %[av0], %[s0] sc1\n\t"
    "v_mfma_f32_16x16x32_bf16 v[88:91], v[128:131], v[100:103], 0\n\t"
    "s_add_u32 m0, %[lb], 32768\n\t"
    "v_mfma_f32_16x16x32_bf16 v[92:95], v[132:135], v[100:103], 0\n\t"
    "global_load_lds_dwordx4 %[av1], %[s0] sc1\n\t"
    "s_waitcnt vmcnt(10)\n\t"
    "s_barrier\n\t"
    "s_waitcnt lgkmcnt(4)\n\t"
    "v_mfma_f32_16x16x32_bf16 v[80:83], v[136:139], v[104:107], v[80:83]\n\t"
    "ds_read_b128 v[96:99], %[ea] offset:8192\n\t"
    "s_add_u32 m0, %[lb], 4096\n\t"
    "v_mfma_f32_16x16x32_bf16 v[84:87], v[140:143], v[104:107], v[84:87]\n\t"
    "ds_read_b128 v[100:103], %[ea] offset:10240\n\t"
    "global_load_lds_dwordx4 %[av2], %[s0] sc1\n\t"
    "v_mfma_f32_16x16x32_bf16 v[88:91], v[136:139], v[108:111], v[88:91]\n\t"
    "s_add_u32 m0, %[lb], 36864\n\t"
    "v_mfma_f32_16x16x32_bf16 v[92:95], v[140:143], v[108:111], v[92:95]\n\t"
    "global_load_lds_dwordx4 %[av3], %[s0] sc1\n\t"
    "s_waitcnt lgkmcnt(4)\n\t"
    "v_mfma_f32_16x16x32_bf16 v[80:83], v[144:147], v[112:115], v[80:83]\n\t"
    "ds_read_b128 v[104:107], %[da] offset:8192\n\t"
    "s_add_u32 m0, %[lb], 8192\n\t"
    "v_mfma_f32_16x16x32_bf16 v[84:87], v[148:151], v[112:115], v[84:87]\n\t"
    "ds_read_b128 v[108:111], %[da] offset:10240\n\t"
    "global_load_lds_dwordx4 %[av4], %[s0] sc1\n\t"
    "v_mfma_f32_16x16x32_bf16 v[88:91], v[144:147], v[116:119], v[88:91]\n\t"
    "s_add_u32 m0, %[lb], 40960\n\t"
    "v_mfma_f32_16x16x32_bf16 v[92:95], v[148:151], v[116:119], v[92:95]\n\t"
    "global_load_lds_dwordx4 %[av5], %[s0] sc1\n\t"
    "s_waitcnt lgkmcnt(4)\n\t"
    "v_mfma_f32_16x16x32_bf16 v[80:83], v[152:155], v[120:123], v[80:83]\n\t"
    "ds_read_b128 v[112:115], %[ea] offset:12288\n\t"
    "s_add_u32 m0, %[lb], 12288\n\t"
    "v_mfma_f32_16x16x32_bf16 v[84:87], v[156:159], v[120:123], v[84:87]\n\t"
    "ds_read_b128 v[116:119], %[ea] offset:14336\n\t"
    "global_load_lds_dwordx4 %[av6], %[s0] sc1\n\t"
    "v_mfma_f32_16x16x32_bf16 v[88:91], v[152:155], v[124:127], v[88:91]\n\t"
    "s_add_u32 m0, %[lb], 45056\n\t"
    "v_mfma_f32_16x16x32_bf16 v[92:95], v[156:159], v[124:127], v[92:95]\n\t"
    "global_load_lds_dwordx4 %[av7], %[s0] sc1\n\t"
    "s_waitcnt lgkmcnt(4)\n\t"
    "v_mfma_f32_16x16x32_bf16 v[80:83], v[160:163], v[96:99], v[80:83]\n\t"
    "ds_read_b128 v[120:123], %[da] offset:12288\n\t"
    "s_add_u32 m0, %[lb], 16384\n\t"
    "v_mfma_f32_16x16x32_bf16 v[84:87], v[164:167], v[96:99], v[84:87]\n\t"
    "ds_read_b128 v[124:127], %[da] offset:14336\n\t"
    "global_load_lds_dwordx4 %[av0], %[s1] sc1\n\t"
    "v_mfma_f32_16x16x32_bf16 v[88:91], v[160:163], v[100:103], v[88:91]\n\t"
    "s_add_u32 m0, %[lb], 49152\n\t"
    "v_mfma_f32_16x16x32_bf16 v[92:95], v[164:167], v[100:103], v[92:95]\n\t"
    "global_load_lds_dwordx4 %[av1], %[s1] sc1\n\t"
    "s_waitcnt vmcnt(14)\n\t"
    "s_barrier\n\t"
    "s_waitcnt lgkmcnt(4)\n\t"
    "v_mfma_f32_16x16x32_bf16 v[80:83], v[168:171], v[104:107], v[80:83]\n\t"
    "ds_read_b128 v[96:99], %[ea] offset:16384\n\t"
    "s_add_u32 m0, %[lb], 20480\n\t"
    "v_mfma_f32_16x16x32_bf16 v[84:87], v[172:175], v[104:107], v[84:87]\n\t"
    "ds_read_b128 v[100:103], %[ea] offset:18432\n\t"
    "global_load_lds_dwordx4 %[av2], %[s1] sc1\n\t"
    "v_mfma_f32_16x16x32_bf16 v[88:91], v[168:171], v[108:111], v[88:91]\n\t"
    "s_add_u32 m0, %[lb], 53248\n\t"
    "v_mfma_f32_16x16x32_bf16 v[92:95], v[172:175], v[108:111], v[92:95]\n\t"
    "global_load_lds_dwordx4 %[av3], %[s1] sc1\n\t"
    "s_waitcnt lgkmcnt(4)\n\t"
    "v_mfma_f32_16x16x32_bf16 v[80:83], v[176:179], v[112:115], v[80:83]\n\t"
    "ds_read_b128 v[104:107], %[da] offset:16384\n\t"
    "s_add_u32 m0, %[lb], 24576\n\t"
    "v_mfma_f32_16x16x32_bf16 v[84:87], v[180:183], v[112:115], v[84:87]\n\t"
    "ds_read_b128 v[108:111], %[da] offset:18432\n\t"
    "global_load_lds_dwordx4 %[av4], %[s1] sc1\n\t"
    "v_mfma_f32_16x16x32_bf16 v[88:91], v[176:179], v[116:119], v[88:91]\n\t"
    "s_add_u32 m0, %[lb], 57344\n\t"
    "v_mfma_f32_16x16x32_bf16 v[92:95], v[180:183], v[116:119], v[92:95]\n\t"
    "global_load_lds_dwordx4 %[av5], %[s1] sc1\n\t"
    "s_waitcnt lgkmcnt(4)\n\t"
    "v_mfma_f32_16x16x32_bf16 v[80:83], v[184:187], v[120:123], v[80:83]\n\t"
    "ds_read_b128 v[112:115], %[ea] offset:20480\n\t"
    "s_add_u32 m0, %[lb], 28672\n\t"
    "v_mfma_f32_16x16x32_bf16 v[84:87], v[188:191], v[120:123], v[84:87]\n\t"
    "ds_read_b128 v[116:119], %[ea] offset:22528\n\t"
    "global_load_lds_dwordx4 %[av6], %[s1] sc1\n\t"
    "v_mfma_f32_16x16x32_bf16 v[88:91], v[184:187], v[124:127], v[88:91]\n\t"
    "s_add_u32 m0, %[lb], 61440\n\t"
    "v_mfma_f32_16x16x32_bf16 v[92:95], v[188:191], v[124:127], v[92:95]\n\t"
    "global_load_lds_dwordx4 %[av7], %[s1] sc1\n\t"
    "s_waitcnt lgkmcnt(4)\n\t"
    "v_mfma_f32_16x16x32_bf16 v[80:83], v[192:195], v[96:99], v[80:83]\n\t"
    "ds_read_b128 v[120:123], %[da] offset:20480\n\t"
    "v_mfma_f32_16x16x32_bf16 v[84:87], v[196:199], v[96:99], v[84:87]\n\t"
    "ds_read_b128 v[124:127], %[da] offset:22528\n\t"
    "v_mfma_f32_16x16x32_bf16 v[88:91], v[192:195], v[100:103], v[88:91]\n\t"
    "v_mfma_f32_16x16x32_bf16 v[92:95], v[196:199], v[100:103], v[92:95]\n\t"
    "s_waitcnt vmcnt(16)\n\t"
    "s_barrier\n\t"
    "s_waitcnt lgkmcnt(4)\n\t"
    "v_mfma_f32_16x16x32_bf16 v[80:83], v[200:203], v[104:107], v[80:83]\n\t"
    "ds_read_b128 v[96:99], %[ea] offset:24576\n\t"
    "v_mfma_f32_16x16x32_bf16 v[84:87], v[204:207], v[104:107], v[84:87]\n\t"
    "ds_read_b128 v[100:103], %[ea] offset:26624\n\t"
    "v_mfma_f32_16x16x32_bf16 v[88:91], v[200:203], v[108:111], v[88:91]\n\t"
    "v_mfma_f32_16x16x32_bf16 v[92:95], v[204:207], v[108:111], v[92:95]\n\t"
    "s_waitcnt lgkmcnt(4)\n\t"
    "v_mfma_f32_16x16x32_bf16 v[80:83], v[208:211], v[112:115], v[80:83]\n\t"
    "ds_read_b128 v[104:107], %[da] offset:24576\n\t"
    "v_mfma_f32_16x16x32_bf16 v[84:87], v[212:215], v[112:115], v[84:87]\n\t"
    "ds_read_b128 v[108:111], %[da] offset:26624\n\t"
    "v_mfma_f32_16x16x32_bf16 v[88:91], v[208:211], v[116:119], v[88:91]\n\t"
    "v_mfma_f32_16x16x32_bf16 v[92:95], v[212:215], v[116:119], v[92:95]\n\t"
    "s_waitcnt lgkmcnt(4)\n\t"
    "v_mfma_f32_16x16x32_bf16 v[80:83], v[216:219], v[120:123], v[80:83]\n\t"
    "ds_read_b128 v[112:115], %[ea] offset:28672\n\t"
    "v_mfma_f32_16x16x32_bf16 v[84:87], v[220:223], v[120:123], v[84:87]\n\t"
    "ds_read_b128 v[116:119], %[ea] offset:30720\n\t"
    "v_mfma_f32_16x16x32_bf16 v[88:91], v[216:219], v[124:127], v[88:91]\n\t"
    "v_mfma_f32_16x16x32_bf16 v[92:95], v[220:223], v[124:127], v[92:95]\n\t"
    "s_waitcnt lgkmcnt(4)\n\t"
    "v_mfma_f32_16x16x32_bf16 v[80:83], v[224:227], v[96:99], v[80:83]\n\t"
    "ds_read_b128 v[120:123], %[da] offset:28672\n\t"
    "v_mfma_f32_16x16x32_bf16 v[84:87], v[228:231], v[96:99], v[84:87]\n\t"
    "ds_read_b128 v[124:127], %[da] offset:30720\n\t"
    "v_mfma_f32_16x16x32_bf16 v[88:91], v[224:227], v[100:103], v[88:91]\n\t"
    "v_mfma_f32_16x16x32_bf16 v[92:95], v[228:231], v[100:103], v[92:95]\n\t"
    "s_waitcnt lgkmcnt(4)\n\t"
    "v_mfma_f32_16x16x32_bf16 v[80:83], v[232:235], v[104:107], v[80:83]\n\t"
    "v_mfma_f32_16x16x32_bf16 v[84:87], v[236:239], v[104:107], v[84:87]\n\t"
    "v_mfma_f32_16x16x32_bf16 v[88:91], v[232:235], v[108:111], v[88:91]\n\t"
    "v_mfma_f32_16x16x32_bf16 v[92:95], v[236:239], v[108:111], v[92:95]\n\t"
    "s_waitcnt lgkmcnt(2)\n\t"
    "v_mfma_f32_16x16x32_bf16 v[80:83], v[240:243], v[112:115], v[80:83]\n\t"
    "v_mfma_f32_16x16x32_bf16 v[84:87], v[244:247], v[112:115], v[84:87]\n\t"
    "v_mfma_f32_16x16x32_bf16 v[88:91], v[240:243], v[116:119], v[88:91]\n\t"
    "v_mfma_f32_16x16x32_bf16 v[92:95], v[244:247], v[116:119], v[92:95]\n\t"
    "s_waitcnt lgkmcnt(0)\n\t"
    "v_mfma_f32_16x16x32_bf16 v[80:83], v[248:251], v[120:123], v[80:83]\n\t"
    "v_mfma_f32_16x16x32_bf16 v[84:87], v[252:255], v[120:123], v[84:87]\n\t"
    "v_mfma_f32_16x16x32_bf16 v[88:91], v[248:251], v[124:127], v[88:91]\n\t"
    "v_mfma_f32_16x16x32_bf16 v[92:95], v[252:255], v[124:127], v[92:95]\n\t"
    "s_cmp_eq_u32 %[ps], 0\n\t"
    "s_cbranch_scc1 10f\n\t"
    "global_load_dword %[sv], %[dv], %[db] sc1\n\t"
    "global_load_dword %[su], %[dv], %[db] offset:64 sc1\n\t"
    "10:\n\t"
    "s_mov_b32 %[got], 0\n\t"
    "v_mov_b32 v76, %[fa]\n\t"
    "v_mov_b32 v77, %[fv]\n\t"
    "v_add_u32 v78, 1, v77\n\t"
    "v_mov_b32 v79, %[ga]\n\t"
    "ds_write_b128 %[rw], v[80:83] offset:0\n\t"
    "ds_write_b128 %[rw], v[84:87] offset:1024\n\t"
    "ds_write_b128 %[rw], v[88:91] offset:2048\n\t"
    "ds_write_b128 %[rw], v[92:95] offset:3072\n\t"
    "ds_write_b32 v76, v77\n\t"
    "s_cmp_eq_u32 %[ps], 0\n\t"
    "s_cbranch_scc1 9f\n\t"
    "s_waitcnt vmcnt(0)\n\t"
    "s_branch 9f\n\t"
    "12:\n\t"
    "s_waitcnt lgkmcnt(0)\n\t"
    "9:"
    : [sv] "+&v"(sv), [su] "+&v"(su), [got] "=&s"(got)
    : [ea] "v"(ea), [da] "v"(da), [x] "s"(x), "{v[128:159]}"(W[0]), "{v[160:191]}"(W[1]), "{v[192:223]}"(W[2]), "{v[224:255]}"(W[3]), [lb] "s"(lb), [s0] "s"(s0), [s1] "s"(s1), [av0] "v"(av[0]), [av1] "v"(av[1]), [av2] "v"(av[2]), [av3] "v"(av[3]), [av4] "v"(av[4]), [av5] "v"(av[5]), [av6] "v"(av[6]), [av7] "v"(av[7]), [ps] "s"(ps), [dv] "v"(dv), [db] "s"(db), [rw] "v"(rw), [fa] "s"(fa), [fv] "s"(fv), [ga] "s"(ga), [mu] "s"(mu), [ad] "v"(ad), [th] "s"(th), [tb] "s"(tb)
    : "v96", "v97", "v98", "v99", "v100", "v101", "v102", "v103", "v104", "v105", "v106", "v107", "v108", "v109", "v110", "v111", "v112", "v113", "v114", "v115", "v116", "v117", "v118", "v119", "v120", "v121", "v122", "v123", "v124", "v125", "v126", "v127", "v76", "v77", "v78", "v79", "v80", "v81", "v82", "v83", "v84", "v85", "v86", "v87", "v88", "v89", "v90", "v91", "v92", "v93", "v94", "v95", "vcc", "scc", "m0", "memory");
}
__device__ __forceinline__ void lean_ld1(const u32x32 (&W)[4], unsigned ea, unsigned da, unsigned x, unsigned lb, const char* s0, const char* s1, const unsigned (&av)[8], unsigned ps, unsigned dv, const char* db, unsigned rw, unsigned fa, unsigned fv,
    unsigned ga, unsigned mu, unsigned ad, int th, int tb, unsigned& sv, unsigned& su, unsigned& got) {
  asm volatile("s_setprio 2\n\t"
    "v_mov_b32 v76, %[ga]\n\t"
    "ds_read_b32 v76, v76 offset:24\n\t"
    "ds_read_b128 v[96:99], %[ea] offset:0\n\t"
    "ds_read_b128 v[100:103], %[ea] offset:2048\n\t"
    "ds_read_b128 v[104:107], %[da] offset:0\n\t"
    "ds_read_b128 v[108:111], %[da] offset:2048\n\t"
    "ds_read_b128 v[112:115], %[ea] offset:4096\n\t"
    "ds_read_b128 v[116:119], %[ea] offset:6144\n\t"
    "s_waitcnt lgkmcnt(6)\n\t"
    "v_cmp_ne_u32 vcc, 0, v76\n\t"
    "s_mov_b32 %[got], -1\n\t"
    "s_cbranch_vccnz 12f\n\t"
    "s_waitcnt lgkmcnt(4)\n\t"
    "v_mfma_f32_16x16x32_bf16 v[80:83], v[128:131], v[96:99], 0\n\t"
    "ds_read_b128 v[120:123], %[da] offset:4096\n\t"
    "v_mfma_f32_16x16x32_bf16 v[84:87], v[132:135], v[96:99], 0\n\t"
    "ds_read_b128 v[124:127], %[da] offset:6144\n\t"
    "v_mfma_f32_16x16x32_bf16 v[88:91], v[128:131], v[100:103], 0\n\t"
    "v_mfma_f32_16x16x32_bf16 v[92:95], v[132:135], v[100:103], 0\n\t"
    "s_waitcnt vmcnt(12)\n\t"
    "s_cmp_lt_u32 %[x], 2\n\t"
    "s_cbranch_scc0 41f\n\t"
    "s_waitcnt vmcnt(4)\n\t"
    "41:\n\t"
    "s_barrier\n\t"
    "s_waitcnt lgkmcnt(4)\n\t"
    "v_mfma_f32_16x16x32_bf16 v[80:83], v[136:139], v[104:107], v[80:83]\n\t"
    "ds_read_b128 v[96:99], %[ea] offset:8192\n\t"
    "v_mfma_f32_16x16x32_bf16 v[84:87], v[140:143], v[104:107], v[84:87]\n\t"
    "ds_read_b128 v[100:103], %[ea] offset:10240\n\t"
    "v_mfma_f32_16x16x32_bf16 v[88:91], v[136:139], v[108:111], v[88:91]\n\t"
    "v_mfma_f32_16x16x32_bf16 v[92:95], v[140:143], v[108:111], v[92:95]\n\t"
    "s_waitcnt lgkmcnt(4)\n\t"
    "v_mfma_f32_16x16x32_bf16 v[80:83], v[144:147], v[112:115], v[80:83]\n\t"
    "ds_read_b128 v[104:107], %[da] offset:8192\n\t"
    "v_mfma_f32_16x16x32_bf16 v[84:87], v[148:151], v[112:115], v[84:87]\n\t"
    "ds_read_b128 v[108:111], %[da] offset:10240\n\t"
    "v_mfma_f32_16x16x32_bf16 v[88:91], v[144:147], v[116:119], v[88:91]\n\t"
    "v_mfma_f32_16x16x32_bf16 v[92:95], v[148:151], v[116:119], v[92:95]\n\t"
    "s_waitcnt lgkmcnt(4)\n\t"
    "v_mfma_f32_16x16x32_bf16 v[80:83], v[152:155], v[120:123], v[80:83]\n\t"
    "ds_read_b128 v[112:115], %[ea] offset:12288\n\t"
    "v_mfma_f32_16x16x32_bf16 v[84:87], v[156:159], v[120:123], v[84:87]\n\t"
    "ds_read_b128 v[116:119], %[ea] offset:14336\n\t"
    "v_mfma_f32_16x16x32_bf16 v[88:91], v[152:155], v[124:127], v[88:91]\n\t"
    "v_mfma_f32_16x16x32_bf16 v[92:95], v[156:159], v[124:127], v[92:95]\n\t"
    "s_waitcnt lgkmcnt(4)\n\t"
    "v_mfma_f32_16x16x32_bf16 v[80:83], v[160:163], v[96:99], v[80:83]\n\t"
    "ds_read_b128 v[120:123], %[da] offset:12288\n\t"
    "v_mfma_f32_16x16x32_bf16 v[84:87], v[164:167], v[96:99], v[84:87]\n\t"
    "ds_read_b128 v[124:127], %[da] offset:14336\n\t"
    "v_mfma_f32_16x16x32_bf16 v[88:91], v[160:163], v[100:103], v[88:91]\n\t"
    "v_mfma_f32_16x16x32_bf16 v[92:95], v[164:167], v[100:103], v[92:95]\n\t"
    "s_waitcnt vmcnt(10)\n\t"
    "s_cmp_lt_u32 %[x], 2\n\t"
    "s_cbranch_scc0 45f\n\t"
    "s_waitcnt vmcnt(2)\n\t"
    "45:\n\t"
    "s_barrier\n\t"
    "s_waitcnt lgkmcnt(4)\n\t"
    "v_mfma_f32_16x16x32_bf16 v[80:83], v[168:171], v[104:107], v[80:83]\n\t"
    "ds_read_b128 v[96:99], %[ea] offset:16384\n\t"
    "v_mfma_f32_16x16x32_bf16 v[84:87], v[172:175], v[104:107], v[84:87]\n\t"
    "ds_read_b128 v[100:103], %[ea] offset:18432\n\t"
    "v_mfma_f32_16x16x32_bf16 v[88:91], v[168:171], v[108:111], v[88:91]\n\t"
    "v_mfma_f32_16x16x32_bf16 v[92:95], v[172:175], v[108:111], v[92:95]\n\t"
    "s_waitcnt lgkmcnt(4)\n\t"
    "v_mfma_f32_16x16x32_bf16 v[80:83], v[176:179], v[112:115], v[80:83]\n\t"
    "ds_read_b128 v[104:107], %[da] offset:16384\n\t"
    "v_mfma_f32_16x16x32_bf16 v[84:87], v[180:183], v[112:115], v[84:87]\n\t"
    "ds_read_b128 v[108:111], %[da] offset:18432\n\t"
    "v_mfma_f32_16x16x32_bf16 v[88:91], v[176:179], v[116:119], v[88:91]\n\t"
    "v_mfma_f32_16x16x32_bf16 v[92:95], v[180:183], v[116:119], v[92:95]\n\t"
    "s_waitcnt lgkmcnt(4)\n\t"
    "v_mfma_f32_16x16x32_bf16 v[80:83], v[184:187], v[120:123], v[80:83]\n\t"
    "ds_read_b128 v[112:115], %[ea] offset:20480\n\t"
    "v_mfma_f32_16x16x32_bf16 v[84:87], v[188:191], v[120:123], v[84:87]\n\t"
    "ds_read_b128 v[116:119], %[ea] offset:22528\n\t"
    "v_mfma_f32_16x16x32_bf16 v[88:91], v[184:187], v[124:127], v[88:91]\n\t"
    "v_mfma_f32_16x16x32_bf16 v[92:95], v[188:191], v[124:127], v[92:95]\n\t"
    "s_waitcnt lgkmcnt(4)\n\t"
    "v_mfma_f32_16x16x32_bf16 v[80:83], v[192:195], v[96:99], v[80:83]\n\t"
    "ds_read_b128 v[120:123], %[da] offset:20480\n\t"
    "v_mfma_f32_16x16x32_bf16 v[84:87], v[196:199], v[96:99], v[84:87]\n\t"
    "ds_read_b128 v[124:127], %[da] offset:22528\n\t"
    "v_mfma_f32_16x16x32_bf16 v[88:91], v[192:195], v[100:103], v[88:91]\n\t"
    "v_mfma_f32_16x16x32_bf16 v[92:95], v[196:199], v[100:103], v[92:95]\n\t"
    "s_waitcnt vmcnt(8)\n\t"
    "s_cmp_lt_u32 %[x], 2\n\t"
    "s_cbranch_scc0 49f\n\t"
    "s_waitcnt vmcnt(0)\n\t"
    "49:\n\t"
    "s_barrier\n\t"
    "s_waitcnt lgkmcnt(4)\n\t"
    "v_mfma_f32_16x16x32_bf16 v[80:83], v[200:203], v[104:107], v[80:83]\n\t"
    "ds_read_b128 v[96:99], %[ea] offset:24576\n\t"
    "v_mfma_f32_16x16x32_bf16 v[84:87], v[204:207], v[104:107], v[84:87]\n\t"
    "ds_read_b128 v[100:103], %[ea] offset:26624\n\t"
    "v_mfma_f32_16x16x32_bf16 v[88:91], v[200:203], v[108:111], v[88:91]\n\t"
    "v_mfma_f32_16x16x32_bf16 v[92:95], v[204:207], v[108:111], v[92:95]\n\t"
    "s_waitcnt lgkmcnt(4)\n\t"
    "v_mfma_f32_16x16x32_bf16 v[80:83], v[208:211], v[112:115], v[80:83]\n\t"
    "ds_read_b128 v[104:107], %[da] offset:24576\n\t"
    "v_mfma_f32_16x16x32_bf16 v[84:87], v[212:215], v[112:115], v[84:87]\n\t"
    "ds_read_b128 v[108:111], %[da] offset:26624\n\t"
    "v_mfma_f32_16x16x32_bf16 v[88:91], v[208:211], v[116:119], v[88:91]\n\t"
    "v_mfma_f32_16x16x32_bf16 v[92:95], v[212:215], v[116:119], v[92:95]\n\t"
    "s_cmp_eq_u32 %[ps], 0\n\t"
    "s_cbranch_scc1 10f\n\t"
    "global_load_dword %[sv], %[dv], %[db] sc1\n\t"
    "global_load_dword %[su], %[dv], %[db] offset:64 sc1\n\t"
    "10:\n\t"
    "s_waitcnt lgkmcnt(4)\n\t"
    "v_mfma_f32_16x16x32_bf16 v[80:83], v[216:219], v[120:123], v[80:83]\n\t"
    "ds_read_b128 v[112:115], %[ea] offset:28672\n\t"
    "v_mfma_f32_16x16x32_bf16 v[84:87], v[220:223], v[120:123], v[84:87]\n\t"
    "ds_read_b128 v[116:119], %[ea] offset:30720\n\t"
    "v_mfma_f32_16x16x32_bf16 v[88:91], v[216:219], v[124:127], v[88:91]\n\t"
    "v_mfma_f32_16x16x32_bf16 v[92:95], v[220:223], v[124:127], v[92:95]\n\t"
    "s_waitcnt lgkmcnt(4)\n\t"
    "v_mfma_f32_16x16x32_bf16 v[80:83], v[224:227], v[96:99], v[80:83]\n\t"
    "ds_read_b128 v[120:123], %[da] offset:28672\n\t"
    "v_mfma_f32_16x16x32_bf16 v[84:87], v[228:231], v[96:99], v[84:87]\n\t"
    "ds_read_b128 v[124:127], %[da] offset:30720\n\t"
    "v_mfma_f32_16x16x32_bf16 v[88:91], v[224:227], v[100:103], v[88:91]\n\t"
    "v_mfma_f32_16x16x32_bf16 v[92:95], v[228:231], v[100:103], v[92:95]\n\t"
    "s_waitcnt lgkmcnt(4)\n\t"
    "v_mfma_f32_16x16x32_bf16 v[80:83], v[232:235], v[104:107], v[80:83]\n\t"
    "v_mfma_f32_16x16x32_bf16 v[84:87], v[236:239], v[104:107], v[84:87]\n\t"
    "v_mfma_f32_16x16x32_bf16 v[88:91], v[232:235], v[108:111], v[88:91]\n\t"
    "v_mfma_f32_16x16x32_bf16 v[92:95], v[236:239], v[108:111], v[92:95]\n\t"
    "s_waitcnt lgkmcnt(2)\n\t"
    "v_mfma_f32_16x16x32_bf16 v[80:83], v[240:243], v[112:115], v[80:83]\n\t"
    "v_mfma_f32_16x16x32_bf16 v[84:87], v[244:247], v[112:115], v[84:87]\n\t"
    "v_mfma_f32_16x16x32_bf16 v[88:91], v[240:243], v[116:119], v[88:91]\n\t"
    "v_mfma_f32_16x16x32_bf16 v[92:95], v[244:247], v[116:119], v[92:95]\n\t"
    "s_waitcnt lgkmcnt(0)\n\t"
    "v_mfma_f32_16x16x32_bf16 v[80:83], v[248:251], v[120:123], v[80:83]\n\t"
    "s_memrealtime s[36:37]\n\t"
    "v_mfma_f32_16x16x32_bf16 v[84:87], v[252:255], v[120:123], v[84:87]\n\t"
    "v_mfma_f32_16x16x32_bf16 v[88:91], v[248:251], v[124:127], v[88:91]\n\t"
    "v_mfma_f32_16x16x32_bf16 v[92:95], v[252:255], v[124:127], v[92:95]\n\t"
    "s_mov_b32 %[got], 0\n\t"
    "v_mov_b32 v76, %[fa]\n\t"
    "v_mov_b32 v77, %[fv]\n\t"
    "v_add_u32 v78, 1, v77\n\t"
    "v_mov_b32 v79, %[ga]\n\t"
    "s_waitcnt lgkmcnt(0)\n\t"
    "ds_write_b128 %[rw], v[80:83] offset:0\n\t"
    "ds_write_b128 %[rw], v[84:87] offset:1024\n\t"
    "ds_write_b128 %[rw], v[88:91] offset:2048\n\t"
    "ds_write_b128 %[rw], v[92:95] offset:3072\n\t"
    "ds_write_b32 v76, v77\n\t"
    "s_cmp_eq_u32 %[ps], 0\n\t"
    "s_cbranch_scc1 9f\n\t"
    "s_waitcnt vmcnt(0)\n\t"
    "v_mad_u32_u24 v76, %[sv], %[mu], %[ad]\n\t"
    "v_cmp_ge_i32 vcc, %[th], v76\n\t"
    "s_cbranch_vccz 11f\n\t"
    "s_sub_u32 s36, s36, 20\n\t"
    "v_sub_u32 v76, s36, %[su]\n\t"
    "v_lshrrev_b32 v77, 24, %[su]\n\t"
    "v_lshlrev_b32 v76, 8, v76\n\t"
    "v_ashrrev_i32 v76, 31, v76\n\t"
    "v_add_u32 v77, v77, v76\n\t"
    "v_cmp_lt_i32 s[36:37], %[tb], v77\n\t"
    "s_andn2_b64 vcc, vcc, s[36:37]\n\t"
    "s_cbranch_scc1 9f\n\t"
    "11:\n\t"
    "ds_write2_b32 v79, v78, v78 offset1:1\n\t"
    "s_add_u32 m0, %[lb], 0\n\t"
    "s_nop 0\n\t"
    "global_load_lds_dwordx4 %[av0], %[s0] sc1\n\t"
    "s_add_u32 m0, %[lb], 4096\n\t"
    "s_nop 0\n\t"
    "global_load_lds_dwordx4 %[av2], %[s0] sc1\n\t"
    "s_add_u32 m0, %[lb], 8192\n\t"
    "s_nop 0\n\t"
    "global_load_lds_dwordx4 %[av4], %[s0] sc1\n\t"
    "s_add_u32 m0, %[lb], 12288\n\t"
    "s_nop 0\n\t"
    "global_load_lds_dwordx4 %[av6], %[s0] sc1\n\t"
    "s_add_u32 m0, %[lb], 16384\n\t"
    "s_nop 0\n\t"
    "global_load_lds_dwordx4 %[av0], %[s1] sc1\n\t"
    "s_add_u32 m0, %[lb], 20480\n\t"
    "s_nop 0\n\t"
    "global_load_lds_dwordx4 %[av2], %[s1] sc1\n\t"
    "s_add_u32 m0, %[lb], 24576\n\t"
    "s_nop 0\n\t"
    "global_load_lds_dwordx4 %[av4], %[s1] sc1\n\t"
    "s_add_u32 m0, %[lb], 28672\n\t"
    "s_nop 0\n\t"
    "global_load_lds_dwordx4 %[av6], %[s1] sc1\n\t"
    "s_mov_b32 %[got], 1\n\t"
    "s_branch 9f\n\t"
    "12:\n\t"
    "s_waitcnt lgkmcnt(0)\n\t"
    "9:"
    : [sv] "+&v"(sv), [su] "+&v"(su), [got] "=&s"(got)
    : [ea] "v"(ea), [da] "v"(da), [x] "s"(x), "{v[128:159]}"(W[0]), "{v[160:191]}"(W[1]), "{v[192:223]}"(W[2]), "{v[224:255]}"(W[3]), [lb] "s"(lb), [s0] "s"(s0), [s1] "s"(s1), [av0] "v"(av[0]), [av1] "v"(av[1]), [av2] "v"(av[2]), [av3] "v"(av[3]), [av4] "v"(av[4]), [av5] "v"(av[5]), [av6] "v"(av[6]), [av7] "v"(av[7]), [ps] "s"(ps), [dv] "v"(dv), [db] "s"(db), [rw] "v"(rw), [fa] "s"(fa), [fv] "s"(fv), [ga] "s"(ga), [mu] "s"(mu), [ad] "v"(ad), [th] "s"(th), [tb] "s"(tb)
    : "v96", "v97", "v98", "v99", "v100", "v101", "v102", "v103", "v104", "v105", "v106", "v107", "v108", "v109", "v110", "v111", "v112", "v113", "v114", "v115", "v116", "v117", "v118", "v119", "v120", "v121", "v122", "v123", "v124", "v125", "v126", "v127", "v76", "v77", "v78", "v79", "v80", "v81", "v82", "v83", "v84", "v85", "v86", "v87", "v88", "v89", "v90", "v91", "v92", "v93", "v94", "v95", "vcc", "scc", "m0", "memory", "s36", "s37", "s38", "s39");
}
__device__ __forceinline__ void lean_dm1(const u32x32 (&W)[4], unsigned ea, unsigned da, unsigned x, unsigned lb, const char* s0, const char* s1, const unsigned (&av)[8], unsigned ps, unsigned dv, const char* db, unsigned rw, unsigned fa, unsigned fv,
    unsigned ga, unsigned mu, unsigned ad, int th, int tb, unsigned& sv, unsigned& su, unsigned& got) {
  asm volatile("s_setprio 2\n\t"
    "v_mov_b32 v76, %[ga]\n\t"
    "ds_read_b32 v76, v76 offset:24\n\t"
    "ds_read_b128 v[96:99], %[ea] offset:0\n\t"
    "ds_read_b128 v[100:103], %[ea] offset:2048\n\t"
    "ds_read_b128 v[104:107], %[da] offset:0\n\t"
    "ds_read_b128 v[108:111], %[da] offset:2048\n\t"
    "ds_read_b128 v[112:115], %[ea] offset:4096\n\t"
    "ds_read_b128 v[116:119], %[ea] offset:6144\n\t"
    "s_waitcnt lgkmcnt(6)\n\t"
    "v_cmp_ne_u32 vcc, 0, v76\n\t"
    "s_mov_b32 %[got], -1\n\t"
    "s_cbranch_vccnz 12f\n\t"
    "s_waitcnt lgkmcnt(4)\n\t"
    "v_mfma_f32_16x16x32_bf16 v[80:83], v[128:131], v[96:99], 0\n\t"
    "ds_read_b128 v[120:123], %[da] offset:4096\n\t"
    "s_add_u32 m0, %[lb], 0\n\t"
    "v_mfma_f32_16x16x32_bf16 v[84:87], v[132:135], v[96:99], 0\n\t"
    "ds_read_b128 v[124:127], %[da] offset:6144\n\t"
    "global_load_lds_dwordx4 %[av0], %[s0] sc1\n\t"
    "v_mfma_f32_16x16x32_bf16 v[88:91], v[128:131], v[100:103], 0\n\t"
    "v_mfma_f32_16x16x32_bf16 v[92:95], v[132:135], v[100:103], 0\n\t"
    "s_waitcnt vmcnt(5)\n\t"
    "s_barrier\n\t"
    "s_waitcnt lgkmcnt(4)\n\t"
    "v_mfma_f32_16x16x32_bf16 v[80:83], v[136:139], v[104:107], v[80:83]\n\t"
    "ds_read_b128 v[96:99], %[ea] offset:8192\n\t"
    "s_add_u32 m0, %[lb], 4096\n\t"
    "v_mfma_f32_16x16x32_bf16 v[84:87], v[140:143], v[104:107], v[84:87]\n\t"
    "ds_read_b128 v[100:103], %[ea] offset:10240\n\t"
    "global_load_lds_dwordx4 %[av2], %[s0] sc1\n\t"
    "v_mfma_f32_16x16x32_bf16 v[88:91], v[136:139], v[108:111], v[88:91]\n\t"
    "v_mfma_f32_16x16x32_bf16 v[92:95], v[140:143], v[108:111], v[92:95]\n\t"
    "s_waitcnt lgkmcnt(4)\n\t"
    "v_mfma_f32_16x16x32_bf16 v[80:83], v[144:147], v[112:115], v[80:83]\n\t"
    "ds_read_b128 v[104:107], %[da] offset:8192\n\t"
    "s_add_u32 m0, %[lb], 8192\n\t"
    "v_mfma_f32_16x16x32_bf16 v[84:87], v[148:151], v[112:115], v[84:87]\n\t"
    "ds_read_b128 v[108:111], %[da] offset:10240\n\t"
    "global_load_lds_dwordx4 %[av4], %[s0] sc1\n\t"
    "v_mfma_f32_16x16x32_bf16 v[88:91], v[144:147], v[116:119], v[88:91]\n\t"
    "v_mfma_f32_16x16x32_bf16 v[92:95], v[148:151], v[116:119], v[92:95]\n\t"
    "s_waitcnt lgkmcnt(4)\n\t"
    "v_mfma_f32_16x16x32_bf16 v[80:83], v[152:155], v[120:123], v[80:83]\n\t"
    "ds_read_b128 v[112:115], %[ea] offset:12288\n\t"
    "s_add_u32 m0, %[lb], 12288\n\t"
    "v_mfma_f32_16x16x32_bf16 v[84:87], v[156:159], v[120:123], v[84:87]\n\t"
    "ds_read_b128 v[116:119], %[ea] offset:14336\n\t"
    "global_load_lds_dwordx4 %[av6], %[s0] sc1\n\t"
    "v_mfma_f32_16x16x32_bf16 v[88:91], v[152:155], v[124:127], v[88:91]\n\t"
    "v_mfma_f32_16x16x32_bf16 v[92:95], v[156:159], v[124:127], v[92:95]\n\t"
    "s_waitcnt lgkmcnt(4)\n\t"
    "v_mfma_f32_16x16x32_bf16 v[80:83], v[160:163], v[96:99], v[80:83]\n\t"
    "ds_read_b128 v[120:123], %[da] offset:12288\n\t"
    "s_add_u32 m0, %[lb], 16384\n\t"
    "v_mfma_f32_16x16x32_bf16 v[84:87], v[164:167], v[96:99], v[84:87]\n\t"
    "ds_read_b128 v[124:127], %[da] offset:14336\n\t"
    "global_load_lds_dwordx4 %[av0], %[s1] sc1\n\t"
    "v_mfma_f32_16x16x32_bf16 v[88:91], v[160:163], v[100:103], v[88:91]\n\t"
    "v_mfma_f32_16x16x32_bf16 v[92:95], v[164:167], v[100:103], v[92:95]\n\t"
    "s_waitcnt vmcnt(7)\n\t"
    "s_barrier\n\t"
    "s_waitcnt lgkmcnt(4)\n\t"
    "v_mfma_f32_16x16x32_bf16 v[80:83], v[168:171], v[104:107], v[80:83]\n\t"
    "ds_read_b128 v[96:99], %[ea] offset:16384\n\t"
    "s_add_u32 m0, %[lb], 20480\n\t"
    "v_mfma_f32_16x16x32_bf16 v[84:87], v[172:175], v[104:107], v[84:87]\n\t"
    "ds_read_b128 v[100:103], %[ea] offset:18432\n\t"
    "global_load_lds_dwordx4 %[av2], %[s1] sc1\n\t"
    "v_mfma_f32_16x16x32_bf16 v[88:91], v[168:171], v[108:111], v[88:91]\n\t"
    "v_mfma_f32_16x16x32_bf16 v[92:95], v[172:175], v[108:111], v[92:95]\n\t"
    "s_waitcnt lgkmcnt(4)\n\t"
    "v_mfma_f32_16x16x32_bf16 v[80:83], v[176:179], v[112:115], v[80:83]\n\t"
    "ds_read_b128 v[104:107], %[da] offset:16384\n\t"
    "s_add_u32 m0, %[lb], 24576\n\t"
    "v_mfma_f32_16x16x32_bf16 v[84:87], v[180:183], v[112:115], v[84:87]\n\t"
    "ds_read_b128 v[108:111], %[da] offset:18432\n\t"
    "global_load_lds_dwordx4 %[av4], %[s1] sc1\n\t"
    "v_mfma_f32_16x16x32_bf16 v[88:91], v[176:179], v[116:119], v[88:91]\n\t"
    "v_mfma_f32_16x16x32_bf16 v[92:95], v[180:183], v[116:119], v[92:95]\n\t"
    "s_waitcnt lgkmcnt(4)\n\t"
    "v_mfma_f32_16x16x32_bf16 v[80:83], v[184:187], v[120:123], v[80:83]\n\t"
    "ds_read_b128 v[112:115], %[ea] offset:20480\n\t"
    "s_add_u32 m0, %[lb], 28672\n\t"
    "v_mfma_f32_16x16x32_bf16 v[84:87], v[188:191], v[120:123], v[84:87]\n\t"
    "ds_read_b128 v[116:119], %[ea] offset:22528\n\t"
    "global_load_lds_dwordx4 %[av6], %[s1] sc1\n\t"
    "v_mfma_f32_16x16x32_bf16 v[88:91], v[184:187], v[124:127], v[88:91]\n\t"
    "v_mfma_f32_16x16x32_bf16 v[92:95], v[188:191], v[124:127], v[92:95]\n\t"
    "s_waitcnt lgkmcnt(4)\n\t"
    "v_mfma_f32_16x16x32_bf16 v[80:83], v[192:195], v[96:99], v[80:83]\n\t"
    "ds_read_b128 v[120:123], %[da] offset:20480\n\t"
    "v_mfma_f32_16x16x32_bf16 v[84:87], v[196:199], v[96:99], v[84:87]\n\t"
    "ds_read_b128 v[124:127], %[da] offset:22528\n\t"
    "v_mfma_f32_16x16x32_bf16 v[88:91], v[192:195], v[100:103], v[88:91]\n\t"
    "v_mfma_f32_16x16x32_bf16 v[92:95], v[196:199], v[100:103], v[92:95]\n\t"
    "s_waitcnt vmcnt(8)\n\t"
    "s_barrier\n\t"
    "s_waitcnt lgkmcnt(4)\n\t"
    "v_mfma_f32_16x16x32_bf16 v[80:83], v[200:203], v[104:107], v[80:83]\n\t"
    "ds_read_b128 v[96:99], %[ea] offset:24576\n\t"
    "v_mfma_f32_16x16x32_bf16 v[84:87], v[204:207], v[104:107], v[84:87]\n\t"
    "ds_read_b128 v[100:103], %[ea] offset:26624\n\t"
    "v_mfma_f32_16x16x32_bf16 v[88:91], v[200:203], v[108:111], v[88:91]\n\t"
    "v_mfma_f32_16x16x32_bf16 v[92:95], v[204:207], v[108:111], v[92:95]\n\t"
    "s_waitcnt lgkmcnt(4)\n\t"
    "v_mfma_f32_16x16x32_bf16 v[80:83], v[208:211], v[112:115], v[80:83]\n\t"
    "ds_read_b128 v[104:107], %[da] offset:24576\n\t"
    "v_mfma_f32_16x16x32_bf16 v[84:87], v[212:215], v[112:115], v[84:87]\n\t"
    "ds_read_b128 v[108:111], %[da] offset:26624\n\t"
    "v_mfma_f32_16x16x32_bf16 v[88:91], v[208:211], v[116:119], v[88:91]\n\t"
    "v_mfma_f32_16x16x32_bf16 v[92:95], v[212:215], v[116:119], v[92:95]\n\t"
    "s_waitcnt lgkmcnt(4)\n\t"
    "v_mfma_f32_16x16x32_bf16 v[80:83], v[216:219], v[120:123], v[80:83]\n\t"
    "ds_read_b128 v[112:115], %[ea] offset:28672\n\t"
    "v_mfma_f32_16x16x32_bf16 v[84:87], v[220:223], v[120:123], v[84:87]\n\t"
    "ds_read_b128 v[116:119], %[ea] offset:30720\n\t"
    "v_mfma_f32_16x16x32_bf16 v[88:91], v[216:219], v[124:127], v[88:91]\n\t"
    "v_mfma_f32_16x16x32_bf16 v[92:95], v[220:223], v[124:127], v[92:95]\n\t"
    "s_waitcnt lgkmcnt(4)\n\t"
    "v_mfma_f32_16x16x32_bf16 v[80:83], v[224:227], v[96:99], v[80:83]\n\t"
    "ds_read_b128 v[120:123], %[da] offset:28672\n\t"
    "v_mfma_f32_16x16x32_bf16 v[84:87], v[228:231], v[96:99], v[84:87]\n\t"
    "ds_read_b128 v[124:127], %[da] offset:30720\n\t"
    "v_mfma_f32_16x16x32_bf16 v[88:91], v[224:227], v[100:103], v[88:91]\n\t"
    "v_mfma_f32_16x16x32_bf16 v[92:95], v[228:231], v[100:103], v[92:95]\n\t"
    "s_waitcnt lgkmcnt(4)\n\t"
    "v_mfma_f32_16x16x32_bf16 v[80:83], v[232:235], v[104:107], v[80:83]\n\t"
    "v_mfma_f32_16x16x32_bf16 v[84:87], v[236:239], v[104:107], v[84:87]\n\t"
    "v_mfma_f32_16x16x32_bf16 v[88:91], v[232:235], v[108:111], v[88:91]\n\t"
    "v_mfma_f32_16x16x32_bf16 v[92:95], v[236:239], v[108:111], v[92:95]\n\t"
    "s_waitcnt lgkmcnt(2)\n\t"
    "v_mfma_f32_16x16x32_bf16 v[80:83], v[240:243], v[112:115], v[80:83]\n\t"
    "v_mfma_f32_16x16x32_bf16 v[84:87], v[244:247], v[112:115], v[84:87]\n\t"
    "v_mfma_f32_16x16x32_bf16 v[88:91], v[240:243], v[116:119], v[88:91]\n\t"
    "v_mfma_f32_16x16x32_bf16 v[92:95], v[244:247], v[116:119], v[92:95]\n\t"
    "s_waitcnt lgkmcnt(0)\n\t"
    "v_mfma_f32_16x16x32_bf16 v[80:83], v[248:251], v[120:123], v[80:83]\n\t"
    "v_mfma_f32_16x16x32_bf16 v[84:87], v[252:255], v[120:123], v[84:87]\n\t"
    "v_mfma_f32_16x16x32_bf16 v[88:91], v[248:251], v[124:127], v[88:91]\n\t"
    "v_mfma_f32_16x16x32_bf16 v[92:95], v[252:255], v[124:127], v[92:95]\n\t"
    "s_cmp_eq_u32 %[ps], 0\n\t"
    "s_cbranch_scc1 10f\n\t"
    "global_load_dword %[sv], %[dv], %[db] sc1\n\t"
    "global_load_dword %[su], %[dv], %[db] offset:64 sc1\n\t"
    "10:\n\t"
    "s_mov_b32 %[got], 0\n\t"
    "v_mov_b32 v76, %[fa]\n\t"
    "v_mov_b32 v77, %[fv]\n\t"
    "v_add_u32 v78, 1, v77\n\t"
    "v_mov_b32 v79, %[ga]\n\t"
    "ds_write_b128 %[rw], v[80:83] offset:0\n\t"
    "ds_write_b128 %[rw], v[84:87] offset:1024\n\t"
    "ds_write_b128 %[rw], v[88:91] offset:2048\n\t"
    "ds_write_b128 %[rw], v[92:95] offset:3072\n\t"
    "ds_write_b32 v76, v77\n\t"
    "s_cmp_eq_u32 %[ps], 0\n\t"
    "s_cbranch_scc1 9f\n\t"
    "s_waitcnt vmcnt(0)\n\t"
    "s_branch 9f\n\t"
    "12:\n\t"
    "s_waitcnt lgkmcnt(0)\n\t"
    "9:"
    : [sv] "+&v"(sv), [su] "+&v"(su), [got] "=&s"(got)
    : [ea] "v"(ea), [da] "v"(da), [x] "s"(x), "{v[128:159]}"(W[0]), "{v[160:191]}"(W[1]), "{v[192:223]}"(W[2]), "{v[224:255]}"(W[3]), [lb] "s"(lb), [s0] "s"(s0), [s1] "s"(s1), [av0] "v"(av[0]), [av1] "v"(av[1]), [av2] "v"(av[2]), [av3] "v"(av[3]), [av4] "v"(av[4]), [av5] "v"(av[5]), [av6] "v"(av[6]), [av7] "v"(av[7]), [ps] "s"(ps), [dv] "v"(dv), [db] "s"(db), [rw] "v"(rw), [fa] "s"(fa), [fv] "s"(fv), [ga] "s"(ga), [mu] "s"(mu), [ad] "v"(ad), [th] "s"(th), [tb] "s"(tb)
    : "v96", "v97", "v98", "v99", "v100", "v101", "v102", "v103", "v104", "v105", "v106", "v107", "v108", "v109", "v110", "v111", "v112", "v113", "v114", "v115", "v116", "v117", "v118", "v119", "v120", "v121", "v122", "v123", "v124", "v125", "v126", "v127", "v76", "v77", "v78", "v79", "v80", "v81", "v82", "v83", "v84", "v85", "v86", "v87", "v88", "v89", "v90", "v91", "v92", "v93", "v94", "v95", "vcc", "scc", "m0", "memory");
}
__device__ __forceinline__ void lean_st0(const u32x32 (&W)[4], unsigned ea, unsigned da, unsigned x, u32x16& pd, unsigned pv, unsigned cn, const char* pg, unsigned bl, unsigned& tk, unsigned& st) {
  asm volatile("v_mov_b32 v76, %[cn]\n\t"
    "ds_read_b32 v76, v76 offset:28\n\t"
    "ds_read_b128 v[96:99], %[ea] offset:0\n\t"
    "ds_read_b128 v[100:103], %[ea] offset:2048\n\t"
    "ds_read_b128 v[104:107], %[da] offset:0\n\t"
    "ds_read_b128 v[108:111], %[da] offset:2048\n\t"
    "ds_read_b128 v[112:115], %[ea] offset:4096\n\t"
    "ds_read_b128 v[116:119], %[ea] offset:6144\n\t"
    "s_waitcnt lgkmcnt(6)\n\t"
    "v_cmp_ne_u32 vcc, 0, v76\n\t"
    "s_mov_b32 %[st], -1\n\t"
    "s_cbranch_vccnz 12f\n\t"
    "s_waitcnt lgkmcnt(4)\n\t"
    "v_mfma_f32_16x16x32_bf16 v[80:83], v[128:131], v[96:99], 0\n\t"
    "ds_read_b128 v[120:123], %[da] offset:4096\n\t"
    "v_mfma_f32_16x16x32_bf16 v[84:87], v[132:135], v[96:99], 0\n\t"
    "ds_read_b128 v[124:127], %[da] offset:6144\n\t"
    "v_mfma_f32_16x16x32_bf16 v[88:91], v[128:131], v[100:103], 0\n\t"
    "v_mfma_f32_16x16x32_bf16 v[92:95], v[132:135], v[100:103], 0\n\t"
    "s_barrier\n\t"
    "s_bitcmp1_b32 %[x], 2\n\t"
    "s_cbranch_scc1 7f\n\t"
    "8:\n\t"
    "s_bitcmp1_b32 %[x], 3\n\t"
    "s_cbranch_scc1 13f\n\t"
    "14:\n\t"
    "s_waitcnt lgkmcnt(4)\n\t"
    "v_mfma_f32_16x16x32_bf16 v[80:83], v[136:139], v[104:107], v[80:83]\n\t"
    "ds_read_b128 v[96:99], %[ea] offset:8192\n\t"
    "v_mfma_f32_16x16x32_bf16 v[84:87], v[140:143], v[104:107], v[84:87]\n\t"
    "ds_read_b128 v[100:103], %[ea] offset:10240\n\t"
    "v_mfma_f32_16x16x32_bf16 v[88:91], v[136:139], v[108:111], v[88:91]\n\t"
    "v_mfma_f32_16x16x32_bf16 v[92:95], v[140:143], v[108:111], v[92:95]\n\t"
    "s_waitcnt lgkmcnt(4)\n\t"
    "v_mfma_f32_16x16x32_bf16 v[80:83], v[144:147], v[112:115], v[80:83]\n\t"
    "ds_read_b128 v[104:107], %[da] offset:8192\n\t"
    "v_mfma_f32_16x16x32_bf16 v[84:87], v[148:151], v[112:115], v[84:87]\n\t"
    "ds_read_b128 v[108:111], %[da] offset:10240\n\t"
    "v_mfma_f32_16x16x32_bf16 v[88:91], v[144:147], v[116:119], v[88:91]\n\t"
    "v_mfma_f32_16x16x32_bf16 v[92:95], v[148:151], v[116:119], v[92:95]\n\t"
    "s_waitcnt lgkmcnt(4)\n\t"
    "v_mfma_f32_16x16x32_bf16 v[80:83], v[152:155], v[120:123], v[80:83]\n\t"
    "ds_read_b128 v[112:115], %[ea] offset:12288\n\t"
    "v_mfma_f32_16x16x32_bf16 v[84:87], v[156:159], v[120:123], v[84:87]\n\t"
    "ds_read_b128 v[116:119], %[ea] offset:14336\n\t"
    "v_mfma_f32_16x16x32_bf16 v[88:91], v[152:155], v[124:127], v[88:91]\n\t"
    "v_mfma_f32_16x16x32_bf16 v[92:95], v[156:159], v[124:127], v[92:95]\n\t"
    "s_waitcnt lgkmcnt(4)\n\t"
    "v_mfma_f32_16x16x32_bf16 v[80:83], v[160:163], v[96:99], v[80:83]\n\t"
    "ds_read_b128 v[120:123], %[da] offset:12288\n\t"
    "v_mfma_f32_16x16x32_bf16 v[84:87], v[164:167], v[96:99], v[84:87]\n\t"
    "ds_read_b128 v[124:127], %[da] offset:14336\n\t"
    "v_mfma_f32_16x16x32_bf16 v[88:91], v[160:163], v[100:103], v[88:91]\n\t"
    "v_mfma_f32_16x16x32_bf16 v[92:95], v[164:167], v[100:103], v[92:95]\n\t"
    "s_barrier\n\t"
    "s_waitcnt lgkmcnt(4)\n\t"
    "v_mfma_f32_16x16x32_bf16 v[80:83], v[168:171], v[104:107], v[80:83]\n\t"
    "ds_read_b128 v[96:99], %[ea] offset:16384\n\t"
    "v_mfma_f32_16x16x32_bf16 v[84:87], v[172:175], v[104:107], v[84:87]\n\t"
    "ds_read_b128 v[100:103], %[ea] offset:18432\n\t"
    "v_mfma_f32_16x16x32_bf16 v[88:91], v[168:171], v[108:111], v[88:91]\n\t"
    "v_mfma_f32_16x16x32_bf16 v[92:95], v[172:175], v[108:111], v[92:95]\n\t"
    "s_waitcnt lgkmcnt(4)\n\t"
    "v_mfma_f32_16x16x32_bf16 v[80:83], v[176:179], v[112:115], v[80:83]\n\t"
    "ds_read_b128 v[104:107], %[da] offset:16384\n\t"
    "v_mfma_f32_16x16x32_bf16 v[84:87], v[180:183], v[112:115], v[84:87]\n\t"
    "ds_read_b128 v[108:111], %[da] offset:18432\n\t"
    "v_mfma_f32_16x16x32_bf16 v[88:91], v[176:179], v[116:119], v[88:91]\n\t"
    "v_mfma_f32_16x16x32_bf16 v[92:95], v[180:183], v[116:119], v[92:95]\n\t"
    "s_waitcnt lgkmcnt(4)\n\t"
    "v_mfma_f32_16x16x32_bf16 v[80:83], v[184:187], v[120:123], v[80:83]\n\t"
    "ds_read_b128 v[112:115], %[ea] offset:20480\n\t"
    "v_mfma_f32_16x16x32_bf16 v[84:87], v[188:191], v[120:123], v[84:87]\n\t"
    "ds_read_b128 v[116:119], %[ea] offset:22528\n\t"
    "v_mfma_f32_16x16x32_bf16 v[88:91], v[184:187], v[124:127], v[88:91]\n\t"
    "v_mfma_f32_16x16x32_bf16 v[92:95], v[188:191], v[124:127], v[92:95]\n\t"
    "s_waitcnt lgkmcnt(4)\n\t"
    "v_mfma_f32_16x16x32_bf16 v[80:83], v[192:195], v[96:99], v[80:83]\n\t"
    "ds_read_b128 v[120:123], %[da] offset:20480\n\t"
    "v_mfma_f32_16x16x32_bf16 v[84:87], v[196:199], v[96:99], v[84:87]\n\t"
    "ds_read_b128 v[124:127], %[da] offset:22528\n\t"
    "v_mfma_f32_16x16x32_bf16 v[88:91], v[192:195], v[100:103], v[88:91]\n\t"
    "v_mfma_f32_16x16x32_bf16 v[92:95], v[196:199], v[100:103], v[92:95]\n\t"
    "s_barrier\n\t"
    "s_and_b32 %[st], %[x], 3\n\t"
    "s_cbranch_scc1 3f\n\t"
    "4:\n\t"
    "s_waitcnt lgkmcnt(4)\n\t"
    "v_mfma_f32_16x16x32_bf16 v[80:83], v[200:203], v[104:107], v[80:83]\n\t"
    "ds_read_b128 v[96:99], %[ea] offset:24576\n\t"
    "v_mfma_f32_16x16x32_bf16 v[84:87], v[204:207], v[104:107], v[84:87]\n\t"
    "ds_read_b128 v[100:103], %[ea] offset:26624\n\t"
    "v_mfma_f32_16x16x32_bf16 v[88:91], v[200:203], v[108:111], v[88:91]\n\t"
    "v_mfma_f32_16x16x32_bf16 v[92:95], v[204:207], v[108:111], v[92:95]\n\t"
    "s_waitcnt lgkmcnt(4)\n\t"
    "v_mfma_f32_16x16x32_bf16 v[80:83], v[208:211], v[112:115], v[80:83]\n\t"
    "ds_read_b128 v[104:107], %[da] offset:24576\n\t"
    "v_mfma_f32_16x16x32_bf16 v[84:87], v[212:215], v[112:115], v[84:87]\n\t"
    "ds_read_b128 v[108:111], %[da] offset:26624\n\t"
    "v_mfma_f32_16x16x32_bf16 v[88:91], v[208:211], v[116:119], v[88:91]\n\t"
    "v_mfma_f32_16x16x32_bf16 v[92:95], v[212:215], v[116:119], v[92:95]\n\t"
    "s_waitcnt lgkmcnt(4)\n\t"
    "v_mfma_f32_16x16x32_bf16 v[80:83], v[216:219], v[120:123], v[80:83]\n\t"
    "ds_read_b128 v[112:115], %[ea] offset:28672\n\t"
    "v_mfma_f32_16x16x32_bf16 v[84:87], v[220:223], v[120:123], v[84:87]\n\t"
    "ds_read_b128 v[116:119], %[ea] offset:30720\n\t"
    "v_mfma_f32_16x16x32_bf16 v[88:91], v[216:219], v[124:127], v[88:91]\n\t"
    "v_mfma_f32_16x16x32_bf16 v[92:95], v[220:223], v[124:127], v[92:95]\n\t"
    "s_waitcnt lgkmcnt(4)\n\t"
    "v_mfma_f32_16x16x32_bf16 v[80:83], v[224:227], v[96:99], v[80:83]\n\t"
    "ds_read_b128 v[120:123], %[da] offset:28672\n\t"
    "v_mfma_f32_16x16x32_bf16 v[84:87], v[228:231], v[96:99], v[84:87]\n\t"
    "ds_read_b128 v[124:127], %[da] offset:30720\n\t"
    "v_mfma_f32_16x16x32_bf16 v[88:91], v[224:227], v[100:103], v[88:91]\n\t"
    "v_mfma_f32_16x16x32_bf16 v[92:95], v[228:231], v[100:103], v[92:95]\n\t"
    "s_waitcnt lgkmcnt(4)\n\t"
    "v_mfma_f32_16x16x32_bf16 v[80:83], v[232:235], v[104:107], v[80:83]\n\t"
    "v_mfma_f32_16x16x32_bf16 v[84:87], v[236:239], v[104:107], v[84:87]\n\t"
    "v_mfma_f32_16x16x32_bf16 v[88:91], v[232:235], v[108:111], v[88:91]\n\t"
    "v_mfma_f32_16x16x32_bf16 v[92:95], v[236:239], v[108:111], v[92:95]\n\t"
    "s_waitcnt lgkmcnt(2)\n\t"
    "v_mfma_f32_16x16x32_bf16 v[80:83], v[240:243], v[112:115], v[80:83]\n\t"
    "v_mfma_f32_16x16x32_bf16 v[84:87], v[244:247], v[112:115], v[84:87]\n\t"
    "v_mfma_f32_16x16x32_bf16 v[88:91], v[240:243], v[116:119], v[88:91]\n\t"
    "v_mfma_f32_16x16x32_bf16 v[92:95], v[244:247], v[116:119], v[92:95]\n\t"
    "s_waitcnt lgkmcnt(0)\n\t"
    "v_mfma_f32_16x16x32_bf16 v[80:83], v[248:251], v[120:123], v[80:83]\n\t"
    "v_mfma_f32_16x16x32_bf16 v[84:87], v[252:255], v[120:123], v[84:87]\n\t"
    "v_mfma_f32_16x16x32_bf16 v[88:91], v[248:251], v[124:127], v[88:91]\n\t"
    "v_mfma_f32_16x16x32_bf16 v[92:95], v[252:255], v[124:127], v[92:95]\n\t"
    "s_mov_b32 %[st], 0\n\t"
    "s_nop 5\n\t"
    "s_branch 9f\n\t"
    "13:\n\t"
    "s_memrealtime s[36:37]\n\t"
    "v_mov_b32 v79, 0\n\t"
    "s_waitcnt lgkmcnt(0)\n\t"
    "s_mov_b32 %[tk], s36\n\t"
    "s_add_u32 s36, s36, %[bl]\n\t"
    "s_and_b32 s36, s36, 0xffffff\n\t"
    "s_and_b32 s37, %[bl], 0xff000000\n\t"
    "s_or_b32 s36, s36, s37\n\t"
    "v_mov_b32 v78, s36\n\t"
    "s_mov_b64 exec, 1\n\t"
    "global_store_dword v79, v78, %[pg] offset:64 sc1\n\t"
    "s_mov_b64 exec, -1\n\t"
    "s_branch 14b\n\t"
    "3:\n\t"
    "7:\n\t"
    "s_and_b32 %[st], %[pv], 1\n\t"
    "s_mul_i32 %[st], %[st], 36\n\t"
    "s_add_u32 %[st], %[st], %[cn]\n\t"
    "v_mov_b32 v76, %[st]\n\t"
    "v_mov_b32 v77, 1\n\t"
    "v_mov_b32 v78, %[pv]\n\t"
    "v_mov_b32 v79, 0\n\t"
    "s_waitcnt vmcnt(0)\n\t"
    "s_mov_b64 exec, 1\n\t"
    "ds_add_rtn_u32 v77, v76, v77\n\t"
    "s_waitcnt lgkmcnt(0)\n\t"
    "v_readfirstlane_b32 %[st], v77\n\t"
    "s_and_b32 %[st], %[st], 3\n\t"
    "s_cmp_lg_u32 %[st], 3\n\t"
    "s_cbranch_scc1 6f\n\t"
    "global_store_dword v79, v78, %[pg] sc1\n\t"
    "6:\n\t"
    "s_mov_b64 exec, -1\n\t"
    "s_bitcmp1_b32 %[x], 2\n\t"
    "s_cbranch_scc1 8b\n\t"
    "s_branch 4b\n\t"
    "12:\n\t"
    "s_waitcnt lgkmcnt(0)\n\t"
    "9:"
    : "=&{v[80:95]}"(pd), [st] "=&s"(st), [tk] "+&s"(tk)
    : [ea] "v"(ea), [da] "v"(da), [x] "s"(x), "{v[128:159]}"(W[0]), "{v[160:191]}"(W[1]), "{v[192:223]}"(W[2]), "{v[224:255]}"(W[3]), [pv] "s"(pv), [cn] "s"(cn), [pg] "s"(pg), [bl] "s"(bl)
    : "v96", "v97", "v98", "v99", "v100", "v101", "v102", "v103", "v104", "v105", "v106", "v107", "v108", "v109", "v110", "v111", "v112", "v113", "v114", "v115", "v116", "v117", "v118", "v119", "v120", "v121", "v122", "v123", "v124", "v125", "v126", "v127", "v76", "v77", "v78", "v79", "vcc", "scc", "m0", "memory", "s36", "s37", "s38", "s39");
}
__device__ __forceinline__ void lean_ep0(const u32x32 (&W)[4], unsigned ea, unsigned da, unsigned x, unsigned pv, unsigned cn, const char* pg, unsigned bl, unsigned& tk, unsigned& st,
    unsigned rw, unsigned fa, unsigned fv, const char* cb, unsigned co0, unsigned co1, unsigned& bad, unsigned& pk) {
  asm volatile("v_mov_b32 v76, %[cn]\n\t"
    "ds_read_b32 v76, v76 offset:28\n\t"
    "ds_read_b128 v[96:99], %[ea] offset:0\n\t"
    "ds_read_b128 v[100:103], %[ea] offset:2048\n\t"
    "ds_read_b128 v[104:107], %[da] offset:0\n\t"
    "ds_read_b128 v[108:111], %[da] offset:2048\n\t"
    "ds_read_b128 v[112:115], %[ea] offset:4096\n\t"
    "ds_read_b128 v[116:119], %[ea] offset:6144\n\t"
    "s_waitcnt lgkmcnt(6)\n\t"
    "v_cmp_ne_u32 vcc, 0, v76\n\t"
    "s_mov_b32 %[st], -1\n\t"
    "s_cbranch_vccnz 12f\n\t"
    "s_waitcnt lgkmcnt(4)\n\t"
    "v_mfma_f32_16x16x32_bf16 v[80:83], v[128:131], v[96:99], 0\n\t"
    "ds_read_b128 v[120:123], %[da] offset:4096\n\t"
    "v_mfma_f32_16x16x32_bf16 v[84:87], v[132:135], v[96:99], 0\n\t"
    "ds_read_b128 v[124:127], %[da] offset:6144\n\t"
    "v_mfma_f32_16x16x32_bf16 v[88:91], v[128:131], v[100:103], 0\n\t"
    "v_mfma_f32_16x16x32_bf16 v[92:95], v[132:135], v[100:103], 0\n\t"
    "s_barrier\n\t"
    "s_bitcmp1_b32 %[x], 2\n\t"
    "s_cbranch_scc1 7f\n\t"
    "8:\n\t"
    "s_bitcmp1_b32 %[x], 3\n\t"
    "s_cbranch_scc1 13f\n\t"
    "14:\n\t"
    "s_waitcnt lgkmcnt(4)\n\t"
    "v_mfma_f32_16x16x32_bf16 v[80:83], v[136:139], v[104:107], v[80:83]\n\t"
    "ds_read_b128 v[96:99], %[ea] offset:8192\n\t"
    "v_mfma_f32_16x16x32_bf16 v[84:87], v[140:143], v[104:107], v[84:87]\n\t"
    "ds_read_b128 v[100:103], %[ea] offset:10240\n\t"
    "v_mfma_f32_16x16x32_bf16 v[88:91], v[136:139], v[108:111], v[88:91]\n\t"
    "v_mfma_f32_16x16x32_bf16 v[92:95], v[140:143], v[108:111], v[92:95]\n\t"
    "s_waitcnt lgkmcnt(4)\n\t"
    "v_mfma_f32_16x16x32_bf16 v[80:83], v[144:147], v[112:115], v[80:83]\n\t"
    "ds_read_b128 v[104:107], %[da] offset:8192\n\t"
    "v_mfma_f32_16x16x32_bf16 v[84:87], v[148:151], v[112:115], v[84:87]\n\t"
    "ds_read_b128 v[108:111], %[da] offset:10240\n\t"
    "v_mfma_f32_16x16x32_bf16 v[88:91], v[144:147], v[116:119], v[88:91]\n\t"
    "v_mfma_f32_16x16x32_bf16 v[92:95], v[148:151], v[116:119], v[92:95]\n\t"
    "s_waitcnt lgkmcnt(4)\n\t"
    "v_mfma_f32_16x16x32_bf16 v[80:83], v[152:155], v[120:123], v[80:83]\n\t"
    "ds_read_b128 v[112:115], %[ea] offset:12288\n\t"
    "v_mfma_f32_16x16x32_bf16 v[84:87], v[156:159], v[120:123], v[84:87]\n\t"
    "ds_read_b128 v[116:119], %[ea] offset:14336\n\t"
    "v_mfma_f32_16x16x32_bf16 v[88:91], v[152:155], v[124:127], v[88:91]\n\t"
    "v_mfma_f32_16x16x32_bf16 v[92:95], v[156:159], v[124:127], v[92:95]\n\t"
    "s_waitcnt lgkmcnt(4)\n\t"
    "v_mfma_f32_16x16x32_bf16 v[80:83], v[160:163], v[96:99], v[80:83]\n\t"
    "ds_read_b128 v[120:123], %[da] offset:12288\n\t"
    "v_mfma_f32_16x16x32_bf16 v[84:87], v[164:167], v[96:99], v[84:87]\n\t"
    "ds_read_b128 v[124:127], %[da] offset:14336\n\t"
    "v_mfma_f32_16x16x32_bf16 v[88:91], v[160:163], v[100:103], v[88:91]\n\t"
    "v_mfma_f32_16x16x32_bf16 v[92:95], v[164:167], v[100:103], v[92:95]\n\t"
    "s_barrier\n\t"
    "s_waitcnt lgkmcnt(4)\n\t"
    "v_mfma_f32_16x16x32_bf16 v[80:83], v[168:171], v[104:107], v[80:83]\n\t"
    "ds_read_b128 v[96:99], %[ea] offset:16384\n\t"
    "v_mfma_f32_16x16x32_bf16 v[84:87], v[172:175], v[104:107], v[84:87]\n\t"
    "ds_read_b128 v[100:103], %[ea] offset:18432\n\t"
    "v_mfma_f32_16x16x32_bf16 v[88:91], v[168:171], v[108:111], v[88:91]\n\t"
    "v_mfma_f32_16x16x32_bf16 v[92:95], v[172:175], v[108:111], v[92:95]\n\t"
    "s_waitcnt lgkmcnt(4)\n\t"
    "v_mfma_f32_16x16x32_bf16 v[80:83], v[176:179], v[112:115], v[80:83]\n\t"
    "ds_read_b128 v[104:107], %[da] offset:16384\n\t"
    "v_mfma_f32_16x16x32_bf16 v[84:87], v[180:183], v[112:115], v[84:87]\n\t"
    "ds_read_b128 v[108:111], %[da] offset:18432\n\t"
    "v_mfma_f32_16x16x32_bf16 v[88:91], v[176:179], v[116:119], v[88:91]\n\t"
    "v_mfma_f32_16x16x32_bf16 v[92:95], v[180:183], v[116:119], v[92:95]\n\t"
    "s_waitcnt lgkmcnt(4)\n\t"
    "v_mfma_f32_16x16x32_bf16 v[80:83], v[184:187], v[120:123], v[80:83]\n\t"
    "ds_read_b128 v[112:115], %[ea] offset:20480\n\t"
    "v_mfma_f32_16x16x32_bf16 v[84:87], v[188:191], v[120:123], v[84:87]\n\t"
    "ds_read_b128 v[116:119], %[ea] offset:22528\n\t"
    "v_mfma_f32_16x16x32_bf16 v[88:91], v[184:187], v[124:127], v[88:91]\n\t"
    "v_mfma_f32_16x16x32_bf16 v[92:95], v[188:191], v[124:127], v[92:95]\n\t"
    "s_waitcnt lgkmcnt(4)\n\t"
    "v_mfma_f32_16x16x32_bf16 v[80:83], v[192:195], v[96:99], v[80:83]\n\t"
    "ds_read_b128 v[120:123], %[da] offset:20480\n\t"
    "v_mfma_f32_16x16x32_bf16 v[84:87], v[196:199], v[96:99], v[84:87]\n\t"
    "ds_read_b128 v[124:127], %[da] offset:22528\n\t"
    "v_mfma_f32_16x16x32_bf16 v[88:91], v[192:195], v[100:103], v[88:91]\n\t"
    "v_mfma_f32_16x16x32_bf16 v[92:95], v[196:199], v[100:103], v[92:95]\n\t"
    "s_barrier\n\t"
    "s_and_b32 %[st], %[x], 3\n\t"
    "s_cbranch_scc1 3f\n\t"
    "4:\n\t"
    "s_waitcnt lgkmcnt(4)\n\t"
    "v_mfma_f32_16x16x32_bf16 v[80:83], v[200:203], v[104:107], v[80:83]\n\t"
    "ds_read_b128 v[96:99], %[ea] offset:24576\n\t"
    "v_mfma_f32_16x16x32_bf16 v[84:87], v[204:207], v[104:107], v[84:87]\n\t"
    "ds_read_b128 v[100:103], %[ea] offset:26624\n\t"
    "v_mfma_f32_16x16x32_bf16 v[88:91], v[200:203], v[108:111], v[88:91]\n\t"
    "v_mfma_f32_16x16x32_bf16 v[92:95], v[204:207], v[108:111], v[92:95]\n\t"
    "s_waitcnt lgkmcnt(4)\n\t"
    "v_mfma_f32_16x16x32_bf16 v[80:83], v[208:211], v[112:115], v[80:83]\n\t"
    "ds_read_b128 v[104:107], %[da] offset:24576\n\t"
    "v_mfma_f32_16x16x32_bf16 v[84:87], v[212:215], v[112:115], v[84:87]\n\t"
    "ds_read_b128 v[108:111], %[da] offset:26624\n\t"
    "v_mfma_f32_16x16x32_bf16 v[88:91], v[208:211], v[116:119], v[88:91]\n\t"
    "v_mfma_f32_16x16x32_bf16 v[92:95], v[212:215], v[116:119], v[92:95]\n\t"
    "s_waitcnt lgkmcnt(4)\n\t"
    "v_mfma_f32_16x16x32_bf16 v[80:83], v[216:219], v[120:123], v[80:83]\n\t"
    "ds_read_b128 v[112:115], %[ea] offset:28672\n\t"
    "v_mfma_f32_16x16x32_bf16 v[84:87], v[220:223], v[120:123], v[84:87]\n\t"
    "ds_read_b128 v[116:119], %[ea] offset:30720\n\t"
    "v_mfma_f32_16x16x32_bf16 v[88:91], v[216:219], v[124:127], v[88:91]\n\t"
    "v_mfma_f32_16x16x32_bf16 v[92:95], v[220:223], v[124:127], v[92:95]\n\t"
    "s_waitcnt lgkmcnt(4)\n\t"
    "v_mfma_f32_16x16x32_bf16 v[80:83], v[224:227], v[96:99], v[80:83]\n\t"
    "ds_read_b128 v[120:123], %[da] offset:28672\n\t"
    "v_mfma_f32_16x16x32_bf16 v[84:87], v[228:231], v[96:99], v[84:87]\n\t"
    "ds_read_b128 v[124:127], %[da] offset:30720\n\t"
    "v_mfma_f32_16x16x32_bf16 v[88:91], v[224:227], v[100:103], v[88:91]\n\t"
    "v_mfma_f32_16x16x32_bf16 v[92:95], v[228:231], v[100:103], v[92:95]\n\t"
    "s_waitcnt lgkmcnt(4)\n\t"
    "v_mfma_f32_16x16x32_bf16 v[80:83], v[232:235], v[104:107], v[80:83]\n\t"
    "v_mfma_f32_16x16x32_bf16 v[84:87], v[236:239], v[104:107], v[84:87]\n\t"
    "v_mfma_f32_16x16x32_bf16 v[88:91], v[232:235], v[108:111], v[88:91]\n\t"
    "v_mfma_f32_16x16x32_bf16 v[92:95], v[236:239], v[108:111], v[92:95]\n\t"
    "s_waitcnt lgkmcnt(2)\n\t"
    "v_mfma_f32_16x16x32_bf16 v[80:83], v[240:243], v[112:115], v[80:83]\n\t"
    "ds_read_b32 v39, %[fa]\n\t"
    "ds_read_b128 v[48:51], %[rw] offset:0\n\t"
    "v_mfma_f32_16x16x32_bf16 v[84:87], v[244:247], v[112:115], v[84:87]\n\t"
    "ds_read_b128 v[52:55], %[rw] offset:1024\n\t"
    "v_mfma_f32_16x16x32_bf16 v[88:91], v[240:243], v[116:119], v[88:91]\n\t"
    "ds_read_b128 v[56:59], %[rw] offset:2048\n\t"
    "v_mfma_f32_16x16x32_bf16 v[92:95], v[244:247], v[116:119], v[92:95]\n\t"
    "ds_read_b128 v[60:63], %[rw] offset:3072\n\t"
    "s_waitcnt lgkmcnt(5)\n\t"
    "v_mfma_f32_16x16x32_bf16 v[80:83], v[248:251], v[120:123], v[80:83]\n\t"
    "v_mfma_f32_16x16x32_bf16 v[84:87], v[252:255], v[120:123], v[84:87]\n\t"
    "v_mfma_f32_16x16x32_bf16 v[88:91], v[248:251], v[124:127], v[88:91]\n\t"
    "v_mfma_f32_16x16x32_bf16 v[92:95], v[252:255], v[124:127], v[92:95]\n\t"
    "s_mov_b32 %[st], 0\n\t"
    "21:\n\t"
    "s_waitcnt lgkmcnt(0)\n\t"
    "v_cmp_ne_u32 vcc, %[fv], v39\n\t"
    "s_cbranch_vccnz 20f\n\t"
    "v_mov_b32 v39, %[cn]\n\t"
    "s_nop 0\n\t"
    "v_pk_add_f32 v[48:49], v[48:49], v[80:81]\n\t"
    "v_pk_add_f32 v[50:51], v[50:51], v[82:83]\n\t"
    "v_pk_add_f32 v[52:53], v[52:53], v[84:85]\n\t"
    "v_pk_add_f32 v[54:55], v[54:55], v[86:87]\n\t"
    "v_pk_add_f32 v[56:57], v[56:57], v[88:89]\n\t"
    "v_pk_add_f32 v[58:59], v[58:59], v[90:91]\n\t"
    "v_pk_add_f32 v[60:61], v[60:61], v[92:93]\n\t"
    "v_pk_add_f32 v[62:63], v[62:63], v[94:95]\n\t"
    "v_cvt_pk_bf16_f32 v40, v48, v49\n\t"
    "v_cvt_pk_bf16_f32 v41, v50, v51\n\t"
    "v_cvt_pk_bf16_f32 v42, v52, v53\n\t"
    "v_cvt_pk_bf16_f32 v43, v54, v55\n\t"
    "global_store_dwordx4 %[co0], v[40:43], %[cb] sc1\n\t"
    "ds_read_b32 v38, v39 offset:4\n\t"
    "v_cvt_pk_bf16_f32 v44, v56, v57\n\t"
    "v_cvt_pk_bf16_f32 v45, v58, v59\n\t"
    "v_cvt_pk_bf16_f32 v46, v60, v61\n\t"
    "v_cvt_pk_bf16_f32 v47, v62, v63\n\t"
    "global_store_dwordx4 %[co1], v[44:47], %[cb] sc1\n\t"
    "s_memrealtime s[36:37]\n\t"
    "v_cmp_u_f32 vcc, v48, v56\n\t"
    "s_waitcnt lgkmcnt(0)\n\t"
    "s_or_b32 %[bad], vcc_lo, vcc_hi\n\t"
    "v_readfirstlane_b32 %[pk], v38\n\t"
    "s_sub_u32 %[tk], s36, %[tk]\n\t"
    "s_branch 9f\n\t"
    "20:\n\t"
    "ds_read_b32 v39, %[fa]\n\t"
    "ds_read_b128 v[48:51], %[rw] offset:0\n\t"
    "ds_read_b128 v[52:55], %[rw] offset:1024\n\t"
    "ds_read_b128 v[56:59], %[rw] offset:2048\n\t"
    "ds_read_b128 v[60:63], %[rw] offset:3072\n\t"
    "s_branch 21b\n\t"
    "13:\n\t"
    "s_memrealtime s[36:37]\n\t"
    "v_mov_b32 v79, 0\n\t"
    "s_waitcnt lgkmcnt(0)\n\t"
    "s_mov_b32 %[tk], s36\n\t"
    "s_add_u32 s36, s36, %[bl]\n\t"
    "s_and_b32 s36, s36, 0xffffff\n\t"
    "s_and_b32 s37, %[bl], 0xff000000\n\t"
    "s_or_b32 s36, s36, s37\n\t"
    "v_mov_b32 v78, s36\n\t"
    "s_mov_b64 exec, 1\n\t"
    "global_store_dword v79, v78, %[pg] offset:64 sc1\n\t"
    "s_mov_b64 exec, -1\n\t"
    "s_branch 14b\n\t"
    "3:\n\t"
    "7:\n\t"
    "s_and_b32 %[st], %[pv], 1\n\t"
    "s_mul_i32 %[st], %[st], 36\n\t"
    "s_add_u32 %[st], %[st], %[cn]\n\t"
    "v_mov_b32 v76, %[st]\n\t"
    "v_mov_b32 v77, 1\n\t"
    "v_mov_b32 v78, %[pv]\n\t"
    "v_mov_b32 v79, 0\n\t"
    "s_waitcnt vmcnt(0)\n\t"
    "s_mov_b64 exec, 1\n\t"
    "ds_add_rtn_u32 v77, v76, v77\n\t"
    "s_waitcnt lgkmcnt(0)\n\t"
    "v_readfirstlane_b32 %[st], v77\n\t"
    "s_and_b32 %[st], %[st], 3\n\t"
    "s_cmp_lg_u32 %[st], 3\n\t"
    "s_cbranch_scc1 6f\n\t"
    "global_store_dword v79, v78, %[pg] sc1\n\t"
    "6:\n\t"
    "s_mov_b64 exec, -1\n\t"
    "s_bitcmp1_b32 %[x], 2\n\t"
    "s_cbranch_scc1 8b\n\t"
    "s_branch 4b\n\t"
    "12:\n\t"
    "s_waitcnt lgkmcnt(0)\n\t"
    "9:"
    : [st] "=&s"(st), [tk] "+&s"(tk), [bad] "=&s"(bad), [pk] "=&s"(pk)
    : [ea] "v"(ea), [da] "v"(da), [x] "s"(x), "{v[128:159]}"(W[0]), "{v[160:191]}"(W[1]), "{v[192:223]}"(W[2]), "{v[224:255]}"(W[3]), [pv] "s"(pv), [cn] "s"(cn), [pg] "s"(pg), [bl] "s"(bl), [rw] "v"(rw), [fa] "v"(fa), [fv] "s"(fv), [cb] "s"(cb), [co0] "v"(co0), [co1] "v"(co1)
    : "v96", "v97", "v98", "v99", "v100", "v101", "v102", "v103", "v104", "v105", "v106", "v107", "v108", "v109", "v110", "v111", "v112", "v113", "v114", "v115", "v116", "v117", "v118", "v119", "v120", "v121", "v122", "v123", "v124", "v125", "v126", "v127", "v76", "v77", "v78", "v79", "v38", "v39", "v40", "v41", "v42", "v43", "v44", "v45", "v46", "v47", "v48", "v49", "v50", "v51", "v52", "v53", "v54", "v55", "v56", "v57", "v58", "v59", "v60", "v61", "v62", "v63", "v80", "v81", "v82", "v83", "v84", "v85", "v86", "v87", "v88", "v89", "v90", "v91", "v92", "v93", "v94", "v95", "vcc", "scc", "m0", "memory", "s36", "s37", "s38", "s39");
}
__device__ __forceinline__ void lean_ep1(const u32x32 (&W)[4], unsigned ea, unsigned da, unsigned x, unsigned pv, unsigned cn, const char* pg, unsigned bl, unsigned& tk, unsigned& st,
    unsigned rw, unsigned fa, unsigned fv, const char* cb, unsigned co0, unsigned co1, unsigned& bad, unsigned& pk) {
  asm volatile("v_mov_b32 v76, %[cn]\n\t"
    "ds_read_b32 v76, v76 offset:28\n\t"
    "ds_read_b128 v[96:99], %[ea] offset:0\n\t"
    "ds_read_b128 v[100:103], %[ea] offset:2048\n\t"
    "ds_read_b128 v[104:107], %[da] offset:0\n\t"
    "ds_read_b128 v[108:111], %[da] offset:2048\n\t"
    "ds_read_b128 v[112:115], %[ea] offset:4096\n\t"
    "ds_read_b128 v[116:119], %[ea] offset:6144\n\t"
    "s_waitcnt lgkmcnt(6)\n\t"
    "v_cmp_ne_u32 vcc, 0, v76\n\t"
    "s_mov_b32 %[st], -1\n\t"
    "s_cbranch_vccnz 12f\n\t"
    "s_waitcnt lgkmcnt(4)\n\t"
    "v_mfma_f32_16x16x32_bf16 v[80:83], v[128:131], v[96:99], 0\n\t"
    "ds_read_b128 v[120:123], %[da] offset:4096\n\t"
    "v_mfma_f32_16x16x32_bf16 v[84:87], v[132:135], v[96:99], 0\n\t"
    "ds_read_b128 v[124:127], %[da] offset:6144\n\t"
    "v_mfma_f32_16x16x32_bf16 v[88:91], v[128:131], v[100:103], 0\n\t"
    "v_mfma_f32_16x16x32_bf16 v[92:95], v[132:135], v[100:103], 0\n\t"
    "s_barrier\n\t"
    "s_bitcmp1_b32 %[x], 2\n\t"
    "s_cbranch_scc1 7f\n\t"
    "8:\n\t"
    "s_bitcmp1_b32 %[x], 3\n\t"
    "s_cbranch_scc1 13f\n\t"
    "14:\n\t"
    "s_waitcnt lgkmcnt(4)\n\t"
    "v_mfma_f32_16x16x32_bf16 v[80:83], v[136:139], v[104:107], v[80:83]\n\t"
    "ds_read_b128 v[96:99], %[ea] offset:8192\n\t"
    "v_mfma_f32_16x16x32_bf16 v[84:87], v[140:143], v[104:107], v[84:87]\n\t"
    "ds_read_b128 v[100:103], %[ea] offset:10240\n\t"
    "v_mfma_f32_16x16x32_bf16 v[88:91], v[136:139], v[108:111], v[88:91]\n\t"
    "v_mfma_f32_16x16x32_bf16 v[92:95], v[140:143], v[108:111], v[92:95]\n\t"
    "s_waitcnt lgkmcnt(4)\n\t"
    "v_mfma_f32_16x16x32_bf16 v[80:83], v[144:147], v[112:115], v[80:83]\n\t"
    "ds_read_b128 v[104:107], %[da] offset:8192\n\t"
    "v_mfma_f32_16x16x32_bf16 v[84:87], v[148:151], v[112:115], v[84:87]\n\t"
    "ds_read_b128 v[108:111], %[da] offset:10240\n\t"
    "v_mfma_f32_16x16x32_bf16 v[88:91], v[144:147], v[116:119], v[88:91]\n\t"
    "v_mfma_f32_16x16x32_bf16 v[92:95], v[148:151], v[116:119], v[92:95]\n\t"
    "s_waitcnt lgkmcnt(4)\n\t"
    "v_mfma_f32_16x16x32_bf16 v[80:83], v[152:155], v[120:123], v[80:83]\n\t"
    "ds_read_b128 v[112:115], %[ea] offset:12288\n\t"
    "v_mfma_f32_16x16x32_bf16 v[84:87], v[156:159], v[120:123], v[84:87]\n\t"
    "ds_read_b128 v[116:119], %[ea] offset:14336\n\t"
    "v_mfma_f32_16x16x32_bf16 v[88:91], v[152:155], v[124:127], v[88:91]\n\t"
    "v_mfma_f32_16x16x32_bf16 v[92:95], v[156:159], v[124:127], v[92:95]\n\t"
    "s_waitcnt lgkmcnt(4)\n\t"
    "v_mfma_f32_16x16x32_bf16 v[80:83], v[160:163], v[96:99], v[80:83]\n\t"
    "ds_read_b128 v[120:123], %[da] offset:12288\n\t"
    "v_mfma_f32_16x16x32_bf16 v[84:87], v[164:167], v[96:99], v[84:87]\n\t"
    "ds_read_b128 v[124:127], %[da] offset:14336\n\t"
    "v_mfma_f32_16x16x32_bf16 v[88:91], v[160:163], v[100:103], v[88:91]\n\t"
    "v_mfma_f32_16x16x32_bf16 v[92:95], v[164:167], v[100:103], v[92:95]\n\t"
    "s_barrier\n\t"
    "s_waitcnt lgkmcnt(4)\n\t"
    "v_mfma_f32_16x16x32_bf16 v[80:83], v[168:171], v[104:107], v[80:83]\n\t"
    "ds_read_b128 v[96:99], %[ea] offset:16384\n\t"
    "v_mfma_f32_16x16x32_bf16 v[84:87], v[172:175], v[104:107], v[84:87]\n\t"
    "ds_read_b128 v[100:103], %[ea] offset:18432\n\t"
    "v_mfma_f32_16x16x32_bf16 v[88:91], v[168:171], v[108:111], v[88:91]\n\t"
    "v_mfma_f32_16x16x32_bf16 v[92:95], v[172:175], v[108:111], v[92:95]\n\t"
    "s_waitcnt lgkmcnt(4)\n\t"
    "v_mfma_f32_16x16x32_bf16 v[80:83], v[176:179], v[112:115], v[80:83]\n\t"
    "ds_read_b128 v[104:107], %[da] offset:16384\n\t"
    "v_mfma_f32_16x16x32_bf16 v[84:87], v[180:183], v[112:115], v[84:87]\n\t"
    "ds_read_b128 v[108:111], %[da] offset:18432\n\t"
    "v_mfma_f32_16x16x32_bf16 v[88:91], v[176:179], v[116:119], v[88:91]\n\t"
    "v_mfma_f32_16x16x32_bf16 v[92:95], v[180:183], v[116:119], v[92:95]\n\t"
    "s_waitcnt lgkmcnt(4)\n\t"
    "v_mfma_f32_16x16x32_bf16 v[80:83], v[184:187], v[120:123], v[80:83]\n\t"
    "ds_read_b128 v[112:115], %[ea] offset:20480\n\t"
    "v_mfma_f32_16x16x32_bf16 v[84:87], v[188:191], v[120:123], v[84:87]\n\t"
    "ds_read_b128 v[116:119], %[ea] offset:22528\n\t"
    "v_mfma_f32_16x16x32_bf16 v[88:91], v[184:187], v[124:127], v[88:91]\n\t"
    "v_mfma_f32_16x16x32_bf16 v[92:95], v[188:191], v[124:127], v[92:95]\n\t"
    "s_waitcnt lgkmcnt(4)\n\t"
    "v_mfma_f32_16x16x32_bf16 v[80:83], v[192:195], v[96:99], v[80:83]\n\t"
    "ds_read_b128 v[120:123], %[da] offset:20480\n\t"
    "v_mfma_f32_16x16x32_bf16 v[84:87], v[196:199], v[96:99], v[84:87]\n\t"
    "ds_read_b128 v[124:127], %[da] offset:22528\n\t"
    "v_mfma_f32_16x16x32_bf16 v[88:91], v[192:195], v[100:103], v[88:91]\n\t"
    "v_mfma_f32_16x16x32_bf16 v[92:95], v[196:199], v[100:103], v[92:95]\n\t"
    "s_barrier\n\t"
    "s_and_b32 %[st], %[x], 3\n\t"
    "s_cbranch_scc1 3f\n\t"
    "4:\n\t"
    "s_waitcnt lgkmcnt(4)\n\t"
    "v_mfma_f32_16x16x32_bf16 v[80:83], v[200:203], v[104:107], v[80:83]\n\t"
    "ds_read_b128 v[96:99], %[ea] offset:24576\n\t"
    "v_mfma_f32_16x16x32_bf16 v[84:87], v[204:207], v[104:107], v[84:87]\n\t"
    "ds_read_b128 v[100:103], %[ea] offset:26624\n\t"
    "v_mfma_f32_16x16x32_bf16 v[88:91], v[200:203], v[108:111], v[88:91]\n\t"
    "v_mfma_f32_16x16x32_bf16 v[92:95], v[204:207], v[108:111], v[92:95]\n\t"
    "s_waitcnt lgkmcnt(4)\n\t"
    "v_mfma_f32_16x16x32_bf16 v[80:83], v[208:211], v[112:115], v[80:83]\n\t"
    "ds_read_b128 v[104:107], %[da] offset:24576\n\t"
    "v_mfma_f32_16x16x32_bf16 v[84:87], v[212:215], v[112:115], v[84:87]\n\t"
    "ds_read_b128 v[108:111], %[da] offset:26624\n\t"
    "v_mfma_f32_16x16x32_bf16 v[88:91], v[208:211], v[116:119], v[88:91]\n\t"
    "v_mfma_f32_16x16x32_bf16 v[92:95], v[212:215], v[116:119], v[92:95]\n\t"
    "s_waitcnt lgkmcnt(4)\n\t"
    "v_mfma_f32_16x16x32_bf16 v[80:83], v[216:219], v[120:123], v[80:83]\n\t"
    "ds_read_b128 v[112:115], %[ea] offset:28672\n\t"
    "v_mfma_f32_16x16x32_bf16 v[84:87], v[220:223], v[120:123], v[84:87]\n\t"
    "ds_read_b128 v[116:119], %[ea] offset:30720\n\t"
    "v_mfma_f32_16x16x32_bf16 v[88:91], v[216:219], v[124:127], v[88:91]\n\t"
    "v_mfma_f32_16x16x32_bf16 v[92:95], v[220:223], v[124:127], v[92:95]\n\t"
    "s_waitcnt lgkmcnt(4)\n\t"
    "v_mfma_f32_16x16x32_bf16 v[80:83], v[224:227], v[96:99], v[80:83]\n\t"
    "ds_read_b128 v[120:123], %[da] offset:28672\n\t"
    "v_mfma_f32_16x16x32_bf16 v[84:87], v[228:231], v[96:99], v[84:87]\n\t"
    "ds_read_b128 v[124:127], %[da] offset:30720\n\t"
    "v_mfma_f32_16x16x32_bf16 v[88:91], v[224:227], v[100:103], v[88:91]\n\t"
    "v_mfma_f32_16x16x32_bf16 v[92:95], v[228:231], v[100:103], v[92:95]\n\t"
    "s_waitcnt lgkmcnt(4)\n\t"
    "v_mfma_f32_16x16x32_bf16 v[80:83], v[232:235], v[104:107], v[80:83]\n\t"
    "v_mfma_f32_16x16x32_bf16 v[84:87], v[236:239], v[104:107], v[84:87]\n\t"
    "v_mfma_f32_16x16x32_bf16 v[88:91], v[232:235], v[108:111], v[88:91]\n\t"
    "v_mfma_f32_16x16x32_bf16 v[92:95], v[236:239], v[108:111], v[92:95]\n\t"
    "s_waitcnt lgkmcnt(2)\n\t"
    "v_mfma_f32_16x16x32_bf16 v[80:83], v[240:243], v[112:115], v[80:83]\n\t"
    "ds_read_b32 v39, %[fa]\n\t"
    "ds_read_b128 v[48:51], %[rw] offset:0\n\t"
    "v_mfma_f32_16x16x32_bf16 v[84:87], v[244:247], v[112:115], v[84:87]\n\t"
    "ds_read_b128 v[52:55], %[rw] offset:1024\n\t"
    "v_mfma_f32_16x16x32_bf16 v[88:91], v[240:243], v[116:119], v[88:91]\n\t"
    "ds_read_b128 v[56:59], %[rw] offset:2048\n\t"
    "v_mfma_f32_16x16x32_bf16 v[92:95], v[244:247], v[116:119], v[92:95]\n\t"
    "ds_read_b128 v[60:63], %[rw] offset:3072\n\t"
    "s_waitcnt lgkmcnt(5)\n\t"
    "v_mfma_f32_16x16x32_bf16 v[80:83], v[248:251], v[120:123], v[80:83]\n\t"
    "v_mfma_f32_16x16x32_bf16 v[84:87], v[252:255], v[120:123], v[84:87]\n\t"
    "v_mfma_f32_16x16x32_bf16 v[88:91], v[248:251], v[124:127], v[88:91]\n\t"
    "v_mfma_f32_16x16x32_bf16 v[92:95], v[252:255], v[124:127], v[92:95]\n\t"
    "s_mov_b32 %[st], 0\n\t"
    "21:\n\t"
    "s_waitcnt lgkmcnt(0)\n\t"
    "v_cmp_ne_u32 vcc, %[fv], v39\n\t"
    "s_cbranch_vccnz 20f\n\t"
    "v_mov_b32 v39, %[cn]\n\t"
    "s_nop 0\n\t"
    "v_pk_add_f32 v[48:49], v[48:49], v[80:81]\n\t"
    "v_pk_add_f32 v[50:51], v[50:51], v[82:83]\n\t"
    "v_pk_add_f32 v[52:53], v[52:53], v[84:85]\n\t"
    "v_pk_add_f32 v[54:55], v[54:55], v[86:87]\n\t"
    "v_pk_add_f32 v[56:57], v[56:57], v[88:89]\n\t"
    "v_pk_add_f32 v[58:59], v[58:59], v[90:91]\n\t"
    "v_pk_add_f32 v[60:61], v[60:61], v[92:93]\n\t"
    "v_pk_add_f32 v[62:63], v[62:63], v[94:95]\n\t"
    "v_mul_f32 v40, %[nl], v48\n\t"
    "v_mul_f32 v41, %[nl], v49\n\t"
    "v_mul_f32 v42, %[nl], v50\n\t"
    "v_mul_f32 v43, %[nl], v51\n\t"
    "v_mul_f32 v44, %[nl], v56\n\t"
    "v_mul_f32 v45, %[nl], v57\n\t"
    "v_mul_f32 v46, %[nl], v58\n\t"
    "v_mul_f32 v47, %[nl], v59\n\t"
    "v_exp_f32 v40, v40\n\t"
    "v_exp_f32 v41, v41\n\t"
    "v_exp_f32 v42, v42\n\t"
    "v_exp_f32 v43, v43\n\t"
    "v_exp_f32 v44, v44\n\t"
    "v_exp_f32 v45, v45\n\t"
    "v_exp_f32 v46, v46\n\t"
    "v_exp_f32 v47, v47\n\t"
    "v_pk_add_f32 v[40:41], v[40:41], 1.0 op_sel_hi:[1,0]\n\t"
    "v_pk_add_f32 v[42:43], v[42:43], 1.0 op_sel_hi:[1,0]\n\t"
    "v_pk_add_f32 v[44:45], v[44:45], 1.0 op_sel_hi:[1,0]\n\t"
    "v_pk_add_f32 v[46:47], v[46:47], 1.0 op_sel_hi:[1,0]\n\t"
    "v_rcp_f32 v40, v40\n\t"
    "v_rcp_f32 v41, v41\n\t"
    "v_rcp_f32 v42, v42\n\t"
    "v_rcp_f32 v43, v43\n\t"
    "v_rcp_f32 v44, v44\n\t"
    "v_rcp_f32 v45, v45\n\t"
    "v_rcp_f32 v46, v46\n\t"
    "v_rcp_f32 v47, v47\n\t"
    "v_pk_mul_f32 v[40:41], v[48:49], v[40:41]\n\t"
    "v_pk_mul_f32 v[42:43], v[50:51], v[42:43]\n\t"
    "v_pk_mul_f32 v[44:45], v[56:57], v[44:45]\n\t"
    "v_pk_mul_f32 v[46:47], v[58:59], v[46:47]\n\t"
    "v_pk_mul_f32 v[40:41], v[40:41], v[52:53]\n\t"
    "v_pk_mul_f32 v[42:43], v[42:43], v[54:55]\n\t"
    "v_pk_mul_f32 v[44:45], v[44:45], v[60:61]\n\t"
    "v_pk_mul_f32 v[46:47], v[46:47], v[62:63]\n\t"
    "v_cvt_pk_bf16_f32 v48, v40, v41\n\t"
    "v_cvt_pk_bf16_f32 v49, v42, v43\n\t"
    "v_cvt_pk_bf16_f32 v50, v44, v45\n\t"
    "ds_read_b32 v38, v39 offset:4\n\t"
    "v_cvt_pk_bf16_f32 v51, v46, v47\n\t"
    "s_nop 1\n\t"
    "v_permlane16_swap_b32 v48, v50\n\t"
    "v_permlane16_swap_b32 v49, v51\n\t"
    "global_store_dwordx4 %[co0], v[48:51], %[cb] sc1\n\t"
    "s_memrealtime s[36:37]\n\t"
    "v_cmp_u_f32 vcc, v52, v60\n\t"
    "s_waitcnt lgkmcnt(0)\n\t"
    "s_or_b32 %[bad], vcc_lo, vcc_hi\n\t"
    "v_readfirstlane_b32 %[pk], v38\n\t"
    "s_sub_u32 %[tk], s36, %[tk]\n\t"
    "s_branch 9f\n\t"
    "20:\n\t"
    "ds_read_b32 v39, %[fa]\n\t"
    "ds_read_b128 v[48:51], %[rw] offset:0\n\t"
    "ds_read_b128 v[52:55], %[rw] offset:1024\n\t"
    "ds_read_b128 v[56:59], %[rw] offset:2048\n\t"
    "ds_read_b128 v[60:63], %[rw] offset:3072\n\t"
    "s_branch 21b\n\t"
    "13:\n\t"
    "s_memrealtime s[36:37]\n\t"
    "v_mov_b32 v79, 0\n\t"
    "s_waitcnt lgkmcnt(0)\n\t"
    "s_mov_b32 %[tk], s36\n\t"
    "s_add_u32 s36, s36, %[bl]\n\t"
    "s_and_b32 s36, s36, 0xffffff\n\t"
    "s_and_b32 s37, %[bl], 0xff000000\n\t"
    "s_or_b32 s36, s36, s37\n\t"
    "v_mov_b32 v78, s36\n\t"
    "s_mov_b64 exec, 1\n\t"
    "global_store_dword v79, v78, %[pg] offset:64 sc1\n\t"
    "s_mov_b64 exec, -1\n\t"
    "s_branch 14b\n\t"
    "3:\n\t"
    "7:\n\t"
    "s_and_b32 %[st], %[pv], 1\n\t"
    "s_mul_i32 %[st], %[st], 36\n\t"
    "s_add_u32 %[st], %[st], %[cn]\n\t"
    "v_mov_b32 v76, %[st]\n\t"
    "v_mov_b32 v77, 1\n\t"
    "v_mov_b32 v78, %[pv]\n\t"
    "v_mov_b32 v79, 0\n\t"
    "s_waitcnt vmcnt(0)\n\t"
    "s_mov_b64 exec, 1\n\t"
    "ds_add_rtn_u32 v77, v76, v77\n\t"
    "s_waitcnt lgkmcnt(0)\n\t"
    "v_readfirstlane_b32 %[st], v77\n\t"
    "s_and_b32 %[st], %[st], 3\n\t"
    "s_cmp_lg_u32 %[st], 3\n\t"
    "s_cbranch_scc1 6f\n\t"
    "global_store_dword v79, v78, %[pg] sc1\n\t"
    "6:\n\t"
    "s_mov_b64 exec, -1\n\t"
    "s_bitcmp1_b32 %[x], 2\n\t"
    "s_cbranch_scc1 8b\n\t"
    "s_branch 4b\n\t"
    "12:\n\t"
    "s_waitcnt lgkmcnt(0)\n\t"
    "9:"
    : [st] "=&s"(st), [tk] "+&s"(tk), [bad] "=&s"(bad), [pk] "=&s"(pk)
    : [ea] "v"(ea), [da] "v"(da), [x] "s"(x), "{v[128:159]}"(W[0]), "{v[160:191]}"(W[1]), "{v[192:223]}"(W[2]), "{v[224:255]}"(W[3]), [pv] "s"(pv), [cn] "s"(cn), [pg] "s"(pg), [bl] "s"(bl), [rw] "v"(rw), [fa] "v"(fa), [fv] "s"(fv), [cb] "s"(cb), [co0] "v"(co0), [nl] "s"(-LOG2E)
    : "v96", "v97", "v98", "v99", "v100", "v101", "v102", "v103", "v104", "v105", "v106", "v107", "v108", "v109", "v110", "v111", "v112", "v113", "v114", "v115", "v116", "v117", "v118", "v119", "v120", "v121", "v122", "v123", "v124", "v125", "v126", "v127", "v76", "v77", "v78", "v79", "v38", "v39", "v40", "v41", "v42", "v43", "v44", "v45", "v46", "v47", "v48", "v49", "v50", "v51", "v52", "v53", "v54", "v55", "v56", "v57", "v58", "v59", "v60", "v61", "v62", "v63", "v80", "v81", "v82", "v83", "v84", "v85", "v86", "v87", "v88", "v89", "v90", "v91", "v92", "v93", "v94", "v95", "vcc", "scc", "m0", "memory", "s36", "s37", "s38", "s39");
}
__device__ __forceinline__ void lean_ep2(const u32x32 (&W)[4], unsigned ea, unsigned da, unsigned x, unsigned pv, unsigned cn, const char* pg, unsigned bl, unsigned& tk, unsigned& st,
    unsigned rw, unsigned fa, unsigned fv, const char* cb, unsigned co0, unsigned co1, unsigned& bad, unsigned& pk) {
  asm volatile("v_mov_b32 v76, %[cn]\n\t"
    "ds_read_b32 v76, v76 offset:28\n\t"
    "ds_read_b128 v[96:99], %[ea] offset:0\n\t"
    "ds_read_b128 v[100:103], %[ea] offset:2048\n\t"
    "ds_read_b128 v[104:107], %[da] offset:0\n\t"
    "ds_read_b128 v[108:111], %[da] offset:2048\n\t"
    "ds_read_b128 v[112:115], %[ea] offset:4096\n\t"
    "ds_read_b128 v[116:119], %[ea] offset:6144\n\t"
    "s_waitcnt lgkmcnt(6)\n\t"
    "v_cmp_ne_u32 vcc, 0, v76\n\t"
    "s_mov_b32 %[st], -1\n\t"
    "s_cbranch_vccnz 12f\n\t"
    "s_waitcnt lgkmcnt(4)\n\t"
    "v_mfma_f32_16x16x32_bf16 v[80:83], v[128:131], v[96:99], 0\n\t"
    "ds_read_b128 v[120:123], %[da] offset:4096\n\t"
    "v_mfma_f32_16x16x32_bf16 v[84:87], v[132:135], v[96:99], 0\n\t"
    "ds_read_b128 v[124:127], %[da] offset:6144\n\t"
    "v_mfma_f32_16x16x32_bf16 v[88:91], v[128:131], v[100:103], 0\n\t"
    "v_mfma_f32_16x16x32_bf16 v[92:95], v[132:135], v[100:103], 0\n\t"
    "s_barrier\n\t"
    "s_bitcmp1_b32 %[x], 2\n\t"
    "s_cbranch_scc1 7f\n\t"
    "8:\n\t"
    "s_bitcmp1_b32 %[x], 3\n\t"
    "s_cbranch_scc1 13f\n\t"
    "14:\n\t"
    "s_waitcnt lgkmcnt(4)\n\t"
    "v_mfma_f32_16x16x32_bf16 v[80:83], v[136:139], v[104:107], v[80:83]\n\t"
    "ds_read_b128 v[96:99], %[ea] offset:8192\n\t"
    "v_mfma_f32_16x16x32_bf16 v[84:87], v[140:143], v[104:107], v[84:87]\n\t"
    "ds_read_b128 v[100:103], %[ea] offset:10240\n\t"
    "v_mfma_f32_16x16x32_bf16 v[88:91], v[136:139], v[108:111], v[88:91]\n\t"
    "v_mfma_f32_16x16x32_bf16 v[92:95], v[140:143], v[108:111], v[92:95]\n\t"
    "s_waitcnt lgkmcnt(4)\n\t"
    "v_mfma_f32_16x16x32_bf16 v[80:83], v[144:147], v[112:115], v[80:83]\n\t"
    "ds_read_b128 v[104:107], %[da] offset:8192\n\t"
    "v_mfma_f32_16x16x32_bf16 v[84:87], v[148:151], v[112:115], v[84:87]\n\t"
    "ds_read_b128 v[108:111], %[da] offset:10240\n\t"
    "v_mfma_f32_16x16x32_bf16 v[88:91], v[144:147], v[116:119], v[88:91]\n\t"
    "v_mfma_f32_16x16x32_bf16 v[92:95], v[148:151], v[116:119], v[92:95]\n\t"
    "s_waitcnt lgkmcnt(4)\n\t"
    "v_mfma_f32_16x16x32_bf16 v[80:83], v[152:155], v[120:123], v[80:83]\n\t"
    "ds_read_b128 v[112:115], %[ea] offset:12288\n\t"
    "v_mfma_f32_16x16x32_bf16 v[84:87], v[156:159], v[120:123], v[84:87]\n\t"
    "ds_read_b128 v[116:119], %[ea] offset:14336\n\t"
    "v_mfma_f32_16x16x32_bf16 v[88:91], v[152:155], v[124:127], v[88:91]\n\t"
    "v_mfma_f32_16x16x32_bf16 v[92:95], v[156:159], v[124:127], v[92:95]\n\t"
    "s_waitcnt lgkmcnt(4)\n\t"
    "v_mfma_f32_16x16x32_bf16 v[80:83], v[160:163], v[96:99], v[80:83]\n\t"
    "ds_read_b128 v[120:123], %[da] offset:12288\n\t"
    "v_mfma_f32_16x16x32_bf16 v[84:87], v[164:167], v[96:99], v[84:87]\n\t"
    "ds_read_b128 v[124:127], %[da] offset:14336\n\t"
    "v_mfma_f32_16x16x32_bf16 v[88:91], v[160:163], v[100:103], v[88:91]\n\t"
    "v_mfma_f32_16x16x32_bf16 v[92:95], v[164:167], v[100:103], v[92:95]\n\t"
    "s_barrier\n\t"
    "s_waitcnt lgkmcnt(4)\n\t"
    "v_mfma_f32_16x16x32_bf16 v[80:83], v[168:171], v[104:107], v[80:83]\n\t"
    "ds_read_b128 v[96:99], %[ea] offset:16384\n\t"
    "v_mfma_f32_16x16x32_bf16 v[84:87], v[172:175], v[104:107], v[84:87]\n\t"
    "ds_read_b128 v[100:103], %[ea] offset:18432\n\t"
    "v_mfma_f32_16x16x32_bf16 v[88:91], v[168:171], v[108:111], v[88:91]\n\t"
    "v_mfma_f32_16x16x32_bf16 v[92:95], v[172:175], v[108:111], v[92:95]\n\t"
    "s_waitcnt lgkmcnt(4)\n\t"
    "v_mfma_f32_16x16x32_bf16 v[80:83], v[176:179], v[112:115], v[80:83]\n\t"
    "ds_read_b128 v[104:107], %[da] offset:16384\n\t"
    "v_mfma_f32_16x16x32_bf16 v[84:87], v[180:183], v[112:115], v[84:87]\n\t"
    "ds_read_b128 v[108:111], %[da] offset:18432\n\t"
    "v_mfma_f32_16x16x32_bf16 v[88:91], v[176:179], v[116:119], v[88:91]\n\t"
    "v_mfma_f32_16x16x32_bf16 v[92:95], v[180:183], v[116:119], v[92:95]\n\t"
    "s_waitcnt lgkmcnt(4)\n\t"
    "v_mfma_f32_16x16x32_bf16 v[80:83], v[184:187], v[120:123], v[80:83]\n\t"
    "ds_read_b128 v[112:115], %[ea] offset:20480\n\t"
    "v_mfma_f32_16x16x32_bf16 v[84:87], v[188:191], v[120:123], v[84:87]\n\t"
    "ds_read_b128 v[116:119], %[ea] offset:22528\n\t"
    "v_mfma_f32_16x16x32_bf16 v[88:91], v[184:187], v[124:127], v[88:91]\n\t"
    "v_mfma_f32_16x16x32_bf16 v[92:95], v[188:191], v[124:127], v[92:95]\n\t"
    "s_waitcnt lgkmcnt(4)\n\t"
    "v_mfma_f32_16x16x32_bf16 v[80:83], v[192:195], v[96:99], v[80:83]\n\t"
    "ds_read_b128 v[120:123], %[da] offset:20480\n\t"
    "v_mfma_f32_16x16x32_bf16 v[84:87], v[196:199], v[96:99], v[84:87]\n\t"
    "ds_read_b128 v[124:127], %[da] offset:22528\n\t"
    "v_mfma_f32_16x16x32_bf16 v[88:91], v[192:195], v[100:103], v[88:91]\n\t"
    "v_mfma_f32_16x16x32_bf16 v[92:95], v[196:199], v[100:103], v[92:95]\n\t"
    "s_barrier\n\t"
    "s_and_b32 %[st], %[x], 3\n\t"
    "s_cbranch_scc1 3f\n\t"
    "4:\n\t"
    "s_waitcnt lgkmcnt(4)\n\t"
    "v_mfma_f32_16x16x32_bf16 v[80:83], v[200:203], v[104:107], v[80:83]\n\t"
    "ds_read_b128 v[96:99], %[ea] offset:24576\n\t"
    "v_mfma_f32_16x16x32_bf16 v[84:87], v[204:207], v[104:107], v[84:87]\n\t"
    "ds_read_b128 v[100:103], %[ea] offset:26624\n\t"
    "v_mfma_f32_16x16x32_bf16 v[88:91], v[200:203], v[108:111], v[88:91]\n\t"
    "v_mfma_f32_16x16x32_bf16 v[92:95], v[204:207], v[108:111], v[92:95]\n\t"
    "s_waitcnt lgkmcnt(4)\n\t"
    "v_mfma_f32_16x16x32_bf16 v[80:83], v[208:211], v[112:115], v[80:83]\n\t"
    "ds_read_b128 v[104:107], %[da] offset:24576\n\t"
    "v_mfma_f32_16x16x32_bf16 v[84:87], v[212:215], v[112:115], v[84:87]\n\t"
    "ds_read_b128 v[108:111], %[da] offset:26624\n\t"
    "v_mfma_f32_16x16x32_bf16 v[88:91], v[208:211], v[116:119], v[88:91]\n\t"
    "v_mfma_f32_16x16x32_bf16 v[92:95], v[212:215], v[116:119], v[92:95]\n\t"
    "s_waitcnt lgkmcnt(4)\n\t"
    "v_mfma_f32_16x16x32_bf16 v[80:83], v[216:219], v[120:123], v[80:83]\n\t"
    "ds_read_b128 v[112:115], %[ea] offset:28672\n\t"
    "v_mfma_f32_16x16x32_bf16 v[84:87], v[220:223], v[120:123], v[84:87]\n\t"
    "ds_read_b128 v[116:119], %[ea] offset:30720\n\t"
    "v_mfma_f32_16x16x32_bf16 v[88:91], v[216:219], v[124:127], v[88:91]\n\t"
    "v_mfma_f32_16x16x32_bf16 v[92:95], v[220:223], v[124:127], v[92:95]\n\t"
    "s_waitcnt lgkmcnt(4)\n\t"
    "v_mfma_f32_16x16x32_bf16 v[80:83], v[224:227], v[96:99], v[80:83]\n\t"
    "ds_read_b128 v[120:123], %[da] offset:28672\n\t"
    "v_mfma_f32_16x16x32_bf16 v[84:87], v[228:231], v[96:99], v[84:87]\n\t"
    "ds_read_b128 v[124:127], %[da] offset:30720\n\t"
    "v_mfma_f32_16x16x32_bf16 v[88:91], v[224:227], v[100:103], v[88:91]\n\t"
    "v_mfma_f32_16x16x32_bf16 v[92:95], v[228:231], v[100:103], v[92:95]\n\t"
    "s_waitcnt lgkmcnt(4)\n\t"
    "v_mfma_f32_16x16x32_bf16 v[80:83], v[232:235], v[104:107], v[80:83]\n\t"
    "v_mfma_f32_16x16x32_bf16 v[84:87], v[236:239], v[104:107], v[84:87]\n\t"
    "v_mfma_f32_16x16x32_bf16 v[88:91], v[232:235], v[108:111], v[88:91]\n\t"
    "v_mfma_f32_16x16x32_bf16 v[92:95], v[236:239], v[108:111], v[92:95]\n\t"
    "s_waitcnt lgkmcnt(2)\n\t"
    "v_mfma_f32_16x16x32_bf16 v[80:83], v[240:243], v[112:115], v[80:83]\n\t"
    "ds_read_b32 v39, %[fa]\n\t"
    "ds_read_b128 v[48:51], %[rw] offset:0\n\t"
    "v_mfma_f32_16x16x32_bf16 v[84:87], v[244:247], v[112:115], v[84:87]\n\t"
    "ds_read_b128 v[52:55], %[rw] offset:1024\n\t"
    "v_mfma_f32_16x16x32_bf16 v[88:91], v[240:243], v[116:119], v[88:91]\n\t"
    "ds_read_b128 v[56:59], %[rw] offset:2048\n\t"
    "v_mfma_f32_16x16x32_bf16 v[92:95], v[244:247], v[116:119], v[92:95]\n\t"
    "ds_read_b128 v[60:63], %[rw] offset:3072\n\t"
    "s_waitcnt lgkmcnt(5)\n\t"
    "v_mfma_f32_16x16x32_bf16 v[80:83], v[248:251], v[120:123], v[80:83]\n\t"
    "v_mfma_f32_16x16x32_bf16 v[84:87], v[252:255], v[120:123], v[84:87]\n\t"
    "v_mfma_f32_16x16x32_bf16 v[88:91], v[248:251], v[124:127], v[88:91]\n\t"
    "v_mfma_f32_16x16x32_bf16 v[92:95], v[252:255], v[124:127], v[92:95]\n\t"
    "s_mov_b32 %[st], 0\n\t"
    "21:\n\t"
    "s_waitcnt lgkmcnt(0)\n\t"
    "v_cmp_ne_u32 vcc, %[fv], v39\n\t"
    "s_cbranch_vccnz 20f\n\t"
    "v_mov_b32 v39, %[cn]\n\t"
    "s_nop 0\n\t"
    "v_cvt_pk_bf16_f32 v40, v80, v81\n\t"
    "v_cvt_pk_bf16_f32 v41, v82, v83\n\t"
    "v_cvt_pk_bf16_f32 v42, v84, v85\n\t"
    "v_cvt_pk_bf16_f32 v43, v86, v87\n\t"
    "global_store_dwordx4 %[co0], v[40:43], %[cb] sc1\n\t"
    "v_cvt_pk_bf16_f32 v44, v88, v89\n\t"
    "v_cvt_pk_bf16_f32 v45, v90, v91\n\t"
    "v_cvt_pk_bf16_f32 v46, v92, v93\n\t"
    "v_cvt_pk_bf16_f32 v47, v94, v95\n\t"
    "global_store_dwordx4 %[co1], v[44:47], %[cb] sc1\n\t"
    "v_cvt_pk_bf16_f32 v48, v48, v49\n\t"
    "v_cvt_pk_bf16_f32 v49, v50, v51\n\t"
    "v_cvt_pk_bf16_f32 v50, v52, v53\n\t"
    "v_cvt_pk_bf16_f32 v51, v54, v55\n\t"
    "global_store_dwordx4 %[co0], v[48:51], %[cb] offset:2048 sc1\n\t"
    "ds_read_b32 v38, v39 offset:4\n\t"
    "v_cvt_pk_bf16_f32 v56, v56, v57\n\t"
    "v_cvt_pk_bf16_f32 v57, v58, v59\n\t"
    "v_cvt_pk_bf16_f32 v58, v60, v61\n\t"
    "v_cvt_pk_bf16_f32 v59, v62, v63\n\t"
    "global_store_dwordx4 %[co1], v[56:59], %[cb] offset:2048 sc1\n\t"
    "s_memrealtime s[36:37]\n\t"
    "v_cmp_u_f32 vcc, v80, v88\n\t"
    "s_waitcnt lgkmcnt(0)\n\t"
    "s_or_b32 %[bad], vcc_lo, vcc_hi\n\t"
    "v_readfirstlane_b32 %[pk], v38\n\t"
    "s_sub_u32 %[tk], s36, %[tk]\n\t"
    "s_branch 9f\n\t"
    "20:\n\t"
    "ds_read_b32 v39, %[fa]\n\t"
    "ds_read_b128 v[48:51], %[rw] offset:0\n\t"
    "ds_read_b128 v[52:55], %[rw] offset:1024\n\t"
    "ds_read_b128 v[56:59], %[rw] offset:2048\n\t"
    "ds_read_b128 v[60:63], %[rw] offset:3072\n\t"
    "s_branch 21b\n\t"
    "13:\n\t"
    "s_memrealtime s[36:37]\n\t"
    "v_mov_b32 v79, 0\n\t"
    "s_waitcnt lgkmcnt(0)\n\t"
    "s_mov_b32 %[tk], s36\n\t"
    "s_add_u32 s36, s36, %[bl]\n\t"
    "s_and_b32 s36, s36, 0xffffff\n\t"
    "s_and_b32 s37, %[bl], 0xff000000\n\t"
    "s_or_b32 s36, s36, s37\n\t"
    "v_mov_b32 v78, s36\n\t"
    "s_mov_b64 exec, 1\n\t"
    "global_store_dword v79, v78, %[pg] offset:64 sc1\n\t"
    "s_mov_b64 exec, -1\n\t"
    "s_branch 14b\n\t"
    "3:\n\t"
    "7:\n\t"
    "s_and_b32 %[st], %[pv], 1\n\t"
    "s_mul_i32 %[st], %[st], 36\n\t"
    "s_add_u32 %[st], %[st], %[cn]\n\t"
    "v_mov_b32 v76, %[st]\n\t"
    "v_mov_b32 v77, 1\n\t"
    "v_mov_b32 v78, %[pv]\n\t"
    "v_mov_b32 v79, 0\n\t"
    "s_waitcnt vmcnt(0)\n\t"
    "s_mov_b64 exec, 1\n\t"
    "ds_add_rtn_u32 v77, v76, v77\n\t"
    "s_waitcnt lgkmcnt(0)\n\t"
    "v_readfirstlane_b32 %[st], v77\n\t"
    "s_and_b32 %[st], %[st], 3\n\t"
    "s_cmp_lg_u32 %[st], 3\n\t"
    "s_cbranch_scc1 6f\n\t"
    "global_store_dword v79, v78, %[pg] sc1\n\t"
    "6:\n\t"
    "s_mov_b64 exec, -1\n\t"
    "s_bitcmp1_b32 %[x], 2\n\t"
    "s_cbranch_scc1 8b\n\t"
    "s_branch 4b\n\t"
    "12:\n\t"
    "s_waitcnt lgkmcnt(0)\n\t"
    "9:"
    : [st] "=&s"(st), [tk] "+&s"(tk), [bad] "=&s"(bad), [pk] "=&s"(pk)
    : [ea] "v"(ea), [da] "v"(da), [x] "s"(x), "{v[128:159]}"(W[0]), "{v[160:191]}"(W[1]), "{v[192:223]}"(W[2]), "{v[224:255]}"(W[3]), [pv] "s"(pv), [cn] "s"(cn), [pg] "s"(pg), [bl] "s"(bl), [rw] "v"(rw), [fa] "v"(fa), [fv] "s"(fv), [cb] "s"(cb), [co0] "v"(co0), [co1] "v"(co1)
    : "v96", "v97", "v98", "v99", "v100", "v101", "v102", "v103", "v104", "v105", "v106", "v107", "v108", "v109", "v110", "v111", "v112", "v113", "v114", "v115", "v116", "v117", "v118", "v119", "v120", "v121", "v122", "v123", "v124", "v125", "v126", "v127", "v76", "v77", "v78", "v79", "v38", "v39", "v40", "v41", "v42", "v43", "v44", "v45", "v46", "v47", "v48", "v49", "v50", "v51", "v52", "v53", "v54", "v55", "v56", "v57", "v58", "v59", "v60", "v61", "v62", "v63", "v80", "v81", "v82", "v83", "v84", "v85", "v86", "v87", "v88", "v89", "v90", "v91", "v92", "v93", "v94", "v95", "vcc", "scc", "m0", "memory", "s36", "s37", "s38", "s39");
}
#define LEAN_QB 1
#define PREK 10
#define NEARLY 16
__device__ __forceinline__ void fin_ld(const u32x16& pd, unsigned rw, unsigned fa, unsigned fv) {
  asm volatile("ds_write_b128 %[rw], v[80:83] offset:0\n\t"
    "ds_write_b128 %[rw], v[84:87] offset:1024\n\t"
    "ds_write_b128 %[rw], v[88:91] offset:2048\n\t"
    "ds_write_b128 %[rw], v[92:95] offset:3072\n\t"
    "ds_write_b32 %[fa], %[fv]"
    :
    : [rw] "v"(rw), "{v[80:95]}"(pd), [fa] "v"(fa), [fv] "v"(fv)
    : "memory");
}
__device__ __forceinline__ void fin_st0(const u32x16& pd, unsigned rw, unsigned fa, unsigned fv, const char* cb, unsigned co0, unsigned co1, unsigned pa, unsigned& bad, unsigned& pk) {
  asm volatile("1:\n\t"
    "ds_read_b32 v39, %[fa]\n\t"
    "ds_read_b128 v[48:51], %[rw] offset:0\n\t"
    "ds_read_b128 v[52:55], %[rw] offset:1024\n\t"
    "ds_read_b128 v[56:59], %[rw] offset:2048\n\t"
    "ds_read_b128 v[60:63], %[rw] offset:3072\n\t"
    "s_waitcnt lgkmcnt(2)\n\t"
    "v_cmp_ne_u32 vcc, v39, %[fv]\n\t"
    "s_cbranch_vccz 2f\n\t"
    "s_waitcnt lgkmcnt(0)\n\t"
    "s_branch 1b\n\t"
    "2:\n\t"
    "v_pk_add_f32 v[48:49], v[48:49], v[80:81]\n\t"
    "v_pk_add_f32 v[50:51], v[50:51], v[82:83]\n\t"
    "v_pk_add_f32 v[52:53], v[52:53], v[84:85]\n\t"
    "v_pk_add_f32 v[54:55], v[54:55], v[86:87]\n\t"
    "v_cvt_pk_bf16_f32 v40, v48, v49\n\t"
    "v_cvt_pk_bf16_f32 v41, v50, v51\n\t"
    "v_cvt_pk_bf16_f32 v42, v52, v53\n\t"
    "v_cvt_pk_bf16_f32 v43, v54, v55\n\t"
    "global_store_dwordx4 %[co0], v[40:43], %[cb] sc1\n\t"
    "s_waitcnt lgkmcnt(0)\n\t"
    "v_mov_b32 v39, %[pa]\n\t"
    "ds_read_b32 v39, v39\n\t"
    "v_pk_add_f32 v[56:57], v[56:57], v[88:89]\n\t"
    "v_pk_add_f32 v[58:59], v[58:59], v[90:91]\n\t"
    "v_pk_add_f32 v[60:61], v[60:61], v[92:93]\n\t"
    "v_pk_add_f32 v[62:63], v[62:63], v[94:95]\n\t"
    "v_cvt_pk_bf16_f32 v44, v56, v57\n\t"
    "v_cvt_pk_bf16_f32 v45, v58, v59\n\t"
    "v_cvt_pk_bf16_f32 v46, v60, v61\n\t"
    "v_cvt_pk_bf16_f32 v47, v62, v63\n\t"
    "global_store_dwordx4 %[co1], v[44:47], %[cb] sc1\n\t"
    "v_cmp_u_f32 vcc, v48, v56\n\t"
    "s_waitcnt lgkmcnt(0)\n\t"
    "s_or_b32 %[bad], vcc_lo, vcc_hi\n\t"
    "v_readfirstlane_b32 %[pk], v39"
    : [bad] "=&s"(bad), [pk] "=&s"(pk)
    : [rw] "v"(rw), "{v[80:95]}"(pd), [fa] "v"(fa), [fv] "v"(fv), [cb] "s"(cb), [co0] "v"(co0), [co1] "v"(co1), [pa] "s"(pa)
    : "v39", "v40", "v41", "v42", "v43", "v44", "v45", "v46", "v47", "v48", "v49", "v50", "v51", "v52", "v53", "v54", "v55", "v56", "v57", "v58", "v59", "v60", "v61", "v62", "v63", "vcc", "scc", "memory");
}
__device__ __forceinline__ void fin_st1(const u32x16& pd, unsigned rw, unsigned fa, unsigned fv, const char* cb, unsigned co0, unsigned co1, unsigned pa, unsigned& bad, unsigned& pk) {
  asm volatile("1:\n\t"
    "ds_read_b32 v39, %[fa]\n\t"
    "ds_read_b128 v[48:51], %[rw] offset:0\n\t"
    "ds_read_b128 v[52:55], %[rw] offset:1024\n\t"
    "ds_read_b128 v[56:59], %[rw] offset:2048\n\t"
    "ds_read_b128 v[60:63], %[rw] offset:3072\n\t"
    "s_waitcnt lgkmcnt(2)\n\t"
    "v_cmp_ne_u32 vcc, v39, %[fv]\n\t"
    "s_cbranch_vccz 2f\n\t"
    "s_waitcnt lgkmcnt(0)\n\t"
    "s_branch 1b\n\t"
    "2:\n\t"
    "v_pk_add_f32 v[48:49], v[48:49], v[80:81]\n\t"
    "v_pk_add_f32 v[50:51], v[50:51], v[82:83]\n\t"
    "v_pk_add_f32 v[52:53], v[52:53], v[84:85]\n\t"
    "v_pk_add_f32 v[54:55], v[54:55], v[86:87]\n\t"
    "s_waitcnt lgkmcnt(0)\n\t"
    "v_mov_b32 v39, %[pa]\n\t"
    "ds_read_b32 v39, v39\n\t"
    "v_pk_add_f32 v[56:57], v[56:57], v[88:89]\n\t"
    "v_pk_add_f32 v[58:59], v[58:59], v[90:91]\n\t"
    "v_pk_add_f32 v[60:61], v[60:61], v[92:93]\n\t"
    "v_pk_add_f32 v[62:63], v[62:63], v[94:95]\n\t"
    "v_mul_f32 v40, %[nl], v48\n\t"
    "v_mul_f32 v41, %[nl], v49\n\t"
    "v_mul_f32 v42, %[nl], v50\n\t"
    "v_mul_f32 v43, %[nl], v51\n\t"
    "v_mul_f32 v44, %[nl], v56\n\t"
    "v_mul_f32 v45, %[nl], v57\n\t"
    "v_mul_f32 v46, %[nl], v58\n\t"
    "v_mul_f32 v47, %[nl], v59\n\t"
    "v_exp_f32 v40, v40\n\t"
    "v_exp_f32 v41, v41\n\t"
    "v_exp_f32 v42, v42\n\t"
    "v_exp_f32 v43, v43\n\t"
    "v_exp_f32 v44, v44\n\t"
    "v_exp_f32 v45, v45\n\t"
    "v_exp_f32 v46, v46\n\t"
    "v_exp_f32 v47, v47\n\t"
    "v_pk_add_f32 v[40:41], v[40:41], 1.0 op_sel_hi:[1,0]\n\t"
    "v_pk_add_f32 v[42:43], v[42:43], 1.0 op_sel_hi:[1,0]\n\t"
    "v_pk_add_f32 v[44:45], v[44:45], 1.0 op_sel_hi:[1,0]\n\t"
    "v_pk_add_f32 v[46:47], v[46:47], 1.0 op_sel_hi:[1,0]\n\t"
    "v_rcp_f32 v40, v40\n\t"
    "v_rcp_f32 v41, v41\n\t"
    "v_rcp_f32 v42, v42\n\t"
    "v_rcp_f32 v43, v43\n\t"
    "v_rcp_f32 v44, v44\n\t"
    "v_rcp_f32 v45, v45\n\t"
    "v_rcp_f32 v46, v46\n\t"
    "v_rcp_f32 v47, v47\n\t"
    "v_pk_mul_f32 v[40:41], v[48:49], v[40:41]\n\t"
    "v_pk_mul_f32 v[42:43], v[50:51], v[42:43]\n\t"
    "v_pk_mul_f32 v[44:45], v[56:57], v[44:45]\n\t"
    "v_pk_mul_f32 v[46:47], v[58:59], v[46:47]\n\t"
    "v_pk_mul_f32 v[40:41], v[40:41], v[52:53]\n\t"
    "v_pk_mul_f32 v[42:43], v[42:43], v[54:55]\n\t"
    "v_pk_mul_f32 v[44:45], v[44:45], v[60:61]\n\t"
    "v_pk_mul_f32 v[46:47], v[46:47], v[62:63]\n\t"
    "v_cvt_pk_bf16_f32 v48, v40, v41\n\t"
    "v_cvt_pk_bf16_f32 v49, v42, v43\n\t"
    "v_cvt_pk_bf16_f32 v50, v44, v45\n\t"
    "v_cvt_pk_bf16_f32 v51, v46, v47\n\t"
    "s_nop 1\n\t"
    "v_permlane16_swap_b32 v48, v50\n\t"
    "v_permlane16_swap_b32 v49, v51\n\t"
    "global_store_dwordx4 %[co0], v[48:51], %[cb] sc1\n\t"
    "v_cmp_u_f32 vcc, v52, v60\n\t"
    "s_waitcnt lgkmcnt(0)\n\t"
    "s_or_b32 %[bad], vcc_lo, vcc_hi\n\t"
    "v_readfirstlane_b32 %[pk], v39"
    : [bad] "=&s"(bad), [pk] "=&s"(pk)
    : [rw] "v"(rw), "{v[80:95]}"(pd), [fa] "v"(fa), [fv] "v"(fv), [cb] "s"(cb), [co0] "v"(co0), [nl] "s"(-LOG2E), [pa] "s"(pa)
    : "v39", "v40", "v41", "v42", "v43", "v44", "v45", "v46", "v47", "v48", "v49", "v50", "v51", "v52", "v53", "v54", "v55", "v56", "v57", "v58", "v59", "v60", "v61", "v62", "v63", "vcc", "scc", "memory");
}
__device__ __forceinline__ void fin_st2(const u32x16& pd, unsigned rw, unsigned fa, unsigned fv, const char* cb, unsigned co0, unsigned co1, unsigned pa, unsigned& bad, unsigned& pk) {
  asm volatile("s_nop 1\n\t"
    "v_cvt_pk_bf16_f32 v40, v80, v81\n\t"
    "v_cvt_pk_bf16_f32 v41, v82, v83\n\t"
    "v_cvt_pk_bf16_f32 v42, v84, v85\n\t"
    "v_cvt_pk_bf16_f32 v43, v86, v87\n\t"
    "global_store_dwordx4 %[co0], v[40:43], %[cb] sc1\n\t"
    "v_cvt_pk_bf16_f32 v44, v88, v89\n\t"
    "v_cvt_pk_bf16_f32 v45, v90, v91\n\t"
    "v_cvt_pk_bf16_f32 v46, v92, v93\n\t"
    "v_cvt_pk_bf16_f32 v47, v94, v95\n\t"
    "global_store_dwordx4 %[co1], v[44:47], %[cb] sc1\n\t"
    "1:\n\t"
    "ds_read_b32 v39, %[fa]\n\t"
    "ds_read_b128 v[48:51], %[rw] offset:0\n\t"
    "ds_read_b128 v[52:55], %[rw] offset:1024\n\t"
    "ds_read_b128 v[56:59], %[rw] offset:2048\n\t"
    "ds_read_b128 v[60:63], %[rw] offset:3072\n\t"
    "s_waitcnt lgkmcnt(2)\n\t"
    "v_cmp_ne_u32 vcc, v39, %[fv]\n\t"
    "s_cbranch_vccz 2f\n\t"
    "s_waitcnt lgkmcnt(0)\n\t"
    "s_branch 1b\n\t"
    "2:\n\t"
    "v_cvt_pk_bf16_f32 v48, v48, v49\n\t"
    "v_cvt_pk_bf16_f32 v49, v50, v51\n\t"
    "v_cvt_pk_bf16_f32 v50, v52, v53\n\t"
    "v_cvt_pk_bf16_f32 v51, v54, v55\n\t"
    "global_store_dwordx4 %[co0], v[48:51], %[cb] offset:2048 sc1\n\t"
    "s_waitcnt lgkmcnt(0)\n\t"
    "v_mov_b32 v39, %[pa]\n\t"
    "ds_read_b32 v39, v39\n\t"
    "v_cvt_pk_bf16_f32 v56, v56, v57\n\t"
    "v_cvt_pk_bf16_f32 v57, v58, v59\n\t"
    "v_cvt_pk_bf16_f32 v58, v60, v61\n\t"
    "v_cvt_pk_bf16_f32 v59, v62, v63\n\t"
    "global_store_dwordx4 %[co1], v[56:59], %[cb] offset:2048 sc1\n\t"
    "v_cmp_u_f32 vcc, v80, v88\n\t"
    "s_waitcnt lgkmcnt(0)\n\t"
    "s_or_b32 %[bad], vcc_lo, vcc_hi\n\t"
    "v_readfirstlane_b32 %[pk], v39"
    : [bad] "=&s"(bad), [pk] "=&s"(pk)
    : [rw] "v"(rw), "{v[80:95]}"(pd), [fa] "v"(fa), [fv] "v"(fv), [cb] "s"(cb), [co0] "v"(co0), [co1] "v"(co1), [pa] "s"(pa)
    : "v39", "v40", "v41", "v42", "v43", "v44", "v45", "v46", "v47", "v48", "v49", "v50", "v51", "v52", "v53", "v54", "v55", "v56", "v57", "v58", "v59", "v60", "v61", "v62", "v63", "vcc", "scc", "memory");
}
template <int R> __device__ __forceinline__ void gw_load(u32x32& w, unsigned a0, unsigned a1);
template <> __device__ __forceinline__ void gw_load<0>(u32x32& w, unsigned a0, unsigned a1) {
  asm volatile("ds_read_b64_tr_b16 v[128:129], %1 offset:0\n\t"
    "ds_read_b64_tr_b16 v[130:131], %1 offset:512\n\t"
    "ds_read_b64_tr_b16 v[132:133], %2 offset:0\n\t"
    "ds_read_b64_tr_b16 v[134:135], %2 offset:512\n\t"
    "ds_read_b64_tr_b16 v[136:137], %1 offset:4096\n\t"
    "ds_read_b64_tr_b16 v[138:139], %1 offset:4608\n\t"
    "ds_read_b64_tr_b16 v[140:141], %2 offset:4096\n\t"
    "ds_read_b64_tr_b16 v[142:143], %2 offset:4608\n\t"
    "s_waitcnt lgkmcnt(0)\n\t"
    "ds_read_b64_tr_b16 v[144:145], %1 offset:16384\n\t"
    "ds_read_b64_tr_b16 v[146:147], %1 offset:16896\n\t"
    "ds_read_b64_tr_b16 v[148:149], %2 offset:16384\n\t"
    "ds_read_b64_tr_b16 v[150:151], %2 offset:16896\n\t"
    "ds_read_b64_tr_b16 v[152:153], %1 offset:20480\n\t"
    "ds_read_b64_tr_b16 v[154:155], %1 offset:20992\n\t"
    "ds_read_b64_tr_b16 v[156:157], %2 offset:20480\n\t"
    "ds_read_b64_tr_b16 v[158:159], %2 offset:20992\n\t"
    "s_waitcnt lgkmcnt(0)"
    : "=&{v[128:159]}"(w) : "v"(a0), "v"(a1) : "memory");
}
template <> __device__ __forceinline__ void gw_load<1>(u32x32& w, unsigned a0, unsigned a1) {
  asm volatile("ds_read_b64_tr_b16 v[160:161], %1 offset:0\n\t"
    "ds_read_b64_tr_b16 v[162:163], %1 offset:512\n\t"
    "ds_read_b64_tr_b16 v[164:165], %2 offset:0\n\t"
    "ds_read_b64_tr_b16 v[166:167], %2 offset:512\n\t"
    "ds_read_b64_tr_b16 v[168:169], %1 offset:4096\n\t"
    "ds_read_b64_tr_b16 v[170:171], %1 offset:4608\n\t"
    "ds_read_b64_tr_b16 v[172:173], %2 offset:4096\n\t"
    "ds_read_b64_tr_b16 v[174:175], %2 offset:4608\n\t"
    "s_waitcnt lgkmcnt(0)\n\t"
    "ds_read_b64_tr_b16 v[176:177], %1 offset:16384\n\t"
    "ds_read_b64_tr_b16 v[178:179], %1 offset:16896\n\t"
    "ds_read_b64_tr_b16 v[180:181], %2 offset:16384\n\t"
    "ds_read_b64_tr_b16 v[182:183], %2 offset:16896\n\t"
    "ds_read_b64_tr_b16 v[184:185], %1 offset:20480\n\t"
    "ds_read_b64_tr_b16 v[186:187], %1 offset:20992\n\t"
    "ds_read_b64_tr_b16 v[188:189], %2 offset:20480\n\t"
    "ds_read_b64_tr_b16 v[190:191], %2 offset:20992\n\t"
    "s_waitcnt lgkmcnt(0)"
    : "=&{v[160:191]}"(w) : "v"(a0), "v"(a1) : "memory");
}
template <> __device__ __forceinline__ void gw_load<2>(u32x32& w, unsigned a0, unsigned a1) {
  asm volatile("ds_read_b64_tr_b16 v[192:193], %1 offset:0\n\t"
    "ds_read_b64_tr_b16 v[194:195], %1 offset:512\n\t"
    "ds_read_b64_tr_b16 v[196:197], %2 offset:0\n\t"
    "ds_read_b64_tr_b16 v[198:199], %2 offset:512\n\t"
    "ds_read_b64_tr_b16 v[200:201], %1 offset:4096\n\t"
    "ds_read_b64_tr_b16 v[202:203], %1 offset:4608\n\t"
    "ds_read_b64_tr_b16 v[204:205], %2 offset:4096\n\t"
    "ds_read_b64_tr_b16 v[206:207], %2 offset:4608\n\t"
    "s_waitcnt lgkmcnt(0)\n\t"
    "ds_read_b64_tr_b16 v[208:209], %1 offset:16384\n\t"
    "ds_read_b64_tr_b16 v[210:211], %1 offset:16896\n\t"
    "ds_read_b64_tr_b16 v[212:213], %2 offset:16384\n\t"
    "ds_read_b64_tr_b16 v[214:215], %2 offset:16896\n\t"
    "ds_read_b64_tr_b16 v[216:217], %1 offset:20480\n\t"
    "ds_read_b64_tr_b16 v[218:219], %1 offset:20992\n\t"
    "ds_read_b64_tr_b16 v[220:221], %2 offset:20480\n\t"
    "ds_read_b64_tr_b16 v[222:223], %2 offset:20992\n\t"
    "s_waitcnt lgkmcnt(0)"
    : "=&{v[192:223]}"(w) : "v"(a0), "v"(a1) : "memory");
}
template <> __device__ __forceinline__ void gw_load<3>(u32x32& w, unsigned a0, unsigned a1) {
  asm volatile("ds_read_b64_tr_b16 v[224:225], %1 offset:0\n\t"
    "ds_read_b64_tr_b16 v[226:227], %1 offset:512\n\t"
    "ds_read_b64_tr_b16 v[228:229], %2 offset:0\n\t"
    "ds_read_b64_tr_b16 v[230:231], %2 offset:512\n\t"
    "ds_read_b64_tr_b16 v[232:233], %1 offset:4096\n\t"
    "ds_read_b64_tr_b16 v[234:235], %1 offset:4608\n\t"
    "ds_read_b64_tr_b16 v[236:237], %2 offset:4096\n\t"
    "ds_read_b64_tr_b16 v[238:239], %2 offset:4608\n\t"
    "s_waitcnt lgkmcnt(0)\n\t"
    "ds_read_b64_tr_b16 v[240:241], %1 offset:16384\n\t"
    "ds_read_b64_tr_b16 v[242:243], %1 offset:16896\n\t"
    "ds_read_b64_tr_b16 v[244:245], %2 offset:16384\n\t"
    "ds_read_b64_tr_b16 v[246:247], %2 offset:16896\n\t"
    "ds_read_b64_tr_b16 v[248:249], %1 offset:20480\n\t"
    "ds_read_b64_tr_b16 v[250:251], %1 offset:20992\n\t"
    "ds_read_b64_tr_b16 v[252:253], %2 offset:20480\n\t"
    "ds_read_b64_tr_b16 v[254:255], %2 offset:20992\n\t"
    "s_waitcnt lgkmcnt(0)"
    : "=&{v[224:255]}"(w) : "v"(a0), "v"(a1) : "memory");
}
template <int I, int E, class F> __device__ __forceinline__ void sfor(F&& f) { if constexpr (I < E) { f(std::integral_constant<int, I>{}); sfor<I + 1, E>(f); } }
#define W2C 0
template <int ROLE, int KQ, class F>
__device__ __forceinline__ void wload(char* smem, int wave_id_, const u16* W0, const u16* W1, int ldb, u32x32 (&bw)[4], int rot, int mode, F&& hook) {
  constexpr int ABUF = 65536;
  const int tid = opaque_tid(), lane = tid & 63, wave = WAVE_ID;
  const int kq = KQ == 2 ? wave >> 2 : 0, nq = KQ == 2 ? wave & 3 : wave;
  const int g = lane >> 4, r16 = lane & 15, q4 = r16 >> 2, p4 = lane & 3;
  const unsigned ldsb = __builtin_amdgcn_readfirstlane((unsigned)(size_t)(LAS char*)smem);
  {
    const int srow = 8 * wave + (lane >> 3), ch = (lane & 7) ^ bswz(srow);
    const unsigned voff = (unsigned)(srow * ldb + ch * 8) * 2;
    const char* w0 = uni((const char*)W0); const char* w1 = uni((const char*)(ROLE == R_F1 ? W1 : W0 + 64));
    const size_t kb = (size_t)64 * ldb * 2;
    auto wstage = [&](int r) {
      const unsigned ld = ldsb + (r & 1) * ABUF + wave * 1024;
#pragma unroll
      for (int i = 0; i < 8; ++i) {
        const int kbi = KQ == 2 ? (ROLE == R_PART2 ? 0 : (i >> 2) * 8) + ((2 * r + ((i >> 1) & 1)) ^ rot) : 2 * r + (i >> 2);
        const char* src = KQ == 2 ? ((i & 1) ? w1 : w0) + kbi * kb + (ROLE == R_PART2 ? (i >> 2) * 256 : 0) : w0 + kbi * kb + (i & 3) * 128;
        unsigned keep;
        if (W2C && (ROLE == R_PART || ROLE == R_PART2)) glds16s<0>(src, voff, ld + i * 8192);
        else
        asm volatile("s_mov_b32 %0, m0\n\ts_mov_b32 m0, %3\n\ts_nop 2\n\tglobal_load_lds_dwordx4 %1, %2" WNT_ "\n\ts_mov_b32 m0, %0"
                     : "=&s"(keep) : "v"(voff), "s"(src), "s"(ld + i * 8192) : "memory");
      }
    };
    if (mode == 1) { wstage(0); wstage(1); return; }
    const int brow = 8 * g + q4, bs = bswz(brow);
    unsigned la[2];
#pragma unroll
    for (int j = 0; j < 2; ++j) {
      int blk, c16, sub;
      if (ROLE == R_F1) { blk = j; c16 = nq * 2 + (p4 >> 1); sub = (p4 & 1) * 8; }
      else { blk = nq >> 1; c16 = (nq & 1) * 4 + p4; sub = j * 8; }
      int set = kq;
      if (ROLE == R_PART2 && TLP) { set = nq >> 1; blk = nq & 1; c16 = kq * 4 + p4; }
      la[j] = (unsigned)((KQ == 2 ? set * 4 + blk : blk) * 8192 + brow * 128 + ((c16 ^ bs) << 4) + sub);
    }
    hook(4);
    if (mode != 2) { wstage(0); wstage(1); }
    sfor<0, 4>([&](auto r_c) __attribute__((always_inline)) {
      constexpr int r = decltype(r_c)::value;
      if (r < 3) WAIT_V(8); else WAIT_V(0);
      __builtin_amdgcn_s_barrier(); asm volatile("" ::: "memory");
      gw_load<r>(bw[r], ldsb + (r & 1) * ABUF + la[0], ldsb + (r & 1) * ABUF + la[1]);
      BAR();
      hook(r);
      if (r + 2 < 4) wstage(r + 2);
    });
  }
}
#define DSR2(d0, d1, a0_, a1_, off) asm volatile("ds_read_b128 %0, %2 offset:%4\n\tds_read_b128 %1, %3 offset:%4" : "=&v"(d0), "=&v"(d1) : "v"(a0_), "v"(a1_), "i"(off))
template <int N> __device__ __forceinline__ void waitl(u32x4& a, u32x4& b) { asm volatile("s_waitcnt lgkmcnt(%2)" : "+v"(a), "+v"(b) : "i"(N)); }
__device__ __forceinline__ void glds1(const char* sbase, unsigned voff, unsigned lds) {
  unsigned keep;
  asm volatile("s_mov_b32 %0, m0\n\ts_mov_b32 m0, %3\n\ts_nop 2\n\tglobal_load_lds_dwordx4 %1, %2 sc1\n\ts_mov_b32 m0, %0"
               : "=&s"(keep) : "v"(voff), "s"(sbase), "s"(lds) : "memory");
}
#define XS(i) do { } while (0)
#define XFL(b_) do { } while (0)
#define XA_ 0
#define XF_ 0
#define XE_ 0
#define XP_ 0
#define PUBK 9
#define LATE 0
#define NOWP 0
#define TB 48
#define TDM 1
#define TDA 15
#define TPUB 1
#define DURH 140
#define DUR0 160
#define PUP 4
#define PDQ 5
#define PDM 2
#define PDEEP 5
template <int ROLE>
__device__ __forceinline__ void gemm7(char* smem, int wave_id_, const u16* W0, const u16* W1, int ldb, const char* A_, size_t a_blk, Dep dep,
                                      char* C_, size_t c_blk, unsigned*& cnt, int myw, unsigned* tmo, int slot, unsigned long long* mts,
                                      bool gl, int nimm = 0, int nseg = NSEG, int wm0 = 0, int pofs = 0, u64 tag = 0, int b0 = 0) {
  dep.base = (const char*)cnt;
  unsigned* myprog = cnt + myw;
  volatile LAS unsigned* wsl = (volatile LAS unsigned*)(smem + L_SLOT + 512);
  if (opaque_tid() < 144) *(volatile LAS unsigned*)(smem + L_SLOT + opaque_tid() * 4) = 0u;
  const int tid = opaque_tid(), lane = tid & 63, wave = WAVE_ID;
  const int kq = wave >> 2, nq = wave & 3;
  const unsigned ldsb = __builtin_amdgcn_readfirstlane((unsigned)(size_t)(LAS char*)smem);
  constexpr int rot = 0;
  const char* A = uni(A_) + nq * 1024;
  const char* Cu = uni(C_);
  constexpr int PD = PDM;
  constexpr bool P2C = ROLE == R_PART2;
  int pw = 0;
  int wm = wm0, nh = 2 * b0;
  constexpr bool SPEC = ROLE != R_QKV;
  const int sbi = gl ? SC : SB, sbo = ROLE == R_F1 || gl ? SC : SB;
  auto lgo = [&](int b, unsigned& v, unsigned& u, bool pre, bool force) {
    if (wave == 4) {
      for (int spins = 0; __builtin_amdgcn_readfirstlane(wsl[2]) != (unsigned)(b + 1); ++spins) {
        if (!pre) ld_fb(v, u, dep.base, dep.voff);
        const bool ok = (int)(v * dep.mul + dep.add) > b;
        if (__all(ok)) break;
        if (SPEC && !force && bells(ok, u, b, b0 ? MARH : MARG, sbi)) break;
        if (!pre) { if (gl) __builtin_amdgcn_s_sleep(GSL); else __builtin_amdgcn_s_sleep(XSL); }
        pre = false;
        if (give_up(spins, tmo, 9)) { v = 255u; break; }
      }
      wsl[2] = (unsigned)(b + 1);
    } else {
      while (__builtin_amdgcn_readfirstlane(wsl[2]) != (unsigned)(b + 1)) __builtin_amdgcn_s_sleep(1);
    }
  };
  auto lwm = [&](int b, unsigned v) {
    unsigned w;
    if (wave == 4) {
      const int w2 = (int)dep_eval(v, dep);
      w = (unsigned)min(max(max(w2, wm), b + 1), 255);
      wsl[0] = ((unsigned)(b + 1) << 8) | w;
    } else {
      for (;;) {
        w = __builtin_amdgcn_readfirstlane(wsl[0]);
        if ((w >> 8) == (unsigned)(b + 1)) break;
        __builtin_amdgcn_s_sleep(1);
      }
      w &= 255u;
    }
    return (int)w;
  };
  auto pubw = [&](int n) {
    WAIT_V(0);
    if (lane == 0) {
      const unsigned o = __hip_atomic_fetch_add((LAS unsigned*)(smem + L_SLOT + 516 + ((n + pofs) & 1) * 36), 1u, __ATOMIC_RELAXED, __HIP_MEMORY_SCOPE_WORKGROUP);
      if ((o & 3u) == 3u) {
        st4_sc1(myprog, (unsigned)(n + pofs));
      }
    }
    asm volatile("s_waitcnt lgkmcnt(0)" ::: "memory");
  };
  auto hsrc = [&](int hh) { return A + (size_t)(hh >> 1) * a_blk + ((hh ^ (rot >> 2)) & 1) * 16384; };
  auto hlds = [&](int hh) { return ldsb + (unsigned)(((hh >> 1) & 1) * 65536 + (hh & 1) * 16384 + nq * 1024); };
#define POFF(i_) (((i_) & 1) * 32768 + ((i_) >> 1) * 4096)
#define PIECE(s_, l_, i_) asm volatile("s_add_u32 m0, %2, %3\n\ts_nop 0\n\tglobal_load_lds_dwordx4 %0, %1 sc1" :: "v"(av[i_]), "s"(s_), "s"(l_), "i"(POFF(i_)) : "memory", "scc", "m0")
  int na = max(sbo, b0);
  auto arm = [&](int bb) {
    unsigned z = SENT; asm volatile("" : "+v"(z));
    constexpr int N = ROLE == R_F1 ? 1 : (P2C ? 4 : 2);
    const unsigned o = nq * N * 1024 + lane_id() * 16;
    const char* p = Cu + (size_t)bb * c_blk;
#pragma unroll
    for (int i = 0; i < N; ++i) st16s(p, o + i * 1024, u32x4{z, z, z, z});
  };
  u32x32 BW[4];
  wload<ROLE, 2>(smem, wave_id_, W0, W1, ldb, BW, rot, SPRO && ROLE == R_QKV && nimm > 0 ? 2 : 0, [&](int r) {
    if (ROLE == R_QKV && nimm > 0 && wave == 0) STS((char*)tmo - OFF_TMO, 128 + slot, 3 + r);
    if (SPEC && r == 2 && (ROLE != R_PART || gl)) { for (na += kq; na < nseg; na += 2) arm(na); na = nseg + 1; }
    if (ROLE == R_QKV && nimm > 0 && r == 1 && wave == 4) init_issue((char*)tmo - OFF_TMO, ldsb + L_RED);
    if (ROLE == R_QKV && nimm > 0 && r == 2) {
      cnt = cnt_of((char*)tmo - OFF_TMO, init_get((char*)tmo - OFF_TMO, tag, smem + L_RED));
      dep.base = (const char*)cnt; myprog = cnt + myw;
      if (kq) {
        const unsigned l16 = lane_id() * 16;
#pragma unroll
        for (int i = 0; i < 16; ++i) glds1(hsrc(i >> 3) + (i & 1) * 32768 + ((((i & 7) >> 1) ^ rot) & 3) * 4096, l16, hlds(i >> 3) + POFF(i & 7));
      }
      nh = 2; wm = 8;
    }
  });
  if (ROLE == R_QKV && nimm > 0 && opaque_tid() == 0) STS((char*)tmo - OFF_TMO, 128 + slot, 0);
  if (wave == 0) WFB();
  auto body = [&](auto ldr_c) __attribute__((always_inline)) {
    constexpr bool LDR = decltype(ldr_c)::value;
    if (XP_ == 0 ? !LDR : (XP_ == 1 ? LDR : false)) __builtin_amdgcn_s_setprio(1);
    unsigned av[8];
#pragma unroll
    for (int i = 0; i < 8; ++i) av[i] = lane * 16 + (P2C ? 0 : (i & 1) * 32768) + (((i >> 1) ^ rot) & 3) * 4096;
    auto issue_all = [&](int hh) {
      if (LDR) {
        const char* s_ = hsrc(hh); const unsigned l_ = hlds(hh);
#pragma unroll
        for (int i = 0; i < 8; ++i) if (!(P2C && (i & 1))) PIECE(s_, l_, i);
      }
    };
    int b = b0;
    bool pre = false;
    unsigned smu = 0, tk = 0;
    unsigned smp = 0, got = 0;
    while (b < nseg) {
      const bool fast = (wm < nseg ? wm : nseg) >= b + PD && b >= nimm && nh <= 2 * b + 2;
      if (SPEC && b >= TB && !fast) break;
      const bool pl = wm <= b || pre, cold = nh < 2 * b + 2, nh3 = nh == 2 * b + 3;
      if (!fast) {
        XS(0);
        if (LDR && wm <= b && !got) lgo(b, smp, smu, false, true);
        XS(1);
        if (got) nh += NEARLY / 8;
        { const int upto = nh3 ? 2 * b + 4 : 2 * b + 2; for (; nh < upto; ++nh) issue_all(nh); }
        if (LDR && pl) wm = lwm(b, smp);
        XS(2);
      }
      const int ln = lane_id(), g = ln >> 4, r16 = ln & 15;
      const unsigned e0 = ldsb + (unsigned)((P2C ? 0 : kq * 32768) + (b & 1) * 65536 + (gl ? (g >> 1) * 512 + r16 * 32 + (g & 1) * 16 : r16 * 128 + ((g ^ (r16 & 7)) << 4)));
      const unsigned d0 = e0 ^ (gl ? 1024u : 64u);
      const unsigned rw = ldsb + L_RED + nq * 4096 + ln * 16;
      const unsigned co0 = ROLE == R_F1 ? (unsigned)((g & 1) * 2048 + nq * 512 + r16 * 32 + (g >> 1) * 16)
                         : ((ROLE == R_QKV ? TLQ : TLP) ? (unsigned)(nq * (P2C ? 4096 : 2048) + r16 * 64 + g * 16)
                         : (ROLE == R_QKV ? (unsigned)(r16 * 256 + (((4 * nq + g) ^ r16) << 4)) : (unsigned)(r16 * PROW + (32 * nq + 8 * g) * 2)));
      const unsigned co1 = co0 + ((ROLE == R_QKV ? TLQ : TLP) ? 1024u : (ROLE == R_QKV ? 4096u : 16u * PROW));
      u32x16 pv_;
      bool pr2 = false;

      if (fast) {
        __builtin_amdgcn_s_barrier(); asm volatile("" ::: "memory");
        const int LA = (wm < nseg ? wm : nseg) >= b + (ROLE == R_QKV ? PDQ : PDEEP) ? 3 : 2;
        for (; nh < 2 * b + LA; ++nh) issue_all(nh);
        int lf = (wm < nseg ? wm : nseg) - (LA - 1), cp = nseg - (LA - 1);
        const unsigned sa = ldsb + L_SLOT + ln * 4, ad = dep.add - (unsigned)(LA - 1);
        const unsigned up = LA == 3 ? 1000u : (unsigned)PUP;
#define FARGS BW, pv_, e0, d0, b, lf, cp, up, sa, dep.mul, ad, rw, (unsigned)a_blk, hsrc(2 * b + LA), hsrc(2 * b + LA + 1), hlds(2 * b + LA), hlds(2 * b + LA + 1), av
        if constexpr (LDR) (P2C ? fast_ld2 : (ROLE == R_F1 ? fast_ld1 : fast_ld0))(FARGS, LA);
#undef FARGS
        else {
          const char* c0_ = uni((const char*)tmo - OFF_TMO + OFF_DUMP); const char* c1_ = Cu + (size_t)b * c_blk;
          const int sf = b + pofs;
          const char* pg = uni((const char*)myprog);
          (ROLE == R_F1 ? fast_st1 : (P2C ? fast_st2 : fast_st0))(BW, pv_, e0, d0, b, lf, cp, up, sa, dep.mul, ad, rw, (unsigned)c_blk, c0_, c1_, co0, co1, pofs, sf, (unsigned)wave,
                                                                   ldsb + L_SLOT, dep.voff, dep.base, pg);
        }
        wm = lf + LA - 1 > wm ? lf + LA - 1 : wm;
        nh = 2 * b + 2;
        --b;
        __builtin_amdgcn_s_barrier(); asm volatile("" ::: "memory");
      } else {
        if (LDR) { if (nh >= 2 * b + 4) { if (P2C) WAIT_V(14); else WAIT_V(28); } else { if (P2C) WAIT_V(6); else WAIT_V(12); } }
        BAR();
        XS(3);
        if (ROLE == R_QKV && nimm > 0 && b < 2 && opaque_tid() == 0) STS((char*)tmo - OFF_TMO, 128 + slot, 1 + b);
        if constexpr (!LDR) {
          const unsigned wv = wsl[0];
          unsigned st;
          lean_st0(BW, e0, d0, __builtin_amdgcn_readfirstlane(pw > 0 ? (cold ? 4u : 1u) : 0u), pv_, (unsigned)(pw + pofs), ldsb + L_SLOT + 516, uni((const char*)myprog), 0u, tk, st);
          if (pl) wm = (int)(__builtin_amdgcn_readfirstlane(wv) & 255u);
        }
        const bool dm = nh == 2 * b + 2 && wm > b + 1 && b + 1 < nseg;
        pr2 = b + 1 < nseg && wm < nseg;
        if constexpr (LDR) {
          const bool er = nh == 2 * b + 2 && !dm;
          const unsigned ps = pr2 && (wave == 4 || er) ? (unsigned)(b + 1) : 0u;
#define LARGS BW, e0, d0, (unsigned)(nh - 2 * b - 2), hlds(nh), hsrc(nh), hsrc(nh + 1), av, ps, dep.voff, dep.base, \
              rw, ldsb + L_SLOT + 528 + nq * 4, (unsigned)(b + 1), ldsb + L_SLOT + 520, dep.mul, dep.add, er ? b + 1 : 0x7FFFFFFF, 0x7FFFFFFF, smp, smu, got
          if (dm) (P2C ? lean_dm1 : lean_dm0)(LARGS); else (P2C ? lean_ld1 : lean_ld0)(LARGS);
#undef LARGS
          got = __builtin_amdgcn_readfirstlane(got);
        }
        if (dm) nh += 2;
        XS(5);
      }
      unsigned bad = 0, pk = 0;
      if constexpr (LDR) { if (fast) fin_ld(pv_, rw, ldsb + L_SLOT + 528 + nq * 4, (unsigned)(b + 1)); }
      else (ROLE == R_F1 ? fin_st1 : (P2C ? fin_st2 : fin_st0))(pv_, rw, ldsb + L_SLOT + 528 + nq * 4, (unsigned)(b + 1), Cu + (size_t)b * c_blk, co0, co1, ldsb + L_SLOT + 524, bad, pk);
      XS(6);
      pre = pr2;
      bool now = (nh < 2 * b + 4 && wm < b + 1 + PD) || b + 1 >= nseg || b < nimm;
      if (!LDR && now) {
        if (NOWP == 0 && b >= nimm && pk == (unsigned)(b + 2)) now = false;
        else pubw(b + 1);
      }
      XS(7);
      XFL(b);
      pw = now ? 0 : b + 1;
      if (SPEC && !LDR && !fast && b >= 16 && na < nseg && na > b + 1) { arm(na); arm(na + 1); na += 2; }
      ++b;
    }
    if (SPEC && b < nseg) {
      static_assert(!SPEC || (NEARLY == 16 && TLP), "");
      const int ln = lane_id(), g = ln >> 4, r16 = ln & 15;
      const unsigned e0 = ldsb + (unsigned)((P2C ? 0 : kq * 32768) + (gl ? (g >> 1) * 512 + r16 * 32 + (g & 1) * 16 : r16 * 128 + ((g ^ (r16 & 7)) << 4)));
      const unsigned rw = ldsb + L_RED + nq * 4096 + ln * 16;
      const unsigned co0 = ROLE == R_F1 ? (unsigned)((g & 1) * 2048 + nq * 512 + r16 * 32 + (g >> 1) * 16) : (unsigned)(nq * (P2C ? 4096 : 2048) + r16 * 64 + g * 16);
      const char* pg = uni((const char*)myprog);
      unsigned fa = ldsb + L_SLOT + 528 + nq * 4; asm volatile("" : "+v"(fa));
      bool force = false, dmx = false;
      unsigned dur = DUR0, dp = 0, x8 = __builtin_amdgcn_readfirstlane(wave == 0 ? 8u : 0u);
      if (nh >= 2 * b + 2) got = 1;
      pre = false;
      for (;;) {
        unsigned rd;
        XS(0);
        if (LDR && b < nseg) {
          if (!got) { lgo(b, smp, smu, pre, force); issue_all(2 * b); issue_all(2 * b + 1); }
          XS(2);
          if (P2C) WAIT_V(6); else WAIT_V(12);
        }
        if (!LDR && b >= sbo && na <= nseg) { for (na = max(na, b); na < nseg; ++na) arm(na); WAIT_V(0); ++na; }
        const unsigned ea = e0 + (b & 1) * 65536;
        BAR();
        XS(3);
        if (b >= nseg) rd = __builtin_amdgcn_readfirstlane(wsl[8]);
        else if constexpr (LDR) {
          const int th = b + 1 < nseg ? b + 1 : 0;
#define LARGS BW, ea, ea ^ (gl ? 1024u : 64u), 0u, hlds(2 * b + 2), hsrc(2 * b + 2), hsrc(2 * b + 3), av, (unsigned)th, dep.voff, dep.base, \
              rw, ldsb + L_SLOT + 528 + nq * 4, (unsigned)(b + 1), ldsb + L_SLOT + 520, dep.mul, dep.add, th, th >= sbi ? th : 0x7FFFFFFF, smp, smu, got
          if (dmx) {
            *(volatile LAS u32x2*)(wsl + 2) = u32x2{(unsigned)(b + 2), (unsigned)(b + 2)};
            (P2C ? lean_dm1 : lean_dm0)(LARGS);
          } else (P2C ? lean_ld1 : lean_ld0)(LARGS);
#undef LARGS
          got = __builtin_amdgcn_readfirstlane(got) | (dmx ? 1u : 0u);
          rd = got >> 1;
          dmx = TDM && got == 1u && b + 2 < nseg
             && __all((int)(smp * dep.mul + dep.add) > b + 2 || (b + 2 >= sbi && (int)(smu >> 24) + ((int)((rt_now() + (dmx ? TDA : 0) - MARG - smu) << 8) >> 31) > b + 2));
          pre = true;
        } else {
          unsigned bad, pk;
          (ROLE == R_F1 ? lean_ep1 : (P2C ? lean_ep2 : lean_ep0))(BW, ea, ea ^ (gl ? 1024u : 64u), (unsigned)min(pw, 1) | (b >= sbo - 2 ? x8 : 0u), (unsigned)pw, ldsb + L_SLOT + 516, pg,
              (unsigned)(b + 1) << 24 | dur, tk, rd, rw, fa, (unsigned)(b + 1), Cu + (size_t)b * c_blk, co0, co0 + 1024u, bad, pk);
          XS(6);
          if (!rd) {
            pw = b + 1;
            { const unsigned d = min(tk, b0 ? (unsigned)DURH : 300u); dur = max(d, dp); dp = d; }
            if (bad && !force && b >= sbi) wsl[8] = 1u;
            else if ((ROLE == R_PART && !gl && b == sbo - 1) || pk != (unsigned)(b + 2)) {
              pubw(b + 1);
              pw = 0;
            }
            if (na < nseg && na > b + 1) { arm(na); arm(na + 1); na += 2; }
          }
        }
        XS(7);
        if (rd) {
          WAIT_V(0);
          BAR();
          if (wave == 4 && ln < 11) wsl[ln] = 0u;
          BAR();
          --b; pw = 0; pre = false; got = 0; force = true;
          continue;
        }
        if (b >= nseg) break;
        force = false;
        ++b;
      }
    }
    __builtin_amdgcn_s_setprio(0);
  };
  if (kq) body(std::true_type{}); else body(std::false_type{});
#undef PIECE
#undef POFF
  WAIT_V(0);
  BAR();
}
__device__ __forceinline__ void fin_role(char* smem, int wave_id_, int fidx, int nf0, int nf1, int nfs, const float* mod, const char* P2, const u16* x1, u16* out, unsigned* cnt, unsigned* tmo,
                                         unsigned long long* mts) {
  const int tid = opaque_tid(), lane = tid & 63, wave = WAVE_ID;
  row_table(smem + L_GB, mod, cnt, tmo, tid);
  volatile LAS unsigned* wsl = (volatile LAS unsigned*)(smem + L_SLOT + 512);
  int wf = 0, par = 0;
  const unsigned pf2 = (unsigned)(PF2 + ((lane & 31) < 28 ? (lane & 31) : 0)) * 128, pln = (unsigned)((PLN + (lane >> 3)) * 128 + (lane & 7) * 4);
  for (int nf = nf0; nf < nf1; nf += nfs) {
    if (wf <= nf) {
      if (wave == 0) {
        unsigned w;
        for (int spins = 0;; ++spins) {
          unsigned v, u;
          ld_fb(v, u, cnt, pf2 + (nf >= HC ? 4 : (nf >= HB ? 8 : 0)));
          if (!__all(v > (unsigned)nf) && bells(v > (unsigned)nf, u, nf, nf >= HB ? MARX : MARF, SC)) { w = 0; break; }
          v = rowmin16(v);
          w = min(rdl(v, 0), rdl(v, 16));
          if ((int)w > nf) break;
          if (nfs == 2) __builtin_amdgcn_s_sleep(FSL); else __builtin_amdgcn_s_sleep(FSH);
          if (give_up(spins, tmo, 9)) { w = NSEG; break; }
        }
        if (lane == 0) wsl[par] = w;
      }
      BAR();
      wf = (int)__builtin_amdgcn_readfirstlane(wsl[par]);
      par ^= 1;
    }
    int ln = lane; asm volatile("" : "+v"(ln));
    if (wave == 0) WTS(256 + 8 + fidx, nf / 2, 0);
    bool fin = nf < SB;
    for (int spins = 0; !fin_half(fin, P2, x1, smem + L_GB + 12288, out, nf * 32 + 4 * (fidx & 7) + (wave >> 1), wave & 1, ln); ++spins)
      if (spins > 1) fin = __all(ld4_sc1((const char*)cnt + pf2 + (nf >= HC ? 4 : (nf >= HB ? 8 : 0))) > (unsigned)nf && ld4_sc1((const char*)cnt + pln) > (unsigned)nf) || give_up(spins, tmo, 9);
    if (wave == 0) WTS(256 + 8 + fidx, nf / 2, 2);
  }
}
#define LD8(r, vo, sb) asm volatile("global_load_dwordx4 %0, %8, %12 sc1\n\tglobal_load_dwordx4 %1, %9, %12 sc1\n\t" \
    "global_load_dwordx4 %2, %10, %12 sc1\n\tglobal_load_dwordx4 %3, %11, %12 sc1\n\t" \
    "global_load_dwordx4 %4, %8, %13 sc1\n\tglobal_load_dwordx4 %5, %9, %13 sc1\n\t" \
    "global_load_dwordx4 %6, %10, %13 sc1\n\tglobal_load_dwordx4 %7, %11, %13 sc1" \
    : "=&v"(r[0][0]), "=&v"(r[0][1]), "=&v"(r[0][2]), "=&v"(r[0][3]), "=&v"(r[1][0]), "=&v"(r[1][1]), "=&v"(r[1][2]), "=&v"(r[1][3]) \
    : "v"(vo), "v"(vo ^ (TLQ ? 2048u : 64u)), "v"(vo ^ (TLQ ? 4096u : 128u)), "v"(vo ^ (TLQ ? 6144u : 192u)), "s"(sb), "s"((sb) + (TLQ ? 1024 : 4096)) : "memory")
#define TIE8(r) "+v"(r[0][0]), "+v"(r[0][1]), "+v"(r[0][2]), "+v"(r[0][3]), "+v"(r[1][0]), "+v"(r[1][1]), "+v"(r[1][2]), "+v"(r[1][3])
#define SADL 255
#define SAE 110
#define DSA 20
#define SASL 8
__device__ __forceinline__ void sa_role(char* smem, int wave_id_, int hbase, const char* __restrict__ Q, const char* __restrict__ Kx, const char* __restrict__ V,
                                        const char* kvm, char* __restrict__ O, unsigned* cnt, unsigned* tmo, int slot, unsigned long long* mts) {
  constexpr float LS = LOG2E * SCALE;
  const int tid = opaque_tid(), wave = WAVE_ID;
  const int h = hbase + (wave & 1), s0 = wave >> 1;
  char* vl = smem + wave * 17408;
  const unsigned ldsw = __builtin_amdgcn_readfirstlane((unsigned)(size_t)(LAS char*)vl);
  unsigned avail = 0;
  {
    static_assert(SB % 4 == 0, "");
    unsigned z = SENT; asm volatile("" : "+v"(z));
    for (int s = SB + s0; s < NSEG; s += 4)
#pragma unroll
      for (int i = 0; i < 8; ++i) st16s(uni(O + ((size_t)s * 64 + 2 * h) * 4096), (unsigned)(i * 1024 + (tid & 63) * 16), u32x4{z, z, z, z});
  }
  for (int s = s0, it = 1; s < NSEG; s += 4, ++it) {
    u32x4 qv[2][4], kv[2][4];
    int lane = tid & 63; asm volatile("" : "+v"(lane));
    const int g = lane >> 4, r16 = lane & 15, q4 = r16 >> 2, p4 = lane & 3;
    auto request = [&](int sr) {
      const size_t o = ((size_t)sr * NH + h) * 8192;
      const char* qs = uni(Q + o); const char* ks = uni(Kx + o); const char* vs = uni(V + o);
      const unsigned fo = TLQ ? (unsigned)(r16 * 64 + g * 16) : (unsigned)(r16 * 256 + ((g ^ r16) << 4));
      LD8(qv, fo, qs); LD8(kv, fo, ks);
      head_to_lds(vs, lane, ldsw);
    };
    {
      for (int spins = 0; avail <= (unsigned)s; ++spins) {
        const unsigned p = ld4_sc1(cnt + (PQKV + h) * 32 + (lane & 15));
        avail = min(min(qkv_avail(p, 0), qkv_avail(p, 1)), qkv_avail(p, 2));
        if (avail > (unsigned)s) break;
        __builtin_amdgcn_s_sleep(32);
        if (give_up(spins, tmo, 10)) break;
      }
      request(s);
      asm volatile("s_waitcnt vmcnt(0)" : TIE8(qv), TIE8(kv) :: "memory");
    }
    WTM(slot, s, 0);
    f32x4 s00 = f32x4{0, 0, 0, 0}, s01 = s00, s11 = s00;
#pragma unroll
    for (int ks = 0; ks < 4; ++ks) {
      s00 = MFMA32(asbf8(kv[0][ks]), asbf8(qv[0][ks]), s00);
      s01 = MFMA32(asbf8(kv[0][ks]), asbf8(qv[1][ks]), s01);
      s11 = MFMA32(asbf8(kv[1][ks]), asbf8(qv[1][ks]), s11);
    }
    float mx[2], sum[2];
    u32x4 pseg[2];
    {
#pragma unroll
      for (int r = 0; r < 4; ++r) { const bool ok = 4 * g + r <= r16; s00[r] = ok ? s00[r] : -3.0e38f; s11[r] = ok ? s11[r] : -3.0e38f; }
      mx[0] = red16_32_max(max3(max3(s00[0], s00[1], s00[2]), s00[3], -1.0e38f));
      mx[1] = red16_32_max(max3(max3(fmaxf(fmaxf(s01[0], s01[1]), fmaxf(s01[2], s01[3])), s11[0], -1.0e38f), max3(s11[1], s11[2], s11[3]), -1.0e38f));
      const float n0 = -mx[0] * LS, n1 = -mx[1] * LS;
#pragma unroll
      for (int r = 0; r < 4; ++r) {
        s00[r] = __builtin_amdgcn_exp2f(s00[r] * LS + n0); s01[r] = __builtin_amdgcn_exp2f(s01[r] * LS + n1); s11[r] = __builtin_amdgcn_exp2f(s11[r] * LS + n1);
      }
      sum[0] = (s00[0] + s00[1]) + (s00[2] + s00[3]);
      sum[1] = ((s01[0] + s01[1]) + (s01[2] + s01[3])) + ((s11[0] + s11[1]) + (s11[2] + s11[3]));
      pseg[0] = u32x4{pack2(s00[0], s00[1]), pack2(s00[2], s00[3]), 0u, 0u};
      pseg[1] = u32x4{pack2(s01[0], s01[1]), pack2(s01[2], s01[3]), pack2(s11[0], s11[1]), pack2(s11[2], s11[3])};
    }
    f32x4 o[8][2];
#pragma unroll
    for (int dt = 0; dt < 8; ++dt) {
      const int row0 = 4 * g + q4;
      const unsigned xo = (unsigned)(row0 * 256 + (((4 * (dt >> 1) + p4) ^ row0) << 4) + (dt & 1) * 8);
      const u32x2 lo = trread(vl, xo), hi = trread(vl, xo + 4096);
      o[dt][0] = MFMA32(asbf8(u32x4{lo.x, lo.y, 0u, 0u}), asbf8(pseg[0]), (f32x4{0, 0, 0, 0}));
      o[dt][1] = MFMA32(asbf8(u32x4{lo.x, lo.y, hi.x, hi.y}), asbf8(pseg[1]), (f32x4{0, 0, 0, 0}));
    }
    float n0[2]; f32x4 sm[2];
#pragma unroll
    for (int b = 0; b < 2; ++b) { const float t = red16_32_add(sum[b]); sm[b] = f32x4{t, t, t, t}; n0[b] = -mx[b] * LS; }
    unsigned oa = (unsigned)(r16 * 128 + ((g ^ (r16 & 7)) << 4)), ka = (unsigned)(g * 256 + (r16 & 7) * 16), va = (unsigned)lane * 16;
    unsigned one = g < 2 ? 0x3F803F80u : 0u;
    u32x4 kmv[4], vmv[4];
#pragma unroll
    for (int i = 0; i < 4; ++i) vmv[i] = u32x4{0u, 0u, 0u, 0u};
#pragma unroll
    for (int dt = 0; dt < 8; ++dt) asm volatile("" : "+v"(o[dt][0]), "+v"(o[dt][1]), "+v"(vmv[dt >> 1]));
    asm volatile("s_waitcnt lgkmcnt(0)" : "+v"(sm[0]), "+v"(sm[1]), "+v"(n0[0]), "+v"(n0[1]), "+v"(oa), "+v"(ka), "+v"(va), "+v"(one) :: "memory");
    unsigned pg;
    for (int spins = 0;; ++spins) {
      pg = ld4_sc1(cnt + (PROG + h) * 32 + ((lane & 4) ? 5 : (lane & 3)));
      if (__all((pg >> 24) + (s >= SDIR ? 1u : 0u) > (unsigned)s)) break;
      __builtin_amdgcn_s_sleep(SASL);
      if (give_up(spins, tmo, 6)) break;
    }
    if (__all((pg >> 24) == (unsigned)s)) wait_rt(latest(pg), s == NSEG - 1 ? SADL - LAST : SADL);
    const char* mk = uni(kvm + ((size_t)s * NH + h) * 4096);
    for (int spins = 0;; ++spins) {
      asm volatile("global_load_dwordx4 %0, %8, %10 sc1\n\tglobal_load_dwordx4 %1, %8, %10 offset:128 sc1\n\t"
                   "global_load_dwordx4 %2, %8, %10 offset:1024 sc1\n\tglobal_load_dwordx4 %3, %8, %10 offset:1152 sc1\n\ts_mov_b32 exec_hi, 0\n\t"
                   "global_load_dwordx4 %4, %9, %10 offset:2048 sc1\n\tglobal_load_dwordx4 %5, %9, %10 offset:2560 sc1\n\t"
                   "global_load_dwordx4 %6, %9, %10 offset:3072 sc1\n\tglobal_load_dwordx4 %7, %9, %10 offset:3584 sc1\n\ts_mov_b32 exec_hi, -1\n\ts_waitcnt vmcnt(0)"
                   : "=&v"(kmv[0]), "=&v"(kmv[1]), "=&v"(kmv[2]), "=&v"(kmv[3]), "+v"(vmv[0]), "+v"(vmv[1]), "+v"(vmv[2]), "+v"(vmv[3])
                   : "v"(ka), "v"(va), "s"(mk) : "memory");
      unsigned mx_ = 0;
#pragma unroll
      for (int i = 0; i < 4; ++i) mx_ = max(max(max(max(mx_, kmv[i].x), kmv[i].y), max(kmv[i].z, kmv[i].w)), max(max(vmv[i].x, vmv[i].y), max(vmv[i].z, vmv[i].w)));
      if (__all(mx_ != SENT)) break;
      __builtin_amdgcn_s_sleep(2);
      if (give_up(spins, tmo, 6)) break;
    }
    if (SAE && s >= SB && lane == 0) st4_sc1(cnt + (PSA + (hbase >> 1)) * 32 + 16 + wave, bell(s, rt_now() + SAE));
    WTM(slot, s, 1);
    f32x4 tt[2] = {f32x4{0, 0, 0, 0}, f32x4{0, 0, 0, 0}};
#pragma unroll
    for (int ks = 0; ks < 4; ++ks) { tt[0] = MFMA32(asbf8(kmv[ks]), asbf8(qv[0][ks]), tt[0]); tt[1] = MFMA32(asbf8(kmv[ks]), asbf8(qv[1][ks]), tt[1]); }
#pragma unroll
    for (int b = 0; b < 2; ++b)
#pragma unroll
      for (int r = 0; r < 4; ++r) tt[b][r] = tt[b][r] * LS + n0[b];
    if (!__all(max3(max3(tt[0][0], tt[0][1], tt[0][2]), max3(tt[0][3], tt[1][0], tt[1][1]), max2(tt[1][2], tt[1][3])) <= 40.0f)) {
#pragma unroll
      for (int b = 0; b < 2; ++b) {
        float m = max3(max3(tt[b][0], tt[b][1], tt[b][2]), tt[b][3], 0.0f);
        { float x, y; sw16(m, x, y); m = max2(x, y); }
        const float f = __builtin_amdgcn_exp2f(-m);
        tt[b] -= m; sm[b] *= f;
#pragma unroll
        for (int dt = 0; dt < 8; ++dt) o[dt][b] *= f;
      }
    }
    u32x2 pm[2]; float inv[2];
#pragma unroll
    for (int b = 0; b < 2; ++b) {
      pm[b] = u32x2{pack2(__builtin_amdgcn_exp2f(tt[b][0]), __builtin_amdgcn_exp2f(tt[b][1])), pack2(__builtin_amdgcn_exp2f(tt[b][2]), __builtin_amdgcn_exp2f(tt[b][3]))};
      sm[b] = __builtin_amdgcn_mfma_f32_16x16x16bf16_1k(ass4(u32x2{one, one}), ass4(pm[b]), sm[b], 0, 0, 0);
    }
#pragma unroll
    for (int dt = 0; dt < 8; ++dt)
#pragma unroll
      for (int b = 0; b < 2; ++b) o[dt][b] = __builtin_amdgcn_mfma_f32_16x16x16bf16_1k(ass4((dt & 1) ? u32x2{vmv[dt >> 1].z, vmv[dt >> 1].w} : u32x2{vmv[dt >> 1].x, vmv[dt >> 1].y}), ass4(pm[b]), o[dt][b], 0, 0, 0);
    inv[0] = __builtin_amdgcn_rcpf(sm[0][0]); inv[1] = __builtin_amdgcn_rcpf(sm[1][0]);
    const char* od = uni(O + ((size_t)s * 64 + 2 * h) * 4096);
#pragma unroll
    for (int j = 0; j < 4; ++j) {
#pragma unroll
      for (int b = 0; b < 2; ++b) {
        const f32x4 r0 = o[2 * j][b] * inv[b], r1 = o[2 * j + 1][b] * inv[b];
        st16s(od + (j >> 1) * 4096 + b * 2048, oa ^ ((j & 1) * 64u), u32x4{pack2(r0[0], r0[1]), pack2(r0[2], r0[3]), pack2(r1[0], r1[1]), pack2(r1[2], r1[3])});
      }
    }
    if (!SAE && s >= SB && lane == 0) st4_sc1(cnt + (PSA + (hbase >> 1)) * 32 + 16 + wave, bell(s, rt_now() + DSA));
    WTM(slot, s, 3);
    WAIT_V(0);
    if (lane == 0) st4_sc1(cnt + (PSA + (hbase >> 1)) * 32 + wave, (unsigned)it);
    WTM(slot, s, 2);
  }
}
#undef LD8
#undef TIE8

constexpr int Q_MEM = 0;
constexpr int Q_K   = 16384;
constexpr int Q_V   = 24576;
constexpr int Q_QK  = 32768;
constexpr int Q_TM  = 36864;
constexpr int Q_CB  = 40960;
constexpr int Q_RING = 32768;
constexpr int V_P1  = 0;
constexpr int V_P2  = 16384;
constexpr int NCHAIN = 96;
#define EKV 1
#define VHN 2
#define ROTH 0
#define NQQ 6
#define NQK 4
#define E_AG 9
#define RDYI -2
#define ROTW 2
#define DQU 6
#define DQM 18
#define DQD 1
#define ARSEL (xg >> 1)
#define ORSEL (xg >> 1)
#define D_Q 105
#define D_O 4
#define E_B 6
#define E_O 1900
#define KDEL 5
#define VPOS 0

typedef __bf16 bf16x16 __attribute__((ext_vector_type(16)));
typedef unsigned u32x8 __attribute__((ext_vector_type(8)));
__device__ __forceinline__ f32x4 SMF(u32x4 a, u32x8 b, f32x4 c, int idx) {
  return __builtin_amdgcn_smfmac_f32_16x16x64_bf16(__builtin_bit_cast(bf16x8, a), __builtin_bit_cast(bf16x16, b), c, idx, 0, 0);
}
__device__ __forceinline__ float fold32(float a, float b) {
  const auto p = __builtin_amdgcn_permlane32_swap(__float_as_uint(a), __float_as_uint(b), false, false);
  return __uint_as_float(p[0]) + __uint_as_float(p[1]);
}
__device__ __forceinline__ f32x2 rowsum(f32x4 x) { return f32x2{fold32(x[0], x[2]), fold32(x[1], x[3])}; }
__device__ __forceinline__ unsigned nosent1(unsigned v) { return v < 0xFFFFFFFEu ? v : 0xFFFFFFFEu; }
__device__ __forceinline__ void st4s(const char* sb, unsigned vo, unsigned v) { asm volatile("global_store_dword %0, %1, %2 sc1\n\ts_nop 0" :: "v"(vo), "v"(v), "s"(sb) : "memory"); }
__device__ __forceinline__ void st8s(const char* sb, unsigned vo, u32x2 v) { asm volatile("global_store_dwordx2 %0, %1, %2 sc1\n\ts_nop 1" :: "v"(vo), "v"(v), "s"(sb) : "memory"); }
__device__ __forceinline__ void gldsw(const char* sbase, unsigned voff, unsigned lds) {
  unsigned keep;
  asm volatile("s_mov_b32 %0, m0\n\ts_mov_b32 m0, %3\n\ts_nop 2\n\tglobal_load_lds_dwordx4 %1, %2" CNT_ "\n\ts_mov_b32 m0, %0"
               : "=&s"(keep) : "v"(voff), "s"(sbase), "s"(lds) : "memory");
}
__device__ __forceinline__ void wait_clk(unsigned t0, unsigned d) {
  while ((unsigned)__builtin_amdgcn_s_memtime() - t0 < d) __builtin_amdgcn_s_sleep(1);
}
__device__ __forceinline__ unsigned bell_time(const char* a, unsigned vo, unsigned tag, unsigned cap) {
  unsigned v;
  for (unsigned spins = 0;; ++spins) {
    asm volatile("global_load_dword %0, %1, %2 sc1\n\ts_waitcnt vmcnt(0)" : "=&v"(v) : "v"(vo), "s"(a) : "memory");
    if (__all((v >> 24) >= tag) || spins >= cap) break;
  }
  const unsigned now = (unsigned)__builtin_amdgcn_s_memrealtime() + 250u;
  const unsigned age = rowmin16((now - v) & 0xFFFFFFu);
  return now - min(min(rdl(age, 0), rdl(age, 16)), 500u);
}
#define WBAR() do { __builtin_amdgcn_s_barrier(); asm volatile("" ::: "memory"); } while (0)
#define TN(k, v) do { } while (0)
__device__ __forceinline__ void stage_proj(const char* src, unsigned lds, int tid, int wave) {
  const int row = tid >> 4, f = 2 * ((row & 1) | (((row >> 2) & 1) << 1));
#pragma unroll
  for (int j = 0; j < 4; ++j)
    gldsw(src + (size_t)j * 32 * HD * 2, (unsigned)(row * HD * 2 + (((tid & 15) ^ f ^ ((j & 1) << 3)) << 4)), lds + (wave * 64 + 512 * j) * 16);
}
__device__ __forceinline__ unsigned proj_base(int T, int lane) {
  const int g = lane >> 4, q4 = (lane & 15) >> 2, p4 = lane & 3, fs = 2 * ((q4 & 1) | ((g & 1) << 1) | ((q4 >> 1) << 2));
  return (unsigned)((4 * g + (q4 & 1) + 32 * (q4 >> 1)) * 256 + (((2 * T) ^ fs) << 4) + p4 * 8);
}
__device__ __forceinline__ u32x8 proj_frag(const char* smem, unsigned a) {
  const u32x2 t0 = trread(smem, a), t1 = trread(smem, a + 512), t2 = trread(smem, a + 4096), t3 = trread(smem, a + 4608);
  return u32x8{t0.x, t0.y, t1.x, t1.y, t2.x, t2.y, t3.x, t3.y};
}
__device__ __forceinline__ void flag_frags(unsigned fa, unsigned a, unsigned want, bf16x8 (&f)[4]) {
  u32x2 t[8];
  for (;;) {
    u32x4 fl;
    asm volatile("ds_read_b128 %0, %9\n\t"
                 "ds_read_b64_tr_b16 %1, %10\n\tds_read_b64_tr_b16 %2, %10 offset:64\n\t"
                 "ds_read_b64_tr_b16 %3, %10 offset:512\n\tds_read_b64_tr_b16 %4, %10 offset:576\n\t"
                 "ds_read_b64_tr_b16 %5, %10 offset:1024\n\tds_read_b64_tr_b16 %6, %10 offset:1088\n\t"
                 "ds_read_b64_tr_b16 %7, %10 offset:1536\n\tds_read_b64_tr_b16 %8, %10 offset:1600\n\ts_waitcnt lgkmcnt(0)"
                 : "=&v"(fl), "=&v"(t[0]), "=&v"(t[1]), "=&v"(t[2]), "=&v"(t[3]), "=&v"(t[4]), "=&v"(t[5]), "=&v"(t[6]), "=&v"(t[7])
                 : "v"(fa), "v"(a) : "memory");
    if (__builtin_amdgcn_readfirstlane(min(min(fl.x, fl.y), min(fl.z, fl.w))) >= want) break;
  }
#pragma unroll
  for (int ks = 0; ks < 4; ++ks) f[ks] = asbf8(u32x4{t[2 * ks].x, t[2 * ks].y, t[2 * ks + 1].x, t[2 * ks + 1].y});
}
__device__ __forceinline__ void wait_flags(const char* p, unsigned want) {
  for (;;) {
    const u32x4 fl = *(volatile LAS u32x4*)p;
    if (__builtin_amdgcn_readfirstlane(min(min(fl.x, fl.y), min(fl.z, fl.w))) >= want) break;
  }
}
__device__ __forceinline__ f32x4 zero4() { float z = 0.0f; asm volatile("" : "+v"(z)); return f32x4{z, z, z, z}; }
__device__ __forceinline__ unsigned lds_word(const char* p) { return __builtin_amdgcn_readfirstlane(*(volatile LAS unsigned*)p); }

#define MFMA16(a, b, c) __builtin_amdgcn_mfma_f32_16x16x16bf16_1k((a), (b), (c), 0, 0, 0)
#define UF(x) __uint_as_float(x)
__device__ __forceinline__ unsigned umax3(unsigned a, unsigned b, unsigned c) { return max(max(a, b), c); }
__device__ __forceinline__ f32x2 logit2(float a, float b) {
  return f32x2{a, b};
}
constexpr float LSC = LOG2E * SCALE;

struct QKW { u32x32 w0, w1, w2, w3, w4, w5; u32x8 x; };
template <int W_> __device__ __forceinline__ void qk_step(const QKW& W, const char* ag, const char* vs, const char* od, const char* kv, unsigned& cap, unsigned& sp,
                                                          unsigned long long& tc, unsigned long long* ts, unsigned& d0, unsigned& d1);
template <> __device__ __forceinline__ void qk_step<0>(const QKW& W, const char* ag, const char* vs, const char* od, const char* kv, unsigned& cap, unsigned& sp,
                                                        unsigned long long& tc, unsigned long long* ts, unsigned& d0, unsigned& d1) {
  unsigned st, bg;
  asm volatile("v_mbcnt_lo_u32_b32 v60, -1, 0\n\t"
    "v_mbcnt_hi_u32_b32 v60, -1, v60\n\t"
    "v_lshlrev_b32 v6, 4, v60\n\t"
    "s_mov_b32 %[sp], 0\n\t"
    "s_setprio 3\n\t"
    "global_load_dwordx4 v[40:43], v6, %[ag] sc1\n\t"
    "global_load_dwordx4 v[44:47], v6, %[ag] offset:1024 sc1\n\t"
    "s_memtime %[tc]\n\t"
    "v_add_u32 v7, 40960, v6\n\t"
    "v_add_u32 v8, 98304, v6\n\t"
    "ds_read_b128 v[24:27], v7 offset:0\n\t"
    "ds_read_b128 v[28:31], v7 offset:1024\n\t"
    "ds_read_b128 v[32:35], v7 offset:8192\n\t"
    "ds_read_b128 v[36:39], v7 offset:9216\n\t"
    "v_lshrrev_b32 v48, 4, v60\n\t"
    "v_and_b32 v49, 15, v60\n\t"
    "v_and_b32 v61, 4, v60\n\t"
    "v_mul_u32_u24 v61, 0x2aaa, v61\n\t"
    "v_lshrrev_b32 v9, 1, v60\n\t"
    "v_and_b32 v9, 2, v9\n\t"
    "v_or3_b32 v9, v9, v61, %[c44]\n\t"
    "v_add_u32 v14, 0, v6\n\t"
    "v_lshlrev_b32 v61, 2, v48\n\t"
    "v_lshl_or_b32 v10, v49, 4, v61\n\t"
    "v_lshrrev_b32 v50, 2, v49\n\t"
    "v_and_b32 v51, 3, v60\n\t"
    "v_and_b32 v61, 1, v60\n\t"
    "v_lshlrev_b32 v61, 3, v61\n\t"
    "v_lshl_or_b32 v11, v50, 4, v61\n\t"
    "v_lshl_or_b32 v11, v48, 7, v11\n\t"
    "v_xor_b32 v12, v48, v49\n\t"
    "v_lshlrev_b32 v12, 4, v12\n\t"
    "v_lshl_or_b32 v12, v49, 8, v12\n\t"
    "v_lshl_or_b32 v50, v48, 2, v50\n\t"
    "v_or_b32 v51, 0, v51\n\t"
    "v_xor_b32 v51, v51, v50\n\t"
    "v_lshlrev_b32 v13, 4, v51\n\t"
    "v_lshl_or_b32 v13, v50, 8, v13\n\t"
    "v_and_b32 v61, 3, v60\n\t"
    "v_lshlrev_b32 v61, 4, v61\n\t"
    "v_lshl_or_b32 v15, v48, 6, v61\n\t"
    "v_and_b32 v61, 8, v60\n\t"
    "v_lshl_or_b32 v15, v61, 14, v15\n\t"
    "v_and_b32 v61, 4, v60\n\t"
    "v_lshl_or_b32 v15, v61, 8, v15\n\t"
    "v_add_u32 v15, 0, v15\n\t"
    "3:\n\t"
    "v_mov_b32 v16, 0\n\t"
    "v_mov_b32 v17, 0\n\t"
    "v_mov_b32 v18, 0\n\t"
    "v_mov_b32 v19, 0\n\t"
    "v_mov_b32 v20, 0\n\t"
    "v_mov_b32 v21, 0\n\t"
    "v_mov_b32 v22, 0\n\t"
    "v_mov_b32 v23, 0\n\t"
    "s_waitcnt vmcnt(0) lgkmcnt(0)\n\t"
    "14:\n\t"
    "ds_write_b128 v6, v[40:43] offset:0\n\t"
    "ds_write_b128 v6, v[44:47] offset:1024\n\t"
    "v_smfmac_f32_16x16x64_bf16 v[16:19], v[40:43], v[64:71], v9\n\t"
    "v_max3_u32 v60, v40, v41, v42\n\t"
    "v_max3_u32 v61, v43, v44, v45\n\t"
    "v_smfmac_f32_16x16x64_bf16 v[20:23], v[40:43], v[72:79], v9\n\t"
    "v_max3_u32 v60, v60, v46, v47\n\t"
    "v_max_u32 v60, v60, v61\n\t"
    "v_cmp_eq_u32 vcc, -1, v60\n\t"
    "s_cbranch_vccnz 1f\n\t"
    "2:\n\t"
    "s_waitcnt lgkmcnt(0)\n\t"
    "s_cmp_eq_u64 %[kv], 0\n\t"
    "s_barrier\n\t"
    "s_cbranch_scc1 20f\n\t"
    "ds_read_b128 v[40:43], v6 offset:12288\n\t"
    "ds_read_b128 v[48:51], v6 offset:2048\n\t"
    "v_smfmac_f32_16x16x64_bf16 v[16:19], v[44:47], v[80:87], v9\n\t"
    "ds_read_b128 v[52:55], v6 offset:3072\n\t"
    "ds_read_b128 v[56:59], v6 offset:4096\n\t"
    "v_smfmac_f32_16x16x64_bf16 v[20:23], v[44:47], v[88:95], v9\n\t"
    "ds_read_b128 v[60:63], v6 offset:13312\n\t"
    "ds_read_b128 v[44:47], v6 offset:5120\n\t"
    "s_waitcnt lgkmcnt(5)\n\t"
    "v_smfmac_f32_16x16x64_bf16 v[16:19], v[40:43], v[24:31], v9\n\t"
    "ds_read_b128 v[24:27], v7 offset:16384\n\t"
    "ds_read_b128 v[28:31], v7 offset:17408\n\t"
    "v_smfmac_f32_16x16x64_bf16 v[20:23], v[40:43], v[32:39], v9\n\t"
    "ds_read_b128 v[32:35], v7 offset:24576\n\t"
    "ds_read_b128 v[36:39], v7 offset:25600\n\t"
    "ds_read_b128 v[40:43], v6 offset:6144\n\t"
    "s_waitcnt lgkmcnt(9)\n\t"
    "v_smfmac_f32_16x16x64_bf16 v[16:19], v[48:51], v[96:103], v9\n\t"
    "v_smfmac_f32_16x16x64_bf16 v[20:23], v[48:51], v[104:111], v9\n\t"
    "ds_read_b128 v[48:51], v6 offset:7168\n\t"
    "s_waitcnt lgkmcnt(9)\n\t"
    "v_smfmac_f32_16x16x64_bf16 v[16:19], v[52:55], v[112:119], v9\n\t"
    "v_smfmac_f32_16x16x64_bf16 v[20:23], v[52:55], v[120:127], v9\n\t"
    "ds_read_b128 v[52:55], v6 offset:14336\n\t"
    "s_waitcnt lgkmcnt(9)\n\t"
    "v_smfmac_f32_16x16x64_bf16 v[16:19], v[56:59], v[128:135], v9\n\t"
    "v_smfmac_f32_16x16x64_bf16 v[20:23], v[56:59], v[136:143], v9\n\t"
    "ds_read_b128 v[56:59], v6 offset:8192\n\t"
    "s_waitcnt lgkmcnt(6)\n\t"
    "v_smfmac_f32_16x16x64_bf16 v[16:19], v[60:63], v[24:31], v9\n\t"
    "ds_read_b128 v[24:27], v8 offset:0\n\t"
    "ds_read_b128 v[28:31], v8 offset:1024\n\t"
    "s_waitcnt lgkmcnt(6)\n\t"
    "v_smfmac_f32_16x16x64_bf16 v[20:23], v[60:63], v[32:39], v9\n\t"
    "ds_read_b128 v[32:35], v8 offset:8192\n\t"
    "ds_read_b128 v[36:39], v8 offset:9216\n\t"
    "ds_read_b128 v[60:63], v6 offset:9216\n\t"
    "v_smfmac_f32_16x16x64_bf16 v[16:19], v[44:47], v[144:151], v9\n\t"
    "v_smfmac_f32_16x16x64_bf16 v[20:23], v[44:47], v[152:159], v9\n\t"
    "ds_read_b128 v[44:47], v6 offset:10240\n\t"
    "s_waitcnt lgkmcnt(9)\n\t"
    "v_smfmac_f32_16x16x64_bf16 v[16:19], v[40:43], v[160:167], v9\n\t"
    "v_smfmac_f32_16x16x64_bf16 v[20:23], v[40:43], v[168:175], v9\n\t"
    "ds_read_b128 v[40:43], v6 offset:15360\n\t"
    "s_waitcnt lgkmcnt(9)\n\t"
    "v_smfmac_f32_16x16x64_bf16 v[16:19], v[48:51], v[176:183], v9\n\t"
    "v_smfmac_f32_16x16x64_bf16 v[20:23], v[48:51], v[184:191], v9\n\t"
    "ds_read_b128 v[48:51], v6 offset:11264\n\t"
    "s_waitcnt lgkmcnt(6)\n\t"
    "v_smfmac_f32_16x16x64_bf16 v[16:19], v[52:55], v[24:31], v9\n\t"
    "ds_read_b128 v[24:27], v8 offset:16384\n\t"
    "ds_read_b128 v[28:31], v8 offset:17408\n\t"
    "s_waitcnt lgkmcnt(6)\n\t"
    "v_smfmac_f32_16x16x64_bf16 v[20:23], v[52:55], v[32:39], v9\n\t"
    "ds_read_b128 v[32:35], v8 offset:24576\n\t"
    "ds_read_b128 v[36:39], v8 offset:25600\n\t"
    "v_smfmac_f32_16x16x64_bf16 v[16:19], v[56:59], v[192:199], v9\n\t"
    "v_smfmac_f32_16x16x64_bf16 v[20:23], v[56:59], v[200:207], v9\n\t"
    "s_waitcnt lgkmcnt(7)\n\t"
    "v_smfmac_f32_16x16x64_bf16 v[16:19], v[60:63], v[208:215], v9\n\t"
    "v_smfmac_f32_16x16x64_bf16 v[20:23], v[60:63], v[216:223], v9\n\t"
    "s_waitcnt lgkmcnt(6)\n\t"
    "v_smfmac_f32_16x16x64_bf16 v[16:19], v[44:47], v[224:231], v9\n\t"
    "v_smfmac_f32_16x16x64_bf16 v[20:23], v[44:47], v[232:239], v9\n\t"
    "s_waitcnt lgkmcnt(2)\n\t"
    "v_smfmac_f32_16x16x64_bf16 v[16:19], v[40:43], v[24:31], v9\n\t"
    "s_waitcnt lgkmcnt(0)\n\t"
    "v_smfmac_f32_16x16x64_bf16 v[20:23], v[40:43], v[32:39], v9\n\t"
    "v_smfmac_f32_16x16x64_bf16 v[16:19], v[48:51], v[240:247], v9\n\t"
    "v_smfmac_f32_16x16x64_bf16 v[20:23], v[48:51], v[248:255], v9\n\t"
    "v_xor_b32 v7, 64, v12\n\t"
    "v_xor_b32 v8, 0x80, v12\n\t"
    "v_xor_b32 v6, 0xc0, v12\n\t"
    "ds_read_b128 v[24:27], v12 offset:16384\n\t"
    "ds_read_b128 v[28:31], v12 offset:20480\n\t"
    "ds_read_b128 v[32:35], v7 offset:16384\n\t"
    "ds_read_b128 v[36:39], v7 offset:20480\n\t"
    "v_permlane16_swap_b32 v16, v18\n\t"
    "v_permlane16_swap_b32 v17, v19\n\t"
    "v_permlane16_swap_b32 v20, v22\n\t"
    "v_permlane16_swap_b32 v21, v23\n\t"
    "v_add_f32 v16, v16, v18\n\t"
    "v_add_f32 v17, v17, v19\n\t"
    "v_add_f32 v20, v20, v22\n\t"
    "v_mul_f32 v16, %[lsc], v16\n\t"
    "v_mul_f32 v17, %[lsc], v17\n\t"
    "v_cvt_pk_bf16_f32 v16, v16, v17\n\t"
    "v_add_f32 v21, v21, v23\n\t"
    "ds_write_b32 v10, v16 offset:32768\n\t"
    "v_mul_f32 v20, %[lsc], v20\n\t"
    "v_mul_f32 v21, %[lsc], v21\n\t"
    "v_cvt_pk_bf16_f32 v20, v20, v21\n\t"
    "ds_write_b32 v10, v20 offset:33024\n\t"
    "ds_read_b128 v[40:43], v8 offset:16384\n\t"
    "ds_read_b128 v[44:47], v8 offset:20480\n\t"
    "ds_read_b128 v[48:51], v6 offset:16384\n\t"
    "ds_read_b128 v[52:55], v6 offset:20480\n\t"
    "s_waitcnt lgkmcnt(4)\n\t"
    "s_barrier\n\t"
    "ds_read_b64_tr_b16 v[16:17], v11 offset:32768\n\t"
    "ds_read_b64_tr_b16 v[18:19], v11 offset:32832\n\t"
    "ds_read_b64_tr_b16 v[20:21], v11 offset:33280\n\t"
    "ds_read_b64_tr_b16 v[22:23], v11 offset:33344\n\t"
    "ds_read_b64_tr_b16 v[56:57], v11 offset:33792\n\t"
    "ds_read_b64_tr_b16 v[58:59], v11 offset:33856\n\t"
    "ds_read_b64_tr_b16 v[60:61], v11 offset:34304\n\t"
    "ds_read_b64_tr_b16 v[62:63], v11 offset:34368\n\t"
    "v_mov_b32 v6, 0\n\t"
    "v_mov_b32 v7, 0\n\t"
    "v_mov_b32 v8, 0\n\t"
    "v_mov_b32 v9, 0\n\t"
    "s_mov_b32 exec_hi, 0\n\t"
    "global_load_dwordx4 v[6:9], v14, %[vs] sc1\n\t"
    "s_mov_b32 exec_hi, -1\n\t"
    "s_waitcnt lgkmcnt(6)\n\t"
    "v_mfma_f32_16x16x32_bf16 v[24:27], v[24:27], v[16:19], 0\n\t"
    "v_mfma_f32_16x16x32_bf16 v[28:31], v[28:31], v[16:19], 0\n\t"
    "s_waitcnt lgkmcnt(4)\n\t"
    "v_mfma_f32_16x16x32_bf16 v[24:27], v[32:35], v[20:23], v[24:27]\n\t"
    "v_mfma_f32_16x16x32_bf16 v[28:31], v[36:39], v[20:23], v[28:31]\n\t"
    "ds_read_b64_tr_b16 v[32:33], v13 offset:24576\n\t"
    "ds_read_b64_tr_b16 v[34:35], v13 offset:28672\n\t"
    "ds_read_b64_tr_b16 v[36:37], v13 offset:24584\n\t"
    "ds_read_b64_tr_b16 v[38:39], v13 offset:28680\n\t"
    "s_waitcnt lgkmcnt(6)\n\t"
    "v_mfma_f32_16x16x32_bf16 v[24:27], v[40:43], v[56:59], v[24:27]\n\t"
    "v_mfma_f32_16x16x32_bf16 v[28:31], v[44:47], v[56:59], v[28:31]\n\t"
    "s_waitcnt lgkmcnt(4)\n\t"
    "v_mfma_f32_16x16x32_bf16 v[24:27], v[48:51], v[60:63], v[24:27]\n\t"
    "v_mfma_f32_16x16x32_bf16 v[28:31], v[52:55], v[60:63], v[28:31]\n\t"
    "s_setprio 1\n\t"
    "v_mov_b32 v52, %[one]\n\t"
    "v_mov_b32 v53, %[one]\n\t"
    "v_mov_b32 v54, %[one]\n\t"
    "v_mov_b32 v55, %[one]\n\t"
    "s_nop 2\n\t"
    "v_max3_f32 v10, |v24|, |v25|, |v26|\n\t"
    "v_max3_f32 v40, |v27|, |v28|, |v29|\n\t"
    "v_max3_f32 v10, v10, |v30|, |v31|\n\t"
    "v_max_f32 v10, v10, v40\n\t"
    "v_cmp_nge_f32 vcc, %[big], v10\n\t"
    "s_mov_b32 %[bg], %[big]\n\t"
    "v_mov_b32 v10, 0\n\t"
    "s_cbranch_vccnz 12f\n\t"
    "13:\n\t"
    "v_exp_f32 v24, v24\n\t"
    "v_exp_f32 v25, v25\n\t"
    "v_exp_f32 v26, v26\n\t"
    "v_exp_f32 v27, v27\n\t"
    "v_exp_f32 v28, v28\n\t"
    "v_exp_f32 v29, v29\n\t"
    "v_exp_f32 v30, v30\n\t"
    "v_exp_f32 v31, v31\n\t"
    "v_cvt_pk_bf16_f32 v40, v24, v25\n\t"
    "v_cvt_pk_bf16_f32 v41, v26, v27\n\t"
    "v_cvt_pk_bf16_f32 v42, v28, v29\n\t"
    "v_cvt_pk_bf16_f32 v43, v30, v31\n\t"
    "s_setprio 3\n\t"
    "s_waitcnt lgkmcnt(0)\n\t"
    "s_nop 0\n\t"
    "v_mfma_f32_16x16x32_bf16 v[44:47], v[32:35], v[40:43], 0\n\t"
    "v_mfma_f32_16x16x32_bf16 v[48:51], v[36:39], v[40:43], 0\n\t"
    "v_mfma_f32_16x16x32_bf16 v[52:55], v[52:55], v[40:43], 0\n\t"
    "v_mov_b32 v12, 0\n\t"
    "v_mov_b32 v13, 0\n\t"
    "s_mov_b32 exec_hi, 0\n\t"
    "v_mov_b32 v12, %[one]\n\t"
    "v_mov_b32 v13, %[one]\n\t"
    "s_mov_b32 exec_hi, -1\n\t"
    "s_barrier\n\t"
    "ds_read_b64_tr_b16 v[24:25], v11 offset:34816\n\t"
    "ds_read_b64_tr_b16 v[26:27], v11 offset:34880\n\t"
    "ds_read_b64_tr_b16 v[28:29], v11 offset:35328\n\t"
    "ds_read_b64_tr_b16 v[30:31], v11 offset:35392\n\t"
    "ds_read_b64_tr_b16 v[32:33], v11 offset:35840\n\t"
    "ds_read_b64_tr_b16 v[34:35], v11 offset:35904\n\t"
    "ds_read_b64_tr_b16 v[36:37], v11 offset:36352\n\t"
    "ds_read_b64_tr_b16 v[38:39], v11 offset:36416\n\t"
    "s_waitcnt lgkmcnt(6)\n\t"
    "v_mfma_f32_16x16x32_bf16 v[40:43], v[24:27], v[16:19], 0\n\t"
    "s_waitcnt lgkmcnt(4)\n\t"
    "v_mfma_f32_16x16x32_bf16 v[40:43], v[28:31], v[20:23], v[40:43]\n\t"
    "s_waitcnt lgkmcnt(2)\n\t"
    "v_mfma_f32_16x16x32_bf16 v[40:43], v[32:35], v[56:59], v[40:43]\n\t"
    "s_waitcnt lgkmcnt(0)\n\t"
    "v_mfma_f32_16x16x32_bf16 v[40:43], v[36:39], v[60:63], v[40:43]\n\t"
    "5:\n\t"
    "s_waitcnt vmcnt(0)\n\t"
    "v_max3_u32 v11, v6, v7, v8\n\t"
    "v_max_u32 v11, v11, v9\n\t"
    "v_cmp_eq_u32 vcc, -1, v11\n\t"
    "s_cbranch_vccnz 4f\n\t"
    "6:\n\t"
    "s_nop 2\n\t"
    "v_exp_f32 v28, v40\n\t"
    "v_max3_f32 v11, v40, v41, v42\n\t"
    "v_exp_f32 v29, v41\n\t"
    "v_max_f32 v11, v11, v43\n\t"
    "v_exp_f32 v30, v42\n\t"
    "v_cmp_nge_f32 vcc, %[bg], v11\n\t"
    "v_exp_f32 v31, v43\n\t"
    "s_cbranch_vccnz 10f\n\t"
    "11:\n\t"
    "v_cvt_pk_bf16_f32 v24, v28, v29\n\t"
    "v_cvt_pk_bf16_f32 v25, v30, v31\n\t"
    "s_nop 1\n\t"
    "v_mfma_f32_16x16x16_bf16 v[52:55], v[12:13], v[24:25], v[52:55]\n\t"
    "v_mfma_f32_16x16x16_bf16 v[44:47], v[6:7], v[24:25], v[44:47]\n\t"
    "v_mfma_f32_16x16x16_bf16 v[48:51], v[8:9], v[24:25], v[48:51]\n\t"
    "s_nop 5\n\t"
    "v_rcp_f32 v26, v52\n\t"
    "s_nop 0\n\t"
    "v_pk_mul_f32 v[44:45], v[44:45], v[26:27] op_sel_hi:[1,0]\n\t"
    "v_pk_mul_f32 v[46:47], v[46:47], v[26:27] op_sel_hi:[1,0]\n\t"
    "v_pk_mul_f32 v[48:49], v[48:49], v[26:27] op_sel_hi:[1,0]\n\t"
    "v_pk_mul_f32 v[50:51], v[50:51], v[26:27] op_sel_hi:[1,0]\n\t"
    "v_cvt_pk_bf16_f32 v36, v44, v45\n\t"
    "v_cvt_pk_bf16_f32 v37, v46, v47\n\t"
    "v_cvt_pk_bf16_f32 v38, v48, v49\n\t"
    "v_cvt_pk_bf16_f32 v39, v50, v51\n\t"
    "v_min_u32 v36, -2, v36\n\t"
    "v_min_u32 v37, -2, v37\n\t"
    "v_min_u32 v38, -2, v38\n\t"
    "v_min_u32 v39, -2, v39\n\t"
    "global_store_dwordx4 v15, v[36:39], %[od] sc1\n\t"
    "s_barrier\n\t"
    "s_mov_b32 exec_hi, 0\n\t"
    "global_store_dwordx4 v14, v[6:9], %[kv] sc1\n\t"
    "s_mov_b32 exec_hi, -1\n\t"
    "s_nop 0\n\t"
    "s_waitcnt lgkmcnt(0)\n\t"
    "s_branch 9f\n\t"
    "1:\n\t"
    "s_add_u32 %[sp], %[sp], 1\n\t"
    "s_cmp_ge_u32 %[sp], %[cap]\n\t"
    "s_cbranch_scc1 7f\n\t"
    "global_load_dwordx4 v[40:43], v6, %[ag] sc1\n\t"
    "global_load_dwordx4 v[44:47], v6, %[ag] offset:1024 sc1\n\t"
    "s_waitcnt lgkmcnt(0)\n\t"
    "s_memtime %[tc]\n\t"
    "s_nop 7\n\t"
    "s_branch 3b\n\t"
    "7:\n\t"
    "s_mov_b32 %[cap], 0\n\t"
    "s_branch 2b\n\t"
    "20:\n\t"
    "s_barrier\n\t"
    "s_barrier\n\t"
    "s_barrier\n\t"
    "s_branch 9f\n\t"
    "4:\n\t"
    "s_add_u32 %[sp], %[sp], 0x10000\n\t"
    "s_lshr_b32 %[st], %[sp], 16\n\t"
    "s_cmp_ge_u32 %[st], %[cap]\n\t"
    "s_cbranch_scc1 8f\n\t"
    "s_mov_b32 exec_hi, 0\n\t"
    "global_load_dwordx4 v[6:9], v14, %[vs] sc1\n\t"
    "s_mov_b32 exec_hi, -1\n\t"
    "s_branch 5b\n\t"
    "8:\n\t"
    "s_mov_b32 %[cap], 0\n\t"
    "s_branch 6b\n\t"
    "10:\n\t"
    "v_max_f32 v11, v11, v10\n\t"
    "v_mov_b32 v26, v11\n\t"
    "s_nop 1\n\t"
    "v_permlane16_swap_b32 v11, v26\n\t"
    "v_max_f32 v11, v11, v26\n\t"
    "v_sub_f32 v28, v40, v11\n\t"
    "v_sub_f32 v29, v41, v11\n\t"
    "v_sub_f32 v30, v42, v11\n\t"
    "v_sub_f32 v31, v43, v11\n\t"
    "v_sub_f32 v11, v10, v11\n\t"
    "v_exp_f32 v11, v11\n\t"
    "v_exp_f32 v28, v28\n\t"
    "v_exp_f32 v29, v29\n\t"
    "v_exp_f32 v30, v30\n\t"
    "v_exp_f32 v31, v31\n\t"
    "v_mul_f32 v44, v44, v11\n\t"
    "v_mul_f32 v45, v45, v11\n\t"
    "v_mul_f32 v46, v46, v11\n\t"
    "v_mul_f32 v47, v47, v11\n\t"
    "v_mul_f32 v48, v48, v11\n\t"
    "v_mul_f32 v49, v49, v11\n\t"
    "v_mul_f32 v50, v50, v11\n\t"
    "v_mul_f32 v51, v51, v11\n\t"
    "v_mul_f32 v52, v52, v11\n\t"
    "v_mul_f32 v53, v53, v11\n\t"
    "v_mul_f32 v54, v54, v11\n\t"
    "v_mul_f32 v55, v55, v11\n\t"
    "s_branch 11b\n\t"
    "12:\n\t"
    "v_max3_f32 v10, v24, v25, v26\n\t"
    "v_max3_f32 v40, v27, v28, v29\n\t"
    "v_max3_f32 v10, v10, v30, v31\n\t"
    "v_max3_f32 v10, v10, v40, %[neg]\n\t"
    "v_mov_b32 v40, v10\n\t"
    "s_nop 1\n\t"
    "v_permlane16_swap_b32 v10, v40\n\t"
    "v_max_f32 v10, v10, v40\n\t"
    "v_mov_b32 v40, v10\n\t"
    "s_nop 1\n\t"
    "v_permlane32_swap_b32 v10, v40\n\t"
    "v_max_f32 v10, v10, v40\n\t"
    "v_sub_f32 v24, v24, v10\n\t"
    "v_sub_f32 v25, v25, v10\n\t"
    "v_sub_f32 v26, v26, v10\n\t"
    "v_sub_f32 v27, v27, v10\n\t"
    "v_sub_f32 v28, v28, v10\n\t"
    "v_sub_f32 v29, v29, v10\n\t"
    "v_sub_f32 v30, v30, v10\n\t"
    "v_sub_f32 v31, v31, v10\n\t"
    "s_mov_b32 %[bg], 0x7fc00000\n\t"
    "s_branch 13b\n\t"
    "9:"
    : [tc] "=&s"(tc), [sp] "=&s"(sp), [cap] "+s"(cap), [st] "=&s"(st), [bg] "=&s"(bg)
    : [ag] "s"(ag), [c44] "s"(0x4444u), [kv] "s"(kv), [vs] "s"(vs), [od] "s"(od), [od2] "s"(od + 262144), [neg] "s"(-1.0e38f), [lsc] "s"(LSC), [one] "s"(0x3F803F80u), [big] "s"(24.0f), "{v[64:95]}"(W.w0), "{v[96:127]}"(W.w1), "{v[128:159]}"(W.w2), "{v[160:191]}"(W.w3), "{v[192:223]}"(W.w4), "{v[224:255]}"(W.w5)
    : "v6", "v7", "v8", "v9", "v10", "v11", "v12", "v13", "v14", "v15", "v16", "v17", "v18", "v19", "v20", "v21", "v22", "v23", "v24", "v25", "v26", "v27", "v28", "v29", "v30", "v31", "v32", "v33", "v34", "v35", "v36", "v37", "v38", "v39", "v40", "v41", "v42", "v43", "v44", "v45", "v46", "v47", "v48", "v49", "v50", "v51", "v52", "v53", "v54", "v55", "v56", "v57", "v58", "v59", "v60", "v61", "v62", "v63", "vcc", "scc", "memory");
}
template <> __device__ __forceinline__ void qk_step<1>(const QKW& W, const char* ag, const char* vs, const char* od, const char* kv, unsigned& cap, unsigned& sp,
                                                        unsigned long long& tc, unsigned long long* ts, unsigned& d0, unsigned& d1) {
  unsigned st, bg;
  asm volatile("v_mbcnt_lo_u32_b32 v60, -1, 0\n\t"
    "v_mbcnt_hi_u32_b32 v60, -1, v60\n\t"
    "v_lshlrev_b32 v6, 4, v60\n\t"
    "s_mov_b32 %[sp], 0\n\t"
    "s_setprio 3\n\t"
    "global_load_dwordx4 v[40:43], v6, %[ag] sc1\n\t"
    "global_load_dwordx4 v[44:47], v6, %[ag] offset:1024 sc1\n\t"
    "s_memtime %[tc]\n\t"
    "v_add_u32 v7, 43008, v6\n\t"
    "v_add_u32 v8, 100352, v6\n\t"
    "ds_read_b128 v[24:27], v7 offset:0\n\t"
    "ds_read_b128 v[28:31], v7 offset:1024\n\t"
    "ds_read_b128 v[32:35], v7 offset:8192\n\t"
    "ds_read_b128 v[36:39], v7 offset:9216\n\t"
    "v_lshrrev_b32 v48, 4, v60\n\t"
    "v_and_b32 v49, 15, v60\n\t"
    "v_and_b32 v61, 4, v60\n\t"
    "v_mul_u32_u24 v61, 0x2aaa, v61\n\t"
    "v_lshrrev_b32 v9, 1, v60\n\t"
    "v_and_b32 v9, 2, v9\n\t"
    "v_or3_b32 v9, v9, v61, %[c44]\n\t"
    "v_add_u32 v14, 512, v6\n\t"
    "v_lshlrev_b32 v61, 2, v48\n\t"
    "v_lshl_or_b32 v10, v49, 4, v61\n\t"
    "v_lshrrev_b32 v50, 2, v49\n\t"
    "v_and_b32 v51, 3, v60\n\t"
    "v_and_b32 v61, 1, v60\n\t"
    "v_lshlrev_b32 v61, 3, v61\n\t"
    "v_lshl_or_b32 v11, v50, 4, v61\n\t"
    "v_lshl_or_b32 v11, v48, 7, v11\n\t"
    "v_xor_b32 v12, v48, v49\n\t"
    "v_lshlrev_b32 v12, 4, v12\n\t"
    "v_lshl_or_b32 v12, v49, 8, v12\n\t"
    "v_lshl_or_b32 v50, v48, 2, v50\n\t"
    "v_or_b32 v51, 4, v51\n\t"
    "v_xor_b32 v51, v51, v50\n\t"
    "v_lshlrev_b32 v13, 4, v51\n\t"
    "v_lshl_or_b32 v13, v50, 8, v13\n\t"
    "v_and_b32 v61, 3, v60\n\t"
    "v_lshlrev_b32 v61, 4, v61\n\t"
    "v_lshl_or_b32 v15, v48, 6, v61\n\t"
    "v_and_b32 v61, 8, v60\n\t"
    "v_lshl_or_b32 v15, v61, 14, v15\n\t"
    "v_and_b32 v61, 4, v60\n\t"
    "v_lshl_or_b32 v15, v61, 8, v15\n\t"
    "v_add_u32 v15, 256, v15\n\t"
    "3:\n\t"
    "v_mov_b32 v16, 0\n\t"
    "v_mov_b32 v17, 0\n\t"
    "v_mov_b32 v18, 0\n\t"
    "v_mov_b32 v19, 0\n\t"
    "v_mov_b32 v20, 0\n\t"
    "v_mov_b32 v21, 0\n\t"
    "v_mov_b32 v22, 0\n\t"
    "v_mov_b32 v23, 0\n\t"
    "s_waitcnt vmcnt(0) lgkmcnt(0)\n\t"
    "14:\n\t"
    "ds_write_b128 v6, v[40:43] offset:2048\n\t"
    "ds_write_b128 v6, v[44:47] offset:3072\n\t"
    "v_smfmac_f32_16x16x64_bf16 v[16:19], v[40:43], v[96:103], v9\n\t"
    "v_max3_u32 v60, v40, v41, v42\n\t"
    "v_max3_u32 v61, v43, v44, v45\n\t"
    "v_smfmac_f32_16x16x64_bf16 v[20:23], v[40:43], v[104:111], v9\n\t"
    "v_max3_u32 v60, v60, v46, v47\n\t"
    "v_max_u32 v60, v60, v61\n\t"
    "v_cmp_eq_u32 vcc, -1, v60\n\t"
    "s_cbranch_vccnz 1f\n\t"
    "2:\n\t"
    "s_waitcnt lgkmcnt(0)\n\t"
    "s_cmp_eq_u64 %[kv], 0\n\t"
    "s_barrier\n\t"
    "s_cbranch_scc1 20f\n\t"
    "ds_read_b128 v[40:43], v6 offset:12288\n\t"
    "ds_read_b128 v[48:51], v6 offset:0\n\t"
    "v_smfmac_f32_16x16x64_bf16 v[16:19], v[44:47], v[112:119], v9\n\t"
    "ds_read_b128 v[52:55], v6 offset:1024\n\t"
    "ds_read_b128 v[56:59], v6 offset:4096\n\t"
    "v_smfmac_f32_16x16x64_bf16 v[20:23], v[44:47], v[120:127], v9\n\t"
    "ds_read_b128 v[60:63], v6 offset:13312\n\t"
    "ds_read_b128 v[44:47], v6 offset:5120\n\t"
    "s_waitcnt lgkmcnt(5)\n\t"
    "v_smfmac_f32_16x16x64_bf16 v[16:19], v[40:43], v[24:31], v9\n\t"
    "ds_read_b128 v[24:27], v7 offset:16384\n\t"
    "ds_read_b128 v[28:31], v7 offset:17408\n\t"
    "v_smfmac_f32_16x16x64_bf16 v[20:23], v[40:43], v[32:39], v9\n\t"
    "ds_read_b128 v[32:35], v7 offset:24576\n\t"
    "ds_read_b128 v[36:39], v7 offset:25600\n\t"
    "ds_read_b128 v[40:43], v6 offset:6144\n\t"
    "s_waitcnt lgkmcnt(9)\n\t"
    "v_smfmac_f32_16x16x64_bf16 v[16:19], v[48:51], v[64:71], v9\n\t"
    "v_smfmac_f32_16x16x64_bf16 v[20:23], v[48:51], v[72:79], v9\n\t"
    "ds_read_b128 v[48:51], v6 offset:7168\n\t"
    "s_waitcnt lgkmcnt(9)\n\t"
    "v_smfmac_f32_16x16x64_bf16 v[16:19], v[52:55], v[80:87], v9\n\t"
    "v_smfmac_f32_16x16x64_bf16 v[20:23], v[52:55], v[88:95], v9\n\t"
    "ds_read_b128 v[52:55], v6 offset:14336\n\t"
    "s_waitcnt lgkmcnt(9)\n\t"
    "v_smfmac_f32_16x16x64_bf16 v[16:19], v[56:59], v[128:135], v9\n\t"
    "v_smfmac_f32_16x16x64_bf16 v[20:23], v[56:59], v[136:143], v9\n\t"
    "ds_read_b128 v[56:59], v6 offset:8192\n\t"
    "s_waitcnt lgkmcnt(6)\n\t"
    "v_smfmac_f32_16x16x64_bf16 v[16:19], v[60:63], v[24:31], v9\n\t"
    "ds_read_b128 v[24:27], v8 offset:0\n\t"
    "ds_read_b128 v[28:31], v8 offset:1024\n\t"
    "s_waitcnt lgkmcnt(6)\n\t"
    "v_smfmac_f32_16x16x64_bf16 v[20:23], v[60:63], v[32:39], v9\n\t"
    "ds_read_b128 v[32:35], v8 offset:8192\n\t"
    "ds_read_b128 v[36:39], v8 offset:9216\n\t"
    "ds_read_b128 v[60:63], v6 offset:9216\n\t"
    "v_smfmac_f32_16x16x64_bf16 v[16:19], v[44:47], v[144:151], v9\n\t"
    "v_smfmac_f32_16x16x64_bf16 v[20:23], v[44:47], v[152:159], v9\n\t"
    "ds_read_b128 v[44:47], v6 offset:10240\n\t"
    "s_waitcnt lgkmcnt(9)\n\t"
    "v_smfmac_f32_16x16x64_bf16 v[16:19], v[40:43], v[160:167], v9\n\t"
    "v_smfmac_f32_16x16x64_bf16 v[20:23], v[40:43], v[168:175], v9\n\t"
    "ds_read_b128 v[40:43], v6 offset:15360\n\t"
    "s_waitcnt lgkmcnt(9)\n\t"
    "v_smfmac_f32_16x16x64_bf16 v[16:19], v[48:51], v[176:183], v9\n\t"
    "v_smfmac_f32_16x16x64_bf16 v[20:23], v[48:51], v[184:191], v9\n\t"
    "ds_read_b128 v[48:51], v6 offset:11264\n\t"
    "s_waitcnt lgkmcnt(6)\n\t"
    "v_smfmac_f32_16x16x64_bf16 v[16:19], v[52:55], v[24:31], v9\n\t"
    "ds_read_b128 v[24:27], v8 offset:16384\n\t"
    "ds_read_b128 v[28:31], v8 offset:17408\n\t"
    "s_waitcnt lgkmcnt(6)\n\t"
    "v_smfmac_f32_16x16x64_bf16 v[20:23], v[52:55], v[32:39], v9\n\t"
    "ds_read_b128 v[32:35], v8 offset:24576\n\t"
    "ds_read_b128 v[36:39], v8 offset:25600\n\t"
    "v_smfmac_f32_16x16x64_bf16 v[16:19], v[56:59], v[192:199], v9\n\t"
    "v_smfmac_f32_16x16x64_bf16 v[20:23], v[56:59], v[200:207], v9\n\t"
    "s_waitcnt lgkmcnt(7)\n\t"
    "v_smfmac_f32_16x16x64_bf16 v[16:19], v[60:63], v[208:215], v9\n\t"
    "v_smfmac_f32_16x16x64_bf16 v[20:23], v[60:63], v[216:223], v9\n\t"
    "s_waitcnt lgkmcnt(6)\n\t"
    "v_smfmac_f32_16x16x64_bf16 v[16:19], v[44:47], v[224:231], v9\n\t"
    "v_smfmac_f32_16x16x64_bf16 v[20:23], v[44:47], v[232:239], v9\n\t"
    "s_waitcnt lgkmcnt(2)\n\t"
    "v_smfmac_f32_16x16x64_bf16 v[16:19], v[40:43], v[24:31], v9\n\t"
    "s_waitcnt lgkmcnt(0)\n\t"
    "v_smfmac_f32_16x16x64_bf16 v[20:23], v[40:43], v[32:39], v9\n\t"
    "v_smfmac_f32_16x16x64_bf16 v[16:19], v[48:51], v[240:247], v9\n\t"
    "v_smfmac_f32_16x16x64_bf16 v[20:23], v[48:51], v[248:255], v9\n\t"
    "v_xor_b32 v7, 64, v12\n\t"
    "v_xor_b32 v8, 0x80, v12\n\t"
    "v_xor_b32 v6, 0xc0, v12\n\t"
    "ds_read_b128 v[24:27], v12 offset:16384\n\t"
    "ds_read_b128 v[28:31], v12 offset:20480\n\t"
    "ds_read_b128 v[32:35], v7 offset:16384\n\t"
    "ds_read_b128 v[36:39], v7 offset:20480\n\t"
    "v_permlane16_swap_b32 v16, v18\n\t"
    "v_permlane16_swap_b32 v17, v19\n\t"
    "v_permlane16_swap_b32 v20, v22\n\t"
    "v_permlane16_swap_b32 v21, v23\n\t"
    "v_add_f32 v16, v16, v18\n\t"
    "v_add_f32 v17, v17, v19\n\t"
    "v_add_f32 v20, v20, v22\n\t"
    "v_mul_f32 v16, %[lsc], v16\n\t"
    "v_mul_f32 v17, %[lsc], v17\n\t"
    "v_cvt_pk_bf16_f32 v16, v16, v17\n\t"
    "v_add_f32 v21, v21, v23\n\t"
    "ds_write_b32 v10, v16 offset:33280\n\t"
    "v_mul_f32 v20, %[lsc], v20\n\t"
    "v_mul_f32 v21, %[lsc], v21\n\t"
    "v_cvt_pk_bf16_f32 v20, v20, v21\n\t"
    "ds_write_b32 v10, v20 offset:33536\n\t"
    "ds_read_b128 v[40:43], v8 offset:16384\n\t"
    "ds_read_b128 v[44:47], v8 offset:20480\n\t"
    "ds_read_b128 v[48:51], v6 offset:16384\n\t"
    "ds_read_b128 v[52:55], v6 offset:20480\n\t"
    "s_waitcnt lgkmcnt(4)\n\t"
    "s_barrier\n\t"
    "ds_read_b64_tr_b16 v[16:17], v11 offset:32768\n\t"
    "ds_read_b64_tr_b16 v[18:19], v11 offset:32832\n\t"
    "ds_read_b64_tr_b16 v[20:21], v11 offset:33280\n\t"
    "ds_read_b64_tr_b16 v[22:23], v11 offset:33344\n\t"
    "ds_read_b64_tr_b16 v[56:57], v11 offset:33792\n\t"
    "ds_read_b64_tr_b16 v[58:59], v11 offset:33856\n\t"
    "ds_read_b64_tr_b16 v[60:61], v11 offset:34304\n\t"
    "ds_read_b64_tr_b16 v[62:63], v11 offset:34368\n\t"
    "v_mov_b32 v6, 0\n\t"
    "v_mov_b32 v7, 0\n\t"
    "v_mov_b32 v8, 0\n\t"
    "v_mov_b32 v9, 0\n\t"
    "s_mov_b32 exec_hi, 0\n\t"
    "global_load_dwordx4 v[6:9], v14, %[vs] sc1\n\t"
    "s_mov_b32 exec_hi, -1\n\t"
    "s_waitcnt lgkmcnt(6)\n\t"
    "v_mfma_f32_16x16x32_bf16 v[24:27], v[24:27], v[16:19], 0\n\t"
    "v_mfma_f32_16x16x32_bf16 v[28:31], v[28:31], v[16:19], 0\n\t"
    "s_waitcnt lgkmcnt(4)\n\t"
    "v_mfma_f32_16x16x32_bf16 v[24:27], v[32:35], v[20:23], v[24:27]\n\t"
    "v_mfma_f32_16x16x32_bf16 v[28:31], v[36:39], v[20:23], v[28:31]\n\t"
    "ds_read_b64_tr_b16 v[32:33], v13 offset:24576\n\t"
    "ds_read_b64_tr_b16 v[34:35], v13 offset:28672\n\t"
    "ds_read_b64_tr_b16 v[36:37], v13 offset:24584\n\t"
    "ds_read_b64_tr_b16 v[38:39], v13 offset:28680\n\t"
    "s_waitcnt lgkmcnt(6)\n\t"
    "v_mfma_f32_16x16x32_bf16 v[24:27], v[40:43], v[56:59], v[24:27]\n\t"
    "v_mfma_f32_16x16x32_bf16 v[28:31], v[44:47], v[56:59], v[28:31]\n\t"
    "s_waitcnt lgkmcnt(4)\n\t"
    "v_mfma_f32_16x16x32_bf16 v[24:27], v[48:51], v[60:63], v[24:27]\n\t"
    "v_mfma_f32_16x16x32_bf16 v[28:31], v[52:55], v[60:63], v[28:31]\n\t"
    "s_setprio 1\n\t"
    "v_mov_b32 v52, %[one]\n\t"
    "v_mov_b32 v53, %[one]\n\t"
    "v_mov_b32 v54, %[one]\n\t"
    "v_mov_b32 v55, %[one]\n\t"
    "s_nop 2\n\t"
    "v_max3_f32 v10, |v24|, |v25|, |v26|\n\t"
    "v_max3_f32 v40, |v27|, |v28|, |v29|\n\t"
    "v_max3_f32 v10, v10, |v30|, |v31|\n\t"
    "v_max_f32 v10, v10, v40\n\t"
    "v_cmp_nge_f32 vcc, %[big], v10\n\t"
    "s_mov_b32 %[bg], %[big]\n\t"
    "v_mov_b32 v10, 0\n\t"
    "s_cbranch_vccnz 12f\n\t"
    "13:\n\t"
    "v_exp_f32 v24, v24\n\t"
    "v_exp_f32 v25, v25\n\t"
    "v_exp_f32 v26, v26\n\t"
    "v_exp_f32 v27, v27\n\t"
    "v_exp_f32 v28, v28\n\t"
    "v_exp_f32 v29, v29\n\t"
    "v_exp_f32 v30, v30\n\t"
    "v_exp_f32 v31, v31\n\t"
    "v_cvt_pk_bf16_f32 v40, v24, v25\n\t"
    "v_cvt_pk_bf16_f32 v41, v26, v27\n\t"
    "v_cvt_pk_bf16_f32 v42, v28, v29\n\t"
    "v_cvt_pk_bf16_f32 v43, v30, v31\n\t"
    "s_setprio 3\n\t"
    "s_waitcnt lgkmcnt(0)\n\t"
    "s_nop 0\n\t"
    "v_mfma_f32_16x16x32_bf16 v[44:47], v[32:35], v[40:43], 0\n\t"
    "v_mfma_f32_16x16x32_bf16 v[48:51], v[36:39], v[40:43], 0\n\t"
    "v_mfma_f32_16x16x32_bf16 v[52:55], v[52:55], v[40:43], 0\n\t"
    "v_mov_b32 v12, 0\n\t"
    "v_mov_b32 v13, 0\n\t"
    "s_mov_b32 exec_hi, 0\n\t"
    "v_mov_b32 v12, %[one]\n\t"
    "v_mov_b32 v13, %[one]\n\t"
    "s_mov_b32 exec_hi, -1\n\t"
    "s_barrier\n\t"
    "ds_read_b64_tr_b16 v[24:25], v11 offset:34816\n\t"
    "ds_read_b64_tr_b16 v[26:27], v11 offset:34880\n\t"
    "ds_read_b64_tr_b16 v[28:29], v11 offset:35328\n\t"
    "ds_read_b64_tr_b16 v[30:31], v11 offset:35392\n\t"
    "ds_read_b64_tr_b16 v[32:33], v11 offset:35840\n\t"
    "ds_read_b64_tr_b16 v[34:35], v11 offset:35904\n\t"
    "ds_read_b64_tr_b16 v[36:37], v11 offset:36352\n\t"
    "ds_read_b64_tr_b16 v[38:39], v11 offset:36416\n\t"
    "s_waitcnt lgkmcnt(6)\n\t"
    "v_mfma_f32_16x16x32_bf16 v[40:43], v[24:27], v[16:19], 0\n\t"
    "s_waitcnt lgkmcnt(4)\n\t"
    "v_mfma_f32_16x16x32_bf16 v[40:43], v[28:31], v[20:23], v[40:43]\n\t"
    "s_waitcnt lgkmcnt(2)\n\t"
    "v_mfma_f32_16x16x32_bf16 v[40:43], v[32:35], v[56:59], v[40:43]\n\t"
    "s_waitcnt lgkmcnt(0)\n\t"
    "v_mfma_f32_16x16x32_bf16 v[40:43], v[36:39], v[60:63], v[40:43]\n\t"
    "5:\n\t"
    "s_waitcnt vmcnt(0)\n\t"
    "v_max3_u32 v11, v6, v7, v8\n\t"
    "v_max_u32 v11, v11, v9\n\t"
    "v_cmp_eq_u32 vcc, -1, v11\n\t"
    "s_cbranch_vccnz 4f\n\t"
    "6:\n\t"
    "s_nop 2\n\t"
    "v_exp_f32 v28, v40\n\t"
    "v_max3_f32 v11, v40, v41, v42\n\t"
    "v_exp_f32 v29, v41\n\t"
    "v_max_f32 v11, v11, v43\n\t"
    "v_exp_f32 v30, v42\n\t"
    "v_cmp_nge_f32 vcc, %[bg], v11\n\t"
    "v_exp_f32 v31, v43\n\t"
    "s_cbranch_vccnz 10f\n\t"
    "11:\n\t"
    "v_cvt_pk_bf16_f32 v24, v28, v29\n\t"
    "v_cvt_pk_bf16_f32 v25, v30, v31\n\t"
    "s_nop 1\n\t"
    "v_mfma_f32_16x16x16_bf16 v[52:55], v[12:13], v[24:25], v[52:55]\n\t"
    "v_mfma_f32_16x16x16_bf16 v[44:47], v[6:7], v[24:25], v[44:47]\n\t"
    "v_mfma_f32_16x16x16_bf16 v[48:51], v[8:9], v[24:25], v[48:51]\n\t"
    "s_nop 5\n\t"
    "v_rcp_f32 v26, v52\n\t"
    "s_nop 0\n\t"
    "v_pk_mul_f32 v[44:45], v[44:45], v[26:27] op_sel_hi:[1,0]\n\t"
    "v_pk_mul_f32 v[46:47], v[46:47], v[26:27] op_sel_hi:[1,0]\n\t"
    "v_pk_mul_f32 v[48:49], v[48:49], v[26:27] op_sel_hi:[1,0]\n\t"
    "v_pk_mul_f32 v[50:51], v[50:51], v[26:27] op_sel_hi:[1,0]\n\t"
    "v_cvt_pk_bf16_f32 v36, v44, v45\n\t"
    "v_cvt_pk_bf16_f32 v37, v46, v47\n\t"
    "v_cvt_pk_bf16_f32 v38, v48, v49\n\t"
    "v_cvt_pk_bf16_f32 v39, v50, v51\n\t"
    "v_min_u32 v36, -2, v36\n\t"
    "v_min_u32 v37, -2, v37\n\t"
    "v_min_u32 v38, -2, v38\n\t"
    "v_min_u32 v39, -2, v39\n\t"
    "global_store_dwordx4 v15, v[36:39], %[od] sc1\n\t"
    "s_barrier\n\t"
    "s_mov_b32 exec_hi, 0\n\t"
    "global_store_dwordx4 v14, v[6:9], %[kv] sc1\n\t"
    "s_mov_b32 exec_hi, -1\n\t"
    "s_nop 0\n\t"
    "s_waitcnt lgkmcnt(0)\n\t"
    "s_branch 9f\n\t"
    "1:\n\t"
    "s_add_u32 %[sp], %[sp], 1\n\t"
    "s_cmp_ge_u32 %[sp], %[cap]\n\t"
    "s_cbranch_scc1 7f\n\t"
    "global_load_dwordx4 v[40:43], v6, %[ag] sc1\n\t"
    "global_load_dwordx4 v[44:47], v6, %[ag] offset:1024 sc1\n\t"
    "s_waitcnt lgkmcnt(0)\n\t"
    "s_memtime %[tc]\n\t"
    "s_nop 7\n\t"
    "s_branch 3b\n\t"
    "7:\n\t"
    "s_mov_b32 %[cap], 0\n\t"
    "s_branch 2b\n\t"
    "20:\n\t"
    "s_barrier\n\t"
    "s_barrier\n\t"
    "s_barrier\n\t"
    "s_branch 9f\n\t"
    "4:\n\t"
    "s_add_u32 %[sp], %[sp], 0x10000\n\t"
    "s_lshr_b32 %[st], %[sp], 16\n\t"
    "s_cmp_ge_u32 %[st], %[cap]\n\t"
    "s_cbranch_scc1 8f\n\t"
    "s_mov_b32 exec_hi, 0\n\t"
    "global_load_dwordx4 v[6:9], v14, %[vs] sc1\n\t"
    "s_mov_b32 exec_hi, -1\n\t"
    "s_branch 5b\n\t"
    "8:\n\t"
    "s_mov_b32 %[cap], 0\n\t"
    "s_branch 6b\n\t"
    "10:\n\t"
    "v_max_f32 v11, v11, v10\n\t"
    "v_mov_b32 v26, v11\n\t"
    "s_nop 1\n\t"
    "v_permlane16_swap_b32 v11, v26\n\t"
    "v_max_f32 v11, v11, v26\n\t"
    "v_sub_f32 v28, v40, v11\n\t"
    "v_sub_f32 v29, v41, v11\n\t"
    "v_sub_f32 v30, v42, v11\n\t"
    "v_sub_f32 v31, v43, v11\n\t"
    "v_sub_f32 v11, v10, v11\n\t"
    "v_exp_f32 v11, v11\n\t"
    "v_exp_f32 v28, v28\n\t"
    "v_exp_f32 v29, v29\n\t"
    "v_exp_f32 v30, v30\n\t"
    "v_exp_f32 v31, v31\n\t"
    "v_mul_f32 v44, v44, v11\n\t"
    "v_mul_f32 v45, v45, v11\n\t"
    "v_mul_f32 v46, v46, v11\n\t"
    "v_mul_f32 v47, v47, v11\n\t"
    "v_mul_f32 v48, v48, v11\n\t"
    "v_mul_f32 v49, v49, v11\n\t"
    "v_mul_f32 v50, v50, v11\n\t"
    "v_mul_f32 v51, v51, v11\n\t"
    "v_mul_f32 v52, v52, v11\n\t"
    "v_mul_f32 v53, v53, v11\n\t"
    "v_mul_f32 v54, v54, v11\n\t"
    "v_mul_f32 v55, v55, v11\n\t"
    "s_branch 11b\n\t"
    "12:\n\t"
    "v_max3_f32 v10, v24, v25, v26\n\t"
    "v_max3_f32 v40, v27, v28, v29\n\t"
    "v_max3_f32 v10, v10, v30, v31\n\t"
    "v_max3_f32 v10, v10, v40, %[neg]\n\t"
    "v_mov_b32 v40, v10\n\t"
    "s_nop 1\n\t"
    "v_permlane16_swap_b32 v10, v40\n\t"
    "v_max_f32 v10, v10, v40\n\t"
    "v_mov_b32 v40, v10\n\t"
    "s_nop 1\n\t"
    "v_permlane32_swap_b32 v10, v40\n\t"
    "v_max_f32 v10, v10, v40\n\t"
    "v_sub_f32 v24, v24, v10\n\t"
    "v_sub_f32 v25, v25, v10\n\t"
    "v_sub_f32 v26, v26, v10\n\t"
    "v_sub_f32 v27, v27, v10\n\t"
    "v_sub_f32 v28, v28, v10\n\t"
    "v_sub_f32 v29, v29, v10\n\t"
    "v_sub_f32 v30, v30, v10\n\t"
    "v_sub_f32 v31, v31, v10\n\t"
    "s_mov_b32 %[bg], 0x7fc00000\n\t"
    "s_branch 13b\n\t"
    "9:"
    : [tc] "=&s"(tc), [sp] "=&s"(sp), [cap] "+s"(cap), [st] "=&s"(st), [bg] "=&s"(bg)
    : [ag] "s"(ag), [c44] "s"(0x4444u), [kv] "s"(kv), [vs] "s"(vs), [od] "s"(od), [od2] "s"(od + 262144), [neg] "s"(-1.0e38f), [lsc] "s"(LSC), [one] "s"(0x3F803F80u), [big] "s"(24.0f), "{v[64:95]}"(W.w0), "{v[96:127]}"(W.w1), "{v[128:159]}"(W.w2), "{v[160:191]}"(W.w3), "{v[192:223]}"(W.w4), "{v[224:255]}"(W.w5)
    : "v6", "v7", "v8", "v9", "v10", "v11", "v12", "v13", "v14", "v15", "v16", "v17", "v18", "v19", "v20", "v21", "v22", "v23", "v24", "v25", "v26", "v27", "v28", "v29", "v30", "v31", "v32", "v33", "v34", "v35", "v36", "v37", "v38", "v39", "v40", "v41", "v42", "v43", "v44", "v45", "v46", "v47", "v48", "v49", "v50", "v51", "v52", "v53", "v54", "v55", "v56", "v57", "v58", "v59", "v60", "v61", "v62", "v63", "vcc", "scc", "memory");
}
template <> __device__ __forceinline__ void qk_step<2>(const QKW& W, const char* ag, const char* vs, const char* od, const char* kv, unsigned& cap, unsigned& sp,
                                                        unsigned long long& tc, unsigned long long* ts, unsigned& d0, unsigned& d1) {
  unsigned st, bg;
  asm volatile("v_mbcnt_lo_u32_b32 v60, -1, 0\n\t"
    "v_mbcnt_hi_u32_b32 v60, -1, v60\n\t"
    "v_lshlrev_b32 v6, 4, v60\n\t"
    "s_mov_b32 %[sp], 0\n\t"
    "s_setprio 3\n\t"
    "global_load_dwordx4 v[40:43], v6, %[ag] sc1\n\t"
    "global_load_dwordx4 v[44:47], v6, %[ag] offset:1024 sc1\n\t"
    "s_memtime %[tc]\n\t"
    "v_add_u32 v7, 45056, v6\n\t"
    "v_add_u32 v8, 102400, v6\n\t"
    "ds_read_b128 v[24:27], v7 offset:0\n\t"
    "ds_read_b128 v[28:31], v7 offset:1024\n\t"
    "ds_read_b128 v[32:35], v7 offset:8192\n\t"
    "ds_read_b128 v[36:39], v7 offset:9216\n\t"
    "v_lshrrev_b32 v48, 4, v60\n\t"
    "v_and_b32 v49, 15, v60\n\t"
    "v_and_b32 v61, 4, v60\n\t"
    "v_mul_u32_u24 v61, 0x2aaa, v61\n\t"
    "v_lshrrev_b32 v9, 1, v60\n\t"
    "v_and_b32 v9, 2, v9\n\t"
    "v_or3_b32 v9, v9, v61, %[c44]\n\t"
    "v_add_u32 v14, 1024, v6\n\t"
    "v_lshlrev_b32 v61, 2, v48\n\t"
    "v_lshl_or_b32 v10, v49, 4, v61\n\t"
    "v_lshrrev_b32 v50, 2, v49\n\t"
    "v_and_b32 v51, 3, v60\n\t"
    "v_and_b32 v61, 1, v60\n\t"
    "v_lshlrev_b32 v61, 3, v61\n\t"
    "v_lshl_or_b32 v11, v50, 4, v61\n\t"
    "v_lshl_or_b32 v11, v48, 7, v11\n\t"
    "v_xor_b32 v12, v48, v49\n\t"
    "v_lshlrev_b32 v12, 4, v12\n\t"
    "v_lshl_or_b32 v12, v49, 8, v12\n\t"
    "v_lshl_or_b32 v50, v48, 2, v50\n\t"
    "v_or_b32 v51, 8, v51\n\t"
    "v_xor_b32 v51, v51, v50\n\t"
    "v_lshlrev_b32 v13, 4, v51\n\t"
    "v_lshl_or_b32 v13, v50, 8, v13\n\t"
    "v_and_b32 v61, 3, v60\n\t"
    "v_lshlrev_b32 v61, 4, v61\n\t"
    "v_lshl_or_b32 v15, v48, 6, v61\n\t"
    "v_and_b32 v61, 8, v60\n\t"
    "v_lshl_or_b32 v15, v61, 14, v15\n\t"
    "v_and_b32 v61, 4, v60\n\t"
    "v_lshl_or_b32 v15, v61, 8, v15\n\t"
    "v_add_u32 v15, 512, v15\n\t"
    "3:\n\t"
    "v_mov_b32 v16, 0\n\t"
    "v_mov_b32 v17, 0\n\t"
    "v_mov_b32 v18, 0\n\t"
    "v_mov_b32 v19, 0\n\t"
    "v_mov_b32 v20, 0\n\t"
    "v_mov_b32 v21, 0\n\t"
    "v_mov_b32 v22, 0\n\t"
    "v_mov_b32 v23, 0\n\t"
    "s_waitcnt vmcnt(0) lgkmcnt(0)\n\t"
    "14:\n\t"
    "ds_write_b128 v6, v[40:43] offset:4096\n\t"
    "ds_write_b128 v6, v[44:47] offset:5120\n\t"
    "v_smfmac_f32_16x16x64_bf16 v[16:19], v[40:43], v[128:135], v9\n\t"
    "v_max3_u32 v60, v40, v41, v42\n\t"
    "v_max3_u32 v61, v43, v44, v45\n\t"
    "v_smfmac_f32_16x16x64_bf16 v[20:23], v[40:43], v[136:143], v9\n\t"
    "v_max3_u32 v60, v60, v46, v47\n\t"
    "v_max_u32 v60, v60, v61\n\t"
    "v_cmp_eq_u32 vcc, -1, v60\n\t"
    "s_cbranch_vccnz 1f\n\t"
    "2:\n\t"
    "s_waitcnt lgkmcnt(0)\n\t"
    "s_cmp_eq_u64 %[kv], 0\n\t"
    "s_barrier\n\t"
    "s_cbranch_scc1 20f\n\t"
    "ds_read_b128 v[40:43], v6 offset:12288\n\t"
    "ds_read_b128 v[48:51], v6 offset:0\n\t"
    "v_smfmac_f32_16x16x64_bf16 v[16:19], v[44:47], v[144:151], v9\n\t"
    "ds_read_b128 v[52:55], v6 offset:1024\n\t"
    "ds_read_b128 v[56:59], v6 offset:2048\n\t"
    "v_smfmac_f32_16x16x64_bf16 v[20:23], v[44:47], v[152:159], v9\n\t"
    "ds_read_b128 v[60:63], v6 offset:13312\n\t"
    "ds_read_b128 v[44:47], v6 offset:3072\n\t"
    "s_waitcnt lgkmcnt(5)\n\t"
    "v_smfmac_f32_16x16x64_bf16 v[16:19], v[40:43], v[24:31], v9\n\t"
    "ds_read_b128 v[24:27], v7 offset:16384\n\t"
    "ds_read_b128 v[28:31], v7 offset:17408\n\t"
    "v_smfmac_f32_16x16x64_bf16 v[20:23], v[40:43], v[32:39], v9\n\t"
    "ds_read_b128 v[32:35], v7 offset:24576\n\t"
    "ds_read_b128 v[36:39], v7 offset:25600\n\t"
    "ds_read_b128 v[40:43], v6 offset:6144\n\t"
    "s_waitcnt lgkmcnt(9)\n\t"
    "v_smfmac_f32_16x16x64_bf16 v[16:19], v[48:51], v[64:71], v9\n\t"
    "v_smfmac_f32_16x16x64_bf16 v[20:23], v[48:51], v[72:79], v9\n\t"
    "ds_read_b128 v[48:51], v6 offset:7168\n\t"
    "s_waitcnt lgkmcnt(9)\n\t"
    "v_smfmac_f32_16x16x64_bf16 v[16:19], v[52:55], v[80:87], v9\n\t"
    "v_smfmac_f32_16x16x64_bf16 v[20:23], v[52:55], v[88:95], v9\n\t"
    "ds_read_b128 v[52:55], v6 offset:14336\n\t"
    "s_waitcnt lgkmcnt(9)\n\t"
    "v_smfmac_f32_16x16x64_bf16 v[16:19], v[56:59], v[96:103], v9\n\t"
    "v_smfmac_f32_16x16x64_bf16 v[20:23], v[56:59], v[104:111], v9\n\t"
    "ds_read_b128 v[56:59], v6 offset:8192\n\t"
    "s_waitcnt lgkmcnt(6)\n\t"
    "v_smfmac_f32_16x16x64_bf16 v[16:19], v[60:63], v[24:31], v9\n\t"
    "ds_read_b128 v[24:27], v8 offset:0\n\t"
    "ds_read_b128 v[28:31], v8 offset:1024\n\t"
    "s_waitcnt lgkmcnt(6)\n\t"
    "v_smfmac_f32_16x16x64_bf16 v[20:23], v[60:63], v[32:39], v9\n\t"
    "ds_read_b128 v[32:35], v8 offset:8192\n\t"
    "ds_read_b128 v[36:39], v8 offset:9216\n\t"
    "ds_read_b128 v[60:63], v6 offset:9216\n\t"
    "v_smfmac_f32_16x16x64_bf16 v[16:19], v[44:47], v[112:119], v9\n\t"
    "v_smfmac_f32_16x16x64_bf16 v[20:23], v[44:47], v[120:127], v9\n\t"
    "ds_read_b128 v[44:47], v6 offset:10240\n\t"
    "s_waitcnt lgkmcnt(9)\n\t"
    "v_smfmac_f32_16x16x64_bf16 v[16:19], v[40:43], v[160:167], v9\n\t"
    "v_smfmac_f32_16x16x64_bf16 v[20:23], v[40:43], v[168:175], v9\n\t"
    "ds_read_b128 v[40:43], v6 offset:15360\n\t"
    "s_waitcnt lgkmcnt(9)\n\t"
    "v_smfmac_f32_16x16x64_bf16 v[16:19], v[48:51], v[176:183], v9\n\t"
    "v_smfmac_f32_16x16x64_bf16 v[20:23], v[48:51], v[184:191], v9\n\t"
    "ds_read_b128 v[48:51], v6 offset:11264\n\t"
    "s_waitcnt lgkmcnt(6)\n\t"
    "v_smfmac_f32_16x16x64_bf16 v[16:19], v[52:55], v[24:31], v9\n\t"
    "ds_read_b128 v[24:27], v8 offset:16384\n\t"
    "ds_read_b128 v[28:31], v8 offset:17408\n\t"
    "s_waitcnt lgkmcnt(6)\n\t"
    "v_smfmac_f32_16x16x64_bf16 v[20:23], v[52:55], v[32:39], v9\n\t"
    "ds_read_b128 v[32:35], v8 offset:24576\n\t"
    "ds_read_b128 v[36:39], v8 offset:25600\n\t"
    "v_smfmac_f32_16x16x64_bf16 v[16:19], v[56:59], v[192:199], v9\n\t"
    "v_smfmac_f32_16x16x64_bf16 v[20:23], v[56:59], v[200:207], v9\n\t"
    "s_waitcnt lgkmcnt(7)\n\t"
    "v_smfmac_f32_16x16x64_bf16 v[16:19], v[60:63], v[208:215], v9\n\t"
    "v_smfmac_f32_16x16x64_bf16 v[20:23], v[60:63], v[216:223], v9\n\t"
    "s_waitcnt lgkmcnt(6)\n\t"
    "v_smfmac_f32_16x16x64_bf16 v[16:19], v[44:47], v[224:231], v9\n\t"
    "v_smfmac_f32_16x16x64_bf16 v[20:23], v[44:47], v[232:239], v9\n\t"
    "s_waitcnt lgkmcnt(2)\n\t"
    "v_smfmac_f32_16x16x64_bf16 v[16:19], v[40:43], v[24:31], v9\n\t"
    "s_waitcnt lgkmcnt(0)\n\t"
    "v_smfmac_f32_16x16x64_bf16 v[20:23], v[40:43], v[32:39], v9\n\t"
    "v_smfmac_f32_16x16x64_bf16 v[16:19], v[48:51], v[240:247], v9\n\t"
    "v_smfmac_f32_16x16x64_bf16 v[20:23], v[48:51], v[248:255], v9\n\t"
    "v_xor_b32 v7, 64, v12\n\t"
    "v_xor_b32 v8, 0x80, v12\n\t"
    "v_xor_b32 v6, 0xc0, v12\n\t"
    "ds_read_b128 v[24:27], v12 offset:16384\n\t"
    "ds_read_b128 v[28:31], v12 offset:20480\n\t"
    "ds_read_b128 v[32:35], v7 offset:16384\n\t"
    "ds_read_b128 v[36:39], v7 offset:20480\n\t"
    "v_permlane16_swap_b32 v16, v18\n\t"
    "v_permlane16_swap_b32 v17, v19\n\t"
    "v_permlane16_swap_b32 v20, v22\n\t"
    "v_permlane16_swap_b32 v21, v23\n\t"
    "v_add_f32 v16, v16, v18\n\t"
    "v_add_f32 v17, v17, v19\n\t"
    "v_add_f32 v20, v20, v22\n\t"
    "v_mul_f32 v16, %[lsc], v16\n\t"
    "v_mul_f32 v17, %[lsc], v17\n\t"
    "v_cvt_pk_bf16_f32 v16, v16, v17\n\t"
    "v_add_f32 v21, v21, v23\n\t"
    "ds_write_b32 v10, v16 offset:33792\n\t"
    "v_mul_f32 v20, %[lsc], v20\n\t"
    "v_mul_f32 v21, %[lsc], v21\n\t"
    "v_cvt_pk_bf16_f32 v20, v20, v21\n\t"
    "ds_write_b32 v10, v20 offset:34048\n\t"
    "ds_read_b128 v[40:43], v8 offset:16384\n\t"
    "ds_read_b128 v[44:47], v8 offset:20480\n\t"
    "ds_read_b128 v[48:51], v6 offset:16384\n\t"
    "ds_read_b128 v[52:55], v6 offset:20480\n\t"
    "s_waitcnt lgkmcnt(4)\n\t"
    "s_barrier\n\t"
    "ds_read_b64_tr_b16 v[16:17], v11 offset:32768\n\t"
    "ds_read_b64_tr_b16 v[18:19], v11 offset:32832\n\t"
    "ds_read_b64_tr_b16 v[20:21], v11 offset:33280\n\t"
    "ds_read_b64_tr_b16 v[22:23], v11 offset:33344\n\t"
    "ds_read_b64_tr_b16 v[56:57], v11 offset:33792\n\t"
    "ds_read_b64_tr_b16 v[58:59], v11 offset:33856\n\t"
    "ds_read_b64_tr_b16 v[60:61], v11 offset:34304\n\t"
    "ds_read_b64_tr_b16 v[62:63], v11 offset:34368\n\t"
    "v_mov_b32 v6, 0\n\t"
    "v_mov_b32 v7, 0\n\t"
    "v_mov_b32 v8, 0\n\t"
    "v_mov_b32 v9, 0\n\t"
    "s_mov_b32 exec_hi, 0\n\t"
    "global_load_dwordx4 v[6:9], v14, %[vs] sc1\n\t"
    "s_mov_b32 exec_hi, -1\n\t"
    "s_waitcnt lgkmcnt(6)\n\t"
    "v_mfma_f32_16x16x32_bf16 v[24:27], v[24:27], v[16:19], 0\n\t"
    "v_mfma_f32_16x16x32_bf16 v[28:31], v[28:31], v[16:19], 0\n\t"
    "s_waitcnt lgkmcnt(4)\n\t"
    "v_mfma_f32_16x16x32_bf16 v[24:27], v[32:35], v[20:23], v[24:27]\n\t"
    "v_mfma_f32_16x16x32_bf16 v[28:31], v[36:39], v[20:23], v[28:31]\n\t"
    "ds_read_b64_tr_b16 v[32:33], v13 offset:24576\n\t"
    "ds_read_b64_tr_b16 v[34:35], v13 offset:28672\n\t"
    "ds_read_b64_tr_b16 v[36:37], v13 offset:24584\n\t"
    "ds_read_b64_tr_b16 v[38:39], v13 offset:28680\n\t"
    "s_waitcnt lgkmcnt(6)\n\t"
    "v_mfma_f32_16x16x32_bf16 v[24:27], v[40:43], v[56:59], v[24:27]\n\t"
    "v_mfma_f32_16x16x32_bf16 v[28:31], v[44:47], v[56:59], v[28:31]\n\t"
    "s_waitcnt lgkmcnt(4)\n\t"
    "v_mfma_f32_16x16x32_bf16 v[24:27], v[48:51], v[60:63], v[24:27]\n\t"
    "v_mfma_f32_16x16x32_bf16 v[28:31], v[52:55], v[60:63], v[28:31]\n\t"
    "s_setprio 1\n\t"
    "v_mov_b32 v52, %[one]\n\t"
    "v_mov_b32 v53, %[one]\n\t"
    "v_mov_b32 v54, %[one]\n\t"
    "v_mov_b32 v55, %[one]\n\t"
    "s_nop 2\n\t"
    "v_max3_f32 v10, |v24|, |v25|, |v26|\n\t"
    "v_max3_f32 v40, |v27|, |v28|, |v29|\n\t"
    "v_max3_f32 v10, v10, |v30|, |v31|\n\t"
    "v_max_f32 v10, v10, v40\n\t"
    "v_cmp_nge_f32 vcc, %[big], v10\n\t"
    "s_mov_b32 %[bg], %[big]\n\t"
    "v_mov_b32 v10, 0\n\t"
    "s_cbranch_vccnz 12f\n\t"
    "13:\n\t"
    "v_exp_f32 v24, v24\n\t"
    "v_exp_f32 v25, v25\n\t"
    "v_exp_f32 v26, v26\n\t"
    "v_exp_f32 v27, v27\n\t"
    "v_exp_f32 v28, v28\n\t"
    "v_exp_f32 v29, v29\n\t"
    "v_exp_f32 v30, v30\n\t"
    "v_exp_f32 v31, v31\n\t"
    "v_cvt_pk_bf16_f32 v40, v24, v25\n\t"
    "v_cvt_pk_bf16_f32 v41, v26, v27\n\t"
    "v_cvt_pk_bf16_f32 v42, v28, v29\n\t"
    "v_cvt_pk_bf16_f32 v43, v30, v31\n\t"
    "s_setprio 3\n\t"
    "s_waitcnt lgkmcnt(0)\n\t"
    "s_nop 0\n\t"
    "v_mfma_f32_16x16x32_bf16 v[44:47], v[32:35], v[40:43], 0\n\t"
    "v_mfma_f32_16x16x32_bf16 v[48:51], v[36:39], v[40:43], 0\n\t"
    "v_mfma_f32_16x16x32_bf16 v[52:55], v[52:55], v[40:43], 0\n\t"
    "v_mov_b32 v12, 0\n\t"
    "v_mov_b32 v13, 0\n\t"
    "s_mov_b32 exec_hi, 0\n\t"
    "v_mov_b32 v12, %[one]\n\t"
    "v_mov_b32 v13, %[one]\n\t"
    "s_mov_b32 exec_hi, -1\n\t"
    "s_barrier\n\t"
    "ds_read_b64_tr_b16 v[24:25], v11 offset:34816\n\t"
    "ds_read_b64_tr_b16 v[26:27], v11 offset:34880\n\t"
    "ds_read_b64_tr_b16 v[28:29], v11 offset:35328\n\t"
    "ds_read_b64_tr_b16 v[30:31], v11 offset:35392\n\t"
    "ds_read_b64_tr_b16 v[32:33], v11 offset:35840\n\t"
    "ds_read_b64_tr_b16 v[34:35], v11 offset:35904\n\t"
    "ds_read_b64_tr_b16 v[36:37], v11 offset:36352\n\t"
    "ds_read_b64_tr_b16 v[38:39], v11 offset:36416\n\t"
    "s_waitcnt lgkmcnt(6)\n\t"
    "v_mfma_f32_16x16x32_bf16 v[40:43], v[24:27], v[16:19], 0\n\t"
    "s_waitcnt lgkmcnt(4)\n\t"
    "v_mfma_f32_16x16x32_bf16 v[40:43], v[28:31], v[20:23], v[40:43]\n\t"
    "s_waitcnt lgkmcnt(2)\n\t"
    "v_mfma_f32_16x16x32_bf16 v[40:43], v[32:35], v[56:59], v[40:43]\n\t"
    "s_waitcnt lgkmcnt(0)\n\t"
    "v_mfma_f32_16x16x32_bf16 v[40:43], v[36:39], v[60:63], v[40:43]\n\t"
    "5:\n\t"
    "s_waitcnt vmcnt(0)\n\t"
    "v_max3_u32 v11, v6, v7, v8\n\t"
    "v_max_u32 v11, v11, v9\n\t"
    "v_cmp_eq_u32 vcc, -1, v11\n\t"
    "s_cbranch_vccnz 4f\n\t"
    "6:\n\t"
    "s_nop 2\n\t"
    "v_exp_f32 v28, v40\n\t"
    "v_max3_f32 v11, v40, v41, v42\n\t"
    "v_exp_f32 v29, v41\n\t"
    "v_max_f32 v11, v11, v43\n\t"
    "v_exp_f32 v30, v42\n\t"
    "v_cmp_nge_f32 vcc, %[bg], v11\n\t"
    "v_exp_f32 v31, v43\n\t"
    "s_cbranch_vccnz 10f\n\t"
    "11:\n\t"
    "v_cvt_pk_bf16_f32 v24, v28, v29\n\t"
    "v_cvt_pk_bf16_f32 v25, v30, v31\n\t"
    "s_nop 1\n\t"
    "v_mfma_f32_16x16x16_bf16 v[52:55], v[12:13], v[24:25], v[52:55]\n\t"
    "v_mfma_f32_16x16x16_bf16 v[44:47], v[6:7], v[24:25], v[44:47]\n\t"
    "v_mfma_f32_16x16x16_bf16 v[48:51], v[8:9], v[24:25], v[48:51]\n\t"
    "s_nop 5\n\t"
    "v_rcp_f32 v26, v52\n\t"
    "s_nop 0\n\t"
    "v_pk_mul_f32 v[44:45], v[44:45], v[26:27] op_sel_hi:[1,0]\n\t"
    "v_pk_mul_f32 v[46:47], v[46:47], v[26:27] op_sel_hi:[1,0]\n\t"
    "v_pk_mul_f32 v[48:49], v[48:49], v[26:27] op_sel_hi:[1,0]\n\t"
    "v_pk_mul_f32 v[50:51], v[50:51], v[26:27] op_sel_hi:[1,0]\n\t"
    "v_cvt_pk_bf16_f32 v36, v44, v45\n\t"
    "v_cvt_pk_bf16_f32 v37, v46, v47\n\t"
    "v_cvt_pk_bf16_f32 v38, v48, v49\n\t"
    "v_cvt_pk_bf16_f32 v39, v50, v51\n\t"
    "v_min_u32 v36, -2, v36\n\t"
    "v_min_u32 v37, -2, v37\n\t"
    "v_min_u32 v38, -2, v38\n\t"
    "v_min_u32 v39, -2, v39\n\t"
    "global_store_dwordx4 v15, v[36:39], %[od] sc1\n\t"
    "s_barrier\n\t"
    "s_mov_b32 exec_hi, 0\n\t"
    "global_store_dwordx4 v14, v[6:9], %[kv] sc1\n\t"
    "s_mov_b32 exec_hi, -1\n\t"
    "s_nop 0\n\t"
    "s_waitcnt lgkmcnt(0)\n\t"
    "s_branch 9f\n\t"
    "1:\n\t"
    "s_add_u32 %[sp], %[sp], 1\n\t"
    "s_cmp_ge_u32 %[sp], %[cap]\n\t"
    "s_cbranch_scc1 7f\n\t"
    "global_load_dwordx4 v[40:43], v6, %[ag] sc1\n\t"
    "global_load_dwordx4 v[44:47], v6, %[ag] offset:1024 sc1\n\t"
    "s_waitcnt lgkmcnt(0)\n\t"
    "s_memtime %[tc]\n\t"
    "s_nop 7\n\t"
    "s_branch 3b\n\t"
    "7:\n\t"
    "s_mov_b32 %[cap], 0\n\t"
    "s_branch 2b\n\t"
    "20:\n\t"
    "s_barrier\n\t"
    "s_barrier\n\t"
    "s_barrier\n\t"
    "s_branch 9f\n\t"
    "4:\n\t"
    "s_add_u32 %[sp], %[sp], 0x10000\n\t"
    "s_lshr_b32 %[st], %[sp], 16\n\t"
    "s_cmp_ge_u32 %[st], %[cap]\n\t"
    "s_cbranch_scc1 8f\n\t"
    "s_mov_b32 exec_hi, 0\n\t"
    "global_load_dwordx4 v[6:9], v14, %[vs] sc1\n\t"
    "s_mov_b32 exec_hi, -1\n\t"
    "s_branch 5b\n\t"
    "8:\n\t"
    "s_mov_b32 %[cap], 0\n\t"
    "s_branch 6b\n\t"
    "10:\n\t"
    "v_max_f32 v11, v11, v10\n\t"
    "v_mov_b32 v26, v11\n\t"
    "s_nop 1\n\t"
    "v_permlane16_swap_b32 v11, v26\n\t"
    "v_max_f32 v11, v11, v26\n\t"
    "v_sub_f32 v28, v40, v11\n\t"
    "v_sub_f32 v29, v41, v11\n\t"
    "v_sub_f32 v30, v42, v11\n\t"
    "v_sub_f32 v31, v43, v11\n\t"
    "v_sub_f32 v11, v10, v11\n\t"
    "v_exp_f32 v11, v11\n\t"
    "v_exp_f32 v28, v28\n\t"
    "v_exp_f32 v29, v29\n\t"
    "v_exp_f32 v30, v30\n\t"
    "v_exp_f32 v31, v31\n\t"
    "v_mul_f32 v44, v44, v11\n\t"
    "v_mul_f32 v45, v45, v11\n\t"
    "v_mul_f32 v46, v46, v11\n\t"
    "v_mul_f32 v47, v47, v11\n\t"
    "v_mul_f32 v48, v48, v11\n\t"
    "v_mul_f32 v49, v49, v11\n\t"
    "v_mul_f32 v50, v50, v11\n\t"
    "v_mul_f32 v51, v51, v11\n\t"
    "v_mul_f32 v52, v52, v11\n\t"
    "v_mul_f32 v53, v53, v11\n\t"
    "v_mul_f32 v54, v54, v11\n\t"
    "v_mul_f32 v55, v55, v11\n\t"
    "s_branch 11b\n\t"
    "12:\n\t"
    "v_max3_f32 v10, v24, v25, v26\n\t"
    "v_max3_f32 v40, v27, v28, v29\n\t"
    "v_max3_f32 v10, v10, v30, v31\n\t"
    "v_max3_f32 v10, v10, v40, %[neg]\n\t"
    "v_mov_b32 v40, v10\n\t"
    "s_nop 1\n\t"
    "v_permlane16_swap_b32 v10, v40\n\t"
    "v_max_f32 v10, v10, v40\n\t"
    "v_mov_b32 v40, v10\n\t"
    "s_nop 1\n\t"
    "v_permlane32_swap_b32 v10, v40\n\t"
    "v_max_f32 v10, v10, v40\n\t"
    "v_sub_f32 v24, v24, v10\n\t"
    "v_sub_f32 v25, v25, v10\n\t"
    "v_sub_f32 v26, v26, v10\n\t"
    "v_sub_f32 v27, v27, v10\n\t"
    "v_sub_f32 v28, v28, v10\n\t"
    "v_sub_f32 v29, v29, v10\n\t"
    "v_sub_f32 v30, v30, v10\n\t"
    "v_sub_f32 v31, v31, v10\n\t"
    "s_mov_b32 %[bg], 0x7fc00000\n\t"
    "s_branch 13b\n\t"
    "9:"
    : [tc] "=&s"(tc), [sp] "=&s"(sp), [cap] "+s"(cap), [st] "=&s"(st), [bg] "=&s"(bg)
    : [ag] "s"(ag), [c44] "s"(0x4444u), [kv] "s"(kv), [vs] "s"(vs), [od] "s"(od), [od2] "s"(od + 262144), [neg] "s"(-1.0e38f), [lsc] "s"(LSC), [one] "s"(0x3F803F80u), [big] "s"(24.0f), "{v[64:95]}"(W.w0), "{v[96:127]}"(W.w1), "{v[128:159]}"(W.w2), "{v[160:191]}"(W.w3), "{v[192:223]}"(W.w4), "{v[224:255]}"(W.w5)
    : "v6", "v7", "v8", "v9", "v10", "v11", "v12", "v13", "v14", "v15", "v16", "v17", "v18", "v19", "v20", "v21", "v22", "v23", "v24", "v25", "v26", "v27", "v28", "v29", "v30", "v31", "v32", "v33", "v34", "v35", "v36", "v37", "v38", "v39", "v40", "v41", "v42", "v43", "v44", "v45", "v46", "v47", "v48", "v49", "v50", "v51", "v52", "v53", "v54", "v55", "v56", "v57", "v58", "v59", "v60", "v61", "v62", "v63", "vcc", "scc", "memory");
}
template <> __device__ __forceinline__ void qk_step<3>(const QKW& W, const char* ag, const char* vs, const char* od, const char* kv, unsigned& cap, unsigned& sp,
                                                        unsigned long long& tc, unsigned long long* ts, unsigned& d0, unsigned& d1) {
  unsigned st, bg;
  asm volatile("v_mbcnt_lo_u32_b32 v60, -1, 0\n\t"
    "v_mbcnt_hi_u32_b32 v60, -1, v60\n\t"
    "v_lshlrev_b32 v6, 4, v60\n\t"
    "s_mov_b32 %[sp], 0\n\t"
    "s_setprio 3\n\t"
    "global_load_dwordx4 v[40:43], v6, %[ag] sc1\n\t"
    "global_load_dwordx4 v[44:47], v6, %[ag] offset:1024 sc1\n\t"
    "s_memtime %[tc]\n\t"
    "v_add_u32 v7, 47104, v6\n\t"
    "v_add_u32 v8, 104448, v6\n\t"
    "ds_read_b128 v[24:27], v7 offset:0\n\t"
    "ds_read_b128 v[28:31], v7 offset:1024\n\t"
    "ds_read_b128 v[32:35], v7 offset:8192\n\t"
    "ds_read_b128 v[36:39], v7 offset:9216\n\t"
    "v_lshrrev_b32 v48, 4, v60\n\t"
    "v_and_b32 v49, 15, v60\n\t"
    "v_and_b32 v61, 4, v60\n\t"
    "v_mul_u32_u24 v61, 0x2aaa, v61\n\t"
    "v_lshrrev_b32 v9, 1, v60\n\t"
    "v_and_b32 v9, 2, v9\n\t"
    "v_or3_b32 v9, v9, v61, %[c44]\n\t"
    "v_add_u32 v14, 1536, v6\n\t"
    "v_lshlrev_b32 v61, 2, v48\n\t"
    "v_lshl_or_b32 v10, v49, 4, v61\n\t"
    "v_lshrrev_b32 v50, 2, v49\n\t"
    "v_and_b32 v51, 3, v60\n\t"
    "v_and_b32 v61, 1, v60\n\t"
    "v_lshlrev_b32 v61, 3, v61\n\t"
    "v_lshl_or_b32 v11, v50, 4, v61\n\t"
    "v_lshl_or_b32 v11, v48, 7, v11\n\t"
    "v_xor_b32 v12, v48, v49\n\t"
    "v_lshlrev_b32 v12, 4, v12\n\t"
    "v_lshl_or_b32 v12, v49, 8, v12\n\t"
    "v_lshl_or_b32 v50, v48, 2, v50\n\t"
    "v_or_b32 v51, 12, v51\n\t"
    "v_xor_b32 v51, v51, v50\n\t"
    "v_lshlrev_b32 v13, 4, v51\n\t"
    "v_lshl_or_b32 v13, v50, 8, v13\n\t"
    "v_and_b32 v61, 3, v60\n\t"
    "v_lshlrev_b32 v61, 4, v61\n\t"
    "v_lshl_or_b32 v15, v48, 6, v61\n\t"
    "v_and_b32 v61, 8, v60\n\t"
    "v_lshl_or_b32 v15, v61, 14, v15\n\t"
    "v_and_b32 v61, 4, v60\n\t"
    "v_lshl_or_b32 v15, v61, 8, v15\n\t"
    "v_add_u32 v15, 768, v15\n\t"
    "3:\n\t"
    "v_mov_b32 v16, 0\n\t"
    "v_mov_b32 v17, 0\n\t"
    "v_mov_b32 v18, 0\n\t"
    "v_mov_b32 v19, 0\n\t"
    "v_mov_b32 v20, 0\n\t"
    "v_mov_b32 v21, 0\n\t"
    "v_mov_b32 v22, 0\n\t"
    "v_mov_b32 v23, 0\n\t"
    "s_waitcnt vmcnt(0) lgkmcnt(0)\n\t"
    "14:\n\t"
    "ds_write_b128 v6, v[40:43] offset:6144\n\t"
    "ds_write_b128 v6, v[44:47] offset:7168\n\t"
    "v_smfmac_f32_16x16x64_bf16 v[16:19], v[40:43], v[160:167], v9\n\t"
    "v_max3_u32 v60, v40, v41, v42\n\t"
    "v_max3_u32 v61, v43, v44, v45\n\t"
    "v_smfmac_f32_16x16x64_bf16 v[20:23], v[40:43], v[168:175], v9\n\t"
    "v_max3_u32 v60, v60, v46, v47\n\t"
    "v_max_u32 v60, v60, v61\n\t"
    "v_cmp_eq_u32 vcc, -1, v60\n\t"
    "s_cbranch_vccnz 1f\n\t"
    "2:\n\t"
    "s_waitcnt lgkmcnt(0)\n\t"
    "s_cmp_eq_u64 %[kv], 0\n\t"
    "s_barrier\n\t"
    "s_cbranch_scc1 20f\n\t"
    "ds_read_b128 v[40:43], v6 offset:12288\n\t"
    "ds_read_b128 v[48:51], v6 offset:0\n\t"
    "v_smfmac_f32_16x16x64_bf16 v[16:19], v[44:47], v[176:183], v9\n\t"
    "ds_read_b128 v[52:55], v6 offset:1024\n\t"
    "ds_read_b128 v[56:59], v6 offset:2048\n\t"
    "v_smfmac_f32_16x16x64_bf16 v[20:23], v[44:47], v[184:191], v9\n\t"
    "ds_read_b128 v[60:63], v6 offset:13312\n\t"
    "ds_read_b128 v[44:47], v6 offset:3072\n\t"
    "s_waitcnt lgkmcnt(5)\n\t"
    "v_smfmac_f32_16x16x64_bf16 v[16:19], v[40:43], v[24:31], v9\n\t"
    "ds_read_b128 v[24:27], v7 offset:16384\n\t"
    "ds_read_b128 v[28:31], v7 offset:17408\n\t"
    "v_smfmac_f32_16x16x64_bf16 v[20:23], v[40:43], v[32:39], v9\n\t"
    "ds_read_b128 v[32:35], v7 offset:24576\n\t"
    "ds_read_b128 v[36:39], v7 offset:25600\n\t"
    "ds_read_b128 v[40:43], v6 offset:4096\n\t"
    "s_waitcnt lgkmcnt(9)\n\t"
    "v_smfmac_f32_16x16x64_bf16 v[16:19], v[48:51], v[64:71], v9\n\t"
    "v_smfmac_f32_16x16x64_bf16 v[20:23], v[48:51], v[72:79], v9\n\t"
    "ds_read_b128 v[48:51], v6 offset:5120\n\t"
    "s_waitcnt lgkmcnt(9)\n\t"
    "v_smfmac_f32_16x16x64_bf16 v[16:19], v[52:55], v[80:87], v9\n\t"
    "v_smfmac_f32_16x16x64_bf16 v[20:23], v[52:55], v[88:95], v9\n\t"
    "ds_read_b128 v[52:55], v6 offset:14336\n\t"
    "s_waitcnt lgkmcnt(9)\n\t"
    "v_smfmac_f32_16x16x64_bf16 v[16:19], v[56:59], v[96:103], v9\n\t"
    "v_smfmac_f32_16x16x64_bf16 v[20:23], v[56:59], v[104:111], v9\n\t"
    "ds_read_b128 v[56:59], v6 offset:8192\n\t"
    "s_waitcnt lgkmcnt(6)\n\t"
    "v_smfmac_f32_16x16x64_bf16 v[16:19], v[60:63], v[24:31], v9\n\t"
    "ds_read_b128 v[24:27], v8 offset:0\n\t"
    "ds_read_b128 v[28:31], v8 offset:1024\n\t"
    "s_waitcnt lgkmcnt(6)\n\t"
    "v_smfmac_f32_16x16x64_bf16 v[20:23], v[60:63], v[32:39], v9\n\t"
    "ds_read_b128 v[32:35], v8 offset:8192\n\t"
    "ds_read_b128 v[36:39], v8 offset:9216\n\t"
    "ds_read_b128 v[60:63], v6 offset:9216\n\t"
    "v_smfmac_f32_16x16x64_bf16 v[16:19], v[44:47], v[112:119], v9\n\t"
    "v_smfmac_f32_16x16x64_bf16 v[20:23], v[44:47], v[120:127], v9\n\t"
    "ds_read_b128 v[44:47], v6 offset:10240\n\t"
    "s_waitcnt lgkmcnt(9)\n\t"
    "v_smfmac_f32_16x16x64_bf16 v[16:19], v[40:43], v[128:135], v9\n\t"
    "v_smfmac_f32_16x16x64_bf16 v[20:23], v[40:43], v[136:143], v9\n\t"
    "ds_read_b128 v[40:43], v6 offset:15360\n\t"
    "s_waitcnt lgkmcnt(9)\n\t"
    "v_smfmac_f32_16x16x64_bf16 v[16:19], v[48:51], v[144:151], v9\n\t"
    "v_smfmac_f32_16x16x64_bf16 v[20:23], v[48:51], v[152:159], v9\n\t"
    "ds_read_b128 v[48:51], v6 offset:11264\n\t"
    "s_waitcnt lgkmcnt(6)\n\t"
    "v_smfmac_f32_16x16x64_bf16 v[16:19], v[52:55], v[24:31], v9\n\t"
    "ds_read_b128 v[24:27], v8 offset:16384\n\t"
    "ds_read_b128 v[28:31], v8 offset:17408\n\t"
    "s_waitcnt lgkmcnt(6)\n\t"
    "v_smfmac_f32_16x16x64_bf16 v[20:23], v[52:55], v[32:39], v9\n\t"
    "ds_read_b128 v[32:35], v8 offset:24576\n\t"
    "ds_read_b128 v[36:39], v8 offset:25600\n\t"
    "v_smfmac_f32_16x16x64_bf16 v[16:19], v[56:59], v[192:199], v9\n\t"
    "v_smfmac_f32_16x16x64_bf16 v[20:23], v[56:59], v[200:207], v9\n\t"
    "s_waitcnt lgkmcnt(7)\n\t"
    "v_smfmac_f32_16x16x64_bf16 v[16:19], v[60:63], v[208:215], v9\n\t"
    "v_smfmac_f32_16x16x64_bf16 v[20:23], v[60:63], v[216:223], v9\n\t"
    "s_waitcnt lgkmcnt(6)\n\t"
    "v_smfmac_f32_16x16x64_bf16 v[16:19], v[44:47], v[224:231], v9\n\t"
    "v_smfmac_f32_16x16x64_bf16 v[20:23], v[44:47], v[232:239], v9\n\t"
    "s_waitcnt lgkmcnt(2)\n\t"
    "v_smfmac_f32_16x16x64_bf16 v[16:19], v[40:43], v[24:31], v9\n\t"
    "s_waitcnt lgkmcnt(0)\n\t"
    "v_smfmac_f32_16x16x64_bf16 v[20:23], v[40:43], v[32:39], v9\n\t"
    "v_smfmac_f32_16x16x64_bf16 v[16:19], v[48:51], v[240:247], v9\n\t"
    "v_smfmac_f32_16x16x64_bf16 v[20:23], v[48:51], v[248:255], v9\n\t"
    "v_xor_b32 v7, 64, v12\n\t"
    "v_xor_b32 v8, 0x80, v12\n\t"
    "v_xor_b32 v6, 0xc0, v12\n\t"
    "ds_read_b128 v[24:27], v12 offset:16384\n\t"
    "ds_read_b128 v[28:31], v12 offset:20480\n\t"
    "ds_read_b128 v[32:35], v7 offset:16384\n\t"
    "ds_read_b128 v[36:39], v7 offset:20480\n\t"
    "v_permlane16_swap_b32 v16, v18\n\t"
    "v_permlane16_swap_b32 v17, v19\n\t"
    "v_permlane16_swap_b32 v20, v22\n\t"
    "v_permlane16_swap_b32 v21, v23\n\t"
    "v_add_f32 v16, v16, v18\n\t"
    "v_add_f32 v17, v17, v19\n\t"
    "v_add_f32 v20, v20, v22\n\t"
    "v_mul_f32 v16, %[lsc], v16\n\t"
    "v_mul_f32 v17, %[lsc], v17\n\t"
    "v_cvt_pk_bf16_f32 v16, v16, v17\n\t"
    "v_add_f32 v21, v21, v23\n\t"
    "ds_write_b32 v10, v16 offset:34304\n\t"
    "v_mul_f32 v20, %[lsc], v20\n\t"
    "v_mul_f32 v21, %[lsc], v21\n\t"
    "v_cvt_pk_bf16_f32 v20, v20, v21\n\t"
    "ds_write_b32 v10, v20 offset:34560\n\t"
    "ds_read_b128 v[40:43], v8 offset:16384\n\t"
    "ds_read_b128 v[44:47], v8 offset:20480\n\t"
    "ds_read_b128 v[48:51], v6 offset:16384\n\t"
    "ds_read_b128 v[52:55], v6 offset:20480\n\t"
    "s_waitcnt lgkmcnt(4)\n\t"
    "s_barrier\n\t"
    "ds_read_b64_tr_b16 v[16:17], v11 offset:32768\n\t"
    "ds_read_b64_tr_b16 v[18:19], v11 offset:32832\n\t"
    "ds_read_b64_tr_b16 v[20:21], v11 offset:33280\n\t"
    "ds_read_b64_tr_b16 v[22:23], v11 offset:33344\n\t"
    "ds_read_b64_tr_b16 v[56:57], v11 offset:33792\n\t"
    "ds_read_b64_tr_b16 v[58:59], v11 offset:33856\n\t"
    "ds_read_b64_tr_b16 v[60:61], v11 offset:34304\n\t"
    "ds_read_b64_tr_b16 v[62:63], v11 offset:34368\n\t"
    "v_mov_b32 v6, 0\n\t"
    "v_mov_b32 v7, 0\n\t"
    "v_mov_b32 v8, 0\n\t"
    "v_mov_b32 v9, 0\n\t"
    "s_mov_b32 exec_hi, 0\n\t"
    "global_load_dwordx4 v[6:9], v14, %[vs] sc1\n\t"
    "s_mov_b32 exec_hi, -1\n\t"
    "s_waitcnt lgkmcnt(6)\n\t"
    "v_mfma_f32_16x16x32_bf16 v[24:27], v[24:27], v[16:19], 0\n\t"
    "v_mfma_f32_16x16x32_bf16 v[28:31], v[28:31], v[16:19], 0\n\t"
    "s_waitcnt lgkmcnt(4)\n\t"
    "v_mfma_f32_16x16x32_bf16 v[24:27], v[32:35], v[20:23], v[24:27]\n\t"
    "v_mfma_f32_16x16x32_bf16 v[28:31], v[36:39], v[20:23], v[28:31]\n\t"
    "ds_read_b64_tr_b16 v[32:33], v13 offset:24576\n\t"
    "ds_read_b64_tr_b16 v[34:35], v13 offset:28672\n\t"
    "ds_read_b64_tr_b16 v[36:37], v13 offset:24584\n\t"
    "ds_read_b64_tr_b16 v[38:39], v13 offset:28680\n\t"
    "s_waitcnt lgkmcnt(6)\n\t"
    "v_mfma_f32_16x16x32_bf16 v[24:27], v[40:43], v[56:59], v[24:27]\n\t"
    "v_mfma_f32_16x16x32_bf16 v[28:31], v[44:47], v[56:59], v[28:31]\n\t"
    "s_waitcnt lgkmcnt(4)\n\t"
    "v_mfma_f32_16x16x32_bf16 v[24:27], v[48:51], v[60:63], v[24:27]\n\t"
    "v_mfma_f32_16x16x32_bf16 v[28:31], v[52:55], v[60:63], v[28:31]\n\t"
    "s_setprio 1\n\t"
    "v_mov_b32 v52, %[one]\n\t"
    "v_mov_b32 v53, %[one]\n\t"
    "v_mov_b32 v54, %[one]\n\t"
    "v_mov_b32 v55, %[one]\n\t"
    "s_nop 2\n\t"
    "v_max3_f32 v10, |v24|, |v25|, |v26|\n\t"
    "v_max3_f32 v40, |v27|, |v28|, |v29|\n\t"
    "v_max3_f32 v10, v10, |v30|, |v31|\n\t"
    "v_max_f32 v10, v10, v40\n\t"
    "v_cmp_nge_f32 vcc, %[big], v10\n\t"
    "s_mov_b32 %[bg], %[big]\n\t"
    "v_mov_b32 v10, 0\n\t"
    "s_cbranch_vccnz 12f\n\t"
    "13:\n\t"
    "v_exp_f32 v24, v24\n\t"
    "v_exp_f32 v25, v25\n\t"
    "v_exp_f32 v26, v26\n\t"
    "v_exp_f32 v27, v27\n\t"
    "v_exp_f32 v28, v28\n\t"
    "v_exp_f32 v29, v29\n\t"
    "v_exp_f32 v30, v30\n\t"
    "v_exp_f32 v31, v31\n\t"
    "v_cvt_pk_bf16_f32 v40, v24, v25\n\t"
    "v_cvt_pk_bf16_f32 v41, v26, v27\n\t"
    "v_cvt_pk_bf16_f32 v42, v28, v29\n\t"
    "v_cvt_pk_bf16_f32 v43, v30, v31\n\t"
    "s_setprio 3\n\t"
    "s_waitcnt lgkmcnt(0)\n\t"
    "s_nop 0\n\t"
    "v_mfma_f32_16x16x32_bf16 v[44:47], v[32:35], v[40:43], 0\n\t"
    "v_mfma_f32_16x16x32_bf16 v[48:51], v[36:39], v[40:43], 0\n\t"
    "v_mfma_f32_16x16x32_bf16 v[52:55], v[52:55], v[40:43], 0\n\t"
    "v_mov_b32 v12, 0\n\t"
    "v_mov_b32 v13, 0\n\t"
    "s_mov_b32 exec_hi, 0\n\t"
    "v_mov_b32 v12, %[one]\n\t"
    "v_mov_b32 v13, %[one]\n\t"
    "s_mov_b32 exec_hi, -1\n\t"
    "s_barrier\n\t"
    "ds_read_b64_tr_b16 v[24:25], v11 offset:34816\n\t"
    "ds_read_b64_tr_b16 v[26:27], v11 offset:34880\n\t"
    "ds_read_b64_tr_b16 v[28:29], v11 offset:35328\n\t"
    "ds_read_b64_tr_b16 v[30:31], v11 offset:35392\n\t"
    "ds_read_b64_tr_b16 v[32:33], v11 offset:35840\n\t"
    "ds_read_b64_tr_b16 v[34:35], v11 offset:35904\n\t"
    "ds_read_b64_tr_b16 v[36:37], v11 offset:36352\n\t"
    "ds_read_b64_tr_b16 v[38:39], v11 offset:36416\n\t"
    "s_waitcnt lgkmcnt(6)\n\t"
    "v_mfma_f32_16x16x32_bf16 v[40:43], v[24:27], v[16:19], 0\n\t"
    "s_waitcnt lgkmcnt(4)\n\t"
    "v_mfma_f32_16x16x32_bf16 v[40:43], v[28:31], v[20:23], v[40:43]\n\t"
    "s_waitcnt lgkmcnt(2)\n\t"
    "v_mfma_f32_16x16x32_bf16 v[40:43], v[32:35], v[56:59], v[40:43]\n\t"
    "s_waitcnt lgkmcnt(0)\n\t"
    "v_mfma_f32_16x16x32_bf16 v[40:43], v[36:39], v[60:63], v[40:43]\n\t"
    "5:\n\t"
    "s_waitcnt vmcnt(0)\n\t"
    "v_max3_u32 v11, v6, v7, v8\n\t"
    "v_max_u32 v11, v11, v9\n\t"
    "v_cmp_eq_u32 vcc, -1, v11\n\t"
    "s_cbranch_vccnz 4f\n\t"
    "6:\n\t"
    "s_nop 2\n\t"
    "v_exp_f32 v28, v40\n\t"
    "v_max3_f32 v11, v40, v41, v42\n\t"
    "v_exp_f32 v29, v41\n\t"
    "v_max_f32 v11, v11, v43\n\t"
    "v_exp_f32 v30, v42\n\t"
    "v_cmp_nge_f32 vcc, %[bg], v11\n\t"
    "v_exp_f32 v31, v43\n\t"
    "s_cbranch_vccnz 10f\n\t"
    "11:\n\t"
    "v_cvt_pk_bf16_f32 v24, v28, v29\n\t"
    "v_cvt_pk_bf16_f32 v25, v30, v31\n\t"
    "s_nop 1\n\t"
    "v_mfma_f32_16x16x16_bf16 v[52:55], v[12:13], v[24:25], v[52:55]\n\t"
    "v_mfma_f32_16x16x16_bf16 v[44:47], v[6:7], v[24:25], v[44:47]\n\t"
    "v_mfma_f32_16x16x16_bf16 v[48:51], v[8:9], v[24:25], v[48:51]\n\t"
    "s_nop 5\n\t"
    "v_rcp_f32 v26, v52\n\t"
    "s_nop 0\n\t"
    "v_pk_mul_f32 v[44:45], v[44:45], v[26:27] op_sel_hi:[1,0]\n\t"
    "v_pk_mul_f32 v[46:47], v[46:47], v[26:27] op_sel_hi:[1,0]\n\t"
    "v_pk_mul_f32 v[48:49], v[48:49], v[26:27] op_sel_hi:[1,0]\n\t"
    "v_pk_mul_f32 v[50:51], v[50:51], v[26:27] op_sel_hi:[1,0]\n\t"
    "v_cvt_pk_bf16_f32 v36, v44, v45\n\t"
    "v_cvt_pk_bf16_f32 v37, v46, v47\n\t"
    "v_cvt_pk_bf16_f32 v38, v48, v49\n\t"
    "v_cvt_pk_bf16_f32 v39, v50, v51\n\t"
    "v_min_u32 v36, -2, v36\n\t"
    "v_min_u32 v37, -2, v37\n\t"
    "v_min_u32 v38, -2, v38\n\t"
    "v_min_u32 v39, -2, v39\n\t"
    "global_store_dwordx4 v15, v[36:39], %[od] sc1\n\t"
    "s_barrier\n\t"
    "s_mov_b32 exec_hi, 0\n\t"
    "global_store_dwordx4 v14, v[6:9], %[kv] sc1\n\t"
    "s_mov_b32 exec_hi, -1\n\t"
    "s_nop 0\n\t"
    "s_waitcnt lgkmcnt(0)\n\t"
    "s_branch 9f\n\t"
    "1:\n\t"
    "s_add_u32 %[sp], %[sp], 1\n\t"
    "s_cmp_ge_u32 %[sp], %[cap]\n\t"
    "s_cbranch_scc1 7f\n\t"
    "global_load_dwordx4 v[40:43], v6, %[ag] sc1\n\t"
    "global_load_dwordx4 v[44:47], v6, %[ag] offset:1024 sc1\n\t"
    "s_waitcnt lgkmcnt(0)\n\t"
    "s_memtime %[tc]\n\t"
    "s_nop 7\n\t"
    "s_branch 3b\n\t"
    "7:\n\t"
    "s_mov_b32 %[cap], 0\n\t"
    "s_branch 2b\n\t"
    "20:\n\t"
    "s_barrier\n\t"
    "s_barrier\n\t"
    "s_barrier\n\t"
    "s_branch 9f\n\t"
    "4:\n\t"
    "s_add_u32 %[sp], %[sp], 0x10000\n\t"
    "s_lshr_b32 %[st], %[sp], 16\n\t"
    "s_cmp_ge_u32 %[st], %[cap]\n\t"
    "s_cbranch_scc1 8f\n\t"
    "s_mov_b32 exec_hi, 0\n\t"
    "global_load_dwordx4 v[6:9], v14, %[vs] sc1\n\t"
    "s_mov_b32 exec_hi, -1\n\t"
    "s_branch 5b\n\t"
    "8:\n\t"
    "s_mov_b32 %[cap], 0\n\t"
    "s_branch 6b\n\t"
    "10:\n\t"
    "v_max_f32 v11, v11, v10\n\t"
    "v_mov_b32 v26, v11\n\t"
    "s_nop 1\n\t"
    "v_permlane16_swap_b32 v11, v26\n\t"
    "v_max_f32 v11, v11, v26\n\t"
    "v_sub_f32 v28, v40, v11\n\t"
    "v_sub_f32 v29, v41, v11\n\t"
    "v_sub_f32 v30, v42, v11\n\t"
    "v_sub_f32 v31, v43, v11\n\t"
    "v_sub_f32 v11, v10, v11\n\t"
    "v_exp_f32 v11, v11\n\t"
    "v_exp_f32 v28, v28\n\t"
    "v_exp_f32 v29, v29\n\t"
    "v_exp_f32 v30, v30\n\t"
    "v_exp_f32 v31, v31\n\t"
    "v_mul_f32 v44, v44, v11\n\t"
    "v_mul_f32 v45, v45, v11\n\t"
    "v_mul_f32 v46, v46, v11\n\t"
    "v_mul_f32 v47, v47, v11\n\t"
    "v_mul_f32 v48, v48, v11\n\t"
    "v_mul_f32 v49, v49, v11\n\t"
    "v_mul_f32 v50, v50, v11\n\t"
    "v_mul_f32 v51, v51, v11\n\t"
    "v_mul_f32 v52, v52, v11\n\t"
    "v_mul_f32 v53, v53, v11\n\t"
    "v_mul_f32 v54, v54, v11\n\t"
    "v_mul_f32 v55, v55, v11\n\t"
    "s_branch 11b\n\t"
    "12:\n\t"
    "v_max3_f32 v10, v24, v25, v26\n\t"
    "v_max3_f32 v40, v27, v28, v29\n\t"
    "v_max3_f32 v10, v10, v30, v31\n\t"
    "v_max3_f32 v10, v10, v40, %[neg]\n\t"
    "v_mov_b32 v40, v10\n\t"
    "s_nop 1\n\t"
    "v_permlane16_swap_b32 v10, v40\n\t"
    "v_max_f32 v10, v10, v40\n\t"
    "v_mov_b32 v40, v10\n\t"
    "s_nop 1\n\t"
    "v_permlane32_swap_b32 v10, v40\n\t"
    "v_max_f32 v10, v10, v40\n\t"
    "v_sub_f32 v24, v24, v10\n\t"
    "v_sub_f32 v25, v25, v10\n\t"
    "v_sub_f32 v26, v26, v10\n\t"
    "v_sub_f32 v27, v27, v10\n\t"
    "v_sub_f32 v28, v28, v10\n\t"
    "v_sub_f32 v29, v29, v10\n\t"
    "v_sub_f32 v30, v30, v10\n\t"
    "v_sub_f32 v31, v31, v10\n\t"
    "s_mov_b32 %[bg], 0x7fc00000\n\t"
    "s_branch 13b\n\t"
    "9:"
    : [tc] "=&s"(tc), [sp] "=&s"(sp), [cap] "+s"(cap), [st] "=&s"(st), [bg] "=&s"(bg)
    : [ag] "s"(ag), [c44] "s"(0x4444u), [kv] "s"(kv), [vs] "s"(vs), [od] "s"(od), [od2] "s"(od + 262144), [neg] "s"(-1.0e38f), [lsc] "s"(LSC), [one] "s"(0x3F803F80u), [big] "s"(24.0f), "{v[64:95]}"(W.w0), "{v[96:127]}"(W.w1), "{v[128:159]}"(W.w2), "{v[160:191]}"(W.w3), "{v[192:223]}"(W.w4), "{v[224:255]}"(W.w5)
    : "v6", "v7", "v8", "v9", "v10", "v11", "v12", "v13", "v14", "v15", "v16", "v17", "v18", "v19", "v20", "v21", "v22", "v23", "v24", "v25", "v26", "v27", "v28", "v29", "v30", "v31", "v32", "v33", "v34", "v35", "v36", "v37", "v38", "v39", "v40", "v41", "v42", "v43", "v44", "v45", "v46", "v47", "v48", "v49", "v50", "v51", "v52", "v53", "v54", "v55", "v56", "v57", "v58", "v59", "v60", "v61", "v62", "v63", "vcc", "scc", "memory");
}
template <> __device__ __forceinline__ void qk_step<4>(const QKW& W, const char* ag, const char* vs, const char* od, const char* kv, unsigned& cap, unsigned& sp,
                                                        unsigned long long& tc, unsigned long long* ts, unsigned& d0, unsigned& d1) {
  unsigned st, bg;
  asm volatile("v_mbcnt_lo_u32_b32 v52, -1, 0\n\t"
    "v_mbcnt_hi_u32_b32 v52, -1, v52\n\t"
    "v_lshlrev_b32 v6, 4, v52\n\t"
    "s_mov_b32 %[sp], 0\n\t"
    "global_load_dwordx4 v[36:39], v6, %[ag] sc1\n\t"
    "global_load_dwordx4 v[40:43], v6, %[ag] offset:1024 sc1\n\t"
    "s_memtime %[tc]\n\t"
    "v_add_u32 v7, 73728, v6\n\t"
    "v_add_u32 v8, 131072, v6\n\t"
    "ds_read_b128 v[20:23], v7 offset:8192\n\t"
    "ds_read_b128 v[24:27], v7 offset:9216\n\t"
    "ds_read_b128 v[28:31], v7 offset:0\n\t"
    "ds_read_b128 v[32:35], v7 offset:1024\n\t"
    "v_lshrrev_b32 v44, 4, v52\n\t"
    "v_and_b32 v45, 15, v52\n\t"
    "v_and_b32 v53, 4, v52\n\t"
    "v_mul_u32_u24 v53, 0x2aaa, v53\n\t"
    "v_lshrrev_b32 v9, 1, v52\n\t"
    "v_and_b32 v9, 2, v9\n\t"
    "v_or3_b32 v9, v9, v53, %[c44]\n\t"
    "v_lshlrev_b32 v53, 2, v44\n\t"
    "v_lshl_or_b32 v10, v45, 4, v53\n\t"
    "3:\n\t"
    "v_mov_b32 v12, 0\n\t"
    "v_mov_b32 v13, 0\n\t"
    "v_mov_b32 v14, 0\n\t"
    "v_mov_b32 v15, 0\n\t"
    "v_mov_b32 v16, 0\n\t"
    "v_mov_b32 v17, 0\n\t"
    "v_mov_b32 v18, 0\n\t"
    "v_mov_b32 v19, 0\n\t"
    "s_waitcnt vmcnt(0) lgkmcnt(0)\n\t"
    "14:\n\t"
    "ds_write_b128 v6, v[36:39] offset:8192\n\t"
    "ds_write_b128 v6, v[40:43] offset:9216\n\t"
    "v_smfmac_f32_16x16x64_bf16 v[12:15], v[36:39], v[128:135], v9\n\t"
    "v_max3_u32 v52, v36, v37, v38\n\t"
    "v_max3_u32 v53, v39, v40, v41\n\t"
    "v_smfmac_f32_16x16x64_bf16 v[16:19], v[36:39], v[136:143], v9\n\t"
    "v_max3_u32 v52, v52, v42, v43\n\t"
    "v_max_u32 v52, v52, v53\n\t"
    "v_cmp_eq_u32 vcc, -1, v52\n\t"
    "s_cbranch_vccnz 1f\n\t"
    "2:\n\t"
    "s_waitcnt lgkmcnt(0)\n\t"
    "s_barrier\n\t"
    "s_memrealtime s[36:37]\n\t"
    "ds_read_b128 v[36:39], v6 offset:0\n\t"
    "ds_read_b128 v[44:47], v6 offset:4096\n\t"
    "v_smfmac_f32_16x16x64_bf16 v[12:15], v[40:43], v[144:151], v9\n\t"
    "ds_read_b128 v[48:51], v6 offset:5120\n\t"
    "ds_read_b128 v[52:55], v6 offset:6144\n\t"
    "v_smfmac_f32_16x16x64_bf16 v[16:19], v[40:43], v[152:159], v9\n\t"
    "ds_read_b128 v[40:43], v6 offset:1024\n\t"
    "s_waitcnt lgkmcnt(0)\n\t"
    "s_mov_b32 s38, s36\n\t"
    "s_min_u32 s37, %[d0], %[d1]\n\t"
    "s_add_u32 s36, s36, s37\n\t"
    "s_and_b32 s36, s36, 0xffffff\n\t"
    "s_or_b32 s36, s36, %[sn]\n\t"
    "v_lshlrev_b32 v11, 10, v6\n\t"
    "v_mov_b32 v5, s36\n\t"
    "s_mov_b64 exec, 15\n\t"
    "global_store_dword v11, v5, %[vs] offset:-4096 sc1\n\t"
    "s_mov_b64 exec, -1\n\t"
    "s_sleep 0\n\t"
    "v_smfmac_f32_16x16x64_bf16 v[12:15], v[36:39], v[56:63], v9\n\t"
    "v_smfmac_f32_16x16x64_bf16 v[16:19], v[36:39], v[28:35], v9\n\t"
    "ds_read_b128 v[28:31], v7 offset:16384\n\t"
    "ds_read_b128 v[32:35], v7 offset:17408\n\t"
    "ds_read_b128 v[36:39], v6 offset:7168\n\t"
    "v_smfmac_f32_16x16x64_bf16 v[12:15], v[44:47], v[64:71], v9\n\t"
    "v_smfmac_f32_16x16x64_bf16 v[16:19], v[44:47], v[72:79], v9\n\t"
    "ds_read_b128 v[44:47], v6 offset:10240\n\t"
    "v_smfmac_f32_16x16x64_bf16 v[12:15], v[48:51], v[80:87], v9\n\t"
    "v_smfmac_f32_16x16x64_bf16 v[16:19], v[48:51], v[88:95], v9\n\t"
    "ds_read_b128 v[48:51], v6 offset:11264\n\t"
    "v_smfmac_f32_16x16x64_bf16 v[12:15], v[52:55], v[96:103], v9\n\t"
    "v_smfmac_f32_16x16x64_bf16 v[16:19], v[52:55], v[104:111], v9\n\t"
    "ds_read_b128 v[52:55], v6 offset:2048\n\t"
    "v_smfmac_f32_16x16x64_bf16 v[12:15], v[40:43], v[20:27], v9\n\t"
    "ds_read_b128 v[20:23], v8 offset:0\n\t"
    "ds_read_b128 v[24:27], v8 offset:1024\n\t"
    "s_waitcnt lgkmcnt(6)\n\t"
    "v_smfmac_f32_16x16x64_bf16 v[16:19], v[40:43], v[28:35], v9\n\t"
    "ds_read_b128 v[28:31], v8 offset:8192\n\t"
    "ds_read_b128 v[32:35], v8 offset:9216\n\t"
    "ds_read_b128 v[40:43], v6 offset:12288\n\t"
    "s_waitcnt lgkmcnt(8)\n\t"
    "v_smfmac_f32_16x16x64_bf16 v[12:15], v[36:39], v[112:119], v9\n\t"
    "v_smfmac_f32_16x16x64_bf16 v[16:19], v[36:39], v[120:127], v9\n\t"
    "ds_read_b128 v[36:39], v6 offset:13312\n\t"
    "s_waitcnt lgkmcnt(8)\n\t"
    "v_smfmac_f32_16x16x64_bf16 v[12:15], v[44:47], v[160:167], v9\n\t"
    "v_smfmac_f32_16x16x64_bf16 v[16:19], v[44:47], v[168:175], v9\n\t"
    "ds_read_b128 v[44:47], v6 offset:14336\n\t"
    "s_barrier\n\t"
    "s_setprio 2\n\t"
    "s_waitcnt lgkmcnt(8)\n\t"
    "v_smfmac_f32_16x16x64_bf16 v[12:15], v[48:51], v[176:183], v9\n\t"
    "v_smfmac_f32_16x16x64_bf16 v[16:19], v[48:51], v[184:191], v9\n\t"
    "ds_read_b128 v[48:51], v6 offset:3072\n\t"
    "s_waitcnt lgkmcnt(6)\n\t"
    "v_smfmac_f32_16x16x64_bf16 v[12:15], v[52:55], v[20:27], v9\n\t"
    "ds_read_b128 v[20:23], v8 offset:16384\n\t"
    "ds_read_b128 v[24:27], v8 offset:17408\n\t"
    "s_waitcnt lgkmcnt(6)\n\t"
    "v_smfmac_f32_16x16x64_bf16 v[16:19], v[52:55], v[28:35], v9\n\t"
    "ds_read_b128 v[28:31], v8 offset:24576\n\t"
    "ds_read_b128 v[32:35], v8 offset:25600\n\t"
    "ds_read_b128 v[52:55], v6 offset:15360\n\t"
    "s_waitcnt lgkmcnt(8)\n\t"
    "v_smfmac_f32_16x16x64_bf16 v[12:15], v[40:43], v[192:199], v9\n\t"
    "v_smfmac_f32_16x16x64_bf16 v[16:19], v[40:43], v[200:207], v9\n\t"
    "s_waitcnt lgkmcnt(7)\n\t"
    "v_smfmac_f32_16x16x64_bf16 v[12:15], v[36:39], v[208:215], v9\n\t"
    "v_smfmac_f32_16x16x64_bf16 v[16:19], v[36:39], v[216:223], v9\n\t"
    "s_waitcnt lgkmcnt(6)\n\t"
    "v_smfmac_f32_16x16x64_bf16 v[12:15], v[44:47], v[224:231], v9\n\t"
    "v_smfmac_f32_16x16x64_bf16 v[16:19], v[44:47], v[232:239], v9\n\t"
    "s_waitcnt lgkmcnt(3)\n\t"
    "v_smfmac_f32_16x16x64_bf16 v[12:15], v[48:51], v[20:27], v9\n\t"
    "s_waitcnt lgkmcnt(1)\n\t"
    "v_smfmac_f32_16x16x64_bf16 v[16:19], v[48:51], v[28:35], v9\n\t"
    "s_waitcnt lgkmcnt(0)\n\t"
    "v_smfmac_f32_16x16x64_bf16 v[12:15], v[52:55], v[240:247], v9\n\t"
    "v_smfmac_f32_16x16x64_bf16 v[16:19], v[52:55], v[248:255], v9\n\t"
    "s_nop 6\n\t"
    "v_permlane16_swap_b32 v12, v14\n\t"
    "v_permlane16_swap_b32 v13, v15\n\t"
    "v_permlane16_swap_b32 v16, v18\n\t"
    "v_permlane16_swap_b32 v17, v19\n\t"
    "v_add_f32 v12, v12, v14\n\t"
    "v_add_f32 v13, v13, v15\n\t"
    "v_add_f32 v16, v16, v18\n\t"
    "v_cvt_pk_bf16_f32 v12, v12, v13\n\t"
    "v_add_f32 v17, v17, v19\n\t"
    "ds_write_b32 v10, v12 offset:34816\n\t"
    "v_cvt_pk_bf16_f32 v16, v16, v17\n\t"
    "ds_write_b32 v10, v16 offset:35072\n\t"
    "s_waitcnt lgkmcnt(0)\n\t"
    "s_setprio 0\n\t"
    "s_barrier\n\t"
    "s_memrealtime s[36:37]\n\t"
    "s_waitcnt lgkmcnt(0)\n\t"
    "s_and_b32 s36, s36, 0xffffff\n\t"
    "s_or_b32 s36, s36, %[sn]\n\t"
    "v_lshlrev_b32 v7, 10, v6\n\t"
    "v_mov_b32 v8, s36\n\t"
    "s_mov_b64 exec, 15\n\t"
    "global_store_dword v7, v8, %[vs] offset:0 sc1\n\t"
    "s_mov_b64 exec, -1\n\t"
    "s_barrier\n\t"
    "s_memrealtime s[36:37]\n\t"
    "s_mov_b32 %[d1], %[d0]\n\t"
    "s_waitcnt lgkmcnt(0)\n\t"
    "s_sub_u32 %[d0], s36, s38\n\t"
    "s_min_u32 %[d0], %[d0], 200\n\t"
    "s_waitcnt lgkmcnt(0)\n\t"
    "s_branch 9f\n\t"
    "1:\n\t"
    "s_add_u32 %[sp], %[sp], 1\n\t"
    "s_cmp_ge_u32 %[sp], %[cap]\n\t"
    "s_cbranch_scc1 7f\n\t"
    "global_load_dwordx4 v[36:39], v6, %[ag] sc1\n\t"
    "global_load_dwordx4 v[40:43], v6, %[ag] offset:1024 sc1\n\t"
    "s_waitcnt lgkmcnt(0)\n\t"
    "s_memtime %[tc]\n\t"
    "s_nop 7\n\t"
    "s_branch 3b\n\t"
    "7:\n\t"
    "s_mov_b32 %[cap], 0\n\t"
    "s_branch 2b\n\t"
    "9:"
    : [tc] "=&s"(tc), [sp] "=&s"(sp), [cap] "+s"(cap), [d0] "+s"(d0), [d1] "+s"(d1)
    : [ag] "s"(ag), [c44] "s"(0x4444u), [vs] "s"(vs), [sn] "s"((unsigned)(size_t)od), "{v[64:95]}"(W.w0), "{v[96:127]}"(W.w1), "{v[128:159]}"(W.w2), "{v[160:191]}"(W.w3), "{v[192:223]}"(W.w4), "{v[224:255]}"(W.w5), "{v[56:63]}"(W.x)
    : "v5", "v6", "v7", "v8", "v9", "v10", "v11", "v12", "v13", "v14", "v15", "v16", "v17", "v18", "v19", "v20", "v21", "v22", "v23", "v24", "v25", "v26", "v27", "v28", "v29", "v30", "v31", "v32", "v33", "v34", "v35", "v36", "v37", "v38", "v39", "v40", "v41", "v42", "v43", "v44", "v45", "v46", "v47", "v48", "v49", "v50", "v51", "v52", "v53", "v54", "v55", "vcc", "scc", "memory", "s36", "s37", "s38");
}
template <> __device__ __forceinline__ void qk_step<5>(const QKW& W, const char* ag, const char* vs, const char* od, const char* kv, unsigned& cap, unsigned& sp,
                                                        unsigned long long& tc, unsigned long long* ts, unsigned& d0, unsigned& d1) {
  unsigned st, bg;
  asm volatile("v_mbcnt_lo_u32_b32 v52, -1, 0\n\t"
    "v_mbcnt_hi_u32_b32 v52, -1, v52\n\t"
    "v_lshlrev_b32 v6, 4, v52\n\t"
    "s_mov_b32 %[sp], 0\n\t"
    "global_load_dwordx4 v[36:39], v6, %[ag] sc1\n\t"
    "global_load_dwordx4 v[40:43], v6, %[ag] offset:1024 sc1\n\t"
    "s_memtime %[tc]\n\t"
    "v_add_u32 v7, 75776, v6\n\t"
    "v_add_u32 v8, 133120, v6\n\t"
    "ds_read_b128 v[20:23], v7 offset:8192\n\t"
    "ds_read_b128 v[24:27], v7 offset:9216\n\t"
    "ds_read_b128 v[28:31], v7 offset:0\n\t"
    "ds_read_b128 v[32:35], v7 offset:1024\n\t"
    "v_lshrrev_b32 v44, 4, v52\n\t"
    "v_and_b32 v45, 15, v52\n\t"
    "v_and_b32 v53, 4, v52\n\t"
    "v_mul_u32_u24 v53, 0x2aaa, v53\n\t"
    "v_lshrrev_b32 v9, 1, v52\n\t"
    "v_and_b32 v9, 2, v9\n\t"
    "v_or3_b32 v9, v9, v53, %[c44]\n\t"
    "v_lshlrev_b32 v53, 2, v44\n\t"
    "v_lshl_or_b32 v10, v45, 4, v53\n\t"
    "v_and_b32 v53, 1, v52\n\t"
    "v_lshlrev_b32 v53, 3, v53\n\t"
    "v_lshl_or_b32 v11, v44, 7, v53\n\t"
    "v_and_b32 v53, 12, v52\n\t"
    "v_lshl_or_b32 v11, v53, 2, v11\n\t"
    "v_and_b32 v53, 2, v52\n\t"
    "v_lshl_or_b32 v11, v53, 8, v11\n\t"
    "3:\n\t"
    "v_mov_b32 v12, 0\n\t"
    "v_mov_b32 v13, 0\n\t"
    "v_mov_b32 v14, 0\n\t"
    "v_mov_b32 v15, 0\n\t"
    "v_mov_b32 v16, 0\n\t"
    "v_mov_b32 v17, 0\n\t"
    "v_mov_b32 v18, 0\n\t"
    "v_mov_b32 v19, 0\n\t"
    "s_waitcnt vmcnt(0) lgkmcnt(0)\n\t"
    "14:\n\t"
    "ds_write_b128 v6, v[36:39] offset:10240\n\t"
    "ds_write_b128 v6, v[40:43] offset:11264\n\t"
    "v_smfmac_f32_16x16x64_bf16 v[12:15], v[36:39], v[160:167], v9\n\t"
    "v_max3_u32 v52, v36, v37, v38\n\t"
    "v_max3_u32 v53, v39, v40, v41\n\t"
    "v_smfmac_f32_16x16x64_bf16 v[16:19], v[36:39], v[168:175], v9\n\t"
    "v_max3_u32 v52, v52, v42, v43\n\t"
    "v_max_u32 v52, v52, v53\n\t"
    "v_cmp_eq_u32 vcc, -1, v52\n\t"
    "s_cbranch_vccnz 1f\n\t"
    "2:\n\t"
    "s_waitcnt lgkmcnt(0)\n\t"
    "s_barrier\n\t"
    "ds_read_b128 v[36:39], v6 offset:0\n\t"
    "ds_read_b128 v[44:47], v6 offset:4096\n\t"
    "v_smfmac_f32_16x16x64_bf16 v[12:15], v[40:43], v[176:183], v9\n\t"
    "ds_read_b128 v[48:51], v6 offset:5120\n\t"
    "ds_read_b128 v[52:55], v6 offset:6144\n\t"
    "v_smfmac_f32_16x16x64_bf16 v[16:19], v[40:43], v[184:191], v9\n\t"
    "ds_read_b128 v[40:43], v6 offset:1024\n\t"
    "s_sleep 0\n\t"
    "s_waitcnt lgkmcnt(4)\n\t"
    "v_smfmac_f32_16x16x64_bf16 v[12:15], v[36:39], v[56:63], v9\n\t"
    "v_smfmac_f32_16x16x64_bf16 v[16:19], v[36:39], v[28:35], v9\n\t"
    "ds_read_b128 v[28:31], v7 offset:16384\n\t"
    "ds_read_b128 v[32:35], v7 offset:17408\n\t"
    "ds_read_b128 v[36:39], v6 offset:7168\n\t"
    "s_waitcnt lgkmcnt(6)\n\t"
    "v_smfmac_f32_16x16x64_bf16 v[12:15], v[44:47], v[64:71], v9\n\t"
    "v_smfmac_f32_16x16x64_bf16 v[16:19], v[44:47], v[72:79], v9\n\t"
    "ds_read_b128 v[44:47], v6 offset:8192\n\t"
    "s_waitcnt lgkmcnt(6)\n\t"
    "v_smfmac_f32_16x16x64_bf16 v[12:15], v[48:51], v[80:87], v9\n\t"
    "v_smfmac_f32_16x16x64_bf16 v[16:19], v[48:51], v[88:95], v9\n\t"
    "ds_read_b128 v[48:51], v6 offset:9216\n\t"
    "s_waitcnt lgkmcnt(6)\n\t"
    "v_smfmac_f32_16x16x64_bf16 v[12:15], v[52:55], v[96:103], v9\n\t"
    "v_smfmac_f32_16x16x64_bf16 v[16:19], v[52:55], v[104:111], v9\n\t"
    "ds_read_b128 v[52:55], v6 offset:2048\n\t"
    "s_waitcnt lgkmcnt(6)\n\t"
    "v_smfmac_f32_16x16x64_bf16 v[12:15], v[40:43], v[20:27], v9\n\t"
    "ds_read_b128 v[20:23], v8 offset:0\n\t"
    "ds_read_b128 v[24:27], v8 offset:1024\n\t"
    "s_waitcnt lgkmcnt(6)\n\t"
    "v_smfmac_f32_16x16x64_bf16 v[16:19], v[40:43], v[28:35], v9\n\t"
    "ds_read_b128 v[28:31], v8 offset:8192\n\t"
    "ds_read_b128 v[32:35], v8 offset:9216\n\t"
    "ds_read_b128 v[40:43], v6 offset:12288\n\t"
    "s_waitcnt lgkmcnt(8)\n\t"
    "v_smfmac_f32_16x16x64_bf16 v[12:15], v[36:39], v[112:119], v9\n\t"
    "v_smfmac_f32_16x16x64_bf16 v[16:19], v[36:39], v[120:127], v9\n\t"
    "ds_read_b128 v[36:39], v6 offset:13312\n\t"
    "s_waitcnt lgkmcnt(8)\n\t"
    "v_smfmac_f32_16x16x64_bf16 v[12:15], v[44:47], v[128:135], v9\n\t"
    "v_smfmac_f32_16x16x64_bf16 v[16:19], v[44:47], v[136:143], v9\n\t"
    "ds_read_b128 v[44:47], v6 offset:14336\n\t"
    "s_barrier\n\t"
    "s_setprio 2\n\t"
    "s_waitcnt lgkmcnt(8)\n\t"
    "v_smfmac_f32_16x16x64_bf16 v[12:15], v[48:51], v[144:151], v9\n\t"
    "v_smfmac_f32_16x16x64_bf16 v[16:19], v[48:51], v[152:159], v9\n\t"
    "ds_read_b128 v[48:51], v6 offset:3072\n\t"
    "s_waitcnt lgkmcnt(6)\n\t"
    "v_smfmac_f32_16x16x64_bf16 v[12:15], v[52:55], v[20:27], v9\n\t"
    "ds_read_b128 v[20:23], v8 offset:16384\n\t"
    "ds_read_b128 v[24:27], v8 offset:17408\n\t"
    "s_waitcnt lgkmcnt(6)\n\t"
    "v_smfmac_f32_16x16x64_bf16 v[16:19], v[52:55], v[28:35], v9\n\t"
    "ds_read_b128 v[28:31], v8 offset:24576\n\t"
    "ds_read_b128 v[32:35], v8 offset:25600\n\t"
    "ds_read_b128 v[52:55], v6 offset:15360\n\t"
    "s_waitcnt lgkmcnt(8)\n\t"
    "v_smfmac_f32_16x16x64_bf16 v[12:15], v[40:43], v[192:199], v9\n\t"
    "v_smfmac_f32_16x16x64_bf16 v[16:19], v[40:43], v[200:207], v9\n\t"
    "s_waitcnt lgkmcnt(7)\n\t"
    "v_smfmac_f32_16x16x64_bf16 v[12:15], v[36:39], v[208:215], v9\n\t"
    "v_smfmac_f32_16x16x64_bf16 v[16:19], v[36:39], v[216:223], v9\n\t"
    "s_waitcnt lgkmcnt(6)\n\t"
    "v_smfmac_f32_16x16x64_bf16 v[12:15], v[44:47], v[224:231], v9\n\t"
    "v_smfmac_f32_16x16x64_bf16 v[16:19], v[44:47], v[232:239], v9\n\t"
    "s_waitcnt lgkmcnt(3)\n\t"
    "v_smfmac_f32_16x16x64_bf16 v[12:15], v[48:51], v[20:27], v9\n\t"
    "s_waitcnt lgkmcnt(1)\n\t"
    "v_smfmac_f32_16x16x64_bf16 v[16:19], v[48:51], v[28:35], v9\n\t"
    "s_waitcnt lgkmcnt(0)\n\t"
    "v_smfmac_f32_16x16x64_bf16 v[12:15], v[52:55], v[240:247], v9\n\t"
    "v_smfmac_f32_16x16x64_bf16 v[16:19], v[52:55], v[248:255], v9\n\t"
    "s_nop 6\n\t"
    "v_permlane16_swap_b32 v12, v14\n\t"
    "v_permlane16_swap_b32 v13, v15\n\t"
    "v_permlane16_swap_b32 v16, v18\n\t"
    "v_permlane16_swap_b32 v17, v19\n\t"
    "v_add_f32 v12, v12, v14\n\t"
    "v_add_f32 v13, v13, v15\n\t"
    "v_add_f32 v16, v16, v18\n\t"
    "v_cvt_pk_bf16_f32 v12, v12, v13\n\t"
    "v_add_f32 v17, v17, v19\n\t"
    "ds_write_b32 v10, v12 offset:35328\n\t"
    "v_cvt_pk_bf16_f32 v16, v16, v17\n\t"
    "ds_write_b32 v10, v16 offset:35584\n\t"
    "s_waitcnt lgkmcnt(0)\n\t"
    "s_setprio 0\n\t"
    "s_barrier\n\t"
    "ds_read_b64_tr_b16 v[36:37], v11 offset:34816\n\t"
    "ds_read_b64_tr_b16 v[38:39], v11 offset:34880\n\t"
    "ds_read_b64_tr_b16 v[40:41], v11 offset:35840\n\t"
    "ds_read_b64_tr_b16 v[42:43], v11 offset:35904\n\t"
    "s_waitcnt lgkmcnt(0)\n\t"
    "v_min_u32 v36, -2, v36\n\t"
    "v_min_u32 v37, -2, v37\n\t"
    "v_min_u32 v38, -2, v38\n\t"
    "v_min_u32 v39, -2, v39\n\t"
    "v_min_u32 v40, -2, v40\n\t"
    "v_min_u32 v41, -2, v41\n\t"
    "v_min_u32 v42, -2, v42\n\t"
    "v_min_u32 v43, -2, v43\n\t"
    "global_store_dwordx4 v6, v[36:39], %[vs] sc1\n\t"
    "global_store_dwordx4 v6, v[40:43], %[vs] offset:1024 sc1\n\t"
    "s_nop 1\n\t"
    "s_barrier\n\t"
    "s_waitcnt lgkmcnt(0)\n\t"
    "s_branch 9f\n\t"
    "1:\n\t"
    "s_add_u32 %[sp], %[sp], 1\n\t"
    "s_cmp_ge_u32 %[sp], %[cap]\n\t"
    "s_cbranch_scc1 7f\n\t"
    "global_load_dwordx4 v[36:39], v6, %[ag] sc1\n\t"
    "global_load_dwordx4 v[40:43], v6, %[ag] offset:1024 sc1\n\t"
    "s_waitcnt lgkmcnt(0)\n\t"
    "s_memtime %[tc]\n\t"
    "s_nop 7\n\t"
    "s_branch 3b\n\t"
    "7:\n\t"
    "s_mov_b32 %[cap], 0\n\t"
    "s_branch 2b\n\t"
    "9:"
    : [tc] "=&s"(tc), [sp] "=&s"(sp), [cap] "+s"(cap)
    : [ag] "s"(ag), [c44] "s"(0x4444u), [vs] "s"(vs), "{v[64:95]}"(W.w0), "{v[96:127]}"(W.w1), "{v[128:159]}"(W.w2), "{v[160:191]}"(W.w3), "{v[192:223]}"(W.w4), "{v[224:255]}"(W.w5), "{v[56:63]}"(W.x)
    : "v6", "v7", "v8", "v9", "v10", "v11", "v12", "v13", "v14", "v15", "v16", "v17", "v18", "v19", "v20", "v21", "v22", "v23", "v24", "v25", "v26", "v27", "v28", "v29", "v30", "v31", "v32", "v33", "v34", "v35", "v36", "v37", "v38", "v39", "v40", "v41", "v42", "v43", "v44", "v45", "v46", "v47", "v48", "v49", "v50", "v51", "v52", "v53", "v54", "v55", "vcc", "scc", "memory");
}
template <> __device__ __forceinline__ void qk_step<6>(const QKW& W, const char* ag, const char* vs, const char* od, const char* kv, unsigned& cap, unsigned& sp,
                                                        unsigned long long& tc, unsigned long long* ts, unsigned& d0, unsigned& d1) {
  unsigned st, bg;
  asm volatile("v_mbcnt_lo_u32_b32 v52, -1, 0\n\t"
    "v_mbcnt_hi_u32_b32 v52, -1, v52\n\t"
    "v_lshlrev_b32 v6, 4, v52\n\t"
    "s_mov_b32 %[sp], 0\n\t"
    "global_load_dwordx4 v[36:39], v6, %[ag] sc1\n\t"
    "global_load_dwordx4 v[40:43], v6, %[ag] offset:1024 sc1\n\t"
    "s_memtime %[tc]\n\t"
    "v_add_u32 v7, 77824, v6\n\t"
    "v_add_u32 v8, 135168, v6\n\t"
    "ds_read_b128 v[20:23], v7 offset:8192\n\t"
    "ds_read_b128 v[24:27], v7 offset:9216\n\t"
    "ds_read_b128 v[28:31], v7 offset:0\n\t"
    "ds_read_b128 v[32:35], v7 offset:1024\n\t"
    "v_lshrrev_b32 v44, 4, v52\n\t"
    "v_and_b32 v45, 15, v52\n\t"
    "v_and_b32 v53, 4, v52\n\t"
    "v_mul_u32_u24 v53, 0x2aaa, v53\n\t"
    "v_lshrrev_b32 v9, 1, v52\n\t"
    "v_and_b32 v9, 2, v9\n\t"
    "v_or3_b32 v9, v9, v53, %[c44]\n\t"
    "v_lshlrev_b32 v53, 2, v44\n\t"
    "v_lshl_or_b32 v10, v45, 4, v53\n\t"
    "3:\n\t"
    "v_mov_b32 v12, 0\n\t"
    "v_mov_b32 v13, 0\n\t"
    "v_mov_b32 v14, 0\n\t"
    "v_mov_b32 v15, 0\n\t"
    "v_mov_b32 v16, 0\n\t"
    "v_mov_b32 v17, 0\n\t"
    "v_mov_b32 v18, 0\n\t"
    "v_mov_b32 v19, 0\n\t"
    "s_waitcnt vmcnt(0) lgkmcnt(0)\n\t"
    "14:\n\t"
    "ds_write_b128 v6, v[36:39] offset:12288\n\t"
    "ds_write_b128 v6, v[40:43] offset:13312\n\t"
    "v_smfmac_f32_16x16x64_bf16 v[12:15], v[36:39], v[192:199], v9\n\t"
    "v_max3_u32 v52, v36, v37, v38\n\t"
    "v_max3_u32 v53, v39, v40, v41\n\t"
    "v_smfmac_f32_16x16x64_bf16 v[16:19], v[36:39], v[200:207], v9\n\t"
    "v_max3_u32 v52, v52, v42, v43\n\t"
    "v_max_u32 v52, v52, v53\n\t"
    "v_cmp_eq_u32 vcc, -1, v52\n\t"
    "s_cbranch_vccnz 1f\n\t"
    "2:\n\t"
    "s_waitcnt lgkmcnt(0)\n\t"
    "s_barrier\n\t"
    "ds_read_b128 v[36:39], v6 offset:0\n\t"
    "ds_read_b128 v[44:47], v6 offset:4096\n\t"
    "v_smfmac_f32_16x16x64_bf16 v[12:15], v[40:43], v[208:215], v9\n\t"
    "ds_read_b128 v[48:51], v6 offset:5120\n\t"
    "ds_read_b128 v[52:55], v6 offset:6144\n\t"
    "v_smfmac_f32_16x16x64_bf16 v[16:19], v[40:43], v[216:223], v9\n\t"
    "ds_read_b128 v[40:43], v6 offset:1024\n\t"
    "s_sleep 0\n\t"
    "s_waitcnt lgkmcnt(4)\n\t"
    "v_smfmac_f32_16x16x64_bf16 v[12:15], v[36:39], v[56:63], v9\n\t"
    "v_smfmac_f32_16x16x64_bf16 v[16:19], v[36:39], v[28:35], v9\n\t"
    "ds_read_b128 v[28:31], v7 offset:16384\n\t"
    "ds_read_b128 v[32:35], v7 offset:17408\n\t"
    "ds_read_b128 v[36:39], v6 offset:7168\n\t"
    "s_waitcnt lgkmcnt(6)\n\t"
    "v_smfmac_f32_16x16x64_bf16 v[12:15], v[44:47], v[64:71], v9\n\t"
    "v_smfmac_f32_16x16x64_bf16 v[16:19], v[44:47], v[72:79], v9\n\t"
    "ds_read_b128 v[44:47], v6 offset:8192\n\t"
    "s_waitcnt lgkmcnt(6)\n\t"
    "v_smfmac_f32_16x16x64_bf16 v[12:15], v[48:51], v[80:87], v9\n\t"
    "v_smfmac_f32_16x16x64_bf16 v[16:19], v[48:51], v[88:95], v9\n\t"
    "ds_read_b128 v[48:51], v6 offset:9216\n\t"
    "s_waitcnt lgkmcnt(6)\n\t"
    "v_smfmac_f32_16x16x64_bf16 v[12:15], v[52:55], v[96:103], v9\n\t"
    "v_smfmac_f32_16x16x64_bf16 v[16:19], v[52:55], v[104:111], v9\n\t"
    "ds_read_b128 v[52:55], v6 offset:2048\n\t"
    "s_waitcnt lgkmcnt(6)\n\t"
    "v_smfmac_f32_16x16x64_bf16 v[12:15], v[40:43], v[20:27], v9\n\t"
    "ds_read_b128 v[20:23], v8 offset:0\n\t"
    "ds_read_b128 v[24:27], v8 offset:1024\n\t"
    "s_waitcnt lgkmcnt(6)\n\t"
    "v_smfmac_f32_16x16x64_bf16 v[16:19], v[40:43], v[28:35], v9\n\t"
    "ds_read_b128 v[28:31], v8 offset:8192\n\t"
    "ds_read_b128 v[32:35], v8 offset:9216\n\t"
    "ds_read_b128 v[40:43], v6 offset:10240\n\t"
    "s_waitcnt lgkmcnt(8)\n\t"
    "v_smfmac_f32_16x16x64_bf16 v[12:15], v[36:39], v[112:119], v9\n\t"
    "v_smfmac_f32_16x16x64_bf16 v[16:19], v[36:39], v[120:127], v9\n\t"
    "ds_read_b128 v[36:39], v6 offset:11264\n\t"
    "s_waitcnt lgkmcnt(8)\n\t"
    "v_smfmac_f32_16x16x64_bf16 v[12:15], v[44:47], v[128:135], v9\n\t"
    "v_smfmac_f32_16x16x64_bf16 v[16:19], v[44:47], v[136:143], v9\n\t"
    "ds_read_b128 v[44:47], v6 offset:14336\n\t"
    "s_barrier\n\t"
    "s_setprio 2\n\t"
    "s_waitcnt lgkmcnt(8)\n\t"
    "v_smfmac_f32_16x16x64_bf16 v[12:15], v[48:51], v[144:151], v9\n\t"
    "v_smfmac_f32_16x16x64_bf16 v[16:19], v[48:51], v[152:159], v9\n\t"
    "ds_read_b128 v[48:51], v6 offset:3072\n\t"
    "s_waitcnt lgkmcnt(6)\n\t"
    "v_smfmac_f32_16x16x64_bf16 v[12:15], v[52:55], v[20:27], v9\n\t"
    "ds_read_b128 v[20:23], v8 offset:16384\n\t"
    "ds_read_b128 v[24:27], v8 offset:17408\n\t"
    "s_waitcnt lgkmcnt(6)\n\t"
    "v_smfmac_f32_16x16x64_bf16 v[16:19], v[52:55], v[28:35], v9\n\t"
    "ds_read_b128 v[28:31], v8 offset:24576\n\t"
    "ds_read_b128 v[32:35], v8 offset:25600\n\t"
    "ds_read_b128 v[52:55], v6 offset:15360\n\t"
    "s_waitcnt lgkmcnt(8)\n\t"
    "v_smfmac_f32_16x16x64_bf16 v[12:15], v[40:43], v[160:167], v9\n\t"
    "v_smfmac_f32_16x16x64_bf16 v[16:19], v[40:43], v[168:175], v9\n\t"
    "s_waitcnt lgkmcnt(7)\n\t"
    "v_smfmac_f32_16x16x64_bf16 v[12:15], v[36:39], v[176:183], v9\n\t"
    "v_smfmac_f32_16x16x64_bf16 v[16:19], v[36:39], v[184:191], v9\n\t"
    "s_waitcnt lgkmcnt(6)\n\t"
    "v_smfmac_f32_16x16x64_bf16 v[12:15], v[44:47], v[224:231], v9\n\t"
    "v_smfmac_f32_16x16x64_bf16 v[16:19], v[44:47], v[232:239], v9\n\t"
    "s_waitcnt lgkmcnt(3)\n\t"
    "v_smfmac_f32_16x16x64_bf16 v[12:15], v[48:51], v[20:27], v9\n\t"
    "s_waitcnt lgkmcnt(1)\n\t"
    "v_smfmac_f32_16x16x64_bf16 v[16:19], v[48:51], v[28:35], v9\n\t"
    "s_waitcnt lgkmcnt(0)\n\t"
    "v_smfmac_f32_16x16x64_bf16 v[12:15], v[52:55], v[240:247], v9\n\t"
    "v_smfmac_f32_16x16x64_bf16 v[16:19], v[52:55], v[248:255], v9\n\t"
    "s_nop 6\n\t"
    "v_permlane16_swap_b32 v12, v14\n\t"
    "v_permlane16_swap_b32 v13, v15\n\t"
    "v_permlane16_swap_b32 v16, v18\n\t"
    "v_permlane16_swap_b32 v17, v19\n\t"
    "v_add_f32 v12, v12, v14\n\t"
    "v_add_f32 v13, v13, v15\n\t"
    "v_add_f32 v16, v16, v18\n\t"
    "v_cvt_pk_bf16_f32 v12, v12, v13\n\t"
    "v_add_f32 v17, v17, v19\n\t"
    "ds_write_b32 v10, v12 offset:35840\n\t"
    "v_cvt_pk_bf16_f32 v16, v16, v17\n\t"
    "ds_write_b32 v10, v16 offset:36096\n\t"
    "s_waitcnt lgkmcnt(0)\n\t"
    "s_setprio 0\n\t"
    "s_barrier\n\t"
    "s_barrier\n\t"
    "s_waitcnt lgkmcnt(0)\n\t"
    "s_branch 9f\n\t"
    "1:\n\t"
    "s_add_u32 %[sp], %[sp], 1\n\t"
    "s_cmp_ge_u32 %[sp], %[cap]\n\t"
    "s_cbranch_scc1 7f\n\t"
    "global_load_dwordx4 v[36:39], v6, %[ag] sc1\n\t"
    "global_load_dwordx4 v[40:43], v6, %[ag] offset:1024 sc1\n\t"
    "s_waitcnt lgkmcnt(0)\n\t"
    "s_memtime %[tc]\n\t"
    "s_nop 7\n\t"
    "s_branch 3b\n\t"
    "7:\n\t"
    "s_mov_b32 %[cap], 0\n\t"
    "s_branch 2b\n\t"
    "9:"
    : [tc] "=&s"(tc), [sp] "=&s"(sp), [cap] "+s"(cap)
    : [ag] "s"(ag), [c44] "s"(0x4444u), "{v[64:95]}"(W.w0), "{v[96:127]}"(W.w1), "{v[128:159]}"(W.w2), "{v[160:191]}"(W.w3), "{v[192:223]}"(W.w4), "{v[224:255]}"(W.w5), "{v[56:63]}"(W.x)
    : "v6", "v7", "v8", "v9", "v10", "v11", "v12", "v13", "v14", "v15", "v16", "v17", "v18", "v19", "v20", "v21", "v22", "v23", "v24", "v25", "v26", "v27", "v28", "v29", "v30", "v31", "v32", "v33", "v34", "v35", "v36", "v37", "v38", "v39", "v40", "v41", "v42", "v43", "v44", "v45", "v46", "v47", "v48", "v49", "v50", "v51", "v52", "v53", "v54", "v55", "vcc", "scc", "memory");
}
template <> __device__ __forceinline__ void qk_step<7>(const QKW& W, const char* ag, const char* vs, const char* od, const char* kv, unsigned& cap, unsigned& sp,
                                                        unsigned long long& tc, unsigned long long* ts, unsigned& d0, unsigned& d1) {
  unsigned st, bg;
  asm volatile("v_mbcnt_lo_u32_b32 v52, -1, 0\n\t"
    "v_mbcnt_hi_u32_b32 v52, -1, v52\n\t"
    "v_lshlrev_b32 v6, 4, v52\n\t"
    "s_mov_b32 %[sp], 0\n\t"
    "global_load_dwordx4 v[36:39], v6, %[ag] sc1\n\t"
    "global_load_dwordx4 v[40:43], v6, %[ag] offset:1024 sc1\n\t"
    "s_memtime %[tc]\n\t"
    "v_add_u32 v7, 79872, v6\n\t"
    "v_add_u32 v8, 137216, v6\n\t"
    "ds_read_b128 v[20:23], v7 offset:8192\n\t"
    "ds_read_b128 v[24:27], v7 offset:9216\n\t"
    "ds_read_b128 v[28:31], v7 offset:0\n\t"
    "ds_read_b128 v[32:35], v7 offset:1024\n\t"
    "v_lshrrev_b32 v44, 4, v52\n\t"
    "v_and_b32 v45, 15, v52\n\t"
    "v_and_b32 v53, 4, v52\n\t"
    "v_mul_u32_u24 v53, 0x2aaa, v53\n\t"
    "v_lshrrev_b32 v9, 1, v52\n\t"
    "v_and_b32 v9, 2, v9\n\t"
    "v_or3_b32 v9, v9, v53, %[c44]\n\t"
    "v_lshlrev_b32 v53, 2, v44\n\t"
    "v_lshl_or_b32 v10, v45, 4, v53\n\t"
    "3:\n\t"
    "v_mov_b32 v12, 0\n\t"
    "v_mov_b32 v13, 0\n\t"
    "v_mov_b32 v14, 0\n\t"
    "v_mov_b32 v15, 0\n\t"
    "v_mov_b32 v16, 0\n\t"
    "v_mov_b32 v17, 0\n\t"
    "v_mov_b32 v18, 0\n\t"
    "v_mov_b32 v19, 0\n\t"
    "s_waitcnt vmcnt(0) lgkmcnt(0)\n\t"
    "14:\n\t"
    "ds_write_b128 v6, v[36:39] offset:14336\n\t"
    "ds_write_b128 v6, v[40:43] offset:15360\n\t"
    "v_smfmac_f32_16x16x64_bf16 v[12:15], v[36:39], v[224:231], v9\n\t"
    "v_max3_u32 v52, v36, v37, v38\n\t"
    "v_max3_u32 v53, v39, v40, v41\n\t"
    "v_smfmac_f32_16x16x64_bf16 v[16:19], v[36:39], v[232:239], v9\n\t"
    "v_max3_u32 v52, v52, v42, v43\n\t"
    "v_max_u32 v52, v52, v53\n\t"
    "v_cmp_eq_u32 vcc, -1, v52\n\t"
    "s_cbranch_vccnz 1f\n\t"
    "2:\n\t"
    "s_waitcnt lgkmcnt(0)\n\t"
    "s_barrier\n\t"
    "ds_read_b128 v[36:39], v6 offset:0\n\t"
    "ds_read_b128 v[44:47], v6 offset:4096\n\t"
    "v_smfmac_f32_16x16x64_bf16 v[12:15], v[40:43], v[240:247], v9\n\t"
    "ds_read_b128 v[48:51], v6 offset:5120\n\t"
    "ds_read_b128 v[52:55], v6 offset:6144\n\t"
    "v_smfmac_f32_16x16x64_bf16 v[16:19], v[40:43], v[248:255], v9\n\t"
    "ds_read_b128 v[40:43], v6 offset:1024\n\t"
    "s_sleep 0\n\t"
    "s_waitcnt lgkmcnt(4)\n\t"
    "v_smfmac_f32_16x16x64_bf16 v[12:15], v[36:39], v[56:63], v9\n\t"
    "v_smfmac_f32_16x16x64_bf16 v[16:19], v[36:39], v[28:35], v9\n\t"
    "ds_read_b128 v[28:31], v7 offset:16384\n\t"
    "ds_read_b128 v[32:35], v7 offset:17408\n\t"
    "ds_read_b128 v[36:39], v6 offset:7168\n\t"
    "s_waitcnt lgkmcnt(6)\n\t"
    "v_smfmac_f32_16x16x64_bf16 v[12:15], v[44:47], v[64:71], v9\n\t"
    "v_smfmac_f32_16x16x64_bf16 v[16:19], v[44:47], v[72:79], v9\n\t"
    "ds_read_b128 v[44:47], v6 offset:8192\n\t"
    "s_waitcnt lgkmcnt(6)\n\t"
    "v_smfmac_f32_16x16x64_bf16 v[12:15], v[48:51], v[80:87], v9\n\t"
    "v_smfmac_f32_16x16x64_bf16 v[16:19], v[48:51], v[88:95], v9\n\t"
    "ds_read_b128 v[48:51], v6 offset:9216\n\t"
    "s_waitcnt lgkmcnt(6)\n\t"
    "v_smfmac_f32_16x16x64_bf16 v[12:15], v[52:55], v[96:103], v9\n\t"
    "v_smfmac_f32_16x16x64_bf16 v[16:19], v[52:55], v[104:111], v9\n\t"
    "ds_read_b128 v[52:55], v6 offset:2048\n\t"
    "s_waitcnt lgkmcnt(6)\n\t"
    "v_smfmac_f32_16x16x64_bf16 v[12:15], v[40:43], v[20:27], v9\n\t"
    "ds_read_b128 v[20:23], v8 offset:0\n\t"
    "ds_read_b128 v[24:27], v8 offset:1024\n\t"
    "s_waitcnt lgkmcnt(6)\n\t"
    "v_smfmac_f32_16x16x64_bf16 v[16:19], v[40:43], v[28:35], v9\n\t"
    "ds_read_b128 v[28:31], v8 offset:8192\n\t"
    "ds_read_b128 v[32:35], v8 offset:9216\n\t"
    "ds_read_b128 v[40:43], v6 offset:10240\n\t"
    "s_waitcnt lgkmcnt(8)\n\t"
    "v_smfmac_f32_16x16x64_bf16 v[12:15], v[36:39], v[112:119], v9\n\t"
    "v_smfmac_f32_16x16x64_bf16 v[16:19], v[36:39], v[120:127], v9\n\t"
    "ds_read_b128 v[36:39], v6 offset:11264\n\t"
    "s_waitcnt lgkmcnt(8)\n\t"
    "v_smfmac_f32_16x16x64_bf16 v[12:15], v[44:47], v[128:135], v9\n\t"
    "v_smfmac_f32_16x16x64_bf16 v[16:19], v[44:47], v[136:143], v9\n\t"
    "ds_read_b128 v[44:47], v6 offset:12288\n\t"
    "s_barrier\n\t"
    "s_setprio 2\n\t"
    "s_waitcnt lgkmcnt(8)\n\t"
    "v_smfmac_f32_16x16x64_bf16 v[12:15], v[48:51], v[144:151], v9\n\t"
    "v_smfmac_f32_16x16x64_bf16 v[16:19], v[48:51], v[152:159], v9\n\t"
    "ds_read_b128 v[48:51], v6 offset:3072\n\t"
    "s_waitcnt lgkmcnt(6)\n\t"
    "v_smfmac_f32_16x16x64_bf16 v[12:15], v[52:55], v[20:27], v9\n\t"
    "ds_read_b128 v[20:23], v8 offset:16384\n\t"
    "ds_read_b128 v[24:27], v8 offset:17408\n\t"
    "s_waitcnt lgkmcnt(6)\n\t"
    "v_smfmac_f32_16x16x64_bf16 v[16:19], v[52:55], v[28:35], v9\n\t"
    "ds_read_b128 v[28:31], v8 offset:24576\n\t"
    "ds_read_b128 v[32:35], v8 offset:25600\n\t"
    "ds_read_b128 v[52:55], v6 offset:13312\n\t"
    "s_waitcnt lgkmcnt(8)\n\t"
    "v_smfmac_f32_16x16x64_bf16 v[12:15], v[40:43], v[160:167], v9\n\t"
    "v_smfmac_f32_16x16x64_bf16 v[16:19], v[40:43], v[168:175], v9\n\t"
    "s_waitcnt lgkmcnt(7)\n\t"
    "v_smfmac_f32_16x16x64_bf16 v[12:15], v[36:39], v[176:183], v9\n\t"
    "v_smfmac_f32_16x16x64_bf16 v[16:19], v[36:39], v[184:191], v9\n\t"
    "s_waitcnt lgkmcnt(6)\n\t"
    "v_smfmac_f32_16x16x64_bf16 v[12:15], v[44:47], v[192:199], v9\n\t"
    "v_smfmac_f32_16x16x64_bf16 v[16:19], v[44:47], v[200:207], v9\n\t"
    "s_waitcnt lgkmcnt(3)\n\t"
    "v_smfmac_f32_16x16x64_bf16 v[12:15], v[48:51], v[20:27], v9\n\t"
    "s_waitcnt lgkmcnt(1)\n\t"
    "v_smfmac_f32_16x16x64_bf16 v[16:19], v[48:51], v[28:35], v9\n\t"
    "s_waitcnt lgkmcnt(0)\n\t"
    "v_smfmac_f32_16x16x64_bf16 v[12:15], v[52:55], v[208:215], v9\n\t"
    "v_smfmac_f32_16x16x64_bf16 v[16:19], v[52:55], v[216:223], v9\n\t"
    "s_nop 6\n\t"
    "v_permlane16_swap_b32 v12, v14\n\t"
    "v_permlane16_swap_b32 v13, v15\n\t"
    "v_permlane16_swap_b32 v16, v18\n\t"
    "v_permlane16_swap_b32 v17, v19\n\t"
    "v_add_f32 v12, v12, v14\n\t"
    "v_add_f32 v13, v13, v15\n\t"
    "v_add_f32 v16, v16, v18\n\t"
    "v_cvt_pk_bf16_f32 v12, v12, v13\n\t"
    "v_add_f32 v17, v17, v19\n\t"
    "ds_write_b32 v10, v12 offset:36352\n\t"
    "v_cvt_pk_bf16_f32 v16, v16, v17\n\t"
    "ds_write_b32 v10, v16 offset:36608\n\t"
    "s_waitcnt lgkmcnt(0)\n\t"
    "s_setprio 0\n\t"
    "s_barrier\n\t"
    "s_barrier\n\t"
    "s_waitcnt lgkmcnt(0)\n\t"
    "s_branch 9f\n\t"
    "1:\n\t"
    "s_add_u32 %[sp], %[sp], 1\n\t"
    "s_cmp_ge_u32 %[sp], %[cap]\n\t"
    "s_cbranch_scc1 7f\n\t"
    "global_load_dwordx4 v[36:39], v6, %[ag] sc1\n\t"
    "global_load_dwordx4 v[40:43], v6, %[ag] offset:1024 sc1\n\t"
    "s_waitcnt lgkmcnt(0)\n\t"
    "s_memtime %[tc]\n\t"
    "s_nop 7\n\t"
    "s_branch 3b\n\t"
    "7:\n\t"
    "s_mov_b32 %[cap], 0\n\t"
    "s_branch 2b\n\t"
    "9:"
    : [tc] "=&s"(tc), [sp] "=&s"(sp), [cap] "+s"(cap)
    : [ag] "s"(ag), [c44] "s"(0x4444u), "{v[64:95]}"(W.w0), "{v[96:127]}"(W.w1), "{v[128:159]}"(W.w2), "{v[160:191]}"(W.w3), "{v[192:223]}"(W.w4), "{v[224:255]}"(W.w5), "{v[56:63]}"(W.x)
    : "v6", "v7", "v8", "v9", "v10", "v11", "v12", "v13", "v14", "v15", "v16", "v17", "v18", "v19", "v20", "v21", "v22", "v23", "v24", "v25", "v26", "v27", "v28", "v29", "v30", "v31", "v32", "v33", "v34", "v35", "v36", "v37", "v38", "v39", "v40", "v41", "v42", "v43", "v44", "v45", "v46", "v47", "v48", "v49", "v50", "v51", "v52", "v53", "v54", "v55", "vcc", "scc", "memory");
}
template <int I> __device__ __forceinline__ void qk_wload(u32x32& w, unsigned a0, unsigned a1);
template <> __device__ __forceinline__ void qk_wload<0>(u32x32& w, unsigned a0, unsigned a1) {
  asm volatile("ds_read_b64_tr_b16 v[64:65], %1 offset:0\n\t"
    "ds_read_b64_tr_b16 v[66:67], %1 offset:512\n\t"
    "ds_read_b64_tr_b16 v[68:69], %1 offset:4096\n\t"
    "ds_read_b64_tr_b16 v[70:71], %1 offset:4608\n\t"
    "ds_read_b64_tr_b16 v[72:73], %2 offset:0\n\t"
    "ds_read_b64_tr_b16 v[74:75], %2 offset:512\n\t"
    "ds_read_b64_tr_b16 v[76:77], %2 offset:4096\n\t"
    "ds_read_b64_tr_b16 v[78:79], %2 offset:4608\n\t"
    "s_waitcnt lgkmcnt(0)\n\t"
    "ds_read_b64_tr_b16 v[80:81], %1 offset:16384\n\t"
    "ds_read_b64_tr_b16 v[82:83], %1 offset:16896\n\t"
    "ds_read_b64_tr_b16 v[84:85], %1 offset:20480\n\t"
    "ds_read_b64_tr_b16 v[86:87], %1 offset:20992\n\t"
    "ds_read_b64_tr_b16 v[88:89], %2 offset:16384\n\t"
    "ds_read_b64_tr_b16 v[90:91], %2 offset:16896\n\t"
    "ds_read_b64_tr_b16 v[92:93], %2 offset:20480\n\t"
    "ds_read_b64_tr_b16 v[94:95], %2 offset:20992\n\t"
    "s_waitcnt lgkmcnt(0)"
    : "=&{v[64:95]}"(w) : "v"(a0), "v"(a1) : "memory");
}
template <> __device__ __forceinline__ void qk_wload<1>(u32x32& w, unsigned a0, unsigned a1) {
  asm volatile("ds_read_b64_tr_b16 v[96:97], %1 offset:0\n\t"
    "ds_read_b64_tr_b16 v[98:99], %1 offset:512\n\t"
    "ds_read_b64_tr_b16 v[100:101], %1 offset:4096\n\t"
    "ds_read_b64_tr_b16 v[102:103], %1 offset:4608\n\t"
    "ds_read_b64_tr_b16 v[104:105], %2 offset:0\n\t"
    "ds_read_b64_tr_b16 v[106:107], %2 offset:512\n\t"
    "ds_read_b64_tr_b16 v[108:109], %2 offset:4096\n\t"
    "ds_read_b64_tr_b16 v[110:111], %2 offset:4608\n\t"
    "s_waitcnt lgkmcnt(0)\n\t"
    "ds_read_b64_tr_b16 v[112:113], %1 offset:16384\n\t"
    "ds_read_b64_tr_b16 v[114:115], %1 offset:16896\n\t"
    "ds_read_b64_tr_b16 v[116:117], %1 offset:20480\n\t"
    "ds_read_b64_tr_b16 v[118:119], %1 offset:20992\n\t"
    "ds_read_b64_tr_b16 v[120:121], %2 offset:16384\n\t"
    "ds_read_b64_tr_b16 v[122:123], %2 offset:16896\n\t"
    "ds_read_b64_tr_b16 v[124:125], %2 offset:20480\n\t"
    "ds_read_b64_tr_b16 v[126:127], %2 offset:20992\n\t"
    "s_waitcnt lgkmcnt(0)"
    : "=&{v[96:127]}"(w) : "v"(a0), "v"(a1) : "memory");
}
template <> __device__ __forceinline__ void qk_wload<2>(u32x32& w, unsigned a0, unsigned a1) {
  asm volatile("ds_read_b64_tr_b16 v[128:129], %1 offset:0\n\t"
    "ds_read_b64_tr_b16 v[130:131], %1 offset:512\n\t"
    "ds_read_b64_tr_b16 v[132:133], %1 offset:4096\n\t"
    "ds_read_b64_tr_b16 v[134:135], %1 offset:4608\n\t"
    "ds_read_b64_tr_b16 v[136:137], %2 offset:0\n\t"
    "ds_read_b64_tr_b16 v[138:139], %2 offset:512\n\t"
    "ds_read_b64_tr_b16 v[140:141], %2 offset:4096\n\t"
    "ds_read_b64_tr_b16 v[142:143], %2 offset:4608\n\t"
    "s_waitcnt lgkmcnt(0)\n\t"
    "ds_read_b64_tr_b16 v[144:145], %1 offset:16384\n\t"
    "ds_read_b64_tr_b16 v[146:147], %1 offset:16896\n\t"
    "ds_read_b64_tr_b16 v[148:149], %1 offset:20480\n\t"
    "ds_read_b64_tr_b16 v[150:151], %1 offset:20992\n\t"
    "ds_read_b64_tr_b16 v[152:153], %2 offset:16384\n\t"
    "ds_read_b64_tr_b16 v[154:155], %2 offset:16896\n\t"
    "ds_read_b64_tr_b16 v[156:157], %2 offset:20480\n\t"
    "ds_read_b64_tr_b16 v[158:159], %2 offset:20992\n\t"
    "s_waitcnt lgkmcnt(0)"
    : "=&{v[128:159]}"(w) : "v"(a0), "v"(a1) : "memory");
}
template <> __device__ __forceinline__ void qk_wload<3>(u32x32& w, unsigned a0, unsigned a1) {
  asm volatile("ds_read_b64_tr_b16 v[160:161], %1 offset:0\n\t"
    "ds_read_b64_tr_b16 v[162:163], %1 offset:512\n\t"
    "ds_read_b64_tr_b16 v[164:165], %1 offset:4096\n\t"
    "ds_read_b64_tr_b16 v[166:167], %1 offset:4608\n\t"
    "ds_read_b64_tr_b16 v[168:169], %2 offset:0\n\t"
    "ds_read_b64_tr_b16 v[170:171], %2 offset:512\n\t"
    "ds_read_b64_tr_b16 v[172:173], %2 offset:4096\n\t"
    "ds_read_b64_tr_b16 v[174:175], %2 offset:4608\n\t"
    "s_waitcnt lgkmcnt(0)\n\t"
    "ds_read_b64_tr_b16 v[176:177], %1 offset:16384\n\t"
    "ds_read_b64_tr_b16 v[178:179], %1 offset:16896\n\t"
    "ds_read_b64_tr_b16 v[180:181], %1 offset:20480\n\t"
    "ds_read_b64_tr_b16 v[182:183], %1 offset:20992\n\t"
    "ds_read_b64_tr_b16 v[184:185], %2 offset:16384\n\t"
    "ds_read_b64_tr_b16 v[186:187], %2 offset:16896\n\t"
    "ds_read_b64_tr_b16 v[188:189], %2 offset:20480\n\t"
    "ds_read_b64_tr_b16 v[190:191], %2 offset:20992\n\t"
    "s_waitcnt lgkmcnt(0)"
    : "=&{v[160:191]}"(w) : "v"(a0), "v"(a1) : "memory");
}
template <> __device__ __forceinline__ void qk_wload<4>(u32x32& w, unsigned a0, unsigned a1) {
  asm volatile("ds_read_b64_tr_b16 v[192:193], %1 offset:0\n\t"
    "ds_read_b64_tr_b16 v[194:195], %1 offset:512\n\t"
    "ds_read_b64_tr_b16 v[196:197], %1 offset:4096\n\t"
    "ds_read_b64_tr_b16 v[198:199], %1 offset:4608\n\t"
    "ds_read_b64_tr_b16 v[200:201], %2 offset:0\n\t"
    "ds_read_b64_tr_b16 v[202:203], %2 offset:512\n\t"
    "ds_read_b64_tr_b16 v[204:205], %2 offset:4096\n\t"
    "ds_read_b64_tr_b16 v[206:207], %2 offset:4608\n\t"
    "s_waitcnt lgkmcnt(0)\n\t"
    "ds_read_b64_tr_b16 v[208:209], %1 offset:16384\n\t"
    "ds_read_b64_tr_b16 v[210:211], %1 offset:16896\n\t"
    "ds_read_b64_tr_b16 v[212:213], %1 offset:20480\n\t"
    "ds_read_b64_tr_b16 v[214:215], %1 offset:20992\n\t"
    "ds_read_b64_tr_b16 v[216:217], %2 offset:16384\n\t"
    "ds_read_b64_tr_b16 v[218:219], %2 offset:16896\n\t"
    "ds_read_b64_tr_b16 v[220:221], %2 offset:20480\n\t"
    "ds_read_b64_tr_b16 v[222:223], %2 offset:20992\n\t"
    "s_waitcnt lgkmcnt(0)"
    : "=&{v[192:223]}"(w) : "v"(a0), "v"(a1) : "memory");
}
template <> __device__ __forceinline__ void qk_wload<5>(u32x32& w, unsigned a0, unsigned a1) {
  asm volatile("ds_read_b64_tr_b16 v[224:225], %1 offset:0\n\t"
    "ds_read_b64_tr_b16 v[226:227], %1 offset:512\n\t"
    "ds_read_b64_tr_b16 v[228:229], %1 offset:4096\n\t"
    "ds_read_b64_tr_b16 v[230:231], %1 offset:4608\n\t"
    "ds_read_b64_tr_b16 v[232:233], %2 offset:0\n\t"
    "ds_read_b64_tr_b16 v[234:235], %2 offset:512\n\t"
    "ds_read_b64_tr_b16 v[236:237], %2 offset:4096\n\t"
    "ds_read_b64_tr_b16 v[238:239], %2 offset:4608\n\t"
    "s_waitcnt lgkmcnt(0)\n\t"
    "ds_read_b64_tr_b16 v[240:241], %1 offset:16384\n\t"
    "ds_read_b64_tr_b16 v[242:243], %1 offset:16896\n\t"
    "ds_read_b64_tr_b16 v[244:245], %1 offset:20480\n\t"
    "ds_read_b64_tr_b16 v[246:247], %1 offset:20992\n\t"
    "ds_read_b64_tr_b16 v[248:249], %2 offset:16384\n\t"
    "ds_read_b64_tr_b16 v[250:251], %2 offset:16896\n\t"
    "ds_read_b64_tr_b16 v[252:253], %2 offset:20480\n\t"
    "ds_read_b64_tr_b16 v[254:255], %2 offset:20992\n\t"
    "s_waitcnt lgkmcnt(0)"
    : "=&{v[224:255]}"(w) : "v"(a0), "v"(a1) : "memory");
}
#define QK_OWN 0
template <int I> __device__ __forceinline__ void qk_swap(u32x32& w, unsigned a);
template <> __device__ __forceinline__ void qk_swap<0>(u32x32& w, unsigned a) {
  asm volatile("ds_read_b128 v[8:11], %1 offset:0\n\t"
    "ds_read_b128 v[12:15], %1 offset:1024\n\t"
    "ds_read_b128 v[16:19], %1 offset:8192\n\t"
    "ds_read_b128 v[20:23], %1 offset:9216\n\t"
    "ds_read_b128 v[24:27], %1 offset:16384\n\t"
    "ds_read_b128 v[28:31], %1 offset:17408\n\t"
    "ds_read_b128 v[32:35], %1 offset:24576\n\t"
    "ds_read_b128 v[36:39], %1 offset:25600\n\t"
    "ds_write_b128 %1, v[64:67] offset:0\n\t"
    "ds_write_b128 %1, v[68:71] offset:1024\n\t"
    "ds_write_b128 %1, v[72:75] offset:8192\n\t"
    "ds_write_b128 %1, v[76:79] offset:9216\n\t"
    "ds_write_b128 %1, v[80:83] offset:16384\n\t"
    "ds_write_b128 %1, v[84:87] offset:17408\n\t"
    "ds_write_b128 %1, v[88:91] offset:24576\n\t"
    "ds_write_b128 %1, v[92:95] offset:25600\n\t"
    "s_waitcnt lgkmcnt(0)\n\t"
    "v_mov_b32 v64, v8\n\t"
    "v_mov_b32 v65, v9\n\t"
    "v_mov_b32 v66, v10\n\t"
    "v_mov_b32 v67, v11\n\t"
    "v_mov_b32 v68, v12\n\t"
    "v_mov_b32 v69, v13\n\t"
    "v_mov_b32 v70, v14\n\t"
    "v_mov_b32 v71, v15\n\t"
    "v_mov_b32 v72, v16\n\t"
    "v_mov_b32 v73, v17\n\t"
    "v_mov_b32 v74, v18\n\t"
    "v_mov_b32 v75, v19\n\t"
    "v_mov_b32 v76, v20\n\t"
    "v_mov_b32 v77, v21\n\t"
    "v_mov_b32 v78, v22\n\t"
    "v_mov_b32 v79, v23\n\t"
    "v_mov_b32 v80, v24\n\t"
    "v_mov_b32 v81, v25\n\t"
    "v_mov_b32 v82, v26\n\t"
    "v_mov_b32 v83, v27\n\t"
    "v_mov_b32 v84, v28\n\t"
    "v_mov_b32 v85, v29\n\t"
    "v_mov_b32 v86, v30\n\t"
    "v_mov_b32 v87, v31\n\t"
    "v_mov_b32 v88, v32\n\t"
    "v_mov_b32 v89, v33\n\t"
    "v_mov_b32 v90, v34\n\t"
    "v_mov_b32 v91, v35\n\t"
    "v_mov_b32 v92, v36\n\t"
    "v_mov_b32 v93, v37\n\t"
    "v_mov_b32 v94, v38\n\t"
    "v_mov_b32 v95, v39"
    :: "{v[64:95]}"(w), "v"(a) : "v8", "v9", "v10", "v11", "v12", "v13", "v14", "v15", "v16", "v17", "v18", "v19", "v20", "v21", "v22", "v23", "v24", "v25", "v26", "v27", "v28", "v29", "v30", "v31", "v32", "v33", "v34", "v35", "v36", "v37", "v38", "v39", "memory");
}
template <> __device__ __forceinline__ void qk_swap<1>(u32x32& w, unsigned a) {
  asm volatile("ds_read_b128 v[8:11], %1 offset:0\n\t"
    "ds_read_b128 v[12:15], %1 offset:1024\n\t"
    "ds_read_b128 v[16:19], %1 offset:8192\n\t"
    "ds_read_b128 v[20:23], %1 offset:9216\n\t"
    "ds_read_b128 v[24:27], %1 offset:16384\n\t"
    "ds_read_b128 v[28:31], %1 offset:17408\n\t"
    "ds_read_b128 v[32:35], %1 offset:24576\n\t"
    "ds_read_b128 v[36:39], %1 offset:25600\n\t"
    "ds_write_b128 %1, v[96:99] offset:0\n\t"
    "ds_write_b128 %1, v[100:103] offset:1024\n\t"
    "ds_write_b128 %1, v[104:107] offset:8192\n\t"
    "ds_write_b128 %1, v[108:111] offset:9216\n\t"
    "ds_write_b128 %1, v[112:115] offset:16384\n\t"
    "ds_write_b128 %1, v[116:119] offset:17408\n\t"
    "ds_write_b128 %1, v[120:123] offset:24576\n\t"
    "ds_write_b128 %1, v[124:127] offset:25600\n\t"
    "s_waitcnt lgkmcnt(0)\n\t"
    "v_mov_b32 v96, v8\n\t"
    "v_mov_b32 v97, v9\n\t"
    "v_mov_b32 v98, v10\n\t"
    "v_mov_b32 v99, v11\n\t"
    "v_mov_b32 v100, v12\n\t"
    "v_mov_b32 v101, v13\n\t"
    "v_mov_b32 v102, v14\n\t"
    "v_mov_b32 v103, v15\n\t"
    "v_mov_b32 v104, v16\n\t"
    "v_mov_b32 v105, v17\n\t"
    "v_mov_b32 v106, v18\n\t"
    "v_mov_b32 v107, v19\n\t"
    "v_mov_b32 v108, v20\n\t"
    "v_mov_b32 v109, v21\n\t"
    "v_mov_b32 v110, v22\n\t"
    "v_mov_b32 v111, v23\n\t"
    "v_mov_b32 v112, v24\n\t"
    "v_mov_b32 v113, v25\n\t"
    "v_mov_b32 v114, v26\n\t"
    "v_mov_b32 v115, v27\n\t"
    "v_mov_b32 v116, v28\n\t"
    "v_mov_b32 v117, v29\n\t"
    "v_mov_b32 v118, v30\n\t"
    "v_mov_b32 v119, v31\n\t"
    "v_mov_b32 v120, v32\n\t"
    "v_mov_b32 v121, v33\n\t"
    "v_mov_b32 v122, v34\n\t"
    "v_mov_b32 v123, v35\n\t"
    "v_mov_b32 v124, v36\n\t"
    "v_mov_b32 v125, v37\n\t"
    "v_mov_b32 v126, v38\n\t"
    "v_mov_b32 v127, v39"
    :: "{v[96:127]}"(w), "v"(a) : "v8", "v9", "v10", "v11", "v12", "v13", "v14", "v15", "v16", "v17", "v18", "v19", "v20", "v21", "v22", "v23", "v24", "v25", "v26", "v27", "v28", "v29", "v30", "v31", "v32", "v33", "v34", "v35", "v36", "v37", "v38", "v39", "memory");
}
template <> __device__ __forceinline__ void qk_swap<2>(u32x32& w, unsigned a) {
  asm volatile("ds_read_b128 v[8:11], %1 offset:0\n\t"
    "ds_read_b128 v[12:15], %1 offset:1024\n\t"
    "ds_read_b128 v[16:19], %1 offset:8192\n\t"
    "ds_read_b128 v[20:23], %1 offset:9216\n\t"
    "ds_read_b128 v[24:27], %1 offset:16384\n\t"
    "ds_read_b128 v[28:31], %1 offset:17408\n\t"
    "ds_read_b128 v[32:35], %1 offset:24576\n\t"
    "ds_read_b128 v[36:39], %1 offset:25600\n\t"
    "ds_write_b128 %1, v[128:131] offset:0\n\t"
    "ds_write_b128 %1, v[132:135] offset:1024\n\t"
    "ds_write_b128 %1, v[136:139] offset:8192\n\t"
    "ds_write_b128 %1, v[140:143] offset:9216\n\t"
    "ds_write_b128 %1, v[144:147] offset:16384\n\t"
    "ds_write_b128 %1, v[148:151] offset:17408\n\t"
    "ds_write_b128 %1, v[152:155] offset:24576\n\t"
    "ds_write_b128 %1, v[156:159] offset:25600\n\t"
    "s_waitcnt lgkmcnt(0)\n\t"
    "v_mov_b32 v128, v8\n\t"
    "v_mov_b32 v129, v9\n\t"
    "v_mov_b32 v130, v10\n\t"
    "v_mov_b32 v131, v11\n\t"
    "v_mov_b32 v132, v12\n\t"
    "v_mov_b32 v133, v13\n\t"
    "v_mov_b32 v134, v14\n\t"
    "v_mov_b32 v135, v15\n\t"
    "v_mov_b32 v136, v16\n\t"
    "v_mov_b32 v137, v17\n\t"
    "v_mov_b32 v138, v18\n\t"
    "v_mov_b32 v139, v19\n\t"
    "v_mov_b32 v140, v20\n\t"
    "v_mov_b32 v141, v21\n\t"
    "v_mov_b32 v142, v22\n\t"
    "v_mov_b32 v143, v23\n\t"
    "v_mov_b32 v144, v24\n\t"
    "v_mov_b32 v145, v25\n\t"
    "v_mov_b32 v146, v26\n\t"
    "v_mov_b32 v147, v27\n\t"
    "v_mov_b32 v148, v28\n\t"
    "v_mov_b32 v149, v29\n\t"
    "v_mov_b32 v150, v30\n\t"
    "v_mov_b32 v151, v31\n\t"
    "v_mov_b32 v152, v32\n\t"
    "v_mov_b32 v153, v33\n\t"
    "v_mov_b32 v154, v34\n\t"
    "v_mov_b32 v155, v35\n\t"
    "v_mov_b32 v156, v36\n\t"
    "v_mov_b32 v157, v37\n\t"
    "v_mov_b32 v158, v38\n\t"
    "v_mov_b32 v159, v39"
    :: "{v[128:159]}"(w), "v"(a) : "v8", "v9", "v10", "v11", "v12", "v13", "v14", "v15", "v16", "v17", "v18", "v19", "v20", "v21", "v22", "v23", "v24", "v25", "v26", "v27", "v28", "v29", "v30", "v31", "v32", "v33", "v34", "v35", "v36", "v37", "v38", "v39", "memory");
}
template <> __device__ __forceinline__ void qk_swap<3>(u32x32& w, unsigned a) {
  asm volatile("ds_read_b128 v[8:11], %1 offset:0\n\t"
    "ds_read_b128 v[12:15], %1 offset:1024\n\t"
    "ds_read_b128 v[16:19], %1 offset:8192\n\t"
    "ds_read_b128 v[20:23], %1 offset:9216\n\t"
    "ds_read_b128 v[24:27], %1 offset:16384\n\t"
    "ds_read_b128 v[28:31], %1 offset:17408\n\t"
    "ds_read_b128 v[32:35], %1 offset:24576\n\t"
    "ds_read_b128 v[36:39], %1 offset:25600\n\t"
    "ds_write_b128 %1, v[160:163] offset:0\n\t"
    "ds_write_b128 %1, v[164:167] offset:1024\n\t"
    "ds_write_b128 %1, v[168:171] offset:8192\n\t"
    "ds_write_b128 %1, v[172:175] offset:9216\n\t"
    "ds_write_b128 %1, v[176:179] offset:16384\n\t"
    "ds_write_b128 %1, v[180:183] offset:17408\n\t"
    "ds_write_b128 %1, v[184:187] offset:24576\n\t"
    "ds_write_b128 %1, v[188:191] offset:25600\n\t"
    "s_waitcnt lgkmcnt(0)\n\t"
    "v_mov_b32 v160, v8\n\t"
    "v_mov_b32 v161, v9\n\t"
    "v_mov_b32 v162, v10\n\t"
    "v_mov_b32 v163, v11\n\t"
    "v_mov_b32 v164, v12\n\t"
    "v_mov_b32 v165, v13\n\t"
    "v_mov_b32 v166, v14\n\t"
    "v_mov_b32 v167, v15\n\t"
    "v_mov_b32 v168, v16\n\t"
    "v_mov_b32 v169, v17\n\t"
    "v_mov_b32 v170, v18\n\t"
    "v_mov_b32 v171, v19\n\t"
    "v_mov_b32 v172, v20\n\t"
    "v_mov_b32 v173, v21\n\t"
    "v_mov_b32 v174, v22\n\t"
    "v_mov_b32 v175, v23\n\t"
    "v_mov_b32 v176, v24\n\t"
    "v_mov_b32 v177, v25\n\t"
    "v_mov_b32 v178, v26\n\t"
    "v_mov_b32 v179, v27\n\t"
    "v_mov_b32 v180, v28\n\t"
    "v_mov_b32 v181, v29\n\t"
    "v_mov_b32 v182, v30\n\t"
    "v_mov_b32 v183, v31\n\t"
    "v_mov_b32 v184, v32\n\t"
    "v_mov_b32 v185, v33\n\t"
    "v_mov_b32 v186, v34\n\t"
    "v_mov_b32 v187, v35\n\t"
    "v_mov_b32 v188, v36\n\t"
    "v_mov_b32 v189, v37\n\t"
    "v_mov_b32 v190, v38\n\t"
    "v_mov_b32 v191, v39"
    :: "{v[160:191]}"(w), "v"(a) : "v8", "v9", "v10", "v11", "v12", "v13", "v14", "v15", "v16", "v17", "v18", "v19", "v20", "v21", "v22", "v23", "v24", "v25", "v26", "v27", "v28", "v29", "v30", "v31", "v32", "v33", "v34", "v35", "v36", "v37", "v38", "v39", "memory");
}
template <> __device__ __forceinline__ void qk_swap<4>(u32x32& w, unsigned a) {
  asm volatile("ds_read_b128 v[8:11], %1 offset:0\n\t"
    "ds_read_b128 v[12:15], %1 offset:1024\n\t"
    "ds_read_b128 v[16:19], %1 offset:8192\n\t"
    "ds_read_b128 v[20:23], %1 offset:9216\n\t"
    "ds_read_b128 v[24:27], %1 offset:16384\n\t"
    "ds_read_b128 v[28:31], %1 offset:17408\n\t"
    "ds_read_b128 v[32:35], %1 offset:24576\n\t"
    "ds_read_b128 v[36:39], %1 offset:25600\n\t"
    "ds_write_b128 %1, v[192:195] offset:0\n\t"
    "ds_write_b128 %1, v[196:199] offset:1024\n\t"
    "ds_write_b128 %1, v[200:203] offset:8192\n\t"
    "ds_write_b128 %1, v[204:207] offset:9216\n\t"
    "ds_write_b128 %1, v[208:211] offset:16384\n\t"
    "ds_write_b128 %1, v[212:215] offset:17408\n\t"
    "ds_write_b128 %1, v[216:219] offset:24576\n\t"
    "ds_write_b128 %1, v[220:223] offset:25600\n\t"
    "s_waitcnt lgkmcnt(0)\n\t"
    "v_mov_b32 v192, v8\n\t"
    "v_mov_b32 v193, v9\n\t"
    "v_mov_b32 v194, v10\n\t"
    "v_mov_b32 v195, v11\n\t"
    "v_mov_b32 v196, v12\n\t"
    "v_mov_b32 v197, v13\n\t"
    "v_mov_b32 v198, v14\n\t"
    "v_mov_b32 v199, v15\n\t"
    "v_mov_b32 v200, v16\n\t"
    "v_mov_b32 v201, v17\n\t"
    "v_mov_b32 v202, v18\n\t"
    "v_mov_b32 v203, v19\n\t"
    "v_mov_b32 v204, v20\n\t"
    "v_mov_b32 v205, v21\n\t"
    "v_mov_b32 v206, v22\n\t"
    "v_mov_b32 v207, v23\n\t"
    "v_mov_b32 v208, v24\n\t"
    "v_mov_b32 v209, v25\n\t"
    "v_mov_b32 v210, v26\n\t"
    "v_mov_b32 v211, v27\n\t"
    "v_mov_b32 v212, v28\n\t"
    "v_mov_b32 v213, v29\n\t"
    "v_mov_b32 v214, v30\n\t"
    "v_mov_b32 v215, v31\n\t"
    "v_mov_b32 v216, v32\n\t"
    "v_mov_b32 v217, v33\n\t"
    "v_mov_b32 v218, v34\n\t"
    "v_mov_b32 v219, v35\n\t"
    "v_mov_b32 v220, v36\n\t"
    "v_mov_b32 v221, v37\n\t"
    "v_mov_b32 v222, v38\n\t"
    "v_mov_b32 v223, v39"
    :: "{v[192:223]}"(w), "v"(a) : "v8", "v9", "v10", "v11", "v12", "v13", "v14", "v15", "v16", "v17", "v18", "v19", "v20", "v21", "v22", "v23", "v24", "v25", "v26", "v27", "v28", "v29", "v30", "v31", "v32", "v33", "v34", "v35", "v36", "v37", "v38", "v39", "memory");
}
template <> __device__ __forceinline__ void qk_swap<5>(u32x32& w, unsigned a) {
  asm volatile("ds_read_b128 v[8:11], %1 offset:0\n\t"
    "ds_read_b128 v[12:15], %1 offset:1024\n\t"
    "ds_read_b128 v[16:19], %1 offset:8192\n\t"
    "ds_read_b128 v[20:23], %1 offset:9216\n\t"
    "ds_read_b128 v[24:27], %1 offset:16384\n\t"
    "ds_read_b128 v[28:31], %1 offset:17408\n\t"
    "ds_read_b128 v[32:35], %1 offset:24576\n\t"
    "ds_read_b128 v[36:39], %1 offset:25600\n\t"
    "ds_write_b128 %1, v[224:227] offset:0\n\t"
    "ds_write_b128 %1, v[228:231] offset:1024\n\t"
    "ds_write_b128 %1, v[232:235] offset:8192\n\t"
    "ds_write_b128 %1, v[236:239] offset:9216\n\t"
    "ds_write_b128 %1, v[240:243] offset:16384\n\t"
    "ds_write_b128 %1, v[244:247] offset:17408\n\t"
    "ds_write_b128 %1, v[248:251] offset:24576\n\t"
    "ds_write_b128 %1, v[252:255] offset:25600\n\t"
    "s_waitcnt lgkmcnt(0)\n\t"
    "v_mov_b32 v224, v8\n\t"
    "v_mov_b32 v225, v9\n\t"
    "v_mov_b32 v226, v10\n\t"
    "v_mov_b32 v227, v11\n\t"
    "v_mov_b32 v228, v12\n\t"
    "v_mov_b32 v229, v13\n\t"
    "v_mov_b32 v230, v14\n\t"
    "v_mov_b32 v231, v15\n\t"
    "v_mov_b32 v232, v16\n\t"
    "v_mov_b32 v233, v17\n\t"
    "v_mov_b32 v234, v18\n\t"
    "v_mov_b32 v235, v19\n\t"
    "v_mov_b32 v236, v20\n\t"
    "v_mov_b32 v237, v21\n\t"
    "v_mov_b32 v238, v22\n\t"
    "v_mov_b32 v239, v23\n\t"
    "v_mov_b32 v240, v24\n\t"
    "v_mov_b32 v241, v25\n\t"
    "v_mov_b32 v242, v26\n\t"
    "v_mov_b32 v243, v27\n\t"
    "v_mov_b32 v244, v28\n\t"
    "v_mov_b32 v245, v29\n\t"
    "v_mov_b32 v246, v30\n\t"
    "v_mov_b32 v247, v31\n\t"
    "v_mov_b32 v248, v32\n\t"
    "v_mov_b32 v249, v33\n\t"
    "v_mov_b32 v250, v34\n\t"
    "v_mov_b32 v251, v35\n\t"
    "v_mov_b32 v252, v36\n\t"
    "v_mov_b32 v253, v37\n\t"
    "v_mov_b32 v254, v38\n\t"
    "v_mov_b32 v255, v39"
    :: "{v[224:255]}"(w), "v"(a) : "v8", "v9", "v10", "v11", "v12", "v13", "v14", "v15", "v16", "v17", "v18", "v19", "v20", "v21", "v22", "v23", "v24", "v25", "v26", "v27", "v28", "v29", "v30", "v31", "v32", "v33", "v34", "v35", "v36", "v37", "v38", "v39", "memory");
}
template <int I> __device__ __forceinline__ void qk_swapk(u32x32& w, u32x8& x, unsigned a);
template <> __device__ __forceinline__ void qk_swapk<2>(u32x32& w, u32x8& x, unsigned a) {
  asm volatile("ds_read_b128 v[16:19], %2 offset:0\n\t"
    "ds_read_b128 v[20:23], %2 offset:1024\n\t"
    "ds_read_b128 v[24:27], %2 offset:8192\n\t"
    "ds_read_b128 v[28:31], %2 offset:9216\n\t"
    "ds_read_b128 v[32:35], %2 offset:16384\n\t"
    "ds_read_b128 v[36:39], %2 offset:17408\n\t"
    "ds_write_b128 %2, v[136:139] offset:0\n\t"
    "ds_write_b128 %2, v[140:143] offset:1024\n\t"
    "ds_write_b128 %2, v[144:147] offset:8192\n\t"
    "ds_write_b128 %2, v[148:151] offset:9216\n\t"
    "ds_write_b128 %2, v[152:155] offset:16384\n\t"
    "ds_write_b128 %2, v[156:159] offset:17408\n\t"
    "v_swap_b32 v128, v56\n\t"
    "v_swap_b32 v129, v57\n\t"
    "v_swap_b32 v130, v58\n\t"
    "v_swap_b32 v131, v59\n\t"
    "v_swap_b32 v132, v60\n\t"
    "v_swap_b32 v133, v61\n\t"
    "v_swap_b32 v134, v62\n\t"
    "v_swap_b32 v135, v63\n\t"
    "s_waitcnt lgkmcnt(0)\n\t"
    "v_mov_b32 v136, v16\n\t"
    "v_mov_b32 v137, v17\n\t"
    "v_mov_b32 v138, v18\n\t"
    "v_mov_b32 v139, v19\n\t"
    "v_mov_b32 v140, v20\n\t"
    "v_mov_b32 v141, v21\n\t"
    "v_mov_b32 v142, v22\n\t"
    "v_mov_b32 v143, v23\n\t"
    "v_mov_b32 v144, v24\n\t"
    "v_mov_b32 v145, v25\n\t"
    "v_mov_b32 v146, v26\n\t"
    "v_mov_b32 v147, v27\n\t"
    "v_mov_b32 v148, v28\n\t"
    "v_mov_b32 v149, v29\n\t"
    "v_mov_b32 v150, v30\n\t"
    "v_mov_b32 v151, v31\n\t"
    "v_mov_b32 v152, v32\n\t"
    "v_mov_b32 v153, v33\n\t"
    "v_mov_b32 v154, v34\n\t"
    "v_mov_b32 v155, v35\n\t"
    "v_mov_b32 v156, v36\n\t"
    "v_mov_b32 v157, v37\n\t"
    "v_mov_b32 v158, v38\n\t"
    "v_mov_b32 v159, v39"
    :: "{v[128:159]}"(w), "{v[56:63]}"(x), "v"(a) : "v16", "v17", "v18", "v19", "v20", "v21", "v22", "v23", "v24", "v25", "v26", "v27", "v28", "v29", "v30", "v31", "v32", "v33", "v34", "v35", "v36", "v37", "v38", "v39", "memory");
}
template <> __device__ __forceinline__ void qk_swapk<3>(u32x32& w, u32x8& x, unsigned a) {
  asm volatile("ds_read_b128 v[16:19], %2 offset:0\n\t"
    "ds_read_b128 v[20:23], %2 offset:1024\n\t"
    "ds_read_b128 v[24:27], %2 offset:8192\n\t"
    "ds_read_b128 v[28:31], %2 offset:9216\n\t"
    "ds_read_b128 v[32:35], %2 offset:16384\n\t"
    "ds_read_b128 v[36:39], %2 offset:17408\n\t"
    "ds_write_b128 %2, v[168:171] offset:0\n\t"
    "ds_write_b128 %2, v[172:175] offset:1024\n\t"
    "ds_write_b128 %2, v[176:179] offset:8192\n\t"
    "ds_write_b128 %2, v[180:183] offset:9216\n\t"
    "ds_write_b128 %2, v[184:187] offset:16384\n\t"
    "ds_write_b128 %2, v[188:191] offset:17408\n\t"
    "v_swap_b32 v160, v56\n\t"
    "v_swap_b32 v161, v57\n\t"
    "v_swap_b32 v162, v58\n\t"
    "v_swap_b32 v163, v59\n\t"
    "v_swap_b32 v164, v60\n\t"
    "v_swap_b32 v165, v61\n\t"
    "v_swap_b32 v166, v62\n\t"
    "v_swap_b32 v167, v63\n\t"
    "s_waitcnt lgkmcnt(0)\n\t"
    "v_mov_b32 v168, v16\n\t"
    "v_mov_b32 v169, v17\n\t"
    "v_mov_b32 v170, v18\n\t"
    "v_mov_b32 v171, v19\n\t"
    "v_mov_b32 v172, v20\n\t"
    "v_mov_b32 v173, v21\n\t"
    "v_mov_b32 v174, v22\n\t"
    "v_mov_b32 v175, v23\n\t"
    "v_mov_b32 v176, v24\n\t"
    "v_mov_b32 v177, v25\n\t"
    "v_mov_b32 v178, v26\n\t"
    "v_mov_b32 v179, v27\n\t"
    "v_mov_b32 v180, v28\n\t"
    "v_mov_b32 v181, v29\n\t"
    "v_mov_b32 v182, v30\n\t"
    "v_mov_b32 v183, v31\n\t"
    "v_mov_b32 v184, v32\n\t"
    "v_mov_b32 v185, v33\n\t"
    "v_mov_b32 v186, v34\n\t"
    "v_mov_b32 v187, v35\n\t"
    "v_mov_b32 v188, v36\n\t"
    "v_mov_b32 v189, v37\n\t"
    "v_mov_b32 v190, v38\n\t"
    "v_mov_b32 v191, v39"
    :: "{v[160:191]}"(w), "{v[56:63]}"(x), "v"(a) : "v16", "v17", "v18", "v19", "v20", "v21", "v22", "v23", "v24", "v25", "v26", "v27", "v28", "v29", "v30", "v31", "v32", "v33", "v34", "v35", "v36", "v37", "v38", "v39", "memory");
}
template <> __device__ __forceinline__ void qk_swapk<4>(u32x32& w, u32x8& x, unsigned a) {
  asm volatile("ds_read_b128 v[16:19], %2 offset:0\n\t"
    "ds_read_b128 v[20:23], %2 offset:1024\n\t"
    "ds_read_b128 v[24:27], %2 offset:8192\n\t"
    "ds_read_b128 v[28:31], %2 offset:9216\n\t"
    "ds_read_b128 v[32:35], %2 offset:16384\n\t"
    "ds_read_b128 v[36:39], %2 offset:17408\n\t"
    "ds_write_b128 %2, v[200:203] offset:0\n\t"
    "ds_write_b128 %2, v[204:207] offset:1024\n\t"
    "ds_write_b128 %2, v[208:211] offset:8192\n\t"
    "ds_write_b128 %2, v[212:215] offset:9216\n\t"
    "ds_write_b128 %2, v[216:219] offset:16384\n\t"
    "ds_write_b128 %2, v[220:223] offset:17408\n\t"
    "v_swap_b32 v192, v56\n\t"
    "v_swap_b32 v193, v57\n\t"
    "v_swap_b32 v194, v58\n\t"
    "v_swap_b32 v195, v59\n\t"
    "v_swap_b32 v196, v60\n\t"
    "v_swap_b32 v197, v61\n\t"
    "v_swap_b32 v198, v62\n\t"
    "v_swap_b32 v199, v63\n\t"
    "s_waitcnt lgkmcnt(0)\n\t"
    "v_mov_b32 v200, v16\n\t"
    "v_mov_b32 v201, v17\n\t"
    "v_mov_b32 v202, v18\n\t"
    "v_mov_b32 v203, v19\n\t"
    "v_mov_b32 v204, v20\n\t"
    "v_mov_b32 v205, v21\n\t"
    "v_mov_b32 v206, v22\n\t"
    "v_mov_b32 v207, v23\n\t"
    "v_mov_b32 v208, v24\n\t"
    "v_mov_b32 v209, v25\n\t"
    "v_mov_b32 v210, v26\n\t"
    "v_mov_b32 v211, v27\n\t"
    "v_mov_b32 v212, v28\n\t"
    "v_mov_b32 v213, v29\n\t"
    "v_mov_b32 v214, v30\n\t"
    "v_mov_b32 v215, v31\n\t"
    "v_mov_b32 v216, v32\n\t"
    "v_mov_b32 v217, v33\n\t"
    "v_mov_b32 v218, v34\n\t"
    "v_mov_b32 v219, v35\n\t"
    "v_mov_b32 v220, v36\n\t"
    "v_mov_b32 v221, v37\n\t"
    "v_mov_b32 v222, v38\n\t"
    "v_mov_b32 v223, v39"
    :: "{v[192:223]}"(w), "{v[56:63]}"(x), "v"(a) : "v16", "v17", "v18", "v19", "v20", "v21", "v22", "v23", "v24", "v25", "v26", "v27", "v28", "v29", "v30", "v31", "v32", "v33", "v34", "v35", "v36", "v37", "v38", "v39", "memory");
}
template <> __device__ __forceinline__ void qk_swapk<5>(u32x32& w, u32x8& x, unsigned a) {
  asm volatile("ds_read_b128 v[16:19], %2 offset:0\n\t"
    "ds_read_b128 v[20:23], %2 offset:1024\n\t"
    "ds_read_b128 v[24:27], %2 offset:8192\n\t"
    "ds_read_b128 v[28:31], %2 offset:9216\n\t"
    "ds_read_b128 v[32:35], %2 offset:16384\n\t"
    "ds_read_b128 v[36:39], %2 offset:17408\n\t"
    "ds_write_b128 %2, v[232:235] offset:0\n\t"
    "ds_write_b128 %2, v[236:239] offset:1024\n\t"
    "ds_write_b128 %2, v[240:243] offset:8192\n\t"
    "ds_write_b128 %2, v[244:247] offset:9216\n\t"
    "ds_write_b128 %2, v[248:251] offset:16384\n\t"
    "ds_write_b128 %2, v[252:255] offset:17408\n\t"
    "v_swap_b32 v224, v56\n\t"
    "v_swap_b32 v225, v57\n\t"
    "v_swap_b32 v226, v58\n\t"
    "v_swap_b32 v227, v59\n\t"
    "v_swap_b32 v228, v60\n\t"
    "v_swap_b32 v229, v61\n\t"
    "v_swap_b32 v230, v62\n\t"
    "v_swap_b32 v231, v63\n\t"
    "s_waitcnt lgkmcnt(0)\n\t"
    "v_mov_b32 v232, v16\n\t"
    "v_mov_b32 v233, v17\n\t"
    "v_mov_b32 v234, v18\n\t"
    "v_mov_b32 v235, v19\n\t"
    "v_mov_b32 v236, v20\n\t"
    "v_mov_b32 v237, v21\n\t"
    "v_mov_b32 v238, v22\n\t"
    "v_mov_b32 v239, v23\n\t"
    "v_mov_b32 v240, v24\n\t"
    "v_mov_b32 v241, v25\n\t"
    "v_mov_b32 v242, v26\n\t"
    "v_mov_b32 v243, v27\n\t"
    "v_mov_b32 v244, v28\n\t"
    "v_mov_b32 v245, v29\n\t"
    "v_mov_b32 v246, v30\n\t"
    "v_mov_b32 v247, v31\n\t"
    "v_mov_b32 v248, v32\n\t"
    "v_mov_b32 v249, v33\n\t"
    "v_mov_b32 v250, v34\n\t"
    "v_mov_b32 v251, v35\n\t"
    "v_mov_b32 v252, v36\n\t"
    "v_mov_b32 v253, v37\n\t"
    "v_mov_b32 v254, v38\n\t"
    "v_mov_b32 v255, v39"
    :: "{v[224:255]}"(w), "{v[56:63]}"(x), "v"(a) : "v16", "v17", "v18", "v19", "v20", "v21", "v22", "v23", "v24", "v25", "v26", "v27", "v28", "v29", "v30", "v31", "v32", "v33", "v34", "v35", "v36", "v37", "v38", "v39", "memory");
}
template <int I> __device__ __forceinline__ void vr_wload(u32x32& w, unsigned a);
template <> __device__ __forceinline__ void vr_wload<0>(u32x32& w, unsigned a) {
  asm volatile("ds_read_b64_tr_b16 v[64:65], %1 offset:0\n\t"
    "ds_read_b64_tr_b16 v[66:67], %1 offset:256\n\t"
    "ds_read_b64_tr_b16 v[68:69], %1 offset:2048\n\t"
    "ds_read_b64_tr_b16 v[70:71], %1 offset:2304\n\t"
    "ds_read_b64_tr_b16 v[72:73], %1 offset:32\n\t"
    "ds_read_b64_tr_b16 v[74:75], %1 offset:288\n\t"
    "ds_read_b64_tr_b16 v[76:77], %1 offset:2080\n\t"
    "ds_read_b64_tr_b16 v[78:79], %1 offset:2336\n\t"
    "s_waitcnt lgkmcnt(0)\n\t"
    "ds_read_b64_tr_b16 v[80:81], %1 offset:64\n\t"
    "ds_read_b64_tr_b16 v[82:83], %1 offset:320\n\t"
    "ds_read_b64_tr_b16 v[84:85], %1 offset:2112\n\t"
    "ds_read_b64_tr_b16 v[86:87], %1 offset:2368\n\t"
    "ds_read_b64_tr_b16 v[88:89], %1 offset:96\n\t"
    "ds_read_b64_tr_b16 v[90:91], %1 offset:352\n\t"
    "ds_read_b64_tr_b16 v[92:93], %1 offset:2144\n\t"
    "ds_read_b64_tr_b16 v[94:95], %1 offset:2400\n\t"
    "s_waitcnt lgkmcnt(0)"
    : "=&{v[64:95]}"(w) : "v"(a) : "memory");
}
template <> __device__ __forceinline__ void vr_wload<1>(u32x32& w, unsigned a) {
  asm volatile("ds_read_b64_tr_b16 v[96:97], %1 offset:8192\n\t"
    "ds_read_b64_tr_b16 v[98:99], %1 offset:8448\n\t"
    "ds_read_b64_tr_b16 v[100:101], %1 offset:10240\n\t"
    "ds_read_b64_tr_b16 v[102:103], %1 offset:10496\n\t"
    "ds_read_b64_tr_b16 v[104:105], %1 offset:8224\n\t"
    "ds_read_b64_tr_b16 v[106:107], %1 offset:8480\n\t"
    "ds_read_b64_tr_b16 v[108:109], %1 offset:10272\n\t"
    "ds_read_b64_tr_b16 v[110:111], %1 offset:10528\n\t"
    "s_waitcnt lgkmcnt(0)\n\t"
    "ds_read_b64_tr_b16 v[112:113], %1 offset:8256\n\t"
    "ds_read_b64_tr_b16 v[114:115], %1 offset:8512\n\t"
    "ds_read_b64_tr_b16 v[116:117], %1 offset:10304\n\t"
    "ds_read_b64_tr_b16 v[118:119], %1 offset:10560\n\t"
    "ds_read_b64_tr_b16 v[120:121], %1 offset:8288\n\t"
    "ds_read_b64_tr_b16 v[122:123], %1 offset:8544\n\t"
    "ds_read_b64_tr_b16 v[124:125], %1 offset:10336\n\t"
    "ds_read_b64_tr_b16 v[126:127], %1 offset:10592\n\t"
    "s_waitcnt lgkmcnt(0)"
    : "=&{v[96:127]}"(w) : "v"(a) : "memory");
}
template <> __device__ __forceinline__ void vr_wload<2>(u32x32& w, unsigned a) {
  asm volatile("ds_read_b64_tr_b16 v[128:129], %1 offset:0\n\t"
    "ds_read_b64_tr_b16 v[130:131], %1 offset:128\n\t"
    "ds_read_b64_tr_b16 v[132:133], %1 offset:1024\n\t"
    "ds_read_b64_tr_b16 v[134:135], %1 offset:1152\n\t"
    "ds_read_b64_tr_b16 v[136:137], %1 offset:32\n\t"
    "ds_read_b64_tr_b16 v[138:139], %1 offset:160\n\t"
    "ds_read_b64_tr_b16 v[140:141], %1 offset:1056\n\t"
    "ds_read_b64_tr_b16 v[142:143], %1 offset:1184\n\t"
    "s_waitcnt lgkmcnt(0)\n\t"
    "ds_read_b64_tr_b16 v[144:145], %1 offset:4096\n\t"
    "ds_read_b64_tr_b16 v[146:147], %1 offset:4224\n\t"
    "ds_read_b64_tr_b16 v[148:149], %1 offset:5120\n\t"
    "ds_read_b64_tr_b16 v[150:151], %1 offset:5248\n\t"
    "ds_read_b64_tr_b16 v[152:153], %1 offset:4128\n\t"
    "ds_read_b64_tr_b16 v[154:155], %1 offset:4256\n\t"
    "ds_read_b64_tr_b16 v[156:157], %1 offset:5152\n\t"
    "ds_read_b64_tr_b16 v[158:159], %1 offset:5280\n\t"
    "s_waitcnt lgkmcnt(0)"
    : "=&{v[128:159]}"(w) : "v"(a) : "memory");
}
template <> __device__ __forceinline__ void vr_wload<3>(u32x32& w, unsigned a) {
  asm volatile("ds_read_b64_tr_b16 v[160:161], %1 offset:8192\n\t"
    "ds_read_b64_tr_b16 v[162:163], %1 offset:8320\n\t"
    "ds_read_b64_tr_b16 v[164:165], %1 offset:9216\n\t"
    "ds_read_b64_tr_b16 v[166:167], %1 offset:9344\n\t"
    "ds_read_b64_tr_b16 v[168:169], %1 offset:8224\n\t"
    "ds_read_b64_tr_b16 v[170:171], %1 offset:8352\n\t"
    "ds_read_b64_tr_b16 v[172:173], %1 offset:9248\n\t"
    "ds_read_b64_tr_b16 v[174:175], %1 offset:9376\n\t"
    "s_waitcnt lgkmcnt(0)\n\t"
    "ds_read_b64_tr_b16 v[176:177], %1 offset:12288\n\t"
    "ds_read_b64_tr_b16 v[178:179], %1 offset:12416\n\t"
    "ds_read_b64_tr_b16 v[180:181], %1 offset:13312\n\t"
    "ds_read_b64_tr_b16 v[182:183], %1 offset:13440\n\t"
    "ds_read_b64_tr_b16 v[184:185], %1 offset:12320\n\t"
    "ds_read_b64_tr_b16 v[186:187], %1 offset:12448\n\t"
    "ds_read_b64_tr_b16 v[188:189], %1 offset:13344\n\t"
    "ds_read_b64_tr_b16 v[190:191], %1 offset:13472\n\t"
    "s_waitcnt lgkmcnt(0)"
    : "=&{v[160:191]}"(w) : "v"(a) : "memory");
}
template <> __device__ __forceinline__ void vr_wload<4>(u32x32& w, unsigned a) {
  asm volatile("ds_read_b64_tr_b16 v[192:193], %1 offset:0\n\t"
    "ds_read_b64_tr_b16 v[194:195], %1 offset:128\n\t"
    "ds_read_b64_tr_b16 v[196:197], %1 offset:1024\n\t"
    "ds_read_b64_tr_b16 v[198:199], %1 offset:1152\n\t"
    "ds_read_b64_tr_b16 v[200:201], %1 offset:32\n\t"
    "ds_read_b64_tr_b16 v[202:203], %1 offset:160\n\t"
    "ds_read_b64_tr_b16 v[204:205], %1 offset:1056\n\t"
    "ds_read_b64_tr_b16 v[206:207], %1 offset:1184\n\t"
    "s_waitcnt lgkmcnt(0)\n\t"
    "ds_read_b64_tr_b16 v[208:209], %1 offset:4096\n\t"
    "ds_read_b64_tr_b16 v[210:211], %1 offset:4224\n\t"
    "ds_read_b64_tr_b16 v[212:213], %1 offset:5120\n\t"
    "ds_read_b64_tr_b16 v[214:215], %1 offset:5248\n\t"
    "ds_read_b64_tr_b16 v[216:217], %1 offset:4128\n\t"
    "ds_read_b64_tr_b16 v[218:219], %1 offset:4256\n\t"
    "ds_read_b64_tr_b16 v[220:221], %1 offset:5152\n\t"
    "ds_read_b64_tr_b16 v[222:223], %1 offset:5280\n\t"
    "s_waitcnt lgkmcnt(0)"
    : "=&{v[192:223]}"(w) : "v"(a) : "memory");
}
template <> __device__ __forceinline__ void vr_wload<5>(u32x32& w, unsigned a) {
  asm volatile("ds_read_b64_tr_b16 v[224:225], %1 offset:8192\n\t"
    "ds_read_b64_tr_b16 v[226:227], %1 offset:8320\n\t"
    "ds_read_b64_tr_b16 v[228:229], %1 offset:9216\n\t"
    "ds_read_b64_tr_b16 v[230:231], %1 offset:9344\n\t"
    "ds_read_b64_tr_b16 v[232:233], %1 offset:8224\n\t"
    "ds_read_b64_tr_b16 v[234:235], %1 offset:8352\n\t"
    "ds_read_b64_tr_b16 v[236:237], %1 offset:9248\n\t"
    "ds_read_b64_tr_b16 v[238:239], %1 offset:9376\n\t"
    "s_waitcnt lgkmcnt(0)\n\t"
    "ds_read_b64_tr_b16 v[240:241], %1 offset:12288\n\t"
    "ds_read_b64_tr_b16 v[242:243], %1 offset:12416\n\t"
    "ds_read_b64_tr_b16 v[244:245], %1 offset:13312\n\t"
    "ds_read_b64_tr_b16 v[246:247], %1 offset:13440\n\t"
    "ds_read_b64_tr_b16 v[248:249], %1 offset:12320\n\t"
    "ds_read_b64_tr_b16 v[250:251], %1 offset:12448\n\t"
    "ds_read_b64_tr_b16 v[252:253], %1 offset:13344\n\t"
    "ds_read_b64_tr_b16 v[254:255], %1 offset:13472\n\t"
    "s_waitcnt lgkmcnt(0)"
    : "=&{v[224:255]}"(w) : "v"(a) : "memory");
}
#define OREP 2
#define AREP 2
struct VRW { u32x32 v0, v1, o0, o1, o2, o3; };
__device__ __forceinline__ void vr_v(const VRW& W, const char* ag, const char* vd, unsigned wa, unsigned ra, unsigned& cap, unsigned& sp, unsigned long long& tc, unsigned long long* ts) {
  asm volatile("v_mbcnt_lo_u32_b32 v16, -1, 0\n\t"
    "v_mbcnt_hi_u32_b32 v16, -1, v16\n\t"
    "v_lshlrev_b32 v17, 4, v16\n\t"
    "s_mov_b32 %[sp], 0\n\t"
    "global_load_dwordx4 v[24:27], v17, %[ag] sc1\n\t"
    "global_load_dwordx4 v[28:31], v17, %[ag] offset:1024 sc1\n\t"
    "s_memtime %[tc]\n\t"
    "v_and_b32 v22, 4, v16\n\t"
    "v_mul_u32_u24 v22, 0x2aaa, v22\n\t"
    "v_lshrrev_b32 v18, 1, v16\n\t"
    "v_and_b32 v18, 2, v18\n\t"
    "v_or3_b32 v18, v18, v22, %[c44]\n\t"
    "v_lshl_add_u32 v19, v16, 3, %[wa]\n\t"
    "v_lshl_add_u32 v20, v16, 3, %[ra]\n\t"
    "v_and_b32 v22, 40, v16\n\t"
    "v_lshlrev_b32 v21, 3, v22\n\t"
    "v_and_b32 v22, 3, v16\n\t"
    "v_lshl_or_b32 v21, v22, 4, v21\n\t"
    "v_and_b32 v22, 4, v16\n\t"
    "v_lshl_or_b32 v21, v22, 1, v21\n\t"
    "v_lshrrev_b32 v22, 2, v16\n\t"
    "v_and_or_b32 v21, v22, 4, v21\n\t"
    "3:\n\t"
    "v_mov_b32 v32, 0\n\t"
    "v_mov_b32 v33, 0\n\t"
    "v_mov_b32 v34, 0\n\t"
    "v_mov_b32 v35, 0\n\t"
    "v_mov_b32 v36, 0\n\t"
    "v_mov_b32 v37, 0\n\t"
    "v_mov_b32 v38, 0\n\t"
    "v_mov_b32 v39, 0\n\t"
    "v_mov_b32 v40, 0\n\t"
    "v_mov_b32 v41, 0\n\t"
    "v_mov_b32 v42, 0\n\t"
    "v_mov_b32 v43, 0\n\t"
    "v_mov_b32 v44, 0\n\t"
    "v_mov_b32 v45, 0\n\t"
    "v_mov_b32 v46, 0\n\t"
    "v_mov_b32 v47, 0\n\t"
    "s_waitcnt vmcnt(0) lgkmcnt(0)\n\t"
    "v_smfmac_f32_16x16x64_bf16 v[32:35], v[24:27], v[64:71], v18\n\t"
    "v_smfmac_f32_16x16x64_bf16 v[32:35], v[28:31], v[96:103], v18\n\t"
    "v_max3_u32 v22, v24, v25, v26\n\t"
    "v_max3_u32 v23, v27, v28, v29\n\t"
    "v_smfmac_f32_16x16x64_bf16 v[36:39], v[24:27], v[72:79], v18\n\t"
    "v_max3_u32 v22, v22, v30, v31\n\t"
    "v_max_u32 v22, v22, v23\n\t"
    "v_smfmac_f32_16x16x64_bf16 v[36:39], v[28:31], v[104:111], v18\n\t"
    "v_cmp_eq_u32 vcc, -1, v22\n\t"
    "s_cbranch_vccnz 1f\n\t"
    "2:\n\t"
    "v_smfmac_f32_16x16x64_bf16 v[40:43], v[24:27], v[80:87], v18\n\t"
    "v_permlane16_swap_b32 v32, v34\n\t"
    "v_permlane16_swap_b32 v33, v35\n\t"
    "v_smfmac_f32_16x16x64_bf16 v[40:43], v[28:31], v[112:119], v18\n\t"
    "v_pk_add_f32 v[32:33], v[32:33], v[34:35]\n\t"
    "ds_write_b64 v19, v[32:33] offset:0\n\t"
    "v_smfmac_f32_16x16x64_bf16 v[44:47], v[24:27], v[88:95], v18\n\t"
    "s_nop 0\n\t"
    "v_permlane16_swap_b32 v36, v38\n\t"
    "v_permlane16_swap_b32 v37, v39\n\t"
    "v_smfmac_f32_16x16x64_bf16 v[44:47], v[28:31], v[120:127], v18\n\t"
    "v_pk_add_f32 v[36:37], v[36:37], v[38:39]\n\t"
    "ds_write_b64 v19, v[36:37] offset:512\n\t"
    "v_permlane16_swap_b32 v40, v42\n\t"
    "v_permlane16_swap_b32 v41, v43\n\t"
    "v_pk_add_f32 v[40:41], v[40:41], v[42:43]\n\t"
    "ds_write_b64 v19, v[40:41] offset:1024\n\t"
    "s_nop 1\n\t"
    "v_permlane16_swap_b32 v44, v46\n\t"
    "v_permlane16_swap_b32 v45, v47\n\t"
    "v_pk_add_f32 v[44:45], v[44:45], v[46:47]\n\t"
    "ds_write_b64 v19, v[44:45] offset:1536\n\t"
    "s_cmp_ge_u32 %[wa], 8192\n\t"
    "s_waitcnt lgkmcnt(0)\n\t"
    "s_barrier\n\t"
    "s_cbranch_scc1 9f\n\t"
    "ds_read_b64 v[48:49], v20 offset:0\n\t"
    "ds_read_b64 v[50:51], v20 offset:2048\n\t"
    "ds_read_b64 v[52:53], v20 offset:4096\n\t"
    "ds_read_b64 v[54:55], v20 offset:6144\n\t"
    "ds_read_b64 v[56:57], v20 offset:8192\n\t"
    "ds_read_b64 v[58:59], v20 offset:10240\n\t"
    "ds_read_b64 v[60:61], v20 offset:12288\n\t"
    "ds_read_b64 v[62:63], v20 offset:14336\n\t"
    "s_waitcnt lgkmcnt(6)\n\t"
    "v_pk_add_f32 v[48:49], v[48:49], v[50:51]\n\t"
    "s_waitcnt lgkmcnt(4)\n\t"
    "v_pk_add_f32 v[52:53], v[52:53], v[54:55]\n\t"
    "v_pk_add_f32 v[48:49], v[48:49], v[52:53]\n\t"
    "s_waitcnt lgkmcnt(2)\n\t"
    "v_pk_add_f32 v[56:57], v[56:57], v[58:59]\n\t"
    "s_waitcnt lgkmcnt(0)\n\t"
    "v_pk_add_f32 v[60:61], v[60:61], v[62:63]\n\t"
    "v_pk_add_f32 v[56:57], v[56:57], v[60:61]\n\t"
    "v_pk_add_f32 v[48:49], v[48:49], v[56:57]\n\t"
    "v_cvt_pk_bf16_f32 v22, v48, v49\n\t"
    "v_min_u32 v22, -2, v22\n\t"
    "global_store_dword v21, v22, %[vd] sc1\n\t"
    "s_waitcnt lgkmcnt(0)\n\t"
    "s_branch 9f\n\t"
    "1:\n\t"
    "s_add_u32 %[sp], %[sp], 1\n\t"
    "s_cmp_ge_u32 %[sp], %[cap]\n\t"
    "s_cbranch_scc1 7f\n\t"
    "global_load_dwordx4 v[24:27], v17, %[ag] sc1\n\t"
    "global_load_dwordx4 v[28:31], v17, %[ag] offset:1024 sc1\n\t"
    "s_waitcnt lgkmcnt(0)\n\t"
    "s_memtime %[tc]\n\t"
    "s_nop 7\n\t"
    "s_branch 3b\n\t"
    "7:\n\t"
    "s_mov_b32 %[cap], 0\n\t"
    "s_branch 2b\n\t"
    "9:"
    : [tc] "=&s"(tc), [sp] "=&s"(sp), [cap] "+s"(cap)
    : [ag] "s"(ag), [vd] "s"(vd), [wa] "s"(wa), [ra] "s"(ra), [c44] "s"(0x4444u), "{v[64:95]}"(W.v0), "{v[96:127]}"(W.v1), "{v[128:159]}"(W.o0), "{v[160:191]}"(W.o1), "{v[192:223]}"(W.o2), "{v[224:255]}"(W.o3)
    : "v16", "v17", "v18", "v19", "v20", "v21", "v22", "v23", "v24", "v25", "v26", "v27", "v28", "v29", "v30", "v31", "v32", "v33", "v34", "v35", "v36", "v37", "v38", "v39", "v40", "v41", "v42", "v43", "v44", "v45", "v46", "v47", "v48", "v49", "v50", "v51", "v52", "v53", "v54", "v55", "v56", "v57", "v58", "v59", "v60", "v61", "v62", "v63", "vcc", "scc", "memory");
}
__device__ __forceinline__ void vr_r(const VRW& W, const char* os, const char* ad, unsigned wa, unsigned jx, unsigned& cap, unsigned& sp, unsigned long long* ts) {
  asm volatile("v_mbcnt_lo_u32_b32 v16, -1, 0\n\t"
    "v_mbcnt_hi_u32_b32 v16, -1, v16\n\t"
    "v_lshl_or_b32 v17, v16, 4, %[jx]\n\t"
    "global_load_dwordx4 v[24:27], v17, %[os] sc1\n\t"
    "v_xor_b32 v18, 2048, v17\n\t"
    "global_load_dwordx4 v[28:31], v18, %[os] sc1\n\t"
    "v_xor_b32 v57, 4096, v17\n\t"
    "global_load_dwordx4 v[32:35], v57, %[os] sc1\n\t"
    "v_xor_b32 v58, 6144, v17\n\t"
    "global_load_dwordx4 v[36:39], v58, %[os] sc1\n\t"
    "s_mov_b32 %[sp], 0\n\t"
    "v_and_b32 v23, 4, v16\n\t"
    "v_mul_u32_u24 v23, 0x2aaa, v23\n\t"
    "v_lshrrev_b32 v19, 1, v16\n\t"
    "v_and_b32 v19, 2, v19\n\t"
    "v_or3_b32 v19, v19, v23, %[c44]\n\t"
    "v_and_b32 v23, 15, v16\n\t"
    "v_lshl_add_u32 v20, v23, 2, %[wa]\n\t"
    "v_and_b32 v23, 48, v16\n\t"
    "v_lshl_add_u32 v20, v23, 3, v20\n\t"
    "v_and_b32 v23, 32, v16\n\t"
    "v_and_b32 v21, 28, v16\n\t"
    "v_lshlrev_b32 v21, 4, v21\n\t"
    "v_or_b32 v21, v21, v23\n\t"
    "v_lshlrev_b32 v22, 3, v23\n\t"
    "v_and_b32 v23, 3, v16\n\t"
    "v_lshl_or_b32 v21, v23, 3, v21\n\t"
    "v_and_b32 v23, 31, v16\n\t"
    "v_lshl_or_b32 v22, v23, 2, v22\n\t"
    "3:\n\t"
    "v_mov_b32 v40, 0\n\t"
    "v_mov_b32 v41, 0\n\t"
    "v_mov_b32 v42, 0\n\t"
    "v_mov_b32 v43, 0\n\t"
    "v_mov_b32 v44, 0\n\t"
    "v_mov_b32 v45, 0\n\t"
    "v_mov_b32 v46, 0\n\t"
    "v_mov_b32 v47, 0\n\t"
    "v_mov_b32 v48, 0\n\t"
    "v_mov_b32 v49, 0\n\t"
    "v_mov_b32 v50, 0\n\t"
    "v_mov_b32 v51, 0\n\t"
    "v_mov_b32 v52, 0\n\t"
    "v_mov_b32 v53, 0\n\t"
    "v_mov_b32 v54, 0\n\t"
    "v_mov_b32 v55, 0\n\t"
    "v_mov_b32 v56, 0\n\t"
    "s_waitcnt vmcnt(3)\n\t"
    "v_smfmac_f32_16x16x64_bf16 v[44:47], v[24:27], v[144:151], v19\n\t"
    "v_max3_u32 v56, v56, v24, v25\n\t"
    "v_smfmac_f32_16x16x64_bf16 v[48:51], v[24:27], v[136:143], v19\n\t"
    "v_max3_u32 v56, v56, v26, v27\n\t"
    "v_smfmac_f32_16x16x64_bf16 v[40:43], v[24:27], v[128:135], v19\n\t"
    "v_smfmac_f32_16x16x64_bf16 v[52:55], v[24:27], v[152:159], v19\n\t"
    "s_waitcnt vmcnt(2)\n\t"
    "v_smfmac_f32_16x16x64_bf16 v[44:47], v[28:31], v[176:183], v19\n\t"
    "v_max3_u32 v56, v56, v28, v29\n\t"
    "v_smfmac_f32_16x16x64_bf16 v[48:51], v[28:31], v[168:175], v19\n\t"
    "v_max3_u32 v56, v56, v30, v31\n\t"
    "v_smfmac_f32_16x16x64_bf16 v[40:43], v[28:31], v[160:167], v19\n\t"
    "v_smfmac_f32_16x16x64_bf16 v[52:55], v[28:31], v[184:191], v19\n\t"
    "s_waitcnt vmcnt(1)\n\t"
    "v_smfmac_f32_16x16x64_bf16 v[44:47], v[32:35], v[208:215], v19\n\t"
    "v_max3_u32 v56, v56, v32, v33\n\t"
    "v_smfmac_f32_16x16x64_bf16 v[48:51], v[32:35], v[200:207], v19\n\t"
    "v_max3_u32 v56, v56, v34, v35\n\t"
    "v_smfmac_f32_16x16x64_bf16 v[40:43], v[32:35], v[192:199], v19\n\t"
    "v_smfmac_f32_16x16x64_bf16 v[52:55], v[32:35], v[216:223], v19\n\t"
    "s_waitcnt vmcnt(0)\n\t"
    "v_smfmac_f32_16x16x64_bf16 v[44:47], v[36:39], v[240:247], v19\n\t"
    "v_max3_u32 v56, v56, v36, v37\n\t"
    "v_smfmac_f32_16x16x64_bf16 v[48:51], v[36:39], v[232:239], v19\n\t"
    "v_max3_u32 v56, v56, v38, v39\n\t"
    "v_smfmac_f32_16x16x64_bf16 v[40:43], v[36:39], v[224:231], v19\n\t"
    "v_cmp_eq_u32 vcc, -1, v56\n\t"
    "v_smfmac_f32_16x16x64_bf16 v[52:55], v[36:39], v[248:255], v19\n\t"
    "s_cbranch_vccnz 4f\n\t"
    "5:\n\t"
    "s_nop 2\n\t"
    "v_permlane32_swap_b32 v44, v48\n\t"
    "v_permlane32_swap_b32 v45, v49\n\t"
    "v_permlane32_swap_b32 v46, v50\n\t"
    "v_permlane32_swap_b32 v47, v51\n\t"
    "s_mov_b32 exec_hi, 0\n\t"
    "v_pk_add_f32 v[40:41], v[40:41], v[48:49]\n\t"
    "v_pk_add_f32 v[42:43], v[42:43], v[50:51]\n\t"
    "s_mov_b64 exec, %[hi]\n\t"
    "v_pk_add_f32 v[40:41], v[44:45], v[52:53]\n\t"
    "v_pk_add_f32 v[42:43], v[46:47], v[54:55]\n\t"
    "s_mov_b32 exec_lo, -1\n\t"
    "s_nop 0\n\t"
    "v_permlane16_swap_b32 v40, v42\n\t"
    "v_permlane16_swap_b32 v41, v43\n\t"
    "v_add_f32 v40, v40, v42\n\t"
    "v_add_f32 v41, v41, v43\n\t"
    "ds_write_b32 v20, v40 offset:16384\n\t"
    "ds_write_b32 v20, v41 offset:16448\n\t"
    "s_cmp_ge_u32 %[wa], 1024\n\t"
    "s_waitcnt lgkmcnt(0)\n\t"
    "s_barrier\n\t"
    "s_cbranch_scc1 9f\n\t"
    "ds_read_b64 v[24:25], v21 offset:16384\n\t"
    "ds_read_b64 v[26:27], v21 offset:16896\n\t"
    "ds_read_b64 v[28:29], v21 offset:17408\n\t"
    "ds_read_b64 v[30:31], v21 offset:17920\n\t"
    "ds_read_b64 v[32:33], v21 offset:18432\n\t"
    "ds_read_b64 v[34:35], v21 offset:18944\n\t"
    "ds_read_b64 v[36:37], v21 offset:19456\n\t"
    "ds_read_b64 v[38:39], v21 offset:19968\n\t"
    "s_waitcnt lgkmcnt(6)\n\t"
    "v_pk_add_f32 v[24:25], v[24:25], v[26:27]\n\t"
    "s_waitcnt lgkmcnt(4)\n\t"
    "v_pk_add_f32 v[28:29], v[28:29], v[30:31]\n\t"
    "v_pk_add_f32 v[24:25], v[24:25], v[28:29]\n\t"
    "s_waitcnt lgkmcnt(2)\n\t"
    "v_pk_add_f32 v[32:33], v[32:33], v[34:35]\n\t"
    "s_waitcnt lgkmcnt(0)\n\t"
    "v_pk_add_f32 v[36:37], v[36:37], v[38:39]\n\t"
    "v_pk_add_f32 v[32:33], v[32:33], v[36:37]\n\t"
    "v_pk_add_f32 v[24:25], v[24:25], v[32:33]\n\t"
    "v_cvt_pk_bf16_f32 v23, v24, v25\n\t"
    "v_min_u32 v23, -2, v23\n\t"
    "global_store_dword v22, v23, %[ad] sc1\n\t"
    "s_waitcnt lgkmcnt(0)\n\t"
    "s_branch 9f\n\t"
    "4:\n\t"
    "s_add_u32 %[sp], %[sp], 1\n\t"
    "s_cmp_ge_u32 %[sp], %[cap]\n\t"
    "s_cbranch_scc1 7f\n\t"
    "global_load_dwordx4 v[24:27], v17, %[os] sc1\n\t"
    "global_load_dwordx4 v[28:31], v18, %[os] sc1\n\t"
    "global_load_dwordx4 v[32:35], v57, %[os] sc1\n\t"
    "global_load_dwordx4 v[36:39], v58, %[os] sc1\n\t"
    "s_nop 7\n\t"
    "s_branch 3b\n\t"
    "7:\n\t"
    "s_mov_b32 %[cap], 0\n\t"
    "s_branch 5b\n\t"
    "9:"
    : [sp] "=&s"(sp), [cap] "+s"(cap)
    : [os] "s"(os), [ad] "s"(ad), [wa] "s"(wa), [jx] "s"(jx), [c44] "s"(0x4444u), [hi] "s"(0xFFFFFFFF00000000ull), "{v[64:95]}"(W.v0), "{v[96:127]}"(W.v1), "{v[128:159]}"(W.o0), "{v[160:191]}"(W.o1), "{v[192:223]}"(W.o2), "{v[224:255]}"(W.o3)
    : "v16", "v17", "v18", "v19", "v20", "v21", "v22", "v23", "v24", "v25", "v26", "v27", "v28", "v29", "v30", "v31", "v32", "v33", "v34", "v35", "v36", "v37", "v38", "v39", "v40", "v41", "v42", "v43", "v44", "v45", "v46", "v47", "v48", "v49", "v50", "v51", "v52", "v53", "v54", "v55", "v56", "v57", "v58", "v59", "v60", "v61", "v62", "v63", "vcc", "scc", "memory");
}
__device__ __forceinline__ void qk_stage(const char* wq, const char* wk, int u, int wave) {
  stage_proj((u & 1) ? wk + (size_t)(((u >> 1) + 2) & 7) * 128 * HD * 2 : wq + (size_t)(u >> 1) * 128 * HD * 2, ((u + 4) % 5) * 32768, wave * 64 + lane_id(), wave);
}
__device__ __forceinline__ void vr_first(const u16* Wv, int r, int wave) {
  const char* sv_ = uni((const char*)(Wv + (size_t)(128 * wave) * HD + 64 * r));
  const int lane = lane_id();
#pragma unroll
  for (int i = 0; i < 16; ++i) gldsw(sv_ + (size_t)i * 8 * HD * 2, (unsigned)((lane >> 3) * HD * 2 + (lane & 7) * 16), wave * 16384 + i * 1024);
}
__device__ __forceinline__ void qk_body(char* smem, int wave_id_, int xg, int h, int wg, const u16* __restrict__ Wq, const u16* __restrict__ Wk,
                                        const char* __restrict__ Kx, const char* __restrict__ V, char* ws, unsigned* tmo, u64 tag) {
  const int wave = WAVE_ID;
  const int ty = wave >> 2, w4 = wave & 3;
  const char* wq = uni((const char*)(Wq + h * DH)); const char* wk = uni((const char*)(Wk + h * DH));
  auto stage = [&](int u) { qk_stage(wq, wk, u, wave); };
  QKW W;
  if (wave == 0) STS(ws, wg, 7);
  if (!SPRO) { stage(0); stage(1); stage(2); stage(3); }
#define WLD(i, w) do { WAIT_V(12); WBAR(); stage(2 * (i) + 4); if (ty) { WAIT_V(12); WBAR(); stage(2 * (i) + 5); } \
    { const int l_ = lane_id(); const unsigned o_ = ty ? ((2 * (i) + 5) % 5) * 32768 : ((2 * (i) + 4) % 5) * 32768; qk_wload<i>(w, proj_base(2 * w4, l_) + o_, proj_base(2 * w4 + 1, l_) + o_); } \
    if (!ty) { WAIT_V(12); WBAR(); stage(2 * (i) + 5); } if (wave == 0) STS(ws, wg, 1 + (i)); } while (0)
  WLD(0, W.w0); WLD(1, W.w1); WLD(2, W.w2); WLD(3, W.w3); WLD(4, W.w4); WLD(5, W.w5);
#undef WLD
  unsigned in = 0, avail = 0;
#pragma unroll
  for (int i = 0; i < 2; ++i) {
    if (i == 0) WAIT_V(8); else WAIT_V(0);
    WBAR();
    if (i == 0 && wave == 1) init_issue(ws, Q_MEM);
    if (i == 1) in = init_get(ws, tag, smem + Q_MEM);
    if (EKV && i == 1 && wave == 6)
      asm volatile("s_mov_b32 m0, %0\n\ts_nop 4\n\tglobal_load_lds_dword %1, %2 sc1" :: "s"(Q_K), "v"((unsigned)((PQKV + h) * 128 + (lane_id() & 15) * 4)), "s"(cnt_of(ws, in)) : "memory", "m0");
    const int lane = lane_id();
    const unsigned o = Q_RING + ty * 32768 + i * 65536, f0 = proj_base(2 * w4, lane) + o, f1 = proj_base(2 * w4 + 1, lane) + o;
    u32x8 x[4] = {proj_frag(smem, f0), proj_frag(smem, f1), proj_frag(smem, f0 + 16384), proj_frag(smem, f1 + 16384)};
    asm volatile("" : "+v"(x[0]), "+v"(x[1]), "+v"(x[2]), "+v"(x[3]));
    BAR();
    char* d = smem + Q_CB + i * 57344 + ty * (i ? 32768 : 24576) + w4 * 2048 + lane * 16;
#pragma unroll
    for (int j = 0; j < 4; ++j)
      if (i || j || !ty) { *(u32x4*)(d + j * 8192) = u32x4{x[j][0], x[j][1], x[j][2], x[j][3]}; *(u32x4*)(d + j * 8192 + 1024) = u32x4{x[j][4], x[j][5], x[j][6], x[j][7]}; }
    if (i == 0 && ty) { *(u32x4*)(smem + Q_MEM + wave * 2048 + lane * 16) = u32x4{x[0][0], x[0][1], x[0][2], x[0][3]}; *(u32x4*)(smem + Q_MEM + wave * 2048 + 1024 + lane * 16) = u32x4{x[0][4], x[0][5], x[0][6], x[0][7]}; }
  }
  if (wave == 4) *(volatile LAS u32x2*)(smem + Q_TM) = u32x2{0u, 0u};
  BAR();
  if (wave == 0) STS(ws, wg, 0);
  unsigned* cnt = cnt_of(ws, in);
  const char* xb = uni(xch_base(ws) + ((size_t)3 << 21) + (size_t)((in & 2u) << 12));
  const char* oh = uni(xch_base(ws) + (size_t)((in & 1u) << 13) + (size_t)(h >> 2) * 16384 + (h & 3) * 2048);
  auto rearm = [&](int ps) {
    unsigned z = SENT; asm volatile("" : "+v"(z));
    const unsigned l16 = lane_id() * 16;
#pragma unroll
    for (int r = 0; r < 2 * OREP; ++r) st16s(oh + (size_t)(ps * 4 + (r >> 1)) * 131072, (r & 1) * 1024u + l16, u32x4{z, z, z, z});
  };
  const char* dbw = xb + (size_t)48 * 16384 + 4096 + h * 128;
  auto stage_kv = [&](int s) {
    if (avail <= (unsigned)s) {
      for (int spins = 0;; ++spins) {
        const unsigned p = ld4_sc1(cnt + (PQKV + h) * 32 + (lane_id() & 15));
        avail = min(qkv_avail(p, 1), qkv_avail(p, 2));
        if (avail > (unsigned)s) break;
        __builtin_amdgcn_s_sleep(8);
        if (give_up(spins, tmo, 5)) break;
      }
    }
    const char* ks = uni(Kx + ((size_t)s * NH + h) * 8192); const char* vs = uni(V + ((size_t)s * NH + h) * 8192);
    const int lane = lane_id();
    head_to_lds(ks, lane, Q_K); head_to_lds(vs, lane, Q_V);
  };
  auto loop = [&](auto w_c) __attribute__((always_inline)) {
    constexpr int WV = decltype(w_c)::value;
    if (WV >= 4) asm volatile("ds_read_b128 v[56:59], %1\n\tds_read_b128 v[60:63], %1 offset:1024\n\ts_waitcnt lgkmcnt(0)" : "=&{v[56:63]}"(W.x) : "v"(Q_MEM + WV * 2048 + lane_id() * 16) : "memory");
    if (WV == 6) {
      if (EKV) { WAIT_V(0); const unsigned p = *(volatile LAS unsigned*)(smem + Q_K + lane_id() * 4); avail = min(qkv_avail(p, 1), qkv_avail(p, 2)); }
      stage_kv(0);
    }
    if (QK_OWN) {
      constexpr int PI = WV < 4 ? WV : WV - 2;
      if constexpr (QK_OWN == 3 && WV >= 4) qk_swapk<PI>(PI == 2 ? W.w2 : PI == 3 ? W.w3 : PI == 4 ? W.w4 : W.w5, W.x, Q_CB + 32768 + (WV & 3) * 2048 + lane_id() * 16);
      else qk_swap<PI>(PI == 0 ? W.w0 : PI == 1 ? W.w1 : PI == 2 ? W.w2 : PI == 3 ? W.w3 : PI == 4 ? W.w4 : W.w5, Q_CB + 57344 + (WV >> 2) * 32768 + (WV & 3) * 2048 + lane_id() * 16);
    }
    if (WV < 4) __builtin_amdgcn_s_setprio(2);
    unsigned t0 = 0, cap = 60000, d0 = 105, d1 = 200;
    int dq = D_Q;
    int sl = 0;
    for (int s = 0; s < NSEG; ++s) {
      TSDECL;
      const char* ag = xb + (size_t)((sl * 4 + (AREP == 4 ? xg : (AREP == 2 ? ARSEL : 0))) * 2 + (WV >> 2)) * 16384 + (WV & 3) * 2048;
      const char* vs = WV == 4 ? dbw : WV == 5 ? ws + OFF_KVM + ((size_t)s * NH + h) * 4096 : xb + (size_t)(24 + sl * 8 + (h >> 2)) * 16384 + (h & 3) * 2048;
      const char* od = WV == 4 ? (const char*)(size_t)((unsigned)(s + 1) << 24) : oh + (size_t)sl * 524288;
      if (s > 0) wait_rt(t0, dq);
      TS(0);
      unsigned long long tc; unsigned sp;
      qk_step<WV>(W, ag, vs, od, LAST && s == NSEG - 1 ? nullptr : ws + OFF_KVM + ((size_t)s * NH + h) * 4096 + 2048, cap, sp, tc, nullptr, d0, d1);
      dq = (sp & 0xFFFFu) ? min(dq + DQU, D_Q + DQM) : max(dq - DQD, D_Q);
      if ((WV < 4 || WV == 5) && !(LAST && s == NSEG - 1)) {
        const unsigned pv = (unsigned)(s + 1) << 24 | (rt_now() & 0xFFFFFFu);
        WAIT_V(0);
        if (lane_id() == 0) st4_sc1(cnt + (PROG + h) * 32 + WV, pv);
      }
      if (WV == 6 && s + 1 < NSEG - 1) stage_kv(s + 1);
      if (WV == 7 && s > 0 && s < NSEG - 1) rearm(sl == 0 ? 2 : sl - 1);
      if (WV == 7 && s >= 8 && s < 8 + NSEG - SDIR) {
        unsigned z = SENT; asm volatile("" : "+v"(z));
        const char* kd = ws + OFF_KVM + ((size_t)(s - 8 + SDIR) * NH + h) * 4096;
#pragma unroll
        for (int r = 0; r < 4; ++r) st16s(kd, r * 1024u + lane_id() * 16, u32x4{z, z, z, z});
      }
      if (s < NSEG - 1) {
        if (WV == 4) {
          delay64<E_B>();
          t0 = bell_time(xb + (size_t)(48 + xg) * 16384 + 4096, (lane_id() & 31) * 128, (unsigned)(s + 1), cap);
          *(volatile LAS u32x2*)(smem + Q_TM) = u32x2{(unsigned)(s + 1), t0};
        } else {
          for (;;) {
            const u32x2 v = *(volatile LAS u32x2*)(smem + Q_TM);
            t0 = __builtin_amdgcn_readfirstlane(v.y);
            if (__builtin_amdgcn_readfirstlane(v.x) == (unsigned)(s + 1)) break;
            __builtin_amdgcn_s_sleep(1);
          }
        }
      }
      sl = sl == 2 ? 0 : sl + 1;
    }
    if (cap == 0 && lane_id() == 0) st4_sc1(tmo, 1u);
  };
  switch (wave) {
    case 0: loop(std::integral_constant<int, 0>{}); break;
    case 1: loop(std::integral_constant<int, 1>{}); break;
    case 2: loop(std::integral_constant<int, 2>{}); break;
    case 3: loop(std::integral_constant<int, 3>{}); break;
    case 4: loop(std::integral_constant<int, 4>{}); break;
    case 5: loop(std::integral_constant<int, 5>{}); break;
    case 6: loop(std::integral_constant<int, 6>{}); break;
    default: loop(std::integral_constant<int, 7>{}); break;
  }
  __builtin_amdgcn_s_setprio(0);
}

__device__ __forceinline__ unsigned vr_body(char* smem, int wave_id_, int xg, int h, int c, int r, int wg, const u16* __restrict__ Wv, const u16* __restrict__ Wo,
                                        const u16* __restrict__ Wq, const u16* __restrict__ Wk, char* ws, unsigned* tmo, u64 tag, const char* code) {
  const int wave = WAVE_ID;
  const unsigned po = (unsigned)(r >> 5) * 16384u + ((r >> 2) & 7) * 1024 + (r & 1) * 512 + ((r >> 1) & 1) * 128;
  const unsigned vo_ = (unsigned)(24 + (h >> 2)) * 16384u + (h & 3) * 2048 + c * 1024;
  const int hg = (wave + ROTW * (r & 3)) & 7;
  const int jr = ROTH ? r & 3 : 0;
  VRW W;
  {
    const int lane = lane_id();
    const int g = lane >> 4, q4 = (lane & 15) >> 2, p4 = lane & 3, fr = 4 * g + (q4 & 1) + 32 * (q4 >> 1);
    const unsigned lw = wave * 16384;
    const char* sv_ = uni((const char*)(Wv + (size_t)(128 * wave) * HD + h * DH + 64 * c));
    const char* so = uni((const char*)(Wo + (size_t)(512 * hg) * DM + 64 * (r >> 2) + 16 * (r & 1)));
    const unsigned vo = (unsigned)((32 * (lane >> 5) + ((lane >> 2) & 7)) * DM * 2 + ((lane >> 1) & 1) * 64 + (lane & 1) * 16);
    auto issue = [&](int p) {
#pragma unroll
      for (int i = 0; i < 8; ++i) {
        if (p >= 2 && (NTM & 8)) gldsw(so + (size_t)(128 * ((p - 2) ^ jr) + 64 * (i & 1) + 8 * (i >> 1)) * DM * 2, vo, lw + (p & 1) * 8192 + i * 1024);
        else if (p >= 2) glds16s<0>(so + (size_t)(128 * ((p - 2) ^ jr) + 64 * (i & 1) + 8 * (i >> 1)) * DM * 2, vo, lw + (p & 1) * 8192 + i * 1024);
        else gldsw(sv_ + (size_t)(8 * p + i) * 8 * HD * 2, (unsigned)((lane >> 3) * HD * 2 + (lane & 7) * 16), lw + (p & 1) * 8192 + i * 1024);
      }
    };
#define VTS(p) do { } while (0)
    if (wave == 0) STS(ws, wg, 7);
    if (!SPRO) { issue(0); issue(1); }
    const unsigned av = lw + (unsigned)(fr * 128 + p4 * 8), ao = lw + (unsigned)(fr * 64 + p4 * 8);
    WAIT_V(8); VTS(0); vr_wload<0>(W.v0, av); issue(2);
    WAIT_V(8); VTS(1); vr_wload<1>(W.v1, av); issue(3);
    WAIT_V(8); VTS(2); vr_wload<2>(W.o0, ao); issue(4);
    WAIT_V(8); vr_wload<3>(W.o1, ao); issue(5);
    if (wave == 0) init_issue(ws, 131072);
    WAIT_V(8); vr_wload<4>(W.o2, ao);
    WAIT_V(0); VTS(3);
    if (VHN) {
      const int rl = r - 20 * xg, hn = ((xg == 1 ? rl : rl - (rl >= (xg ? 8 : 16) ? 4 : 0)) * 8 + wave) * (6 * VHN) + (lane >> 1);
      const int pm = hn / 384, hr = hn - pm * 384, p = 8 - VHN + (pm >> 1), hh = 12 * xg + 1 + 4 * (hr >> 7);
      const u16* hp = ((pm & 1) ? Wk : Wq) + (size_t)(((pm & 1) ? (p + 2) & 7 : p) * 128 + (hr & 127)) * HD + min(hh, 31) * DH + (lane & 1) * 64;
      if (((r >> 2) & 7) != 3 && lane < 12 * VHN && pm < 2 * VHN) asm volatile("s_mov_b32 m0, %0\n\ts_nop 0\n\tglobal_load_lds_dword %1, off" :: "s"(155648 + wave * 256), "v"(hp) : "memory", "m0");
    }
    vr_wload<5>(W.o3, ao);
#undef VTS
  }
  if (wave == 0) STS(ws, wg, 5);
  if (wave == 0) STS(ws, wg, 6);
  BAR();
  if (wave == 0) STS(ws, wg, 0);
  const unsigned in = init_get(ws, tag, smem + 131072);
  const char* xb = uni(xch_base(ws) + ((size_t)3 << 21) + (size_t)((in & 2u) << 12));
  const char* ob = uni(xch_base(ws) + (size_t)((in & 1u) << 13));
  auto rearm = [&](int ps, bool ag) {
    const int lane = lane_id();
    unsigned z = SENT; asm volatile("" : "+v"(z));
    const u32x4 sv = u32x4{z, z, z, z};
    if (ag && lane < 16 * AREP) st16s(xb, (unsigned)(ps * 8 + (lane >> 4) * 2) * 16384u + po + ((lane >> 3) & 1) * 256 + (lane & 7) * 16, sv);
    st16s(xb, (unsigned)ps * 131072u + vo_ + lane * 16, sv);
  };
  __builtin_amdgcn_s_setprio(2);
  unsigned cap = 60000;
  int sl = 0;
  for (int s = 0; s < NSEG; ++s) {
    TSDECL;
    const int sn = sl == 2 ? 0 : sl + 1;
    const char* ag = xb + (size_t)((sl * 4 + (AREP == 4 ? xg : (AREP == 2 ? ARSEL : 0))) * 2 + (wave >> 2)) * 16384 + (wave & 3) * 2048;
    const char* vd = (LAST && s == NSEG - 1 ? ws + OFF_KVM + ((size_t)s * NH + h) * 4096 + 2048 + c * 1024 : xb + (size_t)sl * 131072 + vo_) + (wave & 2) * 256 + (wave & 1) * 128;
    const char* os = ob + (size_t)((sl * 4 + (OREP == 4 ? xg : ORSEL)) * 8 + hg) * 16384 + ((r >> 1) & 1) * 1024;
    const char* ad = xb + (size_t)((sn * 4 + (wave & 3)) * 2) * 16384 + po;
    if (s > 0) delay64<E_AG>();
    TS(0);
    unsigned long long tc; unsigned sp;
    unsigned long long* tsp = nullptr;
    vr_v(W, ag, vd, wave * 2048, (wave & 3) * 512, cap, sp, tc, tsp);
    TN(12, sp + 1);
    if (s == NSEG - 1) break;
    if (wave == 7 && s > 0) rearm(sl == 0 ? 2 : sl - 1, true);
    if (wave == 6 && s == NSEG - 4 && code) touch(code, (unsigned)lane_id() * 128, 155648);
    wait_clk((unsigned)tc, E_O);
    TS(3);
    const unsigned tb = bell_time(xb + (size_t)(48 + xg) * 16384 + hg * 512, (lane_id() & 3) * 128, (unsigned)(s + 1), cap);
    wait_rt(tb, D_O);
    TS(4);
    vr_r(W, os, ad, wave * 512, jr * 2048, cap, sp, tsp);
    TN(14, (sp & 0xFFFF) + 1);
    if (wave == 1) TSFLUSH(0x6078);
    sl = sn;
  }
  __builtin_amdgcn_s_setprio(0);
  if (cap == 0 && lane_id() == 0) st4_sc1(tmo, 2u);
  return in;
}
struct ModIn { u32x4 w[8]; unsigned cv; };
__device__ __forceinline__ void mod_issue(ModIn& I, int wave_id_, int j, const u16* __restrict__ c, const u16* __restrict__ wmod) {
  const int tid = opaque_tid(), k0 = (j >> 5) * 512 + (tid >> 3);
  I.cv = c[k0 + 64 * (tid & 7)];
#pragma unroll
  for (int i = 0; i < 8; ++i) I.w[i] = ldnt<1>(wmod + (size_t)(k0 + 64 * i) * (6 * DM) + (j & 31) * 64 + (tid & 7) * 8);
}
__device__ __forceinline__ void mod_job(char* smem, int wave_id_, int j, u64 tag, const ModIn& I, char* ws) {
  const int tid = opaque_tid(), lane = tid & 63, wave = WAVE_ID, cg = tid & 7;
  const float cv = bflo(I.cv), sl = bfr(cv / (1.0f + __expf(-cv)));
  float acc[8] = {0, 0, 0, 0, 0, 0, 0, 0};
#pragma unroll
  for (int i = 0; i < 8; ++i) {
    const float sv = __shfl(sl, (lane & 56) + i);
#pragma unroll
    for (int e = 0; e < 4; ++e) { acc[2 * e] += sv * bflo(I.w[i][e]); acc[2 * e + 1] += sv * bfhi(I.w[i][e]); }
  }
#pragma unroll
  for (int e = 0; e < 8; ++e) acc[e] = red16_32_add(add_ror8(acc[e]));
  float* red = (float*)(smem + L_GB);
  if (lane < 8) { *(f32x4*)(red + wave * 64 + cg * 8) = f32x4{acc[0], acc[1], acc[2], acc[3]}; *(f32x4*)(red + wave * 64 + cg * 8 + 4) = f32x4{acc[4], acc[5], acc[6], acc[7]}; }
  __syncthreads();
  if (tid < 32) {
    f32x2 s = f32x2{0, 0};
#pragma unroll
    for (int r = 0; r < 8; ++r) s += *(const f32x2*)(red + r * 64 + tid * 2);
    st16_sc1(ws + OFF_MOD + (j * 32 + tid) * 16, u32x4{__float_as_uint(s[0]), (unsigned)tag, __float_as_uint(s[1]), tag2(tag)});
  }
  BAR();
}
#define NIMM 3
constexpr unsigned TK(int m, int hl, int part, int bo, int be) { return (unsigned)(m | hl << 2 | part << 5 | bo << 8 | be << 16); }
#define QX 30
#define CA 36
#define CB 42
#define C2 46
constexpr unsigned task_tab(int t, int it) {
  return t >= 8 && t < 13 ? (it == 0 ? TK(1, t - 8, 0, 0, 64) : it == 1 ? (t < 12 ? TK(2, t - 4, 1, C2, 64) : TK(1, 5, 1, C2, 64)) : 0)
       : it > 0 ? 0
       : t < 8 ? TK(0, t, 1, QX, 64)
       : t < 16 ? TK(1, t - 8, 0, 0, C2)
       : t < 24 ? TK(2, t - 16, 0, 0, t < 18 ? CA : (t < 20 ? CB : C2))
       : t < 26 ? TK(2, t - 24, 1, CA, 64)
       : t == 26 ? TK(1, 6, 1, C2, (C2 + 64) / 2) : t == 27 ? TK(1, 6, 2, (C2 + 64) / 2, 64)
       : t < 30 ? TK(2, t - 26, 1, CB, 64)
       : t == 30 ? TK(1, 7, 1, C2, (C2 + 64) / 2) : t == 31 ? TK(1, 7, 2, (C2 + 64) / 2, 64)
       : TK(0, t - 32, 0, 0, QX);
}
template <int IT, int I = 0> __device__ __forceinline__ unsigned task_sel(int t) {
  if constexpr (I == 40) return 0u;
  else { unsigned r = task_sel<IT, I + 1>(t); constexpr unsigned c = task_tab(I, IT); if (c) asm volatile("s_cmp_eq_u32 %1, %2\n\ts_cselect_b32 %0, %3, %0" : "+s"(r) : "s"(t), "i"(I), "i"(c) : "scc"); return r; }
}
__global__ void __launch_bounds__(512, 2) k_mega(const u16* __restrict__ Wq, const u16* __restrict__ Wk, const u16* __restrict__ Wv,
                                                 const u16* __restrict__ Wo, const u16* __restrict__ w1, const u16* __restrict__ w3,
                                                 const u16* __restrict__ w2, const u16* __restrict__ x, const u16* __restrict__ cvec,
                                                 const u16* __restrict__ wmod, const u16* __restrict__ mem0, char* ws, u16* out) {
  extern __shared__ __attribute__((aligned(16))) char smem[];
  int wave_id_ = __builtin_amdgcn_readfirstlane((int)threadIdx.x >> 6);
  unsigned bid = blockIdx.x;
  asm volatile("" : "+s"(wave_id_), "+s"(bid));
  unsigned* tmo = (unsigned*)(ws + OFF_TMO);
  const unsigned tst = (unsigned)__builtin_amdgcn_s_memrealtime();
  unsigned long long* mts = nullptr;
  const float* mod = (const float*)(ws + OFF_MOD);
  volatile LAS unsigned* tslot = (volatile LAS unsigned*)(smem + L_SLOT + 768);
#define CPF 131072
  u64 pc; asm volatile("s_getpc_b64 %0" : "=s"(pc));
  const u64 pc0 = pc & ~(u64)127;
  if (CPF && wave_id_ == 1) {
    const unsigned o = (((bid >> 3) & 31) * 64 + lane_id()) * 64;
    if (o < CPF) asm volatile("s_mov_b32 m0, %0\n\ts_nop 0\n\tglobal_load_lds_dword %1, %2" :: "s"(155648 + 1024), "v"(o), "s"(pc & ~(u64)127) : "memory", "m0");
  }
  unsigned xcc; asm volatile("s_getreg_b32 %0, hwreg(HW_REG_XCC_ID)" : "=s"(xcc));
  xcc &= 7;
  const unsigned pred = xcc * 32 + ((bid >> 3) & 31);
  ModIn mi;
  auto modj = [](unsigned sl) { return sl >= 128 && ((sl + 8) & 31) < 16 ? (int)(((sl >> 5) & 3) * 16 + (sl & 15)) : -1; };
  if (modj(pred) >= 0) mod_issue(mi, wave_id_, modj(pred), cvec, wmod);
  u64 tag;
  asm volatile("s_load_dwordx2 %0, %1, 0x0\n\ts_waitcnt lgkmcnt(0)" : "=s"(tag) : "s"(ws + OFF_TAG) : "memory");
  tag += 1;
  auto first = [&](unsigned sl) __attribute__((always_inline)) {
    const int g_ = sl >> 5, t_ = sl & 31, nq_ = g_ == 2 ? 8 : 12;
    if (g_ < 3 && t_ < nq_) {
      const char* wq = uni((const char*)(Wq + (g_ * 12 + t_) * DH)); const char* wk = uni((const char*)(Wk + (g_ * 12 + t_) * DH));
#pragma unroll
      for (int u = 0; u < 4; ++u) qk_stage(wq, wk, u, wave_id_);
    } else if (g_ < 3) vr_first(Wv, g_ * 20 + t_ - nq_, wave_id_);
    else if (g_ > 3 && g_ < 8 && t_ >= 8 && t_ < 24) {
      u64 pk = (u64)Wk, pv = (u64)Wv; asm volatile("" : "+s"(pk), "+s"(pv));
      u32x32 none[4];
      wload<R_QKV, 2>(smem, wave_id_, (const u16*)(t_ < 16 ? pk : pv) + (8 * (g_ & 3) + (t_ & 7)) * DH, nullptr, HD, none, 0, 1, [](int) {});
    }
  };
  if (pred < 72 && (pred & 31) < 12) tslot = (volatile LAS unsigned*)(smem + 100000);
  int slot;
  for (unsigned sf = pred, rep = 0;; ++rep) {
    if (rep == 0 && opaque_tid() == 0) {
      unsigned sl = pred;
      const unsigned s0 = sl;
      for (unsigned i = 1; i <= 256; ++i) {
        if (__hip_atomic_exchange((u64*)(ws + OFF_CLAIM + sl * 128), tag, __ATOMIC_RELAXED, __HIP_MEMORY_SCOPE_AGENT) != tag) break;
        sl = i == 256 ? 0xFFFFu : (((s0 + (i & ~31u)) & 0xE0u) | ((s0 + i) & 31u));
      }
      *tslot = sl;
    }
    if (SPRO) first(sf);
    if (rep) break;
    BAR();
    slot = (int)__builtin_amdgcn_readfirstlane(*tslot);
    if (slot != (int)pred) WAIT_V(0);
    BAR();
    if (slot == (int)pred) break;
    sf = (unsigned)slot;
  }
  if (slot >= 256) return;
  if (modj(slot) >= 0) {
    if (slot != (int)pred) mod_issue(mi, wave_id_, modj(slot), cvec, wmod);
    mod_job(smem, wave_id_, modj(slot), tag, mi, ws);
  }
  unsigned* cnt = nullptr;
  const int xg = slot >> 5;
  int t = xg == 3 ? 24 + (slot & 7) : slot & 31;
  int hx = -1, hf = -1; bool hs = false;
  if (xg < 3) {
    const int nq = xg == 2 ? 8 : 12;
#define FWU 32
#define HPF 1
    if (t < nq) {
      const int h = xg * 12 + t;
      qk_body(smem, wave_id_, xg, h, (h >> 3) * 32 + (h & 7), Wq, Wk, ws + OFF_K, ws + OFF_V, ws, tmo, tag);
      if (HPF && HB < NSEG && t < 8) {
        const int lane = lane_id(), wv = t * 8 + wave_id_;
#pragma unroll
        for (int j = 0; j < 2; ++j) touch(w2 + (size_t)(xg * 1024 + 256 + j * 128 + (wv & 32) * 16 + (wv & 31) * 4) * DM, (unsigned)lane * 128, wave_id_ * 256);
        if (wv < (xg == 2 ? 32 : 16)) touch(w2 + (size_t)(3072 + 256 + (wv & 15) * 16) * DM + (wv >= 16 ? 3 : xg) * 256, (unsigned)((lane >> 2) * DM * 2 + (lane & 3) * 128), wave_id_ * 256);
        WAIT_V(0);
      }
    } else {
      const int u = t - nq, r = xg * 20 + u, uu = u % 10;
      cnt = cnt_of(ws, vr_body(smem, wave_id_, xg, r >> 1, r & 1, r, (r >> 4) * 32 + 8 + (r & 15), Wv, Wo, Wq, Wk, ws, tmo, tag, CPF && HB < NSEG && u < 16 ? (const char*)pc0 + u * 8192 : nullptr));
      if (HB < NSEG && u < (HB < HC ? 20 : 10)) hx = uu < 8 ? xg * 8 + uu : (uu == 8 ? 24 + xg : (xg == 2 ? 27 : -1));
      hs = u >= 10;
      if (HF < NSEG && hx < 0) hf = xg == 2 ? u - 16 : xg * 2 + (hs ? 1 : 0);
    }
    if (hx < 0 && hf < 0) return;
    t = hf < 0 ? 8 : 9;
  }
  const bool cx = xg == 3;
  const int xq = cx ? (slot >> 3) & 3 : xg & 3, wsl = xq * 32 + t;
#define GDEL 900
#define GDX 1000
  if (cx || t < 8) l1_role(smem, wave_id_, cx, xq * 8 + (t & 7), x, mod, mem0, ws + OFF_H1, cnt, tag, tmo, mts);
#define KH 0
  if (KH && !cx && t >= 24) {
    const int it = (t & 7) * 8 + wave_id_, lane = lane_id();
    touch(((it & 16) ? Wk : Wv) + (size_t)(256 + (it & 8) * 64 + (it & 7) * 32) * HD + (8 * xq + 1 + 4 * (it >> 5)) * DH,
          (unsigned)((lane >> 1) * HD * 2 + (lane & 1) * 128), 155648 + wave_id_ * 256);
  }
  if (t < 8 || t >= 24) {
    if (!cx && t >= 24) arm_job(wave_id_, xq * 8 + t - 24, tag, ws);
    if (!cx && t >= 24 && wave_id_ == 0) STS(ws, 128 + xq * 32 + t - 24, 4);
    while ((unsigned)__builtin_amdgcn_s_memrealtime() - tst < (cx ? GDX : GDEL)) __builtin_amdgcn_s_sleep(8);
    if (t >= 24) cnt = cnt_of(ws, init_wait(ws, tag));
    if (!cx && t >= 24) {
      mod2_job(smem, wave_id_, xq * 8 + t - 24, mod, cvec, wmod, cnt);
    }
  }
  auto u2_of = [&]() { return cx ? -1 : (t >= 13 && t < 16 ? t - 13 : (t >= 20 && t < 24 ? t - 17 : -1)); };
  auto f_of = [&]() {
    int fl = t < 8 ? t : (t < 18 ? t - 4 : (t < 20 ? t - 10 : t - 14));
    const int sa = (3 + 2 * xq) & 7, sb = sa + 8 < 14 ? sa + 8 : 13;
    if (fl == 12) fl = sa; else if (fl == sa) fl = 12;
    else if (fl == 13) fl = sb; else if (fl == sb) fl = 13;
    return (int)__builtin_amdgcn_readfirstlane(14 * xq + fl);
  };
  unsigned tk = task_sel<0>(xg < 3 ? 99 : (cx ? t + 8 : t)), t2 = task_sel<1>(cx ? t + 8 : t), t3 = task_sel<2>(cx ? t + 8 : t);
  for (int it = 0; tk != 0; ++it, tk = t2, t2 = t3, t3 = 0) {
    const int m = tk & 3, hl = (tk >> 2) & 7, part = (tk >> 5) & 3, bo = (tk >> 8) & 255, ns = (int)(tk >> 16) - bo, h = 8 * xq + hl;
    const bool first = m != 0 && bo == 0, late = it > 0;
    const int lane = lane_id();
    const Dep d{nullptr, (unsigned)(PL1B + (lane & 31)) * 128, 8u, (unsigned)(8 - bo)};
    if (wave_id_ == 0 && late) wait_l1(cnt + (PL1B - PL1) * 32, 7u, tmo);
    BAR();
    gemm7<R_QKV>(smem, wave_id_, (m == 0 ? Wq : (m == 1 ? Wk : Wv)) + h * DH, nullptr, HD, ws + OFF_H1 + (size_t)bo * 65536, 65536, d,
                    ws + (m == 0 ? OFF_Q : (m == 1 ? OFF_K : OFF_V)) + h * 8192 + (size_t)bo * 32 * 8192, 32 * 8192,
                    cnt, (PQKV + h) * 32 + 4 * part + m, tmo, wsl, mts, false, first ? NIMM : 0, ns, late ? ns : 0, bo * 257, tag);
  }
  __syncthreads();
  const int u2 = u2_of();
  const int f2 = hx >= 0 ? hx : (u2 < 0 ? -1 : (xq < 3 ? xq * 8 + u2 : (u2 < 3 ? u2 * 8 + 7 : 21 + u2)));
  const int fb = hx >= 0 ? (hs ? HB : HC) : 0, fe = hx >= 0 ? (hs ? HC : NSEG) : HB;
  if (cx || (f2 >= 0 && f2 < 24)) {
    const int ks = cx ? xq : f2 >> 3, cn = cx ? (t + 1) & 7 : f2 & 7;
    const int lane = lane_id();
    const Dep d{nullptr, (unsigned)(cx ? (PSA + 4 * xq + ((lane >> 3) & 3)) * 128 + (lane & 7) * 4 : (PF1 + 16 * ks + (lane & 15)) * 128), cx ? 4u : 1u, cx ? (unsigned)((lane >> 1) & 3) : 0u};
    gemm7<R_PART>(smem, wave_id_, (cx ? Wo : w2) + (size_t)ks * 1024 * DM + cn * 128, nullptr, DM, ws + (cx ? OFF_O : OFF_G) + ks * 16 * 4096, (cx ? 64 : 56) * 4096, d,
                  ws + (cx ? OFF_P : OFF_P2) + (ks * DM + cn * 128) * (TLP ? 64 : 2), (size_t)32 * PROW, cnt, ((cx ? PWO : PF2) + ks * 8 + cn) * 32 + (hx >= 0 ? (hs ? 2 : 1) : 0), tmo,
                  hx >= 0 ? 280 + hx : 128 + xq * 32 + (cx ? cn : 22 + u2), mts, !cx, 0, cx ? NSEG : fe, 0, 0, 0, fb);
  } else if (f2 >= 0) {
    const int c4 = f2 - 24;
    const int lane = lane_id();
    const Dep d{nullptr, (unsigned)(PF1 + 48 + (lane & 7)) * 128, 1u, 0u};
    gemm7<R_PART2>(smem, wave_id_, w2 + (size_t)3072 * DM + c4 * 256, nullptr, DM, ws + OFF_G + 48 * 4096, 56 * 4096, d,
                   ws + OFF_P2 + (3 * DM + c4 * 256) * (TLP ? 64 : 2), (size_t)32 * PROW, cnt, (PF2 + 24 + c4) * 32 + (hx >= 0 ? (hs ? 2 : 1) : 0), tmo, hx >= 0 ? 280 + hx : 128 + xq * 32 + 22 + u2, mts, true, 0, fe, 0, 0, 0, fb);
  } else if (t < 8 || (t >= 16 && t < 20) || t == 24 || t == 25) {
    int f = f_of();
    asm volatile("" : "+s"(f));
    const int lane = lane_id();
    const Dep d{nullptr, (unsigned)((PLN + (lane >> 3)) * 128 + (lane & 7) * 4), 1u, 0u};
    gemm7<R_F1>(smem, wave_id_, w1 + 64 * f, w3 + 64 * f, FF, ws + OFF_H2, 65536, d, ws + OFF_G + f * 4096, 56 * 4096, cnt, (PF1 + f) * 32, tmo, 128 + xq * 18 + 8 + f, mts, false);
  } else if (t >= 9 && t < 13) {
    fin_role(smem, wave_id_, hf >= 0 ? hf & 15 : xq * 4 + t - 9, hf >= 0 ? HF + (hf >> 3) - FWU : xq >> 1, hf >= 0 ? NSEG : HF, hf >= 0 ? 32 : 2, mod, ws + OFF_P2, (const u16*)(ws + OFF_X1), out, cnt, tmo, mts);
    if ((HF < NSEG ? hf == (NSEG - 1 - HF) * 8 : xq == 3 && t == 12) && opaque_tid() == 0) {
      const u32x4 v = ld16_sc1(ws + OFF_TAG);
      const u64 tn = (((u64)v.y << 32) | v.x) + 1;
      st8_sc1(ws + OFF_TAG, u32x2{(unsigned)tn, (unsigned)(tn >> 32)});
    }
  } else if (t == 28 || t == 29) {
    row_role(smem, wave_id_, 8 * xq, t - 28, mod, ws + OFF_P, x, (u16*)(ws + OFF_X1), ws + OFF_H2, cnt, tmo, 256 + xq, mts);
  } else if (t >= 26) {
    sa_role(smem, wave_id_, 8 * xq + 2 * (t >= 30 ? t - 30 : t - 24), ws + OFF_Q, ws + OFF_K, ws + OFF_V, ws + OFF_KVM, ws + OFF_O, cnt, tmo, 128 + xq * 32 + 30 + (t & 1), mts);
  }
}

extern "C" void kernel_launch(void* const* d_in, const int* in_sizes, int n_in, void* d_out, int out_size, void* d_ws, size_t ws_size,
                              hipStream_t stream) {
  const u16* x    = (const u16*)d_in[0];
  const u16* mem0 = (const u16*)d_in[1];
  const u16* Wq   = (const u16*)d_in[2];
  const u16* Wk   = (const u16*)d_in[3];
  const u16* Wv   = (const u16*)d_in[4];
  const u16* Wo   = (const u16*)d_in[5];
  const u16* w1   = (const u16*)d_in[8];
  const u16* w3   = (const u16*)d_in[9];
  const u16* w2   = (const u16*)d_in[10];
  const u16* c    = (const u16*)d_in[11];
  const u16* wmod = (const u16*)d_in[12];
#define WSHIFT 0
  char* ws = (char*)d_ws + ((size_t)WSHIFT << 20);
  hipFuncSetAttribute((const void*)k_mega, hipFuncAttributeMaxDynamicSharedMemorySize, MEGA_LDS);
  k_mega<<<256, 512, MEGA_LDS, stream>>>(Wq, Wk, Wv, Wo, w1, w3, w2, x, c, wmod, mem0, ws, (u16*)d_out);
}
```
